```python
import torch

def _softmax(x, axis=-1):
    return torch.softmax(x, dim=axis)
def _gelu(x):
    return torch.nn.functional.gelu(x, approximate="tanh")
def _silu(x):
    return torch.nn.functional.silu(x)
def _sigmoid(x):
    return torch.sigmoid(x)
def _layernorm(x, axis=-1, eps=1e-5):
    m = x.mean(dim=axis, keepdim=True)
    v = x.var(dim=axis, keepdim=True, unbiased=False)
    return (x - m) / torch.sqrt(v + eps)
def _scatter_add(tgt, idx, src, axis=0):
    return tgt.clone().index_add_(axis, idx, src)
def _segment_sum(src, seg, n):
    out = torch.zeros((n,) + src.shape[1:], dtype=src.dtype)
    return out.index_add_(0, seg, src)
def _pad(x, pw):
    flat = tuple(v for pair in reversed(pw) for v in pair)
    return torch.nn.functional.pad(x, flat)

kernel_name = 'gated_mix__spectral_fnet__moe_arch_mod__prompt'

def setup_inputs():

    _w = (lambda a: (lambda t: t.to(torch.bfloat16) if t.is_floating_point() else t)(torch.as_tensor(a)))
    def _A_setup():
        B, S, D = 1, 2048, 512
        ns = torch.arange(S)[:, None] * torch.arange(S)[None, :]
        nd = torch.arange(D)[:, None] * torch.arange(D)[None, :]
        Ws_re = torch.cos(-2.0 * torch.pi * ns / S).to(float)
        Ws_im = torch.sin(-2.0 * torch.pi * ns / S).to(float)
        Wd_re = torch.cos(-2.0 * torch.pi * nd / D).to(float)
        Wd_im = torch.sin(-2.0 * torch.pi * nd / D).to(float)
        return {
            "x": _w((lambda *s: torch.randn(*s, dtype=torch.bfloat16) * 0.05)(B, S, D)),
            "dft_seq_re": _w(Ws_re),
            "dft_seq_im": _w(Ws_im),
            "dft_hid_re": _w(Wd_re),
            "dft_hid_im": _w(Wd_im),
        }
    _A_in = _A_setup()

    def _B_setup():
        B, S, D, F, C = 1, 2048, 512, 1792, 1
        return {
            "x": _w((lambda *s: torch.randn(*s, dtype=torch.bfloat16) * 0.05)(B, S, D)),
            "w_r": _w((lambda *s: torch.randn(*s, dtype=torch.bfloat16) * 1.6)(D)),
            "w_gate": _w((lambda *s: torch.randn(*s, dtype=torch.bfloat16) * 1.6)(D, F)),
            "w_up": _w((lambda *s: torch.randn(*s, dtype=torch.bfloat16) * 1.6)(D, F)),
            "w_down": _w((lambda *s: torch.randn(*s, dtype=torch.bfloat16) * 1.6)(F, D)),
        }
    _B_in = _B_setup()

    return {
        "A_x": _A_in["x"],
        "A_dft_seq_re": _A_in["dft_seq_re"],
        "A_dft_seq_im": _A_in["dft_seq_im"],
        "A_dft_hid_re": _A_in["dft_hid_re"],
        "A_dft_hid_im": _A_in["dft_hid_im"],
        "B_x": _B_in["x"],
        "B_w_r": _B_in["w_r"],
        "B_w_gate": _B_in["w_gate"],
        "B_w_up": _B_in["w_up"],
        "B_w_down": _B_in["w_down"],
        "gate_w": _w((lambda *s: torch.randn(*s, dtype=torch.bfloat16) * 0.05)(512) * 16.0),
    }

def reference(A_x, A_dft_seq_re, A_dft_seq_im, A_dft_hid_re, A_dft_hid_im, B_x, B_w_r, B_w_gate, B_w_up, B_w_down, gate_w):
    def _A_ref(x, dft_seq_re, dft_seq_im, dft_hid_re, dft_hid_im):
        a = torch.einsum("bsd,ed->bse", x, dft_hid_re)
        b = torch.einsum("bsd,ed->bse", x, dft_hid_im)
        y = torch.einsum("ts,bse->bte", dft_seq_re, a) - torch.einsum("ts,bse->bte", dft_seq_im, b)
        return y

    def _B_ref(x, w_r, w_gate, w_up, w_down):
        B, S, D = x.shape
        C = 1
        r = x @ w_r
        idx = torch.argsort(-r, axis=-1)[:, :C]
        idx_e = (lambda x, n, axis: torch.repeat_interleave(x, n, dim=axis))(idx.reshape(B, C, 1), D, axis=2)
        x_sel = (lambda x, i, axis: torch.take_along_dim(x, i, dim=axis))(x, idx_e, axis=1)
        g = _silu(x_sel @ w_gate)
        u = x_sel @ w_up
        y = (g * u) @ w_down
        r_sel = _sigmoid((lambda x, i, axis: torch.take_along_dim(x, i, dim=axis))(r, idx, axis=1))
        y = y * r_sel.reshape(B, C, 1)
        b_off = (torch.arange(B) * S).reshape(B, 1)
        flat_idx = (idx + b_off).reshape(B * C)
        zero = (x * 0.0).reshape(B * S, D)
        upd = _scatter_add(zero, flat_idx, y.reshape(B * C, D), axis=0)
        return x + upd.reshape(B, S, D)

    x = A_x
    g = _sigmoid((x * gate_w).sum(-1, keepdims=True))
    a = _A_ref(x, A_dft_seq_re, A_dft_seq_im, A_dft_hid_re, A_dft_hid_im)
    b = _B_ref(x, B_w_r, B_w_gate, B_w_up, B_w_down)
    return g * a + (1 - g) * b
```

```cpp
#include <hip/hip_runtime.h>

typedef unsigned short u16;

__global__ __launch_bounds__(256) void fused_k(const u16* wsre, const u16* wsim, const u16* wdre, const u16* wdim, const u16* x, char* ws,
                                               u16* out, const u16* w_r, const u16* gate_w, const u16* w_gate, const u16* w_up,
                                               const u16* w_down) {
  __shared__ __attribute__((aligned(16))) char smem[163840];
  asm volatile(
      ".Lbegin_%=:\n"
      "s_load_dwordx16 s[8:23], %0, 0\n"
      "s_load_dwordx8 s[24:31], %0, 64\n"
      "v_mbcnt_lo_u32_b32 v255, -1, 0\n"
      "s_mov_b32 s32, %1\n"
      "v_mbcnt_hi_u32_b32 v255, -1, v255\n"
      "s_mov_b32 s33, %2\n"
      "s_bfe_u32 s34, s32, 0x20001\n"
      "s_bfe_u32 s35, s32, 0x20003\n"
      "v_lshrrev_b32 v236, 3, v255\n"
      "s_lshr_b32 s43, s32, 5\n"
      "v_xor_b32 v237, v255, v236\n"
      "s_and_b32 s36, s32, 1\n"
      "v_and_b32 v237, 7, v237\n"
      "s_lshl3_add_u32 s36, s36, s43\n"
      "v_lshlrev_b32 v237, 4, v237\n"
      "s_add_u32 s44, s43, s35\n"
      "v_lshlrev_b32 v252, 4, v255\n"
      "s_lshl_b32 s41, s33, 14\n"
      "s_bitcmp1_b32 s44, 0\n"
      "s_cbranch_scc1 .Lstream_%=\n"
      "v_and_b32 v238, 3, v236\n"
      "s_lshr_b32 s43, s43, 1\n"
      "v_and_b32 v239, 4, v236\n"
      "s_add_u32 s76, s43, s35\n"
      "v_lshl_or_b32 v238, v239, 1, v238\n"
      "s_lshl_b32 s76, s76, 8\n"
      "v_lshl_or_b32 v240, v238, 10, v237\n"
      "s_and_b32 s76, s76, 0x300\n"
      "v_add_u32 v241, 15360, v240\n"
      "s_lshr_b32 s79, s36, 1\n"
      "v_add_u32 v242, 2048, v240\n"
      "s_and_b32 s44, s34, 1\n"
      "v_add_u32 v243, 17408, v240\n"
      "s_lshl3_add_u32 s44, s44, s79\n"
      "s_lshl2_add_u32 s40, s44, s33\n"
      "s_lshl_b32 s44, s40, 15\n"
      "s_lshl_b32 s77, s33, 7\n"
      "s_add_u32 s77, s77, s76\n"
      "s_and_b32 s77, s77, 0x380\n"
      "s_lshl_b32 s45, s35, 17\n"
      "s_lshl_b32 s78, s33, 12\n"
      "s_bitcmp1_b32 s34, 1\n"
      "s_waitcnt lgkmcnt(0)\n"
      "s_cselect_b64 s[12:13], s[14:15], s[12:13]\n"
      "s_add_u32 s68, s16, s44\n"
      "s_addc_u32 s69, s17, 0\n"
      "s_add_u32 s12, s12, s45\n"
      "s_addc_u32 s13, s13, 0\n"
      "s_add_u32 s45, s34, 1\n"
      "s_and_b32 s45, s45, 3\n"
      "s_lshl_b32 s45, s45, 15\n"
      "s_or_b32 s45, s45, s77\n"
      "s_add_u32 s46, s12, s45\n"
      "s_addc_u32 s47, s13, 0\n"
      "s_add_u32 m0, s78, 65536\n"
      "s_nop 0\n"
      "global_load_lds_dwordx4 v240, s[46:47] offset:0\n"
      "global_load_lds_dwordx4 v241, s[46:47] offset:1024\n"
      "global_load_lds_dwordx4 v242, s[46:47] offset:2048\n"
      "global_load_lds_dwordx4 v243, s[46:47] offset:3072\n"
      "s_add_u32 s46, s68, s76\n"
      "s_addc_u32 s47, s69, 0\n"
      "s_add_u32 m0, s41, 0\n"
      "s_nop 0\n"
      "global_load_lds_dwordx4 v240, s[46:47] offset:0\n"
      "global_load_lds_dwordx4 v241, s[46:47] offset:1024\n"
      "global_load_lds_dwordx4 v242, s[46:47] offset:2048\n"
      "global_load_lds_dwordx4 v243, s[46:47] offset:3072\n"
      "s_add_u32 s45, s34, 2\n"
      "s_and_b32 s45, s45, 3\n"
      "s_lshl_b32 s45, s45, 15\n"
      "s_or_b32 s45, s45, s77\n"
      "s_add_u32 s46, s12, s45\n"
      "s_addc_u32 s47, s13, 0\n"
      "s_add_u32 m0, s78, 98304\n"
      "s_nop 0\n"
      "global_load_lds_dwordx4 v240, s[46:47] offset:0\n"
      "global_load_lds_dwordx4 v241, s[46:47] offset:1024\n"
      "s_add_u32 s45, s76, 128\n"
      "s_and_b32 s45, s45, 0x380\n"
      "s_add_u32 s46, s68, s45\n"
      "s_addc_u32 s47, s69, 0\n"
      "s_add_u32 m0, s41, 4096\n"
      "s_nop 0\n"
      "global_load_lds_dwordx4 v240, s[46:47] offset:0\n"
      "global_load_lds_dwordx4 v241, s[46:47] offset:1024\n"
      "global_load_lds_dwordx4 v242, s[46:47] offset:2048\n"
      "global_load_lds_dwordx4 v243, s[46:47] offset:3072\n"
      "s_branch .Lprod_%=\n"
      ".Lstream_%=:\n"
      "v_lshl_or_b32 v244, v236, 12, v237\n"
      "v_add_u32 v245, 31744, v244\n"
      "v_add_u32 v246, 63488, v244\n"
      "v_add_u32 v247, 95232, v244\n"
      "s_getpc_b64 s[46:47]\n"
      ".Lhere_%=:\n"
      "s_lshr_b32 s44, s32, 4\n"
      "s_lshl2_add_u32 s44, s44, s33\n"
      "s_lshl_b32 s44, s44, 10\n"
      "s_sub_u32 s46, s46, .Lhere_%=-.Lbegin_%=\n"
      "s_subb_u32 s47, s47, 0\n"
      "v_add_u32 v238, s44, v252\n"
      "v_min_u32 v238, .Lend_%=-.Lbegin_%=-16, v238\n"
      "global_load_dwordx4 v[192:195], v238, s[46:47]\n"
      "s_waitcnt lgkmcnt(0)\n"
      "s_bitcmp1_b32 s34, 1\n"
      "s_cselect_b64 s[8:9], s[10:11], s[8:9]\n"
      "s_and_b32 s45, s34, 1\n"
      "s_lshl2_add_u32 s45, s45, s33\n"
      "s_lshl_b32 s45, s45, 9\n"
      "s_lshl_b32 s72, s36, 19\n"
      "s_add_u32 s45, s45, s72\n"
      "s_add_u32 s8, s8, s45\n"
      "s_addc_u32 s9, s9, 0\n"
      "s_lshr_b32 s39, s32, 6\n"
      "s_and_b32 s45, s35, 2\n"
      "s_xor_b32 s39, s39, s45\n"
      "s_lshl_b32 s45, s39, 7\n"
      "s_add_u32 s72, s33, 0\n"
      "s_and_b32 s72, s72, 3\n"
      "s_lshl_b32 s72, s72, 17\n"
      "s_or_b32 s72, s72, s45\n"
      "s_add_u32 s73, s33, 1\n"
      "s_and_b32 s73, s73, 3\n"
      "s_lshl_b32 s73, s73, 17\n"
      "s_or_b32 s73, s73, s45\n"
      "s_add_u32 s74, s33, 2\n"
      "s_and_b32 s74, s74, 3\n"
      "s_lshl_b32 s74, s74, 17\n"
      "s_or_b32 s74, s74, s45\n"
      "s_add_u32 s75, s33, 3\n"
      "s_and_b32 s75, s75, 3\n"
      "s_lshl_b32 s75, s75, 17\n"
      "s_or_b32 s75, s75, s45\n"
      "s_sleep 20\n"
      "s_bitcmp1_b32 s34, 1\n"
      "s_cselect_b64 s[46:47], s[14:15], s[12:13]\n"
      "s_min_u32 s44, s33, 2\n"
      "s_add_u32 s44, s44, s34\n"
      "s_add_u32 s44, s44, 2\n"
      "s_and_b32 s44, s44, 3\n"
      "s_lshl2_add_u32 s44, s35, s44\n"
      "s_lshr_b32 s45, s32, 6\n"
      "s_lshl2_add_u32 s44, s44, s45\n"
      "s_lshl_b32 s44, s44, 13\n"
      "v_lshl_add_u32 v238, v255, 7, s44\n"
      "s_cmp_eq_u32 s33, 3\n"
      "s_cbranch_scc1 .Ls_e1_%=\n"
      "global_load_dword v196, v238, s[46:47]\n"
      ".Ls_e1_%=:\n"
      "s_xor_b32 s45, s72, 0\n"
      "s_add_u32 s46, s8, s45\n"
      "s_addc_u32 s47, s9, 0\n"
      "s_add_u32 m0, s41, 0\n"
      "s_nop 0\n"
      "global_load_lds_dwordx4 v244, s[46:47] offset:0\n"
      "global_load_lds_dwordx4 v245, s[46:47] offset:1024\n"
      "global_load_lds_dwordx4 v246, s[46:47] offset:2048\n"
      "global_load_lds_dwordx4 v247, s[46:47] offset:3072\n"
      "s_xor_b32 s45, s73, 0\n"
      "s_add_u32 s46, s8, s45\n"
      "s_addc_u32 s47, s9, 0\n"
      "s_add_u32 m0, s41, 4096\n"
      "s_nop 0\n"
      "global_load_lds_dwordx4 v244, s[46:47] offset:0\n"
      "global_load_lds_dwordx4 v245, s[46:47] offset:1024\n"
      "global_load_lds_dwordx4 v246, s[46:47] offset:2048\n"
      "global_load_lds_dwordx4 v247, s[46:47] offset:3072\n"
      "s_xor_b32 s45, s74, 0\n"
      "s_add_u32 s46, s8, s45\n"
      "s_addc_u32 s47, s9, 0\n"
      "s_add_u32 m0, s41, 8192\n"
      "s_nop 0\n"
      "global_load_lds_dwordx4 v244, s[46:47] offset:0\n"
      "global_load_lds_dwordx4 v245, s[46:47] offset:1024\n"
      "global_load_lds_dwordx4 v246, s[46:47] offset:2048\n"
      "global_load_lds_dwordx4 v247, s[46:47] offset:3072\n"
      "s_add_u32 s58, s18, 16777216\n"
      "s_addc_u32 s59, s19, 0\n"
      "v_mov_b32 v235, 0\n"
      "global_load_dword v235, v235, s[58:59] sc1\n"
      "v_and_b32 v238, 15, v255\n"
      "v_lshrrev_b32 v239, 4, v255\n"
      "v_xor_b32 v239, v239, v238\n"
      "v_and_b32 v239, 7, v239\n"
      "v_lshlrev_b32 v239, 4, v239\n"
      "v_lshl_or_b32 v238, v238, 7, v239\n"
      "v_add_u32 v248, s41, v238\n"
      "v_xor_b32 v249, 64, v248\n"
      "s_lshl2_add_u32 s43, s34, s35\n"
      "s_lshr_b32 s45, s39, 1\n"
      "s_lshl1_add_u32 s45, s33, s45\n"
      "s_lshl_b32 s45, s45, 15\n"
      "s_lshl_b32 s44, s43, 18\n"
      "s_add_u32 s44, s44, s45\n"
      "s_add_u32 s44, s44, 4096\n"
      "s_add_u32 s10, s18, s44\n"
      "s_addc_u32 s11, s19, 0\n"
      "s_xor_b32 s44, s44, 0x8000\n"
      "s_add_u32 s14, s18, s44\n"
      "s_addc_u32 s15, s19, 0\n"
      "s_and_b32 s44, s39, 1\n"
      "s_lshl_b32 s45, s44, 14\n"
      "v_add_u32 v240, s45, v252\n"
      "v_xor_b32 v241, 0x4000, v240\n"
      "v_add_u32 v242, 0x2000, v240\n"
      "v_add_u32 v243, 0x2000, v241\n"
      "s_lshl2_add_u32 s44, s43, s33\n"
      "s_lshl_b32 s44, s44, 11\n"
      "s_add_u32 s44, s44, 65536\n"
      "v_and_b32 v254, 7, v255\n"
      "v_lshl_add_u32 v254, v254, 8, s44\n"
      "s_lshl2_add_u32 s43, s36, s35\n"
      "s_mul_i32 s43, s43, 147456\n"
      "s_mul_i32 s44, s33, 3072\n"
      "s_add_u32 s43, s43, s44\n"
      "s_add_u32 s43, s43, 6291456\n"
      "s_add_u32 s60, s18, s43\n"
      "s_addc_u32 s61, s19, 0\n"
      "s_mul_i32 s44, s34, 36864\n"
      "s_add_u32 s62, s60, s44\n"
      "s_addc_u32 s63, s61, 0\n"
      "s_lshr_b32 s43, s34, 1\n"
      "s_lshl2_add_u32 s43, s43, s35\n"
      "s_lshl_b32 s43, s43, 12\n"
      "s_add_u32 s43, s43, 4096\n"
      "v_lshl_add_u32 v200, v255, 6, s43\n"
      "s_lshr_b32 s76, s32, 4\n"
      "s_bfe_u32 s43, s32, 0x10002\n"
      "s_lshl1_add_u32 s76, s76, s43\n"
      "s_and_b32 s77, s32, 3\n"
      "s_cmp_lt_u32 s76, 28\n"
      "s_cselect_b32 s78, -1, 0\n"
      "s_lshr_b32 s79, s33, 1\n"
      "s_lshl1_add_u32 s79, s77, s79\n"
      "v_add_u32 v250, 0x10000, v248\n"
      "v_add_u32 v251, 0x10000, v249\n"
      "s_xor_b32 s45, s75, 0\n"
      "s_add_u32 s46, s8, s45\n"
      "s_addc_u32 s47, s9, 0\n"
      "s_add_u32 m0, s41, 12288\n"
      "s_nop 0\n"
      "global_load_lds_dwordx4 v244, s[46:47] offset:0\n"
      "global_load_lds_dwordx4 v245, s[46:47] offset:1024\n"
      "global_load_lds_dwordx4 v246, s[46:47] offset:2048\n"
      "global_load_lds_dwordx4 v247, s[46:47] offset:3072\n"
      "s_xor_b32 s45, s72, 128\n"
      "s_add_u32 s46, s8, s45\n"
      "s_addc_u32 s47, s9, 0\n"
      "s_add_u32 m0, s41, 65536\n"
      "s_nop 0\n"
      "global_load_lds_dwordx4 v244, s[46:47] offset:0\n"
      "global_load_lds_dwordx4 v245, s[46:47] offset:1024\n"
      "global_load_lds_dwordx4 v246, s[46:47] offset:2048\n"
      "global_load_lds_dwordx4 v247, s[46:47] offset:3072\n"
      "s_waitcnt vmcnt(17)\n"
      "ds_read_b128 a[0:3], v248 offset:0\n"
      "ds_read_b128 a[4:7], v248 offset:2048\n"
      "ds_read_b128 a[32:35], v249 offset:0\n"
      "ds_read_b128 a[36:39], v249 offset:2048\n"
      "s_xor_b32 s45, s73, 128\n"
      "s_add_u32 s46, s8, s45\n"
      "s_addc_u32 s47, s9, 0\n"
      "s_add_u32 m0, s41, 69632\n"
      "s_nop 0\n"
      "global_load_lds_dwordx4 v244, s[46:47] offset:0\n"
      "global_load_lds_dwordx4 v245, s[46:47] offset:1024\n"
      "global_load_lds_dwordx4 v246, s[46:47] offset:2048\n"
      "global_load_lds_dwordx4 v247, s[46:47] offset:3072\n"
      "s_waitcnt vmcnt(17)\n"
      "ds_read_b128 a[8:11], v248 offset:4096\n"
      "ds_read_b128 a[12:15], v248 offset:6144\n"
      "ds_read_b128 a[40:43], v249 offset:4096\n"
      "ds_read_b128 a[44:47], v249 offset:6144\n"
      "s_xor_b32 s45, s74, 128\n"
      "s_add_u32 s46, s8, s45\n"
      "s_addc_u32 s47, s9, 0\n"
      "s_add_u32 m0, s41, 73728\n"
      "s_nop 0\n"
      "global_load_lds_dwordx4 v244, s[46:47] offset:0\n"
      "global_load_lds_dwordx4 v245, s[46:47] offset:1024\n"
      "global_load_lds_dwordx4 v246, s[46:47] offset:2048\n"
      "global_load_lds_dwordx4 v247, s[46:47] offset:3072\n"
      "s_waitcnt vmcnt(17)\n"
      "ds_read_b128 a[16:19], v248 offset:8192\n"
      "ds_read_b128 a[20:23], v248 offset:10240\n"
      "ds_read_b128 a[48:51], v249 offset:8192\n"
      "ds_read_b128 a[52:55], v249 offset:10240\n"
      "s_waitcnt vmcnt(16)\n"
      "v_readfirstlane_b32 s37, v235\n"
      "s_nop 1\n"
      "s_mul_i32 s37, s37, 0x9E3779B1\n"
      "s_add_u32 s37, s37, 0x7F4A7C15\n"
      "s_mov_b32 s38, 0x8000\n"
      "s_waitcnt lgkmcnt(8)\n"
      "s_xor_b32 s45, s75, 128\n"
      "s_add_u32 s46, s8, s45\n"
      "s_addc_u32 s47, s9, 0\n"
      "s_add_u32 m0, s41, 0\n"
      "s_nop 0\n"
      "global_load_lds_dwordx4 v244, s[46:47] offset:0\n"
      "global_load_lds_dwordx4 v245, s[46:47] offset:1024\n"
      "global_load_lds_dwordx4 v246, s[46:47] offset:2048\n"
      "global_load_lds_dwordx4 v247, s[46:47] offset:3072\n"
      "s_waitcnt vmcnt(16)\n"
      "ds_read_b128 a[24:27], v248 offset:12288\n"
      "ds_read_b128 a[28:31], v248 offset:14336\n"
      "ds_read_b128 a[56:59], v249 offset:12288\n"
      "ds_read_b128 a[60:63], v249 offset:14336\n"
      "s_waitcnt lgkmcnt(8)\n"
      "s_xor_b32 s45, s72, 256\n"
      "s_add_u32 s46, s8, s45\n"
      "s_addc_u32 s47, s9, 0\n"
      "s_add_u32 m0, s41, 4096\n"
      "s_nop 0\n"
      "global_load_lds_dwordx4 v244, s[46:47] offset:0\n"
      "global_load_lds_dwordx4 v245, s[46:47] offset:1024\n"
      "global_load_lds_dwordx4 v246, s[46:47] offset:2048\n"
      "global_load_lds_dwordx4 v247, s[46:47] offset:3072\n"
      "s_waitcnt vmcnt(16)\n"
      "ds_read_b128 a[64:67], v250 offset:0\n"
      "ds_read_b128 a[68:71], v250 offset:2048\n"
      "ds_read_b128 a[96:99], v251 offset:0\n"
      "ds_read_b128 a[100:103], v251 offset:2048\n"
      "s_waitcnt lgkmcnt(8)\n"
      "s_xor_b32 s45, s73, 256\n"
      "s_add_u32 s46, s8, s45\n"
      "s_addc_u32 s47, s9, 0\n"
      "s_add_u32 m0, s41, 8192\n"
      "s_nop 0\n"
      "global_load_lds_dwordx4 v244, s[46:47] offset:0\n"
      "global_load_lds_dwordx4 v245, s[46:47] offset:1024\n"
      "global_load_lds_dwordx4 v246, s[46:47] offset:2048\n"
      "global_load_lds_dwordx4 v247, s[46:47] offset:3072\n"
      "s_waitcnt vmcnt(16)\n"
      "ds_read_b128 a[72:75], v250 offset:4096\n"
      "ds_read_b128 a[76:79], v250 offset:6144\n"
      "ds_read_b128 a[104:107], v251 offset:4096\n"
      "ds_read_b128 a[108:111], v251 offset:6144\n"
      "s_waitcnt lgkmcnt(8)\n"
      "s_xor_b32 s45, s74, 256\n"
      "s_add_u32 s46, s8, s45\n"
      "s_addc_u32 s47, s9, 0\n"
      "s_add_u32 m0, s41, 12288\n"
      "s_nop 0\n"
      "global_load_lds_dwordx4 v244, s[46:47] offset:0\n"
      "global_load_lds_dwordx4 v245, s[46:47] offset:1024\n"
      "global_load_lds_dwordx4 v246, s[46:47] offset:2048\n"
      "global_load_lds_dwordx4 v247, s[46:47] offset:3072\n"
      "s_waitcnt vmcnt(16)\n"
      "ds_read_b128 a[80:83], v250 offset:8192\n"
      "ds_read_b128 a[84:87], v250 offset:10240\n"
      "ds_read_b128 a[112:115], v251 offset:8192\n"
      "ds_read_b128 a[116:119], v251 offset:10240\n"
      "s_waitcnt vmcnt(12)\n"
      "ds_read_b128 a[88:91], v248 offset:0\n"
      "ds_read_b128 a[92:95], v248 offset:2048\n"
      "ds_read_b128 a[120:123], v249 offset:0\n"
      "ds_read_b128 a[124:127], v249 offset:2048\n"
      "global_load_dword v253, v254, s[58:59] offset:0 sc1\n"
      "s_waitcnt lgkmcnt(12)\n"
      "s_xor_b32 s45, s75, 256\n"
      "s_add_u32 s46, s8, s45\n"
      "s_addc_u32 s47, s9, 0\n"
      "s_add_u32 m0, s41, 65536\n"
      "s_nop 0\n"
      "global_load_lds_dwordx4 v244, s[46:47] offset:0\n"
      "global_load_lds_dwordx4 v245, s[46:47] offset:1024\n"
      "global_load_lds_dwordx4 v246, s[46:47] offset:2048\n"
      "global_load_lds_dwordx4 v247, s[46:47] offset:3072\n"
      "s_waitcnt lgkmcnt(8)\n"
      "s_xor_b32 s45, s72, 384\n"
      "s_add_u32 s46, s8, s45\n"
      "s_addc_u32 s47, s9, 0\n"
      "s_add_u32 m0, s41, 69632\n"
      "s_nop 0\n"
      "global_load_lds_dwordx4 v244, s[46:47] offset:0\n"
      "global_load_lds_dwordx4 v245, s[46:47] offset:1024\n"
      "global_load_lds_dwordx4 v246, s[46:47] offset:2048\n"
      "global_load_lds_dwordx4 v247, s[46:47] offset:3072\n"
      "s_waitcnt vmcnt(17)\n"
      "ds_read_b128 a[128:131], v248 offset:4096\n"
      "ds_read_b128 a[132:135], v248 offset:6144\n"
      "ds_read_b128 a[160:163], v249 offset:4096\n"
      "ds_read_b128 a[164:167], v249 offset:6144\n"
      "s_waitcnt vmcnt(13)\n"
      "ds_read_b128 a[136:139], v248 offset:8192\n"
      "ds_read_b128 a[140:143], v248 offset:10240\n"
      "ds_read_b128 a[168:171], v249 offset:8192\n"
      "ds_read_b128 a[172:175], v249 offset:10240\n"
      "s_waitcnt vmcnt(8)\n"
      "v_cmp_ne_u32 vcc, s37, v253\n"
      "s_nop 1\n"
      "s_cbranch_vccnz .Ls_s3_%=\n"
      ".Ls_o4_%=:\n"
      "global_load_dwordx2 v[202:203], v200, s[58:59] sc1\n"
      "global_load_dwordx4 v[128:131], v240, s[10:11] offset:-4096 sc1\n"
      "global_load_dwordx4 v[132:135], v240, s[10:11] offset:-3072 sc1\n"
      "global_load_dwordx4 v[136:139], v240, s[10:11] offset:-2048 sc1\n"
      "global_load_dwordx4 v[140:143], v242, s[10:11] offset:-4096 sc1\n"
      "global_load_dwordx4 v[144:147], v242, s[10:11] offset:-3072 sc1\n"
      "global_load_dwordx4 v[148:151], v242, s[10:11] offset:-2048 sc1\n"
      "s_waitcnt vmcnt(5)\n"
      "v_mfma_f32_16x16x32_bf16 v[0:3], v[128:131], a[0:3], 0\n"
      "global_load_dwordx4 v[152:155], v241, s[10:11] offset:-4096 sc1\n"
      "v_cmp_ne_u32 vcc, s37, v203\n"
      "s_nop 1\n"
      "s_cbranch_vccnz .Ls_s6_%=\n"
      ".Ls_o7_%=:\n"
      "v_mfma_f32_16x16x32_bf16 v[4:7], v[128:131], a[4:7], 0\n"
      "v_max_u32_dpp v202, v202, v202 quad_perm:[1,0,3,2] row_mask:0xf bank_mask:0xf\n"
      "s_nop 1\n"
      "v_max_u32_dpp v202, v202, v202 quad_perm:[2,3,0,1] row_mask:0xf bank_mask:0xf\n"
      "v_mfma_f32_16x16x32_bf16 v[8:11], v[128:131], a[8:11], 0\n"
      "global_load_dwordx4 v[156:159], v241, s[10:11] offset:-3072 sc1\n"
      "v_max_u32_dpp v202, v202, v202 row_half_mirror row_mask:0xf bank_mask:0xf\n"
      "v_mfma_f32_16x16x32_bf16 v[12:15], v[128:131], a[12:15], 0\n"
      "s_nop 0\n"
      "v_max_u32_dpp v202, v202, v202 row_mirror row_mask:0xf bank_mask:0xf\n"
      "v_mfma_f32_16x16x32_bf16 v[16:19], v[128:131], a[16:19], 0\n"
      "global_load_dwordx4 v[160:163], v241, s[10:11] offset:-2048 sc1\n"
      "v_mov_b32 v201, v202\n"
      "v_mfma_f32_16x16x32_bf16 v[20:23], v[128:131], a[20:23], 0\n"
      "s_nop 0\n"
      "v_permlane16_swap_b32 v202, v201\n"
      "v_mfma_f32_16x16x32_bf16 v[24:27], v[128:131], a[24:27], 0\n"
      "global_load_dwordx4 v[164:167], v243, s[10:11] offset:-4096 sc1\n"
      "v_max_u32 v202, v202, v201\n"
      "v_mfma_f32_16x16x32_bf16 v[28:31], v[128:131], a[28:31], 0\n"
      "v_mov_b32 v201, v202\n"
      "s_nop 1\n"
      "v_permlane32_swap_b32 v202, v201\n"
      "s_waitcnt vmcnt(8)\n"
      "v_mfma_f32_16x16x32_bf16 v[32:35], v[132:135], a[0:3], 0\n"
      "global_load_dwordx4 v[168:171], v243, s[10:11] offset:-3072 sc1\n"
      "v_max_u32 v202, v202, v201\n"
      "v_mfma_f32_16x16x32_bf16 v[36:39], v[132:135], a[4:7], 0\n"
      "v_readfirstlane_b32 s40, v202\n"
      "s_nop 1\n"
      "s_andn2_b32 s42, 0x7ff, s40\n"
      "v_mfma_f32_16x16x32_bf16 v[40:43], v[132:135], a[8:11], 0\n"
      "s_lshl_b32 s54, s42, 10\n"
      "global_load_dwordx4 v[172:175], v243, s[10:11] offset:-2048 sc1\n"
      "s_lshl_b32 s55, s79, 7\n"
      "v_mfma_f32_16x16x32_bf16 v[44:47], v[132:135], a[12:15], 0\n"
      "s_add_u32 s54, s54, s55\n"
      "v_and_b32 v203, 48, v255\n"
      "v_add_u32 v203, s54, v203\n"
      "v_mfma_f32_16x16x32_bf16 v[48:51], v[132:135], a[16:19], 0\n"
      "global_load_dwordx4 v[176:179], v240, s[14:15] offset:-4096 sc1\n"
      "global_load_dwordx4 v[192:195], v203, s[16:17]\n"
      "v_mfma_f32_16x16x32_bf16 v[52:55], v[132:135], a[20:23], 0\n"
      "global_load_dwordx4 v[196:199], v203, s[16:17] offset:64\n"
      "v_lshrrev_b32 v201, 4, v255\n"
      "v_mfma_f32_16x16x32_bf16 v[56:59], v[132:135], a[24:27], 0\n"
      "global_load_dwordx4 v[180:183], v240, s[14:15] offset:-3072 sc1\n"
      "v_bfe_u32 v224, v255, 2, 2\n"
      "v_mfma_f32_16x16x32_bf16 v[60:63], v[132:135], a[28:31], 0\n"
      "s_waitcnt vmcnt(12)\n"
      "v_lshl_or_b32 v201, v201, 3, v224\n"
      "v_and_b32 v224, 3, v255\n"
      "v_mfma_f32_16x16x32_bf16 v[64:67], v[136:139], a[0:3], 0\n"
      "global_load_dwordx4 v[184:187], v240, s[14:15] offset:-2048 sc1\n"
      "v_lshlrev_b32 v224, 3, v224\n"
      "v_mfma_f32_16x16x32_bf16 v[68:71], v[136:139], a[4:7], 0\n"
      "global_load_dwordx4 v[188:191], v242, s[14:15] offset:-4096 sc1\n"
      "v_lshl_or_b32 v201, v201, 7, v224\n"
      "v_mfma_f32_16x16x32_bf16 v[72:75], v[136:139], a[8:11], 0\n"
      "s_mov_b32 s64, 0xffff0000\n"
      "s_waitcnt lgkmcnt(12)\n"
      "s_xor_b32 s70, s73, 384\n"
      "s_add_u32 s70, s8, s70\n"
      "s_addc_u32 s71, s9, 0\n"
      "s_add_u32 m0, s41, 73728\n"
      "v_mfma_f32_16x16x32_bf16 v[76:79], v[136:139], a[12:15], 0\n"
      "s_mov_b32 s65, 0xffff0000\n"
      "global_load_lds_dwordx4 v244, s[70:71] offset:0\n"
      "s_mov_b32 s66, -1\n"
      "v_mfma_f32_16x16x32_bf16 v[80:83], v[136:139], a[16:19], 0\n"
      "s_mov_b32 s67, 0\n"
      "global_load_dwordx4 v[128:131], v242, s[14:15] offset:-3072 sc1\n"
      "s_lshl_b32 s54, s33, 13\n"
      "v_mfma_f32_16x16x32_bf16 v[84:87], v[136:139], a[20:23], 0\n"
      "s_add_u32 s54, s54, 131072\n"
      "global_load_lds_dwordx4 v245, s[70:71] offset:1024\n"
      "v_add_u32 v202, s54, v201\n"
      "v_mfma_f32_16x16x32_bf16 v[88:91], v[136:139], a[24:27], 0\n"
      "s_and_b32 s54, s33, 1\n"
      "global_load_dwordx4 v[132:135], v242, s[14:15] offset:-2048 sc1\n"
      "s_lshl3_add_u32 s54, s54, s79\n"
      "v_mfma_f32_16x16x32_bf16 v[92:95], v[136:139], a[28:31], 0\n"
      "s_lshl_b32 s54, s54, 8\n"
      "global_load_lds_dwordx4 v246, s[70:71] offset:2048\n"
      "s_lshl_b32 s55, s76, 13\n"
      "v_mfma_f32_16x16x32_bf16 v[0:3], v[140:143], a[32:35], v[0:3]\n"
      "s_add_u32 s55, s55, 262144\n"
      "global_load_dwordx4 v[136:139], v241, s[14:15] offset:-4096 sc1\n"
      "s_add_u32 s54, s54, s55\n"
      "v_mfma_f32_16x16x32_bf16 v[4:7], v[140:143], a[36:39], v[4:7]\n"
      "global_load_lds_dwordx4 v247, s[70:71] offset:3072\n"
      "v_bfe_u32 v224, v255, 4, 1\n"
      "v_mfma_f32_16x16x32_bf16 v[8:11], v[140:143], a[40:43], v[8:11]\n"
      "s_waitcnt lgkmcnt(8)\n"
      "s_xor_b32 s70, s74, 384\n"
      "s_add_u32 s70, s8, s70\n"
      "s_addc_u32 s71, s9, 0\n"
      "s_add_u32 m0, s41, 0\n"
      "v_mfma_f32_16x16x32_bf16 v[12:15], v[140:143], a[44:47], v[12:15]\n"
      "global_load_lds_dwordx4 v244, s[70:71] offset:0\n"
      "v_and_b32 v203, 0xf0, v252\n"
      "v_mfma_f32_16x16x32_bf16 v[16:19], v[140:143], a[48:51], v[16:19]\n"
      "v_lshl_or_b32 v200, v224, 12, v203\n"
      "v_add_u32 v200, s54, v200\n"
      "v_mfma_f32_16x16x32_bf16 v[20:23], v[140:143], a[52:55], v[20:23]\n"
      "global_load_lds_dwordx4 v245, s[70:71] offset:1024\n"
      "v_lshl_or_b32 v203, v224, 14, v203\n"
      "v_mfma_f32_16x16x32_bf16 v[24:27], v[140:143], a[56:59], v[24:27]\n"
      "ds_read_b128 a[144:147], v248 offset:12288\n"
      "ds_read_b128 a[148:151], v248 offset:14336\n"
      "v_mfma_f32_16x16x32_bf16 v[28:31], v[140:143], a[60:63], v[28:31]\n"
      "s_waitcnt vmcnt(21)\n"
      "global_load_lds_dwordx4 v246, s[70:71] offset:2048\n"
      "global_load_dwordx4 v[140:143], v243, s[14:15] offset:-4096 sc1\n"
      "v_mfma_f32_16x16x32_bf16 v[32:35], v[144:147], a[32:35], v[32:35]\n"
      "ds_read_b128 a[176:179], v249 offset:12288\n"
      "ds_read_b128 a[180:183], v249 offset:14336\n"
      "v_mfma_f32_16x16x32_bf16 v[36:39], v[144:147], a[36:39], v[36:39]\n"
      "global_load_lds_dwordx4 v247, s[70:71] offset:3072\n"
      "ds_read_b128 a[152:155], v250 offset:0\n"
      "v_mfma_f32_16x16x32_bf16 v[40:43], v[144:147], a[40:43], v[40:43]\n"
      "s_waitcnt lgkmcnt(9)\n"
      "s_xor_b32 s70, s75, 384\n"
      "s_add_u32 s70, s8, s70\n"
      "s_addc_u32 s71, s9, 0\n"
      "s_add_u32 m0, s41, 4096\n"
      "v_mfma_f32_16x16x32_bf16 v[44:47], v[144:147], a[44:47], v[44:47]\n"
      "global_load_lds_dwordx4 v244, s[70:71] offset:0\n"
      "ds_read_b128 a[156:159], v250 offset:2048\n"
      "v_mfma_f32_16x16x32_bf16 v[48:51], v[144:147], a[48:51], v[48:51]\n"
      "ds_read_b128 a[184:187], v251 offset:0\n"
      "ds_read_b128 a[188:191], v251 offset:2048\n"
      "v_mfma_f32_16x16x32_bf16 v[52:55], v[144:147], a[52:55], v[52:55]\n"
      "global_load_lds_dwordx4 v245, s[70:71] offset:1024\n"
      "ds_read_b128 a[192:195], v250 offset:4096\n"
      "v_mfma_f32_16x16x32_bf16 v[56:59], v[144:147], a[56:59], v[56:59]\n"
      "ds_read_b128 a[196:199], v250 offset:6144\n"
      "ds_read_b128 a[224:227], v251 offset:4096\n"
      "v_mfma_f32_16x16x32_bf16 v[60:63], v[144:147], a[60:63], v[60:63]\n"
      "global_load_lds_dwordx4 v246, s[70:71] offset:2048\n"
      "global_load_dwordx4 v[144:147], v241, s[14:15] offset:-3072 sc1\n"
      "v_mfma_f32_16x16x32_bf16 v[64:67], v[148:151], a[32:35], v[64:67]\n"
      "ds_read_b128 a[228:231], v251 offset:6144\n"
      "s_lshr_b32 s43, s42, 5\n"
      "s_lshl2_add_u32 s44, s36, s33\n"
      "s_cmp_eq_u32 s43, s44\n"
      "s_cbranch_scc0 .Ls_e8_%=\n"
      "s_lshl_b32 s43, s42, 10\n"
      "v_add_u32 v101, s43, v252\n"
      "global_load_dwordx4 v[112:115], v101, s[16:17]\n"
      "global_load_dwordx4 v[116:119], v252, s[24:25]\n"
      ".Ls_e8_%=:\n"
      "v_mfma_f32_16x16x32_bf16 v[68:71], v[148:151], a[36:39], v[68:71]\n"
      "s_lshl2_add_u32 s54, s36, s33\n"
      "global_load_lds_dwordx4 v247, s[70:71] offset:3072\n"
      "s_lshl_b32 s55, s54, 15\n"
      "v_mfma_f32_16x16x32_bf16 v[72:75], v[148:151], a[40:43], v[72:75]\n"
      "s_lshl2_add_u32 s56, s35, s34\n"
      "s_lshl_b32 s56, s56, 6\n"
      "s_add_u32 s55, s55, s56\n"
      "v_mfma_f32_16x16x32_bf16 v[76:79], v[148:151], a[44:47], v[76:79]\n"
      "v_and_b32 v102, 15, v255\n"
      "v_and_b32 v103, 48, v255\n"
      "v_mfma_f32_16x16x32_bf16 v[80:83], v[148:151], a[48:51], v[80:83]\n"
      "v_lshl_or_b32 v103, v102, 10, v103\n"
      "v_add_u32 v103, s55, v103\n"
      "v_mfma_f32_16x16x32_bf16 v[84:87], v[148:151], a[52:55], v[84:87]\n"
      "v_add_u32 v101, 16384, v103\n"
      "global_load_dwordx4 v[104:107], v103, s[16:17]\n"
      "v_mfma_f32_16x16x32_bf16 v[88:91], v[148:151], a[56:59], v[88:91]\n"
      "global_load_dwordx4 v[108:111], v101, s[16:17]\n"
      "s_lshr_b32 s55, s34, 1\n"
      "v_mfma_f32_16x16x32_bf16 v[92:95], v[148:151], a[60:63], v[92:95]\n"
      "s_waitcnt vmcnt(29)\n"
      "global_load_dwordx4 v[148:151], v243, s[14:15] offset:-3072 sc1\n"
      "s_lshl2_add_u32 s55, s55, s35\n"
      "v_mfma_f32_16x16x32_bf16 v[0:3], v[152:155], a[64:67], v[0:3]\n"
      "s_lshl_b32 s55, s55, 15\n"
      "s_lshl_b32 s54, s54, 9\n"
      "s_add_u32 s54, s54, s55\n"
      "v_mfma_f32_16x16x32_bf16 v[4:7], v[152:155], a[68:71], v[4:7]\n"
      "s_add_u32 s54, s54, 786432\n"
      "v_lshl_add_u32 v102, v102, 4, s54\n"
      "global_load_dwordx2 v[204:205], v102, s[58:59] offset:0 sc1\n"
      "v_mfma_f32_16x16x32_bf16 v[8:11], v[152:155], a[72:75], v[8:11]\n"
      "global_load_dwordx2 v[206:207], v102, s[58:59] offset:256 sc1\n"
      "s_add_u32 s68, s20, 16384\n"
      "s_addc_u32 s69, s21, 0\n"
      "v_mfma_f32_16x16x32_bf16 v[12:15], v[152:155], a[76:79], v[12:15]\n"
      "s_ashr_i32 s54, s40, 31\n"
      "s_not_b32 s54, s54\n"
      "s_or_b32 s54, s54, 0x80000000\n"
      "v_mfma_f32_16x16x32_bf16 v[16:19], v[152:155], a[80:83], v[16:19]\n"
      "s_xor_b32 s54, s54, s40\n"
      "s_and_b32 s54, s54, 0xffff0000\n"
      "v_mov_b32 v96, s54\n"
      "v_mfma_f32_16x16x32_bf16 v[20:23], v[152:155], a[84:87], v[20:23]\n"
      "v_mul_f32 v96, 0xbfb8aa3b, v96\n"
      "v_exp_f32 v96, v96\n"
      "v_mfma_f32_16x16x32_bf16 v[24:27], v[152:155], a[88:91], v[24:27]\n"
      "v_add_f32 v96, 1.0, v96\n"
      "v_rcp_f32 v96, v96\n"
      "v_mfma_f32_16x16x32_bf16 v[28:31], v[152:155], a[92:95], v[28:31]\n"
      "global_load_dwordx4 v[152:155], v241, s[14:15] offset:-2048 sc1\n"
      "v_cvt_pk_bf16_f32 v96, v96, v96\n"
      "v_mfma_f32_16x16x32_bf16 v[32:35], v[156:159], a[64:67], v[32:35]\n"
      "s_waitcnt vmcnt(16)\n"
      "ds_read_b128 a[200:203], v250 offset:8192\n"
      "v_mfma_f32_16x16x32_bf16 v[36:39], v[156:159], a[68:71], v[36:39]\n"
      "ds_read_b128 a[204:207], v250 offset:10240\n"
      "v_and_b32 v96, 0xffff0000, v96\n"
      "v_mfma_f32_16x16x32_bf16 v[40:43], v[156:159], a[72:75], v[40:43]\n"
      "ds_read_b128 a[232:235], v251 offset:8192\n"
      "v_readfirstlane_b32 s80, v96\n"
      "v_mfma_f32_16x16x32_bf16 v[44:47], v[156:159], a[76:79], v[44:47]\n"
      "ds_read_b128 a[236:239], v251 offset:10240\n"
      "v_mfma_f32_16x16x32_bf16 v[48:51], v[156:159], a[80:83], v[48:51]\n"
      "v_mfma_f32_16x16x32_bf16 v[52:55], v[156:159], a[84:87], v[52:55]\n"
      "v_mfma_f32_16x16x32_bf16 v[56:59], v[156:159], a[88:91], v[56:59]\n"
      "v_mfma_f32_16x16x32_bf16 v[60:63], v[156:159], a[92:95], v[60:63]\n"
      "global_load_dwordx4 v[156:159], v243, s[14:15] offset:-2048 sc1\n"
      "s_waitcnt vmcnt(12)\n"
      "ds_read_b128 a[208:211], v248 offset:0\n"
      "v_mfma_f32_16x16x32_bf16 v[64:67], v[160:163], a[64:67], v[64:67]\n"
      "ds_read_b128 a[212:215], v248 offset:2048\n"
      "ds_read_b128 a[240:243], v249 offset:0\n"
      "v_mfma_f32_16x16x32_bf16 v[68:71], v[160:163], a[68:71], v[68:71]\n"
      "ds_read_b128 a[244:247], v249 offset:2048\n"
      "v_mfma_f32_16x16x32_bf16 v[72:75], v[160:163], a[72:75], v[72:75]\n"
      "v_mfma_f32_16x16x32_bf16 v[76:79], v[160:163], a[76:79], v[76:79]\n"
      "v_mfma_f32_16x16x32_bf16 v[80:83], v[160:163], a[80:83], v[80:83]\n"
      "v_mfma_f32_16x16x32_bf16 v[84:87], v[160:163], a[84:87], v[84:87]\n"
      "v_mfma_f32_16x16x32_bf16 v[88:91], v[160:163], a[88:91], v[88:91]\n"
      "s_min_u32 s43, s76, 27\n"
      "v_lshrrev_b32 v236, 3, v255\n"
      "v_and_b32 v237, 0x70, v252\n"
      "s_movk_i32 s44, 3584\n"
      "v_mad_u32_u24 v232, v236, s44, v237\n"
      "v_add_u32 v233, 27648, v232\n"
      "v_add_u32 v234, 55296, v232\n"
      "v_add_u32 v235, 82944, v232\n"
      "s_bitcmp1_b32 s33, 0\n"
      "s_cselect_b64 s[70:71], s[28:29], s[26:27]\n"
      "s_mul_i32 s44, s79, 229376\n"
      "s_lshl_b32 s45, s43, 7\n"
      "s_add_u32 s44, s44, s45\n"
      "s_add_u32 s70, s70, s44\n"
      "s_addc_u32 s71, s71, 0\n"
      "s_lshl_b32 s45, s33, 13\n"
      "s_add_u32 m0, s45, 131072\n"
      "v_mfma_f32_16x16x32_bf16 v[92:95], v[160:163], a[92:95], v[92:95]\n"
      "global_load_lds_dwordx4 v232, s[70:71] offset:0\n"
      "s_waitcnt vmcnt(8)\n"
      "ds_read_b128 a[216:219], v248 offset:4096\n"
      "v_mfma_f32_16x16x32_bf16 v[0:3], v[164:167], a[96:99], v[0:3]\n"
      "global_load_lds_dwordx4 v233, s[70:71] offset:1024\n"
      "ds_read_b128 a[220:223], v248 offset:6144\n"
      "v_mfma_f32_16x16x32_bf16 v[4:7], v[164:167], a[100:103], v[4:7]\n"
      "global_load_lds_dwordx4 v234, s[70:71] offset:2048\n"
      "ds_read_b128 a[248:251], v249 offset:4096\n"
      "v_mfma_f32_16x16x32_bf16 v[8:11], v[164:167], a[104:107], v[8:11]\n"
      "global_load_lds_dwordx4 v235, s[70:71] offset:3072\n"
      "ds_read_b128 a[252:255], v249 offset:6144\n"
      "v_mfma_f32_16x16x32_bf16 v[12:15], v[164:167], a[108:111], v[12:15]\n"
      "s_add_u32 s70, s70, 114688\n"
      "s_addc_u32 s71, s71, 0\n"
      "s_add_u32 m0, m0, 4096\n"
      "v_mfma_f32_16x16x32_bf16 v[16:19], v[164:167], a[112:115], v[16:19]\n"
      "global_load_lds_dwordx4 v232, s[70:71] offset:0\n"
      "v_mfma_f32_16x16x32_bf16 v[20:23], v[164:167], a[116:119], v[20:23]\n"
      "global_load_lds_dwordx4 v233, s[70:71] offset:1024\n"
      "v_mfma_f32_16x16x32_bf16 v[24:27], v[164:167], a[120:123], v[24:27]\n"
      "global_load_lds_dwordx4 v234, s[70:71] offset:2048\n"
      "v_mfma_f32_16x16x32_bf16 v[28:31], v[164:167], a[124:127], v[28:31]\n"
      "global_load_lds_dwordx4 v235, s[70:71] offset:3072\n"
      "s_sub_u32 s48, s33, 1\n"
      "v_mfma_f32_16x16x32_bf16 v[32:35], v[168:171], a[96:99], v[32:35]\n"
      "s_and_b32 s48, s48, 3\n"
      "global_load_dword v253, v254, s[58:59] offset:128 sc1\n"
      "s_lshl_b32 s48, s48, 14\n"
      "v_mfma_f32_16x16x32_bf16 v[36:39], v[168:171], a[100:103], v[36:39]\n"
      "v_lshl_add_u32 v244, v255, 2, s48\n"
      "s_sub_u32 s48, s33, 2\n"
      "v_mfma_f32_16x16x32_bf16 v[40:43], v[168:171], a[104:107], v[40:43]\n"
      "s_and_b32 s48, s48, 3\n"
      "s_lshl_b32 s48, s48, 14\n"
      "v_lshl_add_u32 v245, v255, 2, s48\n"
      "v_mfma_f32_16x16x32_bf16 v[44:47], v[168:171], a[108:111], v[44:47]\n"
      "s_sub_u32 s48, s33, 3\n"
      "s_and_b32 s48, s48, 3\n"
      "s_lshl_b32 s48, s48, 14\n"
      "v_mfma_f32_16x16x32_bf16 v[48:51], v[168:171], a[112:115], v[48:51]\n"
      "v_lshl_add_u32 v246, v255, 2, s48\n"
      "v_mfma_f32_16x16x32_bf16 v[52:55], v[168:171], a[116:119], v[52:55]\n"
      "v_mfma_f32_16x16x32_bf16 v[56:59], v[168:171], a[120:123], v[56:59]\n"
      "v_mfma_f32_16x16x32_bf16 v[60:63], v[168:171], a[124:127], v[60:63]\n"
      "v_mfma_f32_16x16x32_bf16 v[64:67], v[172:175], a[96:99], v[64:67]\n"
      "v_mfma_f32_16x16x32_bf16 v[68:71], v[172:175], a[100:103], v[68:71]\n"
      "v_mfma_f32_16x16x32_bf16 v[72:75], v[172:175], a[104:107], v[72:75]\n"
      "v_mfma_f32_16x16x32_bf16 v[76:79], v[172:175], a[108:111], v[76:79]\n"
      "v_mfma_f32_16x16x32_bf16 v[80:83], v[172:175], a[112:115], v[80:83]\n"
      "v_mfma_f32_16x16x32_bf16 v[84:87], v[172:175], a[116:119], v[84:87]\n"
      "v_mfma_f32_16x16x32_bf16 v[88:91], v[172:175], a[120:123], v[88:91]\n"
      "v_mfma_f32_16x16x32_bf16 v[92:95], v[172:175], a[124:127], v[92:95]\n"
      "v_mfma_f32_16x16x32_bf16 v[0:3], v[176:179], a[128:131], v[0:3]\n"
      "v_mfma_f32_16x16x32_bf16 v[4:7], v[176:179], a[132:135], v[4:7]\n"
      "s_waitcnt lgkmcnt(15)\n"
      "v_mfma_f32_16x16x32_bf16 v[8:11], v[176:179], a[136:139], v[8:11]\n"
      "v_mfma_f32_16x16x32_bf16 v[12:15], v[176:179], a[140:143], v[12:15]\n"
      "v_mfma_f32_16x16x32_bf16 v[16:19], v[176:179], a[144:147], v[16:19]\n"
      "v_mfma_f32_16x16x32_bf16 v[20:23], v[176:179], a[148:151], v[20:23]\n"
      "v_mfma_f32_16x16x32_bf16 v[24:27], v[176:179], a[152:155], v[24:27]\n"
      "v_mfma_f32_16x16x32_bf16 v[28:31], v[176:179], a[156:159], v[28:31]\n"
      "v_mfma_f32_16x16x32_bf16 v[32:35], v[180:183], a[128:131], v[32:35]\n"
      "v_mfma_f32_16x16x32_bf16 v[36:39], v[180:183], a[132:135], v[36:39]\n"
      "v_mfma_f32_16x16x32_bf16 v[40:43], v[180:183], a[136:139], v[40:43]\n"
      "v_mfma_f32_16x16x32_bf16 v[44:47], v[180:183], a[140:143], v[44:47]\n"
      "v_mfma_f32_16x16x32_bf16 v[48:51], v[180:183], a[144:147], v[48:51]\n"
      "s_waitcnt vmcnt(11)\n"
      "v_bitop3_b32 v101, v205, v207, s37 bitop3:0x7e\n"
      "v_cmp_ne_u32 vcc, 0, v101\n"
      "s_nop 1\n"
      "s_cbranch_vccnz .Ls_s10_%=\n"
      ".Ls_o11_%=:\n"
      "v_mfma_f32_16x16x32_bf16 v[52:55], v[180:183], a[148:151], v[52:55]\n"
      "s_lshr_b32 s43, s42, 5\n"
      "s_lshl2_add_u32 s44, s36, s33\n"
      "s_cmp_eq_u32 s43, s44\n"
      "s_cbranch_scc0 .Ls_e12_%=\n"
      "v_lshlrev_b32 v96, 16, v112\n"
      "v_lshlrev_b32 v97, 16, v116\n"
      "v_and_b32 v98, 0xffff0000, v112\n"
      "v_and_b32 v99, 0xffff0000, v116\n"
      "v_mul_f32 v96, v96, v97\n"
      "v_mul_f32 v98, v98, v99\n"
      "v_cvt_pk_bf16_f32 v96, v96, v98\n"
      "v_lshlrev_b32 v97, 16, v96\n"
      "v_and_b32 v96, 0xffff0000, v96\n"
      "v_add_f32 v100, v96, v97\n"
      "v_lshlrev_b32 v96, 16, v113\n"
      "v_lshlrev_b32 v97, 16, v117\n"
      "v_and_b32 v98, 0xffff0000, v113\n"
      "v_and_b32 v99, 0xffff0000, v117\n"
      "v_mul_f32 v96, v96, v97\n"
      "v_mul_f32 v98, v98, v99\n"
      "v_cvt_pk_bf16_f32 v96, v96, v98\n"
      "v_lshlrev_b32 v97, 16, v96\n"
      "v_and_b32 v96, 0xffff0000, v96\n"
      "v_add_f32 v96, v96, v97\n"
      "v_add_f32 v100, v100, v96\n"
      "v_lshlrev_b32 v96, 16, v114\n"
      "v_lshlrev_b32 v97, 16, v118\n"
      "v_and_b32 v98, 0xffff0000, v114\n"
      "v_and_b32 v99, 0xffff0000, v118\n"
      "v_mul_f32 v96, v96, v97\n"
      "v_mul_f32 v98, v98, v99\n"
      "v_cvt_pk_bf16_f32 v96, v96, v98\n"
      "v_lshlrev_b32 v97, 16, v96\n"
      "v_and_b32 v96, 0xffff0000, v96\n"
      "v_add_f32 v96, v96, v97\n"
      "v_add_f32 v100, v100, v96\n"
      "v_lshlrev_b32 v96, 16, v115\n"
      "v_lshlrev_b32 v97, 16, v119\n"
      "v_and_b32 v98, 0xffff0000, v115\n"
      "v_and_b32 v99, 0xffff0000, v119\n"
      "v_mul_f32 v96, v96, v97\n"
      "v_mul_f32 v98, v98, v99\n"
      "v_cvt_pk_bf16_f32 v96, v96, v98\n"
      "v_lshlrev_b32 v97, 16, v96\n"
      "v_and_b32 v96, 0xffff0000, v96\n"
      "v_add_f32 v96, v96, v97\n"
      "v_add_f32 v100, v100, v96\n"
      "s_nop 1\n"
      "v_add_f32_dpp v100, v100, v100 quad_perm:[1,0,3,2] row_mask:0xf bank_mask:0xf\n"
      "s_nop 1\n"
      "v_add_f32_dpp v100, v100, v100 quad_perm:[2,3,0,1] row_mask:0xf bank_mask:0xf\n"
      "s_nop 1\n"
      "v_add_f32_dpp v100, v100, v100 row_half_mirror row_mask:0xf bank_mask:0xf\n"
      "s_nop 1\n"
      "v_add_f32_dpp v100, v100, v100 row_mirror row_mask:0xf bank_mask:0xf\n"
      "v_mov_b32 v96, v100\n"
      "s_nop 1\n"
      "v_permlane16_swap_b32 v100, v96\n"
      "v_add_f32 v100, v100, v96\n"
      "v_mov_b32 v96, v100\n"
      "s_nop 1\n"
      "v_permlane32_swap_b32 v100, v96\n"
      "v_add_f32 v100, v100, v96\n"
      "v_cvt_pk_bf16_f32 v100, v100, v100\n"
      "v_and_b32 v100, 0xffff0000, v100\n"
      "v_mul_f32 v100, 0xbfb8aa3b, v100\n"
      "v_exp_f32 v100, v100\n"
      "v_lshrrev_b32 v96, 10, v103\n"
      "v_add_f32 v100, 1.0, v100\n"
      "v_rcp_f32 v100, v100\n"
      "v_subrev_u32 v96, s42, v96\n"
      "v_cvt_pk_bf16_f32 v100, v100, v100\n"
      "v_and_b32 v100, 0xffff0000, v100\n"
      "v_cmp_eq_u32 vcc, 0, v96\n"
      "s_nop 1\n"
      "v_cndmask_b32 v204, v204, v100, vcc\n"
      "v_cmp_eq_u32 vcc, -16, v96\n"
      "s_nop 1\n"
      "v_cndmask_b32 v206, v206, v100, vcc\n"
      ".Ls_e12_%=:\n"
      "v_mfma_f32_16x16x32_bf16 v[56:59], v[180:183], a[152:155], v[56:59]\n"
      "s_lshl_b32 s54, s34, 30\n"
      "s_and_b32 s54, s54, 0x80000000\n"
      "v_sub_f32 v205, 1.0, v204\n"
      "v_mfma_f32_16x16x32_bf16 v[60:63], v[180:183], a[156:159], v[60:63]\n"
      "v_cvt_pk_bf16_f32 v205, v205, v205\n"
      "v_xor_b32 v204, s54, v204\n"
      "v_mfma_f32_16x16x32_bf16 v[64:67], v[184:187], a[128:131], v[64:67]\n"
      "v_and_b32 v205, 0xffff0000, v205\n"
      "v_lshlrev_b32 v208, 16, v104\n"
      "v_mfma_f32_16x16x32_bf16 v[68:71], v[184:187], a[132:135], v[68:71]\n"
      "v_and_b32 v209, 0xffff0000, v104\n"
      "v_mul_f32 v208, v205, v208\n"
      "v_mfma_f32_16x16x32_bf16 v[72:75], v[184:187], a[136:139], v[72:75]\n"
      "v_mul_f32 v209, v205, v209\n"
      "v_lshlrev_b32 v210, 16, v105\n"
      "v_mfma_f32_16x16x32_bf16 v[76:79], v[184:187], a[140:143], v[76:79]\n"
      "v_and_b32 v211, 0xffff0000, v105\n"
      "v_mul_f32 v210, v205, v210\n"
      "v_mfma_f32_16x16x32_bf16 v[80:83], v[184:187], a[144:147], v[80:83]\n"
      "v_mul_f32 v211, v205, v211\n"
      "v_lshlrev_b32 v216, 16, v106\n"
      "v_mfma_f32_16x16x32_bf16 v[84:87], v[184:187], a[148:151], v[84:87]\n"
      "v_and_b32 v217, 0xffff0000, v106\n"
      "v_mul_f32 v216, v205, v216\n"
      "v_mfma_f32_16x16x32_bf16 v[88:91], v[184:187], a[152:155], v[88:91]\n"
      "v_mul_f32 v217, v205, v217\n"
      "v_lshlrev_b32 v218, 16, v107\n"
      "v_mfma_f32_16x16x32_bf16 v[92:95], v[184:187], a[156:159], v[92:95]\n"
      "v_and_b32 v219, 0xffff0000, v107\n"
      "v_mul_f32 v218, v205, v218\n"
      "v_mfma_f32_16x16x32_bf16 v[0:3], v[188:191], a[160:163], v[0:3]\n"
      "v_mul_f32 v219, v205, v219\n"
      "v_sub_f32 v207, 1.0, v206\n"
      "v_mfma_f32_16x16x32_bf16 v[4:7], v[188:191], a[164:167], v[4:7]\n"
      "v_cvt_pk_bf16_f32 v207, v207, v207\n"
      "v_xor_b32 v206, s54, v206\n"
      "v_mfma_f32_16x16x32_bf16 v[8:11], v[188:191], a[168:171], v[8:11]\n"
      "v_and_b32 v207, 0xffff0000, v207\n"
      "v_lshlrev_b32 v212, 16, v108\n"
      "v_mfma_f32_16x16x32_bf16 v[12:15], v[188:191], a[172:175], v[12:15]\n"
      "v_and_b32 v213, 0xffff0000, v108\n"
      "v_mul_f32 v212, v207, v212\n"
      "v_mfma_f32_16x16x32_bf16 v[16:19], v[188:191], a[176:179], v[16:19]\n"
      "v_mul_f32 v213, v207, v213\n"
      "v_lshlrev_b32 v214, 16, v109\n"
      "v_mfma_f32_16x16x32_bf16 v[20:23], v[188:191], a[180:183], v[20:23]\n"
      "v_and_b32 v215, 0xffff0000, v109\n"
      "v_mul_f32 v214, v207, v214\n"
      "v_mfma_f32_16x16x32_bf16 v[24:27], v[188:191], a[184:187], v[24:27]\n"
      "v_mul_f32 v215, v207, v215\n"
      "v_lshlrev_b32 v220, 16, v110\n"
      "v_mfma_f32_16x16x32_bf16 v[28:31], v[188:191], a[188:191], v[28:31]\n"
      "v_and_b32 v221, 0xffff0000, v110\n"
      "v_mul_f32 v220, v207, v220\n"
      "v_mfma_f32_16x16x32_bf16 v[32:35], v[128:131], a[160:163], v[32:35]\n"
      "v_mul_f32 v221, v207, v221\n"
      "v_lshlrev_b32 v222, 16, v111\n"
      "v_mfma_f32_16x16x32_bf16 v[36:39], v[128:131], a[164:167], v[36:39]\n"
      "v_and_b32 v223, 0xffff0000, v111\n"
      "v_mul_f32 v222, v207, v222\n"
      "v_mfma_f32_16x16x32_bf16 v[40:43], v[128:131], a[168:171], v[40:43]\n"
      "v_mul_f32 v223, v207, v223\n"
      "v_mfma_f32_16x16x32_bf16 v[44:47], v[128:131], a[172:175], v[44:47]\n"
      "v_mfma_f32_16x16x32_bf16 v[48:51], v[128:131], a[176:179], v[48:51]\n"
      "v_mfma_f32_16x16x32_bf16 v[52:55], v[128:131], a[180:183], v[52:55]\n"
      "v_mfma_f32_16x16x32_bf16 v[56:59], v[128:131], a[184:187], v[56:59]\n"
      "v_mfma_f32_16x16x32_bf16 v[60:63], v[128:131], a[188:191], v[60:63]\n"
      "v_mfma_f32_16x16x32_bf16 v[64:67], v[132:135], a[160:163], v[64:67]\n"
      "v_mfma_f32_16x16x32_bf16 v[68:71], v[132:135], a[164:167], v[68:71]\n"
      "v_mfma_f32_16x16x32_bf16 v[72:75], v[132:135], a[168:171], v[72:75]\n"
      "s_waitcnt vmcnt(0)\n"
      "v_cmp_ne_u32 vcc, s37, v253\n"
      "s_nop 1\n"
      "s_cbranch_vccnz .Ls_s14_%=\n"
      ".Ls_o15_%=:\n"
      "v_mfma_f32_16x16x32_bf16 v[76:79], v[132:135], a[172:175], v[76:79]\n"
      "v_mfma_f32_16x16x32_bf16 v[80:83], v[132:135], a[176:179], v[80:83]\n"
      "global_load_dwordx4 v[160:163], v240, s[10:11] offset:-1024 sc1\n"
      "global_load_dwordx4 v[164:167], v242, s[10:11] offset:-1024 sc1\n"
      "v_mfma_f32_16x16x32_bf16 v[84:87], v[132:135], a[180:183], v[84:87]\n"
      "global_load_dwordx4 v[168:171], v241, s[10:11] offset:-1024 sc1\n"
      "global_load_dwordx4 v[172:175], v243, s[10:11] offset:-1024 sc1\n"
      "v_mfma_f32_16x16x32_bf16 v[88:91], v[132:135], a[184:187], v[88:91]\n"
      "global_load_dwordx4 v[176:179], v240, s[14:15] offset:-1024 sc1\n"
      "global_load_dwordx4 v[180:183], v242, s[14:15] offset:-1024 sc1\n"
      "v_mfma_f32_16x16x32_bf16 v[92:95], v[132:135], a[188:191], v[92:95]\n"
      "global_load_dwordx4 v[184:187], v241, s[14:15] offset:-1024 sc1\n"
      "global_load_dwordx4 v[188:191], v243, s[14:15] offset:-1024 sc1\n"
      "v_mfma_f32_16x16x32_bf16 v[0:3], v[136:139], a[192:195], v[0:3]\n"
      "global_load_dwordx4 v[128:131], v240, s[10:11] offset:0 sc1\n"
      "global_load_dwordx4 v[132:135], v242, s[10:11] offset:0 sc1\n"
      "s_waitcnt lgkmcnt(14)\n"
      "v_mfma_f32_16x16x32_bf16 v[4:7], v[136:139], a[196:199], v[4:7]\n"
      "s_waitcnt lgkmcnt(11)\n"
      "v_mfma_f32_16x16x32_bf16 v[8:11], v[136:139], a[200:203], v[8:11]\n"
      "s_waitcnt lgkmcnt(10)\n"
      "v_mfma_f32_16x16x32_bf16 v[12:15], v[136:139], a[204:207], v[12:15]\n"
      "s_waitcnt lgkmcnt(7)\n"
      "v_mfma_f32_16x16x32_bf16 v[16:19], v[136:139], a[208:211], v[16:19]\n"
      "s_waitcnt lgkmcnt(6)\n"
      "v_mfma_f32_16x16x32_bf16 v[20:23], v[136:139], a[212:215], v[20:23]\n"
      "ds_read_b64_tr_b16 v[224:225], v202 offset:0\n"
      "ds_read_b64_tr_b16 v[226:227], v202 offset:512\n"
      "s_waitcnt lgkmcnt(5)\n"
      "v_mfma_f32_16x16x32_bf16 v[24:27], v[136:139], a[216:219], v[24:27]\n"
      "ds_read_b64_tr_b16 v[228:229], v202 offset:4096\n"
      "ds_read_b64_tr_b16 v[230:231], v202 offset:4608\n"
      "s_waitcnt lgkmcnt(6)\n"
      "v_mfma_f32_16x16x32_bf16 v[28:31], v[136:139], a[220:223], v[28:31]\n"
      "global_load_dwordx4 v[136:139], v241, s[10:11] offset:0 sc1\n"
      "ds_read_b64_tr_b16 v[232:233], v202 offset:64\n"
      "v_mfma_f32_16x16x32_bf16 v[0:3], v[140:143], a[224:227], v[0:3]\n"
      "ds_read_b64_tr_b16 v[234:235], v202 offset:576\n"
      "ds_read_b64_tr_b16 v[236:237], v202 offset:4160\n"
      "v_mfma_f32_16x16x32_bf16 v[4:7], v[140:143], a[228:231], v[4:7]\n"
      "ds_read_b64_tr_b16 v[238:239], v202 offset:4672\n"
      "v_mfma_f32_16x16x32_bf16 v[8:11], v[140:143], a[232:235], v[8:11]\n"
      "v_mfma_f32_16x16x32_bf16 v[12:15], v[140:143], a[236:239], v[12:15]\n"
      "v_mfma_f32_16x16x32_bf16 v[16:19], v[140:143], a[240:243], v[16:19]\n"
      "s_waitcnt lgkmcnt(6)\n"
      "v_mfma_f32_16x16x32_bf16 v[248:251], v[192:195], v[224:227], 0\n"
      "v_mfma_f32_16x16x32_bf16 v[20:23], v[140:143], a[244:247], v[20:23]\n"
      "s_waitcnt lgkmcnt(4)\n"
      "v_mfma_f32_16x16x32_bf16 v[248:251], v[196:199], v[228:231], v[248:251]\n"
      "v_mfma_f32_16x16x32_bf16 v[24:27], v[140:143], a[248:251], v[24:27]\n"
      "s_waitcnt lgkmcnt(2)\n"
      "v_mfma_f32_16x16x32_bf16 v[224:227], v[192:195], v[232:235], 0\n"
      "v_mfma_f32_16x16x32_bf16 v[28:31], v[140:143], a[252:255], v[28:31]\n"
      "global_load_dwordx4 v[140:143], v243, s[10:11] offset:0 sc1\n"
      "s_waitcnt lgkmcnt(0)\n"
      "v_mfma_f32_16x16x32_bf16 v[224:227], v[196:199], v[236:239], v[224:227]\n"
      "v_mfma_f32_16x16x32_bf16 v[32:35], v[144:147], a[192:195], v[32:35]\n"
      "v_mfma_f32_16x16x32_bf16 v[36:39], v[144:147], a[196:199], v[36:39]\n"
      "s_add_u32 m0, s41, 0\n"
      "s_nop 0\n"
      "ds_write_addtid_b32 v8 offset:0\n"
      "v_mfma_f32_16x16x32_bf16 v[40:43], v[144:147], a[200:203], v[40:43]\n"
      "ds_write_addtid_b32 v9 offset:256\n"
      "s_nop 0\n"
      "v_cndmask_b32 v247, v248, v224, s[64:65]\n"
      "v_mfma_f32_16x16x32_bf16 v[44:47], v[144:147], a[204:207], v[44:47]\n"
      "ds_read_b64_tr_b16 v[224:225], v202 offset:32\n"
      "ds_read_b64_tr_b16 v[226:227], v202 offset:544\n"
      "v_mfma_f32_16x16x32_bf16 v[48:51], v[144:147], a[208:211], v[48:51]\n"
      "ds_read_b64_tr_b16 v[228:229], v202 offset:4128\n"
      "ds_read_b64_tr_b16 v[230:231], v202 offset:4640\n"
      "v_mfma_f32_16x16x32_bf16 v[52:55], v[144:147], a[212:215], v[52:55]\n"
      "ds_read_b64_tr_b16 v[232:233], v202 offset:96\n"
      "ds_read_b64_tr_b16 v[234:235], v202 offset:608\n"
      "v_mfma_f32_16x16x32_bf16 v[56:59], v[144:147], a[216:219], v[56:59]\n"
      "ds_read_b64_tr_b16 v[236:237], v202 offset:4192\n"
      "ds_read_b64_tr_b16 v[238:239], v202 offset:4704\n"
      "v_mfma_f32_16x16x32_bf16 v[60:63], v[144:147], a[220:223], v[60:63]\n"
      "global_load_dwordx4 v[144:147], v240, s[14:15] offset:0 sc1\n"
      "ds_write_addtid_b32 v10 offset:512\n"
      "v_mfma_f32_16x16x32_bf16 v[32:35], v[148:151], a[224:227], v[32:35]\n"
      "ds_write_addtid_b32 v11 offset:768\n"
      "ds_write_addtid_b32 v12 offset:1024\n"
      "v_mfma_f32_16x16x32_bf16 v[36:39], v[148:151], a[228:231], v[36:39]\n"
      "s_waitcnt lgkmcnt(9)\n"
      "v_mfma_f32_16x16x32_bf16 v[248:251], v[192:195], v[224:227], 0\n"
      "v_mfma_f32_16x16x32_bf16 v[40:43], v[148:151], a[232:235], v[40:43]\n"
      "s_waitcnt lgkmcnt(7)\n"
      "v_mfma_f32_16x16x32_bf16 v[248:251], v[196:199], v[228:231], v[248:251]\n"
      "v_mfma_f32_16x16x32_bf16 v[44:47], v[148:151], a[236:239], v[44:47]\n"
      "s_waitcnt lgkmcnt(5)\n"
      "v_mfma_f32_16x16x32_bf16 v[224:227], v[192:195], v[232:235], 0\n"
      "v_mfma_f32_16x16x32_bf16 v[48:51], v[148:151], a[240:243], v[48:51]\n"
      "s_waitcnt lgkmcnt(3)\n"
      "v_mfma_f32_16x16x32_bf16 v[224:227], v[196:199], v[236:239], v[224:227]\n"
      "v_mfma_f32_16x16x32_bf16 v[52:55], v[148:151], a[244:247], v[52:55]\n"
      "ds_write_addtid_b32 v13 offset:1280\n"
      "ds_write_addtid_b32 v14 offset:1536\n"
      "v_mfma_f32_16x16x32_bf16 v[56:59], v[148:151], a[248:251], v[56:59]\n"
      "ds_write_addtid_b32 v15 offset:1792\n"
      "ds_write_addtid_b32 v16 offset:2048\n"
      "v_mfma_f32_16x16x32_bf16 v[60:63], v[148:151], a[252:255], v[60:63]\n"
      "global_load_dwordx4 v[148:151], v242, s[14:15] offset:0 sc1\n"
      "v_cndmask_b32 v250, v248, v224, s[64:65]\n"
      "v_mfma_f32_16x16x32_bf16 v[64:67], v[152:155], a[192:195], v[64:67]\n"
      "ds_write_addtid_b32 v17 offset:2304\n"
      "v_and_b32 v248, s78, v247\n"
      "v_mfma_f32_16x16x32_bf16 v[68:71], v[152:155], a[196:199], v[68:71]\n"
      "ds_write_addtid_b32 v18 offset:2560\n"
      "v_mov_b32 v249, s37\n"
      "v_mfma_f32_16x16x32_bf16 v[72:75], v[152:155], a[200:203], v[72:75]\n"
      "ds_write_addtid_b32 v19 offset:2816\n"
      "v_and_b32 v250, s78, v250\n"
      "v_mfma_f32_16x16x32_bf16 v[76:79], v[152:155], a[204:207], v[76:79]\n"
      "ds_write_addtid_b32 v20 offset:3072\n"
      "v_mov_b32 v251, s37\n"
      "v_mfma_f32_16x16x32_bf16 v[80:83], v[152:155], a[208:211], v[80:83]\n"
      "s_mov_b64 exec, s[66:67]\n"
      "global_store_dwordx4 v200, v[248:251], s[58:59] sc1\n"
      "s_mov_b64 exec, -1\n"
      "v_mfma_f32_16x16x32_bf16 v[84:87], v[152:155], a[212:215], v[84:87]\n"
      "s_add_u32 s48, s34, 1\n"
      "s_mov_b32 s81, m0\n"
      "s_min_u32 s43, s76, 27\n"
      "v_lshrrev_b32 v236, 3, v255\n"
      "v_and_b32 v237, 0x70, v252\n"
      "v_lshl_or_b32 v232, v236, 10, v237\n"
      "v_add_u32 v233, 7168, v232\n"
      "v_add_u32 v234, 14336, v232\n"
      "v_add_u32 v235, 21504, v232\n"
      "s_lshr_b32 s44, s33, 1\n"
      "s_lshl1_add_u32 s44, s43, s44\n"
      "s_lshl_b32 s44, s44, 15\n"
      "s_and_b32 s45, s33, 1\n"
      "s_lshl1_add_u32 s45, s77, s45\n"
      "s_lshl_b32 s45, s45, 7\n"
      "s_add_u32 s44, s44, s45\n"
      "s_add_u32 s70, s30, s44\n"
      "s_addc_u32 s71, s31, 0\n"
      "s_add_u32 m0, s41, 77824\n"
      "s_nop 0\n"
      "global_load_lds_dwordx4 v232, s[70:71] offset:0\n"
      "global_load_lds_dwordx4 v233, s[70:71] offset:1024\n"
      "global_load_lds_dwordx4 v234, s[70:71] offset:2048\n"
      "global_load_lds_dwordx4 v235, s[70:71] offset:3072\n"
      "s_mov_b32 m0, s81\n"
      "v_mfma_f32_16x16x32_bf16 v[88:91], v[152:155], a[216:219], v[88:91]\n"
      "s_lshl2_add_u32 s54, s36, s33\n"
      "ds_write_addtid_b32 v21 offset:3328\n"
      "s_lshl_b32 s55, s54, 15\n"
      "v_mfma_f32_16x16x32_bf16 v[92:95], v[152:155], a[220:223], v[92:95]\n"
      "s_lshl2_add_u32 s56, s35, s34\n"
      "global_load_dwordx4 v[152:155], v241, s[14:15] offset:0 sc1\n"
      "s_lshl_b32 s56, s56, 6\n"
      "v_mfma_f32_16x16x32_bf16 v[64:67], v[156:159], a[224:227], v[64:67]\n"
      "s_add_u32 s55, s55, s56\n"
      "ds_write_addtid_b32 v22 offset:3584\n"
      "v_and_b32 v248, 15, v255\n"
      "v_mfma_f32_16x16x32_bf16 v[68:71], v[156:159], a[228:231], v[68:71]\n"
      "s_and_b32 s48, s48, 3\n"
      "ds_write_addtid_b32 v23 offset:3840\n"
      "v_and_b32 v251, 48, v255\n"
      "v_mfma_f32_16x16x32_bf16 v[72:75], v[156:159], a[232:235], v[72:75]\n"
      "s_mul_i32 s48, s48, 36864\n"
      "ds_write_addtid_b32 v24 offset:4096\n"
      "v_lshl_or_b32 v251, v248, 10, v251\n"
      "v_mfma_f32_16x16x32_bf16 v[76:79], v[156:159], a[236:239], v[76:79]\n"
      "s_add_u32 s48, s48, 0\n"
      "ds_write_addtid_b32 v25 offset:4352\n"
      "v_add_u32 v251, s55, v251\n"
      "v_mfma_f32_16x16x32_bf16 v[80:83], v[156:159], a[240:243], v[80:83]\n"
      "s_add_u32 s52, s60, s48\n"
      "s_addc_u32 s53, s61, 0\n"
      "ds_write_addtid_b32 v26 offset:4608\n"
      "v_mfma_f32_16x16x32_bf16 v[84:87], v[156:159], a[244:247], v[84:87]\n"
      "ds_write_addtid_b32 v27 offset:4864\n"
      "v_add_u32 v248, 0x10000, v244\n"
      "v_mfma_f32_16x16x32_bf16 v[88:91], v[156:159], a[248:251], v[88:91]\n"
      "ds_write_addtid_b32 v28 offset:5120\n"
      "v_add_u32 v249, 0x10000, v245\n"
      "v_mfma_f32_16x16x32_bf16 v[92:95], v[156:159], a[252:255], v[92:95]\n"
      "s_waitcnt vmcnt(18)\n"
      "global_load_dwordx4 v[156:159], v243, s[14:15] offset:0 sc1\n"
      "v_add_u32 v250, 0x10000, v246\n"
      "v_mfma_f32_16x16x32_bf16 v[96:99], v[160:163], a[0:3], 0\n"
      "s_sub_u32 s54, s34, 1\n"
      "ds_write_addtid_b32 v29 offset:5376\n"
      "s_xor_b32 s54, s54, s34\n"
      "v_mfma_f32_16x16x32_bf16 v[100:103], v[160:163], a[4:7], 0\n"
      "s_lshl_b32 s54, s54, 30\n"
      "ds_write_addtid_b32 v30 offset:5632\n"
      "s_and_b32 s72, s54, 0x80000000\n"
      "v_mfma_f32_16x16x32_bf16 v[104:107], v[160:163], a[8:11], 0\n"
      "s_sub_u32 s54, s34, 2\n"
      "ds_write_addtid_b32 v31 offset:5888\n"
      "s_xor_b32 s54, s54, s34\n"
      "v_mfma_f32_16x16x32_bf16 v[108:111], v[160:163], a[12:15], 0\n"
      "s_lshl_b32 s54, s54, 30\n"
      "s_and_b32 s73, s54, 0x80000000\n"
      "s_sub_u32 s54, s34, 3\n"
      "v_mfma_f32_16x16x32_bf16 v[112:115], v[160:163], a[16:19], 0\n"
      "s_xor_b32 s54, s54, s34\n"
      "s_lshl_b32 s54, s54, 30\n"
      "s_and_b32 s74, s54, 0x80000000\n"
      "v_mfma_f32_16x16x32_bf16 v[116:119], v[160:163], a[20:23], 0\n"
      "s_lshr_b32 s54, s33, 1\n"
      "s_lshl1_add_u32 s54, s76, s54\n"
      "s_lshl_b32 s54, s54, 12\n"
      "v_mfma_f32_16x16x32_bf16 v[120:123], v[160:163], a[24:27], 0\n"
      "s_add_u32 s54, s54, 262144\n"
      "v_add_u32 v200, s54, v252\n"
      "s_add_u32 s54, s41, 77824\n"
      "v_mfma_f32_16x16x32_bf16 v[124:127], v[160:163], a[28:31], 0\n"
      "s_add_u32 m0, s41, 0\n"
      "global_load_dwordx4 v[160:163], v240, s[10:11] offset:1024 sc1\n"
      "v_add_u32 v201, s54, v201\n"
      "v_mfma_f32_16x16x32_bf16 v[96:99], v[164:167], a[32:35], v[96:99]\n"
      "ds_write_addtid_b32 v40 offset:6144\n"
      "v_and_b32 v202, 15, v255\n"
      "v_mfma_f32_16x16x32_bf16 v[100:103], v[164:167], a[36:39], v[100:103]\n"
      "ds_write_addtid_b32 v41 offset:6400\n"
      "v_lshrrev_b32 v199, 5, v255\n"
      "v_mfma_f32_16x16x32_bf16 v[104:107], v[164:167], a[40:43], v[104:107]\n"
      "ds_write_addtid_b32 v42 offset:6656\n"
      "v_lshl_or_b32 v202, v199, 4, v202\n"
      "v_mfma_f32_16x16x32_bf16 v[108:111], v[164:167], a[44:47], v[108:111]\n"
      "s_add_u32 s54, s41, 12288\n"
      "ds_write_addtid_b32 v43 offset:6912\n"
      "v_lshl_add_u32 v202, v202, 1, s54\n"
      "v_mfma_f32_16x16x32_bf16 v[112:115], v[164:167], a[48:51], v[112:115]\n"
      "ds_write_addtid_b32 v44 offset:7168\n"
      "v_and_b32 v199, 48, v255\n"
      "v_mfma_f32_16x16x32_bf16 v[116:119], v[164:167], a[52:55], v[116:119]\n"
      "ds_write_addtid_b32 v45 offset:7424\n"
      "v_add_u32 v199, s54, v199\n"
      "v_mfma_f32_16x16x32_bf16 v[120:123], v[164:167], a[56:59], v[120:123]\n"
      "s_and_b32 s54, s33, 1\n"
      "ds_write_addtid_b32 v46 offset:7680\n"
      "s_lshl1_add_u32 s54, s77, s54\n"
      "v_mfma_f32_16x16x32_bf16 v[124:127], v[164:167], a[60:63], v[124:127]\n"
      "s_waitcnt vmcnt(18)\n"
      "global_load_dwordx4 v[164:167], v242, s[10:11] offset:1024 sc1\n"
      "s_lshl_b32 s54, s54, 15\n"
      "v_mfma_f32_16x16x32_bf16 v[96:99], v[168:171], a[64:67], v[96:99]\n"
      "s_lshr_b32 s55, s33, 1\n"
      "ds_write_addtid_b32 v47 offset:7936\n"
      "s_lshl1_add_u32 s55, s76, s55\n"
      "v_mfma_f32_16x16x32_bf16 v[100:103], v[168:171], a[68:71], v[100:103]\n"
      "s_lshl_b32 s55, s55, 8\n"
      "ds_write_addtid_b32 v48 offset:8192\n"
      "s_add_u32 s54, s54, s55\n"
      "v_mfma_f32_16x16x32_bf16 v[104:107], v[168:171], a[72:75], v[104:107]\n"
      "s_add_u32 s54, s54, 524288\n"
      "ds_write_addtid_b32 v49 offset:8448\n"
      "v_add_u32 v203, s54, v203\n"
      "v_mfma_f32_16x16x32_bf16 v[108:111], v[168:171], a[76:79], v[108:111]\n"
      "ds_write_addtid_b32 v50 offset:8704\n"
      "ds_write_addtid_b32 v51 offset:8960\n"
      "v_mfma_f32_16x16x32_bf16 v[112:115], v[168:171], a[80:83], v[112:115]\n"
      "ds_write_addtid_b32 v52 offset:9216\n"
      "ds_write_addtid_b32 v53 offset:9472\n"
      "v_mfma_f32_16x16x32_bf16 v[116:119], v[168:171], a[84:87], v[116:119]\n"
      "ds_write_addtid_b32 v54 offset:9728\n"
      "ds_write_addtid_b32 v55 offset:9984\n"
      "v_mfma_f32_16x16x32_bf16 v[120:123], v[168:171], a[88:91], v[120:123]\n"
      "ds_write_addtid_b32 v56 offset:10240\n"
      "ds_write_addtid_b32 v57 offset:10496\n"
      "v_mfma_f32_16x16x32_bf16 v[124:127], v[168:171], a[92:95], v[124:127]\n"
      "global_load_dwordx4 v[168:171], v241, s[10:11] offset:1024 sc1\n"
      "ds_write_addtid_b32 v58 offset:10752\n"
      "v_mfma_f32_16x16x32_bf16 v[96:99], v[172:175], a[96:99], v[96:99]\n"
      "ds_write_addtid_b32 v59 offset:11008\n"
      "ds_write_addtid_b32 v60 offset:11264\n"
      "v_mfma_f32_16x16x32_bf16 v[100:103], v[172:175], a[100:103], v[100:103]\n"
      "ds_write_addtid_b32 v61 offset:11520\n"
      "ds_write_addtid_b32 v62 offset:11776\n"
      "v_mfma_f32_16x16x32_bf16 v[104:107], v[172:175], a[104:107], v[104:107]\n"
      "ds_write_addtid_b32 v63 offset:12032\n"
      "v_mfma_f32_16x16x32_bf16 v[108:111], v[172:175], a[108:111], v[108:111]\n"
      "v_mfma_f32_16x16x32_bf16 v[112:115], v[172:175], a[112:115], v[112:115]\n"
      "v_mfma_f32_16x16x32_bf16 v[116:119], v[172:175], a[116:119], v[116:119]\n"
      "s_waitcnt lgkmcnt(0)\n"
      "s_barrier\n"
      "v_mfma_f32_16x16x32_bf16 v[120:123], v[172:175], a[120:123], v[120:123]\n"
      "ds_read2st64_b32 v[8:9], v244 offset0:0 offset1:1\n"
      "ds_read2st64_b32 v[10:11], v244 offset0:2 offset1:3\n"
      "v_mfma_f32_16x16x32_bf16 v[124:127], v[172:175], a[124:127], v[124:127]\n"
      "s_waitcnt vmcnt(18)\n"
      "global_load_dwordx4 v[172:175], v243, s[10:11] offset:1024 sc1\n"
      "ds_read2st64_b32 v[12:13], v244 offset0:4 offset1:5\n"
      "v_mfma_f32_16x16x32_bf16 v[96:99], v[176:179], a[128:131], v[96:99]\n"
      "ds_read2st64_b32 v[14:15], v244 offset0:6 offset1:7\n"
      "ds_read2st64_b32 v[16:17], v245 offset0:8 offset1:9\n"
      "v_mfma_f32_16x16x32_bf16 v[100:103], v[176:179], a[132:135], v[100:103]\n"
      "ds_read2st64_b32 v[18:19], v245 offset0:10 offset1:11\n"
      "ds_read2st64_b32 v[20:21], v245 offset0:12 offset1:13\n"
      "v_mfma_f32_16x16x32_bf16 v[104:107], v[176:179], a[136:139], v[104:107]\n"
      "ds_read2st64_b32 v[22:23], v245 offset0:14 offset1:15\n"
      "ds_read2st64_b32 v[24:25], v246 offset0:16 offset1:17\n"
      "v_mfma_f32_16x16x32_bf16 v[108:111], v[176:179], a[140:143], v[108:111]\n"
      "ds_read2st64_b32 v[26:27], v246 offset0:18 offset1:19\n"
      "ds_read2st64_b32 v[28:29], v246 offset0:20 offset1:21\n"
      "v_mfma_f32_16x16x32_bf16 v[112:115], v[176:179], a[144:147], v[112:115]\n"
      "ds_read2st64_b32 v[30:31], v246 offset0:22 offset1:23\n"
      "ds_read2st64_b32 v[40:41], v244 offset0:24 offset1:25\n"
      "v_mfma_f32_16x16x32_bf16 v[116:119], v[176:179], a[148:151], v[116:119]\n"
      "ds_read2st64_b32 v[42:43], v244 offset0:26 offset1:27\n"
      "ds_read2st64_b32 v[44:45], v244 offset0:28 offset1:29\n"
      "v_mfma_f32_16x16x32_bf16 v[120:123], v[176:179], a[152:155], v[120:123]\n"
      "ds_read2st64_b32 v[46:47], v244 offset0:30 offset1:31\n"
      "v_mfma_f32_16x16x32_bf16 v[124:127], v[176:179], a[156:159], v[124:127]\n"
      "s_waitcnt lgkmcnt(12)\n"
      "global_load_dwordx4 v[176:179], v240, s[14:15] offset:1024 sc1\n"
      "v_add_f32 v0, v0, v8\n"
      "v_mfma_f32_16x16x32_bf16 v[96:99], v[180:183], a[160:163], v[96:99]\n"
      "ds_read2st64_b32 v[48:49], v245 offset0:32 offset1:33\n"
      "v_add_f32 v1, v1, v9\n"
      "v_mfma_f32_16x16x32_bf16 v[100:103], v[180:183], a[164:167], v[100:103]\n"
      "ds_read2st64_b32 v[50:51], v245 offset0:34 offset1:35\n"
      "v_add_f32 v2, v2, v10\n"
      "v_mfma_f32_16x16x32_bf16 v[104:107], v[180:183], a[168:171], v[104:107]\n"
      "ds_read2st64_b32 v[52:53], v245 offset0:36 offset1:37\n"
      "v_add_f32 v3, v3, v11\n"
      "v_mfma_f32_16x16x32_bf16 v[108:111], v[180:183], a[172:175], v[108:111]\n"
      "ds_read2st64_b32 v[54:55], v245 offset0:38 offset1:39\n"
      "v_add_f32 v4, v4, v12\n"
      "v_mfma_f32_16x16x32_bf16 v[112:115], v[180:183], a[176:179], v[112:115]\n"
      "ds_read2st64_b32 v[56:57], v246 offset0:40 offset1:41\n"
      "v_add_f32 v5, v5, v13\n"
      "v_mfma_f32_16x16x32_bf16 v[116:119], v[180:183], a[180:183], v[116:119]\n"
      "ds_read2st64_b32 v[58:59], v246 offset0:42 offset1:43\n"
      "v_add_f32 v6, v6, v14\n"
      "v_mfma_f32_16x16x32_bf16 v[120:123], v[180:183], a[184:187], v[120:123]\n"
      "ds_read2st64_b32 v[60:61], v246 offset0:44 offset1:45\n"
      "v_add_f32 v7, v7, v15\n"
      "v_mfma_f32_16x16x32_bf16 v[124:127], v[180:183], a[188:191], v[124:127]\n"
      "s_waitcnt vmcnt(18)\n"
      "global_load_dwordx4 v[180:183], v242, s[14:15] offset:1024 sc1\n"
      "s_waitcnt lgkmcnt(15)\n"
      "v_mfma_f32_16x16x32_bf16 v[96:99], v[184:187], a[192:195], v[96:99]\n"
      "ds_read2st64_b32 v[62:63], v246 offset0:46 offset1:47\n"
      "v_add_f32 v0, v0, v16\n"
      "v_mfma_f32_16x16x32_bf16 v[100:103], v[184:187], a[196:199], v[100:103]\n"
      "s_add_u32 m0, s41, 12288\n"
      "s_nop 0\n"
      "ds_write_addtid_b32 v72 offset:53248\n"
      "v_mfma_f32_16x16x32_bf16 v[104:107], v[184:187], a[200:203], v[104:107]\n"
      "ds_write_addtid_b32 v73 offset:53504\n"
      "v_add_f32 v1, v1, v17\n"
      "v_mfma_f32_16x16x32_bf16 v[108:111], v[184:187], a[204:207], v[108:111]\n"
      "ds_write_addtid_b32 v74 offset:53760\n"
      "v_add_f32 v2, v2, v18\n"
      "v_mfma_f32_16x16x32_bf16 v[112:115], v[184:187], a[208:211], v[112:115]\n"
      "ds_write_addtid_b32 v75 offset:54016\n"
      "v_add_f32 v3, v3, v19\n"
      "v_mfma_f32_16x16x32_bf16 v[116:119], v[184:187], a[212:215], v[116:119]\n"
      "ds_write_addtid_b32 v76 offset:54272\n"
      "v_add_f32 v4, v4, v20\n"
      "v_mfma_f32_16x16x32_bf16 v[120:123], v[184:187], a[216:219], v[120:123]\n"
      "ds_write_addtid_b32 v77 offset:54528\n"
      "v_add_f32 v5, v5, v21\n"
      "v_mfma_f32_16x16x32_bf16 v[124:127], v[184:187], a[220:223], v[124:127]\n"
      "global_load_dwordx4 v[184:187], v241, s[14:15] offset:1024 sc1\n"
      "v_add_f32 v6, v6, v22\n"
      "v_mfma_f32_16x16x32_bf16 v[96:99], v[188:191], a[224:227], v[96:99]\n"
      "ds_write_addtid_b32 v78 offset:54784\n"
      "v_add_f32 v7, v7, v23\n"
      "v_mfma_f32_16x16x32_bf16 v[100:103], v[188:191], a[228:231], v[100:103]\n"
      "s_waitcnt lgkmcnt(15)\n"
      "ds_write_addtid_b32 v79 offset:55040\n"
      "v_add_f32 v0, v0, v24\n"
      "v_mfma_f32_16x16x32_bf16 v[104:107], v[188:191], a[232:235], v[104:107]\n"
      "ds_write_addtid_b32 v80 offset:55296\n"
      "v_add_f32 v1, v1, v25\n"
      "v_mfma_f32_16x16x32_bf16 v[108:111], v[188:191], a[236:239], v[108:111]\n"
      "ds_write_addtid_b32 v81 offset:55552\n"
      "v_add_f32 v2, v2, v26\n"
      "v_mfma_f32_16x16x32_bf16 v[112:115], v[188:191], a[240:243], v[112:115]\n"
      "ds_write_addtid_b32 v82 offset:55808\n"
      "v_add_f32 v3, v3, v27\n"
      "v_mfma_f32_16x16x32_bf16 v[116:119], v[188:191], a[244:247], v[116:119]\n"
      "ds_write_addtid_b32 v83 offset:56064\n"
      "v_add_f32 v4, v4, v28\n"
      "v_mfma_f32_16x16x32_bf16 v[120:123], v[188:191], a[248:251], v[120:123]\n"
      "ds_write_addtid_b32 v84 offset:56320\n"
      "v_add_f32 v5, v5, v29\n"
      "v_mfma_f32_16x16x32_bf16 v[124:127], v[188:191], a[252:255], v[124:127]\n"
      "s_waitcnt vmcnt(18)\n"
      "global_load_dwordx4 v[188:191], v243, s[14:15] offset:1024 sc1\n"
      "v_add_f32 v6, v6, v30\n"
      "v_add_f32 v7, v7, v31\n"
      "v_cvt_pk_bf16_f32 v8, v0, v1\n"
      "v_cvt_pk_bf16_f32 v9, v2, v3\n"
      "v_cvt_pk_bf16_f32 v10, v4, v5\n"
      "v_mov_b32 v11, s37\n"
      "v_cvt_pk_bf16_f32 v247, v6, v7\n"
      "global_store_dwordx4 v252, v[8:11], s[52:53] offset:0 sc1\n"
      "v_mfma_f32_16x16x32_bf16 v[0:3], v[128:131], a[0:3], 0\n"
      "ds_write_addtid_b32 v85 offset:56576\n"
      "v_add_f32 v32, v32, v40\n"
      "v_mfma_f32_16x16x32_bf16 v[4:7], v[128:131], a[4:7], 0\n"
      "ds_write_addtid_b32 v86 offset:56832\n"
      "v_add_f32 v33, v33, v41\n"
      "v_mfma_f32_16x16x32_bf16 v[8:11], v[128:131], a[8:11], 0\n"
      "ds_write_addtid_b32 v87 offset:57088\n"
      "v_add_f32 v34, v34, v42\n"
      "v_mfma_f32_16x16x32_bf16 v[12:15], v[128:131], a[12:15], 0\n"
      "ds_write_addtid_b32 v88 offset:57344\n"
      "v_add_f32 v35, v35, v43\n"
      "v_mfma_f32_16x16x32_bf16 v[16:19], v[128:131], a[16:19], 0\n"
      "ds_write_addtid_b32 v89 offset:57600\n"
      "v_add_f32 v36, v36, v44\n"
      "v_mfma_f32_16x16x32_bf16 v[20:23], v[128:131], a[20:23], 0\n"
      "ds_write_addtid_b32 v90 offset:57856\n"
      "v_add_f32 v37, v37, v45\n"
      "v_mfma_f32_16x16x32_bf16 v[24:27], v[128:131], a[24:27], 0\n"
      "ds_write_addtid_b32 v91 offset:58112\n"
      "v_add_f32 v38, v38, v46\n"
      "v_mfma_f32_16x16x32_bf16 v[28:31], v[128:131], a[28:31], 0\n"
      "global_load_dwordx4 v[128:131], v240, s[10:11] offset:2048 sc1\n"
      "v_add_f32 v39, v39, v47\n"
      "v_mfma_f32_16x16x32_bf16 v[0:3], v[132:135], a[32:35], v[0:3]\n"
      "s_waitcnt lgkmcnt(15)\n"
      "ds_write_addtid_b32 v92 offset:58368\n"
      "v_add_f32 v32, v32, v48\n"
      "v_mfma_f32_16x16x32_bf16 v[4:7], v[132:135], a[36:39], v[4:7]\n"
      "ds_write_addtid_b32 v93 offset:58624\n"
      "v_add_f32 v33, v33, v49\n"
      "v_mfma_f32_16x16x32_bf16 v[8:11], v[132:135], a[40:43], v[8:11]\n"
      "ds_write_addtid_b32 v94 offset:58880\n"
      "v_add_f32 v34, v34, v50\n"
      "v_mfma_f32_16x16x32_bf16 v[12:15], v[132:135], a[44:47], v[12:15]\n"
      "ds_write_addtid_b32 v95 offset:59136\n"
      "v_add_f32 v35, v35, v51\n"
      "v_mfma_f32_16x16x32_bf16 v[16:19], v[132:135], a[48:51], v[16:19]\n"
      "s_add_u32 m0, s41, 12288\n"
      "s_nop 0\n"
      "ds_write_addtid_b32 v104 offset:59392\n"
      "v_mfma_f32_16x16x32_bf16 v[20:23], v[132:135], a[52:55], v[20:23]\n"
      "ds_write_addtid_b32 v105 offset:59648\n"
      "v_add_f32 v36, v36, v52\n"
      "v_mfma_f32_16x16x32_bf16 v[24:27], v[132:135], a[56:59], v[24:27]\n"
      "ds_write_addtid_b32 v106 offset:59904\n"
      "v_add_f32 v37, v37, v53\n"
      "v_mfma_f32_16x16x32_bf16 v[28:31], v[132:135], a[60:63], v[28:31]\n"
      "s_waitcnt vmcnt(19)\n"
      "global_load_dwordx4 v[132:135], v242, s[10:11] offset:2048 sc1\n"
      "v_add_f32 v38, v38, v54\n"
      "v_mfma_f32_16x16x32_bf16 v[0:3], v[136:139], a[64:67], v[0:3]\n"
      "ds_write_addtid_b32 v107 offset:60160\n"
      "v_add_f32 v39, v39, v55\n"
      "v_mfma_f32_16x16x32_bf16 v[4:7], v[136:139], a[68:71], v[4:7]\n"
      "ds_write_addtid_b32 v108 offset:60416\n"
      "v_add_f32 v32, v32, v56\n"
      "v_mfma_f32_16x16x32_bf16 v[8:11], v[136:139], a[72:75], v[8:11]\n"
      "ds_write_addtid_b32 v109 offset:60672\n"
      "v_add_f32 v33, v33, v57\n"
      "v_mfma_f32_16x16x32_bf16 v[12:15], v[136:139], a[76:79], v[12:15]\n"
      "ds_write_addtid_b32 v110 offset:60928\n"
      "v_add_f32 v34, v34, v58\n"
      "v_mfma_f32_16x16x32_bf16 v[16:19], v[136:139], a[80:83], v[16:19]\n"
      "ds_write_addtid_b32 v111 offset:61184\n"
      "v_add_f32 v35, v35, v59\n"
      "v_mfma_f32_16x16x32_bf16 v[20:23], v[136:139], a[84:87], v[20:23]\n"
      "ds_write_addtid_b32 v112 offset:61440\n"
      "v_add_f32 v36, v36, v60\n"
      "v_mfma_f32_16x16x32_bf16 v[24:27], v[136:139], a[88:91], v[24:27]\n"
      "ds_write_addtid_b32 v113 offset:61696\n"
      "v_add_f32 v37, v37, v61\n"
      "v_mfma_f32_16x16x32_bf16 v[28:31], v[136:139], a[92:95], v[28:31]\n"
      "global_load_dwordx4 v[136:139], v241, s[10:11] offset:2048 sc1\n"
      "v_add_f32 v38, v38, v62\n"
      "v_mfma_f32_16x16x32_bf16 v[0:3], v[140:143], a[96:99], v[0:3]\n"
      "ds_write_addtid_b32 v114 offset:61952\n"
      "v_add_f32 v39, v39, v63\n"
      "v_mfma_f32_16x16x32_bf16 v[4:7], v[140:143], a[100:103], v[4:7]\n"
      "ds_write_addtid_b32 v115 offset:62208\n"
      "v_mov_b32 v44, v247\n"
      "v_mfma_f32_16x16x32_bf16 v[8:11], v[140:143], a[104:107], v[8:11]\n"
      "ds_write_addtid_b32 v116 offset:62464\n"
      "v_mov_b32 v47, s37\n"
      "v_mfma_f32_16x16x32_bf16 v[12:15], v[140:143], a[108:111], v[12:15]\n"
      "ds_write_addtid_b32 v117 offset:62720\n"
      "v_cvt_pk_bf16_f32 v40, v32, v33\n"
      "v_mfma_f32_16x16x32_bf16 v[16:19], v[140:143], a[112:115], v[16:19]\n"
      "ds_write_addtid_b32 v118 offset:62976\n"
      "v_cvt_pk_bf16_f32 v41, v34, v35\n"
      "v_mfma_f32_16x16x32_bf16 v[20:23], v[140:143], a[116:119], v[20:23]\n"
      "ds_write_addtid_b32 v119 offset:63232\n"
      "v_cvt_pk_bf16_f32 v42, v36, v37\n"
      "v_mfma_f32_16x16x32_bf16 v[24:27], v[140:143], a[120:123], v[24:27]\n"
      "ds_write_addtid_b32 v120 offset:63488\n"
      "v_mov_b32 v43, s37\n"
      "v_mfma_f32_16x16x32_bf16 v[28:31], v[140:143], a[124:127], v[28:31]\n"
      "s_waitcnt vmcnt(19)\n"
      "global_load_dwordx4 v[140:143], v243, s[10:11] offset:2048 sc1\n"
      "v_cvt_pk_bf16_f32 v45, v38, v39\n"
      "v_mfma_f32_16x16x32_bf16 v[0:3], v[144:147], a[128:131], v[0:3]\n"
      "global_store_dwordx4 v252, v[40:43], s[52:53] offset:1024 sc1\n"
      "v_mfma_f32_16x16x32_bf16 v[4:7], v[144:147], a[132:135], v[4:7]\n"
      "global_store_dwordx4 v252, v[44:47], s[52:53] offset:2048 sc1\n"
      "s_add_u32 s48, s34, 2\n"
      "v_mfma_f32_16x16x32_bf16 v[8:11], v[144:147], a[136:139], v[8:11]\n"
      "s_and_b32 s48, s48, 3\n"
      "ds_write_addtid_b32 v121 offset:63744\n"
      "s_mul_i32 s48, s48, 36864\n"
      "v_mfma_f32_16x16x32_bf16 v[12:15], v[144:147], a[140:143], v[12:15]\n"
      "s_add_u32 s48, s48, 12288\n"
      "ds_write_addtid_b32 v122 offset:64000\n"
      "s_add_u32 s52, s60, s48\n"
      "s_addc_u32 s53, s61, 0\n"
      "v_mfma_f32_16x16x32_bf16 v[16:19], v[144:147], a[144:147], v[16:19]\n"
      "ds_write_addtid_b32 v123 offset:64256\n"
      "v_mfma_f32_16x16x32_bf16 v[20:23], v[144:147], a[148:151], v[20:23]\n"
      "ds_write_addtid_b32 v124 offset:64512\n"
      "v_mfma_f32_16x16x32_bf16 v[24:27], v[144:147], a[152:155], v[24:27]\n"
      "ds_write_addtid_b32 v125 offset:64768\n"
      "v_mfma_f32_16x16x32_bf16 v[28:31], v[144:147], a[156:159], v[28:31]\n"
      "global_load_dwordx4 v[144:147], v240, s[14:15] offset:2048 sc1\n"
      "v_mfma_f32_16x16x32_bf16 v[0:3], v[148:151], a[160:163], v[0:3]\n"
      "ds_write_addtid_b32 v126 offset:65024\n"
      "v_mfma_f32_16x16x32_bf16 v[4:7], v[148:151], a[164:167], v[4:7]\n"
      "ds_write_addtid_b32 v127 offset:65280\n"
      "v_mfma_f32_16x16x32_bf16 v[8:11], v[148:151], a[168:171], v[8:11]\n"
      "v_mfma_f32_16x16x32_bf16 v[12:15], v[148:151], a[172:175], v[12:15]\n"
      "v_mfma_f32_16x16x32_bf16 v[16:19], v[148:151], a[176:179], v[16:19]\n"
      "s_waitcnt lgkmcnt(0)\n"
      "s_barrier\n"
      "v_mfma_f32_16x16x32_bf16 v[20:23], v[148:151], a[180:183], v[20:23]\n"
      "ds_read2st64_b32 v[72:73], v248 offset0:0 offset1:1\n"
      "v_mfma_f32_16x16x32_bf16 v[24:27], v[148:151], a[184:187], v[24:27]\n"
      "ds_read2st64_b32 v[74:75], v248 offset0:2 offset1:3\n"
      "v_mfma_f32_16x16x32_bf16 v[28:31], v[148:151], a[188:191], v[28:31]\n"
      "s_waitcnt vmcnt(16)\n"
      "global_load_dwordx4 v[148:151], v242, s[14:15] offset:2048 sc1\n"
      "v_mfma_f32_16x16x32_bf16 v[0:3], v[152:155], a[192:195], v[0:3]\n"
      "ds_read2st64_b32 v[76:77], v248 offset0:4 offset1:5\n"
      "v_mfma_f32_16x16x32_bf16 v[4:7], v[152:155], a[196:199], v[4:7]\n"
      "ds_read2st64_b32 v[78:79], v248 offset0:6 offset1:7\n"
      "v_mfma_f32_16x16x32_bf16 v[8:11], v[152:155], a[200:203], v[8:11]\n"
      "ds_read2st64_b32 v[80:81], v249 offset0:8 offset1:9\n"
      "v_mfma_f32_16x16x32_bf16 v[12:15], v[152:155], a[204:207], v[12:15]\n"
      "ds_read2st64_b32 v[82:83], v249 offset0:10 offset1:11\n"
      "v_mfma_f32_16x16x32_bf16 v[16:19], v[152:155], a[208:211], v[16:19]\n"
      "ds_read2st64_b32 v[84:85], v249 offset0:12 offset1:13\n"
      "v_mfma_f32_16x16x32_bf16 v[20:23], v[152:155], a[212:215], v[20:23]\n"
      "ds_read2st64_b32 v[86:87], v249 offset0:14 offset1:15\n"
      "v_mfma_f32_16x16x32_bf16 v[24:27], v[152:155], a[216:219], v[24:27]\n"
      "ds_read2st64_b32 v[88:89], v250 offset0:16 offset1:17\n"
      "v_mfma_f32_16x16x32_bf16 v[28:31], v[152:155], a[220:223], v[28:31]\n"
      "global_load_dwordx4 v[152:155], v241, s[14:15] offset:2048 sc1\n"
      "v_mfma_f32_16x16x32_bf16 v[0:3], v[156:159], a[224:227], v[0:3]\n"
      "s_waitcnt lgkmcnt(5)\n"
      "ds_read2st64_b32 v[90:91], v250 offset0:18 offset1:19\n"
      "v_add_f32 v64, v64, v72\n"
      "v_mfma_f32_16x16x32_bf16 v[4:7], v[156:159], a[228:231], v[4:7]\n"
      "ds_read2st64_b32 v[92:93], v250 offset0:20 offset1:21\n"
      "v_add_f32 v65, v65, v73\n"
      "v_mfma_f32_16x16x32_bf16 v[8:11], v[156:159], a[232:235], v[8:11]\n"
      "ds_read2st64_b32 v[94:95], v250 offset0:22 offset1:23\n"
      "v_add_f32 v66, v66, v74\n"
      "v_mfma_f32_16x16x32_bf16 v[12:15], v[156:159], a[236:239], v[12:15]\n"
      "ds_read2st64_b32 v[104:105], v248 offset0:24 offset1:25\n"
      "v_add_f32 v67, v67, v75\n"
      "v_mfma_f32_16x16x32_bf16 v[16:19], v[156:159], a[240:243], v[16:19]\n"
      "ds_read2st64_b32 v[106:107], v248 offset0:26 offset1:27\n"
      "v_add_f32 v68, v68, v76\n"
      "v_mfma_f32_16x16x32_bf16 v[20:23], v[156:159], a[244:247], v[20:23]\n"
      "ds_read2st64_b32 v[108:109], v248 offset0:28 offset1:29\n"
      "v_add_f32 v69, v69, v77\n"
      "v_mfma_f32_16x16x32_bf16 v[24:27], v[156:159], a[248:251], v[24:27]\n"
      "ds_read2st64_b32 v[110:111], v248 offset0:30 offset1:31\n"
      "v_add_f32 v70, v70, v78\n"
      "v_mfma_f32_16x16x32_bf16 v[28:31], v[156:159], a[252:255], v[28:31]\n"
      "s_waitcnt vmcnt(16)\n"
      "global_load_dwordx4 v[156:159], v243, s[14:15] offset:2048 sc1\n"
      "v_add_f32 v71, v71, v79\n"
      "v_mfma_f32_16x16x32_bf16 v[32:35], v[160:163], a[0:3], 0\n"
      "s_waitcnt lgkmcnt(8)\n"
      "global_load_dwordx4 v[224:227], v200, s[58:59] offset:0 sc1\n"
      "v_add_f32 v64, v64, v80\n"
      "v_mfma_f32_16x16x32_bf16 v[36:39], v[160:163], a[4:7], 0\n"
      "global_load_dwordx4 v[228:231], v200, s[58:59] offset:1024 sc1\n"
      "v_add_f32 v65, v65, v81\n"
      "v_mfma_f32_16x16x32_bf16 v[40:43], v[160:163], a[8:11], 0\n"
      "global_load_dwordx4 v[232:235], v200, s[58:59] offset:2048 sc1\n"
      "v_add_f32 v66, v66, v82\n"
      "v_mfma_f32_16x16x32_bf16 v[44:47], v[160:163], a[12:15], 0\n"
      "global_load_dwordx4 v[236:239], v200, s[58:59] offset:3072 sc1\n"
      "v_add_f32 v67, v67, v83\n"
      "v_mfma_f32_16x16x32_bf16 v[48:51], v[160:163], a[16:19], 0\n"
      "ds_read2st64_b32 v[112:113], v249 offset0:32 offset1:33\n"
      "v_add_f32 v68, v68, v84\n"
      "v_mfma_f32_16x16x32_bf16 v[52:55], v[160:163], a[20:23], 0\n"
      "ds_read2st64_b32 v[114:115], v249 offset0:34 offset1:35\n"
      "v_add_f32 v69, v69, v85\n"
      "v_mfma_f32_16x16x32_bf16 v[56:59], v[160:163], a[24:27], 0\n"
      "ds_read2st64_b32 v[116:117], v249 offset0:36 offset1:37\n"
      "v_add_f32 v70, v70, v86\n"
      "v_mfma_f32_16x16x32_bf16 v[60:63], v[160:163], a[28:31], 0\n"
      "global_load_dwordx4 v[160:163], v240, s[10:11] offset:3072 sc1\n"
      "v_add_f32 v71, v71, v87\n"
      "v_mfma_f32_16x16x32_bf16 v[32:35], v[164:167], a[32:35], v[32:35]\n"
      "s_waitcnt lgkmcnt(7)\n"
      "ds_read2st64_b32 v[118:119], v249 offset0:38 offset1:39\n"
      "v_add_f32 v64, v64, v88\n"
      "v_mfma_f32_16x16x32_bf16 v[36:39], v[164:167], a[36:39], v[36:39]\n"
      "ds_read2st64_b32 v[120:121], v250 offset0:40 offset1:41\n"
      "v_add_f32 v65, v65, v89\n"
      "v_mfma_f32_16x16x32_bf16 v[40:43], v[164:167], a[40:43], v[40:43]\n"
      "ds_read2st64_b32 v[122:123], v250 offset0:42 offset1:43\n"
      "ds_read2st64_b32 v[124:125], v250 offset0:44 offset1:45\n"
      "v_add_f32 v66, v66, v90\n"
      "v_mfma_f32_16x16x32_bf16 v[44:47], v[164:167], a[44:47], v[44:47]\n"
      "ds_read2st64_b32 v[126:127], v250 offset0:46 offset1:47\n"
      "v_add_f32 v67, v67, v91\n"
      "v_add_f32 v68, v68, v92\n"
      "v_mfma_f32_16x16x32_bf16 v[48:51], v[164:167], a[48:51], v[48:51]\n"
      "s_add_u32 m0, s41, 0\n"
      "s_nop 0\n"
      "ds_write_addtid_b32 v8 offset:0\n"
      "v_mfma_f32_16x16x32_bf16 v[52:55], v[164:167], a[52:55], v[52:55]\n"
      "ds_write_addtid_b32 v9 offset:256\n"
      "ds_write_addtid_b32 v10 offset:512\n"
      "v_add_f32 v69, v69, v93\n"
      "v_mfma_f32_16x16x32_bf16 v[56:59], v[164:167], a[56:59], v[56:59]\n"
      "ds_write_addtid_b32 v11 offset:768\n"
      "ds_write_addtid_b32 v12 offset:1024\n"
      "v_add_f32 v70, v70, v94\n"
      "v_mfma_f32_16x16x32_bf16 v[60:63], v[164:167], a[60:63], v[60:63]\n"
      "s_waitcnt vmcnt(20)\n"
      "global_load_dwordx4 v[164:167], v242, s[10:11] offset:3072 sc1\n"
      "ds_write_addtid_b32 v13 offset:1280\n"
      "v_add_f32 v71, v71, v95\n"
      "v_mfma_f32_16x16x32_bf16 v[32:35], v[168:171], a[64:67], v[32:35]\n"
      "ds_write_addtid_b32 v14 offset:1536\n"
      "ds_write_addtid_b32 v15 offset:1792\n"
      "v_cvt_pk_bf16_f32 v72, v64, v65\n"
      "v_mfma_f32_16x16x32_bf16 v[36:39], v[168:171], a[68:71], v[36:39]\n"
      "ds_write_addtid_b32 v16 offset:2048\n"
      "ds_write_addtid_b32 v17 offset:2304\n"
      "v_cvt_pk_bf16_f32 v73, v66, v67\n"
      "v_mfma_f32_16x16x32_bf16 v[40:43], v[168:171], a[72:75], v[40:43]\n"
      "ds_write_addtid_b32 v18 offset:2560\n"
      "ds_write_addtid_b32 v19 offset:2816\n"
      "v_cvt_pk_bf16_f32 v74, v68, v69\n"
      "v_mfma_f32_16x16x32_bf16 v[44:47], v[168:171], a[76:79], v[44:47]\n"
      "ds_write_addtid_b32 v20 offset:3072\n"
      "ds_write_addtid_b32 v21 offset:3328\n"
      "v_mov_b32 v75, s37\n"
      "v_mfma_f32_16x16x32_bf16 v[48:51], v[168:171], a[80:83], v[48:51]\n"
      "ds_write_addtid_b32 v22 offset:3584\n"
      "ds_write_addtid_b32 v23 offset:3840\n"
      "v_cvt_pk_bf16_f32 v247, v70, v71\n"
      "v_mfma_f32_16x16x32_bf16 v[52:55], v[168:171], a[84:87], v[52:55]\n"
      "global_store_dwordx4 v252, v[72:75], s[52:53] offset:0 sc1\n"
      "ds_write_addtid_b32 v24 offset:4096\n"
      "s_waitcnt lgkmcnt(15)\n"
      "v_mfma_f32_16x16x32_bf16 v[56:59], v[168:171], a[88:91], v[56:59]\n"
      "ds_write_addtid_b32 v25 offset:4352\n"
      "ds_write_addtid_b32 v26 offset:4608\n"
      "v_add_f32 v96, v96, v104\n"
      "v_mfma_f32_16x16x32_bf16 v[60:63], v[168:171], a[92:95], v[60:63]\n"
      "global_load_dwordx4 v[168:171], v241, s[10:11] offset:3072 sc1\n"
      "ds_write_addtid_b32 v27 offset:4864\n"
      "v_add_f32 v97, v97, v105\n"
      "v_mfma_f32_16x16x32_bf16 v[32:35], v[172:175], a[96:99], v[32:35]\n"
      "ds_write_addtid_b32 v28 offset:5120\n"
      "ds_write_addtid_b32 v29 offset:5376\n"
      "v_add_f32 v98, v98, v106\n"
      "v_mfma_f32_16x16x32_bf16 v[36:39], v[172:175], a[100:103], v[36:39]\n"
      "ds_write_addtid_b32 v30 offset:5632\n"
      "ds_write_addtid_b32 v31 offset:5888\n"
      "v_add_f32 v99, v99, v107\n"
      "v_mfma_f32_16x16x32_bf16 v[40:43], v[172:175], a[104:107], v[40:43]\n"
      "v_add_f32 v100, v100, v108\n"
      "v_add_f32 v101, v101, v109\n"
      "v_add_f32 v102, v102, v110\n"
      "v_mfma_f32_16x16x32_bf16 v[44:47], v[172:175], a[108:111], v[44:47]\n"
      "v_add_f32 v103, v103, v111\n"
      "v_add_f32 v96, v96, v112\n"
      "v_add_f32 v97, v97, v113\n"
      "v_mfma_f32_16x16x32_bf16 v[48:51], v[172:175], a[112:115], v[48:51]\n"
      "s_waitcnt lgkmcnt(0)\n"
      "s_barrier\n"
      "v_mfma_f32_16x16x32_bf16 v[52:55], v[172:175], a[116:119], v[52:55]\n"
      "ds_read2st64_b32 v[8:9], v244 offset0:0 offset1:1\n"
      "ds_read2st64_b32 v[10:11], v244 offset0:2 offset1:3\n"
      "v_add_f32 v98, v98, v114\n"
      "v_mfma_f32_16x16x32_bf16 v[56:59], v[172:175], a[120:123], v[56:59]\n"
      "ds_read2st64_b32 v[12:13], v244 offset0:4 offset1:5\n"
      "ds_read2st64_b32 v[14:15], v244 offset0:6 offset1:7\n"
      "v_add_f32 v99, v99, v115\n"
      "v_mfma_f32_16x16x32_bf16 v[60:63], v[172:175], a[124:127], v[60:63]\n"
      "s_waitcnt vmcnt(21)\n"
      "global_load_dwordx4 v[172:175], v243, s[10:11] offset:3072 sc1\n"
      "ds_read2st64_b32 v[16:17], v245 offset0:8 offset1:9\n"
      "v_add_f32 v100, v100, v116\n"
      "v_mfma_f32_16x16x32_bf16 v[40:43], v[176:179], a[136:139], v[40:43]\n"
      "ds_read2st64_b32 v[18:19], v245 offset0:10 offset1:11\n"
      "ds_read2st64_b32 v[20:21], v245 offset0:12 offset1:13\n"
      "v_add_f32 v101, v101, v117\n"
      "v_mfma_f32_16x16x32_bf16 v[44:47], v[176:179], a[140:143], v[44:47]\n"
      "ds_read2st64_b32 v[22:23], v245 offset0:14 offset1:15\n"
      "ds_read2st64_b32 v[24:25], v246 offset0:16 offset1:17\n"
      "v_add_f32 v102, v102, v118\n"
      "v_mfma_f32_16x16x32_bf16 v[48:51], v[176:179], a[144:147], v[48:51]\n"
      "ds_read2st64_b32 v[26:27], v246 offset0:18 offset1:19\n"
      "ds_read2st64_b32 v[28:29], v246 offset0:20 offset1:21\n"
      "v_add_f32 v103, v103, v119\n"
      "v_mfma_f32_16x16x32_bf16 v[52:55], v[176:179], a[148:151], v[52:55]\n"
      "ds_read2st64_b32 v[30:31], v246 offset0:22 offset1:23\n"
      "v_add_f32 v96, v96, v120\n"
      "v_add_f32 v97, v97, v121\n"
      "v_mfma_f32_16x16x32_bf16 v[56:59], v[176:179], a[152:155], v[56:59]\n"
      "v_add_f32 v98, v98, v122\n"
      "v_add_f32 v99, v99, v123\n"
      "v_add_f32 v100, v100, v124\n"
      "v_mfma_f32_16x16x32_bf16 v[60:63], v[176:179], a[156:159], v[60:63]\n"
      "v_add_f32 v101, v101, v125\n"
      "v_add_f32 v102, v102, v126\n"
      "v_add_f32 v103, v103, v127\n"
      "v_mfma_f32_16x16x32_bf16 v[40:43], v[180:183], a[168:171], v[40:43]\n"
      "v_mov_b32 v108, v247\n"
      "v_mov_b32 v111, s37\n"
      "v_cvt_pk_bf16_f32 v104, v96, v97\n"
      "v_mfma_f32_16x16x32_bf16 v[44:47], v[180:183], a[172:175], v[44:47]\n"
      "v_cvt_pk_bf16_f32 v105, v98, v99\n"
      "v_cvt_pk_bf16_f32 v106, v100, v101\n"
      "v_mov_b32 v107, s37\n"
      "v_mfma_f32_16x16x32_bf16 v[48:51], v[180:183], a[176:179], v[48:51]\n"
      "v_cvt_pk_bf16_f32 v109, v102, v103\n"
      "v_mfma_f32_16x16x32_bf16 v[52:55], v[180:183], a[180:183], v[52:55]\n"
      "global_store_dwordx4 v252, v[104:107], s[52:53] offset:1024 sc1\n"
      "global_store_dwordx4 v252, v[108:111], s[52:53] offset:2048 sc1\n"
      "s_add_u32 s48, s34, 3\n"
      "v_mfma_f32_16x16x32_bf16 v[56:59], v[180:183], a[184:187], v[56:59]\n"
      "s_and_b32 s48, s48, 3\n"
      "s_mul_i32 s48, s48, 36864\n"
      "s_add_u32 s48, s48, 24576\n"
      "s_add_u32 s52, s60, s48\n"
      "s_addc_u32 s53, s61, 0\n"
      "v_mfma_f32_16x16x32_bf16 v[60:63], v[180:183], a[188:191], v[60:63]\n"
      "s_waitcnt vmcnt(22)\n"
      "s_waitcnt lgkmcnt(8)\n"
      "v_add_f32 v0, v0, v8\n"
      "v_add_f32 v1, v1, v9\n"
      "v_mfma_f32_16x16x32_bf16 v[40:43], v[184:187], a[200:203], v[40:43]\n"
      "v_add_f32 v2, v2, v10\n"
      "v_add_f32 v3, v3, v11\n"
      "v_add_f32 v4, v4, v12\n"
      "v_mfma_f32_16x16x32_bf16 v[44:47], v[184:187], a[204:207], v[44:47]\n"
      "v_add_f32 v5, v5, v13\n"
      "v_add_f32 v6, v6, v14\n"
      "v_add_f32 v7, v7, v15\n"
      "v_mfma_f32_16x16x32_bf16 v[48:51], v[184:187], a[208:211], v[48:51]\n"
      "s_waitcnt lgkmcnt(4)\n"
      "v_add_f32 v0, v0, v16\n"
      "v_add_f32 v1, v1, v17\n"
      "v_add_f32 v2, v2, v18\n"
      "v_mfma_f32_16x16x32_bf16 v[52:55], v[184:187], a[212:215], v[52:55]\n"
      "v_add_f32 v3, v3, v19\n"
      "v_add_f32 v4, v4, v20\n"
      "v_add_f32 v5, v5, v21\n"
      "v_mfma_f32_16x16x32_bf16 v[56:59], v[184:187], a[216:219], v[56:59]\n"
      "v_add_f32 v6, v6, v22\n"
      "v_add_f32 v7, v7, v23\n"
      "s_waitcnt lgkmcnt(0)\n"
      "v_mfma_f32_16x16x32_bf16 v[60:63], v[184:187], a[220:223], v[60:63]\n"
      "v_add_f32 v0, v0, v24\n"
      "v_add_f32 v1, v1, v25\n"
      "v_add_f32 v2, v2, v26\n"
      "v_mfma_f32_16x16x32_bf16 v[40:43], v[188:191], a[232:235], v[40:43]\n"
      "v_add_f32 v3, v3, v27\n"
      "v_add_f32 v4, v4, v28\n"
      "v_add_f32 v5, v5, v29\n"
      "v_mfma_f32_16x16x32_bf16 v[44:47], v[188:191], a[236:239], v[44:47]\n"
      "v_add_f32 v6, v6, v30\n"
      "v_add_f32 v7, v7, v31\n"
      "v_cvt_pk_bf16_f32 v8, v0, v1\n"
      "v_mfma_f32_16x16x32_bf16 v[48:51], v[188:191], a[240:243], v[48:51]\n"
      "v_cvt_pk_bf16_f32 v9, v2, v3\n"
      "v_cvt_pk_bf16_f32 v10, v4, v5\n"
      "v_mov_b32 v11, s37\n"
      "v_mfma_f32_16x16x32_bf16 v[52:55], v[188:191], a[244:247], v[52:55]\n"
      "s_waitcnt vmcnt(7)\n"
      "v_bitop3_b32 v192, v225, v227, s37 bitop3:0x7e\n"
      "v_bitop3_b32 v192, v192, v229, s37 bitop3:0xf6\n"
      "v_bitop3_b32 v192, v192, v231, s37 bitop3:0xf6\n"
      "v_bitop3_b32 v192, v192, v233, s37 bitop3:0xf6\n"
      "v_bitop3_b32 v192, v192, v235, s37 bitop3:0xf6\n"
      "v_bitop3_b32 v192, v192, v237, s37 bitop3:0xf6\n"
      "v_bitop3_b32 v192, v192, v239, s37 bitop3:0xf6\n"
      "v_cmp_ne_u32 vcc, 0, v192\n"
      "s_nop 1\n"
      "s_cbranch_vccnz .Ls_s17_%=\n"
      ".Ls_o18_%=:\n"
      "v_mfma_f32_16x16x32_bf16 v[56:59], v[188:191], a[248:251], v[56:59]\n"
      "v_add_f32 v192, v224, v228\n"
      "v_add_f32 v193, v226, v230\n"
      "v_add_f32 v194, v232, v236\n"
      "v_mfma_f32_16x16x32_bf16 v[60:63], v[188:191], a[252:255], v[60:63]\n"
      "v_add_f32 v195, v234, v238\n"
      "v_cvt_pk_bf16_f32 v247, v6, v7\n"
      "v_mfma_f32_16x16x32_bf16 v[32:35], v[176:179], a[128:131], v[32:35]\n"
      "ds_read_b64_tr_b16 v[224:225], v201 offset:0\n"
      "global_store_dwordx4 v252, v[8:11], s[52:53] offset:0 sc1\n"
      "v_permlane32_swap_b32 v192, v193\n"
      "v_mfma_f32_16x16x32_bf16 v[36:39], v[176:179], a[132:135], v[36:39]\n"
      "s_add_u32 m0, s41, 0\n"
      "global_load_dwordx4 v[176:179], v240, s[14:15] offset:3072 sc1\n"
      "ds_read_b64_tr_b16 v[226:227], v201 offset:512\n"
      "v_permlane32_swap_b32 v194, v195\n"
      "v_mfma_f32_16x16x32_bf16 v[32:35], v[180:183], a[160:163], v[32:35]\n"
      "ds_read_b64_tr_b16 v[228:229], v201 offset:32\n"
      "ds_write_addtid_b32 v40 offset:6144\n"
      "v_add_f32 v192, v192, v193\n"
      "v_mfma_f32_16x16x32_bf16 v[36:39], v[180:183], a[164:167], v[36:39]\n"
      "global_load_dwordx4 v[180:183], v242, s[14:15] offset:3072 sc1\n"
      "ds_read_b64_tr_b16 v[230:231], v201 offset:544\n"
      "v_add_f32 v194, v194, v195\n"
      "v_mfma_f32_16x16x32_bf16 v[32:35], v[184:187], a[192:195], v[32:35]\n"
      "ds_read_b64_tr_b16 v[232:233], v201 offset:64\n"
      "ds_write_addtid_b32 v41 offset:6400\n"
      "v_mov_b32 v193, v192\n"
      "v_mfma_f32_16x16x32_bf16 v[36:39], v[184:187], a[196:199], v[36:39]\n"
      "global_load_dwordx4 v[184:187], v241, s[14:15] offset:3072 sc1\n"
      "ds_read_b64_tr_b16 v[234:235], v201 offset:576\n"
      "v_mov_b32 v195, v194\n"
      "v_mfma_f32_16x16x32_bf16 v[32:35], v[188:191], a[224:227], v[32:35]\n"
      "ds_read_b64_tr_b16 v[236:237], v201 offset:96\n"
      "ds_write_addtid_b32 v42 offset:6656\n"
      "v_permlane16_swap_b32 v192, v193\n"
      "v_mfma_f32_16x16x32_bf16 v[36:39], v[188:191], a[228:231], v[36:39]\n"
      "global_load_dwordx4 v[188:191], v243, s[14:15] offset:3072 sc1\n"
      "ds_read_b64_tr_b16 v[238:239], v201 offset:608\n"
      "v_permlane16_swap_b32 v194, v195\n"
      "v_mfma_f32_16x16x32_bf16 v[72:75], v[128:131], a[8:11], 0\n"
      "ds_write_addtid_b32 v43 offset:6912\n"
      "ds_write_addtid_b32 v44 offset:7168\n"
      "v_add_f32 v192, v192, v193\n"
      "v_mfma_f32_16x16x32_bf16 v[76:79], v[128:131], a[12:15], 0\n"
      "ds_write_addtid_b32 v45 offset:7424\n"
      "ds_write_addtid_b32 v46 offset:7680\n"
      "v_add_f32 v194, v194, v195\n"
      "v_mfma_f32_16x16x32_bf16 v[80:83], v[128:131], a[16:19], 0\n"
      "ds_write_addtid_b32 v47 offset:7936\n"
      "ds_write_addtid_b32 v48 offset:8192\n"
      "v_cvt_pk_bf16_f32 v192, v192, v194\n"
      "v_mfma_f32_16x16x32_bf16 v[84:87], v[128:131], a[20:23], 0\n"
      "ds_write_addtid_b32 v49 offset:8448\n"
      "ds_write_addtid_b32 v50 offset:8704\n"
      "v_and_b32 v194, 0xffff0000, v192\n"
      "v_mfma_f32_16x16x32_bf16 v[88:91], v[128:131], a[24:27], 0\n"
      "ds_write_addtid_b32 v51 offset:8960\n"
      "ds_write_addtid_b32 v52 offset:9216\n"
      "v_lshlrev_b32 v192, 16, v192\n"
      "v_mfma_f32_16x16x32_bf16 v[92:95], v[128:131], a[28:31], 0\n"
      "ds_write_addtid_b32 v53 offset:9472\n"
      "ds_write_addtid_b32 v54 offset:9728\n"
      "v_mul_f32 v193, 0xbfb8aa3b, v192\n"
      "v_mfma_f32_16x16x32_bf16 v[72:75], v[132:135], a[40:43], v[72:75]\n"
      "ds_write_addtid_b32 v55 offset:9984\n"
      "ds_write_addtid_b32 v56 offset:10240\n"
      "v_exp_f32 v193, v193\n"
      "v_mfma_f32_16x16x32_bf16 v[76:79], v[132:135], a[44:47], v[76:79]\n"
      "ds_write_addtid_b32 v57 offset:10496\n"
      "ds_write_addtid_b32 v58 offset:10752\n"
      "v_add_f32 v193, 1.0, v193\n"
      "v_mfma_f32_16x16x32_bf16 v[80:83], v[132:135], a[48:51], v[80:83]\n"
      "ds_write_addtid_b32 v59 offset:11008\n"
      "ds_write_addtid_b32 v60 offset:11264\n"
      "v_rcp_f32 v193, v193\n"
      "v_mfma_f32_16x16x32_bf16 v[84:87], v[132:135], a[52:55], v[84:87]\n"
      "ds_write_addtid_b32 v61 offset:11520\n"
      "ds_write_addtid_b32 v62 offset:11776\n"
      "v_mul_f32 v192, v192, v193\n"
      "v_mfma_f32_16x16x32_bf16 v[88:91], v[132:135], a[56:59], v[88:91]\n"
      "ds_write_addtid_b32 v63 offset:12032\n"
      "v_cvt_pk_bf16_f32 v192, v192, v192\n"
      "v_and_b32 v192, 0xffff0000, v192\n"
      "v_mfma_f32_16x16x32_bf16 v[92:95], v[132:135], a[60:63], v[92:95]\n"
      "v_mul_f32 v192, v192, v194\n"
      "v_cvt_pk_bf16_f32 v192, v192, v192\n"
      "v_mfma_f32_16x16x32_bf16 v[72:75], v[136:139], a[72:75], v[72:75]\n"
      "ds_write_b16 v202, v192\n"
      "ds_read_b128 v[192:195], v199\n"
      "v_mfma_f32_16x16x32_bf16 v[76:79], v[136:139], a[76:79], v[76:79]\n"
      "s_waitcnt lgkmcnt(2)\n"
      "s_barrier\n"
      "v_mfma_f32_16x16x32_bf16 v[80:83], v[136:139], a[80:83], v[80:83]\n"
      "ds_read2st64_b32 v[40:41], v244 offset0:24 offset1:25\n"
      "ds_read2st64_b32 v[42:43], v244 offset0:26 offset1:27\n"
      "v_mfma_f32_16x16x32_bf16 v[84:87], v[136:139], a[84:87], v[84:87]\n"
      "ds_read2st64_b32 v[44:45], v244 offset0:28 offset1:29\n"
      "ds_read2st64_b32 v[46:47], v244 offset0:30 offset1:31\n"
      "v_mfma_f32_16x16x32_bf16 v[88:91], v[136:139], a[88:91], v[88:91]\n"
      "ds_read2st64_b32 v[48:49], v245 offset0:32 offset1:33\n"
      "ds_read2st64_b32 v[50:51], v245 offset0:34 offset1:35\n"
      "v_mfma_f32_16x16x32_bf16 v[92:95], v[136:139], a[92:95], v[92:95]\n"
      "ds_read2st64_b32 v[52:53], v245 offset0:36 offset1:37\n"
      "ds_read2st64_b32 v[54:55], v245 offset0:38 offset1:39\n"
      "v_mfma_f32_16x16x32_bf16 v[72:75], v[140:143], a[104:107], v[72:75]\n"
      "s_waitcnt lgkmcnt(8)\n"
      "v_mfma_f32_16x16x32_bf16 v[196:199], v[192:195], v[224:227], 0\n"
      "v_mfma_f32_16x16x32_bf16 v[76:79], v[140:143], a[108:111], v[76:79]\n"
      "v_mfma_f32_16x16x32_bf16 v[224:227], v[192:195], v[228:231], 0\n"
      "v_mfma_f32_16x16x32_bf16 v[228:231], v[192:195], v[232:235], 0\n"
      "v_mfma_f32_16x16x32_bf16 v[80:83], v[140:143], a[112:115], v[80:83]\n"
      "v_mfma_f32_16x16x32_bf16 v[232:235], v[192:195], v[236:239], 0\n"
      "ds_read2st64_b32 v[56:57], v246 offset0:40 offset1:41\n"
      "v_mov_b32 v193, s37\n"
      "v_mfma_f32_16x16x32_bf16 v[84:87], v[140:143], a[116:119], v[84:87]\n"
      "ds_read2st64_b32 v[58:59], v246 offset0:42 offset1:43\n"
      "ds_read2st64_b32 v[60:61], v246 offset0:44 offset1:45\n"
      "v_mov_b32 v195, s37\n"
      "v_mfma_f32_16x16x32_bf16 v[88:91], v[140:143], a[120:123], v[88:91]\n"
      "s_waitcnt lgkmcnt(7)\n"
      "ds_read2st64_b32 v[62:63], v246 offset0:46 offset1:47\n"
      "v_add_f32 v32, v32, v40\n"
      "v_add_f32 v33, v33, v41\n"
      "v_mfma_f32_16x16x32_bf16 v[92:95], v[140:143], a[124:127], v[92:95]\n"
      "v_cndmask_b32 v192, v196, v228, s[64:65]\n"
      "v_cndmask_b32 v194, v224, v232, s[64:65]\n"
      "v_and_b32 v192, s78, v192\n"
      "v_mfma_f32_16x16x32_bf16 v[72:75], v[144:147], a[136:139], v[72:75]\n"
      "v_and_b32 v194, s78, v194\n"
      "v_add_f32 v34, v34, v42\n"
      "v_add_f32 v35, v35, v43\n"
      "v_mfma_f32_16x16x32_bf16 v[76:79], v[144:147], a[140:143], v[76:79]\n"
      "s_mov_b64 exec, s[66:67]\n"
      "global_store_dwordx4 v203, v[192:195], s[58:59] sc1\n"
      "s_mov_b64 exec, -1\n"
      "v_mfma_f32_16x16x32_bf16 v[80:83], v[144:147], a[144:147], v[80:83]\n"
      "v_add_u32 v253, 0, v252\n"
      "v_add_f32 v36, v36, v44\n"
      "v_add_f32 v37, v37, v45\n"
      "v_mfma_f32_16x16x32_bf16 v[84:87], v[144:147], a[148:151], v[84:87]\n"
      "global_load_dwordx4 v[224:227], v253, s[62:63] offset:0 sc1\n"
      "global_load_dwordx4 v[228:231], v253, s[62:63] offset:1024 sc1\n"
      "v_add_f32 v38, v38, v46\n"
      "v_mfma_f32_16x16x32_bf16 v[88:91], v[144:147], a[152:155], v[88:91]\n"
      "global_load_dwordx4 v[232:235], v253, s[62:63] offset:2048 sc1\n"
      "v_add_f32 v39, v39, v47\n"
      "s_waitcnt lgkmcnt(4)\n"
      "v_mfma_f32_16x16x32_bf16 v[92:95], v[144:147], a[156:159], v[92:95]\n"
      "v_add_f32 v32, v32, v48\n"
      "v_add_f32 v33, v33, v49\n"
      "v_add_f32 v34, v34, v50\n"
      "v_mfma_f32_16x16x32_bf16 v[72:75], v[148:151], a[168:171], v[72:75]\n"
      "v_add_f32 v35, v35, v51\n"
      "v_add_f32 v36, v36, v52\n"
      "v_add_f32 v37, v37, v53\n"
      "v_mfma_f32_16x16x32_bf16 v[76:79], v[148:151], a[172:175], v[76:79]\n"
      "v_add_f32 v38, v38, v54\n"
      "v_add_f32 v39, v39, v55\n"
      "s_waitcnt lgkmcnt(0)\n"
      "v_mfma_f32_16x16x32_bf16 v[80:83], v[148:151], a[176:179], v[80:83]\n"
      "v_add_f32 v32, v32, v56\n"
      "v_add_f32 v33, v33, v57\n"
      "v_add_f32 v34, v34, v58\n"
      "v_mfma_f32_16x16x32_bf16 v[84:87], v[148:151], a[180:183], v[84:87]\n"
      "v_add_f32 v35, v35, v59\n"
      "v_add_f32 v36, v36, v60\n"
      "v_add_f32 v37, v37, v61\n"
      "v_mfma_f32_16x16x32_bf16 v[88:91], v[148:151], a[184:187], v[88:91]\n"
      "v_add_f32 v38, v38, v62\n"
      "v_add_f32 v39, v39, v63\n"
      "v_mov_b32 v44, v247\n"
      "v_mfma_f32_16x16x32_bf16 v[92:95], v[148:151], a[188:191], v[92:95]\n"
      "v_mov_b32 v47, s37\n"
      "v_cvt_pk_bf16_f32 v40, v32, v33\n"
      "v_cvt_pk_bf16_f32 v41, v34, v35\n"
      "v_mfma_f32_16x16x32_bf16 v[72:75], v[152:155], a[200:203], v[72:75]\n"
      "v_cvt_pk_bf16_f32 v42, v36, v37\n"
      "v_mov_b32 v43, s37\n"
      "v_mfma_f32_16x16x32_bf16 v[76:79], v[152:155], a[204:207], v[76:79]\n"
      "v_cvt_pk_bf16_f32 v45, v38, v39\n"
      "v_mfma_f32_16x16x32_bf16 v[80:83], v[152:155], a[208:211], v[80:83]\n"
      "global_store_dwordx4 v252, v[40:43], s[52:53] offset:1024 sc1\n"
      "v_mfma_f32_16x16x32_bf16 v[84:87], v[152:155], a[212:215], v[84:87]\n"
      "global_store_dwordx4 v252, v[44:47], s[52:53] offset:2048 sc1\n"
      "v_mfma_f32_16x16x32_bf16 v[88:91], v[152:155], a[216:219], v[88:91]\n"
      "v_mfma_f32_16x16x32_bf16 v[92:95], v[152:155], a[220:223], v[92:95]\n"
      "v_mfma_f32_16x16x32_bf16 v[72:75], v[156:159], a[232:235], v[72:75]\n"
      "v_mfma_f32_16x16x32_bf16 v[76:79], v[156:159], a[236:239], v[76:79]\n"
      "v_mfma_f32_16x16x32_bf16 v[80:83], v[156:159], a[240:243], v[80:83]\n"
      "v_mfma_f32_16x16x32_bf16 v[84:87], v[156:159], a[244:247], v[84:87]\n"
      "v_mfma_f32_16x16x32_bf16 v[88:91], v[156:159], a[248:251], v[88:91]\n"
      "v_mfma_f32_16x16x32_bf16 v[92:95], v[156:159], a[252:255], v[92:95]\n"
      "s_waitcnt vmcnt(16)\n"
      "v_mfma_f32_16x16x32_bf16 v[104:107], v[160:163], a[8:11], 0\n"
      "v_mfma_f32_16x16x32_bf16 v[108:111], v[160:163], a[12:15], 0\n"
      "s_add_u32 m0, s41, 12288\n"
      "s_nop 0\n"
      "ds_write_addtid_b32 v72 offset:53248\n"
      "v_mfma_f32_16x16x32_bf16 v[112:115], v[160:163], a[16:19], 0\n"
      "ds_write_addtid_b32 v73 offset:53504\n"
      "v_mfma_f32_16x16x32_bf16 v[116:119], v[160:163], a[20:23], 0\n"
      "ds_write_addtid_b32 v74 offset:53760\n"
      "v_mfma_f32_16x16x32_bf16 v[120:123], v[160:163], a[24:27], 0\n"
      "ds_write_addtid_b32 v75 offset:54016\n"
      "v_mfma_f32_16x16x32_bf16 v[124:127], v[160:163], a[28:31], 0\n"
      "ds_write_addtid_b32 v76 offset:54272\n"
      "v_mfma_f32_16x16x32_bf16 v[104:107], v[164:167], a[40:43], v[104:107]\n"
      "ds_write_addtid_b32 v77 offset:54528\n"
      "v_mfma_f32_16x16x32_bf16 v[108:111], v[164:167], a[44:47], v[108:111]\n"
      "ds_write_addtid_b32 v78 offset:54784\n"
      "v_mfma_f32_16x16x32_bf16 v[112:115], v[164:167], a[48:51], v[112:115]\n"
      "ds_write_addtid_b32 v79 offset:55040\n"
      "v_mfma_f32_16x16x32_bf16 v[116:119], v[164:167], a[52:55], v[116:119]\n"
      "ds_write_addtid_b32 v80 offset:55296\n"
      "v_mfma_f32_16x16x32_bf16 v[120:123], v[164:167], a[56:59], v[120:123]\n"
      "ds_write_addtid_b32 v81 offset:55552\n"
      "v_mfma_f32_16x16x32_bf16 v[124:127], v[164:167], a[60:63], v[124:127]\n"
      "s_waitcnt vmcnt(13)\n"
      "ds_write_addtid_b32 v82 offset:55808\n"
      "v_mfma_f32_16x16x32_bf16 v[104:107], v[168:171], a[72:75], v[104:107]\n"
      "ds_write_addtid_b32 v83 offset:56064\n"
      "v_mfma_f32_16x16x32_bf16 v[108:111], v[168:171], a[76:79], v[108:111]\n"
      "ds_write_addtid_b32 v84 offset:56320\n"
      "v_mfma_f32_16x16x32_bf16 v[112:115], v[168:171], a[80:83], v[112:115]\n"
      "ds_write_addtid_b32 v85 offset:56576\n"
      "v_mfma_f32_16x16x32_bf16 v[116:119], v[168:171], a[84:87], v[116:119]\n"
      "ds_write_addtid_b32 v86 offset:56832\n"
      "v_mfma_f32_16x16x32_bf16 v[120:123], v[168:171], a[88:91], v[120:123]\n"
      "s_waitcnt vmcnt(2)\n"
      "v_bitop3_b32 v234, v227, v231, s37 bitop3:0x7e\n"
      "v_bitop3_b32 v234, v234, v235, s37 bitop3:0xf6\n"
      "v_cmp_ne_u32 vcc, 0, v234\n"
      "s_nop 1\n"
      "s_cbranch_vccnz .Ls_s20_%=\n"
      ".Ls_o21_%=:\n"
      "v_mfma_f32_16x16x32_bf16 v[124:127], v[168:171], a[92:95], v[124:127]\n"
      "ds_write_addtid_b32 v87 offset:57088\n"
      "v_xor_b32 v202, s72, v204\n"
      "v_mfma_f32_16x16x32_bf16 v[104:107], v[172:175], a[104:107], v[104:107]\n"
      "ds_write_addtid_b32 v88 offset:57344\n"
      "v_xor_b32 v203, s72, v206\n"
      "v_mfma_f32_16x16x32_bf16 v[108:111], v[172:175], a[108:111], v[108:111]\n"
      "ds_write_addtid_b32 v89 offset:57600\n"
      "v_lshlrev_b32 v227, 16, v224\n"
      "v_mfma_f32_16x16x32_bf16 v[112:115], v[172:175], a[112:115], v[112:115]\n"
      "ds_write_addtid_b32 v90 offset:57856\n"
      "v_and_b32 v224, 0xffff0000, v224\n"
      "v_mfma_f32_16x16x32_bf16 v[116:119], v[172:175], a[116:119], v[116:119]\n"
      "ds_write_addtid_b32 v91 offset:58112\n"
      "v_fma_f32 v208, v202, v227, v208\n"
      "v_mfma_f32_16x16x32_bf16 v[120:123], v[172:175], a[120:123], v[120:123]\n"
      "ds_write_addtid_b32 v92 offset:58368\n"
      "v_fma_f32 v209, v202, v224, v209\n"
      "v_mfma_f32_16x16x32_bf16 v[124:127], v[172:175], a[124:127], v[124:127]\n"
      "ds_write_addtid_b32 v93 offset:58624\n"
      "v_lshlrev_b32 v231, 16, v225\n"
      "v_mfma_f32_16x16x32_bf16 v[104:107], v[176:179], a[136:139], v[104:107]\n"
      "ds_write_addtid_b32 v94 offset:58880\n"
      "v_and_b32 v225, 0xffff0000, v225\n"
      "v_mfma_f32_16x16x32_bf16 v[108:111], v[176:179], a[140:143], v[108:111]\n"
      "ds_write_addtid_b32 v95 offset:59136\n"
      "v_fma_f32 v210, v202, v231, v210\n"
      "v_mfma_f32_16x16x32_bf16 v[112:115], v[176:179], a[144:147], v[112:115]\n"
      "v_fma_f32 v211, v202, v225, v211\n"
      "v_lshlrev_b32 v227, 16, v226\n"
      "v_mfma_f32_16x16x32_bf16 v[116:119], v[176:179], a[148:151], v[116:119]\n"
      "v_and_b32 v226, 0xffff0000, v226\n"
      "v_fma_f32 v212, v203, v227, v212\n"
      "v_mfma_f32_16x16x32_bf16 v[120:123], v[176:179], a[152:155], v[120:123]\n"
      "s_waitcnt lgkmcnt(0)\n"
      "s_barrier\n"
      "v_mfma_f32_16x16x32_bf16 v[124:127], v[176:179], a[156:159], v[124:127]\n"
      "ds_read2st64_b32 v[72:73], v248 offset0:0 offset1:1\n"
      "v_fma_f32 v213, v203, v226, v213\n"
      "v_mfma_f32_16x16x32_bf16 v[104:107], v[180:183], a[168:171], v[104:107]\n"
      "ds_read2st64_b32 v[74:75], v248 offset0:2 offset1:3\n"
      "v_lshlrev_b32 v231, 16, v232\n"
      "v_mfma_f32_16x16x32_bf16 v[108:111], v[180:183], a[172:175], v[108:111]\n"
      "ds_read2st64_b32 v[76:77], v248 offset0:4 offset1:5\n"
      "v_and_b32 v232, 0xffff0000, v232\n"
      "v_mfma_f32_16x16x32_bf16 v[112:115], v[180:183], a[176:179], v[112:115]\n"
      "ds_read2st64_b32 v[78:79], v248 offset0:6 offset1:7\n"
      "v_fma_f32 v214, v203, v231, v214\n"
      "v_mfma_f32_16x16x32_bf16 v[116:119], v[180:183], a[180:183], v[116:119]\n"
      "ds_read2st64_b32 v[80:81], v249 offset0:8 offset1:9\n"
      "v_fma_f32 v215, v203, v232, v215\n"
      "v_mfma_f32_16x16x32_bf16 v[120:123], v[180:183], a[184:187], v[120:123]\n"
      "ds_read2st64_b32 v[82:83], v249 offset0:10 offset1:11\n"
      "v_lshlrev_b32 v227, 16, v228\n"
      "v_mfma_f32_16x16x32_bf16 v[124:127], v[180:183], a[188:191], v[124:127]\n"
      "ds_read2st64_b32 v[84:85], v249 offset0:12 offset1:13\n"
      "v_and_b32 v228, 0xffff0000, v228\n"
      "v_mfma_f32_16x16x32_bf16 v[104:107], v[184:187], a[200:203], v[104:107]\n"
      "ds_read2st64_b32 v[86:87], v249 offset0:14 offset1:15\n"
      "v_fma_f32 v216, v202, v227, v216\n"
      "v_mfma_f32_16x16x32_bf16 v[108:111], v[184:187], a[204:207], v[108:111]\n"
      "ds_read2st64_b32 v[88:89], v250 offset0:16 offset1:17\n"
      "v_fma_f32 v217, v202, v228, v217\n"
      "v_mfma_f32_16x16x32_bf16 v[112:115], v[184:187], a[208:211], v[112:115]\n"
      "ds_read2st64_b32 v[90:91], v250 offset0:18 offset1:19\n"
      "v_lshlrev_b32 v231, 16, v229\n"
      "v_mfma_f32_16x16x32_bf16 v[116:119], v[184:187], a[212:215], v[116:119]\n"
      "s_waitcnt lgkmcnt(6)\n"
      "ds_read2st64_b32 v[92:93], v250 offset0:20 offset1:21\n"
      "v_and_b32 v229, 0xffff0000, v229\n"
      "v_mfma_f32_16x16x32_bf16 v[120:123], v[184:187], a[216:219], v[120:123]\n"
      "ds_read2st64_b32 v[94:95], v250 offset0:22 offset1:23\n"
      "v_fma_f32 v218, v202, v231, v218\n"
      "v_mfma_f32_16x16x32_bf16 v[124:127], v[184:187], a[220:223], v[124:127]\n"
      "v_fma_f32 v219, v202, v229, v219\n"
      "v_lshlrev_b32 v227, 16, v230\n"
      "v_mfma_f32_16x16x32_bf16 v[104:107], v[188:191], a[232:235], v[104:107]\n"
      "v_and_b32 v230, 0xffff0000, v230\n"
      "v_fma_f32 v220, v203, v227, v220\n"
      "v_mfma_f32_16x16x32_bf16 v[108:111], v[188:191], a[236:239], v[108:111]\n"
      "v_fma_f32 v221, v203, v230, v221\n"
      "v_lshlrev_b32 v231, 16, v233\n"
      "v_mfma_f32_16x16x32_bf16 v[112:115], v[188:191], a[240:243], v[112:115]\n"
      "v_and_b32 v233, 0xffff0000, v233\n"
      "v_fma_f32 v222, v203, v231, v222\n"
      "v_mfma_f32_16x16x32_bf16 v[116:119], v[188:191], a[244:247], v[116:119]\n"
      "v_fma_f32 v223, v203, v233, v223\n"
      "v_add_u32 v254, 12288, v252\n"
      "v_mfma_f32_16x16x32_bf16 v[120:123], v[188:191], a[248:251], v[120:123]\n"
      "global_load_dwordx4 v[12:15], v254, s[62:63] offset:0 sc1\n"
      "v_fma_f32 v208, v204, v72, v208\n"
      "v_mfma_f32_16x16x32_bf16 v[124:127], v[188:191], a[252:255], v[124:127]\n"
      "global_load_dwordx4 v[16:19], v254, s[62:63] offset:1024 sc1\n"
      "v_fma_f32 v209, v204, v73, v209\n"
      "v_mfma_f32_16x16x32_bf16 v[64:67], v[128:131], a[0:3], 0\n"
      "global_load_dwordx4 v[20:23], v254, s[62:63] offset:2048 sc1\n"
      "s_lshr_b32 s43, s42, 7\n"
      "s_cmp_eq_u32 s43, s36\n"
      "s_cbranch_scc0 .Ls_e22_%=\n"
      "s_lshl2_add_u32 s44, s35, s34\n"
      "s_lshl2_add_u32 s43, s44, s33\n"
      "s_lshl_b32 s43, s43, 12\n"
      "s_add_u32 s43, s43, 524288\n"
      "v_add_u32 v60, s43, v252\n"
      "global_load_dwordx4 v[32:35], v60, s[58:59] offset:0 sc1\n"
      "global_load_dwordx4 v[36:39], v60, s[58:59] offset:1024 sc1\n"
      "global_load_dwordx4 v[40:43], v60, s[58:59] offset:2048 sc1\n"
      "global_load_dwordx4 v[44:47], v60, s[58:59] offset:3072 sc1\n"
      "v_and_b32 v58, 15, v255\n"
      "v_lshrrev_b32 v59, 5, v255\n"
      "v_lshl_or_b32 v58, v59, 4, v58\n"
      "s_lshl4_add_u32 s44, s42, s44\n"
      "s_lshl_b32 s44, s44, 6\n"
      "v_lshl_add_u32 v59, v58, 1, s44\n"
      "global_load_ushort v57, v59, s[16:17]\n"
      "s_mov_b32 s44, 131072\n"
      "v_lshl_add_u32 v58, v58, 2, s44\n"
      "s_lshl_b32 s43, s33, 13\n"
      "v_add_u32 v59, s43, v58\n"
      ".Ls_e22_%=:\n"
      "v_mfma_f32_16x16x32_bf16 v[68:71], v[128:131], a[4:7], 0\n"
      "s_add_u32 m0, s41, 12288\n"
      "s_nop 0\n"
      "ds_write_addtid_b32 v104 offset:59392\n"
      "v_mfma_f32_16x16x32_bf16 v[64:67], v[132:135], a[32:35], v[64:67]\n"
      "ds_write_addtid_b32 v105 offset:59648\n"
      "ds_write_addtid_b32 v106 offset:59904\n"
      "v_fma_f32 v210, v204, v74, v210\n"
      "v_mfma_f32_16x16x32_bf16 v[68:71], v[132:135], a[36:39], v[68:71]\n"
      "ds_write_addtid_b32 v107 offset:60160\n"
      "ds_write_addtid_b32 v108 offset:60416\n"
      "v_fma_f32 v211, v204, v75, v211\n"
      "v_mfma_f32_16x16x32_bf16 v[64:67], v[136:139], a[64:67], v[64:67]\n"
      "ds_write_addtid_b32 v109 offset:60672\n"
      "ds_write_addtid_b32 v110 offset:60928\n"
      "v_fma_f32 v212, v206, v76, v212\n"
      "v_mfma_f32_16x16x32_bf16 v[68:71], v[136:139], a[68:71], v[68:71]\n"
      "ds_write_addtid_b32 v111 offset:61184\n"
      "ds_write_addtid_b32 v112 offset:61440\n"
      "v_fma_f32 v213, v206, v77, v213\n"
      "v_mfma_f32_16x16x32_bf16 v[64:67], v[140:143], a[96:99], v[64:67]\n"
      "ds_write_addtid_b32 v113 offset:61696\n"
      "ds_write_addtid_b32 v114 offset:61952\n"
      "v_fma_f32 v214, v206, v78, v214\n"
      "v_mfma_f32_16x16x32_bf16 v[68:71], v[140:143], a[100:103], v[68:71]\n"
      "ds_write_addtid_b32 v115 offset:62208\n"
      "ds_write_addtid_b32 v116 offset:62464\n"
      "v_fma_f32 v215, v206, v79, v215\n"
      "v_mfma_f32_16x16x32_bf16 v[64:67], v[144:147], a[128:131], v[64:67]\n"
      "s_waitcnt lgkmcnt(15)\n"
      "ds_write_addtid_b32 v117 offset:62720\n"
      "ds_write_addtid_b32 v118 offset:62976\n"
      "v_fma_f32 v208, v204, v80, v208\n"
      "v_mfma_f32_16x16x32_bf16 v[68:71], v[144:147], a[132:135], v[68:71]\n"
      "ds_write_addtid_b32 v119 offset:63232\n"
      "ds_write_addtid_b32 v120 offset:63488\n"
      "v_fma_f32 v209, v204, v81, v209\n"
      "v_mfma_f32_16x16x32_bf16 v[64:67], v[148:151], a[160:163], v[64:67]\n"
      "ds_write_addtid_b32 v121 offset:63744\n"
      "ds_write_addtid_b32 v122 offset:64000\n"
      "v_fma_f32 v210, v204, v82, v210\n"
      "v_mfma_f32_16x16x32_bf16 v[68:71], v[148:151], a[164:167], v[68:71]\n"
      "ds_write_addtid_b32 v123 offset:64256\n"
      "ds_write_addtid_b32 v124 offset:64512\n"
      "v_fma_f32 v211, v204, v83, v211\n"
      "v_mfma_f32_16x16x32_bf16 v[64:67], v[152:155], a[192:195], v[64:67]\n"
      "ds_write_addtid_b32 v125 offset:64768\n"
      "ds_write_addtid_b32 v126 offset:65024\n"
      "v_fma_f32 v212, v206, v84, v212\n"
      "v_mfma_f32_16x16x32_bf16 v[68:71], v[152:155], a[196:199], v[68:71]\n"
      "ds_write_addtid_b32 v127 offset:65280\n"
      "v_fma_f32 v213, v206, v85, v213\n"
      "v_fma_f32 v214, v206, v86, v214\n"
      "v_mfma_f32_16x16x32_bf16 v[64:67], v[156:159], a[224:227], v[64:67]\n"
      "v_fma_f32 v215, v206, v87, v215\n"
      "s_waitcnt lgkmcnt(15)\n"
      "v_fma_f32 v208, v204, v88, v208\n"
      "v_mfma_f32_16x16x32_bf16 v[68:71], v[156:159], a[228:231], v[68:71]\n"
      "v_add_u32 v253, 24576, v252\n"
      "v_fma_f32 v209, v204, v89, v209\n"
      "v_fma_f32 v210, v204, v90, v210\n"
      "v_mfma_f32_16x16x32_bf16 v[96:99], v[160:163], a[0:3], 0\n"
      "global_load_dwordx4 v[224:227], v253, s[62:63] offset:0 sc1\n"
      "global_load_dwordx4 v[228:231], v253, s[62:63] offset:1024 sc1\n"
      "v_fma_f32 v211, v204, v91, v211\n"
      "v_mfma_f32_16x16x32_bf16 v[100:103], v[160:163], a[4:7], 0\n"
      "global_load_dwordx4 v[232:235], v253, s[62:63] offset:2048 sc1\n"
      "s_lshr_b32 s43, s42, 7\n"
      "s_cmp_eq_u32 s43, s36\n"
      "s_cbranch_scc0 .Ls_e23_%=\n"
      "s_waitcnt vmcnt(3)\n"
      "v_bitop3_b32 v61, v33, v35, s37 bitop3:0x7e\n"
      "v_bitop3_b32 v61, v61, v37, s37 bitop3:0xf6\n"
      "v_bitop3_b32 v61, v61, v39, s37 bitop3:0xf6\n"
      "v_bitop3_b32 v61, v61, v41, s37 bitop3:0xf6\n"
      "v_bitop3_b32 v61, v61, v43, s37 bitop3:0xf6\n"
      "v_bitop3_b32 v61, v61, v45, s37 bitop3:0xf6\n"
      "v_bitop3_b32 v61, v61, v47, s37 bitop3:0xf6\n"
      "v_cmp_ne_u32 vcc, 0, v61\n"
      "s_nop 1\n"
      "s_cbranch_vccnz .Ls_s25_%=\n"
      ".Ls_o26_%=:\n"
      "v_add_f32 v32, v32, v36\n"
      "v_add_f32 v34, v34, v38\n"
      "v_add_f32 v40, v40, v44\n"
      "v_add_f32 v42, v42, v46\n"
      "v_add_f32 v32, v32, v40\n"
      "v_add_f32 v34, v34, v42\n"
      "s_nop 1\n"
      "v_permlane32_swap_b32 v32, v34\n"
      "v_add_f32 v32, v32, v34\n"
      "v_mov_b32 v34, v32\n"
      "s_nop 1\n"
      "v_permlane16_swap_b32 v32, v34\n"
      "v_add_f32 v32, v32, v34\n"
      "ds_write_b32 v59, v32\n"
      "s_waitcnt lgkmcnt(0)\n"
      ".Ls_e23_%=:\n"
      "v_mfma_f32_16x16x32_bf16 v[96:99], v[164:167], a[32:35], v[96:99]\n"
      "s_waitcnt lgkmcnt(0)\n"
      "s_barrier\n"
      "v_mfma_f32_16x16x32_bf16 v[100:103], v[164:167], a[36:39], v[100:103]\n"
      "s_lshr_b32 s43, s42, 5\n"
      "s_lshl2_add_u32 s44, s36, s33\n"
      "s_cmp_eq_u32 s43, s44\n"
      "s_cbranch_scc0 .Ls_e27_%=\n"
      "ds_read_b32 v32, v58 offset:0\n"
      "ds_read_b32 v33, v58 offset:8192\n"
      "ds_read_b32 v34, v58 offset:16384\n"
      "ds_read_b32 v35, v58 offset:24576\n"
      "v_mov_b32 v62, 0\n"
      "v_mov_b32 v63, s37\n"
      "s_or_b32 s43, s34, s35\n"
      "s_cmp_eq_u32 s43, 0\n"
      "s_cbranch_scc0 .Ls_z28_%=\n"
      "s_mov_b64 exec, 1\n"
      "global_store_dword v62, v63, s[58:59] sc1\n"
      ".Ls_z28_%=:\n"
      "s_mov_b64 exec, -1\n"
      "s_waitcnt vmcnt(3)\n"
      "v_lshlrev_b32 v57, 16, v57\n"
      "v_and_b32 v56, 48, v255\n"
      "s_mov_b32 s44, 131072\n"
      "v_lshl_add_u32 v56, v56, 1, s44\n"
      "s_waitcnt lgkmcnt(0)\n"
      "v_add_f32 v32, v32, v33\n"
      "s_waitcnt lgkmcnt(0)\n"
      "v_add_f32 v34, v34, v35\n"
      "v_add_f32 v32, v32, v34\n"
      "v_cvt_pk_bf16_f32 v32, v32, v32\n"
      "v_and_b32 v32, 0xffff0000, v32\n"
      "v_mul_f32 v32, s80, v32\n"
      "v_cvt_pk_bf16_f32 v32, v32, v32\n"
      "v_and_b32 v32, 0xffff0000, v32\n"
      "v_add_f32 v32, v32, v57\n"
      "v_cvt_pk_bf16_f32 v32, v32, v32\n"
      "v_and_b32 v32, 0xffff0000, v32\n"
      "v_sub_f32 v32, v32, v57\n"
      "ds_write_b32 v58, v32 offset:1024\n"
      "ds_read_b128 v[36:39], v56 offset:1024\n"
      "ds_read_b128 v[40:43], v56 offset:1040\n"
      ".Ls_e27_%=:\n"
      "v_mfma_f32_16x16x32_bf16 v[96:99], v[168:171], a[64:67], v[96:99]\n"
      "ds_read2st64_b32 v[104:105], v248 offset0:24 offset1:25\n"
      "ds_read2st64_b32 v[106:107], v248 offset0:26 offset1:27\n"
      "v_fma_f32 v212, v206, v92, v212\n"
      "v_mfma_f32_16x16x32_bf16 v[100:103], v[168:171], a[68:71], v[100:103]\n"
      "ds_read2st64_b32 v[108:109], v248 offset0:28 offset1:29\n"
      "ds_read2st64_b32 v[110:111], v248 offset0:30 offset1:31\n"
      "v_fma_f32 v213, v206, v93, v213\n"
      "v_mfma_f32_16x16x32_bf16 v[96:99], v[172:175], a[96:99], v[96:99]\n"
      "ds_read2st64_b32 v[112:113], v249 offset0:32 offset1:33\n"
      "ds_read2st64_b32 v[114:115], v249 offset0:34 offset1:35\n"
      "v_fma_f32 v214, v206, v94, v214\n"
      "v_mfma_f32_16x16x32_bf16 v[100:103], v[172:175], a[100:103], v[100:103]\n"
      "ds_read2st64_b32 v[116:117], v249 offset0:36 offset1:37\n"
      "ds_read2st64_b32 v[118:119], v249 offset0:38 offset1:39\n"
      "v_fma_f32 v215, v206, v95, v215\n"
      "v_mfma_f32_16x16x32_bf16 v[96:99], v[176:179], a[128:131], v[96:99]\n"
      "ds_read2st64_b32 v[120:121], v250 offset0:40 offset1:41\n"
      "ds_read2st64_b32 v[122:123], v250 offset0:42 offset1:43\n"
      "v_mfma_f32_16x16x32_bf16 v[100:103], v[176:179], a[132:135], v[100:103]\n"
      "ds_read2st64_b32 v[124:125], v250 offset0:44 offset1:45\n"
      "ds_read2st64_b32 v[126:127], v250 offset0:46 offset1:47\n"
      "v_mfma_f32_16x16x32_bf16 v[96:99], v[180:183], a[160:163], v[96:99]\n"
      "v_mfma_f32_16x16x32_bf16 v[100:103], v[180:183], a[164:167], v[100:103]\n"
      "v_mfma_f32_16x16x32_bf16 v[96:99], v[184:187], a[192:195], v[96:99]\n"
      "v_mfma_f32_16x16x32_bf16 v[100:103], v[184:187], a[196:199], v[100:103]\n"
      "v_mfma_f32_16x16x32_bf16 v[96:99], v[188:191], a[224:227], v[96:99]\n"
      "v_mfma_f32_16x16x32_bf16 v[100:103], v[188:191], a[228:231], v[100:103]\n"
      "s_waitcnt vmcnt(3)\n"
      "v_bitop3_b32 v22, v15, v19, s37 bitop3:0x7e\n"
      "v_bitop3_b32 v22, v22, v23, s37 bitop3:0xf6\n"
      "v_cmp_ne_u32 vcc, 0, v22\n"
      "s_nop 1\n"
      "s_cbranch_vccnz .Ls_s30_%=\n"
      ".Ls_o31_%=:\n"
      "v_xor_b32 v202, s73, v204\n"
      "v_xor_b32 v203, s73, v206\n"
      "v_lshlrev_b32 v15, 16, v12\n"
      "v_and_b32 v12, 0xffff0000, v12\n"
      "v_fma_f32 v208, v202, v15, v208\n"
      "v_fma_f32 v209, v202, v12, v209\n"
      "v_lshlrev_b32 v19, 16, v13\n"
      "v_and_b32 v13, 0xffff0000, v13\n"
      "v_fma_f32 v210, v202, v19, v210\n"
      "v_fma_f32 v211, v202, v13, v211\n"
      "v_lshlrev_b32 v15, 16, v14\n"
      "v_and_b32 v14, 0xffff0000, v14\n"
      "v_fma_f32 v212, v203, v15, v212\n"
      "v_fma_f32 v213, v203, v14, v213\n"
      "v_lshlrev_b32 v19, 16, v20\n"
      "v_and_b32 v20, 0xffff0000, v20\n"
      "v_fma_f32 v214, v203, v19, v214\n"
      "v_fma_f32 v215, v203, v20, v215\n"
      "v_lshlrev_b32 v15, 16, v16\n"
      "v_and_b32 v16, 0xffff0000, v16\n"
      "v_fma_f32 v216, v202, v15, v216\n"
      "v_fma_f32 v217, v202, v16, v217\n"
      "v_lshlrev_b32 v19, 16, v17\n"
      "v_and_b32 v17, 0xffff0000, v17\n"
      "v_fma_f32 v218, v202, v19, v218\n"
      "v_fma_f32 v219, v202, v17, v219\n"
      "v_lshlrev_b32 v15, 16, v18\n"
      "v_and_b32 v18, 0xffff0000, v18\n"
      "v_fma_f32 v220, v203, v15, v220\n"
      "v_fma_f32 v221, v203, v18, v221\n"
      "v_lshlrev_b32 v19, 16, v21\n"
      "v_and_b32 v21, 0xffff0000, v21\n"
      "v_fma_f32 v222, v203, v19, v222\n"
      "v_fma_f32 v223, v203, v21, v223\n"
      "s_add_u32 s54, s41, 12288\n"
      "v_and_b32 v8, 15, v255\n"
      "v_and_b32 v9, 48, v255\n"
      "v_lshl_or_b32 v8, v8, 6, v9\n"
      "v_add_u32 v8, s54, v8\n"
      "v_add_u32 v9, s54, v252\n"
      "v_readfirstlane_b32 s55, v251\n"
      "v_lshrrev_b32 v10, 2, v255\n"
      "v_and_b32 v11, 48, v252\n"
      "v_lshl_or_b32 v10, v10, 10, v11\n"
      "v_add_u32 v10, s55, v10\n"
      "v_fma_f32 v208, v204, v64, v208\n"
      "v_fma_f32 v209, v204, v65, v209\n"
      "v_fma_f32 v210, v204, v66, v210\n"
      "v_fma_f32 v211, v204, v67, v211\n"
      "v_fma_f32 v212, v206, v68, v212\n"
      "v_fma_f32 v213, v206, v69, v213\n"
      "v_fma_f32 v214, v206, v70, v214\n"
      "v_fma_f32 v215, v206, v71, v215\n"
      "v_fma_f32 v216, v204, v96, v216\n"
      "v_fma_f32 v217, v204, v97, v217\n"
      "v_fma_f32 v218, v204, v98, v218\n"
      "v_fma_f32 v219, v204, v99, v219\n"
      "v_fma_f32 v220, v206, v100, v220\n"
      "v_fma_f32 v221, v206, v101, v221\n"
      "v_fma_f32 v222, v206, v102, v222\n"
      "v_fma_f32 v223, v206, v103, v223\n"
      "s_waitcnt lgkmcnt(7)\n"
      "v_pk_add_f32 v[104:105], v[104:105], v[112:113]\n"
      "s_waitcnt lgkmcnt(6)\n"
      "v_pk_add_f32 v[106:107], v[106:107], v[114:115]\n"
      "s_waitcnt lgkmcnt(5)\n"
      "v_pk_add_f32 v[108:109], v[108:109], v[116:117]\n"
      "s_waitcnt lgkmcnt(4)\n"
      "v_pk_add_f32 v[110:111], v[110:111], v[118:119]\n"
      "s_waitcnt lgkmcnt(3)\n"
      "v_pk_add_f32 v[104:105], v[104:105], v[120:121]\n"
      "s_waitcnt lgkmcnt(2)\n"
      "v_pk_add_f32 v[106:107], v[106:107], v[122:123]\n"
      "s_waitcnt lgkmcnt(1)\n"
      "v_pk_add_f32 v[108:109], v[108:109], v[124:125]\n"
      "s_waitcnt lgkmcnt(0)\n"
      "v_pk_add_f32 v[110:111], v[110:111], v[126:127]\n"
      "v_fma_f32 v216, v204, v104, v216\n"
      "v_fma_f32 v217, v204, v105, v217\n"
      "v_fma_f32 v218, v204, v106, v218\n"
      "v_fma_f32 v219, v204, v107, v219\n"
      "v_fma_f32 v220, v206, v108, v220\n"
      "v_fma_f32 v221, v206, v109, v221\n"
      "v_fma_f32 v222, v206, v110, v222\n"
      "v_fma_f32 v223, v206, v111, v223\n"
      "s_lshr_b32 s43, s42, 5\n"
      "s_lshl2_add_u32 s44, s36, s33\n"
      "s_cmp_eq_u32 s43, s44\n"
      "s_cbranch_scc0 .Ls_e32_%=\n"
      "v_lshrrev_b32 v32, 10, v251\n"
      "v_subrev_u32 v32, s42, v32\n"
      "v_cmp_eq_u32 vcc, 0, v32\n"
      "s_nop 1\n"
      "s_mov_b64 exec, vcc\n"
      "v_fma_f32 v208, v205, v36, v208\n"
      "v_fma_f32 v209, v205, v37, v209\n"
      "v_fma_f32 v210, v205, v38, v210\n"
      "v_fma_f32 v211, v205, v39, v211\n"
      "v_fma_f32 v216, v205, v40, v216\n"
      "v_fma_f32 v217, v205, v41, v217\n"
      "v_fma_f32 v218, v205, v42, v218\n"
      "v_fma_f32 v219, v205, v43, v219\n"
      "s_mov_b64 exec, -1\n"
      "v_cmp_eq_u32 vcc, -16, v32\n"
      "s_nop 1\n"
      "s_mov_b64 exec, vcc\n"
      "v_fma_f32 v212, v207, v36, v212\n"
      "v_fma_f32 v213, v207, v37, v213\n"
      "v_fma_f32 v214, v207, v38, v214\n"
      "v_fma_f32 v215, v207, v39, v215\n"
      "v_fma_f32 v220, v207, v40, v220\n"
      "v_fma_f32 v221, v207, v41, v221\n"
      "v_fma_f32 v222, v207, v42, v222\n"
      "v_fma_f32 v223, v207, v43, v223\n"
      "s_mov_b64 exec, -1\n"
      ".Ls_e32_%=:\n"
      "v_xor_b32 v202, s74, v204\n"
      "v_xor_b32 v203, s74, v206\n"
      "s_waitcnt vmcnt(0)\n"
      "v_bitop3_b32 v234, v227, v231, s37 bitop3:0x7e\n"
      "v_bitop3_b32 v234, v234, v235, s37 bitop3:0xf6\n"
      "v_cmp_ne_u32 vcc, 0, v234\n"
      "s_nop 1\n"
      "s_cbranch_vccnz .Ls_s34_%=\n"
      ".Ls_o35_%=:\n"
      "v_lshlrev_b32 v227, 16, v224\n"
      "v_and_b32 v224, 0xffff0000, v224\n"
      "v_fma_f32 v208, v202, v227, v208\n"
      "v_fma_f32 v209, v202, v224, v209\n"
      "v_lshlrev_b32 v231, 16, v225\n"
      "v_and_b32 v225, 0xffff0000, v225\n"
      "v_fma_f32 v210, v202, v231, v210\n"
      "v_fma_f32 v211, v202, v225, v211\n"
      "v_lshlrev_b32 v227, 16, v228\n"
      "v_and_b32 v228, 0xffff0000, v228\n"
      "v_fma_f32 v216, v202, v227, v216\n"
      "v_fma_f32 v217, v202, v228, v217\n"
      "v_lshlrev_b32 v231, 16, v229\n"
      "v_and_b32 v229, 0xffff0000, v229\n"
      "v_fma_f32 v218, v202, v231, v218\n"
      "v_fma_f32 v219, v202, v229, v219\n"
      "v_cvt_pk_bf16_f32 v72, v208, v209\n"
      "v_cvt_pk_bf16_f32 v73, v210, v211\n"
      "v_cvt_pk_bf16_f32 v74, v216, v217\n"
      "v_cvt_pk_bf16_f32 v75, v218, v219\n"
      "ds_write_b128 v8, v[72:75] offset:0\n"
      "ds_read_b128 v[80:83], v9 offset:0\n"
      "v_lshlrev_b32 v227, 16, v226\n"
      "v_and_b32 v226, 0xffff0000, v226\n"
      "v_fma_f32 v212, v203, v227, v212\n"
      "v_fma_f32 v213, v203, v226, v213\n"
      "v_lshlrev_b32 v231, 16, v232\n"
      "v_and_b32 v232, 0xffff0000, v232\n"
      "v_fma_f32 v214, v203, v231, v214\n"
      "v_fma_f32 v215, v203, v232, v215\n"
      "v_lshlrev_b32 v227, 16, v230\n"
      "v_and_b32 v230, 0xffff0000, v230\n"
      "v_fma_f32 v220, v203, v227, v220\n"
      "v_fma_f32 v221, v203, v230, v221\n"
      "v_lshlrev_b32 v231, 16, v233\n"
      "v_and_b32 v233, 0xffff0000, v233\n"
      "v_fma_f32 v222, v203, v231, v222\n"
      "v_fma_f32 v223, v203, v233, v223\n"
      "v_cvt_pk_bf16_f32 v76, v212, v213\n"
      "v_cvt_pk_bf16_f32 v77, v214, v215\n"
      "v_cvt_pk_bf16_f32 v78, v220, v221\n"
      "v_cvt_pk_bf16_f32 v79, v222, v223\n"
      "ds_write_b128 v8, v[76:79] offset:1024\n"
      "ds_read_b128 v[84:87], v9 offset:1024\n"
      "s_waitcnt lgkmcnt(2)\n"
      "global_store_dwordx4 v10, v[80:83], s[20:21]\n"
      "s_waitcnt lgkmcnt(0)\n"
      "global_store_dwordx4 v10, v[84:87], s[68:69]\n"
      "s_endpgm\n"
      ".Lprod_%=:\n"
      "s_add_u32 s58, s18, 16777216\n"
      "s_addc_u32 s59, s19, 0\n"
      "v_mov_b32 v235, 0\n"
      "global_load_dword v235, v235, s[58:59] sc1\n"
      "v_and_b32 v238, 15, v255\n"
      "v_lshrrev_b32 v239, 4, v255\n"
      "v_xor_b32 v239, v239, v238\n"
      "v_and_b32 v239, 7, v239\n"
      "v_lshlrev_b32 v239, 4, v239\n"
      "v_lshl_or_b32 v238, v238, 7, v239\n"
      "v_add_u32 v248, s41, v238\n"
      "v_xor_b32 v249, 64, v248\n"
      "v_add_u32 v250, 65536, v238\n"
      "v_xor_b32 v251, 64, v250\n"
      "v_and_b32 v253, 8, v255\n"
      "v_lshlrev_b32 v253, 7, v253\n"
      "v_and_or_b32 v253, v255, 48, v253\n"
      "v_add_u32 v253, s41, v253\n"
      "v_add_u32 v238, s76, v252\n"
      "v_and_b32 v238, 0x3ff, v238\n"
      "v_lshl_or_b32 v244, v236, 12, v237\n"
      "v_add_u32 v245, 31744, v244\n"
      "v_add_u32 v246, 63488, v244\n"
      "v_add_u32 v247, 95232, v244\n"
      "s_bitcmp1_b32 s34, 1\n"
      "s_cselect_b64 s[8:9], s[10:11], s[8:9]\n"
      "s_and_b32 s45, s34, 1\n"
      "s_lshl2_add_u32 s45, s45, s33\n"
      "s_lshl_b32 s45, s45, 9\n"
      "s_lshl_b32 s72, s36, 19\n"
      "s_add_u32 s45, s45, s72\n"
      "s_add_u32 s8, s8, s45\n"
      "s_addc_u32 s9, s9, 0\n"
      "s_lshr_b32 s39, s32, 6\n"
      "s_and_b32 s45, s35, 2\n"
      "s_xor_b32 s39, s39, s45\n"
      "s_lshl_b32 s45, s39, 7\n"
      "s_add_u32 s72, s33, 0\n"
      "s_and_b32 s72, s72, 3\n"
      "s_lshl_b32 s72, s72, 17\n"
      "s_or_b32 s72, s72, s45\n"
      "s_add_u32 s73, s33, 1\n"
      "s_and_b32 s73, s73, 3\n"
      "s_lshl_b32 s73, s73, 17\n"
      "s_or_b32 s73, s73, s45\n"
      "s_add_u32 s74, s33, 2\n"
      "s_and_b32 s74, s74, 3\n"
      "s_lshl_b32 s74, s74, 17\n"
      "s_or_b32 s74, s74, s45\n"
      "s_add_u32 s75, s33, 3\n"
      "s_and_b32 s75, s75, 3\n"
      "s_lshl_b32 s75, s75, 17\n"
      "s_or_b32 s75, s75, s45\n"
      "s_lshl2_add_u32 s43, s34, s35\n"
      "s_and_b32 s44, s40, 31\n"
      "s_lshl_b32 s45, s43, 18\n"
      "s_lshl_b32 s42, s44, 13\n"
      "s_add_u32 s45, s45, s42\n"
      "s_add_u32 s45, s45, 4096\n"
      "s_add_u32 s62, s18, s45\n"
      "s_addc_u32 s63, s19, 0\n"
      "s_lshl3_add_u32 s42, s43, s79\n"
      "s_lshl2_add_u32 s42, s42, s33\n"
      "s_lshl_b32 s42, s42, 8\n"
      "s_add_u32 s42, s42, 65536\n"
      "s_mov_b32 s64, 0xaaaaaaaa\n"
      "s_mov_b32 s65, 0xaaaaaaaa\n"
      "s_mov_b32 s66, 0xcccccccc\n"
      "s_mov_b32 s67, 0xcccccccc\n"
      "s_add_u32 s45, s76, 256\n"
      "s_and_b32 s45, s45, 0x380\n"
      "s_add_u32 s46, s68, s45\n"
      "s_addc_u32 s47, s69, 0\n"
      "s_add_u32 m0, s41, 8192\n"
      "s_nop 0\n"
      "global_load_lds_dwordx4 v240, s[46:47] offset:0\n"
      "global_load_lds_dwordx4 v241, s[46:47] offset:1024\n"
      "global_load_lds_dwordx4 v242, s[46:47] offset:2048\n"
      "global_load_lds_dwordx4 v243, s[46:47] offset:3072\n"
      "s_add_u32 s45, s76, 384\n"
      "s_and_b32 s45, s45, 0x380\n"
      "s_add_u32 s46, s68, s45\n"
      "s_addc_u32 s47, s69, 0\n"
      "s_add_u32 m0, s41, 12288\n"
      "s_nop 0\n"
      "global_load_lds_dwordx4 v240, s[46:47] offset:0\n"
      "global_load_lds_dwordx4 v241, s[46:47] offset:1024\n"
      "global_load_lds_dwordx4 v242, s[46:47] offset:2048\n"
      "global_load_lds_dwordx4 v243, s[46:47] offset:3072\n"
      "s_waitcnt vmcnt(13)\n"
      "s_barrier\n"
      "ds_read_b128 v[0:3], v248 offset:0\n"
      "ds_read_b128 v[4:7], v248 offset:2048\n"
      "ds_read_b128 v[8:11], v249 offset:0\n"
      "ds_read_b128 v[12:15], v249 offset:2048\n"
      "ds_read_b128 a[192:195], v250 offset:0\n"
      "ds_read_b128 a[196:199], v250 offset:2048\n"
      "ds_read_b128 a[200:203], v250 offset:32768\n"
      "ds_read_b128 a[204:207], v251 offset:0\n"
      "ds_read_b128 a[208:211], v251 offset:2048\n"
      "ds_read_b128 a[212:215], v251 offset:32768\n"
      "s_add_u32 s45, s34, 1\n"
      "s_and_b32 s45, s45, 3\n"
      "s_lshl_b32 s45, s45, 15\n"
      "s_or_b32 s45, s45, s77\n"
      "s_xor_b32 s45, s45, 512\n"
      "s_add_u32 s46, s12, s45\n"
      "s_addc_u32 s47, s13, 0\n"
      "s_add_u32 m0, s78, 81920\n"
      "s_nop 0\n"
      "global_load_lds_dwordx4 v240, s[46:47] offset:0\n"
      "global_load_lds_dwordx4 v241, s[46:47] offset:1024\n"
      "global_load_lds_dwordx4 v242, s[46:47] offset:2048\n"
      "global_load_lds_dwordx4 v243, s[46:47] offset:3072\n"
      "s_add_u32 s45, s34, 2\n"
      "s_and_b32 s45, s45, 3\n"
      "s_lshl_b32 s45, s45, 15\n"
      "s_or_b32 s45, s45, s77\n"
      "s_xor_b32 s45, s45, 512\n"
      "s_add_u32 s46, s12, s45\n"
      "s_addc_u32 s47, s13, 0\n"
      "s_add_u32 m0, s78, 114688\n"
      "s_nop 0\n"
      "global_load_lds_dwordx4 v240, s[46:47] offset:0\n"
      "global_load_lds_dwordx4 v241, s[46:47] offset:1024\n"
      "s_waitcnt lgkmcnt(5)\n"
      "v_mfma_f32_16x16x32_bf16 v[192:195], v[0:3], a[192:195], 0\n"
      "s_add_u32 s45, s76, 512\n"
      "s_and_b32 s45, s45, 0x380\n"
      "s_add_u32 s46, s68, s45\n"
      "s_addc_u32 s47, s69, 0\n"
      "s_add_u32 m0, s41, 0\n"
      "s_nop 0\n"
      "global_load_lds_dwordx4 v240, s[46:47] offset:0\n"
      "global_load_lds_dwordx4 v241, s[46:47] offset:1024\n"
      "global_load_lds_dwordx4 v242, s[46:47] offset:2048\n"
      "global_load_lds_dwordx4 v243, s[46:47] offset:3072\n"
      "v_mfma_f32_16x16x32_bf16 v[196:199], v[4:7], a[192:195], 0\n"
      "ds_read_b128 a[216:219], v250 offset:4096\n"
      "ds_read_b128 a[220:223], v250 offset:6144\n"
      "ds_read_b128 a[224:227], v250 offset:36864\n"
      "s_waitcnt lgkmcnt(7)\n"
      "v_mfma_f32_16x16x32_bf16 v[200:203], v[0:3], a[196:199], 0\n"
      "ds_read_b128 a[228:231], v251 offset:4096\n"
      "ds_read_b128 a[232:235], v251 offset:6144\n"
      "ds_read_b128 a[236:239], v251 offset:36864\n"
      "v_mfma_f32_16x16x32_bf16 v[204:207], v[4:7], a[196:199], 0\n"
      "s_waitcnt vmcnt(19)\n"
      "ds_read_b128 v[16:19], v248 offset:4096\n"
      "ds_read_b128 v[20:23], v248 offset:6144\n"
      "s_waitcnt lgkmcnt(11)\n"
      "v_mfma_f32_16x16x32_bf16 v[208:211], v[0:3], a[200:203], 0\n"
      "ds_read_b128 v[24:27], v249 offset:4096\n"
      "ds_read_b128 v[28:31], v249 offset:6144\n"
      "v_mfma_f32_16x16x32_bf16 v[212:215], v[4:7], a[200:203], 0\n"
      "s_waitcnt lgkmcnt(12)\n"
      "v_mfma_f32_16x16x32_bf16 v[192:195], v[8:11], a[204:207], v[192:195]\n"
      "v_mfma_f32_16x16x32_bf16 v[196:199], v[12:15], a[204:207], v[196:199]\n"
      "s_waitcnt lgkmcnt(11)\n"
      "v_mfma_f32_16x16x32_bf16 v[200:203], v[8:11], a[208:211], v[200:203]\n"
      "v_mfma_f32_16x16x32_bf16 v[204:207], v[12:15], a[208:211], v[204:207]\n"
      "s_waitcnt lgkmcnt(10)\n"
      "v_mfma_f32_16x16x32_bf16 v[208:211], v[8:11], a[212:215], v[208:211]\n"
      "v_mfma_f32_16x16x32_bf16 v[212:215], v[12:15], a[212:215], v[212:215]\n"
      "s_waitcnt lgkmcnt(3)\n"
      "v_mfma_f32_16x16x32_bf16 v[192:195], v[16:19], a[216:219], v[192:195]\n"
      "s_waitcnt lgkmcnt(0)\n"
      "s_add_u32 s45, s76, 640\n"
      "s_and_b32 s45, s45, 0x380\n"
      "s_add_u32 s46, s68, s45\n"
      "s_addc_u32 s47, s69, 0\n"
      "s_add_u32 m0, s41, 4096\n"
      "s_nop 0\n"
      "global_load_lds_dwordx4 v240, s[46:47] offset:0\n"
      "global_load_lds_dwordx4 v241, s[46:47] offset:1024\n"
      "global_load_lds_dwordx4 v242, s[46:47] offset:2048\n"
      "global_load_lds_dwordx4 v243, s[46:47] offset:3072\n"
      "v_mfma_f32_16x16x32_bf16 v[196:199], v[20:23], a[216:219], v[196:199]\n"
      "ds_read_b128 a[240:243], v250 offset:8192\n"
      "ds_read_b128 a[244:247], v250 offset:10240\n"
      "ds_read_b128 a[248:251], v250 offset:40960\n"
      "v_mfma_f32_16x16x32_bf16 v[200:203], v[16:19], a[220:223], v[200:203]\n"
      "ds_read_b128 a[252:255], v251 offset:8192\n"
      "ds_read_b128 a[192:195], v251 offset:10240\n"
      "ds_read_b128 a[196:199], v251 offset:40960\n"
      "v_mfma_f32_16x16x32_bf16 v[204:207], v[20:23], a[220:223], v[204:207]\n"
      "s_waitcnt vmcnt(18)\n"
      "ds_read_b128 v[32:35], v248 offset:8192\n"
      "ds_read_b128 v[36:39], v248 offset:10240\n"
      "v_mfma_f32_16x16x32_bf16 v[208:211], v[16:19], a[224:227], v[208:211]\n"
      "ds_read_b128 v[40:43], v249 offset:8192\n"
      "ds_read_b128 v[44:47], v249 offset:10240\n"
      "v_mfma_f32_16x16x32_bf16 v[212:215], v[20:23], a[224:227], v[212:215]\n"
      "v_mfma_f32_16x16x32_bf16 v[192:195], v[24:27], a[228:231], v[192:195]\n"
      "v_mfma_f32_16x16x32_bf16 v[196:199], v[28:31], a[228:231], v[196:199]\n"
      "v_mfma_f32_16x16x32_bf16 v[200:203], v[24:27], a[232:235], v[200:203]\n"
      "v_mfma_f32_16x16x32_bf16 v[204:207], v[28:31], a[232:235], v[204:207]\n"
      "v_mfma_f32_16x16x32_bf16 v[208:211], v[24:27], a[236:239], v[208:211]\n"
      "v_mfma_f32_16x16x32_bf16 v[212:215], v[28:31], a[236:239], v[212:215]\n"
      "s_waitcnt lgkmcnt(3)\n"
      "v_mfma_f32_16x16x32_bf16 v[192:195], v[32:35], a[240:243], v[192:195]\n"
      "s_waitcnt lgkmcnt(0)\n"
      "s_add_u32 s45, s76, 768\n"
      "s_and_b32 s45, s45, 0x380\n"
      "s_add_u32 s46, s68, s45\n"
      "s_addc_u32 s47, s69, 0\n"
      "s_add_u32 m0, s41, 8192\n"
      "s_nop 0\n"
      "global_load_lds_dwordx4 v240, s[46:47] offset:0\n"
      "global_load_lds_dwordx4 v241, s[46:47] offset:1024\n"
      "global_load_lds_dwordx4 v242, s[46:47] offset:2048\n"
      "global_load_lds_dwordx4 v243, s[46:47] offset:3072\n"
      "v_mfma_f32_16x16x32_bf16 v[196:199], v[36:39], a[240:243], v[196:199]\n"
      "v_readfirstlane_b32 s37, v235\n"
      "s_nop 1\n"
      "s_mul_i32 s37, s37, 0x9E3779B1\n"
      "v_mfma_f32_16x16x32_bf16 v[200:203], v[32:35], a[244:247], v[200:203]\n"
      "s_add_u32 s37, s37, 0x7F4A7C15\n"
      "s_mov_b32 s38, 0x8000\n"
      "ds_read_b128 a[200:203], v250 offset:12288\n"
      "ds_read_b128 a[204:207], v250 offset:14336\n"
      "v_mfma_f32_16x16x32_bf16 v[204:207], v[36:39], a[244:247], v[204:207]\n"
      "ds_read_b128 a[208:211], v250 offset:45056\n"
      "ds_read_b128 a[212:215], v251 offset:12288\n"
      "ds_read_b128 a[216:219], v251 offset:14336\n"
      "v_mfma_f32_16x16x32_bf16 v[208:211], v[32:35], a[248:251], v[208:211]\n"
      "ds_read_b128 a[220:223], v251 offset:45056\n"
      "s_waitcnt vmcnt(18)\n"
      "ds_read_b128 v[48:51], v248 offset:12288\n"
      "v_mfma_f32_16x16x32_bf16 v[212:215], v[36:39], a[248:251], v[212:215]\n"
      "ds_read_b128 v[52:55], v248 offset:14336\n"
      "ds_read_b128 v[56:59], v249 offset:12288\n"
      "ds_read_b128 v[60:63], v249 offset:14336\n"
      "v_mfma_f32_16x16x32_bf16 v[192:195], v[40:43], a[252:255], v[192:195]\n"
      "v_mfma_f32_16x16x32_bf16 v[196:199], v[44:47], a[252:255], v[196:199]\n"
      "v_mfma_f32_16x16x32_bf16 v[200:203], v[40:43], a[192:195], v[200:203]\n"
      "v_mfma_f32_16x16x32_bf16 v[204:207], v[44:47], a[192:195], v[204:207]\n"
      "v_mfma_f32_16x16x32_bf16 v[208:211], v[40:43], a[196:199], v[208:211]\n"
      "v_mfma_f32_16x16x32_bf16 v[212:215], v[44:47], a[196:199], v[212:215]\n"
      "s_waitcnt lgkmcnt(3)\n"
      "v_mfma_f32_16x16x32_bf16 v[192:195], v[48:51], a[200:203], v[192:195]\n"
      "s_waitcnt lgkmcnt(0)\n"
      "s_add_u32 s45, s76, 896\n"
      "s_and_b32 s45, s45, 0x380\n"
      "s_add_u32 s46, s68, s45\n"
      "s_addc_u32 s47, s69, 0\n"
      "s_add_u32 m0, s41, 12288\n"
      "s_nop 0\n"
      "global_load_lds_dwordx4 v240, s[46:47] offset:0\n"
      "global_load_lds_dwordx4 v241, s[46:47] offset:1024\n"
      "global_load_lds_dwordx4 v242, s[46:47] offset:2048\n"
      "global_load_lds_dwordx4 v243, s[46:47] offset:3072\n"
      "v_mfma_f32_16x16x32_bf16 v[196:199], v[52:55], a[200:203], v[196:199]\n"
      "s_waitcnt vmcnt(16)\n"
      "s_barrier\n"
      "ds_read_b128 a[224:227], v250 offset:16384\n"
      "v_mfma_f32_16x16x32_bf16 v[200:203], v[48:51], a[204:207], v[200:203]\n"
      "ds_read_b128 a[228:231], v250 offset:18432\n"
      "ds_read_b128 a[232:235], v250 offset:49152\n"
      "ds_read_b128 a[236:239], v251 offset:16384\n"
      "v_mfma_f32_16x16x32_bf16 v[204:207], v[52:55], a[204:207], v[204:207]\n"
      "ds_read_b128 a[240:243], v251 offset:18432\n"
      "ds_read_b128 a[244:247], v251 offset:49152\n"
      "s_waitcnt vmcnt(12)\n"
      "ds_read_b128 v[64:67], v248 offset:0\n"
      "v_mfma_f32_16x16x32_bf16 v[208:211], v[48:51], a[208:211], v[208:211]\n"
      "ds_read_b128 v[68:71], v248 offset:2048\n"
      "ds_read_b128 v[72:75], v249 offset:0\n"
      "ds_read_b128 v[76:79], v249 offset:2048\n"
      "v_mfma_f32_16x16x32_bf16 v[212:215], v[52:55], a[208:211], v[212:215]\n"
      "v_mfma_f32_16x16x32_bf16 v[192:195], v[56:59], a[212:215], v[192:195]\n"
      "v_mfma_f32_16x16x32_bf16 v[196:199], v[60:63], a[212:215], v[196:199]\n"
      "v_mfma_f32_16x16x32_bf16 v[200:203], v[56:59], a[216:219], v[200:203]\n"
      "v_mfma_f32_16x16x32_bf16 v[204:207], v[60:63], a[216:219], v[204:207]\n"
      "v_mfma_f32_16x16x32_bf16 v[208:211], v[56:59], a[220:223], v[208:211]\n"
      "v_mfma_f32_16x16x32_bf16 v[212:215], v[60:63], a[220:223], v[212:215]\n"
      "s_waitcnt lgkmcnt(3)\n"
      "v_mfma_f32_16x16x32_bf16 v[192:195], v[64:67], a[224:227], v[192:195]\n"
      "s_waitcnt lgkmcnt(0)\n"
      "s_add_u32 m0, s41, 0\n"
      "s_nop 0\n"
      "global_load_lds_dwordx4 v238, s[24:25]\n"
      "s_add_u32 m0, s41, 1024\n"
      "s_nop 0\n"
      "global_load_lds_dwordx4 v238, s[22:23]\n"
      "v_mfma_f32_16x16x32_bf16 v[196:199], v[68:71], a[224:227], v[196:199]\n"
      "s_add_u32 s45, s34, 2\n"
      "s_and_b32 s45, s45, 3\n"
      "s_lshl_b32 s45, s45, 15\n"
      "s_or_b32 s45, s45, s77\n"
      "s_add_u32 s46, s12, s45\n"
      "s_addc_u32 s47, s13, 0\n"
      "s_add_u32 m0, s78, 98304\n"
      "s_nop 0\n"
      "global_load_lds_dwordx4 v242, s[46:47] offset:2048\n"
      "global_load_lds_dwordx4 v243, s[46:47] offset:3072\n"
      "v_mfma_f32_16x16x32_bf16 v[200:203], v[64:67], a[228:231], v[200:203]\n"
      "ds_read_b128 a[248:251], v250 offset:20480\n"
      "ds_read_b128 a[252:255], v250 offset:22528\n"
      "ds_read_b128 a[192:195], v250 offset:53248\n"
      "v_mfma_f32_16x16x32_bf16 v[204:207], v[68:71], a[228:231], v[204:207]\n"
      "ds_read_b128 a[196:199], v251 offset:20480\n"
      "ds_read_b128 a[200:203], v251 offset:22528\n"
      "ds_read_b128 a[204:207], v251 offset:53248\n"
      "v_mfma_f32_16x16x32_bf16 v[208:211], v[64:67], a[232:235], v[208:211]\n"
      "s_waitcnt vmcnt(12)\n"
      "ds_read_b128 v[80:83], v248 offset:4096\n"
      "ds_read_b128 v[84:87], v248 offset:6144\n"
      "v_mfma_f32_16x16x32_bf16 v[212:215], v[68:71], a[232:235], v[212:215]\n"
      "ds_read_b128 v[88:91], v249 offset:4096\n"
      "ds_read_b128 v[92:95], v249 offset:6144\n"
      "v_mfma_f32_16x16x32_bf16 v[192:195], v[72:75], a[236:239], v[192:195]\n"
      "v_mfma_f32_16x16x32_bf16 v[196:199], v[76:79], a[236:239], v[196:199]\n"
      "v_mfma_f32_16x16x32_bf16 v[200:203], v[72:75], a[240:243], v[200:203]\n"
      "v_mfma_f32_16x16x32_bf16 v[204:207], v[76:79], a[240:243], v[204:207]\n"
      "v_mfma_f32_16x16x32_bf16 v[208:211], v[72:75], a[244:247], v[208:211]\n"
      "v_mfma_f32_16x16x32_bf16 v[212:215], v[76:79], a[244:247], v[212:215]\n"
      "s_waitcnt lgkmcnt(3)\n"
      "v_mfma_f32_16x16x32_bf16 v[192:195], v[80:83], a[248:251], v[192:195]\n"
      "s_add_u32 s45, s34, 2\n"
      "s_and_b32 s45, s45, 3\n"
      "s_lshl_b32 s45, s45, 15\n"
      "s_or_b32 s45, s45, s77\n"
      "s_xor_b32 s45, s45, 512\n"
      "s_add_u32 s46, s12, s45\n"
      "s_addc_u32 s47, s13, 0\n"
      "s_add_u32 m0, s78, 114688\n"
      "s_nop 0\n"
      "global_load_lds_dwordx4 v242, s[46:47] offset:2048\n"
      "global_load_lds_dwordx4 v243, s[46:47] offset:3072\n"
      "s_waitcnt lgkmcnt(2)\n"
      "v_mfma_f32_16x16x32_bf16 v[196:199], v[84:87], a[248:251], v[196:199]\n"
      "s_add_u32 s45, s34, 3\n"
      "s_and_b32 s45, s45, 3\n"
      "s_lshl_b32 s45, s45, 15\n"
      "s_or_b32 s45, s45, s77\n"
      "s_add_u32 s46, s12, s45\n"
      "s_addc_u32 s47, s13, 0\n"
      "s_add_u32 m0, s78, 131072\n"
      "s_nop 0\n"
      "global_load_lds_dwordx4 v240, s[46:47] offset:0\n"
      "global_load_lds_dwordx4 v241, s[46:47] offset:1024\n"
      "global_load_lds_dwordx4 v242, s[46:47] offset:2048\n"
      "global_load_lds_dwordx4 v243, s[46:47] offset:3072\n"
      "v_mfma_f32_16x16x32_bf16 v[200:203], v[80:83], a[252:255], v[200:203]\n"
      "ds_read_b128 a[208:211], v250 offset:24576\n"
      "ds_read_b128 a[212:215], v250 offset:26624\n"
      "ds_read_b128 a[216:219], v250 offset:57344\n"
      "v_mfma_f32_16x16x32_bf16 v[204:207], v[84:87], a[252:255], v[204:207]\n"
      "ds_read_b128 a[220:223], v251 offset:24576\n"
      "ds_read_b128 a[224:227], v251 offset:26624\n"
      "ds_read_b128 a[228:231], v251 offset:57344\n"
      "v_mfma_f32_16x16x32_bf16 v[208:211], v[80:83], a[192:195], v[208:211]\n"
      "s_waitcnt vmcnt(14)\n"
      "ds_read_b128 v[96:99], v248 offset:8192\n"
      "ds_read_b128 v[100:103], v248 offset:10240\n"
      "v_mfma_f32_16x16x32_bf16 v[212:215], v[84:87], a[192:195], v[212:215]\n"
      "ds_read_b128 v[104:107], v249 offset:8192\n"
      "ds_read_b128 v[108:111], v249 offset:10240\n"
      "s_waitcnt lgkmcnt(11)\n"
      "v_mfma_f32_16x16x32_bf16 v[192:195], v[88:91], a[196:199], v[192:195]\n"
      "s_waitcnt lgkmcnt(10)\n"
      "v_mfma_f32_16x16x32_bf16 v[196:199], v[92:95], a[196:199], v[196:199]\n"
      "v_mfma_f32_16x16x32_bf16 v[200:203], v[88:91], a[200:203], v[200:203]\n"
      "v_mfma_f32_16x16x32_bf16 v[204:207], v[92:95], a[200:203], v[204:207]\n"
      "v_mfma_f32_16x16x32_bf16 v[208:211], v[88:91], a[204:207], v[208:211]\n"
      "v_mfma_f32_16x16x32_bf16 v[212:215], v[92:95], a[204:207], v[212:215]\n"
      "s_waitcnt lgkmcnt(3)\n"
      "v_mfma_f32_16x16x32_bf16 v[192:195], v[96:99], a[208:211], v[192:195]\n"
      "s_add_u32 s45, s34, 3\n"
      "s_and_b32 s45, s45, 3\n"
      "s_lshl_b32 s45, s45, 15\n"
      "s_or_b32 s45, s45, s77\n"
      "s_xor_b32 s45, s45, 512\n"
      "s_add_u32 s46, s12, s45\n"
      "s_addc_u32 s47, s13, 0\n"
      "s_add_u32 m0, s78, 147456\n"
      "s_nop 0\n"
      "global_load_lds_dwordx4 v240, s[46:47] offset:0\n"
      "global_load_lds_dwordx4 v241, s[46:47] offset:1024\n"
      "global_load_lds_dwordx4 v242, s[46:47] offset:2048\n"
      "global_load_lds_dwordx4 v243, s[46:47] offset:3072\n"
      "s_waitcnt lgkmcnt(2)\n"
      "v_mfma_f32_16x16x32_bf16 v[196:199], v[100:103], a[208:211], v[196:199]\n"
      "ds_read_b128 a[232:235], v250 offset:28672\n"
      "ds_read_b128 a[236:239], v250 offset:30720\n"
      "ds_read_b128 a[240:243], v250 offset:61440\n"
      "v_mfma_f32_16x16x32_bf16 v[200:203], v[96:99], a[212:215], v[200:203]\n"
      "ds_read_b128 a[244:247], v251 offset:28672\n"
      "ds_read_b128 a[248:251], v251 offset:30720\n"
      "ds_read_b128 a[252:255], v251 offset:61440\n"
      "v_mfma_f32_16x16x32_bf16 v[204:207], v[100:103], a[212:215], v[204:207]\n"
      "s_waitcnt vmcnt(14)\n"
      "ds_read_b128 v[112:115], v248 offset:12288\n"
      "ds_read_b128 v[116:119], v248 offset:14336\n"
      "v_mfma_f32_16x16x32_bf16 v[208:211], v[96:99], a[216:219], v[208:211]\n"
      "ds_read_b128 v[120:123], v249 offset:12288\n"
      "ds_read_b128 v[124:127], v249 offset:14336\n"
      "v_mfma_f32_16x16x32_bf16 v[212:215], v[100:103], a[216:219], v[212:215]\n"
      "s_waitcnt lgkmcnt(11)\n"
      "v_mfma_f32_16x16x32_bf16 v[192:195], v[104:107], a[220:223], v[192:195]\n"
      "s_waitcnt lgkmcnt(10)\n"
      "v_mfma_f32_16x16x32_bf16 v[196:199], v[108:111], a[220:223], v[196:199]\n"
      "v_mfma_f32_16x16x32_bf16 v[200:203], v[104:107], a[224:227], v[200:203]\n"
      "v_mfma_f32_16x16x32_bf16 v[204:207], v[108:111], a[224:227], v[204:207]\n"
      "v_mfma_f32_16x16x32_bf16 v[208:211], v[104:107], a[228:231], v[208:211]\n"
      "v_mfma_f32_16x16x32_bf16 v[212:215], v[108:111], a[228:231], v[212:215]\n"
      "s_waitcnt lgkmcnt(3)\n"
      "v_mfma_f32_16x16x32_bf16 v[192:195], v[112:115], a[232:235], v[192:195]\n"
      "s_waitcnt vmcnt(12)\n"
      "ds_read_b128 v[136:139], v253 offset:0\n"
      "s_waitcnt lgkmcnt(3)\n"
      "v_mfma_f32_16x16x32_bf16 v[196:199], v[116:119], a[232:235], v[196:199]\n"
      "ds_read_b128 v[140:143], v253 offset:64\n"
      "s_waitcnt lgkmcnt(3)\n"
      "v_mfma_f32_16x16x32_bf16 v[192:195], v[120:123], a[244:247], v[192:195]\n"
      "ds_read_b128 v[144:147], v253 offset:128\n"
      "s_waitcnt lgkmcnt(3)\n"
      "v_mfma_f32_16x16x32_bf16 v[196:199], v[124:127], a[244:247], v[196:199]\n"
      "v_mfma_f32_16x16x32_bf16 v[200:203], v[112:115], a[236:239], v[200:203]\n"
      "ds_read_b128 v[148:151], v253 offset:192\n"
      "v_mfma_f32_16x16x32_bf16 v[204:207], v[116:119], a[236:239], v[204:207]\n"
      "v_mfma_f32_16x16x32_bf16 v[200:203], v[120:123], a[248:251], v[200:203]\n"
      "ds_read_b128 v[152:155], v253 offset:256\n"
      "v_cvt_pk_bf16_f32 v224, v192, v193\n"
      "v_cvt_pk_bf16_f32 v225, v194, v195\n"
      "v_mfma_f32_16x16x32_bf16 v[204:207], v[124:127], a[248:251], v[204:207]\n"
      "v_cvt_pk_bf16_f32 v226, v196, v197\n"
      "v_cvt_pk_bf16_f32 v227, v198, v199\n"
      "v_mfma_f32_16x16x32_bf16 v[208:211], v[112:115], a[240:243], v[208:211]\n"
      "ds_read_b128 v[156:159], v253 offset:320\n"
      "global_store_dwordx4 v252, v[224:227], s[62:63] offset:-4096 sc1\n"
      "v_mfma_f32_16x16x32_bf16 v[212:215], v[116:119], a[240:243], v[212:215]\n"
      "v_mfma_f32_16x16x32_bf16 v[208:211], v[120:123], a[252:255], v[208:211]\n"
      "ds_read_b128 v[160:163], v253 offset:384\n"
      "v_cvt_pk_bf16_f32 v228, v200, v201\n"
      "v_cvt_pk_bf16_f32 v229, v202, v203\n"
      "v_mfma_f32_16x16x32_bf16 v[212:215], v[124:127], a[252:255], v[212:215]\n"
      "v_cvt_pk_bf16_f32 v230, v204, v205\n"
      "v_cvt_pk_bf16_f32 v231, v206, v207\n"
      "s_waitcnt lgkmcnt(6)\n"
      "v_mfma_f32_16x16x32_bf16 v[128:131], v[0:3], v[136:139], 0\n"
      "ds_read_b128 v[164:167], v253 offset:448\n"
      "global_store_dwordx4 v252, v[228:231], s[62:63] offset:-3072 sc1\n"
      "v_mfma_f32_16x16x32_bf16 v[132:135], v[4:7], v[136:139], 0\n"
      "s_waitcnt lgkmcnt(6)\n"
      "v_mfma_f32_16x16x32_bf16 v[128:131], v[8:11], v[140:143], v[128:131]\n"
      "ds_read_b128 v[136:139], v253 offset:512\n"
      "v_cvt_pk_bf16_f32 v232, v208, v209\n"
      "v_cvt_pk_bf16_f32 v233, v210, v211\n"
      "v_mfma_f32_16x16x32_bf16 v[132:135], v[12:15], v[140:143], v[132:135]\n"
      "v_cvt_pk_bf16_f32 v234, v212, v213\n"
      "v_cvt_pk_bf16_f32 v235, v214, v215\n"
      "s_waitcnt lgkmcnt(6)\n"
      "v_mfma_f32_16x16x32_bf16 v[128:131], v[16:19], v[144:147], v[128:131]\n"
      "ds_read_b128 v[140:143], v253 offset:576\n"
      "global_store_dwordx4 v252, v[232:235], s[62:63] offset:-2048 sc1\n"
      "v_mfma_f32_16x16x32_bf16 v[132:135], v[20:23], v[144:147], v[132:135]\n"
      "s_xor_b32 s70, s72, 0\n"
      "s_add_u32 s70, s8, s70\n"
      "s_addc_u32 s71, s9, 0\n"
      "s_add_u32 m0, s41, 4096\n"
      "s_waitcnt lgkmcnt(6)\n"
      "v_mfma_f32_16x16x32_bf16 v[128:131], v[24:27], v[148:151], v[128:131]\n"
      "s_add_u32 s48, s78, 32768\n"
      "ds_read_b128 v[144:147], v253 offset:640\n"
      "global_load_lds_dwordx4 v244, s[70:71] offset:0\n"
      "v_subrev_u32 v236, s41, v248\n"
      "v_mfma_f32_16x16x32_bf16 v[132:135], v[28:31], v[148:151], v[132:135]\n"
      "s_add_u32 s48, s48, 65536\n"
      "v_add_u32 v236, s48, v236\n"
      "v_xor_b32 v237, 64, v236\n"
      "s_waitcnt lgkmcnt(6)\n"
      "v_mfma_f32_16x16x32_bf16 v[128:131], v[32:35], v[152:155], v[128:131]\n"
      "ds_read_b128 v[148:151], v253 offset:704\n"
      "s_waitcnt vmcnt(4)\n"
      "s_barrier\n"
      "v_mfma_f32_16x16x32_bf16 v[132:135], v[36:39], v[152:155], v[132:135]\n"
      "ds_read_b128 a[192:195], v250 offset:34816\n"
      "s_waitcnt lgkmcnt(7)\n"
      "v_mfma_f32_16x16x32_bf16 v[128:131], v[40:43], v[156:159], v[128:131]\n"
      "ds_read_b128 v[152:155], v253 offset:768\n"
      "global_load_lds_dwordx4 v245, s[70:71] offset:1024\n"
      "v_mfma_f32_16x16x32_bf16 v[132:135], v[44:47], v[156:159], v[132:135]\n"
      "ds_read_b128 a[196:199], v251 offset:34816\n"
      "s_waitcnt lgkmcnt(8)\n"
      "v_mfma_f32_16x16x32_bf16 v[128:131], v[48:51], v[160:163], v[128:131]\n"
      "ds_read_b128 v[156:159], v253 offset:832\n"
      "v_mfma_f32_16x16x32_bf16 v[132:135], v[52:55], v[160:163], v[132:135]\n"
      "ds_read_b128 a[200:203], v250 offset:38912\n"
      "s_waitcnt lgkmcnt(9)\n"
      "v_mfma_f32_16x16x32_bf16 v[128:131], v[56:59], v[164:167], v[128:131]\n"
      "ds_read_b128 v[160:163], v253 offset:896\n"
      "global_load_lds_dwordx4 v246, s[70:71] offset:2048\n"
      "v_mfma_f32_16x16x32_bf16 v[132:135], v[60:63], v[164:167], v[132:135]\n"
      "ds_read_b128 a[204:207], v251 offset:38912\n"
      "s_waitcnt lgkmcnt(10)\n"
      "v_mfma_f32_16x16x32_bf16 v[128:131], v[64:67], v[136:139], v[128:131]\n"
      "ds_read_b128 v[164:167], v253 offset:960\n"
      "v_mfma_f32_16x16x32_bf16 v[132:135], v[68:71], v[136:139], v[132:135]\n"
      "ds_read_b128 a[208:211], v250 offset:43008\n"
      "s_waitcnt lgkmcnt(11)\n"
      "v_mfma_f32_16x16x32_bf16 v[128:131], v[72:75], v[140:143], v[128:131]\n"
      "global_load_lds_dwordx4 v247, s[70:71] offset:3072\n"
      "v_mfma_f32_16x16x32_bf16 v[132:135], v[76:79], v[140:143], v[132:135]\n"
      "ds_read_b128 a[212:215], v251 offset:43008\n"
      "s_waitcnt lgkmcnt(11)\n"
      "v_mfma_f32_16x16x32_bf16 v[128:131], v[80:83], v[144:147], v[128:131]\n"
      "v_mfma_f32_16x16x32_bf16 v[132:135], v[84:87], v[144:147], v[132:135]\n"
      "s_lshl_b32 s70, s34, 15\n"
      "s_or_b32 s70, s70, s77\n"
      "s_add_u32 s70, s12, s70\n"
      "s_addc_u32 s71, s13, 0\n"
      "s_add_u32 m0, s78, 65536\n"
      "s_waitcnt lgkmcnt(10)\n"
      "v_mfma_f32_16x16x32_bf16 v[128:131], v[88:91], v[148:151], v[128:131]\n"
      "global_load_lds_dwordx4 v240, s[70:71] offset:0\n"
      "ds_read_b128 a[216:219], v250 offset:47104\n"
      "v_mfma_f32_16x16x32_bf16 v[132:135], v[92:95], v[148:151], v[132:135]\n"
      "ds_read_b128 a[220:223], v251 offset:47104\n"
      "s_waitcnt lgkmcnt(10)\n"
      "v_mfma_f32_16x16x32_bf16 v[128:131], v[96:99], v[152:155], v[128:131]\n"
      "v_mfma_f32_16x16x32_bf16 v[132:135], v[100:103], v[152:155], v[132:135]\n"
      "ds_read_b128 a[224:227], v250 offset:51200\n"
      "s_waitcnt lgkmcnt(9)\n"
      "v_mfma_f32_16x16x32_bf16 v[128:131], v[104:107], v[156:159], v[128:131]\n"
      "global_load_lds_dwordx4 v241, s[70:71] offset:1024\n"
      "v_mfma_f32_16x16x32_bf16 v[132:135], v[108:111], v[156:159], v[132:135]\n"
      "ds_read_b128 a[228:231], v251 offset:51200\n"
      "s_waitcnt lgkmcnt(8)\n"
      "v_mfma_f32_16x16x32_bf16 v[128:131], v[112:115], v[160:163], v[128:131]\n"
      "v_mfma_f32_16x16x32_bf16 v[132:135], v[116:119], v[160:163], v[132:135]\n"
      "ds_read_b128 a[232:235], v250 offset:55296\n"
      "s_waitcnt lgkmcnt(7)\n"
      "v_mfma_f32_16x16x32_bf16 v[128:131], v[120:123], v[164:167], v[128:131]\n"
      "global_load_lds_dwordx4 v242, s[70:71] offset:2048\n"
      "v_mfma_f32_16x16x32_bf16 v[132:135], v[124:127], v[164:167], v[132:135]\n"
      "ds_read_b128 a[236:239], v251 offset:55296\n"
      "v_mfma_f32_16x16x32_bf16 v[216:219], v[0:3], a[192:195], 0\n"
      "v_mfma_f32_16x16x32_bf16 v[220:223], v[4:7], a[192:195], 0\n"
      "ds_read_b128 a[240:243], v250 offset:59392\n"
      "v_mfma_f32_16x16x32_bf16 v[216:219], v[8:11], a[196:199], v[216:219]\n"
      "s_mov_b32 s22, 0xf0f0f0f0\n"
      "global_load_lds_dwordx4 v243, s[70:71] offset:3072\n"
      "s_mov_b32 s23, 0xf0f0f0f0\n"
      "v_cndmask_b32 v128, v128, v129, s[64:65]\n"
      "v_mfma_f32_16x16x32_bf16 v[220:223], v[12:15], a[196:199], v[220:223]\n"
      "ds_read_b128 a[244:247], v251 offset:59392\n"
      "v_cndmask_b32 v130, v130, v131, s[64:65]\n"
      "v_cndmask_b32 v132, v132, v133, s[64:65]\n"
      "v_mfma_f32_16x16x32_bf16 v[216:219], v[16:19], a[200:203], v[216:219]\n"
      "v_cndmask_b32 v134, v134, v135, s[64:65]\n"
      "v_cndmask_b32 v128, v128, v130, s[66:67]\n"
      "v_cndmask_b32 v132, v132, v134, s[66:67]\n"
      "v_mfma_f32_16x16x32_bf16 v[220:223], v[20:23], a[200:203], v[220:223]\n"
      "ds_read_b128 a[248:251], v250 offset:63488\n"
      "v_lshrrev_b32 v133, 4, v255\n"
      "v_cndmask_b32 v128, v128, v132, s[22:23]\n"
      "v_mfma_f32_16x16x32_bf16 v[216:219], v[24:27], a[204:207], v[216:219]\n"
      "s_lshl_b32 s70, s34, 15\n"
      "s_or_b32 s70, s70, s77\n"
      "s_xor_b32 s70, s70, 512\n"
      "s_add_u32 s70, s12, s70\n"
      "s_addc_u32 s71, s13, 0\n"
      "s_add_u32 m0, s78, 81920\n"
      "v_mfma_f32_16x16x32_bf16 v[220:223], v[28:31], a[204:207], v[220:223]\n"
      "global_load_lds_dwordx4 v240, s[70:71] offset:0\n"
      "ds_read_b128 a[252:255], v251 offset:63488\n"
      "v_and_b32 v134, 7, v255\n"
      "s_waitcnt lgkmcnt(11)\n"
      "v_mfma_f32_16x16x32_bf16 v[216:219], v[32:35], a[208:211], v[216:219]\n"
      "v_xor_b32 v250, 196608, v250\n"
      "v_xor_b32 v251, 196608, v251\n"
      "v_mfma_f32_16x16x32_bf16 v[220:223], v[36:39], a[208:211], v[220:223]\n"
      "ds_read_b128 a[192:195], v250 offset:0\n"
      "v_cvt_pk_bf16_f32 v128, v128, v128\n"
      "s_lshl_b32 s48, s40, 5\n"
      "s_waitcnt lgkmcnt(11)\n"
      "v_mfma_f32_16x16x32_bf16 v[216:219], v[40:43], a[212:215], v[216:219]\n"
      "v_and_b32 v128, 0xffff0000, v128\n"
      "v_lshl_or_b32 v133, v133, 3, v134\n"
      "v_add_u32 v133, s48, v133\n"
      "v_mfma_f32_16x16x32_bf16 v[220:223], v[44:47], a[212:215], v[220:223]\n"
      "global_load_lds_dwordx4 v241, s[70:71] offset:1024\n"
      "ds_read_b128 a[196:199], v250 offset:2048\n"
      "v_ashrrev_i32 v129, 31, v128\n"
      "s_waitcnt lgkmcnt(11)\n"
      "v_mfma_f32_16x16x32_bf16 v[216:219], v[48:51], a[216:219], v[216:219]\n"
      "v_mul_f32 v130, 0xbfb8aa3b, v128\n"
      "v_or_b32 v129, 0x80000000, v129\n"
      "v_exp_f32 v130, v130\n"
      "v_mfma_f32_16x16x32_bf16 v[220:223], v[52:55], a[216:219], v[220:223]\n"
      "ds_read_b128 a[200:203], v251 offset:0\n"
      "v_xor_b32 v129, v129, v128\n"
      "v_sub_u32 v132, 0xfff, v133\n"
      "s_waitcnt lgkmcnt(11)\n"
      "v_mfma_f32_16x16x32_bf16 v[216:219], v[56:59], a[220:223], v[216:219]\n"
      "v_add_f32 v130, 1.0, v130\n"
      "s_mov_b32 s49, 0xffff0000\n"
      "v_and_or_b32 v129, v129, s49, v132\n"
      "v_mfma_f32_16x16x32_bf16 v[220:223], v[60:63], a[220:223], v[220:223]\n"
      "global_load_lds_dwordx4 v242, s[70:71] offset:2048\n"
      "ds_read_b128 a[204:207], v251 offset:2048\n"
      "v_rcp_f32 v130, v130\n"
      "s_waitcnt lgkmcnt(11)\n"
      "v_mfma_f32_16x16x32_bf16 v[216:219], v[64:67], a[224:227], v[216:219]\n"
      "s_mov_b32 s64, 0x00ff00ff\n"
      "s_mov_b32 s65, 0x00ff00ff\n"
      "v_cndmask_b32 v129, v129, 0, s[64:65]\n"
      "v_cvt_pk_bf16_f32 v130, v130, v130\n"
      "v_mfma_f32_16x16x32_bf16 v[220:223], v[68:71], a[224:227], v[220:223]\n"
      "s_lshr_b32 s48, s34, 1\n"
      "ds_read_b128 a[208:211], v250 offset:4096\n"
      "s_lshl2_add_u32 s48, s48, s35\n"
      "v_max_u32_dpp v129, v129, v129 quad_perm:[1,0,3,2] row_mask:0xf bank_mask:0xf\n"
      "s_waitcnt lgkmcnt(11)\n"
      "v_mfma_f32_16x16x32_bf16 v[216:219], v[72:75], a[228:231], v[216:219]\n"
      "v_and_b32 v130, 0xffff0000, v130\n"
      "s_lshl_b32 s49, s48, 15\n"
      "v_max_u32_dpp v129, v129, v129 quad_perm:[2,3,0,1] row_mask:0xf bank_mask:0xf\n"
      "v_mfma_f32_16x16x32_bf16 v[220:223], v[76:79], a[228:231], v[220:223]\n"
      "s_add_u32 s49, s49, 786432\n"
      "global_load_lds_dwordx4 v243, s[70:71] offset:3072\n"
      "ds_read_b128 a[212:215], v250 offset:6144\n"
      "v_lshl_add_u32 v133, v133, 4, s49\n"
      "s_waitcnt lgkmcnt(11)\n"
      "v_mfma_f32_16x16x32_bf16 v[216:219], v[80:83], a[232:235], v[216:219]\n"
      "v_max_u32_dpp v129, v129, v129 row_half_mirror row_mask:0xf bank_mask:0xf\n"
      "s_lshl_b32 s48, s48, 6\n"
      "s_add_u32 s48, s48, s40\n"
      "v_mfma_f32_16x16x32_bf16 v[220:223], v[84:87], a[232:235], v[220:223]\n"
      "ds_read_b128 a[216:219], v251 offset:4096\n"
      "v_max_u32_dpp v129, v129, v129 row_mirror row_mask:0xf bank_mask:0xf\n"
      "s_lshl_b32 s48, s48, 6\n"
      "s_waitcnt lgkmcnt(11)\n"
      "v_mfma_f32_16x16x32_bf16 v[216:219], v[88:91], a[236:239], v[216:219]\n"
      "s_add_u32 s48, s48, 4096\n"
      "v_mov_b32 v131, v129\n"
      "v_lshl_add_u32 v132, v134, 4, s48\n"
      "v_mov_b32 v137, s37\n"
      "v_mfma_f32_16x16x32_bf16 v[220:223], v[92:95], a[236:239], v[220:223]\n"
      "s_xor_b32 s70, s73, 0\n"
      "s_add_u32 s70, s8, s70\n"
      "s_addc_u32 s71, s9, 0\n"
      "s_add_u32 m0, s41, 8192\n"
      "s_waitcnt lgkmcnt(10)\n"
      "v_mfma_f32_16x16x32_bf16 v[216:219], v[96:99], a[240:243], v[216:219]\n"
      "global_load_lds_dwordx4 v244, s[70:71] offset:0\n"
      "ds_read_b128 a[220:223], v251 offset:6144\n"
      "v_permlane16_swap_b32 v129, v131\n"
      "v_mfma_f32_16x16x32_bf16 v[220:223], v[100:103], a[240:243], v[220:223]\n"
      "ds_read_b128 a[224:227], v250 offset:8192\n"
      "v_mov_b32 v138, s37\n"
      "v_mov_b32 v139, s37\n"
      "s_waitcnt lgkmcnt(11)\n"
      "v_mfma_f32_16x16x32_bf16 v[216:219], v[104:107], a[244:247], v[216:219]\n"
      "v_max_u32 v129, v129, v131\n"
      "v_cndmask_b32 v133, v132, v133, s[64:65]\n"
      "v_mov_b32 v131, v129\n"
      "v_mfma_f32_16x16x32_bf16 v[220:223], v[108:111], a[244:247], v[220:223]\n"
      "ds_read_b128 a[228:231], v250 offset:10240\n"
      "s_waitcnt vmcnt(9)\n"
      "ds_read_b128 a[0:3], v248 offset:4096\n"
      "s_waitcnt lgkmcnt(12)\n"
      "v_mfma_f32_16x16x32_bf16 v[216:219], v[112:115], a[248:251], v[216:219]\n"
      "global_load_lds_dwordx4 v245, s[70:71] offset:1024\n"
      "ds_read_b128 a[4:7], v248 offset:6144\n"
      "v_permlane32_swap_b32 v129, v131\n"
      "v_mfma_f32_16x16x32_bf16 v[220:223], v[116:119], a[248:251], v[220:223]\n"
      "ds_read_b128 a[232:235], v251 offset:8192\n"
      "ds_read_b128 a[32:35], v249 offset:4096\n"
      "v_max_u32 v129, v129, v131\n"
      "s_waitcnt lgkmcnt(14)\n"
      "v_mfma_f32_16x16x32_bf16 v[216:219], v[120:123], a[252:255], v[216:219]\n"
      "ds_read_b128 a[36:39], v249 offset:6144\n"
      "v_cndmask_b32 v136, v129, v130, s[64:65]\n"
      "s_mov_b32 s64, 0x00ff0fff\n"
      "v_mfma_f32_16x16x32_bf16 v[220:223], v[124:127], a[252:255], v[220:223]\n"
      "s_mov_b64 exec, s[64:65]\n"
      "global_store_dwordx4 v133, v[136:139], s[58:59] sc1\n"
      "s_mov_b64 exec, -1\n"
      "s_waitcnt lgkmcnt(14)\n"
      "v_mfma_f32_16x16x32_bf16 v[192:195], v[0:3], a[192:195], 0\n"
      "v_add_u32 v238, s42, v252\n"
      "v_mov_b32 v224, s37\n"
      "v_mov_b32 v225, s37\n"
      "v_mov_b32 v226, s37\n"
      "v_mov_b32 v227, s37\n"
      "s_mov_b64 exec, 0xff\n"
      "global_store_dwordx4 v238, v[224:227], s[58:59] offset:0 sc1\n"
      "s_mov_b64 exec, -1\n"
      "v_mfma_f32_16x16x32_bf16 v[196:199], v[4:7], a[192:195], 0\n"
      "global_load_lds_dwordx4 v246, s[70:71] offset:2048\n"
      "ds_read_b128 a[236:239], v251 offset:10240\n"
      "s_waitcnt lgkmcnt(14)\n"
      "v_mfma_f32_16x16x32_bf16 v[200:203], v[0:3], a[196:199], 0\n"
      "ds_read_b128 a[240:243], v250 offset:12288\n"
      "v_cvt_pk_bf16_f32 v224, v216, v217\n"
      "v_cvt_pk_bf16_f32 v225, v218, v219\n"
      "v_mfma_f32_16x16x32_bf16 v[204:207], v[4:7], a[196:199], 0\n"
      "ds_read_b128 a[244:247], v250 offset:14336\n"
      "v_cvt_pk_bf16_f32 v226, v220, v221\n"
      "v_cvt_pk_bf16_f32 v227, v222, v223\n"
      "s_waitcnt lgkmcnt(15)\n"
      "v_mfma_f32_16x16x32_bf16 v[192:195], v[8:11], a[200:203], v[192:195]\n"
      "s_lshl2_add_u32 s54, s34, s35\n"
      "s_lshr_b32 s56, s39, 1\n"
      "s_lshl1_add_u32 s56, s33, s56\n"
      "s_lshl_b32 s56, s56, 15\n"
      "v_mfma_f32_16x16x32_bf16 v[196:199], v[12:15], a[200:203], v[196:199]\n"
      "s_lshl_b32 s55, s54, 18\n"
      "global_load_lds_dwordx4 v247, s[70:71] offset:3072\n"
      "ds_read_b128 a[248:251], v251 offset:12288\n"
      "s_add_u32 s55, s55, s56\n"
      "s_waitcnt lgkmcnt(15)\n"
      "v_mfma_f32_16x16x32_bf16 v[200:203], v[8:11], a[204:207], v[200:203]\n"
      "s_add_u32 s55, s55, 4096\n"
      "s_add_u32 s10, s18, s55\n"
      "s_addc_u32 s11, s19, 0\n"
      "s_xor_b32 s55, s55, 0x8000\n"
      "v_mfma_f32_16x16x32_bf16 v[204:207], v[12:15], a[204:207], v[204:207]\n"
      "s_add_u32 s14, s18, s55\n"
      "s_addc_u32 s15, s19, 0\n"
      "ds_read_b128 a[252:255], v251 offset:14336\n"
      "s_waitcnt lgkmcnt(15)\n"
      "v_mfma_f32_16x16x32_bf16 v[192:195], v[16:19], a[208:211], v[192:195]\n"
      "s_and_b32 s55, s39, 1\n"
      "s_xor_b32 s70, s74, 0\n"
      "s_add_u32 s70, s8, s70\n"
      "s_addc_u32 s71, s9, 0\n"
      "s_add_u32 m0, s41, 12288\n"
      "v_mfma_f32_16x16x32_bf16 v[196:199], v[20:23], a[208:211], v[196:199]\n"
      "s_lshl_b32 s56, s55, 14\n"
      "global_load_lds_dwordx4 v244, s[70:71] offset:0\n"
      "ds_read_b128 a[192:195], v250 offset:16384\n"
      "v_add_u32 v240, s56, v252\n"
      "s_waitcnt lgkmcnt(15)\n"
      "v_mfma_f32_16x16x32_bf16 v[200:203], v[16:19], a[212:215], v[200:203]\n"
      "v_xor_b32 v241, 0x4000, v240\n"
      "v_add_u32 v242, 0x2000, v240\n"
      "v_add_u32 v243, 0x2000, v241\n"
      "v_mfma_f32_16x16x32_bf16 v[204:207], v[20:23], a[212:215], v[204:207]\n"
      "s_lshl2_add_u32 s55, s54, s33\n"
      "ds_read_b128 a[196:199], v250 offset:18432\n"
      "s_lshl_b32 s55, s55, 11\n"
      "s_add_u32 s55, s55, 65536\n"
      "s_waitcnt lgkmcnt(15)\n"
      "v_mfma_f32_16x16x32_bf16 v[192:195], v[24:27], a[216:219], v[192:195]\n"
      "v_and_b32 v254, 7, v255\n"
      "v_lshl_add_u32 v254, v254, 8, s55\n"
      "v_mfma_f32_16x16x32_bf16 v[196:199], v[28:31], a[216:219], v[196:199]\n"
      "global_load_lds_dwordx4 v245, s[70:71] offset:1024\n"
      "ds_read_b128 a[200:203], v251 offset:16384\n"
      "s_waitcnt lgkmcnt(15)\n"
      "v_mfma_f32_16x16x32_bf16 v[200:203], v[24:27], a[220:223], v[200:203]\n"
      "v_mfma_f32_16x16x32_bf16 v[204:207], v[28:31], a[220:223], v[204:207]\n"
      "ds_read_b128 a[204:207], v251 offset:18432\n"
      "s_waitcnt lgkmcnt(15)\n"
      "v_mfma_f32_16x16x32_bf16 v[192:195], v[32:35], a[224:227], v[192:195]\n"
      "v_mfma_f32_16x16x32_bf16 v[196:199], v[36:39], a[224:227], v[196:199]\n"
      "global_load_lds_dwordx4 v246, s[70:71] offset:2048\n"
      "ds_read_b128 a[208:211], v250 offset:20480\n"
      "s_waitcnt lgkmcnt(15)\n"
      "v_mfma_f32_16x16x32_bf16 v[200:203], v[32:35], a[228:231], v[200:203]\n"
      "v_mfma_f32_16x16x32_bf16 v[204:207], v[36:39], a[228:231], v[204:207]\n"
      "ds_read_b128 a[212:215], v250 offset:22528\n"
      "s_waitcnt lgkmcnt(13)\n"
      "v_mfma_f32_16x16x32_bf16 v[192:195], v[40:43], a[232:235], v[192:195]\n"
      "v_mfma_f32_16x16x32_bf16 v[196:199], v[44:47], a[232:235], v[196:199]\n"
      "global_load_lds_dwordx4 v247, s[70:71] offset:3072\n"
      "ds_read_b128 a[216:219], v251 offset:20480\n"
      "s_waitcnt lgkmcnt(11)\n"
      "v_mfma_f32_16x16x32_bf16 v[200:203], v[40:43], a[236:239], v[200:203]\n"
      "v_mfma_f32_16x16x32_bf16 v[204:207], v[44:47], a[236:239], v[204:207]\n"
      "ds_read_b128 a[220:223], v251 offset:22528\n"
      "s_waitcnt vmcnt(10)\n"
      "s_barrier\n"
      "s_waitcnt lgkmcnt(11)\n"
      "v_mfma_f32_16x16x32_bf16 v[192:195], v[48:51], a[240:243], v[192:195]\n"
      "s_xor_b32 s70, s75, 0\n"
      "s_add_u32 s70, s8, s70\n"
      "s_addc_u32 s71, s9, 0\n"
      "s_add_u32 m0, s78, 98304\n"
      "v_mfma_f32_16x16x32_bf16 v[196:199], v[52:55], a[240:243], v[196:199]\n"
      "global_load_lds_dwordx4 v244, s[70:71] offset:0\n"
      "ds_read_b128 a[224:227], v250 offset:24576\n"
      "s_waitcnt lgkmcnt(11)\n"
      "v_mfma_f32_16x16x32_bf16 v[200:203], v[48:51], a[244:247], v[200:203]\n"
      "v_mfma_f32_16x16x32_bf16 v[204:207], v[52:55], a[244:247], v[204:207]\n"
      "ds_read_b128 a[228:231], v250 offset:26624\n"
      "s_waitcnt lgkmcnt(11)\n"
      "v_mfma_f32_16x16x32_bf16 v[192:195], v[56:59], a[248:251], v[192:195]\n"
      "v_mfma_f32_16x16x32_bf16 v[196:199], v[60:63], a[248:251], v[196:199]\n"
      "global_load_lds_dwordx4 v245, s[70:71] offset:1024\n"
      "ds_read_b128 a[232:235], v251 offset:24576\n"
      "s_waitcnt lgkmcnt(11)\n"
      "v_mfma_f32_16x16x32_bf16 v[200:203], v[56:59], a[252:255], v[200:203]\n"
      "v_mfma_f32_16x16x32_bf16 v[204:207], v[60:63], a[252:255], v[204:207]\n"
      "ds_read_b128 a[236:239], v251 offset:26624\n"
      "s_waitcnt lgkmcnt(11)\n"
      "v_mfma_f32_16x16x32_bf16 v[192:195], v[64:67], a[192:195], v[192:195]\n"
      "v_mfma_f32_16x16x32_bf16 v[196:199], v[68:71], a[192:195], v[196:199]\n"
      "global_load_lds_dwordx4 v246, s[70:71] offset:2048\n"
      "ds_read_b128 a[240:243], v250 offset:28672\n"
      "s_waitcnt lgkmcnt(11)\n"
      "v_mfma_f32_16x16x32_bf16 v[200:203], v[64:67], a[196:199], v[200:203]\n"
      "v_mfma_f32_16x16x32_bf16 v[204:207], v[68:71], a[196:199], v[204:207]\n"
      "ds_read_b128 a[244:247], v250 offset:30720\n"
      "s_waitcnt vmcnt(7)\n"
      "ds_read_b128 a[8:11], v248 offset:8192\n"
      "s_waitcnt lgkmcnt(12)\n"
      "v_mfma_f32_16x16x32_bf16 v[192:195], v[72:75], a[200:203], v[192:195]\n"
      "ds_read_b128 a[12:15], v248 offset:10240\n"
      "ds_read_b128 a[40:43], v249 offset:8192\n"
      "v_mfma_f32_16x16x32_bf16 v[196:199], v[76:79], a[200:203], v[196:199]\n"
      "global_load_lds_dwordx4 v247, s[70:71] offset:3072\n"
      "ds_read_b128 a[248:251], v251 offset:28672\n"
      "s_waitcnt lgkmcnt(14)\n"
      "v_mfma_f32_16x16x32_bf16 v[200:203], v[72:75], a[204:207], v[200:203]\n"
      "ds_read_b128 a[44:47], v249 offset:10240\n"
      "v_mfma_f32_16x16x32_bf16 v[204:207], v[76:79], a[204:207], v[204:207]\n"
      "ds_read_b128 a[252:255], v251 offset:30720\n"
      "v_xor_b32 v250, 196608, v250\n"
      "v_xor_b32 v251, 196608, v251\n"
      "s_waitcnt lgkmcnt(15)\n"
      "v_mfma_f32_16x16x32_bf16 v[192:195], v[80:83], a[208:211], v[192:195]\n"
      "s_xor_b32 s70, s72, 256\n"
      "s_add_u32 s70, s8, s70\n"
      "s_addc_u32 s71, s9, 0\n"
      "s_add_u32 m0, s78, 114688\n"
      "v_mfma_f32_16x16x32_bf16 v[196:199], v[84:87], a[208:211], v[196:199]\n"
      "global_load_lds_dwordx4 v244, s[70:71] offset:0\n"
      "ds_read_b128 a[192:195], v250 offset:0\n"
      "s_waitcnt lgkmcnt(15)\n"
      "v_mfma_f32_16x16x32_bf16 v[200:203], v[80:83], a[212:215], v[200:203]\n"
      "v_mfma_f32_16x16x32_bf16 v[204:207], v[84:87], a[212:215], v[204:207]\n"
      "ds_read_b128 a[196:199], v250 offset:2048\n"
      "s_waitcnt lgkmcnt(15)\n"
      "v_mfma_f32_16x16x32_bf16 v[192:195], v[88:91], a[216:219], v[192:195]\n"
      "v_mfma_f32_16x16x32_bf16 v[196:199], v[92:95], a[216:219], v[196:199]\n"
      "global_load_lds_dwordx4 v245, s[70:71] offset:1024\n"
      "ds_read_b128 a[200:203], v251 offset:0\n"
      "s_waitcnt lgkmcnt(15)\n"
      "v_mfma_f32_16x16x32_bf16 v[200:203], v[88:91], a[220:223], v[200:203]\n"
      "v_mfma_f32_16x16x32_bf16 v[204:207], v[92:95], a[220:223], v[204:207]\n"
      "ds_read_b128 a[204:207], v251 offset:2048\n"
      "s_waitcnt lgkmcnt(15)\n"
      "v_mfma_f32_16x16x32_bf16 v[192:195], v[96:99], a[224:227], v[192:195]\n"
      "v_mfma_f32_16x16x32_bf16 v[196:199], v[100:103], a[224:227], v[196:199]\n"
      "global_load_lds_dwordx4 v246, s[70:71] offset:2048\n"
      "ds_read_b128 a[208:211], v250 offset:4096\n"
      "s_waitcnt lgkmcnt(15)\n"
      "v_mfma_f32_16x16x32_bf16 v[200:203], v[96:99], a[228:231], v[200:203]\n"
      "v_mfma_f32_16x16x32_bf16 v[204:207], v[100:103], a[228:231], v[204:207]\n"
      "ds_read_b128 a[212:215], v250 offset:6144\n"
      "s_waitcnt vmcnt(7)\n"
      "ds_read_b128 a[16:19], v248 offset:12288\n"
      "s_waitcnt lgkmcnt(15)\n"
      "v_mfma_f32_16x16x32_bf16 v[192:195], v[104:107], a[232:235], v[192:195]\n"
      "ds_read_b128 a[20:23], v248 offset:14336\n"
      "ds_read_b128 a[48:51], v249 offset:12288\n"
      "v_mfma_f32_16x16x32_bf16 v[196:199], v[108:111], a[232:235], v[196:199]\n"
      "global_load_lds_dwordx4 v247, s[70:71] offset:3072\n"
      "ds_read_b128 a[216:219], v251 offset:4096\n"
      "v_mfma_f32_16x16x32_bf16 v[200:203], v[104:107], a[236:239], v[200:203]\n"
      "ds_read_b128 a[52:55], v249 offset:14336\n"
      "v_mfma_f32_16x16x32_bf16 v[204:207], v[108:111], a[236:239], v[204:207]\n"
      "ds_read_b128 a[220:223], v251 offset:6144\n"
      "s_waitcnt lgkmcnt(15)\n"
      "v_mfma_f32_16x16x32_bf16 v[192:195], v[112:115], a[240:243], v[192:195]\n"
      "s_xor_b32 s70, s73, 256\n"
      "s_add_u32 s70, s8, s70\n"
      "s_addc_u32 s71, s9, 0\n"
      "s_add_u32 m0, s41, 0\n"
      "v_mfma_f32_16x16x32_bf16 v[196:199], v[116:119], a[240:243], v[196:199]\n"
      "global_load_lds_dwordx4 v244, s[70:71] offset:0\n"
      "ds_read_b128 a[224:227], v250 offset:8192\n"
      "v_mfma_f32_16x16x32_bf16 v[200:203], v[112:115], a[244:247], v[200:203]\n"
      "v_mfma_f32_16x16x32_bf16 v[204:207], v[116:119], a[244:247], v[204:207]\n"
      "ds_read_b128 a[228:231], v250 offset:10240\n"
      "s_waitcnt lgkmcnt(15)\n"
      "v_mfma_f32_16x16x32_bf16 v[192:195], v[120:123], a[248:251], v[192:195]\n"
      "v_mfma_f32_16x16x32_bf16 v[196:199], v[124:127], a[248:251], v[196:199]\n"
      "global_load_lds_dwordx4 v245, s[70:71] offset:1024\n"
      "ds_read_b128 a[232:235], v251 offset:8192\n"
      "s_waitcnt lgkmcnt(15)\n"
      "v_mfma_f32_16x16x32_bf16 v[200:203], v[120:123], a[252:255], v[200:203]\n"
      "v_mfma_f32_16x16x32_bf16 v[204:207], v[124:127], a[252:255], v[204:207]\n"
      "ds_read_b128 a[236:239], v251 offset:10240\n"
      "s_barrier\n"
      "s_waitcnt lgkmcnt(15)\n"
      "v_mfma_f32_16x16x32_bf16 v[208:211], v[0:3], a[192:195], 0\n"
      "v_mfma_f32_16x16x32_bf16 v[212:215], v[4:7], a[192:195], 0\n"
      "global_load_lds_dwordx4 v246, s[70:71] offset:2048\n"
      "ds_read_b128 a[240:243], v250 offset:12288\n"
      "s_waitcnt lgkmcnt(15)\n"
      "v_mfma_f32_16x16x32_bf16 v[216:219], v[0:3], a[196:199], 0\n"
      "v_cvt_pk_bf16_f32 v228, v192, v193\n"
      "v_cvt_pk_bf16_f32 v229, v194, v195\n"
      "v_cvt_pk_bf16_f32 v230, v196, v197\n"
      "v_mfma_f32_16x16x32_bf16 v[220:223], v[4:7], a[196:199], 0\n"
      "ds_read_b128 a[244:247], v250 offset:14336\n"
      "s_waitcnt vmcnt(7)\n"
      "ds_read_b128 a[24:27], v236 offset:0\n"
      "s_waitcnt lgkmcnt(15)\n"
      "v_mfma_f32_16x16x32_bf16 v[208:211], v[8:11], a[200:203], v[208:211]\n"
      "ds_read_b128 a[28:31], v236 offset:2048\n"
      "ds_read_b128 a[56:59], v237 offset:0\n"
      "v_cvt_pk_bf16_f32 v231, v198, v199\n"
      "v_mfma_f32_16x16x32_bf16 v[212:215], v[12:15], a[200:203], v[212:215]\n"
      "global_load_lds_dwordx4 v247, s[70:71] offset:3072\n"
      "ds_read_b128 a[248:251], v251 offset:12288\n"
      "v_cvt_pk_bf16_f32 v232, v200, v201\n"
      "v_mfma_f32_16x16x32_bf16 v[216:219], v[8:11], a[204:207], v[216:219]\n"
      "ds_read_b128 a[60:63], v237 offset:2048\n"
      "v_cvt_pk_bf16_f32 v233, v202, v203\n"
      "v_cvt_pk_bf16_f32 v234, v204, v205\n"
      "v_mfma_f32_16x16x32_bf16 v[220:223], v[12:15], a[204:207], v[220:223]\n"
      "ds_read_b128 a[252:255], v251 offset:14336\n"
      "v_cvt_pk_bf16_f32 v235, v206, v207\n"
      "s_waitcnt lgkmcnt(15)\n"
      "v_mfma_f32_16x16x32_bf16 v[208:211], v[16:19], a[208:211], v[208:211]\n"
      "s_xor_b32 s70, s74, 256\n"
      "s_add_u32 s70, s8, s70\n"
      "s_addc_u32 s71, s9, 0\n"
      "s_add_u32 m0, s78, 131072\n"
      "v_mfma_f32_16x16x32_bf16 v[212:215], v[20:23], a[208:211], v[212:215]\n"
      "global_load_lds_dwordx4 v244, s[70:71] offset:0\n"
      "ds_read_b128 a[192:195], v250 offset:16384\n"
      "v_mfma_f32_16x16x32_bf16 v[216:219], v[16:19], a[212:215], v[216:219]\n"
      "v_mfma_f32_16x16x32_bf16 v[220:223], v[20:23], a[212:215], v[220:223]\n"
      "ds_read_b128 a[196:199], v250 offset:18432\n"
      "s_waitcnt lgkmcnt(15)\n"
      "v_mfma_f32_16x16x32_bf16 v[208:211], v[24:27], a[216:219], v[208:211]\n"
      "v_mfma_f32_16x16x32_bf16 v[212:215], v[28:31], a[216:219], v[212:215]\n"
      "global_load_lds_dwordx4 v245, s[70:71] offset:1024\n"
      "ds_read_b128 a[200:203], v251 offset:16384\n"
      "s_waitcnt lgkmcnt(15)\n"
      "v_mfma_f32_16x16x32_bf16 v[216:219], v[24:27], a[220:223], v[216:219]\n"
      "v_mfma_f32_16x16x32_bf16 v[220:223], v[28:31], a[220:223], v[220:223]\n"
      "ds_read_b128 a[204:207], v251 offset:18432\n"
      "s_waitcnt lgkmcnt(15)\n"
      "v_mfma_f32_16x16x32_bf16 v[208:211], v[32:35], a[224:227], v[208:211]\n"
      "v_mfma_f32_16x16x32_bf16 v[212:215], v[36:39], a[224:227], v[212:215]\n"
      "global_load_lds_dwordx4 v246, s[70:71] offset:2048\n"
      "ds_read_b128 a[208:211], v250 offset:20480\n"
      "s_waitcnt lgkmcnt(15)\n"
      "v_mfma_f32_16x16x32_bf16 v[216:219], v[32:35], a[228:231], v[216:219]\n"
      "v_mfma_f32_16x16x32_bf16 v[220:223], v[36:39], a[228:231], v[220:223]\n"
      "ds_read_b128 a[212:215], v250 offset:22528\n"
      "s_waitcnt vmcnt(7)\n"
      "ds_read_b128 a[64:67], v236 offset:16384\n"
      "s_waitcnt lgkmcnt(15)\n"
      "v_mfma_f32_16x16x32_bf16 v[208:211], v[40:43], a[232:235], v[208:211]\n"
      "ds_read_b128 a[68:71], v236 offset:18432\n"
      "ds_read_b128 a[96:99], v237 offset:16384\n"
      "v_mfma_f32_16x16x32_bf16 v[212:215], v[44:47], a[232:235], v[212:215]\n"
      "global_load_lds_dwordx4 v247, s[70:71] offset:3072\n"
      "ds_read_b128 a[216:219], v251 offset:20480\n"
      "v_mfma_f32_16x16x32_bf16 v[216:219], v[40:43], a[236:239], v[216:219]\n"
      "ds_read_b128 a[100:103], v237 offset:18432\n"
      "v_mfma_f32_16x16x32_bf16 v[220:223], v[44:47], a[236:239], v[220:223]\n"
      "ds_read_b128 a[220:223], v251 offset:22528\n"
      "s_waitcnt lgkmcnt(15)\n"
      "v_mfma_f32_16x16x32_bf16 v[208:211], v[48:51], a[240:243], v[208:211]\n"
      "global_load_dword v253, v254, s[58:59] offset:0 sc1\n"
      "s_xor_b32 s70, s75, 256\n"
      "s_add_u32 s70, s8, s70\n"
      "s_addc_u32 s71, s9, 0\n"
      "s_add_u32 m0, s78, 147456\n"
      "v_mfma_f32_16x16x32_bf16 v[212:215], v[52:55], a[240:243], v[212:215]\n"
      "global_load_lds_dwordx4 v244, s[70:71] offset:0\n"
      "ds_read_b128 a[224:227], v250 offset:24576\n"
      "v_mfma_f32_16x16x32_bf16 v[216:219], v[48:51], a[244:247], v[216:219]\n"
      "v_mfma_f32_16x16x32_bf16 v[220:223], v[52:55], a[244:247], v[220:223]\n"
      "ds_read_b128 a[228:231], v250 offset:26624\n"
      "s_waitcnt lgkmcnt(15)\n"
      "v_mfma_f32_16x16x32_bf16 v[208:211], v[56:59], a[248:251], v[208:211]\n"
      "v_mfma_f32_16x16x32_bf16 v[212:215], v[60:63], a[248:251], v[212:215]\n"
      "global_load_lds_dwordx4 v245, s[70:71] offset:1024\n"
      "ds_read_b128 a[232:235], v251 offset:24576\n"
      "s_waitcnt lgkmcnt(15)\n"
      "v_mfma_f32_16x16x32_bf16 v[216:219], v[56:59], a[252:255], v[216:219]\n"
      "v_mfma_f32_16x16x32_bf16 v[220:223], v[60:63], a[252:255], v[220:223]\n"
      "ds_read_b128 a[236:239], v251 offset:26624\n"
      "s_waitcnt lgkmcnt(15)\n"
      "v_mfma_f32_16x16x32_bf16 v[208:211], v[64:67], a[192:195], v[208:211]\n"
      "v_mfma_f32_16x16x32_bf16 v[212:215], v[68:71], a[192:195], v[212:215]\n"
      "global_load_lds_dwordx4 v246, s[70:71] offset:2048\n"
      "ds_read_b128 a[240:243], v250 offset:28672\n"
      "s_waitcnt lgkmcnt(15)\n"
      "v_mfma_f32_16x16x32_bf16 v[216:219], v[64:67], a[196:199], v[216:219]\n"
      "v_mfma_f32_16x16x32_bf16 v[220:223], v[68:71], a[196:199], v[220:223]\n"
      "ds_read_b128 a[244:247], v250 offset:30720\n"
      "s_waitcnt vmcnt(8)\n"
      "ds_read_b128 a[72:75], v248 offset:0\n"
      "s_waitcnt lgkmcnt(15)\n"
      "v_mfma_f32_16x16x32_bf16 v[208:211], v[72:75], a[200:203], v[208:211]\n"
      "ds_read_b128 a[76:79], v248 offset:2048\n"
      "ds_read_b128 a[104:107], v249 offset:0\n"
      "v_mfma_f32_16x16x32_bf16 v[212:215], v[76:79], a[200:203], v[212:215]\n"
      "global_load_lds_dwordx4 v247, s[70:71] offset:3072\n"
      "ds_read_b128 a[248:251], v251 offset:28672\n"
      "v_mfma_f32_16x16x32_bf16 v[216:219], v[72:75], a[204:207], v[216:219]\n"
      "ds_read_b128 a[108:111], v249 offset:2048\n"
      "v_mfma_f32_16x16x32_bf16 v[220:223], v[76:79], a[204:207], v[220:223]\n"
      "ds_read_b128 a[252:255], v251 offset:30720\n"
      "s_waitcnt lgkmcnt(15)\n"
      "v_mfma_f32_16x16x32_bf16 v[208:211], v[80:83], a[208:211], v[208:211]\n"
      "s_waitcnt vmcnt(4)\n"
      "v_cmp_ne_u32 vcc, s37, v253\n"
      "s_nop 1\n"
      "s_cbranch_vccnz .Lp_s2_%=\n"
      ".Lp_o3_%=:\n"
      "v_mfma_f32_16x16x32_bf16 v[212:215], v[84:87], a[208:211], v[212:215]\n"
      "global_load_dwordx4 v[128:131], v240, s[10:11] offset:-4096 sc1\n"
      "v_mfma_f32_16x16x32_bf16 v[216:219], v[80:83], a[212:215], v[216:219]\n"
      "v_mfma_f32_16x16x32_bf16 v[220:223], v[84:87], a[212:215], v[220:223]\n"
      "s_waitcnt lgkmcnt(14)\n"
      "v_mfma_f32_16x16x32_bf16 v[208:211], v[88:91], a[216:219], v[208:211]\n"
      "v_mfma_f32_16x16x32_bf16 v[212:215], v[92:95], a[216:219], v[212:215]\n"
      "global_load_dwordx4 v[132:135], v240, s[10:11] offset:-3072 sc1\n"
      "s_waitcnt lgkmcnt(12)\n"
      "v_mfma_f32_16x16x32_bf16 v[216:219], v[88:91], a[220:223], v[216:219]\n"
      "v_mfma_f32_16x16x32_bf16 v[220:223], v[92:95], a[220:223], v[220:223]\n"
      "s_waitcnt lgkmcnt(11)\n"
      "v_mfma_f32_16x16x32_bf16 v[208:211], v[96:99], a[224:227], v[208:211]\n"
      "v_mfma_f32_16x16x32_bf16 v[212:215], v[100:103], a[224:227], v[212:215]\n"
      "global_load_dwordx4 v[136:139], v240, s[10:11] offset:-2048 sc1\n"
      "s_waitcnt lgkmcnt(10)\n"
      "v_mfma_f32_16x16x32_bf16 v[216:219], v[96:99], a[228:231], v[216:219]\n"
      "v_mfma_f32_16x16x32_bf16 v[220:223], v[100:103], a[228:231], v[220:223]\n"
      "ds_read_b128 a[80:83], v236 offset:32768\n"
      "ds_read_b128 a[84:87], v236 offset:34816\n"
      "s_waitcnt lgkmcnt(11)\n"
      "v_mfma_f32_16x16x32_bf16 v[208:211], v[104:107], a[232:235], v[208:211]\n"
      "ds_read_b128 a[112:115], v237 offset:32768\n"
      "ds_read_b128 a[116:119], v237 offset:34816\n"
      "v_mfma_f32_16x16x32_bf16 v[212:215], v[108:111], a[232:235], v[212:215]\n"
      "global_load_dwordx4 v[140:143], v242, s[10:11] offset:-4096 sc1\n"
      "s_waitcnt lgkmcnt(12)\n"
      "v_mfma_f32_16x16x32_bf16 v[216:219], v[104:107], a[236:239], v[216:219]\n"
      "v_mfma_f32_16x16x32_bf16 v[220:223], v[108:111], a[236:239], v[220:223]\n"
      "s_waitcnt lgkmcnt(11)\n"
      "v_mfma_f32_16x16x32_bf16 v[208:211], v[112:115], a[240:243], v[208:211]\n"
      "global_store_dwordx4 v252, v[224:227], s[62:63] offset:-1024 sc1\n"
      "global_store_dwordx4 v252, v[228:231], s[62:63] offset:0 sc1\n"
      "v_mfma_f32_16x16x32_bf16 v[212:215], v[116:119], a[240:243], v[212:215]\n"
      "global_store_dwordx4 v252, v[232:235], s[62:63] offset:1024 sc1\n"
      "global_load_dwordx4 v[144:147], v242, s[10:11] offset:-3072 sc1\n"
      "s_waitcnt lgkmcnt(10)\n"
      "v_mfma_f32_16x16x32_bf16 v[216:219], v[112:115], a[244:247], v[216:219]\n"
      "v_mfma_f32_16x16x32_bf16 v[220:223], v[116:119], a[244:247], v[220:223]\n"
      "s_waitcnt lgkmcnt(6)\n"
      "v_mfma_f32_16x16x32_bf16 v[208:211], v[120:123], a[248:251], v[208:211]\n"
      "v_mfma_f32_16x16x32_bf16 v[212:215], v[124:127], a[248:251], v[212:215]\n"
      "global_load_dwordx4 v[148:151], v242, s[10:11] offset:-2048 sc1\n"
      "s_waitcnt lgkmcnt(4)\n"
      "v_mfma_f32_16x16x32_bf16 v[216:219], v[120:123], a[252:255], v[216:219]\n"
      "v_mfma_f32_16x16x32_bf16 v[220:223], v[124:127], a[252:255], v[220:223]\n"
      "s_waitcnt vmcnt(8)\n"
      "v_mfma_f32_16x16x32_bf16 v[0:3], v[128:131], a[0:3], 0\n"
      "v_mfma_f32_16x16x32_bf16 v[4:7], v[128:131], a[4:7], 0\n"
      "v_mfma_f32_16x16x32_bf16 v[8:11], v[128:131], a[8:11], 0\n"
      "v_cvt_pk_bf16_f32 v192, v208, v209\n"
      "v_cvt_pk_bf16_f32 v193, v210, v211\n"
      "v_mfma_f32_16x16x32_bf16 v[12:15], v[128:131], a[12:15], 0\n"
      "ds_read_b128 a[88:91], v236 offset:49152\n"
      "v_cvt_pk_bf16_f32 v194, v212, v213\n"
      "v_mfma_f32_16x16x32_bf16 v[16:19], v[128:131], a[16:19], 0\n"
      "ds_read_b128 a[92:95], v236 offset:51200\n"
      "v_cvt_pk_bf16_f32 v195, v214, v215\n"
      "v_mfma_f32_16x16x32_bf16 v[20:23], v[128:131], a[20:23], 0\n"
      "ds_read_b128 a[120:123], v237 offset:49152\n"
      "v_cvt_pk_bf16_f32 v196, v216, v217\n"
      "v_mfma_f32_16x16x32_bf16 v[24:27], v[128:131], a[24:27], 0\n"
      "ds_read_b128 a[124:127], v237 offset:51200\n"
      "v_cvt_pk_bf16_f32 v197, v218, v219\n"
      "v_mfma_f32_16x16x32_bf16 v[28:31], v[128:131], a[28:31], 0\n"
      "v_cvt_pk_bf16_f32 v198, v220, v221\n"
      "v_cvt_pk_bf16_f32 v199, v222, v223\n"
      "s_waitcnt vmcnt(7)\n"
      "v_mfma_f32_16x16x32_bf16 v[32:35], v[132:135], a[0:3], 0\n"
      "s_xor_b32 s70, s72, 128\n"
      "s_add_u32 s70, s8, s70\n"
      "s_addc_u32 s71, s9, 0\n"
      "s_add_u32 m0, s41, 4096\n"
      "v_mfma_f32_16x16x32_bf16 v[36:39], v[132:135], a[4:7], 0\n"
      "global_load_lds_dwordx4 v244, s[70:71] offset:0\n"
      "global_store_dwordx4 v252, v[192:195], s[62:63] offset:2048 sc1\n"
      "v_mfma_f32_16x16x32_bf16 v[40:43], v[132:135], a[8:11], 0\n"
      "global_store_dwordx4 v252, v[196:199], s[62:63] offset:3072 sc1\n"
      "v_mfma_f32_16x16x32_bf16 v[44:47], v[132:135], a[12:15], 0\n"
      "v_mfma_f32_16x16x32_bf16 v[48:51], v[132:135], a[16:19], 0\n"
      "global_load_lds_dwordx4 v245, s[70:71] offset:1024\n"
      "v_mfma_f32_16x16x32_bf16 v[52:55], v[132:135], a[20:23], 0\n"
      "v_mfma_f32_16x16x32_bf16 v[56:59], v[132:135], a[24:27], 0\n"
      "v_mfma_f32_16x16x32_bf16 v[60:63], v[132:135], a[28:31], 0\n"
      "s_waitcnt vmcnt(9)\n"
      "global_load_lds_dwordx4 v246, s[70:71] offset:2048\n"
      "v_mfma_f32_16x16x32_bf16 v[64:67], v[136:139], a[0:3], 0\n"
      "v_mfma_f32_16x16x32_bf16 v[68:71], v[136:139], a[4:7], 0\n"
      "v_mfma_f32_16x16x32_bf16 v[72:75], v[136:139], a[8:11], 0\n"
      "global_load_lds_dwordx4 v247, s[70:71] offset:3072\n"
      "v_mfma_f32_16x16x32_bf16 v[76:79], v[136:139], a[12:15], 0\n"
      "v_mfma_f32_16x16x32_bf16 v[80:83], v[136:139], a[16:19], 0\n"
      "global_load_dwordx4 v[152:155], v240, s[14:15] offset:-4096 sc1\n"
      "v_mfma_f32_16x16x32_bf16 v[84:87], v[136:139], a[20:23], 0\n"
      "v_mfma_f32_16x16x32_bf16 v[88:91], v[136:139], a[24:27], 0\n"
      "v_mfma_f32_16x16x32_bf16 v[92:95], v[136:139], a[28:31], 0\n"
      "global_load_dwordx4 v[156:159], v240, s[14:15] offset:-3072 sc1\n"
      "v_mfma_f32_16x16x32_bf16 v[0:3], v[140:143], a[32:35], v[0:3]\n"
      "v_mfma_f32_16x16x32_bf16 v[4:7], v[140:143], a[36:39], v[4:7]\n"
      "v_mfma_f32_16x16x32_bf16 v[8:11], v[140:143], a[40:43], v[8:11]\n"
      "global_load_dwordx4 v[160:163], v240, s[14:15] offset:-2048 sc1\n"
      "v_mfma_f32_16x16x32_bf16 v[12:15], v[140:143], a[44:47], v[12:15]\n"
      "v_mfma_f32_16x16x32_bf16 v[16:19], v[140:143], a[48:51], v[16:19]\n"
      "v_mfma_f32_16x16x32_bf16 v[20:23], v[140:143], a[52:55], v[20:23]\n"
      "s_xor_b32 s70, s73, 128\n"
      "s_add_u32 s70, s8, s70\n"
      "s_addc_u32 s71, s9, 0\n"
      "s_add_u32 m0, s41, 8192\n"
      "v_mfma_f32_16x16x32_bf16 v[24:27], v[140:143], a[56:59], v[24:27]\n"
      "global_load_lds_dwordx4 v244, s[70:71] offset:0\n"
      "v_mfma_f32_16x16x32_bf16 v[28:31], v[140:143], a[60:63], v[28:31]\n"
      "s_waitcnt vmcnt(10)\n"
      "v_mfma_f32_16x16x32_bf16 v[32:35], v[144:147], a[32:35], v[32:35]\n"
      "v_mfma_f32_16x16x32_bf16 v[36:39], v[144:147], a[36:39], v[36:39]\n"
      "global_load_lds_dwordx4 v245, s[70:71] offset:1024\n"
      "v_mfma_f32_16x16x32_bf16 v[40:43], v[144:147], a[40:43], v[40:43]\n"
      "v_mfma_f32_16x16x32_bf16 v[44:47], v[144:147], a[44:47], v[44:47]\n"
      "v_mfma_f32_16x16x32_bf16 v[48:51], v[144:147], a[48:51], v[48:51]\n"
      "global_load_lds_dwordx4 v246, s[70:71] offset:2048\n"
      "v_mfma_f32_16x16x32_bf16 v[52:55], v[144:147], a[52:55], v[52:55]\n"
      "v_mfma_f32_16x16x32_bf16 v[56:59], v[144:147], a[56:59], v[56:59]\n"
      "v_mfma_f32_16x16x32_bf16 v[60:63], v[144:147], a[60:63], v[60:63]\n"
      "global_load_lds_dwordx4 v247, s[70:71] offset:3072\n"
      "v_mfma_f32_16x16x32_bf16 v[64:67], v[148:151], a[32:35], v[64:67]\n"
      "v_add_u32 v238, s42, v252\n"
      "v_mov_b32 v200, s37\n"
      "v_mov_b32 v201, s37\n"
      "v_mov_b32 v202, s37\n"
      "v_mov_b32 v203, s37\n"
      "s_waitcnt vmcnt(10)\n"
      "s_mov_b64 exec, 0xff\n"
      "global_store_dwordx4 v238, v[200:203], s[58:59] offset:128 sc1\n"
      "s_mov_b64 exec, -1\n"
      "v_mfma_f32_16x16x32_bf16 v[68:71], v[148:151], a[36:39], v[68:71]\n"
      "s_lshl2_add_u32 s48, s36, s35\n"
      "s_xor_b32 s70, s74, 128\n"
      "s_add_u32 s70, s8, s70\n"
      "s_addc_u32 s71, s9, 0\n"
      "s_add_u32 m0, s41, 12288\n"
      "v_mfma_f32_16x16x32_bf16 v[72:75], v[148:151], a[40:43], v[72:75]\n"
      "s_mul_i32 s48, s48, 147456\n"
      "global_load_lds_dwordx4 v244, s[70:71] offset:0\n"
      "s_mul_i32 s49, s33, 3072\n"
      "v_mfma_f32_16x16x32_bf16 v[76:79], v[148:151], a[44:47], v[76:79]\n"
      "s_add_u32 s48, s48, s49\n"
      "s_add_u32 s48, s48, 6291456\n"
      "s_add_u32 s60, s18, s48\n"
      "s_addc_u32 s61, s19, 0\n"
      "v_mfma_f32_16x16x32_bf16 v[80:83], v[148:151], a[48:51], v[80:83]\n"
      "s_mul_i32 s49, s34, 36864\n"
      "s_add_u32 s62, s60, s49\n"
      "s_addc_u32 s63, s61, 0\n"
      "v_mfma_f32_16x16x32_bf16 v[84:87], v[148:151], a[52:55], v[84:87]\n"
      "global_load_lds_dwordx4 v245, s[70:71] offset:1024\n"
      "v_mfma_f32_16x16x32_bf16 v[88:91], v[148:151], a[56:59], v[88:91]\n"
      "v_mfma_f32_16x16x32_bf16 v[92:95], v[148:151], a[60:63], v[92:95]\n"
      "s_waitcnt vmcnt(8)\n"
      "v_mfma_f32_16x16x32_bf16 v[0:3], v[152:155], a[64:67], v[0:3]\n"
      "global_load_lds_dwordx4 v246, s[70:71] offset:2048\n"
      "ds_read_b128 a[128:131], v248 offset:4096\n"
      "v_mfma_f32_16x16x32_bf16 v[4:7], v[152:155], a[68:71], v[4:7]\n"
      "ds_read_b128 a[132:135], v248 offset:6144\n"
      "ds_read_b128 a[160:163], v249 offset:4096\n"
      "v_mfma_f32_16x16x32_bf16 v[8:11], v[152:155], a[72:75], v[8:11]\n"
      "ds_read_b128 a[164:167], v249 offset:6144\n"
      "v_mfma_f32_16x16x32_bf16 v[12:15], v[152:155], a[76:79], v[12:15]\n"
      "global_load_lds_dwordx4 v247, s[70:71] offset:3072\n"
      "s_waitcnt lgkmcnt(11)\n"
      "v_mfma_f32_16x16x32_bf16 v[16:19], v[152:155], a[80:83], v[16:19]\n"
      "s_waitcnt lgkmcnt(10)\n"
      "v_mfma_f32_16x16x32_bf16 v[20:23], v[152:155], a[84:87], v[20:23]\n"
      "global_load_dwordx4 v[164:167], v242, s[14:15] offset:-4096 sc1\n"
      "s_waitcnt lgkmcnt(7)\n"
      "v_mfma_f32_16x16x32_bf16 v[24:27], v[152:155], a[88:91], v[24:27]\n"
      "s_waitcnt lgkmcnt(6)\n"
      "v_mfma_f32_16x16x32_bf16 v[28:31], v[152:155], a[92:95], v[28:31]\n"
      "v_mfma_f32_16x16x32_bf16 v[32:35], v[156:159], a[64:67], v[32:35]\n"
      "global_load_dwordx4 v[168:171], v242, s[14:15] offset:-3072 sc1\n"
      "v_mfma_f32_16x16x32_bf16 v[36:39], v[156:159], a[68:71], v[36:39]\n"
      "v_mfma_f32_16x16x32_bf16 v[40:43], v[156:159], a[72:75], v[40:43]\n"
      "v_mfma_f32_16x16x32_bf16 v[44:47], v[156:159], a[76:79], v[44:47]\n"
      "global_load_dwordx4 v[172:175], v242, s[14:15] offset:-2048 sc1\n"
      "v_mfma_f32_16x16x32_bf16 v[48:51], v[156:159], a[80:83], v[48:51]\n"
      "v_mfma_f32_16x16x32_bf16 v[52:55], v[156:159], a[84:87], v[52:55]\n"
      "v_mfma_f32_16x16x32_bf16 v[56:59], v[156:159], a[88:91], v[56:59]\n"
      "s_xor_b32 s70, s75, 128\n"
      "s_add_u32 s70, s8, s70\n"
      "s_addc_u32 s71, s9, 0\n"
      "s_add_u32 m0, s78, 98304\n"
      "v_mfma_f32_16x16x32_bf16 v[60:63], v[156:159], a[92:95], v[60:63]\n"
      "s_waitcnt vmcnt(2)\n"
      "global_load_lds_dwordx4 v244, s[70:71] offset:0\n"
      "v_mfma_f32_16x16x32_bf16 v[64:67], v[160:163], a[64:67], v[64:67]\n"
      "v_mfma_f32_16x16x32_bf16 v[68:71], v[160:163], a[68:71], v[68:71]\n"
      "v_mfma_f32_16x16x32_bf16 v[72:75], v[160:163], a[72:75], v[72:75]\n"
      "global_load_lds_dwordx4 v245, s[70:71] offset:1024\n"
      "v_mfma_f32_16x16x32_bf16 v[76:79], v[160:163], a[76:79], v[76:79]\n"
      "v_mfma_f32_16x16x32_bf16 v[80:83], v[160:163], a[80:83], v[80:83]\n"
      "v_mfma_f32_16x16x32_bf16 v[84:87], v[160:163], a[84:87], v[84:87]\n"
      "global_load_lds_dwordx4 v246, s[70:71] offset:2048\n"
      "ds_read_b128 a[136:139], v248 offset:8192\n"
      "v_mfma_f32_16x16x32_bf16 v[88:91], v[160:163], a[88:91], v[88:91]\n"
      "ds_read_b128 a[140:143], v248 offset:10240\n"
      "ds_read_b128 a[168:171], v249 offset:8192\n"
      "v_mfma_f32_16x16x32_bf16 v[92:95], v[160:163], a[92:95], v[92:95]\n"
      "ds_read_b128 a[172:175], v249 offset:10240\n"
      "v_mfma_f32_16x16x32_bf16 v[0:3], v[164:167], a[96:99], v[0:3]\n"
      "global_load_lds_dwordx4 v247, s[70:71] offset:3072\n"
      "v_mfma_f32_16x16x32_bf16 v[4:7], v[164:167], a[100:103], v[4:7]\n"
      "v_mfma_f32_16x16x32_bf16 v[8:11], v[164:167], a[104:107], v[8:11]\n"
      "global_load_dwordx4 v[176:179], v241, s[10:11] offset:-4096 sc1\n"
      "v_mfma_f32_16x16x32_bf16 v[12:15], v[164:167], a[108:111], v[12:15]\n"
      "v_mfma_f32_16x16x32_bf16 v[16:19], v[164:167], a[112:115], v[16:19]\n"
      "v_mfma_f32_16x16x32_bf16 v[20:23], v[164:167], a[116:119], v[20:23]\n"
      "global_load_dwordx4 v[180:183], v241, s[10:11] offset:-3072 sc1\n"
      "s_waitcnt lgkmcnt(9)\n"
      "v_mfma_f32_16x16x32_bf16 v[24:27], v[164:167], a[120:123], v[24:27]\n"
      "s_waitcnt lgkmcnt(8)\n"
      "v_mfma_f32_16x16x32_bf16 v[28:31], v[164:167], a[124:127], v[28:31]\n"
      "s_waitcnt vmcnt(6)\n"
      "v_mfma_f32_16x16x32_bf16 v[32:35], v[168:171], a[96:99], v[32:35]\n"
      "global_load_dwordx4 v[184:187], v241, s[10:11] offset:-2048 sc1\n"
      "global_load_dwordx4 v[188:191], v243, s[10:11] offset:-4096 sc1\n"
      "v_mfma_f32_16x16x32_bf16 v[36:39], v[168:171], a[100:103], v[36:39]\n"
      "global_load_dwordx4 v[128:131], v243, s[10:11] offset:-3072 sc1\n"
      "global_load_dwordx4 v[132:135], v243, s[10:11] offset:-2048 sc1\n"
      "v_mfma_f32_16x16x32_bf16 v[40:43], v[168:171], a[104:107], v[40:43]\n"
      "global_load_dwordx4 v[136:139], v241, s[14:15] offset:-4096 sc1\n"
      "global_load_dwordx4 v[140:143], v243, s[14:15] offset:-4096 sc1\n"
      "v_mfma_f32_16x16x32_bf16 v[44:47], v[168:171], a[108:111], v[44:47]\n"
      "s_xor_b32 s70, s72, 384\n"
      "s_add_u32 s70, s8, s70\n"
      "s_addc_u32 s71, s9, 0\n"
      "s_add_u32 m0, s78, 114688\n"
      "v_mfma_f32_16x16x32_bf16 v[48:51], v[168:171], a[112:115], v[48:51]\n"
      "global_load_lds_dwordx4 v244, s[70:71] offset:0\n"
      "global_load_dwordx4 v[144:147], v241, s[14:15] offset:-3072 sc1\n"
      "v_mfma_f32_16x16x32_bf16 v[52:55], v[168:171], a[116:119], v[52:55]\n"
      "global_load_dwordx4 v[148:151], v243, s[14:15] offset:-3072 sc1\n"
      "global_load_dwordx4 v[152:155], v241, s[14:15] offset:-2048 sc1\n"
      "v_mfma_f32_16x16x32_bf16 v[56:59], v[168:171], a[120:123], v[56:59]\n"
      "global_load_dwordx4 v[156:159], v243, s[14:15] offset:-2048 sc1\n"
      "global_load_dword v253, v254, s[58:59] offset:128 sc1\n"
      "v_mfma_f32_16x16x32_bf16 v[60:63], v[168:171], a[124:127], v[60:63]\n"
      "global_load_lds_dwordx4 v245, s[70:71] offset:1024\n"
      "ds_read_b128 a[144:147], v248 offset:12288\n"
      "v_mfma_f32_16x16x32_bf16 v[64:67], v[172:175], a[96:99], v[64:67]\n"
      "ds_read_b128 a[148:151], v248 offset:14336\n"
      "ds_read_b128 a[176:179], v249 offset:12288\n"
      "v_mfma_f32_16x16x32_bf16 v[68:71], v[172:175], a[100:103], v[68:71]\n"
      "ds_read_b128 a[180:183], v249 offset:14336\n"
      "v_mfma_f32_16x16x32_bf16 v[72:75], v[172:175], a[104:107], v[72:75]\n"
      "global_load_lds_dwordx4 v246, s[70:71] offset:2048\n"
      "v_mfma_f32_16x16x32_bf16 v[76:79], v[172:175], a[108:111], v[76:79]\n"
      "v_mfma_f32_16x16x32_bf16 v[80:83], v[172:175], a[112:115], v[80:83]\n"
      "v_mfma_f32_16x16x32_bf16 v[84:87], v[172:175], a[116:119], v[84:87]\n"
      "global_load_lds_dwordx4 v247, s[70:71] offset:3072\n"
      "v_mfma_f32_16x16x32_bf16 v[88:91], v[172:175], a[120:123], v[88:91]\n"
      "v_mfma_f32_16x16x32_bf16 v[92:95], v[172:175], a[124:127], v[92:95]\n"
      "s_waitcnt vmcnt(15)\n"
      "s_xor_b32 s70, s73, 384\n"
      "s_add_u32 s70, s8, s70\n"
      "s_addc_u32 s71, s9, 0\n"
      "s_add_u32 m0, s41, 0\n"
      "s_waitcnt lgkmcnt(11)\n"
      "v_mfma_f32_16x16x32_bf16 v[0:3], v[176:179], a[128:131], v[0:3]\n"
      "global_load_lds_dwordx4 v244, s[70:71] offset:0\n"
      "s_waitcnt lgkmcnt(10)\n"
      "v_mfma_f32_16x16x32_bf16 v[4:7], v[176:179], a[132:135], v[4:7]\n"
      "s_waitcnt lgkmcnt(7)\n"
      "v_mfma_f32_16x16x32_bf16 v[8:11], v[176:179], a[136:139], v[8:11]\n"
      "s_waitcnt lgkmcnt(6)\n"
      "v_mfma_f32_16x16x32_bf16 v[12:15], v[176:179], a[140:143], v[12:15]\n"
      "global_load_lds_dwordx4 v245, s[70:71] offset:1024\n"
      "s_waitcnt lgkmcnt(3)\n"
      "v_mfma_f32_16x16x32_bf16 v[16:19], v[176:179], a[144:147], v[16:19]\n"
      "s_lshr_b32 s54, s34, 1\n"
      "s_lshl2_add_u32 s54, s54, s35\n"
      "s_lshl_b32 s54, s54, 12\n"
      "s_waitcnt lgkmcnt(2)\n"
      "v_mfma_f32_16x16x32_bf16 v[20:23], v[176:179], a[148:151], v[20:23]\n"
      "s_add_u32 s54, s54, 4096\n"
      "v_lshl_add_u32 v200, v255, 6, s54\n"
      "global_load_dwordx2 v[202:203], v200, s[58:59] sc1\n"
      "ds_read_b128 a[152:155], v236 offset:0\n"
      "ds_read_b128 a[156:159], v236 offset:2048\n"
      "ds_read_b128 a[184:187], v237 offset:0\n"
      "ds_read_b128 a[188:191], v237 offset:2048\n"
      "s_waitcnt lgkmcnt(3)\n"
      "v_mfma_f32_16x16x32_bf16 v[24:27], v[176:179], a[152:155], v[24:27]\n"
      "global_load_lds_dwordx4 v246, s[70:71] offset:2048\n"
      "s_waitcnt vmcnt(4)\n"
      "ds_read_b128 a[192:195], v236 offset:16384\n"
      "s_waitcnt lgkmcnt(3)\n"
      "v_mfma_f32_16x16x32_bf16 v[28:31], v[176:179], a[156:159], v[28:31]\n"
      "ds_read_b128 a[196:199], v236 offset:18432\n"
      "ds_read_b128 a[224:227], v237 offset:16384\n"
      "v_mfma_f32_16x16x32_bf16 v[32:35], v[180:183], a[128:131], v[32:35]\n"
      "ds_read_b128 a[228:231], v237 offset:18432\n"
      "v_mfma_f32_16x16x32_bf16 v[36:39], v[180:183], a[132:135], v[36:39]\n"
      "global_load_lds_dwordx4 v247, s[70:71] offset:3072\n"
      "v_mfma_f32_16x16x32_bf16 v[40:43], v[180:183], a[136:139], v[40:43]\n"
      "v_mfma_f32_16x16x32_bf16 v[44:47], v[180:183], a[140:143], v[44:47]\n"
      "s_xor_b32 s70, s74, 384\n"
      "s_add_u32 s70, s8, s70\n"
      "s_addc_u32 s71, s9, 0\n"
      "s_add_u32 m0, s78, 131072\n"
      "v_mfma_f32_16x16x32_bf16 v[48:51], v[180:183], a[144:147], v[48:51]\n"
      "global_load_lds_dwordx4 v244, s[70:71] offset:0\n"
      "v_mfma_f32_16x16x32_bf16 v[52:55], v[180:183], a[148:151], v[52:55]\n"
      "v_mfma_f32_16x16x32_bf16 v[56:59], v[180:183], a[152:155], v[56:59]\n"
      "v_mfma_f32_16x16x32_bf16 v[60:63], v[180:183], a[156:159], v[60:63]\n"
      "global_load_lds_dwordx4 v245, s[70:71] offset:1024\n"
      "v_mfma_f32_16x16x32_bf16 v[64:67], v[184:187], a[128:131], v[64:67]\n"
      "v_mfma_f32_16x16x32_bf16 v[68:71], v[184:187], a[132:135], v[68:71]\n"
      "v_mfma_f32_16x16x32_bf16 v[72:75], v[184:187], a[136:139], v[72:75]\n"
      "global_load_lds_dwordx4 v246, s[70:71] offset:2048\n"
      "v_mfma_f32_16x16x32_bf16 v[76:79], v[184:187], a[140:143], v[76:79]\n"
      "v_mfma_f32_16x16x32_bf16 v[80:83], v[184:187], a[144:147], v[80:83]\n"
      "v_mfma_f32_16x16x32_bf16 v[84:87], v[184:187], a[148:151], v[84:87]\n"
      "global_load_lds_dwordx4 v247, s[70:71] offset:3072\n"
      "v_mfma_f32_16x16x32_bf16 v[88:91], v[184:187], a[152:155], v[88:91]\n"
      "v_mfma_f32_16x16x32_bf16 v[92:95], v[184:187], a[156:159], v[92:95]\n"
      "s_xor_b32 s70, s75, 384\n"
      "s_add_u32 s70, s8, s70\n"
      "s_addc_u32 s71, s9, 0\n"
      "s_add_u32 m0, s78, 147456\n"
      "v_mfma_f32_16x16x32_bf16 v[0:3], v[188:191], a[160:163], v[0:3]\n"
      "global_load_lds_dwordx4 v244, s[70:71] offset:0\n"
      "v_mfma_f32_16x16x32_bf16 v[4:7], v[188:191], a[164:167], v[4:7]\n"
      "v_mfma_f32_16x16x32_bf16 v[8:11], v[188:191], a[168:171], v[8:11]\n"
      "v_mfma_f32_16x16x32_bf16 v[12:15], v[188:191], a[172:175], v[12:15]\n"
      "global_load_lds_dwordx4 v245, s[70:71] offset:1024\n"
      "v_mfma_f32_16x16x32_bf16 v[16:19], v[188:191], a[176:179], v[16:19]\n"
      "v_mfma_f32_16x16x32_bf16 v[20:23], v[188:191], a[180:183], v[20:23]\n"
      "s_waitcnt lgkmcnt(5)\n"
      "v_mfma_f32_16x16x32_bf16 v[24:27], v[188:191], a[184:187], v[24:27]\n"
      "global_load_lds_dwordx4 v246, s[70:71] offset:2048\n"
      "s_waitcnt lgkmcnt(4)\n"
      "v_mfma_f32_16x16x32_bf16 v[28:31], v[188:191], a[188:191], v[28:31]\n"
      "v_mfma_f32_16x16x32_bf16 v[32:35], v[128:131], a[160:163], v[32:35]\n"
      "v_mfma_f32_16x16x32_bf16 v[36:39], v[128:131], a[164:167], v[36:39]\n"
      "global_load_lds_dwordx4 v247, s[70:71] offset:3072\n"
      "v_mfma_f32_16x16x32_bf16 v[40:43], v[128:131], a[168:171], v[40:43]\n"
      "v_mfma_f32_16x16x32_bf16 v[44:47], v[128:131], a[172:175], v[44:47]\n"
      "v_mfma_f32_16x16x32_bf16 v[48:51], v[128:131], a[176:179], v[48:51]\n"
      "v_mfma_f32_16x16x32_bf16 v[52:55], v[128:131], a[180:183], v[52:55]\n"
      "v_mfma_f32_16x16x32_bf16 v[56:59], v[128:131], a[184:187], v[56:59]\n"
      "v_mfma_f32_16x16x32_bf16 v[60:63], v[128:131], a[188:191], v[60:63]\n"
      "s_waitcnt vmcnt(8)\n"
      "ds_read_b128 a[200:203], v248 offset:0\n"
      "v_mfma_f32_16x16x32_bf16 v[64:67], v[132:135], a[160:163], v[64:67]\n"
      "ds_read_b128 a[204:207], v248 offset:2048\n"
      "ds_read_b128 a[232:235], v249 offset:0\n"
      "v_mfma_f32_16x16x32_bf16 v[68:71], v[132:135], a[164:167], v[68:71]\n"
      "ds_read_b128 a[236:239], v249 offset:2048\n"
      "v_mfma_f32_16x16x32_bf16 v[72:75], v[132:135], a[168:171], v[72:75]\n"
      "v_cmp_ne_u32 vcc, s37, v253\n"
      "s_nop 1\n"
      "s_cbranch_vccnz .Lp_s5_%=\n"
      ".Lp_o6_%=:\n"
      "v_mfma_f32_16x16x32_bf16 v[76:79], v[132:135], a[172:175], v[76:79]\n"
      "v_mfma_f32_16x16x32_bf16 v[80:83], v[132:135], a[176:179], v[80:83]\n"
      "global_load_dwordx4 v[160:163], v240, s[10:11] offset:-1024 sc1\n"
      "global_load_dwordx4 v[164:167], v242, s[10:11] offset:-1024 sc1\n"
      "v_mfma_f32_16x16x32_bf16 v[84:87], v[132:135], a[180:183], v[84:87]\n"
      "global_load_dwordx4 v[168:171], v240, s[14:15] offset:-1024 sc1\n"
      "global_load_dwordx4 v[172:175], v242, s[14:15] offset:-1024 sc1\n"
      "v_mfma_f32_16x16x32_bf16 v[88:91], v[132:135], a[184:187], v[88:91]\n"
      "global_load_dwordx4 v[176:179], v241, s[10:11] offset:-1024 sc1\n"
      "global_load_dwordx4 v[180:183], v243, s[10:11] offset:-1024 sc1\n"
      "v_mfma_f32_16x16x32_bf16 v[92:95], v[132:135], a[188:191], v[92:95]\n"
      "global_load_dwordx4 v[184:187], v241, s[14:15] offset:-1024 sc1\n"
      "global_load_dwordx4 v[188:191], v243, s[14:15] offset:-1024 sc1\n"
      "s_waitcnt lgkmcnt(7)\n"
      "v_mfma_f32_16x16x32_bf16 v[0:3], v[136:139], a[192:195], v[0:3]\n"
      "global_load_dwordx4 v[128:131], v240, s[10:11] offset:0 sc1\n"
      "global_load_dwordx4 v[132:135], v242, s[10:11] offset:0 sc1\n"
      "s_waitcnt lgkmcnt(6)\n"
      "v_mfma_f32_16x16x32_bf16 v[4:7], v[136:139], a[196:199], v[4:7]\n"
      "s_waitcnt lgkmcnt(3)\n"
      "v_mfma_f32_16x16x32_bf16 v[8:11], v[136:139], a[200:203], v[8:11]\n"
      "s_waitcnt lgkmcnt(2)\n"
      "v_mfma_f32_16x16x32_bf16 v[12:15], v[136:139], a[204:207], v[12:15]\n"
      "s_waitcnt vmcnt(14)\n"
      "ds_read_b128 a[208:211], v236 offset:32768\n"
      "ds_read_b128 a[212:215], v236 offset:34816\n"
      "ds_read_b128 a[240:243], v237 offset:32768\n"
      "ds_read_b128 a[244:247], v237 offset:34816\n"
      "s_waitcnt lgkmcnt(3)\n"
      "v_mfma_f32_16x16x32_bf16 v[16:19], v[136:139], a[208:211], v[16:19]\n"
      "s_waitcnt vmcnt(10)\n"
      "ds_read_b128 a[216:219], v236 offset:49152\n"
      "s_waitcnt lgkmcnt(3)\n"
      "v_mfma_f32_16x16x32_bf16 v[20:23], v[136:139], a[212:215], v[20:23]\n"
      "ds_read_b128 a[220:223], v236 offset:51200\n"
      "ds_read_b128 a[248:251], v237 offset:49152\n"
      "ds_read_b128 a[252:255], v237 offset:51200\n"
      "s_waitcnt lgkmcnt(3)\n"
      "v_mfma_f32_16x16x32_bf16 v[24:27], v[136:139], a[216:219], v[24:27]\n"
      "s_sub_u32 s48, s33, 1\n"
      "s_and_b32 s48, s48, 3\n"
      "s_lshl_b32 s48, s48, 14\n"
      "s_waitcnt lgkmcnt(2)\n"
      "v_mfma_f32_16x16x32_bf16 v[28:31], v[136:139], a[220:223], v[28:31]\n"
      "global_load_dwordx4 v[136:139], v240, s[14:15] offset:0 sc1\n"
      "v_lshl_add_u32 v244, v255, 2, s48\n"
      "v_mfma_f32_16x16x32_bf16 v[0:3], v[140:143], a[224:227], v[0:3]\n"
      "s_sub_u32 s48, s33, 2\n"
      "s_and_b32 s48, s48, 3\n"
      "s_lshl_b32 s48, s48, 14\n"
      "v_mfma_f32_16x16x32_bf16 v[4:7], v[140:143], a[228:231], v[4:7]\n"
      "v_lshl_add_u32 v245, v255, 2, s48\n"
      "s_sub_u32 s48, s33, 3\n"
      "v_mfma_f32_16x16x32_bf16 v[8:11], v[140:143], a[232:235], v[8:11]\n"
      "s_and_b32 s48, s48, 3\n"
      "s_lshl_b32 s48, s48, 14\n"
      "v_lshl_add_u32 v246, v255, 2, s48\n"
      "v_mfma_f32_16x16x32_bf16 v[12:15], v[140:143], a[236:239], v[12:15]\n"
      "s_add_u32 s48, s34, 1\n"
      "s_and_b32 s48, s48, 3\n"
      "s_mul_i32 s48, s48, 36864\n"
      "v_mfma_f32_16x16x32_bf16 v[16:19], v[140:143], a[240:243], v[16:19]\n"
      "s_add_u32 s48, s48, 0\n"
      "s_add_u32 s52, s60, s48\n"
      "s_addc_u32 s53, s61, 0\n"
      "v_mfma_f32_16x16x32_bf16 v[20:23], v[140:143], a[244:247], v[20:23]\n"
      "s_waitcnt lgkmcnt(1)\n"
      "v_mfma_f32_16x16x32_bf16 v[24:27], v[140:143], a[248:251], v[24:27]\n"
      "s_waitcnt lgkmcnt(0)\n"
      "v_mfma_f32_16x16x32_bf16 v[28:31], v[140:143], a[252:255], v[28:31]\n"
      "global_load_dwordx4 v[140:143], v242, s[14:15] offset:0 sc1\n"
      "v_mfma_f32_16x16x32_bf16 v[32:35], v[144:147], a[192:195], v[32:35]\n"
      "v_cmp_ne_u32 vcc, s37, v203\n"
      "s_nop 1\n"
      "s_cbranch_vccnz .Lp_s8_%=\n"
      ".Lp_o9_%=:\n"
      "v_mfma_f32_16x16x32_bf16 v[36:39], v[144:147], a[196:199], v[36:39]\n"
      "s_add_u32 m0, s41, 0\n"
      "s_nop 0\n"
      "ds_write_addtid_b32 v8 offset:0\n"
      "v_mfma_f32_16x16x32_bf16 v[40:43], v[144:147], a[200:203], v[40:43]\n"
      "ds_write_addtid_b32 v9 offset:256\n"
      "v_max_u32_dpp v202, v202, v202 quad_perm:[1,0,3,2] row_mask:0xf bank_mask:0xf\n"
      "v_mfma_f32_16x16x32_bf16 v[44:47], v[144:147], a[204:207], v[44:47]\n"
      "ds_write_addtid_b32 v10 offset:512\n"
      "v_max_u32_dpp v202, v202, v202 quad_perm:[2,3,0,1] row_mask:0xf bank_mask:0xf\n"
      "v_mfma_f32_16x16x32_bf16 v[48:51], v[144:147], a[208:211], v[48:51]\n"
      "ds_write_addtid_b32 v11 offset:768\n"
      "v_max_u32_dpp v202, v202, v202 row_half_mirror row_mask:0xf bank_mask:0xf\n"
      "v_mfma_f32_16x16x32_bf16 v[52:55], v[144:147], a[212:215], v[52:55]\n"
      "ds_write_addtid_b32 v12 offset:1024\n"
      "v_max_u32_dpp v202, v202, v202 row_mirror row_mask:0xf bank_mask:0xf\n"
      "v_mfma_f32_16x16x32_bf16 v[56:59], v[144:147], a[216:219], v[56:59]\n"
      "ds_write_addtid_b32 v13 offset:1280\n"
      "v_mov_b32 v201, v202\n"
      "v_mfma_f32_16x16x32_bf16 v[60:63], v[144:147], a[220:223], v[60:63]\n"
      "global_load_dwordx4 v[144:147], v241, s[10:11] offset:0 sc1\n"
      "v_permlane16_swap_b32 v202, v201\n"
      "v_mfma_f32_16x16x32_bf16 v[32:35], v[148:151], a[224:227], v[32:35]\n"
      "ds_write_addtid_b32 v14 offset:1536\n"
      "v_max_u32 v202, v202, v201\n"
      "v_mfma_f32_16x16x32_bf16 v[36:39], v[148:151], a[228:231], v[36:39]\n"
      "ds_write_addtid_b32 v15 offset:1792\n"
      "v_mov_b32 v201, v202\n"
      "v_mfma_f32_16x16x32_bf16 v[40:43], v[148:151], a[232:235], v[40:43]\n"
      "ds_write_addtid_b32 v16 offset:2048\n"
      "v_permlane32_swap_b32 v202, v201\n"
      "v_mfma_f32_16x16x32_bf16 v[44:47], v[148:151], a[236:239], v[44:47]\n"
      "ds_write_addtid_b32 v17 offset:2304\n"
      "v_max_u32 v202, v202, v201\n"
      "v_mfma_f32_16x16x32_bf16 v[48:51], v[148:151], a[240:243], v[48:51]\n"
      "ds_write_addtid_b32 v18 offset:2560\n"
      "v_readfirstlane_b32 s40, v202\n"
      "v_mfma_f32_16x16x32_bf16 v[52:55], v[148:151], a[244:247], v[52:55]\n"
      "s_nop 0\n"
      "s_andn2_b32 s42, 0x7ff, s40\n"
      "ds_write_addtid_b32 v19 offset:2816\n"
      "v_mfma_f32_16x16x32_bf16 v[56:59], v[148:151], a[248:251], v[56:59]\n"
      "s_lshl2_add_u32 s54, s36, s33\n"
      "ds_write_addtid_b32 v20 offset:3072\n"
      "s_lshl_b32 s55, s54, 15\n"
      "v_mfma_f32_16x16x32_bf16 v[60:63], v[148:151], a[252:255], v[60:63]\n"
      "global_load_dwordx4 v[148:151], v243, s[10:11] offset:0 sc1\n"
      "s_lshl2_add_u32 s56, s35, s34\n"
      "v_mfma_f32_16x16x32_bf16 v[64:67], v[152:155], a[192:195], v[64:67]\n"
      "s_lshl_b32 s56, s56, 6\n"
      "ds_write_addtid_b32 v21 offset:3328\n"
      "s_add_u32 s55, s55, s56\n"
      "v_mfma_f32_16x16x32_bf16 v[68:71], v[152:155], a[196:199], v[68:71]\n"
      "ds_write_addtid_b32 v22 offset:3584\n"
      "v_and_b32 v248, 15, v255\n"
      "v_mfma_f32_16x16x32_bf16 v[72:75], v[152:155], a[200:203], v[72:75]\n"
      "ds_write_addtid_b32 v23 offset:3840\n"
      "v_and_b32 v251, 48, v255\n"
      "v_mfma_f32_16x16x32_bf16 v[76:79], v[152:155], a[204:207], v[76:79]\n"
      "ds_write_addtid_b32 v24 offset:4096\n"
      "v_lshl_or_b32 v251, v248, 10, v251\n"
      "v_mfma_f32_16x16x32_bf16 v[80:83], v[152:155], a[208:211], v[80:83]\n"
      "ds_write_addtid_b32 v25 offset:4352\n"
      "v_add_u32 v251, s55, v251\n"
      "v_mfma_f32_16x16x32_bf16 v[84:87], v[152:155], a[212:215], v[84:87]\n"
      "ds_write_addtid_b32 v26 offset:4608\n"
      "v_add_u32 v248, 0x10000, v244\n"
      "v_mfma_f32_16x16x32_bf16 v[88:91], v[152:155], a[216:219], v[88:91]\n"
      "ds_write_addtid_b32 v27 offset:4864\n"
      "v_add_u32 v249, 0x10000, v245\n"
      "v_mfma_f32_16x16x32_bf16 v[92:95], v[152:155], a[220:223], v[92:95]\n"
      "global_load_dwordx4 v[152:155], v241, s[14:15] offset:0 sc1\n"
      "v_add_u32 v250, 0x10000, v246\n"
      "v_mfma_f32_16x16x32_bf16 v[64:67], v[156:159], a[224:227], v[64:67]\n"
      "s_sub_u32 s54, s34, 1\n"
      "ds_write_addtid_b32 v28 offset:5120\n"
      "s_xor_b32 s54, s54, s34\n"
      "v_mfma_f32_16x16x32_bf16 v[68:71], v[156:159], a[228:231], v[68:71]\n"
      "s_lshl_b32 s54, s54, 30\n"
      "ds_write_addtid_b32 v29 offset:5376\n"
      "s_and_b32 s72, s54, 0x80000000\n"
      "v_mfma_f32_16x16x32_bf16 v[72:75], v[156:159], a[232:235], v[72:75]\n"
      "s_sub_u32 s54, s34, 2\n"
      "ds_write_addtid_b32 v30 offset:5632\n"
      "s_xor_b32 s54, s54, s34\n"
      "v_mfma_f32_16x16x32_bf16 v[76:79], v[156:159], a[236:239], v[76:79]\n"
      "s_lshl_b32 s54, s54, 30\n"
      "ds_write_addtid_b32 v31 offset:5888\n"
      "s_and_b32 s73, s54, 0x80000000\n"
      "v_mfma_f32_16x16x32_bf16 v[80:83], v[156:159], a[240:243], v[80:83]\n"
      "s_sub_u32 s54, s34, 3\n"
      "s_xor_b32 s54, s54, s34\n"
      "s_lshl_b32 s54, s54, 30\n"
      "v_mfma_f32_16x16x32_bf16 v[84:87], v[156:159], a[244:247], v[84:87]\n"
      "s_and_b32 s74, s54, 0x80000000\n"
      "s_lshr_b32 s43, s42, 5\n"
      "s_lshl2_add_u32 s44, s36, s33\n"
      "s_cmp_eq_u32 s43, s44\n"
      "s_cbranch_scc0 .Lp_e10_%=\n"
      "s_lshl_b32 s43, s42, 10\n"
      "v_add_u32 v197, s43, v252\n"
      "global_load_dwordx4 v[232:235], v197, s[16:17]\n"
      "global_load_dwordx4 v[236:239], v252, s[24:25]\n"
      ".Lp_e10_%=:\n"
      "v_mfma_f32_16x16x32_bf16 v[88:91], v[156:159], a[248:251], v[88:91]\n"
      "s_lshl2_add_u32 s54, s36, s33\n"
      "s_lshl_b32 s55, s54, 15\n"
      "s_lshl2_add_u32 s56, s35, s34\n"
      "v_mfma_f32_16x16x32_bf16 v[92:95], v[156:159], a[252:255], v[92:95]\n"
      "s_waitcnt vmcnt(13)\n"
      "global_load_dwordx4 v[156:159], v243, s[14:15] offset:0 sc1\n"
      "s_lshl_b32 s56, s56, 6\n"
      "v_mfma_f32_16x16x32_bf16 v[96:99], v[160:163], a[0:3], 0\n"
      "s_add_u32 s55, s55, s56\n"
      "v_and_b32 v198, 15, v255\n"
      "v_and_b32 v199, 48, v255\n"
      "v_mfma_f32_16x16x32_bf16 v[100:103], v[160:163], a[4:7], 0\n"
      "s_add_u32 m0, s41, 0\n"
      "s_nop 0\n"
      "ds_write_addtid_b32 v40 offset:6144\n"
      "v_mfma_f32_16x16x32_bf16 v[104:107], v[160:163], a[8:11], 0\n"
      "ds_write_addtid_b32 v41 offset:6400\n"
      "v_lshl_or_b32 v199, v198, 10, v199\n"
      "v_mfma_f32_16x16x32_bf16 v[108:111], v[160:163], a[12:15], 0\n"
      "ds_write_addtid_b32 v42 offset:6656\n"
      "v_add_u32 v199, s55, v199\n"
      "v_mfma_f32_16x16x32_bf16 v[112:115], v[160:163], a[16:19], 0\n"
      "ds_write_addtid_b32 v43 offset:6912\n"
      "v_add_u32 v197, 16384, v199\n"
      "v_mfma_f32_16x16x32_bf16 v[116:119], v[160:163], a[20:23], 0\n"
      "global_load_dwordx4 v[224:227], v199, s[16:17]\n"
      "global_load_dwordx4 v[228:231], v197, s[16:17]\n"
      "v_mfma_f32_16x16x32_bf16 v[120:123], v[160:163], a[24:27], 0\n"
      "s_lshr_b32 s55, s34, 1\n"
      "ds_write_addtid_b32 v44 offset:7168\n"
      "s_lshl2_add_u32 s55, s55, s35\n"
      "v_mfma_f32_16x16x32_bf16 v[124:127], v[160:163], a[28:31], 0\n"
      "s_lshl_b32 s55, s55, 15\n"
      "global_load_dwordx4 v[160:163], v240, s[10:11] offset:1024 sc1\n"
      "s_lshl_b32 s54, s54, 9\n"
      "v_mfma_f32_16x16x32_bf16 v[96:99], v[164:167], a[32:35], v[96:99]\n"
      "s_add_u32 s54, s54, s55\n"
      "ds_write_addtid_b32 v45 offset:7424\n"
      "s_add_u32 s54, s54, 786432\n"
      "v_mfma_f32_16x16x32_bf16 v[100:103], v[164:167], a[36:39], v[100:103]\n"
      "ds_write_addtid_b32 v46 offset:7680\n"
      "v_lshl_add_u32 v198, v198, 4, s54\n"
      "v_mfma_f32_16x16x32_bf16 v[104:107], v[164:167], a[40:43], v[104:107]\n"
      "global_load_dwordx2 v[204:205], v198, s[58:59] offset:0 sc1\n"
      "global_load_dwordx2 v[206:207], v198, s[58:59] offset:256 sc1\n"
      "v_mfma_f32_16x16x32_bf16 v[108:111], v[164:167], a[44:47], v[108:111]\n"
      "s_add_u32 s68, s20, 16384\n"
      "s_addc_u32 s69, s21, 0\n"
      "ds_write_addtid_b32 v47 offset:7936\n"
      "v_mfma_f32_16x16x32_bf16 v[112:115], v[164:167], a[48:51], v[112:115]\n"
      "s_ashr_i32 s54, s40, 31\n"
      "ds_write_addtid_b32 v48 offset:8192\n"
      "s_not_b32 s54, s54\n"
      "v_mfma_f32_16x16x32_bf16 v[116:119], v[164:167], a[52:55], v[116:119]\n"
      "s_or_b32 s54, s54, 0x80000000\n"
      "ds_write_addtid_b32 v49 offset:8448\n"
      "s_xor_b32 s54, s54, s40\n"
      "v_mfma_f32_16x16x32_bf16 v[120:123], v[164:167], a[56:59], v[120:123]\n"
      "s_and_b32 s54, s54, 0xffff0000\n"
      "ds_write_addtid_b32 v50 offset:8704\n"
      "v_mov_b32 v192, s54\n"
      "v_mfma_f32_16x16x32_bf16 v[124:127], v[164:167], a[60:63], v[124:127]\n"
      "s_waitcnt vmcnt(17)\n"
      "global_load_dwordx4 v[164:167], v242, s[10:11] offset:1024 sc1\n"
      "v_mul_f32 v192, 0xbfb8aa3b, v192\n"
      "v_mfma_f32_16x16x32_bf16 v[96:99], v[168:171], a[64:67], v[96:99]\n"
      "ds_write_addtid_b32 v51 offset:8960\n"
      "v_exp_f32 v192, v192\n"
      "v_mfma_f32_16x16x32_bf16 v[100:103], v[168:171], a[68:71], v[100:103]\n"
      "ds_write_addtid_b32 v52 offset:9216\n"
      "v_add_f32 v192, 1.0, v192\n"
      "v_mfma_f32_16x16x32_bf16 v[104:107], v[168:171], a[72:75], v[104:107]\n"
      "ds_write_addtid_b32 v53 offset:9472\n"
      "v_rcp_f32 v192, v192\n"
      "v_mfma_f32_16x16x32_bf16 v[108:111], v[168:171], a[76:79], v[108:111]\n"
      "ds_write_addtid_b32 v54 offset:9728\n"
      "v_cvt_pk_bf16_f32 v192, v192, v192\n"
      "v_mfma_f32_16x16x32_bf16 v[112:115], v[168:171], a[80:83], v[112:115]\n"
      "ds_write_addtid_b32 v55 offset:9984\n"
      "v_and_b32 v192, 0xffff0000, v192\n"
      "v_mfma_f32_16x16x32_bf16 v[116:119], v[168:171], a[84:87], v[116:119]\n"
      "ds_write_addtid_b32 v56 offset:10240\n"
      "v_readfirstlane_b32 s80, v192\n"
      "v_mfma_f32_16x16x32_bf16 v[120:123], v[168:171], a[88:91], v[120:123]\n"
      "ds_write_addtid_b32 v57 offset:10496\n"
      "ds_write_addtid_b32 v58 offset:10752\n"
      "v_mfma_f32_16x16x32_bf16 v[124:127], v[168:171], a[92:95], v[124:127]\n"
      "global_load_dwordx4 v[168:171], v240, s[14:15] offset:1024 sc1\n"
      "ds_write_addtid_b32 v59 offset:11008\n"
      "v_mfma_f32_16x16x32_bf16 v[96:99], v[172:175], a[96:99], v[96:99]\n"
      "ds_write_addtid_b32 v60 offset:11264\n"
      "ds_write_addtid_b32 v61 offset:11520\n"
      "v_mfma_f32_16x16x32_bf16 v[100:103], v[172:175], a[100:103], v[100:103]\n"
      "ds_write_addtid_b32 v62 offset:11776\n"
      "ds_write_addtid_b32 v63 offset:12032\n"
      "v_mfma_f32_16x16x32_bf16 v[104:107], v[172:175], a[104:107], v[104:107]\n"
      "v_mfma_f32_16x16x32_bf16 v[108:111], v[172:175], a[108:111], v[108:111]\n"
      "v_mfma_f32_16x16x32_bf16 v[112:115], v[172:175], a[112:115], v[112:115]\n"
      "s_waitcnt lgkmcnt(0)\n"
      "s_barrier\n"
      "v_mfma_f32_16x16x32_bf16 v[116:119], v[172:175], a[116:119], v[116:119]\n"
      "ds_read2st64_b32 v[8:9], v244 offset0:0 offset1:1\n"
      "ds_read2st64_b32 v[10:11], v244 offset0:2 offset1:3\n"
      "v_mfma_f32_16x16x32_bf16 v[120:123], v[172:175], a[120:123], v[120:123]\n"
      "ds_read2st64_b32 v[12:13], v244 offset0:4 offset1:5\n"
      "ds_read2st64_b32 v[14:15], v244 offset0:6 offset1:7\n"
      "v_mfma_f32_16x16x32_bf16 v[124:127], v[172:175], a[124:127], v[124:127]\n"
      "s_waitcnt vmcnt(17)\n"
      "global_load_dwordx4 v[172:175], v242, s[14:15] offset:1024 sc1\n"
      "ds_read2st64_b32 v[16:17], v245 offset0:8 offset1:9\n"
      "v_mfma_f32_16x16x32_bf16 v[96:99], v[176:179], a[128:131], v[96:99]\n"
      "ds_read2st64_b32 v[18:19], v245 offset0:10 offset1:11\n"
      "ds_read2st64_b32 v[20:21], v245 offset0:12 offset1:13\n"
      "v_mfma_f32_16x16x32_bf16 v[100:103], v[176:179], a[132:135], v[100:103]\n"
      "ds_read2st64_b32 v[22:23], v245 offset0:14 offset1:15\n"
      "ds_read2st64_b32 v[24:25], v246 offset0:16 offset1:17\n"
      "v_mfma_f32_16x16x32_bf16 v[104:107], v[176:179], a[136:139], v[104:107]\n"
      "ds_read2st64_b32 v[26:27], v246 offset0:18 offset1:19\n"
      "ds_read2st64_b32 v[28:29], v246 offset0:20 offset1:21\n"
      "v_mfma_f32_16x16x32_bf16 v[108:111], v[176:179], a[140:143], v[108:111]\n"
      "ds_read2st64_b32 v[30:31], v246 offset0:22 offset1:23\n"
      "ds_read2st64_b32 v[40:41], v244 offset0:24 offset1:25\n"
      "v_mfma_f32_16x16x32_bf16 v[112:115], v[176:179], a[144:147], v[112:115]\n"
      "ds_read2st64_b32 v[42:43], v244 offset0:26 offset1:27\n"
      "ds_read2st64_b32 v[44:45], v244 offset0:28 offset1:29\n"
      "v_mfma_f32_16x16x32_bf16 v[116:119], v[176:179], a[148:151], v[116:119]\n"
      "s_waitcnt lgkmcnt(11)\n"
      "ds_read2st64_b32 v[46:47], v244 offset0:30 offset1:31\n"
      "v_add_f32 v0, v0, v8\n"
      "v_mfma_f32_16x16x32_bf16 v[120:123], v[176:179], a[152:155], v[120:123]\n"
      "ds_read2st64_b32 v[48:49], v245 offset0:32 offset1:33\n"
      "v_add_f32 v1, v1, v9\n"
      "v_mfma_f32_16x16x32_bf16 v[124:127], v[176:179], a[156:159], v[124:127]\n"
      "global_load_dwordx4 v[176:179], v241, s[10:11] offset:1024 sc1\n"
      "v_add_f32 v2, v2, v10\n"
      "v_mfma_f32_16x16x32_bf16 v[96:99], v[180:183], a[160:163], v[96:99]\n"
      "ds_read2st64_b32 v[50:51], v245 offset0:34 offset1:35\n"
      "v_add_f32 v3, v3, v11\n"
      "v_mfma_f32_16x16x32_bf16 v[100:103], v[180:183], a[164:167], v[100:103]\n"
      "ds_read2st64_b32 v[52:53], v245 offset0:36 offset1:37\n"
      "v_add_f32 v4, v4, v12\n"
      "v_mfma_f32_16x16x32_bf16 v[104:107], v[180:183], a[168:171], v[104:107]\n"
      "ds_read2st64_b32 v[54:55], v245 offset0:38 offset1:39\n"
      "v_add_f32 v5, v5, v13\n"
      "v_mfma_f32_16x16x32_bf16 v[108:111], v[180:183], a[172:175], v[108:111]\n"
      "ds_read2st64_b32 v[56:57], v246 offset0:40 offset1:41\n"
      "v_add_f32 v6, v6, v14\n"
      "v_mfma_f32_16x16x32_bf16 v[112:115], v[180:183], a[176:179], v[112:115]\n"
      "ds_read2st64_b32 v[58:59], v246 offset0:42 offset1:43\n"
      "v_add_f32 v7, v7, v15\n"
      "v_mfma_f32_16x16x32_bf16 v[116:119], v[180:183], a[180:183], v[116:119]\n"
      "s_waitcnt lgkmcnt(14)\n"
      "ds_read2st64_b32 v[60:61], v246 offset0:44 offset1:45\n"
      "v_add_f32 v0, v0, v16\n"
      "v_mfma_f32_16x16x32_bf16 v[120:123], v[180:183], a[184:187], v[120:123]\n"
      "ds_read2st64_b32 v[62:63], v246 offset0:46 offset1:47\n"
      "v_add_f32 v1, v1, v17\n"
      "v_mfma_f32_16x16x32_bf16 v[124:127], v[180:183], a[188:191], v[124:127]\n"
      "s_waitcnt vmcnt(17)\n"
      "global_load_dwordx4 v[180:183], v243, s[10:11] offset:1024 sc1\n"
      "v_add_f32 v2, v2, v18\n"
      "v_mfma_f32_16x16x32_bf16 v[96:99], v[184:187], a[192:195], v[96:99]\n"
      "s_add_u32 m0, s41, 12288\n"
      "s_nop 0\n"
      "ds_write_addtid_b32 v72 offset:53248\n"
      "v_mfma_f32_16x16x32_bf16 v[100:103], v[184:187], a[196:199], v[100:103]\n"
      "ds_write_addtid_b32 v73 offset:53504\n"
      "v_add_f32 v3, v3, v19\n"
      "v_mfma_f32_16x16x32_bf16 v[104:107], v[184:187], a[200:203], v[104:107]\n"
      "ds_write_addtid_b32 v74 offset:53760\n"
      "v_add_f32 v4, v4, v20\n"
      "v_mfma_f32_16x16x32_bf16 v[108:111], v[184:187], a[204:207], v[108:111]\n"
      "ds_write_addtid_b32 v75 offset:54016\n"
      "v_add_f32 v5, v5, v21\n"
      "v_mfma_f32_16x16x32_bf16 v[112:115], v[184:187], a[208:211], v[112:115]\n"
      "ds_write_addtid_b32 v76 offset:54272\n"
      "v_add_f32 v6, v6, v22\n"
      "v_mfma_f32_16x16x32_bf16 v[116:119], v[184:187], a[212:215], v[116:119]\n"
      "ds_write_addtid_b32 v77 offset:54528\n"
      "v_add_f32 v7, v7, v23\n"
      "v_mfma_f32_16x16x32_bf16 v[120:123], v[184:187], a[216:219], v[120:123]\n"
      "s_waitcnt lgkmcnt(15)\n"
      "ds_write_addtid_b32 v78 offset:54784\n"
      "v_add_f32 v0, v0, v24\n"
      "v_mfma_f32_16x16x32_bf16 v[124:127], v[184:187], a[220:223], v[124:127]\n"
      "global_load_dwordx4 v[184:187], v241, s[14:15] offset:1024 sc1\n"
      "v_add_f32 v1, v1, v25\n"
      "v_mfma_f32_16x16x32_bf16 v[96:99], v[188:191], a[224:227], v[96:99]\n"
      "ds_write_addtid_b32 v79 offset:55040\n"
      "v_add_f32 v2, v2, v26\n"
      "v_mfma_f32_16x16x32_bf16 v[100:103], v[188:191], a[228:231], v[100:103]\n"
      "ds_write_addtid_b32 v80 offset:55296\n"
      "v_add_f32 v3, v3, v27\n"
      "v_mfma_f32_16x16x32_bf16 v[104:107], v[188:191], a[232:235], v[104:107]\n"
      "ds_write_addtid_b32 v81 offset:55552\n"
      "v_add_f32 v4, v4, v28\n"
      "v_mfma_f32_16x16x32_bf16 v[108:111], v[188:191], a[236:239], v[108:111]\n"
      "ds_write_addtid_b32 v82 offset:55808\n"
      "v_add_f32 v5, v5, v29\n"
      "v_mfma_f32_16x16x32_bf16 v[112:115], v[188:191], a[240:243], v[112:115]\n"
      "ds_write_addtid_b32 v83 offset:56064\n"
      "v_add_f32 v6, v6, v30\n"
      "v_mfma_f32_16x16x32_bf16 v[116:119], v[188:191], a[244:247], v[116:119]\n"
      "ds_write_addtid_b32 v84 offset:56320\n"
      "v_add_f32 v7, v7, v31\n"
      "v_mfma_f32_16x16x32_bf16 v[120:123], v[188:191], a[248:251], v[120:123]\n"
      "ds_write_addtid_b32 v85 offset:56576\n"
      "v_cvt_pk_bf16_f32 v8, v0, v1\n"
      "v_mfma_f32_16x16x32_bf16 v[124:127], v[188:191], a[252:255], v[124:127]\n"
      "s_waitcnt vmcnt(17)\n"
      "global_load_dwordx4 v[188:191], v243, s[14:15] offset:1024 sc1\n"
      "v_cvt_pk_bf16_f32 v9, v2, v3\n"
      "v_cvt_pk_bf16_f32 v10, v4, v5\n"
      "v_mov_b32 v11, s37\n"
      "v_cvt_pk_bf16_f32 v247, v6, v7\n"
      "global_store_dwordx4 v252, v[8:11], s[52:53] offset:0 sc1\n"
      "v_mfma_f32_16x16x32_bf16 v[0:3], v[128:131], a[0:3], 0\n"
      "s_waitcnt lgkmcnt(15)\n"
      "ds_write_addtid_b32 v86 offset:56832\n"
      "v_add_f32 v32, v32, v40\n"
      "v_mfma_f32_16x16x32_bf16 v[4:7], v[128:131], a[4:7], 0\n"
      "ds_write_addtid_b32 v87 offset:57088\n"
      "v_add_f32 v33, v33, v41\n"
      "v_mfma_f32_16x16x32_bf16 v[8:11], v[128:131], a[8:11], 0\n"
      "ds_write_addtid_b32 v88 offset:57344\n"
      "v_add_f32 v34, v34, v42\n"
      "v_mfma_f32_16x16x32_bf16 v[12:15], v[128:131], a[12:15], 0\n"
      "ds_write_addtid_b32 v89 offset:57600\n"
      "v_add_f32 v35, v35, v43\n"
      "v_mfma_f32_16x16x32_bf16 v[16:19], v[128:131], a[16:19], 0\n"
      "ds_write_addtid_b32 v90 offset:57856\n"
      "v_add_f32 v36, v36, v44\n"
      "v_mfma_f32_16x16x32_bf16 v[20:23], v[128:131], a[20:23], 0\n"
      "ds_write_addtid_b32 v91 offset:58112\n"
      "v_add_f32 v37, v37, v45\n"
      "v_mfma_f32_16x16x32_bf16 v[24:27], v[128:131], a[24:27], 0\n"
      "ds_write_addtid_b32 v92 offset:58368\n"
      "v_add_f32 v38, v38, v46\n"
      "v_mfma_f32_16x16x32_bf16 v[28:31], v[128:131], a[28:31], 0\n"
      "global_load_dwordx4 v[128:131], v240, s[10:11] offset:2048 sc1\n"
      "v_add_f32 v39, v39, v47\n"
      "v_mfma_f32_16x16x32_bf16 v[0:3], v[132:135], a[32:35], v[0:3]\n"
      "s_waitcnt vmcnt(9)\n"
      "v_bitop3_b32 v197, v205, v207, s37 bitop3:0x7e\n"
      "v_cmp_ne_u32 vcc, 0, v197\n"
      "s_nop 1\n"
      "s_cbranch_vccnz .Lp_s12_%=\n"
      ".Lp_o13_%=:\n"
      "v_mfma_f32_16x16x32_bf16 v[4:7], v[132:135], a[36:39], v[4:7]\n"
      "s_lshr_b32 s43, s42, 5\n"
      "s_lshl2_add_u32 s44, s36, s33\n"
      "s_cmp_eq_u32 s43, s44\n"
      "s_cbranch_scc0 .Lp_e14_%=\n"
      "v_lshlrev_b32 v192, 16, v232\n"
      "v_lshlrev_b32 v193, 16, v236\n"
      "v_and_b32 v194, 0xffff0000, v232\n"
      "v_and_b32 v195, 0xffff0000, v236\n"
      "v_mul_f32 v192, v192, v193\n"
      "v_mul_f32 v194, v194, v195\n"
      "v_cvt_pk_bf16_f32 v192, v192, v194\n"
      "v_lshlrev_b32 v193, 16, v192\n"
      "v_and_b32 v192, 0xffff0000, v192\n"
      "v_add_f32 v196, v192, v193\n"
      "v_lshlrev_b32 v192, 16, v233\n"
      "v_lshlrev_b32 v193, 16, v237\n"
      "v_and_b32 v194, 0xffff0000, v233\n"
      "v_and_b32 v195, 0xffff0000, v237\n"
      "v_mul_f32 v192, v192, v193\n"
      "v_mul_f32 v194, v194, v195\n"
      "v_cvt_pk_bf16_f32 v192, v192, v194\n"
      "v_lshlrev_b32 v193, 16, v192\n"
      "v_and_b32 v192, 0xffff0000, v192\n"
      "v_add_f32 v192, v192, v193\n"
      "v_add_f32 v196, v196, v192\n"
      "v_lshlrev_b32 v192, 16, v234\n"
      "v_lshlrev_b32 v193, 16, v238\n"
      "v_and_b32 v194, 0xffff0000, v234\n"
      "v_and_b32 v195, 0xffff0000, v238\n"
      "v_mul_f32 v192, v192, v193\n"
      "v_mul_f32 v194, v194, v195\n"
      "v_cvt_pk_bf16_f32 v192, v192, v194\n"
      "v_lshlrev_b32 v193, 16, v192\n"
      "v_and_b32 v192, 0xffff0000, v192\n"
      "v_add_f32 v192, v192, v193\n"
      "v_add_f32 v196, v196, v192\n"
      "v_lshlrev_b32 v192, 16, v235\n"
      "v_lshlrev_b32 v193, 16, v239\n"
      "v_and_b32 v194, 0xffff0000, v235\n"
      "v_and_b32 v195, 0xffff0000, v239\n"
      "v_mul_f32 v192, v192, v193\n"
      "v_mul_f32 v194, v194, v195\n"
      "v_cvt_pk_bf16_f32 v192, v192, v194\n"
      "v_lshlrev_b32 v193, 16, v192\n"
      "v_and_b32 v192, 0xffff0000, v192\n"
      "v_add_f32 v192, v192, v193\n"
      "v_add_f32 v196, v196, v192\n"
      "s_nop 1\n"
      "v_add_f32_dpp v196, v196, v196 quad_perm:[1,0,3,2] row_mask:0xf bank_mask:0xf\n"
      "s_nop 1\n"
      "v_add_f32_dpp v196, v196, v196 quad_perm:[2,3,0,1] row_mask:0xf bank_mask:0xf\n"
      "s_nop 1\n"
      "v_add_f32_dpp v196, v196, v196 row_half_mirror row_mask:0xf bank_mask:0xf\n"
      "s_nop 1\n"
      "v_add_f32_dpp v196, v196, v196 row_mirror row_mask:0xf bank_mask:0xf\n"
      "v_mov_b32 v192, v196\n"
      "s_nop 1\n"
      "v_permlane16_swap_b32 v196, v192\n"
      "v_add_f32 v196, v196, v192\n"
      "v_mov_b32 v192, v196\n"
      "s_nop 1\n"
      "v_permlane32_swap_b32 v196, v192\n"
      "v_add_f32 v196, v196, v192\n"
      "v_cvt_pk_bf16_f32 v196, v196, v196\n"
      "v_and_b32 v196, 0xffff0000, v196\n"
      "v_mul_f32 v196, 0xbfb8aa3b, v196\n"
      "v_exp_f32 v196, v196\n"
      "v_lshrrev_b32 v192, 10, v199\n"
      "v_add_f32 v196, 1.0, v196\n"
      "v_rcp_f32 v196, v196\n"
      "v_subrev_u32 v192, s42, v192\n"
      "v_cvt_pk_bf16_f32 v196, v196, v196\n"
      "v_and_b32 v196, 0xffff0000, v196\n"
      "v_cmp_eq_u32 vcc, 0, v192\n"
      "s_nop 1\n"
      "v_cndmask_b32 v204, v204, v196, vcc\n"
      "v_cmp_eq_u32 vcc, -16, v192\n"
      "s_nop 1\n"
      "v_cndmask_b32 v206, v206, v196, vcc\n"
      ".Lp_e14_%=:\n"
      "v_mfma_f32_16x16x32_bf16 v[8:11], v[132:135], a[40:43], v[8:11]\n"
      "s_lshl_b32 s54, s34, 30\n"
      "ds_write_addtid_b32 v93 offset:58624\n"
      "v_add_f32 v32, v32, v48\n"
      "v_mfma_f32_16x16x32_bf16 v[12:15], v[132:135], a[44:47], v[12:15]\n"
      "s_and_b32 s54, s54, 0x80000000\n"
      "ds_write_addtid_b32 v94 offset:58880\n"
      "v_add_f32 v33, v33, v49\n"
      "v_mfma_f32_16x16x32_bf16 v[16:19], v[132:135], a[48:51], v[16:19]\n"
      "ds_write_addtid_b32 v95 offset:59136\n"
      "v_add_f32 v34, v34, v50\n"
      "v_mfma_f32_16x16x32_bf16 v[20:23], v[132:135], a[52:55], v[20:23]\n"
      "s_add_u32 m0, s41, 12288\n"
      "s_nop 0\n"
      "ds_write_addtid_b32 v104 offset:59392\n"
      "v_mfma_f32_16x16x32_bf16 v[24:27], v[132:135], a[56:59], v[24:27]\n"
      "ds_write_addtid_b32 v105 offset:59648\n"
      "v_add_f32 v35, v35, v51\n"
      "v_mfma_f32_16x16x32_bf16 v[28:31], v[132:135], a[60:63], v[28:31]\n"
      "global_load_dwordx4 v[132:135], v242, s[10:11] offset:2048 sc1\n"
      "v_add_f32 v36, v36, v52\n"
      "v_mfma_f32_16x16x32_bf16 v[0:3], v[136:139], a[64:67], v[0:3]\n"
      "ds_write_addtid_b32 v106 offset:59904\n"
      "v_add_f32 v37, v37, v53\n"
      "v_mfma_f32_16x16x32_bf16 v[4:7], v[136:139], a[68:71], v[4:7]\n"
      "ds_write_addtid_b32 v107 offset:60160\n"
      "v_add_f32 v38, v38, v54\n"
      "v_mfma_f32_16x16x32_bf16 v[8:11], v[136:139], a[72:75], v[8:11]\n"
      "ds_write_addtid_b32 v108 offset:60416\n"
      "v_add_f32 v39, v39, v55\n"
      "v_mfma_f32_16x16x32_bf16 v[12:15], v[136:139], a[76:79], v[12:15]\n"
      "s_waitcnt lgkmcnt(15)\n"
      "ds_write_addtid_b32 v109 offset:60672\n"
      "v_add_f32 v32, v32, v56\n"
      "v_mfma_f32_16x16x32_bf16 v[16:19], v[136:139], a[80:83], v[16:19]\n"
      "ds_write_addtid_b32 v110 offset:60928\n"
      "v_add_f32 v33, v33, v57\n"
      "v_mfma_f32_16x16x32_bf16 v[20:23], v[136:139], a[84:87], v[20:23]\n"
      "ds_write_addtid_b32 v111 offset:61184\n"
      "v_add_f32 v34, v34, v58\n"
      "v_mfma_f32_16x16x32_bf16 v[24:27], v[136:139], a[88:91], v[24:27]\n"
      "ds_write_addtid_b32 v112 offset:61440\n"
      "v_add_f32 v35, v35, v59\n"
      "v_mfma_f32_16x16x32_bf16 v[28:31], v[136:139], a[92:95], v[28:31]\n"
      "global_load_dwordx4 v[136:139], v240, s[14:15] offset:2048 sc1\n"
      "v_add_f32 v36, v36, v60\n"
      "v_mfma_f32_16x16x32_bf16 v[0:3], v[140:143], a[96:99], v[0:3]\n"
      "ds_write_addtid_b32 v113 offset:61696\n"
      "v_add_f32 v37, v37, v61\n"
      "v_mfma_f32_16x16x32_bf16 v[4:7], v[140:143], a[100:103], v[4:7]\n"
      "ds_write_addtid_b32 v114 offset:61952\n"
      "v_add_f32 v38, v38, v62\n"
      "v_mfma_f32_16x16x32_bf16 v[8:11], v[140:143], a[104:107], v[8:11]\n"
      "ds_write_addtid_b32 v115 offset:62208\n"
      "v_add_f32 v39, v39, v63\n"
      "v_mfma_f32_16x16x32_bf16 v[12:15], v[140:143], a[108:111], v[12:15]\n"
      "ds_write_addtid_b32 v116 offset:62464\n"
      "v_mov_b32 v44, v247\n"
      "v_mfma_f32_16x16x32_bf16 v[16:19], v[140:143], a[112:115], v[16:19]\n"
      "ds_write_addtid_b32 v117 offset:62720\n"
      "v_mov_b32 v47, s37\n"
      "v_mfma_f32_16x16x32_bf16 v[20:23], v[140:143], a[116:119], v[20:23]\n"
      "ds_write_addtid_b32 v118 offset:62976\n"
      "v_cvt_pk_bf16_f32 v40, v32, v33\n"
      "v_mfma_f32_16x16x32_bf16 v[24:27], v[140:143], a[120:123], v[24:27]\n"
      "ds_write_addtid_b32 v119 offset:63232\n"
      "v_cvt_pk_bf16_f32 v41, v34, v35\n"
      "v_mfma_f32_16x16x32_bf16 v[28:31], v[140:143], a[124:127], v[28:31]\n"
      "global_load_dwordx4 v[140:143], v242, s[14:15] offset:2048 sc1\n"
      "v_cvt_pk_bf16_f32 v42, v36, v37\n"
      "v_mfma_f32_16x16x32_bf16 v[0:3], v[144:147], a[128:131], v[0:3]\n"
      "ds_write_addtid_b32 v120 offset:63488\n"
      "v_mov_b32 v43, s37\n"
      "v_mfma_f32_16x16x32_bf16 v[4:7], v[144:147], a[132:135], v[4:7]\n"
      "ds_write_addtid_b32 v121 offset:63744\n"
      "v_cvt_pk_bf16_f32 v45, v38, v39\n"
      "v_mfma_f32_16x16x32_bf16 v[8:11], v[144:147], a[136:139], v[8:11]\n"
      "global_store_dwordx4 v252, v[40:43], s[52:53] offset:1024 sc1\n"
      "v_sub_f32 v205, 1.0, v204\n"
      "v_mfma_f32_16x16x32_bf16 v[12:15], v[144:147], a[140:143], v[12:15]\n"
      "global_store_dwordx4 v252, v[44:47], s[52:53] offset:2048 sc1\n"
      "s_add_u32 s48, s34, 2\n"
      "v_mfma_f32_16x16x32_bf16 v[16:19], v[144:147], a[144:147], v[16:19]\n"
      "s_and_b32 s48, s48, 3\n"
      "ds_write_addtid_b32 v122 offset:64000\n"
      "s_mul_i32 s48, s48, 36864\n"
      "v_mfma_f32_16x16x32_bf16 v[20:23], v[144:147], a[148:151], v[20:23]\n"
      "s_add_u32 s48, s48, 12288\n"
      "ds_write_addtid_b32 v123 offset:64256\n"
      "s_add_u32 s52, s60, s48\n"
      "s_addc_u32 s53, s61, 0\n"
      "v_mfma_f32_16x16x32_bf16 v[24:27], v[144:147], a[152:155], v[24:27]\n"
      "ds_write_addtid_b32 v124 offset:64512\n"
      "v_cvt_pk_bf16_f32 v205, v205, v205\n"
      "v_mfma_f32_16x16x32_bf16 v[28:31], v[144:147], a[156:159], v[28:31]\n"
      "global_load_dwordx4 v[144:147], v241, s[10:11] offset:2048 sc1\n"
      "v_xor_b32 v204, s54, v204\n"
      "v_mfma_f32_16x16x32_bf16 v[0:3], v[148:151], a[160:163], v[0:3]\n"
      "ds_write_addtid_b32 v125 offset:64768\n"
      "v_and_b32 v205, 0xffff0000, v205\n"
      "v_mfma_f32_16x16x32_bf16 v[4:7], v[148:151], a[164:167], v[4:7]\n"
      "ds_write_addtid_b32 v126 offset:65024\n"
      "v_lshlrev_b32 v208, 16, v224\n"
      "v_mfma_f32_16x16x32_bf16 v[8:11], v[148:151], a[168:171], v[8:11]\n"
      "ds_write_addtid_b32 v127 offset:65280\n"
      "v_and_b32 v209, 0xffff0000, v224\n"
      "v_mfma_f32_16x16x32_bf16 v[12:15], v[148:151], a[172:175], v[12:15]\n"
      "v_mul_f32 v208, v205, v208\n"
      "v_mul_f32 v209, v205, v209\n"
      "v_mfma_f32_16x16x32_bf16 v[16:19], v[148:151], a[176:179], v[16:19]\n"
      "v_lshlrev_b32 v210, 16, v225\n"
      "v_and_b32 v211, 0xffff0000, v225\n"
      "v_mfma_f32_16x16x32_bf16 v[20:23], v[148:151], a[180:183], v[20:23]\n"
      "s_waitcnt lgkmcnt(0)\n"
      "s_barrier\n"
      "v_mfma_f32_16x16x32_bf16 v[24:27], v[148:151], a[184:187], v[24:27]\n"
      "ds_read2st64_b32 v[72:73], v248 offset0:0 offset1:1\n"
      "v_mul_f32 v210, v205, v210\n"
      "v_mfma_f32_16x16x32_bf16 v[28:31], v[148:151], a[188:191], v[28:31]\n"
      "global_load_dwordx4 v[148:151], v243, s[10:11] offset:2048 sc1\n"
      "v_mul_f32 v211, v205, v211\n"
      "v_mfma_f32_16x16x32_bf16 v[0:3], v[152:155], a[192:195], v[0:3]\n"
      "ds_read2st64_b32 v[74:75], v248 offset0:2 offset1:3\n"
      "v_lshlrev_b32 v216, 16, v226\n"
      "v_mfma_f32_16x16x32_bf16 v[4:7], v[152:155], a[196:199], v[4:7]\n"
      "ds_read2st64_b32 v[76:77], v248 offset0:4 offset1:5\n"
      "v_and_b32 v217, 0xffff0000, v226\n"
      "v_mfma_f32_16x16x32_bf16 v[8:11], v[152:155], a[200:203], v[8:11]\n"
      "ds_read2st64_b32 v[78:79], v248 offset0:6 offset1:7\n"
      "v_mul_f32 v216, v205, v216\n"
      "v_mfma_f32_16x16x32_bf16 v[12:15], v[152:155], a[204:207], v[12:15]\n"
      "ds_read2st64_b32 v[80:81], v249 offset0:8 offset1:9\n"
      "v_mul_f32 v217, v205, v217\n"
      "v_mfma_f32_16x16x32_bf16 v[16:19], v[152:155], a[208:211], v[16:19]\n"
      "ds_read2st64_b32 v[82:83], v249 offset0:10 offset1:11\n"
      "v_lshlrev_b32 v218, 16, v227\n"
      "v_mfma_f32_16x16x32_bf16 v[20:23], v[152:155], a[212:215], v[20:23]\n"
      "ds_read2st64_b32 v[84:85], v249 offset0:12 offset1:13\n"
      "v_and_b32 v219, 0xffff0000, v227\n"
      "v_mfma_f32_16x16x32_bf16 v[24:27], v[152:155], a[216:219], v[24:27]\n"
      "ds_read2st64_b32 v[86:87], v249 offset0:14 offset1:15\n"
      "v_mul_f32 v218, v205, v218\n"
      "v_mfma_f32_16x16x32_bf16 v[28:31], v[152:155], a[220:223], v[28:31]\n"
      "global_load_dwordx4 v[152:155], v241, s[14:15] offset:2048 sc1\n"
      "v_mul_f32 v219, v205, v219\n"
      "v_mfma_f32_16x16x32_bf16 v[0:3], v[156:159], a[224:227], v[0:3]\n"
      "ds_read2st64_b32 v[88:89], v250 offset0:16 offset1:17\n"
      "v_sub_f32 v207, 1.0, v206\n"
      "v_mfma_f32_16x16x32_bf16 v[4:7], v[156:159], a[228:231], v[4:7]\n"
      "s_waitcnt lgkmcnt(5)\n"
      "ds_read2st64_b32 v[90:91], v250 offset0:18 offset1:19\n"
      "v_add_f32 v64, v64, v72\n"
      "v_mfma_f32_16x16x32_bf16 v[8:11], v[156:159], a[232:235], v[8:11]\n"
      "ds_read2st64_b32 v[92:93], v250 offset0:20 offset1:21\n"
      "v_add_f32 v65, v65, v73\n"
      "v_mfma_f32_16x16x32_bf16 v[12:15], v[156:159], a[236:239], v[12:15]\n"
      "ds_read2st64_b32 v[94:95], v250 offset0:22 offset1:23\n"
      "v_add_f32 v66, v66, v74\n"
      "v_mfma_f32_16x16x32_bf16 v[16:19], v[156:159], a[240:243], v[16:19]\n"
      "ds_read2st64_b32 v[104:105], v248 offset0:24 offset1:25\n"
      "v_add_f32 v67, v67, v75\n"
      "v_mfma_f32_16x16x32_bf16 v[20:23], v[156:159], a[244:247], v[20:23]\n"
      "ds_read2st64_b32 v[106:107], v248 offset0:26 offset1:27\n"
      "v_add_f32 v68, v68, v76\n"
      "v_mfma_f32_16x16x32_bf16 v[24:27], v[156:159], a[248:251], v[24:27]\n"
      "ds_read2st64_b32 v[108:109], v248 offset0:28 offset1:29\n"
      "v_add_f32 v69, v69, v77\n"
      "v_mfma_f32_16x16x32_bf16 v[28:31], v[156:159], a[252:255], v[28:31]\n"
      "s_waitcnt vmcnt(16)\n"
      "global_load_dwordx4 v[156:159], v243, s[14:15] offset:2048 sc1\n"
      "v_add_f32 v70, v70, v78\n"
      "v_mfma_f32_16x16x32_bf16 v[32:35], v[160:163], a[0:3], 0\n"
      "ds_read2st64_b32 v[110:111], v248 offset0:30 offset1:31\n"
      "v_add_f32 v71, v71, v79\n"
      "v_mfma_f32_16x16x32_bf16 v[36:39], v[160:163], a[4:7], 0\n"
      "s_waitcnt lgkmcnt(8)\n"
      "ds_read2st64_b32 v[112:113], v249 offset0:32 offset1:33\n"
      "v_add_f32 v64, v64, v80\n"
      "v_mfma_f32_16x16x32_bf16 v[40:43], v[160:163], a[8:11], 0\n"
      "ds_read2st64_b32 v[114:115], v249 offset0:34 offset1:35\n"
      "v_add_f32 v65, v65, v81\n"
      "v_mfma_f32_16x16x32_bf16 v[44:47], v[160:163], a[12:15], 0\n"
      "ds_read2st64_b32 v[116:117], v249 offset0:36 offset1:37\n"
      "v_add_f32 v66, v66, v82\n"
      "v_mfma_f32_16x16x32_bf16 v[48:51], v[160:163], a[16:19], 0\n"
      "ds_read2st64_b32 v[118:119], v249 offset0:38 offset1:39\n"
      "v_add_f32 v67, v67, v83\n"
      "v_mfma_f32_16x16x32_bf16 v[52:55], v[160:163], a[20:23], 0\n"
      "ds_read2st64_b32 v[120:121], v250 offset0:40 offset1:41\n"
      "v_add_f32 v68, v68, v84\n"
      "v_mfma_f32_16x16x32_bf16 v[56:59], v[160:163], a[24:27], 0\n"
      "ds_read2st64_b32 v[122:123], v250 offset0:42 offset1:43\n"
      "v_add_f32 v69, v69, v85\n"
      "v_mfma_f32_16x16x32_bf16 v[60:63], v[160:163], a[28:31], 0\n"
      "global_load_dwordx4 v[160:163], v240, s[10:11] offset:3072 sc1\n"
      "v_add_f32 v70, v70, v86\n"
      "v_mfma_f32_16x16x32_bf16 v[32:35], v[164:167], a[32:35], v[32:35]\n"
      "ds_read2st64_b32 v[124:125], v250 offset0:44 offset1:45\n"
      "v_add_f32 v71, v71, v87\n"
      "v_mfma_f32_16x16x32_bf16 v[36:39], v[164:167], a[36:39], v[36:39]\n"
      "s_waitcnt lgkmcnt(11)\n"
      "ds_read2st64_b32 v[126:127], v250 offset0:46 offset1:47\n"
      "v_add_f32 v64, v64, v88\n"
      "v_mfma_f32_16x16x32_bf16 v[40:43], v[164:167], a[40:43], v[40:43]\n"
      "s_add_u32 m0, s41, 0\n"
      "s_nop 0\n"
      "ds_write_addtid_b32 v8 offset:0\n"
      "v_mfma_f32_16x16x32_bf16 v[44:47], v[164:167], a[44:47], v[44:47]\n"
      "ds_write_addtid_b32 v9 offset:256\n"
      "ds_write_addtid_b32 v10 offset:512\n"
      "v_add_f32 v65, v65, v89\n"
      "v_mfma_f32_16x16x32_bf16 v[48:51], v[164:167], a[48:51], v[48:51]\n"
      "ds_write_addtid_b32 v11 offset:768\n"
      "ds_write_addtid_b32 v12 offset:1024\n"
      "v_add_f32 v66, v66, v90\n"
      "v_mfma_f32_16x16x32_bf16 v[52:55], v[164:167], a[52:55], v[52:55]\n"
      "ds_write_addtid_b32 v13 offset:1280\n"
      "ds_write_addtid_b32 v14 offset:1536\n"
      "v_add_f32 v67, v67, v91\n"
      "v_mfma_f32_16x16x32_bf16 v[56:59], v[164:167], a[56:59], v[56:59]\n"
      "ds_write_addtid_b32 v15 offset:1792\n"
      "ds_write_addtid_b32 v16 offset:2048\n"
      "v_add_f32 v68, v68, v92\n"
      "v_mfma_f32_16x16x32_bf16 v[60:63], v[164:167], a[60:63], v[60:63]\n"
      "s_waitcnt vmcnt(16)\n"
      "global_load_dwordx4 v[164:167], v242, s[10:11] offset:3072 sc1\n"
      "ds_write_addtid_b32 v17 offset:2304\n"
      "v_add_f32 v69, v69, v93\n"
      "v_mfma_f32_16x16x32_bf16 v[32:35], v[168:171], a[64:67], v[32:35]\n"
      "ds_write_addtid_b32 v18 offset:2560\n"
      "ds_write_addtid_b32 v19 offset:2816\n"
      "v_add_f32 v70, v70, v94\n"
      "v_mfma_f32_16x16x32_bf16 v[36:39], v[168:171], a[68:71], v[36:39]\n"
      "ds_write_addtid_b32 v20 offset:3072\n"
      "ds_write_addtid_b32 v21 offset:3328\n"
      "v_add_f32 v71, v71, v95\n"
      "v_mfma_f32_16x16x32_bf16 v[40:43], v[168:171], a[72:75], v[40:43]\n"
      "ds_write_addtid_b32 v22 offset:3584\n"
      "ds_write_addtid_b32 v23 offset:3840\n"
      "v_cvt_pk_bf16_f32 v72, v64, v65\n"
      "v_mfma_f32_16x16x32_bf16 v[44:47], v[168:171], a[76:79], v[44:47]\n"
      "ds_write_addtid_b32 v24 offset:4096\n"
      "ds_write_addtid_b32 v25 offset:4352\n"
      "v_cvt_pk_bf16_f32 v73, v66, v67\n"
      "v_mfma_f32_16x16x32_bf16 v[48:51], v[168:171], a[80:83], v[48:51]\n"
      "ds_write_addtid_b32 v26 offset:4608\n"
      "ds_write_addtid_b32 v27 offset:4864\n"
      "v_cvt_pk_bf16_f32 v74, v68, v69\n"
      "v_mfma_f32_16x16x32_bf16 v[52:55], v[168:171], a[84:87], v[52:55]\n"
      "ds_write_addtid_b32 v28 offset:5120\n"
      "ds_write_addtid_b32 v29 offset:5376\n"
      "v_mov_b32 v75, s37\n"
      "v_mfma_f32_16x16x32_bf16 v[56:59], v[168:171], a[88:91], v[56:59]\n"
      "ds_write_addtid_b32 v30 offset:5632\n"
      "ds_write_addtid_b32 v31 offset:5888\n"
      "v_cvt_pk_bf16_f32 v247, v70, v71\n"
      "v_mfma_f32_16x16x32_bf16 v[60:63], v[168:171], a[92:95], v[60:63]\n"
      "global_load_dwordx4 v[168:171], v240, s[14:15] offset:3072 sc1\n"
      "global_store_dwordx4 v252, v[72:75], s[52:53] offset:0 sc1\n"
      "s_waitcnt lgkmcnt(15)\n"
      "v_mfma_f32_16x16x32_bf16 v[32:35], v[172:175], a[96:99], v[32:35]\n"
      "v_add_f32 v96, v96, v104\n"
      "v_add_f32 v97, v97, v105\n"
      "v_add_f32 v98, v98, v106\n"
      "v_mfma_f32_16x16x32_bf16 v[36:39], v[172:175], a[100:103], v[36:39]\n"
      "s_waitcnt lgkmcnt(0)\n"
      "s_barrier\n"
      "v_mfma_f32_16x16x32_bf16 v[40:43], v[172:175], a[104:107], v[40:43]\n"
      "ds_read2st64_b32 v[8:9], v244 offset0:0 offset1:1\n"
      "ds_read2st64_b32 v[10:11], v244 offset0:2 offset1:3\n"
      "v_add_f32 v99, v99, v107\n"
      "v_mfma_f32_16x16x32_bf16 v[44:47], v[172:175], a[108:111], v[44:47]\n"
      "ds_read2st64_b32 v[12:13], v244 offset0:4 offset1:5\n"
      "ds_read2st64_b32 v[14:15], v244 offset0:6 offset1:7\n"
      "v_add_f32 v100, v100, v108\n"
      "v_mfma_f32_16x16x32_bf16 v[48:51], v[172:175], a[112:115], v[48:51]\n"
      "ds_read2st64_b32 v[16:17], v245 offset0:8 offset1:9\n"
      "ds_read2st64_b32 v[18:19], v245 offset0:10 offset1:11\n"
      "v_add_f32 v101, v101, v109\n"
      "v_mfma_f32_16x16x32_bf16 v[52:55], v[172:175], a[116:119], v[52:55]\n"
      "ds_read2st64_b32 v[20:21], v245 offset0:12 offset1:13\n"
      "ds_read2st64_b32 v[22:23], v245 offset0:14 offset1:15\n"
      "v_add_f32 v102, v102, v110\n"
      "v_mfma_f32_16x16x32_bf16 v[56:59], v[172:175], a[120:123], v[56:59]\n"
      "ds_read2st64_b32 v[24:25], v246 offset0:16 offset1:17\n"
      "ds_read2st64_b32 v[26:27], v246 offset0:18 offset1:19\n"
      "v_add_f32 v103, v103, v111\n"
      "v_mfma_f32_16x16x32_bf16 v[60:63], v[172:175], a[124:127], v[60:63]\n"
      "s_waitcnt vmcnt(17)\n"
      "global_load_dwordx4 v[172:175], v242, s[14:15] offset:3072 sc1\n"
      "ds_read2st64_b32 v[28:29], v246 offset0:20 offset1:21\n"
      "v_add_f32 v96, v96, v112\n"
      "v_mfma_f32_16x16x32_bf16 v[40:43], v[176:179], a[136:139], v[40:43]\n"
      "ds_read2st64_b32 v[30:31], v246 offset0:22 offset1:23\n"
      "v_add_f32 v97, v97, v113\n"
      "v_add_f32 v98, v98, v114\n"
      "v_mfma_f32_16x16x32_bf16 v[44:47], v[176:179], a[140:143], v[44:47]\n"
      "v_add_f32 v99, v99, v115\n"
      "v_add_f32 v100, v100, v116\n"
      "v_add_f32 v101, v101, v117\n"
      "v_mfma_f32_16x16x32_bf16 v[48:51], v[176:179], a[144:147], v[48:51]\n"
      "v_add_f32 v102, v102, v118\n"
      "v_add_f32 v103, v103, v119\n"
      "v_add_f32 v96, v96, v120\n"
      "v_mfma_f32_16x16x32_bf16 v[52:55], v[176:179], a[148:151], v[52:55]\n"
      "v_add_f32 v97, v97, v121\n"
      "v_add_f32 v98, v98, v122\n"
      "v_add_f32 v99, v99, v123\n"
      "v_mfma_f32_16x16x32_bf16 v[56:59], v[176:179], a[152:155], v[56:59]\n"
      "v_add_f32 v100, v100, v124\n"
      "v_add_f32 v101, v101, v125\n"
      "v_add_f32 v102, v102, v126\n"
      "v_mfma_f32_16x16x32_bf16 v[60:63], v[176:179], a[156:159], v[60:63]\n"
      "v_add_f32 v103, v103, v127\n"
      "v_mov_b32 v108, v247\n"
      "v_mov_b32 v111, s37\n"
      "v_mfma_f32_16x16x32_bf16 v[40:43], v[180:183], a[168:171], v[40:43]\n"
      "v_cvt_pk_bf16_f32 v104, v96, v97\n"
      "v_cvt_pk_bf16_f32 v105, v98, v99\n"
      "v_cvt_pk_bf16_f32 v106, v100, v101\n"
      "v_mfma_f32_16x16x32_bf16 v[44:47], v[180:183], a[172:175], v[44:47]\n"
      "v_mov_b32 v107, s37\n"
      "v_cvt_pk_bf16_f32 v109, v102, v103\n"
      "v_cvt_pk_bf16_f32 v207, v207, v207\n"
      "v_mfma_f32_16x16x32_bf16 v[48:51], v[180:183], a[176:179], v[48:51]\n"
      "global_store_dwordx4 v252, v[104:107], s[52:53] offset:1024 sc1\n"
      "global_store_dwordx4 v252, v[108:111], s[52:53] offset:2048 sc1\n"
      "s_add_u32 s48, s34, 3\n"
      "v_mfma_f32_16x16x32_bf16 v[52:55], v[180:183], a[180:183], v[52:55]\n"
      "s_and_b32 s48, s48, 3\n"
      "s_mul_i32 s48, s48, 36864\n"
      "s_add_u32 s48, s48, 24576\n"
      "s_add_u32 s52, s60, s48\n"
      "s_addc_u32 s53, s61, 0\n"
      "v_mfma_f32_16x16x32_bf16 v[56:59], v[180:183], a[184:187], v[56:59]\n"
      "s_waitcnt lgkmcnt(8)\n"
      "v_add_f32 v0, v0, v8\n"
      "v_add_f32 v1, v1, v9\n"
      "v_add_f32 v2, v2, v10\n"
      "v_mfma_f32_16x16x32_bf16 v[60:63], v[180:183], a[188:191], v[60:63]\n"
      "s_waitcnt vmcnt(18)\n"
      "v_add_f32 v3, v3, v11\n"
      "v_add_f32 v4, v4, v12\n"
      "v_add_f32 v5, v5, v13\n"
      "v_mfma_f32_16x16x32_bf16 v[40:43], v[184:187], a[200:203], v[40:43]\n"
      "v_add_f32 v6, v6, v14\n"
      "v_add_f32 v7, v7, v15\n"
      "s_waitcnt lgkmcnt(4)\n"
      "v_mfma_f32_16x16x32_bf16 v[44:47], v[184:187], a[204:207], v[44:47]\n"
      "v_add_f32 v0, v0, v16\n"
      "v_add_f32 v1, v1, v17\n"
      "v_add_f32 v2, v2, v18\n"
      "v_mfma_f32_16x16x32_bf16 v[48:51], v[184:187], a[208:211], v[48:51]\n"
      "v_add_f32 v3, v3, v19\n"
      "v_add_f32 v4, v4, v20\n"
      "v_add_f32 v5, v5, v21\n"
      "v_mfma_f32_16x16x32_bf16 v[52:55], v[184:187], a[212:215], v[52:55]\n"
      "v_add_f32 v6, v6, v22\n"
      "v_add_f32 v7, v7, v23\n"
      "s_waitcnt lgkmcnt(0)\n"
      "v_mfma_f32_16x16x32_bf16 v[56:59], v[184:187], a[216:219], v[56:59]\n"
      "v_add_f32 v0, v0, v24\n"
      "v_add_f32 v1, v1, v25\n"
      "v_add_f32 v2, v2, v26\n"
      "v_mfma_f32_16x16x32_bf16 v[60:63], v[184:187], a[220:223], v[60:63]\n"
      "v_add_f32 v3, v3, v27\n"
      "v_add_f32 v4, v4, v28\n"
      "v_add_f32 v5, v5, v29\n"
      "v_mfma_f32_16x16x32_bf16 v[40:43], v[188:191], a[232:235], v[40:43]\n"
      "v_add_f32 v6, v6, v30\n"
      "v_add_f32 v7, v7, v31\n"
      "v_cvt_pk_bf16_f32 v8, v0, v1\n"
      "v_mfma_f32_16x16x32_bf16 v[44:47], v[188:191], a[236:239], v[44:47]\n"
      "v_cvt_pk_bf16_f32 v9, v2, v3\n"
      "v_cvt_pk_bf16_f32 v10, v4, v5\n"
      "v_mov_b32 v11, s37\n"
      "v_mfma_f32_16x16x32_bf16 v[48:51], v[188:191], a[240:243], v[48:51]\n"
      "v_cvt_pk_bf16_f32 v247, v6, v7\n"
      "v_xor_b32 v206, s54, v206\n"
      "v_and_b32 v207, 0xffff0000, v207\n"
      "v_mfma_f32_16x16x32_bf16 v[52:55], v[188:191], a[244:247], v[52:55]\n"
      "global_store_dwordx4 v252, v[8:11], s[52:53] offset:0 sc1\n"
      "v_lshlrev_b32 v212, 16, v228\n"
      "v_and_b32 v213, 0xffff0000, v228\n"
      "v_mfma_f32_16x16x32_bf16 v[56:59], v[188:191], a[248:251], v[56:59]\n"
      "v_mul_f32 v212, v207, v212\n"
      "v_mul_f32 v213, v207, v213\n"
      "v_lshlrev_b32 v214, 16, v229\n"
      "v_mfma_f32_16x16x32_bf16 v[60:63], v[188:191], a[252:255], v[60:63]\n"
      "v_and_b32 v215, 0xffff0000, v229\n"
      "v_mul_f32 v214, v207, v214\n"
      "v_mul_f32 v215, v207, v215\n"
      "v_mfma_f32_16x16x32_bf16 v[32:35], v[176:179], a[128:131], v[32:35]\n"
      "v_lshlrev_b32 v220, 16, v230\n"
      "v_and_b32 v221, 0xffff0000, v230\n"
      "v_mul_f32 v220, v207, v220\n"
      "v_mfma_f32_16x16x32_bf16 v[36:39], v[176:179], a[132:135], v[36:39]\n"
      "s_add_u32 m0, s41, 0\n"
      "global_load_dwordx4 v[176:179], v241, s[10:11] offset:3072 sc1\n"
      "ds_write_addtid_b32 v40 offset:6144\n"
      "v_mul_f32 v221, v207, v221\n"
      "v_mfma_f32_16x16x32_bf16 v[32:35], v[180:183], a[160:163], v[32:35]\n"
      "ds_write_addtid_b32 v41 offset:6400\n"
      "ds_write_addtid_b32 v42 offset:6656\n"
      "v_lshlrev_b32 v222, 16, v231\n"
      "v_mfma_f32_16x16x32_bf16 v[36:39], v[180:183], a[164:167], v[36:39]\n"
      "global_load_dwordx4 v[180:183], v243, s[10:11] offset:3072 sc1\n"
      "ds_write_addtid_b32 v43 offset:6912\n"
      "v_and_b32 v223, 0xffff0000, v231\n"
      "v_mfma_f32_16x16x32_bf16 v[32:35], v[184:187], a[192:195], v[32:35]\n"
      "ds_write_addtid_b32 v44 offset:7168\n"
      "ds_write_addtid_b32 v45 offset:7424\n"
      "v_mul_f32 v222, v207, v222\n"
      "v_mfma_f32_16x16x32_bf16 v[36:39], v[184:187], a[196:199], v[36:39]\n"
      "global_load_dwordx4 v[184:187], v241, s[14:15] offset:3072 sc1\n"
      "ds_write_addtid_b32 v46 offset:7680\n"
      "v_mul_f32 v223, v207, v223\n"
      "v_mfma_f32_16x16x32_bf16 v[32:35], v[188:191], a[224:227], v[32:35]\n"
      "ds_write_addtid_b32 v47 offset:7936\n"
      "ds_write_addtid_b32 v48 offset:8192\n"
      "v_add_u32 v253, 0, v252\n"
      "v_mfma_f32_16x16x32_bf16 v[36:39], v[188:191], a[228:231], v[36:39]\n"
      "s_waitcnt vmcnt(19)\n"
      "global_load_dwordx4 v[188:191], v243, s[14:15] offset:3072 sc1\n"
      "global_load_dwordx4 v[224:227], v253, s[62:63] offset:0 sc1\n"
      "v_mfma_f32_16x16x32_bf16 v[72:75], v[128:131], a[8:11], 0\n"
      "global_load_dwordx4 v[228:231], v253, s[62:63] offset:1024 sc1\n"
      "global_load_dwordx4 v[232:235], v253, s[62:63] offset:2048 sc1\n"
      "v_mfma_f32_16x16x32_bf16 v[76:79], v[128:131], a[12:15], 0\n"
      "ds_write_addtid_b32 v49 offset:8448\n"
      "ds_write_addtid_b32 v50 offset:8704\n"
      "v_mfma_f32_16x16x32_bf16 v[80:83], v[128:131], a[16:19], 0\n"
      "ds_write_addtid_b32 v51 offset:8960\n"
      "ds_write_addtid_b32 v52 offset:9216\n"
      "v_mfma_f32_16x16x32_bf16 v[84:87], v[128:131], a[20:23], 0\n"
      "ds_write_addtid_b32 v53 offset:9472\n"
      "ds_write_addtid_b32 v54 offset:9728\n"
      "v_mfma_f32_16x16x32_bf16 v[88:91], v[128:131], a[24:27], 0\n"
      "ds_write_addtid_b32 v55 offset:9984\n"
      "ds_write_addtid_b32 v56 offset:10240\n"
      "v_mfma_f32_16x16x32_bf16 v[92:95], v[128:131], a[28:31], 0\n"
      "ds_write_addtid_b32 v57 offset:10496\n"
      "ds_write_addtid_b32 v58 offset:10752\n"
      "v_mfma_f32_16x16x32_bf16 v[72:75], v[132:135], a[40:43], v[72:75]\n"
      "ds_write_addtid_b32 v59 offset:11008\n"
      "ds_write_addtid_b32 v60 offset:11264\n"
      "v_mfma_f32_16x16x32_bf16 v[76:79], v[132:135], a[44:47], v[76:79]\n"
      "ds_write_addtid_b32 v61 offset:11520\n"
      "ds_write_addtid_b32 v62 offset:11776\n"
      "v_mfma_f32_16x16x32_bf16 v[80:83], v[132:135], a[48:51], v[80:83]\n"
      "ds_write_addtid_b32 v63 offset:12032\n"
      "v_mfma_f32_16x16x32_bf16 v[84:87], v[132:135], a[52:55], v[84:87]\n"
      "v_mfma_f32_16x16x32_bf16 v[88:91], v[132:135], a[56:59], v[88:91]\n"
      "v_mfma_f32_16x16x32_bf16 v[92:95], v[132:135], a[60:63], v[92:95]\n"
      "s_waitcnt vmcnt(21)\n"
      "s_waitcnt lgkmcnt(0)\n"
      "s_barrier\n"
      "v_mfma_f32_16x16x32_bf16 v[72:75], v[136:139], a[72:75], v[72:75]\n"
      "ds_read2st64_b32 v[40:41], v244 offset0:24 offset1:25\n"
      "ds_read2st64_b32 v[42:43], v244 offset0:26 offset1:27\n"
      "v_mfma_f32_16x16x32_bf16 v[76:79], v[136:139], a[76:79], v[76:79]\n"
      "ds_read2st64_b32 v[44:45], v244 offset0:28 offset1:29\n"
      "ds_read2st64_b32 v[46:47], v244 offset0:30 offset1:31\n"
      "v_mfma_f32_16x16x32_bf16 v[80:83], v[136:139], a[80:83], v[80:83]\n"
      "ds_read2st64_b32 v[48:49], v245 offset0:32 offset1:33\n"
      "ds_read2st64_b32 v[50:51], v245 offset0:34 offset1:35\n"
      "v_mfma_f32_16x16x32_bf16 v[84:87], v[136:139], a[84:87], v[84:87]\n"
      "ds_read2st64_b32 v[52:53], v245 offset0:36 offset1:37\n"
      "ds_read2st64_b32 v[54:55], v245 offset0:38 offset1:39\n"
      "v_mfma_f32_16x16x32_bf16 v[88:91], v[136:139], a[88:91], v[88:91]\n"
      "ds_read2st64_b32 v[56:57], v246 offset0:40 offset1:41\n"
      "ds_read2st64_b32 v[58:59], v246 offset0:42 offset1:43\n"
      "v_mfma_f32_16x16x32_bf16 v[92:95], v[136:139], a[92:95], v[92:95]\n"
      "ds_read2st64_b32 v[60:61], v246 offset0:44 offset1:45\n"
      "ds_read2st64_b32 v[62:63], v246 offset0:46 offset1:47\n"
      "v_mfma_f32_16x16x32_bf16 v[72:75], v[140:143], a[104:107], v[72:75]\n"
      "v_mfma_f32_16x16x32_bf16 v[76:79], v[140:143], a[108:111], v[76:79]\n"
      "v_mfma_f32_16x16x32_bf16 v[80:83], v[140:143], a[112:115], v[80:83]\n"
      "s_waitcnt lgkmcnt(8)\n"
      "v_add_f32 v32, v32, v40\n"
      "v_add_f32 v33, v33, v41\n"
      "v_add_f32 v34, v34, v42\n"
      "v_mfma_f32_16x16x32_bf16 v[84:87], v[140:143], a[116:119], v[84:87]\n"
      "v_add_f32 v35, v35, v43\n"
      "v_add_f32 v36, v36, v44\n"
      "v_add_f32 v37, v37, v45\n"
      "v_mfma_f32_16x16x32_bf16 v[88:91], v[140:143], a[120:123], v[88:91]\n"
      "v_add_f32 v38, v38, v46\n"
      "v_add_f32 v39, v39, v47\n"
      "s_waitcnt lgkmcnt(4)\n"
      "v_mfma_f32_16x16x32_bf16 v[92:95], v[140:143], a[124:127], v[92:95]\n"
      "s_waitcnt vmcnt(17)\n"
      "v_add_f32 v32, v32, v48\n"
      "v_add_f32 v33, v33, v49\n"
      "v_add_f32 v34, v34, v50\n"
      "v_mfma_f32_16x16x32_bf16 v[72:75], v[144:147], a[136:139], v[72:75]\n"
      "v_add_f32 v35, v35, v51\n"
      "v_add_f32 v36, v36, v52\n"
      "v_add_f32 v37, v37, v53\n"
      "v_mfma_f32_16x16x32_bf16 v[76:79], v[144:147], a[140:143], v[76:79]\n"
      "v_add_f32 v38, v38, v54\n"
      "v_add_f32 v39, v39, v55\n"
      "s_waitcnt lgkmcnt(0)\n"
      "v_mfma_f32_16x16x32_bf16 v[80:83], v[144:147], a[144:147], v[80:83]\n"
      "v_add_f32 v32, v32, v56\n"
      "v_add_f32 v33, v33, v57\n"
      "v_add_f32 v34, v34, v58\n"
      "v_mfma_f32_16x16x32_bf16 v[84:87], v[144:147], a[148:151], v[84:87]\n"
      "v_add_f32 v35, v35, v59\n"
      "v_add_f32 v36, v36, v60\n"
      "v_add_f32 v37, v37, v61\n"
      "v_mfma_f32_16x16x32_bf16 v[88:91], v[144:147], a[152:155], v[88:91]\n"
      "v_add_f32 v38, v38, v62\n"
      "v_add_f32 v39, v39, v63\n"
      "v_mov_b32 v44, v247\n"
      "v_mfma_f32_16x16x32_bf16 v[92:95], v[144:147], a[156:159], v[92:95]\n"
      "v_mov_b32 v47, s37\n"
      "v_cvt_pk_bf16_f32 v40, v32, v33\n"
      "v_cvt_pk_bf16_f32 v41, v34, v35\n"
      "v_mfma_f32_16x16x32_bf16 v[72:75], v[148:151], a[168:171], v[72:75]\n"
      "v_cvt_pk_bf16_f32 v42, v36, v37\n"
      "v_mov_b32 v43, s37\n"
      "v_cvt_pk_bf16_f32 v45, v38, v39\n"
      "v_mfma_f32_16x16x32_bf16 v[76:79], v[148:151], a[172:175], v[76:79]\n"
      "global_store_dwordx4 v252, v[40:43], s[52:53] offset:1024 sc1\n"
      "global_store_dwordx4 v252, v[44:47], s[52:53] offset:2048 sc1\n"
      "v_mfma_f32_16x16x32_bf16 v[80:83], v[148:151], a[176:179], v[80:83]\n"
      "v_mfma_f32_16x16x32_bf16 v[84:87], v[148:151], a[180:183], v[84:87]\n"
      "v_mfma_f32_16x16x32_bf16 v[88:91], v[148:151], a[184:187], v[88:91]\n"
      "v_mfma_f32_16x16x32_bf16 v[92:95], v[148:151], a[188:191], v[92:95]\n"
      "s_waitcnt vmcnt(17)\n"
      "v_mfma_f32_16x16x32_bf16 v[72:75], v[152:155], a[200:203], v[72:75]\n"
      "s_waitcnt vmcnt(2)\n"
      "v_bitop3_b32 v234, v227, v231, s37 bitop3:0x7e\n"
      "v_bitop3_b32 v234, v234, v235, s37 bitop3:0xf6\n"
      "v_cmp_ne_u32 vcc, 0, v234\n"
      "s_nop 1\n"
      "s_cbranch_vccnz .Lp_s16_%=\n"
      ".Lp_o17_%=:\n"
      "v_mfma_f32_16x16x32_bf16 v[76:79], v[152:155], a[204:207], v[76:79]\n"
      "v_xor_b32 v202, s72, v204\n"
      "v_xor_b32 v203, s72, v206\n"
      "v_mfma_f32_16x16x32_bf16 v[80:83], v[152:155], a[208:211], v[80:83]\n"
      "v_lshlrev_b32 v227, 16, v224\n"
      "v_and_b32 v224, 0xffff0000, v224\n"
      "v_mfma_f32_16x16x32_bf16 v[84:87], v[152:155], a[212:215], v[84:87]\n"
      "v_fma_f32 v208, v202, v227, v208\n"
      "v_fma_f32 v209, v202, v224, v209\n"
      "v_mfma_f32_16x16x32_bf16 v[88:91], v[152:155], a[216:219], v[88:91]\n"
      "v_lshlrev_b32 v231, 16, v225\n"
      "v_and_b32 v225, 0xffff0000, v225\n"
      "v_mfma_f32_16x16x32_bf16 v[92:95], v[152:155], a[220:223], v[92:95]\n"
      "v_fma_f32 v210, v202, v231, v210\n"
      "v_fma_f32 v211, v202, v225, v211\n"
      "v_mfma_f32_16x16x32_bf16 v[72:75], v[156:159], a[232:235], v[72:75]\n"
      "v_lshlrev_b32 v227, 16, v226\n"
      "v_and_b32 v226, 0xffff0000, v226\n"
      "v_mfma_f32_16x16x32_bf16 v[76:79], v[156:159], a[236:239], v[76:79]\n"
      "v_fma_f32 v212, v203, v227, v212\n"
      "v_fma_f32 v213, v203, v226, v213\n"
      "v_mfma_f32_16x16x32_bf16 v[80:83], v[156:159], a[240:243], v[80:83]\n"
      "v_lshlrev_b32 v231, 16, v232\n"
      "v_and_b32 v232, 0xffff0000, v232\n"
      "v_mfma_f32_16x16x32_bf16 v[84:87], v[156:159], a[244:247], v[84:87]\n"
      "v_fma_f32 v214, v203, v231, v214\n"
      "v_fma_f32 v215, v203, v232, v215\n"
      "v_mfma_f32_16x16x32_bf16 v[88:91], v[156:159], a[248:251], v[88:91]\n"
      "v_lshlrev_b32 v227, 16, v228\n"
      "v_and_b32 v228, 0xffff0000, v228\n"
      "v_mfma_f32_16x16x32_bf16 v[92:95], v[156:159], a[252:255], v[92:95]\n"
      "v_fma_f32 v216, v202, v227, v216\n"
      "v_fma_f32 v217, v202, v228, v217\n"
      "v_mfma_f32_16x16x32_bf16 v[104:107], v[160:163], a[8:11], 0\n"
      "v_lshlrev_b32 v231, 16, v229\n"
      "v_and_b32 v229, 0xffff0000, v229\n"
      "v_mfma_f32_16x16x32_bf16 v[108:111], v[160:163], a[12:15], 0\n"
      "s_add_u32 m0, s41, 12288\n"
      "s_nop 0\n"
      "ds_write_addtid_b32 v72 offset:53248\n"
      "v_mfma_f32_16x16x32_bf16 v[112:115], v[160:163], a[16:19], 0\n"
      "ds_write_addtid_b32 v73 offset:53504\n"
      "v_fma_f32 v218, v202, v231, v218\n"
      "v_mfma_f32_16x16x32_bf16 v[116:119], v[160:163], a[20:23], 0\n"
      "ds_write_addtid_b32 v74 offset:53760\n"
      "v_fma_f32 v219, v202, v229, v219\n"
      "v_mfma_f32_16x16x32_bf16 v[120:123], v[160:163], a[24:27], 0\n"
      "ds_write_addtid_b32 v75 offset:54016\n"
      "v_lshlrev_b32 v227, 16, v230\n"
      "v_mfma_f32_16x16x32_bf16 v[124:127], v[160:163], a[28:31], 0\n"
      "ds_write_addtid_b32 v76 offset:54272\n"
      "v_and_b32 v230, 0xffff0000, v230\n"
      "v_mfma_f32_16x16x32_bf16 v[104:107], v[164:167], a[40:43], v[104:107]\n"
      "ds_write_addtid_b32 v77 offset:54528\n"
      "v_fma_f32 v220, v203, v227, v220\n"
      "v_mfma_f32_16x16x32_bf16 v[108:111], v[164:167], a[44:47], v[108:111]\n"
      "ds_write_addtid_b32 v78 offset:54784\n"
      "v_fma_f32 v221, v203, v230, v221\n"
      "v_mfma_f32_16x16x32_bf16 v[112:115], v[164:167], a[48:51], v[112:115]\n"
      "ds_write_addtid_b32 v79 offset:55040\n"
      "v_lshlrev_b32 v231, 16, v233\n"
      "v_mfma_f32_16x16x32_bf16 v[116:119], v[164:167], a[52:55], v[116:119]\n"
      "ds_write_addtid_b32 v80 offset:55296\n"
      "v_and_b32 v233, 0xffff0000, v233\n"
      "v_mfma_f32_16x16x32_bf16 v[120:123], v[164:167], a[56:59], v[120:123]\n"
      "ds_write_addtid_b32 v81 offset:55552\n"
      "v_fma_f32 v222, v203, v231, v222\n"
      "v_mfma_f32_16x16x32_bf16 v[124:127], v[164:167], a[60:63], v[124:127]\n"
      "ds_write_addtid_b32 v82 offset:55808\n"
      "v_fma_f32 v223, v203, v233, v223\n"
      "v_mfma_f32_16x16x32_bf16 v[104:107], v[168:171], a[72:75], v[104:107]\n"
      "ds_write_addtid_b32 v83 offset:56064\n"
      "v_add_u32 v254, 12288, v252\n"
      "v_mfma_f32_16x16x32_bf16 v[108:111], v[168:171], a[76:79], v[108:111]\n"
      "global_load_dwordx4 v[12:15], v254, s[62:63] offset:0 sc1\n"
      "v_mfma_f32_16x16x32_bf16 v[112:115], v[168:171], a[80:83], v[112:115]\n"
      "global_load_dwordx4 v[16:19], v254, s[62:63] offset:1024 sc1\n"
      "v_mfma_f32_16x16x32_bf16 v[116:119], v[168:171], a[84:87], v[116:119]\n"
      "global_load_dwordx4 v[20:23], v254, s[62:63] offset:2048 sc1\n"
      "v_mfma_f32_16x16x32_bf16 v[120:123], v[168:171], a[88:91], v[120:123]\n"
      "ds_write_addtid_b32 v84 offset:56320\n"
      "v_mfma_f32_16x16x32_bf16 v[124:127], v[168:171], a[92:95], v[124:127]\n"
      "ds_write_addtid_b32 v85 offset:56576\n"
      "v_mfma_f32_16x16x32_bf16 v[104:107], v[172:175], a[104:107], v[104:107]\n"
      "ds_write_addtid_b32 v86 offset:56832\n"
      "v_mfma_f32_16x16x32_bf16 v[108:111], v[172:175], a[108:111], v[108:111]\n"
      "ds_write_addtid_b32 v87 offset:57088\n"
      "v_mfma_f32_16x16x32_bf16 v[112:115], v[172:175], a[112:115], v[112:115]\n"
      "ds_write_addtid_b32 v88 offset:57344\n"
      "v_mfma_f32_16x16x32_bf16 v[116:119], v[172:175], a[116:119], v[116:119]\n"
      "ds_write_addtid_b32 v89 offset:57600\n"
      "v_mfma_f32_16x16x32_bf16 v[120:123], v[172:175], a[120:123], v[120:123]\n"
      "ds_write_addtid_b32 v90 offset:57856\n"
      "v_mfma_f32_16x16x32_bf16 v[124:127], v[172:175], a[124:127], v[124:127]\n"
      "ds_write_addtid_b32 v91 offset:58112\n"
      "v_mfma_f32_16x16x32_bf16 v[104:107], v[176:179], a[136:139], v[104:107]\n"
      "ds_write_addtid_b32 v92 offset:58368\n"
      "v_mfma_f32_16x16x32_bf16 v[108:111], v[176:179], a[140:143], v[108:111]\n"
      "ds_write_addtid_b32 v93 offset:58624\n"
      "v_mfma_f32_16x16x32_bf16 v[112:115], v[176:179], a[144:147], v[112:115]\n"
      "ds_write_addtid_b32 v94 offset:58880\n"
      "v_mfma_f32_16x16x32_bf16 v[116:119], v[176:179], a[148:151], v[116:119]\n"
      "ds_write_addtid_b32 v95 offset:59136\n"
      "v_mfma_f32_16x16x32_bf16 v[120:123], v[176:179], a[152:155], v[120:123]\n"
      "s_lshr_b32 s43, s42, 7\n"
      "s_cmp_eq_u32 s43, s36\n"
      "s_cbranch_scc0 .Lp_e18_%=\n"
      "s_lshl2_add_u32 s44, s35, s34\n"
      "s_lshl2_add_u32 s43, s44, s33\n"
      "s_lshl_b32 s43, s43, 12\n"
      "s_add_u32 s43, s43, 524288\n"
      "v_add_u32 v60, s43, v252\n"
      "global_load_dwordx4 v[32:35], v60, s[58:59] offset:0 sc1\n"
      "global_load_dwordx4 v[36:39], v60, s[58:59] offset:1024 sc1\n"
      "global_load_dwordx4 v[40:43], v60, s[58:59] offset:2048 sc1\n"
      "global_load_dwordx4 v[44:47], v60, s[58:59] offset:3072 sc1\n"
      "v_and_b32 v58, 15, v255\n"
      "v_lshrrev_b32 v59, 5, v255\n"
      "v_lshl_or_b32 v58, v59, 4, v58\n"
      "s_lshl4_add_u32 s44, s42, s44\n"
      "s_lshl_b32 s44, s44, 6\n"
      "v_lshl_add_u32 v59, v58, 1, s44\n"
      "global_load_ushort v57, v59, s[16:17]\n"
      "s_mov_b32 s44, 131072\n"
      "v_lshl_add_u32 v58, v58, 2, s44\n"
      "s_lshl_b32 s43, s33, 13\n"
      "v_add_u32 v59, s43, v58\n"
      ".Lp_e18_%=:\n"
      "v_mfma_f32_16x16x32_bf16 v[124:127], v[176:179], a[156:159], v[124:127]\n"
      "v_mfma_f32_16x16x32_bf16 v[104:107], v[180:183], a[168:171], v[104:107]\n"
      "s_waitcnt lgkmcnt(0)\n"
      "s_barrier\n"
      "v_mfma_f32_16x16x32_bf16 v[108:111], v[180:183], a[172:175], v[108:111]\n"
      "ds_read2st64_b32 v[72:73], v248 offset0:0 offset1:1\n"
      "v_mfma_f32_16x16x32_bf16 v[112:115], v[180:183], a[176:179], v[112:115]\n"
      "ds_read2st64_b32 v[74:75], v248 offset0:2 offset1:3\n"
      "v_mfma_f32_16x16x32_bf16 v[116:119], v[180:183], a[180:183], v[116:119]\n"
      "ds_read2st64_b32 v[76:77], v248 offset0:4 offset1:5\n"
      "v_mfma_f32_16x16x32_bf16 v[120:123], v[180:183], a[184:187], v[120:123]\n"
      "ds_read2st64_b32 v[78:79], v248 offset0:6 offset1:7\n"
      "v_mfma_f32_16x16x32_bf16 v[124:127], v[180:183], a[188:191], v[124:127]\n"
      "ds_read2st64_b32 v[80:81], v249 offset0:8 offset1:9\n"
      "v_mfma_f32_16x16x32_bf16 v[104:107], v[184:187], a[200:203], v[104:107]\n"
      "ds_read2st64_b32 v[82:83], v249 offset0:10 offset1:11\n"
      "v_mfma_f32_16x16x32_bf16 v[108:111], v[184:187], a[204:207], v[108:111]\n"
      "ds_read2st64_b32 v[84:85], v249 offset0:12 offset1:13\n"
      "v_mfma_f32_16x16x32_bf16 v[112:115], v[184:187], a[208:211], v[112:115]\n"
      "ds_read2st64_b32 v[86:87], v249 offset0:14 offset1:15\n"
      "v_mfma_f32_16x16x32_bf16 v[116:119], v[184:187], a[212:215], v[116:119]\n"
      "ds_read2st64_b32 v[88:89], v250 offset0:16 offset1:17\n"
      "v_mfma_f32_16x16x32_bf16 v[120:123], v[184:187], a[216:219], v[120:123]\n"
      "ds_read2st64_b32 v[90:91], v250 offset0:18 offset1:19\n"
      "v_mfma_f32_16x16x32_bf16 v[124:127], v[184:187], a[220:223], v[124:127]\n"
      "s_waitcnt lgkmcnt(6)\n"
      "ds_read2st64_b32 v[92:93], v250 offset0:20 offset1:21\n"
      "v_fma_f32 v208, v204, v72, v208\n"
      "v_mfma_f32_16x16x32_bf16 v[104:107], v[188:191], a[232:235], v[104:107]\n"
      "ds_read2st64_b32 v[94:95], v250 offset0:22 offset1:23\n"
      "v_fma_f32 v209, v204, v73, v209\n"
      "v_mfma_f32_16x16x32_bf16 v[108:111], v[188:191], a[236:239], v[108:111]\n"
      "v_fma_f32 v210, v204, v74, v210\n"
      "v_fma_f32 v211, v204, v75, v211\n"
      "v_mfma_f32_16x16x32_bf16 v[112:115], v[188:191], a[240:243], v[112:115]\n"
      "v_fma_f32 v212, v206, v76, v212\n"
      "v_fma_f32 v213, v206, v77, v213\n"
      "v_mfma_f32_16x16x32_bf16 v[116:119], v[188:191], a[244:247], v[116:119]\n"
      "v_fma_f32 v214, v206, v78, v214\n"
      "v_fma_f32 v215, v206, v79, v215\n"
      "v_mfma_f32_16x16x32_bf16 v[120:123], v[188:191], a[248:251], v[120:123]\n"
      "s_waitcnt lgkmcnt(4)\n"
      "v_fma_f32 v208, v204, v80, v208\n"
      "v_fma_f32 v209, v204, v81, v209\n"
      "v_mfma_f32_16x16x32_bf16 v[124:127], v[188:191], a[252:255], v[124:127]\n"
      "v_fma_f32 v210, v204, v82, v210\n"
      "v_fma_f32 v211, v204, v83, v211\n"
      "v_mfma_f32_16x16x32_bf16 v[64:67], v[128:131], a[0:3], 0\n"
      "v_fma_f32 v212, v206, v84, v212\n"
      "v_fma_f32 v213, v206, v85, v213\n"
      "v_fma_f32 v214, v206, v86, v214\n"
      "v_mfma_f32_16x16x32_bf16 v[68:71], v[128:131], a[4:7], 0\n"
      "s_add_u32 m0, s41, 12288\n"
      "s_nop 0\n"
      "ds_write_addtid_b32 v104 offset:59392\n"
      "v_mfma_f32_16x16x32_bf16 v[64:67], v[132:135], a[32:35], v[64:67]\n"
      "ds_write_addtid_b32 v105 offset:59648\n"
      "ds_write_addtid_b32 v106 offset:59904\n"
      "v_fma_f32 v215, v206, v87, v215\n"
      "v_mfma_f32_16x16x32_bf16 v[68:71], v[132:135], a[36:39], v[68:71]\n"
      "s_waitcnt lgkmcnt(3)\n"
      "s_waitcnt vmcnt(0)\n"
      "v_bitop3_b32 v22, v15, v19, s37 bitop3:0x7e\n"
      "v_bitop3_b32 v22, v22, v23, s37 bitop3:0xf6\n"
      "v_cmp_ne_u32 vcc, 0, v22\n"
      "s_nop 1\n"
      "s_cbranch_vccnz .Lp_s20_%=\n"
      ".Lp_o21_%=:\n"
      "v_mfma_f32_16x16x32_bf16 v[64:67], v[136:139], a[64:67], v[64:67]\n"
      "ds_write_addtid_b32 v107 offset:60160\n"
      "ds_write_addtid_b32 v108 offset:60416\n"
      "v_fma_f32 v208, v204, v88, v208\n"
      "v_mfma_f32_16x16x32_bf16 v[68:71], v[136:139], a[68:71], v[68:71]\n"
      "ds_write_addtid_b32 v109 offset:60672\n"
      "ds_write_addtid_b32 v110 offset:60928\n"
      "v_fma_f32 v209, v204, v89, v209\n"
      "v_mfma_f32_16x16x32_bf16 v[64:67], v[140:143], a[96:99], v[64:67]\n"
      "ds_write_addtid_b32 v111 offset:61184\n"
      "ds_write_addtid_b32 v112 offset:61440\n"
      "v_fma_f32 v210, v204, v90, v210\n"
      "v_mfma_f32_16x16x32_bf16 v[68:71], v[140:143], a[100:103], v[68:71]\n"
      "ds_write_addtid_b32 v113 offset:61696\n"
      "ds_write_addtid_b32 v114 offset:61952\n"
      "v_fma_f32 v211, v204, v91, v211\n"
      "v_mfma_f32_16x16x32_bf16 v[64:67], v[144:147], a[128:131], v[64:67]\n"
      "ds_write_addtid_b32 v115 offset:62208\n"
      "ds_write_addtid_b32 v116 offset:62464\n"
      "v_fma_f32 v212, v206, v92, v212\n"
      "v_mfma_f32_16x16x32_bf16 v[68:71], v[144:147], a[132:135], v[68:71]\n"
      "ds_write_addtid_b32 v117 offset:62720\n"
      "ds_write_addtid_b32 v118 offset:62976\n"
      "v_fma_f32 v213, v206, v93, v213\n"
      "v_mfma_f32_16x16x32_bf16 v[64:67], v[148:151], a[160:163], v[64:67]\n"
      "ds_write_addtid_b32 v119 offset:63232\n"
      "ds_write_addtid_b32 v120 offset:63488\n"
      "v_fma_f32 v214, v206, v94, v214\n"
      "v_mfma_f32_16x16x32_bf16 v[68:71], v[148:151], a[164:167], v[68:71]\n"
      "ds_write_addtid_b32 v121 offset:63744\n"
      "ds_write_addtid_b32 v122 offset:64000\n"
      "v_fma_f32 v215, v206, v95, v215\n"
      "v_mfma_f32_16x16x32_bf16 v[64:67], v[152:155], a[192:195], v[64:67]\n"
      "ds_write_addtid_b32 v123 offset:64256\n"
      "ds_write_addtid_b32 v124 offset:64512\n"
      "v_xor_b32 v202, s73, v204\n"
      "v_mfma_f32_16x16x32_bf16 v[68:71], v[152:155], a[196:199], v[68:71]\n"
      "ds_write_addtid_b32 v125 offset:64768\n"
      "ds_write_addtid_b32 v126 offset:65024\n"
      "v_xor_b32 v203, s73, v206\n"
      "v_mfma_f32_16x16x32_bf16 v[64:67], v[156:159], a[224:227], v[64:67]\n"
      "ds_write_addtid_b32 v127 offset:65280\n"
      "v_lshlrev_b32 v15, 16, v12\n"
      "v_and_b32 v12, 0xffff0000, v12\n"
      "v_mfma_f32_16x16x32_bf16 v[68:71], v[156:159], a[228:231], v[68:71]\n"
      "v_add_u32 v253, 24576, v252\n"
      "v_fma_f32 v208, v202, v15, v208\n"
      "v_fma_f32 v209, v202, v12, v209\n"
      "v_mfma_f32_16x16x32_bf16 v[96:99], v[160:163], a[0:3], 0\n"
      "global_load_dwordx4 v[224:227], v253, s[62:63] offset:0 sc1\n"
      "global_load_dwordx4 v[228:231], v253, s[62:63] offset:1024 sc1\n"
      "v_lshlrev_b32 v19, 16, v13\n"
      "v_mfma_f32_16x16x32_bf16 v[100:103], v[160:163], a[4:7], 0\n"
      "global_load_dwordx4 v[232:235], v253, s[62:63] offset:2048 sc1\n"
      "s_lshr_b32 s43, s42, 7\n"
      "s_cmp_eq_u32 s43, s36\n"
      "s_cbranch_scc0 .Lp_e22_%=\n"
      "s_waitcnt vmcnt(3)\n"
      "v_bitop3_b32 v61, v33, v35, s37 bitop3:0x7e\n"
      "v_bitop3_b32 v61, v61, v37, s37 bitop3:0xf6\n"
      "v_bitop3_b32 v61, v61, v39, s37 bitop3:0xf6\n"
      "v_bitop3_b32 v61, v61, v41, s37 bitop3:0xf6\n"
      "v_bitop3_b32 v61, v61, v43, s37 bitop3:0xf6\n"
      "v_bitop3_b32 v61, v61, v45, s37 bitop3:0xf6\n"
      "v_bitop3_b32 v61, v61, v47, s37 bitop3:0xf6\n"
      "v_cmp_ne_u32 vcc, 0, v61\n"
      "s_nop 1\n"
      "s_cbranch_vccnz .Lp_s24_%=\n"
      ".Lp_o25_%=:\n"
      "v_add_f32 v32, v32, v36\n"
      "v_add_f32 v34, v34, v38\n"
      "v_add_f32 v40, v40, v44\n"
      "v_add_f32 v42, v42, v46\n"
      "v_add_f32 v32, v32, v40\n"
      "v_add_f32 v34, v34, v42\n"
      "s_nop 1\n"
      "v_permlane32_swap_b32 v32, v34\n"
      "v_add_f32 v32, v32, v34\n"
      "v_mov_b32 v34, v32\n"
      "s_nop 1\n"
      "v_permlane16_swap_b32 v32, v34\n"
      "v_add_f32 v32, v32, v34\n"
      "ds_write_b32 v59, v32\n"
      "s_waitcnt lgkmcnt(0)\n"
      ".Lp_e22_%=:\n"
      "v_mfma_f32_16x16x32_bf16 v[96:99], v[164:167], a[32:35], v[96:99]\n"
      "s_waitcnt lgkmcnt(0)\n"
      "s_barrier\n"
      "v_mfma_f32_16x16x32_bf16 v[100:103], v[164:167], a[36:39], v[100:103]\n"
      "s_lshr_b32 s43, s42, 5\n"
      "s_lshl2_add_u32 s44, s36, s33\n"
      "s_cmp_eq_u32 s43, s44\n"
      "s_cbranch_scc0 .Lp_e26_%=\n"
      "ds_read_b32 v32, v58 offset:0\n"
      "ds_read_b32 v33, v58 offset:8192\n"
      "ds_read_b32 v34, v58 offset:16384\n"
      "ds_read_b32 v35, v58 offset:24576\n"
      "v_mov_b32 v62, 0\n"
      "v_mov_b32 v63, s37\n"
      "s_or_b32 s43, s34, s35\n"
      "s_cmp_eq_u32 s43, 0\n"
      "s_cbranch_scc0 .Lp_z27_%=\n"
      "s_mov_b64 exec, 1\n"
      "global_store_dword v62, v63, s[58:59] sc1\n"
      ".Lp_z27_%=:\n"
      "s_mov_b64 exec, -1\n"
      "s_waitcnt vmcnt(3)\n"
      "v_lshlrev_b32 v57, 16, v57\n"
      "v_and_b32 v56, 48, v255\n"
      "s_mov_b32 s44, 131072\n"
      "v_lshl_add_u32 v56, v56, 1, s44\n"
      "s_waitcnt lgkmcnt(0)\n"
      "v_add_f32 v32, v32, v33\n"
      "s_waitcnt lgkmcnt(0)\n"
      "v_add_f32 v34, v34, v35\n"
      "v_add_f32 v32, v32, v34\n"
      "v_cvt_pk_bf16_f32 v32, v32, v32\n"
      "v_and_b32 v32, 0xffff0000, v32\n"
      "v_mul_f32 v32, s80, v32\n"
      "v_cvt_pk_bf16_f32 v32, v32, v32\n"
      "v_and_b32 v32, 0xffff0000, v32\n"
      "v_add_f32 v32, v32, v57\n"
      "v_cvt_pk_bf16_f32 v32, v32, v32\n"
      "v_and_b32 v32, 0xffff0000, v32\n"
      "v_sub_f32 v32, v32, v57\n"
      "ds_write_b32 v58, v32 offset:1024\n"
      "ds_read_b128 v[36:39], v56 offset:1024\n"
      "ds_read_b128 v[40:43], v56 offset:1040\n"
      ".Lp_e26_%=:\n"
      "v_mfma_f32_16x16x32_bf16 v[96:99], v[168:171], a[64:67], v[96:99]\n"
      "ds_read2st64_b32 v[104:105], v248 offset0:24 offset1:25\n"
      "ds_read2st64_b32 v[106:107], v248 offset0:26 offset1:27\n"
      "v_and_b32 v13, 0xffff0000, v13\n"
      "v_mfma_f32_16x16x32_bf16 v[100:103], v[168:171], a[68:71], v[100:103]\n"
      "ds_read2st64_b32 v[108:109], v248 offset0:28 offset1:29\n"
      "ds_read2st64_b32 v[110:111], v248 offset0:30 offset1:31\n"
      "v_fma_f32 v210, v202, v19, v210\n"
      "v_mfma_f32_16x16x32_bf16 v[96:99], v[172:175], a[96:99], v[96:99]\n"
      "ds_read2st64_b32 v[112:113], v249 offset0:32 offset1:33\n"
      "ds_read2st64_b32 v[114:115], v249 offset0:34 offset1:35\n"
      "v_fma_f32 v211, v202, v13, v211\n"
      "v_mfma_f32_16x16x32_bf16 v[100:103], v[172:175], a[100:103], v[100:103]\n"
      "ds_read2st64_b32 v[116:117], v249 offset0:36 offset1:37\n"
      "ds_read2st64_b32 v[118:119], v249 offset0:38 offset1:39\n"
      "v_lshlrev_b32 v15, 16, v14\n"
      "v_mfma_f32_16x16x32_bf16 v[96:99], v[176:179], a[128:131], v[96:99]\n"
      "ds_read2st64_b32 v[120:121], v250 offset0:40 offset1:41\n"
      "ds_read2st64_b32 v[122:123], v250 offset0:42 offset1:43\n"
      "v_and_b32 v14, 0xffff0000, v14\n"
      "v_mfma_f32_16x16x32_bf16 v[100:103], v[176:179], a[132:135], v[100:103]\n"
      "ds_read2st64_b32 v[124:125], v250 offset0:44 offset1:45\n"
      "ds_read2st64_b32 v[126:127], v250 offset0:46 offset1:47\n"
      "v_fma_f32 v212, v203, v15, v212\n"
      "v_mfma_f32_16x16x32_bf16 v[96:99], v[180:183], a[160:163], v[96:99]\n"
      "v_fma_f32 v213, v203, v14, v213\n"
      "v_lshlrev_b32 v19, 16, v20\n"
      "v_and_b32 v20, 0xffff0000, v20\n"
      "v_mfma_f32_16x16x32_bf16 v[100:103], v[180:183], a[164:167], v[100:103]\n"
      "v_fma_f32 v214, v203, v19, v214\n"
      "v_fma_f32 v215, v203, v20, v215\n"
      "v_lshlrev_b32 v15, 16, v16\n"
      "v_mfma_f32_16x16x32_bf16 v[96:99], v[184:187], a[192:195], v[96:99]\n"
      "v_and_b32 v16, 0xffff0000, v16\n"
      "v_fma_f32 v216, v202, v15, v216\n"
      "v_fma_f32 v217, v202, v16, v217\n"
      "v_mfma_f32_16x16x32_bf16 v[100:103], v[184:187], a[196:199], v[100:103]\n"
      "v_lshlrev_b32 v19, 16, v17\n"
      "v_and_b32 v17, 0xffff0000, v17\n"
      "v_fma_f32 v218, v202, v19, v218\n"
      "v_mfma_f32_16x16x32_bf16 v[96:99], v[188:191], a[224:227], v[96:99]\n"
      "v_fma_f32 v219, v202, v17, v219\n"
      "v_lshlrev_b32 v15, 16, v18\n"
      "v_and_b32 v18, 0xffff0000, v18\n"
      "v_mfma_f32_16x16x32_bf16 v[100:103], v[188:191], a[228:231], v[100:103]\n"
      "v_fma_f32 v220, v203, v15, v220\n"
      "v_fma_f32 v221, v203, v18, v221\n"
      "v_lshlrev_b32 v19, 16, v21\n"
      "v_and_b32 v21, 0xffff0000, v21\n"
      "v_fma_f32 v222, v203, v19, v222\n"
      "v_fma_f32 v223, v203, v21, v223\n"
      "s_add_u32 s54, s41, 12288\n"
      "v_and_b32 v8, 15, v255\n"
      "v_and_b32 v9, 48, v255\n"
      "v_lshl_or_b32 v8, v8, 6, v9\n"
      "v_add_u32 v8, s54, v8\n"
      "v_add_u32 v9, s54, v252\n"
      "v_readfirstlane_b32 s55, v251\n"
      "v_lshrrev_b32 v10, 2, v255\n"
      "v_and_b32 v11, 48, v252\n"
      "v_lshl_or_b32 v10, v10, 10, v11\n"
      "v_add_u32 v10, s55, v10\n"
      "v_fma_f32 v208, v204, v64, v208\n"
      "v_fma_f32 v209, v204, v65, v209\n"
      "v_fma_f32 v210, v204, v66, v210\n"
      "v_fma_f32 v211, v204, v67, v211\n"
      "v_fma_f32 v212, v206, v68, v212\n"
      "v_fma_f32 v213, v206, v69, v213\n"
      "v_fma_f32 v214, v206, v70, v214\n"
      "v_fma_f32 v215, v206, v71, v215\n"
      "v_fma_f32 v216, v204, v96, v216\n"
      "v_fma_f32 v217, v204, v97, v217\n"
      "v_fma_f32 v218, v204, v98, v218\n"
      "v_fma_f32 v219, v204, v99, v219\n"
      "v_fma_f32 v220, v206, v100, v220\n"
      "v_fma_f32 v221, v206, v101, v221\n"
      "v_fma_f32 v222, v206, v102, v222\n"
      "v_fma_f32 v223, v206, v103, v223\n"
      "s_waitcnt lgkmcnt(7)\n"
      "v_pk_add_f32 v[104:105], v[104:105], v[112:113]\n"
      "s_waitcnt lgkmcnt(6)\n"
      "v_pk_add_f32 v[106:107], v[106:107], v[114:115]\n"
      "s_waitcnt lgkmcnt(5)\n"
      "v_pk_add_f32 v[108:109], v[108:109], v[116:117]\n"
      "s_waitcnt lgkmcnt(4)\n"
      "v_pk_add_f32 v[110:111], v[110:111], v[118:119]\n"
      "s_waitcnt lgkmcnt(3)\n"
      "v_pk_add_f32 v[104:105], v[104:105], v[120:121]\n"
      "s_waitcnt lgkmcnt(2)\n"
      "v_pk_add_f32 v[106:107], v[106:107], v[122:123]\n"
      "s_waitcnt lgkmcnt(1)\n"
      "v_pk_add_f32 v[108:109], v[108:109], v[124:125]\n"
      "s_waitcnt lgkmcnt(0)\n"
      "v_pk_add_f32 v[110:111], v[110:111], v[126:127]\n"
      "v_fma_f32 v216, v204, v104, v216\n"
      "v_fma_f32 v217, v204, v105, v217\n"
      "v_fma_f32 v218, v204, v106, v218\n"
      "v_fma_f32 v219, v204, v107, v219\n"
      "v_fma_f32 v220, v206, v108, v220\n"
      "v_fma_f32 v221, v206, v109, v221\n"
      "v_fma_f32 v222, v206, v110, v222\n"
      "v_fma_f32 v223, v206, v111, v223\n"
      "s_lshr_b32 s43, s42, 5\n"
      "s_lshl2_add_u32 s44, s36, s33\n"
      "s_cmp_eq_u32 s43, s44\n"
      "s_cbranch_scc0 .Lp_e28_%=\n"
      "v_lshrrev_b32 v32, 10, v251\n"
      "v_subrev_u32 v32, s42, v32\n"
      "v_cmp_eq_u32 vcc, 0, v32\n"
      "s_nop 1\n"
      "s_mov_b64 exec, vcc\n"
      "v_fma_f32 v208, v205, v36, v208\n"
      "v_fma_f32 v209, v205, v37, v209\n"
      "v_fma_f32 v210, v205, v38, v210\n"
      "v_fma_f32 v211, v205, v39, v211\n"
      "v_fma_f32 v216, v205, v40, v216\n"
      "v_fma_f32 v217, v205, v41, v217\n"
      "v_fma_f32 v218, v205, v42, v218\n"
      "v_fma_f32 v219, v205, v43, v219\n"
      "s_mov_b64 exec, -1\n"
      "v_cmp_eq_u32 vcc, -16, v32\n"
      "s_nop 1\n"
      "s_mov_b64 exec, vcc\n"
      "v_fma_f32 v212, v207, v36, v212\n"
      "v_fma_f32 v213, v207, v37, v213\n"
      "v_fma_f32 v214, v207, v38, v214\n"
      "v_fma_f32 v215, v207, v39, v215\n"
      "v_fma_f32 v220, v207, v40, v220\n"
      "v_fma_f32 v221, v207, v41, v221\n"
      "v_fma_f32 v222, v207, v42, v222\n"
      "v_fma_f32 v223, v207, v43, v223\n"
      "s_mov_b64 exec, -1\n"
      ".Lp_e28_%=:\n"
      "v_xor_b32 v202, s74, v204\n"
      "v_xor_b32 v203, s74, v206\n"
      "s_waitcnt vmcnt(0)\n"
      "v_bitop3_b32 v234, v227, v231, s37 bitop3:0x7e\n"
      "v_bitop3_b32 v234, v234, v235, s37 bitop3:0xf6\n"
      "v_cmp_ne_u32 vcc, 0, v234\n"
      "s_nop 1\n"
      "s_cbranch_vccnz .Lp_s30_%=\n"
      ".Lp_o31_%=:\n"
      "v_lshlrev_b32 v227, 16, v224\n"
      "v_and_b32 v224, 0xffff0000, v224\n"
      "v_fma_f32 v208, v202, v227, v208\n"
      "v_fma_f32 v209, v202, v224, v209\n"
      "v_lshlrev_b32 v231, 16, v225\n"
      "v_and_b32 v225, 0xffff0000, v225\n"
      "v_fma_f32 v210, v202, v231, v210\n"
      "v_fma_f32 v211, v202, v225, v211\n"
      "v_lshlrev_b32 v227, 16, v228\n"
      "v_and_b32 v228, 0xffff0000, v228\n"
      "v_fma_f32 v216, v202, v227, v216\n"
      "v_fma_f32 v217, v202, v228, v217\n"
      "v_lshlrev_b32 v231, 16, v229\n"
      "v_and_b32 v229, 0xffff0000, v229\n"
      "v_fma_f32 v218, v202, v231, v218\n"
      "v_fma_f32 v219, v202, v229, v219\n"
      "v_cvt_pk_bf16_f32 v72, v208, v209\n"
      "v_cvt_pk_bf16_f32 v73, v210, v211\n"
      "v_cvt_pk_bf16_f32 v74, v216, v217\n"
      "v_cvt_pk_bf16_f32 v75, v218, v219\n"
      "ds_write_b128 v8, v[72:75] offset:0\n"
      "ds_read_b128 v[80:83], v9 offset:0\n"
      "v_lshlrev_b32 v227, 16, v226\n"
      "v_and_b32 v226, 0xffff0000, v226\n"
      "v_fma_f32 v212, v203, v227, v212\n"
      "v_fma_f32 v213, v203, v226, v213\n"
      "v_lshlrev_b32 v231, 16, v232\n"
      "v_and_b32 v232, 0xffff0000, v232\n"
      "v_fma_f32 v214, v203, v231, v214\n"
      "v_fma_f32 v215, v203, v232, v215\n"
      "v_lshlrev_b32 v227, 16, v230\n"
      "v_and_b32 v230, 0xffff0000, v230\n"
      "v_fma_f32 v220, v203, v227, v220\n"
      "v_fma_f32 v221, v203, v230, v221\n"
      "v_lshlrev_b32 v231, 16, v233\n"
      "v_and_b32 v233, 0xffff0000, v233\n"
      "v_fma_f32 v222, v203, v231, v222\n"
      "v_fma_f32 v223, v203, v233, v223\n"
      "v_cvt_pk_bf16_f32 v76, v212, v213\n"
      "v_cvt_pk_bf16_f32 v77, v214, v215\n"
      "v_cvt_pk_bf16_f32 v78, v220, v221\n"
      "v_cvt_pk_bf16_f32 v79, v222, v223\n"
      "ds_write_b128 v8, v[76:79] offset:1024\n"
      "ds_read_b128 v[84:87], v9 offset:1024\n"
      "s_waitcnt lgkmcnt(2)\n"
      "global_store_dwordx4 v10, v[80:83], s[20:21]\n"
      "s_waitcnt lgkmcnt(0)\n"
      "global_store_dwordx4 v10, v[84:87], s[68:69]\n"
      "s_endpgm\n"
      ".Lp_s2_%=:\n"
      "s_cmp_eq_u32 s38, 0\n"
      "s_cbranch_scc1 .Lp_o3_%=\n"
      "s_sub_u32 s38, s38, 1\n"
      "s_sleep 4\n"
      "s_nop 7\n"
      "global_load_dword v253, v254, s[58:59] offset:0 sc1\n"
      "s_waitcnt vmcnt(0)\n"
      "v_cmp_ne_u32 vcc, s37, v253\n"
      "s_nop 1\n"
      "s_cbranch_vccnz .Lp_s2_%=\n"
      "s_nop 7\n"
      "s_branch .Lp_o3_%=\n"
      ".Lp_s5_%=:\n"
      "s_cmp_eq_u32 s38, 0\n"
      "s_cbranch_scc1 .Lp_o6_%=\n"
      "s_sub_u32 s38, s38, 1\n"
      "s_sleep 4\n"
      "s_nop 7\n"
      "global_load_dword v253, v254, s[58:59] offset:128 sc1\n"
      "s_waitcnt vmcnt(0)\n"
      "v_cmp_ne_u32 vcc, s37, v253\n"
      "s_nop 1\n"
      "s_cbranch_vccnz .Lp_s5_%=\n"
      "s_nop 7\n"
      "s_branch .Lp_o6_%=\n"
      ".Lp_s8_%=:\n"
      "s_cmp_eq_u32 s38, 0\n"
      "s_cbranch_scc1 .Lp_o9_%=\n"
      "s_sub_u32 s38, s38, 1\n"
      "s_sleep 2\n"
      "s_nop 7\n"
      "global_load_dwordx2 v[202:203], v200, s[58:59] sc1\n"
      "s_waitcnt vmcnt(0)\n"
      "v_cmp_ne_u32 vcc, s37, v203\n"
      "s_nop 1\n"
      "s_cbranch_vccnz .Lp_s8_%=\n"
      "s_nop 7\n"
      "s_branch .Lp_o9_%=\n"
      ".Lp_s12_%=:\n"
      "s_cmp_eq_u32 s38, 0\n"
      "s_cbranch_scc1 .Lp_o13_%=\n"
      "s_sub_u32 s38, s38, 1\n"
      "s_sleep 1\n"
      "s_nop 7\n"
      "global_load_dwordx2 v[204:205], v198, s[58:59] offset:0 sc1\n"
      "global_load_dwordx2 v[206:207], v198, s[58:59] offset:256 sc1\n"
      "s_waitcnt vmcnt(0)\n"
      "v_bitop3_b32 v197, v205, v207, s37 bitop3:0x7e\n"
      "v_cmp_ne_u32 vcc, 0, v197\n"
      "s_nop 1\n"
      "s_cbranch_vccnz .Lp_s12_%=\n"
      "s_nop 7\n"
      "s_branch .Lp_o13_%=\n"
      ".Lp_s16_%=:\n"
      "s_cmp_eq_u32 s38, 0\n"
      "s_cbranch_scc1 .Lp_o17_%=\n"
      "s_sub_u32 s38, s38, 1\n"
      "s_nop 7\n"
      "global_load_dwordx4 v[224:227], v253, s[62:63] offset:0 sc1\n"
      "global_load_dwordx4 v[228:231], v253, s[62:63] offset:1024 sc1\n"
      "global_load_dwordx4 v[232:235], v253, s[62:63] offset:2048 sc1\n"
      "s_waitcnt vmcnt(0)\n"
      "v_bitop3_b32 v234, v227, v231, s37 bitop3:0x7e\n"
      "v_bitop3_b32 v234, v234, v235, s37 bitop3:0xf6\n"
      "v_cmp_ne_u32 vcc, 0, v234\n"
      "s_nop 1\n"
      "s_cbranch_vccnz .Lp_s16_%=\n"
      "s_nop 7\n"
      "s_branch .Lp_o17_%=\n"
      ".Lp_s20_%=:\n"
      "s_cmp_eq_u32 s38, 0\n"
      "s_cbranch_scc1 .Lp_o21_%=\n"
      "s_sub_u32 s38, s38, 1\n"
      "s_nop 7\n"
      "global_load_dwordx4 v[12:15], v254, s[62:63] offset:0 sc1\n"
      "global_load_dwordx4 v[16:19], v254, s[62:63] offset:1024 sc1\n"
      "global_load_dwordx4 v[20:23], v254, s[62:63] offset:2048 sc1\n"
      "s_waitcnt vmcnt(0)\n"
      "v_bitop3_b32 v22, v15, v19, s37 bitop3:0x7e\n"
      "v_bitop3_b32 v22, v22, v23, s37 bitop3:0xf6\n"
      "v_cmp_ne_u32 vcc, 0, v22\n"
      "s_nop 1\n"
      "s_cbranch_vccnz .Lp_s20_%=\n"
      "s_nop 7\n"
      "s_branch .Lp_o21_%=\n"
      ".Lp_s24_%=:\n"
      "s_cmp_eq_u32 s38, 0\n"
      "s_cbranch_scc1 .Lp_o25_%=\n"
      "s_sub_u32 s38, s38, 1\n"
      "s_sleep 2\n"
      "s_nop 7\n"
      "global_load_dwordx4 v[32:35], v60, s[58:59] offset:0 sc1\n"
      "global_load_dwordx4 v[36:39], v60, s[58:59] offset:1024 sc1\n"
      "global_load_dwordx4 v[40:43], v60, s[58:59] offset:2048 sc1\n"
      "global_load_dwordx4 v[44:47], v60, s[58:59] offset:3072 sc1\n"
      "s_waitcnt vmcnt(0)\n"
      "v_bitop3_b32 v61, v33, v35, s37 bitop3:0x7e\n"
      "v_bitop3_b32 v61, v61, v37, s37 bitop3:0xf6\n"
      "v_bitop3_b32 v61, v61, v39, s37 bitop3:0xf6\n"
      "v_bitop3_b32 v61, v61, v41, s37 bitop3:0xf6\n"
      "v_bitop3_b32 v61, v61, v43, s37 bitop3:0xf6\n"
      "v_bitop3_b32 v61, v61, v45, s37 bitop3:0xf6\n"
      "v_bitop3_b32 v61, v61, v47, s37 bitop3:0xf6\n"
      "v_cmp_ne_u32 vcc, 0, v61\n"
      "s_nop 1\n"
      "s_cbranch_vccnz .Lp_s24_%=\n"
      "s_nop 7\n"
      "s_branch .Lp_o25_%=\n"
      ".Lp_s30_%=:\n"
      "s_cmp_eq_u32 s38, 0\n"
      "s_cbranch_scc1 .Lp_o31_%=\n"
      "s_sub_u32 s38, s38, 1\n"
      "s_nop 7\n"
      "global_load_dwordx4 v[224:227], v253, s[62:63] offset:0 sc1\n"
      "global_load_dwordx4 v[228:231], v253, s[62:63] offset:1024 sc1\n"
      "global_load_dwordx4 v[232:235], v253, s[62:63] offset:2048 sc1\n"
      "s_waitcnt vmcnt(0)\n"
      "v_bitop3_b32 v234, v227, v231, s37 bitop3:0x7e\n"
      "v_bitop3_b32 v234, v234, v235, s37 bitop3:0xf6\n"
      "v_cmp_ne_u32 vcc, 0, v234\n"
      "s_nop 1\n"
      "s_cbranch_vccnz .Lp_s30_%=\n"
      "s_nop 7\n"
      "s_branch .Lp_o31_%=\n"
      ".Ls_s3_%=:\n"
      "s_cmp_eq_u32 s38, 0\n"
      "s_cbranch_scc1 .Ls_o4_%=\n"
      "s_sub_u32 s38, s38, 1\n"
      "s_sleep 4\n"
      "s_nop 7\n"
      "global_load_dword v253, v254, s[58:59] offset:0 sc1\n"
      "s_waitcnt vmcnt(0)\n"
      "v_cmp_ne_u32 vcc, s37, v253\n"
      "s_nop 1\n"
      "s_cbranch_vccnz .Ls_s3_%=\n"
      "s_nop 7\n"
      "s_branch .Ls_o4_%=\n"
      ".Ls_s6_%=:\n"
      "s_cmp_eq_u32 s38, 0\n"
      "s_cbranch_scc1 .Ls_o7_%=\n"
      "s_sub_u32 s38, s38, 1\n"
      "s_sleep 2\n"
      "s_nop 7\n"
      "global_load_dwordx2 v[202:203], v200, s[58:59] sc1\n"
      "s_waitcnt vmcnt(0)\n"
      "v_cmp_ne_u32 vcc, s37, v203\n"
      "s_nop 1\n"
      "s_cbranch_vccnz .Ls_s6_%=\n"
      "s_nop 7\n"
      "s_branch .Ls_o7_%=\n"
      ".Ls_s10_%=:\n"
      "s_cmp_eq_u32 s38, 0\n"
      "s_cbranch_scc1 .Ls_o11_%=\n"
      "s_sub_u32 s38, s38, 1\n"
      "s_sleep 1\n"
      "s_nop 7\n"
      "global_load_dwordx2 v[204:205], v102, s[58:59] offset:0 sc1\n"
      "global_load_dwordx2 v[206:207], v102, s[58:59] offset:256 sc1\n"
      "s_waitcnt vmcnt(0)\n"
      "v_bitop3_b32 v101, v205, v207, s37 bitop3:0x7e\n"
      "v_cmp_ne_u32 vcc, 0, v101\n"
      "s_nop 1\n"
      "s_cbranch_vccnz .Ls_s10_%=\n"
      "s_nop 7\n"
      "s_branch .Ls_o11_%=\n"
      ".Ls_s14_%=:\n"
      "s_cmp_eq_u32 s38, 0\n"
      "s_cbranch_scc1 .Ls_o15_%=\n"
      "s_sub_u32 s38, s38, 1\n"
      "s_sleep 4\n"
      "s_nop 7\n"
      "global_load_dword v253, v254, s[58:59] offset:128 sc1\n"
      "s_waitcnt vmcnt(0)\n"
      "v_cmp_ne_u32 vcc, s37, v253\n"
      "s_nop 1\n"
      "s_cbranch_vccnz .Ls_s14_%=\n"
      "s_nop 7\n"
      "s_branch .Ls_o15_%=\n"
      ".Ls_s17_%=:\n"
      "s_cmp_eq_u32 s38, 0\n"
      "s_cbranch_scc1 .Ls_o18_%=\n"
      "s_sub_u32 s38, s38, 1\n"
      "s_sleep 8\n"
      "s_nop 7\n"
      "global_load_dwordx4 v[224:227], v200, s[58:59] offset:0 sc1\n"
      "global_load_dwordx4 v[228:231], v200, s[58:59] offset:1024 sc1\n"
      "global_load_dwordx4 v[232:235], v200, s[58:59] offset:2048 sc1\n"
      "global_load_dwordx4 v[236:239], v200, s[58:59] offset:3072 sc1\n"
      "s_waitcnt vmcnt(0)\n"
      "v_bitop3_b32 v192, v225, v227, s37 bitop3:0x7e\n"
      "v_bitop3_b32 v192, v192, v229, s37 bitop3:0xf6\n"
      "v_bitop3_b32 v192, v192, v231, s37 bitop3:0xf6\n"
      "v_bitop3_b32 v192, v192, v233, s37 bitop3:0xf6\n"
      "v_bitop3_b32 v192, v192, v235, s37 bitop3:0xf6\n"
      "v_bitop3_b32 v192, v192, v237, s37 bitop3:0xf6\n"
      "v_bitop3_b32 v192, v192, v239, s37 bitop3:0xf6\n"
      "v_cmp_ne_u32 vcc, 0, v192\n"
      "s_nop 1\n"
      "s_cbranch_vccnz .Ls_s17_%=\n"
      "s_nop 7\n"
      "s_branch .Ls_o18_%=\n"
      ".Ls_s20_%=:\n"
      "s_cmp_eq_u32 s38, 0\n"
      "s_cbranch_scc1 .Ls_o21_%=\n"
      "s_sub_u32 s38, s38, 1\n"
      "s_nop 7\n"
      "global_load_dwordx4 v[224:227], v253, s[62:63] offset:0 sc1\n"
      "global_load_dwordx4 v[228:231], v253, s[62:63] offset:1024 sc1\n"
      "global_load_dwordx4 v[232:235], v253, s[62:63] offset:2048 sc1\n"
      "s_waitcnt vmcnt(0)\n"
      "v_bitop3_b32 v234, v227, v231, s37 bitop3:0x7e\n"
      "v_bitop3_b32 v234, v234, v235, s37 bitop3:0xf6\n"
      "v_cmp_ne_u32 vcc, 0, v234\n"
      "s_nop 1\n"
      "s_cbranch_vccnz .Ls_s20_%=\n"
      "s_nop 7\n"
      "s_branch .Ls_o21_%=\n"
      ".Ls_s25_%=:\n"
      "s_cmp_eq_u32 s38, 0\n"
      "s_cbranch_scc1 .Ls_o26_%=\n"
      "s_sub_u32 s38, s38, 1\n"
      "s_sleep 2\n"
      "s_nop 7\n"
      "global_load_dwordx4 v[32:35], v60, s[58:59] offset:0 sc1\n"
      "global_load_dwordx4 v[36:39], v60, s[58:59] offset:1024 sc1\n"
      "global_load_dwordx4 v[40:43], v60, s[58:59] offset:2048 sc1\n"
      "global_load_dwordx4 v[44:47], v60, s[58:59] offset:3072 sc1\n"
      "s_waitcnt vmcnt(0)\n"
      "v_bitop3_b32 v61, v33, v35, s37 bitop3:0x7e\n"
      "v_bitop3_b32 v61, v61, v37, s37 bitop3:0xf6\n"
      "v_bitop3_b32 v61, v61, v39, s37 bitop3:0xf6\n"
      "v_bitop3_b32 v61, v61, v41, s37 bitop3:0xf6\n"
      "v_bitop3_b32 v61, v61, v43, s37 bitop3:0xf6\n"
      "v_bitop3_b32 v61, v61, v45, s37 bitop3:0xf6\n"
      "v_bitop3_b32 v61, v61, v47, s37 bitop3:0xf6\n"
      "v_cmp_ne_u32 vcc, 0, v61\n"
      "s_nop 1\n"
      "s_cbranch_vccnz .Ls_s25_%=\n"
      "s_nop 7\n"
      "s_branch .Ls_o26_%=\n"
      ".Ls_s30_%=:\n"
      "s_cmp_eq_u32 s38, 0\n"
      "s_cbranch_scc1 .Ls_o31_%=\n"
      "s_sub_u32 s38, s38, 1\n"
      "s_nop 7\n"
      "global_load_dwordx4 v[12:15], v254, s[62:63] offset:0 sc1\n"
      "global_load_dwordx4 v[16:19], v254, s[62:63] offset:1024 sc1\n"
      "global_load_dwordx4 v[20:23], v254, s[62:63] offset:2048 sc1\n"
      "s_waitcnt vmcnt(0)\n"
      "v_bitop3_b32 v22, v15, v19, s37 bitop3:0x7e\n"
      "v_bitop3_b32 v22, v22, v23, s37 bitop3:0xf6\n"
      "v_cmp_ne_u32 vcc, 0, v22\n"
      "s_nop 1\n"
      "s_cbranch_vccnz .Ls_s30_%=\n"
      "s_nop 7\n"
      "s_branch .Ls_o31_%=\n"
      ".Ls_s34_%=:\n"
      "s_cmp_eq_u32 s38, 0\n"
      "s_cbranch_scc1 .Ls_o35_%=\n"
      "s_sub_u32 s38, s38, 1\n"
      "s_nop 7\n"
      "global_load_dwordx4 v[224:227], v253, s[62:63] offset:0 sc1\n"
      "global_load_dwordx4 v[228:231], v253, s[62:63] offset:1024 sc1\n"
      "global_load_dwordx4 v[232:235], v253, s[62:63] offset:2048 sc1\n"
      "s_waitcnt vmcnt(0)\n"
      "v_bitop3_b32 v234, v227, v231, s37 bitop3:0x7e\n"
      "v_bitop3_b32 v234, v234, v235, s37 bitop3:0xf6\n"
      "v_cmp_ne_u32 vcc, 0, v234\n"
      "s_nop 1\n"
      "s_cbranch_vccnz .Ls_s34_%=\n"
      "s_nop 7\n"
      "s_branch .Ls_o35_%=\n"
      ".Lend_%=:\n"
      :
      : "s"(__builtin_amdgcn_kernarg_segment_ptr()), "s"(blockIdx.x), "s"(__builtin_amdgcn_readfirstlane(threadIdx.x >> 6)),
        "s"((unsigned)(size_t)smem)
      : "memory", "scc", "vcc",
        "v0", "v1", "v2", "v3", "v4", "v5", "v6", "v7", "v8", "v9", "v10", "v11", "v12", "v13", "v14", "v15", "v16", "v17", "v18", "v19", "v20", "v21", "v22", "v23", "v24", "v25", "v26", "v27", "v28", "v29", "v30", "v31", "v32", "v33", "v34", "v35", "v36", "v37", "v38", "v39", "v40", "v41", "v42", "v43", "v44", "v45", "v46", "v47", "v48", "v49", "v50", "v51", "v52", "v53", "v54", "v55", "v56", "v57", "v58", "v59", "v60", "v61", "v62", "v63", "v64", "v65", "v66", "v67", "v68", "v69", "v70", "v71", "v72", "v73", "v74", "v75", "v76", "v77", "v78", "v79", "v80", "v81", "v82", "v83", "v84", "v85", "v86", "v87", "v88", "v89", "v90", "v91", "v92", "v93", "v94", "v95", "v96", "v97", "v98", "v99", "v100", "v101", "v102", "v103", "v104", "v105", "v106", "v107", "v108", "v109", "v110", "v111", "v112", "v113", "v114", "v115", "v116", "v117", "v118", "v119", "v120", "v121", "v122", "v123", "v124", "v125", "v126", "v127", "v128", "v129", "v130", "v131", "v132", "v133", "v134", "v135", "v136", "v137", "v138", "v139", "v140", "v141", "v142", "v143", "v144", "v145", "v146", "v147", "v148", "v149", "v150", "v151", "v152", "v153", "v154", "v155", "v156", "v157", "v158", "v159", "v160", "v161", "v162", "v163", "v164", "v165", "v166", "v167", "v168", "v169", "v170", "v171", "v172", "v173", "v174", "v175", "v176", "v177", "v178", "v179", "v180", "v181", "v182", "v183", "v184", "v185", "v186", "v187", "v188", "v189", "v190", "v191", "v192", "v193", "v194", "v195", "v196", "v197", "v198", "v199", "v200", "v201", "v202", "v203", "v204", "v205", "v206", "v207", "v208", "v209", "v210", "v211", "v212", "v213", "v214", "v215", "v216", "v217", "v218", "v219", "v220", "v221", "v222", "v223", "v224", "v225", "v226", "v227", "v228", "v229", "v230", "v231", "v232", "v233", "v234", "v235", "v236", "v237", "v238", "v239", "v240", "v241", "v242", "v243", "v244", "v245", "v246", "v247", "v248", "v249", "v250", "v251", "v252", "v253", "v254", "v255",
        "a0", "a1", "a2", "a3", "a4", "a5", "a6", "a7", "a8", "a9", "a10", "a11", "a12", "a13", "a14", "a15", "a16", "a17", "a18", "a19", "a20", "a21", "a22", "a23", "a24", "a25", "a26", "a27", "a28", "a29", "a30", "a31", "a32", "a33", "a34", "a35", "a36", "a37", "a38", "a39", "a40", "a41", "a42", "a43", "a44", "a45", "a46", "a47", "a48", "a49", "a50", "a51", "a52", "a53", "a54", "a55", "a56", "a57", "a58", "a59", "a60", "a61", "a62", "a63", "a64", "a65", "a66", "a67", "a68", "a69", "a70", "a71", "a72", "a73", "a74", "a75", "a76", "a77", "a78", "a79", "a80", "a81", "a82", "a83", "a84", "a85", "a86", "a87", "a88", "a89", "a90", "a91", "a92", "a93", "a94", "a95", "a96", "a97", "a98", "a99", "a100", "a101", "a102", "a103", "a104", "a105", "a106", "a107", "a108", "a109", "a110", "a111", "a112", "a113", "a114", "a115", "a116", "a117", "a118", "a119", "a120", "a121", "a122", "a123", "a124", "a125", "a126", "a127", "a128", "a129", "a130", "a131", "a132", "a133", "a134", "a135", "a136", "a137", "a138", "a139", "a140", "a141", "a142", "a143", "a144", "a145", "a146", "a147", "a148", "a149", "a150", "a151", "a152", "a153", "a154", "a155", "a156", "a157", "a158", "a159", "a160", "a161", "a162", "a163", "a164", "a165", "a166", "a167", "a168", "a169", "a170", "a171", "a172", "a173", "a174", "a175", "a176", "a177", "a178", "a179", "a180", "a181", "a182", "a183", "a184", "a185", "a186", "a187", "a188", "a189", "a190", "a191", "a192", "a193", "a194", "a195", "a196", "a197", "a198", "a199", "a200", "a201", "a202", "a203", "a204", "a205", "a206", "a207", "a208", "a209", "a210", "a211", "a212", "a213", "a214", "a215", "a216", "a217", "a218", "a219", "a220", "a221", "a222", "a223", "a224", "a225", "a226", "a227", "a228", "a229", "a230", "a231", "a232", "a233", "a234", "a235", "a236", "a237", "a238", "a239", "a240", "a241", "a242", "a243", "a244", "a245", "a246", "a247", "a248", "a249", "a250", "a251", "a252", "a253", "a254", "a255",
        "s8", "s9", "s10", "s11", "s12", "s13", "s14", "s15", "s16", "s17", "s18", "s19", "s20", "s21", "s22", "s23", "s24", "s25", "s26", "s27", "s28", "s29", "s30", "s31", "s32", "s33", "s34", "s35", "s36", "s37", "s38", "s39", "s40", "s41", "s42", "s43", "s44", "s45", "s46", "s47", "s48", "s49", "s50", "s51", "s52", "s53", "s54", "s55", "s56", "s57", "s58", "s59", "s60", "s61", "s62", "s63", "s64", "s65", "s66", "s67", "s68", "s69", "s70", "s71", "s72", "s73", "s74", "s75", "s76", "s77", "s78", "s79", "s80", "s81");
}

extern "C" void kernel_launch(void* const* d_in, const int* in_sizes, int n_in, void* d_out, int out_size, void* d_ws, size_t ws_size,
                              hipStream_t stream) {
  fused_k<<<256, 256, 0, stream>>>((const u16*)d_in[1], (const u16*)d_in[2], (const u16*)d_in[3], (const u16*)d_in[4], (const u16*)d_in[0],
                                   (char*)d_ws, (u16*)d_out, (const u16*)d_in[6], (const u16*)d_in[10], (const u16*)d_in[7],
                                   (const u16*)d_in[8], (const u16*)d_in[9]);
}
```

```cpp
#include <hip/hip_runtime.h>

typedef unsigned short u16;

__global__ __launch_bounds__(256) void fused_k(const u16* wsre, const u16* wsim, const u16* wdre, const u16* wdim, const u16* x, char* ws,
                                               u16* out, const u16* w_r, const u16* gate_w, const u16* w_gate, const u16* w_up,
                                               const u16* w_down) {
  __shared__ __attribute__((aligned(16))) char smem[163840];
  asm volatile(
      ".Lbegin_%=:\n"
      "s_load_dwordx16 s[8:23], %0, 0\n"
      "s_load_dwordx8 s[24:31], %0, 64\n"
      "v_mbcnt_lo_u32_b32 v243, -1, 0\n"
      "s_mov_b32 s32, %1\n"
      "v_mbcnt_hi_u32_b32 v243, -1, v243\n"
      "s_mov_b32 s33, %2\n"
      "s_bfe_u32 s34, s32, 0x20001\n"
      "s_bfe_u32 s35, s32, 0x20003\n"
      "v_lshrrev_b32 v236, 3, v243\n"
      "s_lshr_b32 s43, s32, 5\n"
      "v_xor_b32 v237, v243, v236\n"
      "s_and_b32 s36, s32, 1\n"
      "v_and_b32 v237, 7, v237\n"
      "s_lshl3_add_u32 s36, s36, s43\n"
      "v_lshlrev_b32 v237, 4, v237\n"
      "s_add_u32 s44, s43, s35\n"
      "v_lshlrev_b32 v242, 4, v243\n"
      "s_lshl_b32 s41, s33, 14\n"
      "s_bitcmp1_b32 s44, 0\n"
      "s_cbranch_scc1 .Lstream_%=\n"
      "v_and_b32 v238, 3, v236\n"
      "s_lshr_b32 s43, s43, 1\n"
      "v_and_b32 v239, 4, v236\n"
      "s_add_u32 s80, s43, s35\n"
      "v_lshl_or_b32 v238, v239, 1, v238\n"
      "s_lshl_b32 s80, s80, 8\n"
      "v_lshl_or_b32 v240, v238, 10, v237\n"
      "s_and_b32 s80, s80, 0x300\n"
      "v_add_u32 v241, 15360, v240\n"
      "s_lshr_b32 s83, s36, 1\n"
      "v_add_u32 v254, 2048, v240\n"
      "s_and_b32 s44, s34, 1\n"
      "v_add_u32 v255, 17408, v240\n"
      "s_lshl3_add_u32 s44, s44, s83\n"
      "s_lshl2_add_u32 s40, s44, s33\n"
      "s_lshl_b32 s44, s40, 15\n"
      "s_lshl_b32 s81, s33, 7\n"
      "s_add_u32 s81, s81, s80\n"
      "s_and_b32 s81, s81, 0x380\n"
      "s_lshl_b32 s45, s35, 17\n"
      "s_lshl_b32 s82, s33, 12\n"
      "s_bitcmp1_b32 s34, 1\n"
      "s_waitcnt lgkmcnt(0)\n"
      "s_cselect_b64 s[12:13], s[14:15], s[12:13]\n"
      "s_add_u32 s68, s16, s44\n"
      "s_addc_u32 s69, s17, 0\n"
      "s_add_u32 s12, s12, s45\n"
      "s_addc_u32 s13, s13, 0\n"
      "s_add_u32 s45, s34, 1\n"
      "s_and_b32 s45, s45, 3\n"
      "s_lshl_b32 s45, s45, 15\n"
      "s_or_b32 s45, s45, s81\n"
      "s_add_u32 s46, s12, s45\n"
      "s_addc_u32 s47, s13, 0\n"
      "s_add_u32 m0, s82, 65536\n"
      "s_nop 0\n"
      "global_load_lds_dwordx4 v240, s[46:47] offset:0\n"
      "global_load_lds_dwordx4 v241, s[46:47] offset:1024\n"
      "global_load_lds_dwordx4 v254, s[46:47] offset:2048\n"
      "global_load_lds_dwordx4 v255, s[46:47] offset:3072\n"
      "s_add_u32 s46, s68, s80\n"
      "s_addc_u32 s47, s69, 0\n"
      "s_add_u32 m0, s41, 0\n"
      "s_nop 0\n"
      "global_load_lds_dwordx4 v240, s[46:47] offset:0\n"
      "global_load_lds_dwordx4 v241, s[46:47] offset:1024\n"
      "global_load_lds_dwordx4 v254, s[46:47] offset:2048\n"
      "global_load_lds_dwordx4 v255, s[46:47] offset:3072\n"
      "s_add_u32 s45, s34, 2\n"
      "s_and_b32 s45, s45, 3\n"
      "s_lshl_b32 s45, s45, 15\n"
      "s_or_b32 s45, s45, s81\n"
      "s_add_u32 s46, s12, s45\n"
      "s_addc_u32 s47, s13, 0\n"
      "s_add_u32 m0, s82, 98304\n"
      "s_nop 0\n"
      "global_load_lds_dwordx4 v240, s[46:47] offset:0\n"
      "global_load_lds_dwordx4 v241, s[46:47] offset:1024\n"
      "s_add_u32 s45, s80, 128\n"
      "s_and_b32 s45, s45, 0x380\n"
      "s_add_u32 s46, s68, s45\n"
      "s_addc_u32 s47, s69, 0\n"
      "s_add_u32 m0, s41, 4096\n"
      "s_nop 0\n"
      "global_load_lds_dwordx4 v240, s[46:47] offset:0\n"
      "global_load_lds_dwordx4 v241, s[46:47] offset:1024\n"
      "global_load_lds_dwordx4 v254, s[46:47] offset:2048\n"
      "global_load_lds_dwordx4 v255, s[46:47] offset:3072\n"
      "s_branch .Lprod_%=\n"
      ".Lstream_%=:\n"
      "v_lshl_or_b32 v244, v236, 12, v237\n"
      "v_add_u32 v245, 31744, v244\n"
      "v_add_u32 v246, 63488, v244\n"
      "v_add_u32 v247, 95232, v244\n"
      "s_getpc_b64 s[46:47]\n"
      ".Lhere_%=:\n"
      "s_lshr_b32 s44, s32, 4\n"
      "s_lshl2_add_u32 s44, s44, s33\n"
      "s_lshl_b32 s44, s44, 10\n"
      "s_sub_u32 s46, s46, .Lhere_%=-.Lbegin_%=\n"
      "s_subb_u32 s47, s47, 0\n"
      "v_add_u32 v238, s44, v242\n"
      "v_min_u32 v238, .Lend_%=-.Lbegin_%=-16, v238\n"
      "global_load_dwordx4 v[192:195], v238, s[46:47]\n"
      "s_waitcnt lgkmcnt(0)\n"
      "s_bitcmp1_b32 s34, 1\n"
      "s_cselect_b64 s[8:9], s[10:11], s[8:9]\n"
      "s_and_b32 s45, s34, 1\n"
      "s_lshl2_add_u32 s45, s45, s33\n"
      "s_lshl_b32 s45, s45, 9\n"
      "s_lshl_b32 s76, s36, 19\n"
      "s_add_u32 s45, s45, s76\n"
      "s_add_u32 s8, s8, s45\n"
      "s_addc_u32 s9, s9, 0\n"
      "s_lshr_b32 s39, s32, 6\n"
      "s_and_b32 s45, s35, 2\n"
      "s_xor_b32 s39, s39, s45\n"
      "s_lshl_b32 s45, s39, 7\n"
      "s_add_u32 s76, s33, 0\n"
      "s_and_b32 s76, s76, 3\n"
      "s_lshl_b32 s76, s76, 17\n"
      "s_or_b32 s76, s76, s45\n"
      "s_add_u32 s77, s33, 1\n"
      "s_and_b32 s77, s77, 3\n"
      "s_lshl_b32 s77, s77, 17\n"
      "s_or_b32 s77, s77, s45\n"
      "s_add_u32 s78, s33, 2\n"
      "s_and_b32 s78, s78, 3\n"
      "s_lshl_b32 s78, s78, 17\n"
      "s_or_b32 s78, s78, s45\n"
      "s_add_u32 s79, s33, 3\n"
      "s_and_b32 s79, s79, 3\n"
      "s_lshl_b32 s79, s79, 17\n"
      "s_or_b32 s79, s79, s45\n"
      "s_sleep 20\n"
      "s_bitcmp1_b32 s34, 1\n"
      "s_cselect_b64 s[46:47], s[14:15], s[12:13]\n"
      "s_min_u32 s44, s33, 2\n"
      "s_add_u32 s44, s44, s34\n"
      "s_add_u32 s44, s44, 2\n"
      "s_and_b32 s44, s44, 3\n"
      "s_lshl2_add_u32 s44, s35, s44\n"
      "s_lshr_b32 s45, s32, 6\n"
      "s_lshl2_add_u32 s44, s44, s45\n"
      "s_lshl_b32 s44, s44, 13\n"
      "v_lshl_add_u32 v238, v243, 7, s44\n"
      "s_cmp_eq_u32 s33, 3\n"
      "s_cbranch_scc1 .Ls_e1_%=\n"
      "global_load_dword v196, v238, s[46:47]\n"
      ".Ls_e1_%=:\n"
      "s_xor_b32 s45, s76, 0\n"
      "s_add_u32 s46, s8, s45\n"
      "s_addc_u32 s47, s9, 0\n"
      "s_add_u32 m0, s41, 0\n"
      "s_nop 0\n"
      "global_load_lds_dwordx4 v244, s[46:47] offset:0\n"
      "global_load_lds_dwordx4 v245, s[46:47] offset:1024\n"
      "global_load_lds_dwordx4 v246, s[46:47] offset:2048\n"
      "global_load_lds_dwordx4 v247, s[46:47] offset:3072\n"
      "s_xor_b32 s45, s77, 0\n"
      "s_add_u32 s46, s8, s45\n"
      "s_addc_u32 s47, s9, 0\n"
      "s_add_u32 m0, s41, 4096\n"
      "s_nop 0\n"
      "global_load_lds_dwordx4 v244, s[46:47] offset:0\n"
      "global_load_lds_dwordx4 v245, s[46:47] offset:1024\n"
      "global_load_lds_dwordx4 v246, s[46:47] offset:2048\n"
      "global_load_lds_dwordx4 v247, s[46:47] offset:3072\n"
      "s_xor_b32 s45, s78, 0\n"
      "s_add_u32 s46, s8, s45\n"
      "s_addc_u32 s47, s9, 0\n"
      "s_add_u32 m0, s41, 8192\n"
      "s_nop 0\n"
      "global_load_lds_dwordx4 v244, s[46:47] offset:0\n"
      "global_load_lds_dwordx4 v245, s[46:47] offset:1024\n"
      "global_load_lds_dwordx4 v246, s[46:47] offset:2048\n"
      "global_load_lds_dwordx4 v247, s[46:47] offset:3072\n"
      "s_add_u32 s58, s18, 16777216\n"
      "s_addc_u32 s59, s19, 0\n"
      "v_mov_b32 v235, 0\n"
      "global_load_dword v235, v235, s[58:59] sc1\n"
      "v_and_b32 v238, 15, v243\n"
      "v_lshrrev_b32 v239, 4, v243\n"
      "v_xor_b32 v239, v239, v238\n"
      "v_and_b32 v239, 7, v239\n"
      "v_lshlrev_b32 v239, 4, v239\n"
      "v_lshl_or_b32 v238, v238, 7, v239\n"
      "v_add_u32 v248, s41, v238\n"
      "v_xor_b32 v249, 64, v248\n"
      "s_lshl2_add_u32 s43, s34, s35\n"
      "s_lshr_b32 s45, s39, 1\n"
      "s_lshl1_add_u32 s45, s33, s45\n"
      "s_lshl_b32 s45, s45, 15\n"
      "s_lshl_b32 s44, s43, 18\n"
      "s_add_u32 s44, s44, s45\n"
      "s_add_u32 s44, s44, 4096\n"
      "s_add_u32 s10, s18, s44\n"
      "s_addc_u32 s11, s19, 0\n"
      "s_xor_b32 s44, s44, 0x8000\n"
      "s_add_u32 s14, s18, s44\n"
      "s_addc_u32 s15, s19, 0\n"
      "s_and_b32 s44, s39, 1\n"
      "s_lshl_b32 s45, s44, 14\n"
      "v_add_u32 v240, s45, v242\n"
      "v_xor_b32 v241, 0x4000, v240\n"
      "s_add_u32 s72, s10, 0x2000\n"
      "s_addc_u32 s73, s11, 0\n"
      "s_add_u32 s74, s14, 0x2000\n"
      "s_addc_u32 s75, s15, 0\n"
      "s_lshl2_add_u32 s44, s43, s33\n"
      "s_lshl_b32 s44, s44, 12\n"
      "s_add_u32 s44, s44, 1048576\n"
      "v_and_b32 v253, 7, v243\n"
      "v_lshl_add_u32 v253, v253, 9, s44\n"
      "s_lshl2_add_u32 s43, s36, s35\n"
      "s_mul_i32 s43, s43, 147456\n"
      "s_mul_i32 s44, s33, 3072\n"
      "s_add_u32 s43, s43, s44\n"
      "s_add_u32 s43, s43, 6291456\n"
      "s_add_u32 s60, s18, s43\n"
      "s_addc_u32 s61, s19, 0\n"
      "s_mul_i32 s44, s34, 36864\n"
      "s_add_u32 s62, s60, s44\n"
      "s_addc_u32 s63, s61, 0\n"
      "s_lshr_b32 s43, s34, 1\n"
      "s_lshl2_add_u32 s43, s43, s35\n"
      "s_lshl_b32 s43, s43, 12\n"
      "s_add_u32 s43, s43, 4096\n"
      "v_lshl_add_u32 v204, v243, 6, s43\n"
      "s_lshr_b32 s80, s32, 4\n"
      "s_bfe_u32 s43, s32, 0x10002\n"
      "s_lshl1_add_u32 s80, s80, s43\n"
      "s_and_b32 s81, s32, 3\n"
      "s_cmp_lt_u32 s80, 28\n"
      "s_cselect_b32 s82, -1, 0\n"
      "s_lshr_b32 s83, s33, 1\n"
      "s_lshl1_add_u32 s83, s81, s83\n"
      "v_add_u32 v250, 0x10000, v248\n"
      "v_add_u32 v251, 0x10000, v249\n"
      "s_xor_b32 s45, s79, 0\n"
      "s_add_u32 s46, s8, s45\n"
      "s_addc_u32 s47, s9, 0\n"
      "s_add_u32 m0, s41, 12288\n"
      "s_nop 0\n"
      "global_load_lds_dwordx4 v244, s[46:47] offset:0\n"
      "global_load_lds_dwordx4 v245, s[46:47] offset:1024\n"
      "global_load_lds_dwordx4 v246, s[46:47] offset:2048\n"
      "global_load_lds_dwordx4 v247, s[46:47] offset:3072\n"
      "s_xor_b32 s45, s76, 128\n"
      "s_add_u32 s46, s8, s45\n"
      "s_addc_u32 s47, s9, 0\n"
      "s_add_u32 m0, s41, 65536\n"
      "s_nop 0\n"
      "global_load_lds_dwordx4 v244, s[46:47] offset:0\n"
      "global_load_lds_dwordx4 v245, s[46:47] offset:1024\n"
      "global_load_lds_dwordx4 v246, s[46:47] offset:2048\n"
      "global_load_lds_dwordx4 v247, s[46:47] offset:3072\n"
      "s_waitcnt vmcnt(17)\n"
      "ds_read_b128 a[0:3], v248 offset:0\n"
      "ds_read_b128 a[4:7], v248 offset:2048\n"
      "ds_read_b128 a[32:35], v249 offset:0\n"
      "ds_read_b128 a[36:39], v249 offset:2048\n"
      "s_xor_b32 s45, s77, 128\n"
      "s_add_u32 s46, s8, s45\n"
      "s_addc_u32 s47, s9, 0\n"
      "s_add_u32 m0, s41, 69632\n"
      "s_nop 0\n"
      "global_load_lds_dwordx4 v244, s[46:47] offset:0\n"
      "global_load_lds_dwordx4 v245, s[46:47] offset:1024\n"
      "global_load_lds_dwordx4 v246, s[46:47] offset:2048\n"
      "global_load_lds_dwordx4 v247, s[46:47] offset:3072\n"
      "s_waitcnt vmcnt(17)\n"
      "ds_read_b128 a[8:11], v248 offset:4096\n"
      "ds_read_b128 a[12:15], v248 offset:6144\n"
      "ds_read_b128 a[40:43], v249 offset:4096\n"
      "ds_read_b128 a[44:47], v249 offset:6144\n"
      "s_xor_b32 s45, s78, 128\n"
      "s_add_u32 s46, s8, s45\n"
      "s_addc_u32 s47, s9, 0\n"
      "s_add_u32 m0, s41, 73728\n"
      "s_nop 0\n"
      "global_load_lds_dwordx4 v244, s[46:47] offset:0\n"
      "global_load_lds_dwordx4 v245, s[46:47] offset:1024\n"
      "global_load_lds_dwordx4 v246, s[46:47] offset:2048\n"
      "global_load_lds_dwordx4 v247, s[46:47] offset:3072\n"
      "s_waitcnt vmcnt(17)\n"
      "ds_read_b128 a[16:19], v248 offset:8192\n"
      "ds_read_b128 a[20:23], v248 offset:10240\n"
      "ds_read_b128 a[48:51], v249 offset:8192\n"
      "ds_read_b128 a[52:55], v249 offset:10240\n"
      "s_waitcnt vmcnt(16)\n"
      "v_readfirstlane_b32 s37, v235\n"
      "s_nop 1\n"
      "s_mul_i32 s37, s37, 0x9E3779B1\n"
      "s_add_u32 s37, s37, 0x7F4A7C15\n"
      "s_mov_b32 s38, 0x8000\n"
      "s_waitcnt lgkmcnt(8)\n"
      "s_xor_b32 s45, s79, 128\n"
      "s_add_u32 s46, s8, s45\n"
      "s_addc_u32 s47, s9, 0\n"
      "s_add_u32 m0, s41, 0\n"
      "s_nop 0\n"
      "global_load_lds_dwordx4 v244, s[46:47] offset:0\n"
      "global_load_lds_dwordx4 v245, s[46:47] offset:1024\n"
      "global_load_lds_dwordx4 v246, s[46:47] offset:2048\n"
      "global_load_lds_dwordx4 v247, s[46:47] offset:3072\n"
      "s_min_u32 s43, s80, 27\n"
      "v_lshrrev_b32 v202, 3, v243\n"
      "v_and_b32 v203, 0x70, v242\n"
      "v_lshl_or_b32 v200, v202, 10, v203\n"
      "v_add_u32 v201, 7168, v200\n"
      "v_add_u32 v202, 14336, v200\n"
      "v_add_u32 v203, 21504, v200\n"
      "s_lshr_b32 s44, s33, 1\n"
      "s_lshl1_add_u32 s44, s43, s44\n"
      "s_lshl_b32 s44, s44, 15\n"
      "s_and_b32 s45, s33, 1\n"
      "s_lshl1_add_u32 s45, s81, s45\n"
      "s_lshl_b32 s45, s45, 7\n"
      "s_add_u32 s44, s44, s45\n"
      "s_add_u32 s70, s30, s44\n"
      "s_addc_u32 s71, s31, 0\n"
      "s_add_u32 m0, s41, 77824\n"
      "s_nop 0\n"
      "global_load_lds_dwordx4 v200, s[70:71] offset:0\n"
      "global_load_lds_dwordx4 v201, s[70:71] offset:1024\n"
      "global_load_lds_dwordx4 v202, s[70:71] offset:2048\n"
      "global_load_lds_dwordx4 v203, s[70:71] offset:3072\n"
      "s_waitcnt vmcnt(20)\n"
      "ds_read_b128 a[24:27], v248 offset:12288\n"
      "ds_read_b128 a[28:31], v248 offset:14336\n"
      "ds_read_b128 a[56:59], v249 offset:12288\n"
      "ds_read_b128 a[60:63], v249 offset:14336\n"
      "s_waitcnt lgkmcnt(8)\n"
      "s_xor_b32 s45, s76, 256\n"
      "s_add_u32 s46, s8, s45\n"
      "s_addc_u32 s47, s9, 0\n"
      "s_add_u32 m0, s41, 4096\n"
      "s_nop 0\n"
      "global_load_lds_dwordx4 v244, s[46:47] offset:0\n"
      "global_load_lds_dwordx4 v245, s[46:47] offset:1024\n"
      "global_load_lds_dwordx4 v246, s[46:47] offset:2048\n"
      "global_load_lds_dwordx4 v247, s[46:47] offset:3072\n"
      "s_waitcnt vmcnt(20)\n"
      "ds_read_b128 a[64:67], v250 offset:0\n"
      "ds_read_b128 a[68:71], v250 offset:2048\n"
      "ds_read_b128 a[96:99], v251 offset:0\n"
      "ds_read_b128 a[100:103], v251 offset:2048\n"
      "s_waitcnt lgkmcnt(8)\n"
      "s_xor_b32 s45, s77, 256\n"
      "s_add_u32 s46, s8, s45\n"
      "s_addc_u32 s47, s9, 0\n"
      "s_add_u32 m0, s41, 8192\n"
      "s_nop 0\n"
      "global_load_lds_dwordx4 v244, s[46:47] offset:0\n"
      "global_load_lds_dwordx4 v245, s[46:47] offset:1024\n"
      "global_load_lds_dwordx4 v246, s[46:47] offset:2048\n"
      "global_load_lds_dwordx4 v247, s[46:47] offset:3072\n"
      "s_waitcnt vmcnt(20)\n"
      "ds_read_b128 a[72:75], v250 offset:4096\n"
      "ds_read_b128 a[76:79], v250 offset:6144\n"
      "ds_read_b128 a[104:107], v251 offset:4096\n"
      "ds_read_b128 a[108:111], v251 offset:6144\n"
      "s_waitcnt lgkmcnt(8)\n"
      "s_xor_b32 s45, s78, 256\n"
      "s_add_u32 s46, s8, s45\n"
      "s_addc_u32 s47, s9, 0\n"
      "s_add_u32 m0, s41, 12288\n"
      "s_nop 0\n"
      "global_load_lds_dwordx4 v244, s[46:47] offset:0\n"
      "global_load_lds_dwordx4 v245, s[46:47] offset:1024\n"
      "global_load_lds_dwordx4 v246, s[46:47] offset:2048\n"
      "global_load_lds_dwordx4 v247, s[46:47] offset:3072\n"
      "s_waitcnt vmcnt(20)\n"
      "ds_read_b128 a[80:83], v250 offset:8192\n"
      "ds_read_b128 a[84:87], v250 offset:10240\n"
      "ds_read_b128 a[112:115], v251 offset:8192\n"
      "ds_read_b128 a[116:119], v251 offset:10240\n"
      "s_waitcnt vmcnt(16)\n"
      "ds_read_b128 a[88:91], v248 offset:0\n"
      "ds_read_b128 a[92:95], v248 offset:2048\n"
      "ds_read_b128 a[120:123], v249 offset:0\n"
      "ds_read_b128 a[124:127], v249 offset:2048\n"
      "global_load_dword v252, v253, s[58:59] offset:0 sc1\n"
      "s_waitcnt lgkmcnt(12)\n"
      "s_xor_b32 s45, s79, 256\n"
      "s_add_u32 s46, s8, s45\n"
      "s_addc_u32 s47, s9, 0\n"
      "s_add_u32 m0, s41, 65536\n"
      "s_nop 0\n"
      "global_load_lds_dwordx4 v244, s[46:47] offset:0\n"
      "global_load_lds_dwordx4 v245, s[46:47] offset:1024\n"
      "global_load_lds_dwordx4 v246, s[46:47] offset:2048\n"
      "global_load_lds_dwordx4 v247, s[46:47] offset:3072\n"
      "s_waitcnt lgkmcnt(8)\n"
      "s_xor_b32 s45, s76, 384\n"
      "s_add_u32 s46, s8, s45\n"
      "s_addc_u32 s47, s9, 0\n"
      "s_add_u32 m0, s41, 69632\n"
      "s_nop 0\n"
      "global_load_lds_dwordx4 v244, s[46:47] offset:0\n"
      "global_load_lds_dwordx4 v245, s[46:47] offset:1024\n"
      "global_load_lds_dwordx4 v246, s[46:47] offset:2048\n"
      "global_load_lds_dwordx4 v247, s[46:47] offset:3072\n"
      "s_waitcnt vmcnt(17)\n"
      "ds_read_b128 a[128:131], v248 offset:4096\n"
      "ds_read_b128 a[132:135], v248 offset:6144\n"
      "ds_read_b128 a[160:163], v249 offset:4096\n"
      "ds_read_b128 a[164:167], v249 offset:6144\n"
      "s_waitcnt vmcnt(13)\n"
      "ds_read_b128 a[136:139], v248 offset:8192\n"
      "ds_read_b128 a[140:143], v248 offset:10240\n"
      "ds_read_b128 a[168:171], v249 offset:8192\n"
      "ds_read_b128 a[172:175], v249 offset:10240\n"
      "s_waitcnt vmcnt(8)\n"
      "v_cmp_ne_u32 vcc, s37, v252\n"
      "s_nop 1\n"
      "s_cbranch_vccnz .Ls_s3_%=\n"
      ".Ls_o4_%=:\n"
      "global_load_dwordx2 v[206:207], v204, s[58:59] sc1\n"
      "global_load_dwordx4 v[128:131], v240, s[10:11] offset:-4096 sc1\n"
      "global_load_dwordx4 v[132:135], v240, s[10:11] offset:-3072 sc1\n"
      "global_load_dwordx4 v[136:139], v240, s[10:11] offset:-2048 sc1\n"
      "global_load_dwordx4 v[140:143], v240, s[72:73] offset:-4096 sc1\n"
      "global_load_dwordx4 v[144:147], v240, s[72:73] offset:-3072 sc1\n"
      "global_load_dwordx4 v[148:151], v240, s[72:73] offset:-2048 sc1\n"
      "s_waitcnt vmcnt(5)\n"
      "v_mfma_f32_16x16x32_bf16 v[0:3], v[128:131], a[0:3], 0\n"
      "global_load_dwordx4 v[152:155], v241, s[10:11] offset:-4096 sc1\n"
      "v_cmp_ne_u32 vcc, s37, v207\n"
      "s_nop 1\n"
      "s_cbranch_vccnz .Ls_s6_%=\n"
      ".Ls_o7_%=:\n"
      "v_mfma_f32_16x16x32_bf16 v[4:7], v[128:131], a[4:7], 0\n"
      "v_max_u32_dpp v206, v206, v206 quad_perm:[1,0,3,2] row_mask:0xf bank_mask:0xf\n"
      "s_nop 1\n"
      "v_max_u32_dpp v206, v206, v206 quad_perm:[2,3,0,1] row_mask:0xf bank_mask:0xf\n"
      "v_mfma_f32_16x16x32_bf16 v[8:11], v[128:131], a[8:11], 0\n"
      "global_load_dwordx4 v[156:159], v241, s[10:11] offset:-3072 sc1\n"
      "v_max_u32_dpp v206, v206, v206 row_half_mirror row_mask:0xf bank_mask:0xf\n"
      "v_mfma_f32_16x16x32_bf16 v[12:15], v[128:131], a[12:15], 0\n"
      "s_nop 0\n"
      "v_max_u32_dpp v206, v206, v206 row_mirror row_mask:0xf bank_mask:0xf\n"
      "v_mfma_f32_16x16x32_bf16 v[16:19], v[128:131], a[16:19], 0\n"
      "global_load_dwordx4 v[160:163], v241, s[10:11] offset:-2048 sc1\n"
      "v_mov_b32 v205, v206\n"
      "v_mfma_f32_16x16x32_bf16 v[20:23], v[128:131], a[20:23], 0\n"
      "s_nop 0\n"
      "v_permlane16_swap_b32 v206, v205\n"
      "v_mfma_f32_16x16x32_bf16 v[24:27], v[128:131], a[24:27], 0\n"
      "global_load_dwordx4 v[164:167], v241, s[72:73] offset:-4096 sc1\n"
      "v_max_u32 v206, v206, v205\n"
      "v_mfma_f32_16x16x32_bf16 v[28:31], v[128:131], a[28:31], 0\n"
      "v_mov_b32 v205, v206\n"
      "s_nop 1\n"
      "v_permlane32_swap_b32 v206, v205\n"
      "s_waitcnt vmcnt(8)\n"
      "v_mfma_f32_16x16x32_bf16 v[32:35], v[132:135], a[0:3], 0\n"
      "global_load_dwordx4 v[168:171], v241, s[72:73] offset:-3072 sc1\n"
      "v_max_u32 v206, v206, v205\n"
      "v_mfma_f32_16x16x32_bf16 v[36:39], v[132:135], a[4:7], 0\n"
      "v_readfirstlane_b32 s40, v206\n"
      "s_nop 1\n"
      "s_andn2_b32 s42, 0x7ff, s40\n"
      "v_mfma_f32_16x16x32_bf16 v[40:43], v[132:135], a[8:11], 0\n"
      "global_load_dwordx4 v[172:175], v241, s[72:73] offset:-2048 sc1\n"
      "s_lshr_b32 s43, s42, 5\n"
      "s_lshl2_add_u32 s44, s36, s33\n"
      "s_cmp_eq_u32 s43, s44\n"
      "s_cbranch_scc0 .Ls_e8_%=\n"
      "s_lshl_b32 s43, s42, 10\n"
      "v_add_u32 v197, s43, v242\n"
      "global_load_dwordx4 v[232:235], v197, s[16:17]\n"
      "global_load_dwordx4 v[236:239], v242, s[24:25]\n"
      ".Ls_e8_%=:\n"
      "v_mfma_f32_16x16x32_bf16 v[44:47], v[132:135], a[12:15], 0\n"
      "s_lshl2_add_u32 s54, s36, s33\n"
      "s_lshl_b32 s55, s54, 15\n"
      "s_lshl2_add_u32 s56, s35, s34\n"
      "v_mfma_f32_16x16x32_bf16 v[48:51], v[132:135], a[16:19], 0\n"
      "s_lshl_b32 s56, s56, 6\n"
      "global_load_dwordx4 v[176:179], v240, s[14:15] offset:-4096 sc1\n"
      "s_add_u32 s55, s55, s56\n"
      "v_mfma_f32_16x16x32_bf16 v[52:55], v[132:135], a[20:23], 0\n"
      "v_and_b32 v198, 15, v243\n"
      "v_and_b32 v199, 48, v243\n"
      "v_mfma_f32_16x16x32_bf16 v[56:59], v[132:135], a[24:27], 0\n"
      "global_load_dwordx4 v[180:183], v240, s[14:15] offset:-3072 sc1\n"
      "v_lshl_or_b32 v199, v198, 10, v199\n"
      "v_mfma_f32_16x16x32_bf16 v[60:63], v[132:135], a[28:31], 0\n"
      "s_waitcnt vmcnt(10)\n"
      "v_add_u32 v199, s55, v199\n"
      "v_add_u32 v197, 16384, v199\n"
      "v_mfma_f32_16x16x32_bf16 v[64:67], v[136:139], a[0:3], 0\n"
      "global_load_dwordx4 v[184:187], v240, s[14:15] offset:-2048 sc1\n"
      "global_load_dwordx4 v[188:191], v240, s[74:75] offset:-4096 sc1\n"
      "v_mfma_f32_16x16x32_bf16 v[68:71], v[136:139], a[4:7], 0\n"
      "global_load_dwordx4 v[128:131], v240, s[74:75] offset:-3072 sc1\n"
      "global_load_dwordx4 v[132:135], v240, s[74:75] offset:-2048 sc1\n"
      "v_mfma_f32_16x16x32_bf16 v[72:75], v[136:139], a[8:11], 0\n"
      "s_waitcnt lgkmcnt(12)\n"
      "s_xor_b32 s70, s77, 384\n"
      "s_add_u32 s70, s8, s70\n"
      "s_addc_u32 s71, s9, 0\n"
      "s_add_u32 m0, s41, 73728\n"
      "v_mfma_f32_16x16x32_bf16 v[76:79], v[136:139], a[12:15], 0\n"
      "global_load_lds_dwordx4 v244, s[70:71] offset:0\n"
      "global_load_dwordx4 v[224:227], v199, s[16:17]\n"
      "v_mfma_f32_16x16x32_bf16 v[80:83], v[136:139], a[16:19], 0\n"
      "global_load_dwordx4 v[228:231], v197, s[16:17]\n"
      "s_lshr_b32 s55, s34, 1\n"
      "v_mfma_f32_16x16x32_bf16 v[84:87], v[136:139], a[20:23], 0\n"
      "s_lshl2_add_u32 s55, s55, s35\n"
      "global_load_lds_dwordx4 v245, s[70:71] offset:1024\n"
      "s_lshl_b32 s55, s55, 15\n"
      "v_mfma_f32_16x16x32_bf16 v[88:91], v[136:139], a[24:27], 0\n"
      "s_lshl_b32 s54, s54, 9\n"
      "s_add_u32 s54, s54, s55\n"
      "s_add_u32 s54, s54, 786432\n"
      "v_mfma_f32_16x16x32_bf16 v[92:95], v[136:139], a[28:31], 0\n"
      "global_load_lds_dwordx4 v246, s[70:71] offset:2048\n"
      "v_lshl_add_u32 v198, v198, 4, s54\n"
      "v_mfma_f32_16x16x32_bf16 v[0:3], v[140:143], a[32:35], v[0:3]\n"
      "global_load_dwordx4 v[136:139], v241, s[14:15] offset:-4096 sc1\n"
      "global_load_dwordx2 v[204:205], v198, s[58:59] offset:0 sc1\n"
      "v_mfma_f32_16x16x32_bf16 v[4:7], v[140:143], a[36:39], v[4:7]\n"
      "global_load_lds_dwordx4 v247, s[70:71] offset:3072\n"
      "global_load_dwordx2 v[206:207], v198, s[58:59] offset:256 sc1\n"
      "v_mfma_f32_16x16x32_bf16 v[8:11], v[140:143], a[40:43], v[8:11]\n"
      "s_add_u32 s68, s20, 16384\n"
      "s_addc_u32 s69, s21, 0\n"
      "s_waitcnt lgkmcnt(8)\n"
      "s_xor_b32 s70, s78, 384\n"
      "s_add_u32 s70, s8, s70\n"
      "s_addc_u32 s71, s9, 0\n"
      "s_add_u32 m0, s41, 0\n"
      "v_mfma_f32_16x16x32_bf16 v[12:15], v[140:143], a[44:47], v[12:15]\n"
      "s_ashr_i32 s54, s40, 31\n"
      "global_load_lds_dwordx4 v244, s[70:71] offset:0\n"
      "s_not_b32 s54, s54\n"
      "v_mfma_f32_16x16x32_bf16 v[16:19], v[140:143], a[48:51], v[16:19]\n"
      "s_or_b32 s54, s54, 0x80000000\n"
      "s_xor_b32 s54, s54, s40\n"
      "s_and_b32 s54, s54, 0xffff0000\n"
      "v_mfma_f32_16x16x32_bf16 v[20:23], v[140:143], a[52:55], v[20:23]\n"
      "global_load_lds_dwordx4 v245, s[70:71] offset:1024\n"
      "v_mov_b32 v192, s54\n"
      "v_mfma_f32_16x16x32_bf16 v[24:27], v[140:143], a[56:59], v[24:27]\n"
      "ds_read_b128 a[144:147], v248 offset:12288\n"
      "v_mul_f32 v192, 0xbfb8aa3b, v192\n"
      "v_mfma_f32_16x16x32_bf16 v[28:31], v[140:143], a[60:63], v[28:31]\n"
      "s_waitcnt vmcnt(23)\n"
      "global_load_lds_dwordx4 v246, s[70:71] offset:2048\n"
      "v_exp_f32 v192, v192\n"
      "v_mfma_f32_16x16x32_bf16 v[32:35], v[144:147], a[32:35], v[32:35]\n"
      "global_load_dwordx4 v[140:143], v241, s[74:75] offset:-4096 sc1\n"
      "v_add_f32 v192, 1.0, v192\n"
      "v_mfma_f32_16x16x32_bf16 v[36:39], v[144:147], a[36:39], v[36:39]\n"
      "global_load_lds_dwordx4 v247, s[70:71] offset:3072\n"
      "v_rcp_f32 v192, v192\n"
      "v_mfma_f32_16x16x32_bf16 v[40:43], v[144:147], a[40:43], v[40:43]\n"
      "s_waitcnt lgkmcnt(5)\n"
      "s_xor_b32 s70, s79, 384\n"
      "s_add_u32 s70, s8, s70\n"
      "s_addc_u32 s71, s9, 0\n"
      "s_add_u32 m0, s41, 4096\n"
      "v_mfma_f32_16x16x32_bf16 v[44:47], v[144:147], a[44:47], v[44:47]\n"
      "global_load_lds_dwordx4 v244, s[70:71] offset:0\n"
      "v_cvt_pk_bf16_f32 v192, v192, v192\n"
      "v_mfma_f32_16x16x32_bf16 v[48:51], v[144:147], a[48:51], v[48:51]\n"
      "ds_read_b128 a[148:151], v248 offset:14336\n"
      "v_and_b32 v192, 0xffff0000, v192\n"
      "v_mfma_f32_16x16x32_bf16 v[52:55], v[144:147], a[52:55], v[52:55]\n"
      "global_load_lds_dwordx4 v245, s[70:71] offset:1024\n"
      "v_readfirstlane_b32 s84, v192\n"
      "v_mfma_f32_16x16x32_bf16 v[56:59], v[144:147], a[56:59], v[56:59]\n"
      "ds_read_b128 a[176:179], v249 offset:12288\n"
      "ds_read_b128 a[180:183], v249 offset:14336\n"
      "v_mfma_f32_16x16x32_bf16 v[60:63], v[144:147], a[60:63], v[60:63]\n"
      "global_load_lds_dwordx4 v246, s[70:71] offset:2048\n"
      "global_load_dwordx4 v[144:147], v241, s[14:15] offset:-3072 sc1\n"
      "v_mfma_f32_16x16x32_bf16 v[64:67], v[148:151], a[32:35], v[64:67]\n"
      "ds_read_b128 a[152:155], v250 offset:0\n"
      "ds_read_b128 a[156:159], v250 offset:2048\n"
      "v_mfma_f32_16x16x32_bf16 v[68:71], v[148:151], a[36:39], v[68:71]\n"
      "global_load_lds_dwordx4 v247, s[70:71] offset:3072\n"
      "ds_read_b128 a[184:187], v251 offset:0\n"
      "v_mfma_f32_16x16x32_bf16 v[72:75], v[148:151], a[40:43], v[72:75]\n"
      "ds_read_b128 a[188:191], v251 offset:2048\n"
      "ds_read_b128 a[192:195], v250 offset:4096\n"
      "v_mfma_f32_16x16x32_bf16 v[76:79], v[148:151], a[44:47], v[76:79]\n"
      "ds_read_b128 a[196:199], v250 offset:6144\n"
      "ds_read_b128 a[224:227], v251 offset:4096\n"
      "v_mfma_f32_16x16x32_bf16 v[80:83], v[148:151], a[48:51], v[80:83]\n"
      "ds_read_b128 a[228:231], v251 offset:6144\n"
      "v_mfma_f32_16x16x32_bf16 v[84:87], v[148:151], a[52:55], v[84:87]\n"
      "v_mfma_f32_16x16x32_bf16 v[88:91], v[148:151], a[56:59], v[88:91]\n"
      "v_mfma_f32_16x16x32_bf16 v[92:95], v[148:151], a[60:63], v[92:95]\n"
      "s_waitcnt vmcnt(29)\n"
      "global_load_dwordx4 v[148:151], v241, s[74:75] offset:-3072 sc1\n"
      "v_mfma_f32_16x16x32_bf16 v[0:3], v[152:155], a[64:67], v[0:3]\n"
      "v_mfma_f32_16x16x32_bf16 v[4:7], v[152:155], a[68:71], v[4:7]\n"
      "v_mfma_f32_16x16x32_bf16 v[8:11], v[152:155], a[72:75], v[8:11]\n"
      "v_mfma_f32_16x16x32_bf16 v[12:15], v[152:155], a[76:79], v[12:15]\n"
      "v_mfma_f32_16x16x32_bf16 v[16:19], v[152:155], a[80:83], v[16:19]\n"
      "v_mfma_f32_16x16x32_bf16 v[20:23], v[152:155], a[84:87], v[20:23]\n"
      "v_mfma_f32_16x16x32_bf16 v[24:27], v[152:155], a[88:91], v[24:27]\n"
      "v_mfma_f32_16x16x32_bf16 v[28:31], v[152:155], a[92:95], v[28:31]\n"
      "global_load_dwordx4 v[152:155], v241, s[14:15] offset:-2048 sc1\n"
      "s_waitcnt vmcnt(13)\n"
      "ds_read_b128 a[200:203], v250 offset:8192\n"
      "v_mfma_f32_16x16x32_bf16 v[32:35], v[156:159], a[64:67], v[32:35]\n"
      "ds_read_b128 a[204:207], v250 offset:10240\n"
      "ds_read_b128 a[232:235], v251 offset:8192\n"
      "v_mfma_f32_16x16x32_bf16 v[36:39], v[156:159], a[68:71], v[36:39]\n"
      "ds_read_b128 a[236:239], v251 offset:10240\n"
      "v_mfma_f32_16x16x32_bf16 v[40:43], v[156:159], a[72:75], v[40:43]\n"
      "v_mfma_f32_16x16x32_bf16 v[44:47], v[156:159], a[76:79], v[44:47]\n"
      "v_mfma_f32_16x16x32_bf16 v[48:51], v[156:159], a[80:83], v[48:51]\n"
      "v_mfma_f32_16x16x32_bf16 v[52:55], v[156:159], a[84:87], v[52:55]\n"
      "v_mfma_f32_16x16x32_bf16 v[56:59], v[156:159], a[88:91], v[56:59]\n"
      "v_mfma_f32_16x16x32_bf16 v[60:63], v[156:159], a[92:95], v[60:63]\n"
      "global_load_dwordx4 v[156:159], v241, s[74:75] offset:-2048 sc1\n"
      "s_waitcnt vmcnt(8)\n"
      "ds_read_b128 a[208:211], v248 offset:0\n"
      "v_mfma_f32_16x16x32_bf16 v[64:67], v[160:163], a[64:67], v[64:67]\n"
      "ds_read_b128 a[212:215], v248 offset:2048\n"
      "ds_read_b128 a[240:243], v249 offset:0\n"
      "v_mfma_f32_16x16x32_bf16 v[68:71], v[160:163], a[68:71], v[68:71]\n"
      "ds_read_b128 a[244:247], v249 offset:2048\n"
      "v_mfma_f32_16x16x32_bf16 v[72:75], v[160:163], a[72:75], v[72:75]\n"
      "v_mfma_f32_16x16x32_bf16 v[76:79], v[160:163], a[76:79], v[76:79]\n"
      "v_mfma_f32_16x16x32_bf16 v[80:83], v[160:163], a[80:83], v[80:83]\n"
      "v_mfma_f32_16x16x32_bf16 v[84:87], v[160:163], a[84:87], v[84:87]\n"
      "v_mfma_f32_16x16x32_bf16 v[88:91], v[160:163], a[88:91], v[88:91]\n"
      "s_min_u32 s43, s80, 27\n"
      "v_lshrrev_b32 v202, 3, v243\n"
      "v_and_b32 v203, 0x70, v242\n"
      "s_movk_i32 s44, 3584\n"
      "v_mad_u32_u24 v200, v202, s44, v203\n"
      "v_add_u32 v201, 27648, v200\n"
      "v_add_u32 v202, 55296, v200\n"
      "v_add_u32 v203, 82944, v200\n"
      "s_bitcmp1_b32 s33, 0\n"
      "s_cselect_b64 s[70:71], s[28:29], s[26:27]\n"
      "s_mul_i32 s44, s83, 229376\n"
      "s_lshl_b32 s45, s43, 7\n"
      "s_add_u32 s44, s44, s45\n"
      "s_add_u32 s70, s70, s44\n"
      "s_addc_u32 s71, s71, 0\n"
      "s_lshl_b32 s45, s33, 13\n"
      "s_add_u32 m0, s45, 131072\n"
      "v_mfma_f32_16x16x32_bf16 v[92:95], v[160:163], a[92:95], v[92:95]\n"
      "global_load_lds_dwordx4 v200, s[70:71] offset:0\n"
      "s_waitcnt vmcnt(4)\n"
      "ds_read_b128 a[216:219], v248 offset:4096\n"
      "v_mfma_f32_16x16x32_bf16 v[0:3], v[164:167], a[96:99], v[0:3]\n"
      "global_load_lds_dwordx4 v201, s[70:71] offset:1024\n"
      "ds_read_b128 a[220:223], v248 offset:6144\n"
      "v_mfma_f32_16x16x32_bf16 v[4:7], v[164:167], a[100:103], v[4:7]\n"
      "global_load_lds_dwordx4 v202, s[70:71] offset:2048\n"
      "ds_read_b128 a[248:251], v249 offset:4096\n"
      "v_mfma_f32_16x16x32_bf16 v[8:11], v[164:167], a[104:107], v[8:11]\n"
      "global_load_lds_dwordx4 v203, s[70:71] offset:3072\n"
      "ds_read_b128 a[252:255], v249 offset:6144\n"
      "v_mfma_f32_16x16x32_bf16 v[12:15], v[164:167], a[108:111], v[12:15]\n"
      "s_add_u32 s70, s70, 114688\n"
      "s_addc_u32 s71, s71, 0\n"
      "s_add_u32 m0, m0, 4096\n"
      "v_mfma_f32_16x16x32_bf16 v[16:19], v[164:167], a[112:115], v[16:19]\n"
      "global_load_lds_dwordx4 v200, s[70:71] offset:0\n"
      "v_mfma_f32_16x16x32_bf16 v[20:23], v[164:167], a[116:119], v[20:23]\n"
      "global_load_lds_dwordx4 v201, s[70:71] offset:1024\n"
      "v_mfma_f32_16x16x32_bf16 v[24:27], v[164:167], a[120:123], v[24:27]\n"
      "global_load_lds_dwordx4 v202, s[70:71] offset:2048\n"
      "v_mfma_f32_16x16x32_bf16 v[28:31], v[164:167], a[124:127], v[28:31]\n"
      "global_load_lds_dwordx4 v203, s[70:71] offset:3072\n"
      "s_sub_u32 s48, s33, 1\n"
      "v_mfma_f32_16x16x32_bf16 v[32:35], v[168:171], a[96:99], v[32:35]\n"
      "s_and_b32 s48, s48, 3\n"
      "global_load_dword v252, v253, s[58:59] offset:128 sc1\n"
      "s_lshl_b32 s48, s48, 14\n"
      "v_mfma_f32_16x16x32_bf16 v[36:39], v[168:171], a[100:103], v[36:39]\n"
      "v_lshl_add_u32 v244, v243, 2, s48\n"
      "s_sub_u32 s48, s33, 2\n"
      "v_mfma_f32_16x16x32_bf16 v[40:43], v[168:171], a[104:107], v[40:43]\n"
      "s_and_b32 s48, s48, 3\n"
      "s_lshl_b32 s48, s48, 14\n"
      "v_lshl_add_u32 v245, v243, 2, s48\n"
      "v_mfma_f32_16x16x32_bf16 v[44:47], v[168:171], a[108:111], v[44:47]\n"
      "s_sub_u32 s48, s33, 3\n"
      "s_and_b32 s48, s48, 3\n"
      "s_lshl_b32 s48, s48, 14\n"
      "v_mfma_f32_16x16x32_bf16 v[48:51], v[168:171], a[112:115], v[48:51]\n"
      "v_lshl_add_u32 v246, v243, 2, s48\n"
      "v_mfma_f32_16x16x32_bf16 v[52:55], v[168:171], a[116:119], v[52:55]\n"
      "v_mfma_f32_16x16x32_bf16 v[56:59], v[168:171], a[120:123], v[56:59]\n"
      "v_mfma_f32_16x16x32_bf16 v[60:63], v[168:171], a[124:127], v[60:63]\n"
      "v_bitop3_b32 v197, v205, v207, s37 bitop3:0x7e\n"
      "v_cmp_ne_u32 vcc, 0, v197\n"
      "s_nop 1\n"
      "s_cbranch_vccnz .Ls_s10_%=\n"
      ".Ls_o11_%=:\n"
      "v_mfma_f32_16x16x32_bf16 v[64:67], v[172:175], a[96:99], v[64:67]\n"
      "s_lshr_b32 s43, s42, 5\n"
      "s_lshl2_add_u32 s44, s36, s33\n"
      "s_cmp_eq_u32 s43, s44\n"
      "s_cbranch_scc0 .Ls_e12_%=\n"
      "v_lshlrev_b32 v192, 16, v232\n"
      "v_lshlrev_b32 v193, 16, v236\n"
      "v_and_b32 v194, 0xffff0000, v232\n"
      "v_and_b32 v195, 0xffff0000, v236\n"
      "v_mul_f32 v192, v192, v193\n"
      "v_mul_f32 v194, v194, v195\n"
      "v_cvt_pk_bf16_f32 v192, v192, v194\n"
      "v_lshlrev_b32 v193, 16, v192\n"
      "v_and_b32 v192, 0xffff0000, v192\n"
      "v_add_f32 v196, v192, v193\n"
      "v_lshlrev_b32 v192, 16, v233\n"
      "v_lshlrev_b32 v193, 16, v237\n"
      "v_and_b32 v194, 0xffff0000, v233\n"
      "v_and_b32 v195, 0xffff0000, v237\n"
      "v_mul_f32 v192, v192, v193\n"
      "v_mul_f32 v194, v194, v195\n"
      "v_cvt_pk_bf16_f32 v192, v192, v194\n"
      "v_lshlrev_b32 v193, 16, v192\n"
      "v_and_b32 v192, 0xffff0000, v192\n"
      "v_add_f32 v192, v192, v193\n"
      "v_add_f32 v196, v196, v192\n"
      "v_lshlrev_b32 v192, 16, v234\n"
      "v_lshlrev_b32 v193, 16, v238\n"
      "v_and_b32 v194, 0xffff0000, v234\n"
      "v_and_b32 v195, 0xffff0000, v238\n"
      "v_mul_f32 v192, v192, v193\n"
      "v_mul_f32 v194, v194, v195\n"
      "v_cvt_pk_bf16_f32 v192, v192, v194\n"
      "v_lshlrev_b32 v193, 16, v192\n"
      "v_and_b32 v192, 0xffff0000, v192\n"
      "v_add_f32 v192, v192, v193\n"
      "v_add_f32 v196, v196, v192\n"
      "v_lshlrev_b32 v192, 16, v235\n"
      "v_lshlrev_b32 v193, 16, v239\n"
      "v_and_b32 v194, 0xffff0000, v235\n"
      "v_and_b32 v195, 0xffff0000, v239\n"
      "v_mul_f32 v192, v192, v193\n"
      "v_mul_f32 v194, v194, v195\n"
      "v_cvt_pk_bf16_f32 v192, v192, v194\n"
      "v_lshlrev_b32 v193, 16, v192\n"
      "v_and_b32 v192, 0xffff0000, v192\n"
      "v_add_f32 v192, v192, v193\n"
      "v_add_f32 v196, v196, v192\n"
      "s_nop 1\n"
      "v_add_f32_dpp v196, v196, v196 quad_perm:[1,0,3,2] row_mask:0xf bank_mask:0xf\n"
      "s_nop 1\n"
      "v_add_f32_dpp v196, v196, v196 quad_perm:[2,3,0,1] row_mask:0xf bank_mask:0xf\n"
      "s_nop 1\n"
      "v_add_f32_dpp v196, v196, v196 row_half_mirror row_mask:0xf bank_mask:0xf\n"
      "s_nop 1\n"
      "v_add_f32_dpp v196, v196, v196 row_mirror row_mask:0xf bank_mask:0xf\n"
      "v_mov_b32 v192, v196\n"
      "s_nop 1\n"
      "v_permlane16_swap_b32 v196, v192\n"
      "v_add_f32 v196, v196, v192\n"
      "v_mov_b32 v192, v196\n"
      "s_nop 1\n"
      "v_permlane32_swap_b32 v196, v192\n"
      "v_add_f32 v196, v196, v192\n"
      "v_cvt_pk_bf16_f32 v196, v196, v196\n"
      "v_and_b32 v196, 0xffff0000, v196\n"
      "v_mul_f32 v196, 0xbfb8aa3b, v196\n"
      "v_exp_f32 v196, v196\n"
      "v_lshrrev_b32 v192, 10, v199\n"
      "v_add_f32 v196, 1.0, v196\n"
      "v_rcp_f32 v196, v196\n"
      "v_subrev_u32 v192, s42, v192\n"
      "v_cvt_pk_bf16_f32 v196, v196, v196\n"
      "v_and_b32 v196, 0xffff0000, v196\n"
      "v_cmp_eq_u32 vcc, 0, v192\n"
      "s_nop 1\n"
      "v_cndmask_b32 v204, v204, v196, vcc\n"
      "v_cmp_eq_u32 vcc, -16, v192\n"
      "s_nop 1\n"
      "v_cndmask_b32 v206, v206, v196, vcc\n"
      ".Ls_e12_%=:\n"
      "v_mfma_f32_16x16x32_bf16 v[68:71], v[172:175], a[100:103], v[68:71]\n"
      "v_sub_f32 v205, 1.0, v204\n"
      "v_cvt_pk_bf16_f32 v205, v205, v205\n"
      "v_mfma_f32_16x16x32_bf16 v[72:75], v[172:175], a[104:107], v[72:75]\n"
      "v_and_b32 v205, 0xffff0000, v205\n"
      "v_lshlrev_b32 v208, 16, v224\n"
      "v_mfma_f32_16x16x32_bf16 v[76:79], v[172:175], a[108:111], v[76:79]\n"
      "v_and_b32 v209, 0xffff0000, v224\n"
      "v_mul_f32 v208, v205, v208\n"
      "v_mfma_f32_16x16x32_bf16 v[80:83], v[172:175], a[112:115], v[80:83]\n"
      "v_mul_f32 v209, v205, v209\n"
      "v_lshlrev_b32 v210, 16, v225\n"
      "v_mfma_f32_16x16x32_bf16 v[84:87], v[172:175], a[116:119], v[84:87]\n"
      "v_and_b32 v211, 0xffff0000, v225\n"
      "v_mul_f32 v210, v205, v210\n"
      "v_mfma_f32_16x16x32_bf16 v[88:91], v[172:175], a[120:123], v[88:91]\n"
      "v_mul_f32 v211, v205, v211\n"
      "v_lshlrev_b32 v216, 16, v226\n"
      "v_mfma_f32_16x16x32_bf16 v[92:95], v[172:175], a[124:127], v[92:95]\n"
      "v_and_b32 v217, 0xffff0000, v226\n"
      "v_mul_f32 v216, v205, v216\n"
      "v_mfma_f32_16x16x32_bf16 v[0:3], v[176:179], a[128:131], v[0:3]\n"
      "v_mul_f32 v217, v205, v217\n"
      "v_lshlrev_b32 v218, 16, v227\n"
      "v_mfma_f32_16x16x32_bf16 v[4:7], v[176:179], a[132:135], v[4:7]\n"
      "v_and_b32 v219, 0xffff0000, v227\n"
      "v_mul_f32 v218, v205, v218\n"
      "s_waitcnt lgkmcnt(15)\n"
      "v_mfma_f32_16x16x32_bf16 v[8:11], v[176:179], a[136:139], v[8:11]\n"
      "v_mul_f32 v219, v205, v219\n"
      "v_sub_f32 v207, 1.0, v206\n"
      "v_mfma_f32_16x16x32_bf16 v[12:15], v[176:179], a[140:143], v[12:15]\n"
      "v_cvt_pk_bf16_f32 v207, v207, v207\n"
      "v_and_b32 v207, 0xffff0000, v207\n"
      "v_mfma_f32_16x16x32_bf16 v[16:19], v[176:179], a[144:147], v[16:19]\n"
      "v_lshlrev_b32 v212, 16, v228\n"
      "v_and_b32 v213, 0xffff0000, v228\n"
      "v_mfma_f32_16x16x32_bf16 v[20:23], v[176:179], a[148:151], v[20:23]\n"
      "v_mul_f32 v212, v207, v212\n"
      "v_mul_f32 v213, v207, v213\n"
      "v_mfma_f32_16x16x32_bf16 v[24:27], v[176:179], a[152:155], v[24:27]\n"
      "v_lshlrev_b32 v214, 16, v229\n"
      "v_and_b32 v215, 0xffff0000, v229\n"
      "v_mfma_f32_16x16x32_bf16 v[28:31], v[176:179], a[156:159], v[28:31]\n"
      "v_mul_f32 v214, v207, v214\n"
      "v_mul_f32 v215, v207, v215\n"
      "v_mfma_f32_16x16x32_bf16 v[32:35], v[180:183], a[128:131], v[32:35]\n"
      "v_lshlrev_b32 v220, 16, v230\n"
      "v_and_b32 v221, 0xffff0000, v230\n"
      "v_mfma_f32_16x16x32_bf16 v[36:39], v[180:183], a[132:135], v[36:39]\n"
      "v_mul_f32 v220, v207, v220\n"
      "v_mul_f32 v221, v207, v221\n"
      "v_mfma_f32_16x16x32_bf16 v[40:43], v[180:183], a[136:139], v[40:43]\n"
      "v_lshlrev_b32 v222, 16, v231\n"
      "v_and_b32 v223, 0xffff0000, v231\n"
      "v_mfma_f32_16x16x32_bf16 v[44:47], v[180:183], a[140:143], v[44:47]\n"
      "v_mul_f32 v222, v207, v222\n"
      "v_mul_f32 v223, v207, v223\n"
      "v_mfma_f32_16x16x32_bf16 v[48:51], v[180:183], a[144:147], v[48:51]\n"
      "s_lshl_b32 s54, s42, 10\n"
      "s_lshl_b32 s55, s83, 7\n"
      "s_add_u32 s54, s54, s55\n"
      "v_mfma_f32_16x16x32_bf16 v[52:55], v[180:183], a[148:151], v[52:55]\n"
      "v_and_b32 v224, 48, v243\n"
      "v_add_u32 v224, s54, v224\n"
      "v_mfma_f32_16x16x32_bf16 v[56:59], v[180:183], a[152:155], v[56:59]\n"
      "global_load_dwordx4 v[192:195], v224, s[16:17]\n"
      "global_load_dwordx4 v[196:199], v224, s[16:17] offset:64\n"
      "v_mfma_f32_16x16x32_bf16 v[60:63], v[180:183], a[156:159], v[60:63]\n"
      "v_lshrrev_b32 v201, 4, v243\n"
      "v_bfe_u32 v224, v243, 2, 2\n"
      "v_mfma_f32_16x16x32_bf16 v[64:67], v[184:187], a[128:131], v[64:67]\n"
      "v_lshl_or_b32 v201, v201, 3, v224\n"
      "v_and_b32 v224, 3, v243\n"
      "v_mfma_f32_16x16x32_bf16 v[68:71], v[184:187], a[132:135], v[68:71]\n"
      "v_lshlrev_b32 v224, 3, v224\n"
      "v_lshl_or_b32 v201, v201, 7, v224\n"
      "v_mfma_f32_16x16x32_bf16 v[72:75], v[184:187], a[136:139], v[72:75]\n"
      "s_mov_b32 s64, 0xffff0000\n"
      "s_mov_b32 s65, 0xffff0000\n"
      "s_mov_b32 s66, -1\n"
      "v_mfma_f32_16x16x32_bf16 v[76:79], v[184:187], a[140:143], v[76:79]\n"
      "s_mov_b32 s67, 0\n"
      "s_lshl_b32 s54, s33, 13\n"
      "s_add_u32 s54, s54, 131072\n"
      "v_mfma_f32_16x16x32_bf16 v[80:83], v[184:187], a[144:147], v[80:83]\n"
      "v_add_u32 v202, s54, v201\n"
      "s_and_b32 s54, s33, 1\n"
      "v_mfma_f32_16x16x32_bf16 v[84:87], v[184:187], a[148:151], v[84:87]\n"
      "s_lshl3_add_u32 s54, s54, s83\n"
      "s_lshl_b32 s54, s54, 8\n"
      "s_lshl_b32 s55, s80, 13\n"
      "v_mfma_f32_16x16x32_bf16 v[88:91], v[184:187], a[152:155], v[88:91]\n"
      "s_add_u32 s55, s55, 262144\n"
      "s_add_u32 s54, s54, s55\n"
      "v_bfe_u32 v224, v243, 4, 1\n"
      "v_mfma_f32_16x16x32_bf16 v[92:95], v[184:187], a[156:159], v[92:95]\n"
      "v_and_b32 v203, 0xf0, v242\n"
      "v_lshl_or_b32 v200, v224, 12, v203\n"
      "v_mfma_f32_16x16x32_bf16 v[0:3], v[188:191], a[160:163], v[0:3]\n"
      "v_add_u32 v200, s54, v200\n"
      "v_lshl_or_b32 v203, v224, 14, v203\n"
      "v_mfma_f32_16x16x32_bf16 v[4:7], v[188:191], a[164:167], v[4:7]\n"
      "v_mfma_f32_16x16x32_bf16 v[8:11], v[188:191], a[168:171], v[8:11]\n"
      "v_mfma_f32_16x16x32_bf16 v[12:15], v[188:191], a[172:175], v[12:15]\n"
      "v_mfma_f32_16x16x32_bf16 v[16:19], v[188:191], a[176:179], v[16:19]\n"
      "v_mfma_f32_16x16x32_bf16 v[20:23], v[188:191], a[180:183], v[20:23]\n"
      "v_mfma_f32_16x16x32_bf16 v[24:27], v[188:191], a[184:187], v[24:27]\n"
      "v_mfma_f32_16x16x32_bf16 v[28:31], v[188:191], a[188:191], v[28:31]\n"
      "v_mfma_f32_16x16x32_bf16 v[32:35], v[128:131], a[160:163], v[32:35]\n"
      "v_mfma_f32_16x16x32_bf16 v[36:39], v[128:131], a[164:167], v[36:39]\n"
      "v_mfma_f32_16x16x32_bf16 v[40:43], v[128:131], a[168:171], v[40:43]\n"
      "v_mfma_f32_16x16x32_bf16 v[44:47], v[128:131], a[172:175], v[44:47]\n"
      "v_mfma_f32_16x16x32_bf16 v[48:51], v[128:131], a[176:179], v[48:51]\n"
      "v_mfma_f32_16x16x32_bf16 v[52:55], v[128:131], a[180:183], v[52:55]\n"
      "v_mfma_f32_16x16x32_bf16 v[56:59], v[128:131], a[184:187], v[56:59]\n"
      "v_mfma_f32_16x16x32_bf16 v[60:63], v[128:131], a[188:191], v[60:63]\n"
      "v_mfma_f32_16x16x32_bf16 v[64:67], v[132:135], a[160:163], v[64:67]\n"
      "v_mfma_f32_16x16x32_bf16 v[68:71], v[132:135], a[164:167], v[68:71]\n"
      "v_mfma_f32_16x16x32_bf16 v[72:75], v[132:135], a[168:171], v[72:75]\n"
      "s_waitcnt vmcnt(2)\n"
      "v_cmp_ne_u32 vcc, s37, v252\n"
      "s_nop 1\n"
      "s_cbranch_vccnz .Ls_s14_%=\n"
      ".Ls_o15_%=:\n"
      "v_mfma_f32_16x16x32_bf16 v[76:79], v[132:135], a[172:175], v[76:79]\n"
      "v_mfma_f32_16x16x32_bf16 v[80:83], v[132:135], a[176:179], v[80:83]\n"
      "global_load_dwordx4 v[160:163], v240, s[10:11] offset:-1024 sc1\n"
      "global_load_dwordx4 v[164:167], v240, s[72:73] offset:-1024 sc1\n"
      "v_mfma_f32_16x16x32_bf16 v[84:87], v[132:135], a[180:183], v[84:87]\n"
      "global_load_dwordx4 v[168:171], v241, s[10:11] offset:-1024 sc1\n"
      "global_load_dwordx4 v[172:175], v241, s[72:73] offset:-1024 sc1\n"
      "v_mfma_f32_16x16x32_bf16 v[88:91], v[132:135], a[184:187], v[88:91]\n"
      "global_load_dwordx4 v[176:179], v240, s[14:15] offset:-1024 sc1\n"
      "global_load_dwordx4 v[180:183], v240, s[74:75] offset:-1024 sc1\n"
      "v_mfma_f32_16x16x32_bf16 v[92:95], v[132:135], a[188:191], v[92:95]\n"
      "global_load_dwordx4 v[184:187], v241, s[14:15] offset:-1024 sc1\n"
      "global_load_dwordx4 v[188:191], v241, s[74:75] offset:-1024 sc1\n"
      "v_mfma_f32_16x16x32_bf16 v[0:3], v[136:139], a[192:195], v[0:3]\n"
      "global_load_dwordx4 v[128:131], v240, s[10:11] offset:0 sc1\n"
      "global_load_dwordx4 v[132:135], v240, s[72:73] offset:0 sc1\n"
      "s_waitcnt lgkmcnt(14)\n"
      "v_mfma_f32_16x16x32_bf16 v[4:7], v[136:139], a[196:199], v[4:7]\n"
      "s_waitcnt lgkmcnt(11)\n"
      "v_mfma_f32_16x16x32_bf16 v[8:11], v[136:139], a[200:203], v[8:11]\n"
      "s_waitcnt lgkmcnt(10)\n"
      "v_mfma_f32_16x16x32_bf16 v[12:15], v[136:139], a[204:207], v[12:15]\n"
      "s_waitcnt lgkmcnt(7)\n"
      "v_mfma_f32_16x16x32_bf16 v[16:19], v[136:139], a[208:211], v[16:19]\n"
      "s_waitcnt lgkmcnt(6)\n"
      "v_mfma_f32_16x16x32_bf16 v[20:23], v[136:139], a[212:215], v[20:23]\n"
      "ds_read_b64_tr_b16 v[224:225], v202 offset:0\n"
      "ds_read_b64_tr_b16 v[226:227], v202 offset:512\n"
      "s_waitcnt lgkmcnt(5)\n"
      "v_mfma_f32_16x16x32_bf16 v[24:27], v[136:139], a[216:219], v[24:27]\n"
      "ds_read_b64_tr_b16 v[228:229], v202 offset:4096\n"
      "ds_read_b64_tr_b16 v[230:231], v202 offset:4608\n"
      "s_waitcnt lgkmcnt(6)\n"
      "v_mfma_f32_16x16x32_bf16 v[28:31], v[136:139], a[220:223], v[28:31]\n"
      "global_load_dwordx4 v[136:139], v241, s[10:11] offset:0 sc1\n"
      "ds_read_b64_tr_b16 v[232:233], v202 offset:64\n"
      "v_mfma_f32_16x16x32_bf16 v[0:3], v[140:143], a[224:227], v[0:3]\n"
      "ds_read_b64_tr_b16 v[234:235], v202 offset:576\n"
      "ds_read_b64_tr_b16 v[236:237], v202 offset:4160\n"
      "v_mfma_f32_16x16x32_bf16 v[4:7], v[140:143], a[228:231], v[4:7]\n"
      "ds_read_b64_tr_b16 v[238:239], v202 offset:4672\n"
      "v_mfma_f32_16x16x32_bf16 v[8:11], v[140:143], a[232:235], v[8:11]\n"
      "v_mfma_f32_16x16x32_bf16 v[12:15], v[140:143], a[236:239], v[12:15]\n"
      "v_mfma_f32_16x16x32_bf16 v[16:19], v[140:143], a[240:243], v[16:19]\n"
      "s_waitcnt vmcnt(12) lgkmcnt(6)\n"
      "v_mfma_f32_16x16x32_bf16 v[248:251], v[192:195], v[224:227], 0\n"
      "v_mfma_f32_16x16x32_bf16 v[20:23], v[140:143], a[244:247], v[20:23]\n"
      "s_waitcnt vmcnt(11) lgkmcnt(4)\n"
      "v_mfma_f32_16x16x32_bf16 v[248:251], v[196:199], v[228:231], v[248:251]\n"
      "v_mfma_f32_16x16x32_bf16 v[24:27], v[140:143], a[248:251], v[24:27]\n"
      "s_waitcnt lgkmcnt(2)\n"
      "v_mfma_f32_16x16x32_bf16 v[224:227], v[192:195], v[232:235], 0\n"
      "v_mfma_f32_16x16x32_bf16 v[28:31], v[140:143], a[252:255], v[28:31]\n"
      "global_load_dwordx4 v[140:143], v241, s[72:73] offset:0 sc1\n"
      "s_waitcnt lgkmcnt(0)\n"
      "v_mfma_f32_16x16x32_bf16 v[224:227], v[196:199], v[236:239], v[224:227]\n"
      "v_mfma_f32_16x16x32_bf16 v[32:35], v[144:147], a[192:195], v[32:35]\n"
      "v_mfma_f32_16x16x32_bf16 v[36:39], v[144:147], a[196:199], v[36:39]\n"
      "s_mov_b32 m0, s41\n"
      "v_cvt_pk_bf16_f32 v8, v8, v9\n"
      "v_cvt_pk_bf16_f32 v9, v10, v11\n"
      "v_mfma_f32_16x16x32_bf16 v[40:43], v[144:147], a[200:203], v[40:43]\n"
      "ds_write_addtid_b32 v8 offset:0\n"
      "s_nop 0\n"
      "v_cndmask_b32 v247, v248, v224, s[64:65]\n"
      "v_mfma_f32_16x16x32_bf16 v[44:47], v[144:147], a[204:207], v[44:47]\n"
      "ds_read_b64_tr_b16 v[224:225], v202 offset:32\n"
      "v_cvt_pk_bf16_f32 v10, v12, v13\n"
      "v_mfma_f32_16x16x32_bf16 v[48:51], v[144:147], a[208:211], v[48:51]\n"
      "ds_read_b64_tr_b16 v[226:227], v202 offset:544\n"
      "ds_read_b64_tr_b16 v[228:229], v202 offset:4128\n"
      "v_mfma_f32_16x16x32_bf16 v[52:55], v[144:147], a[212:215], v[52:55]\n"
      "ds_read_b64_tr_b16 v[230:231], v202 offset:4640\n"
      "ds_read_b64_tr_b16 v[232:233], v202 offset:96\n"
      "v_mfma_f32_16x16x32_bf16 v[56:59], v[144:147], a[216:219], v[56:59]\n"
      "ds_read_b64_tr_b16 v[234:235], v202 offset:608\n"
      "ds_read_b64_tr_b16 v[236:237], v202 offset:4192\n"
      "v_mfma_f32_16x16x32_bf16 v[60:63], v[144:147], a[220:223], v[60:63]\n"
      "global_load_dwordx4 v[144:147], v240, s[14:15] offset:0 sc1\n"
      "ds_read_b64_tr_b16 v[238:239], v202 offset:4704\n"
      "v_mfma_f32_16x16x32_bf16 v[32:35], v[148:151], a[224:227], v[32:35]\n"
      "ds_write_addtid_b32 v9 offset:256\n"
      "v_cvt_pk_bf16_f32 v11, v14, v15\n"
      "v_mfma_f32_16x16x32_bf16 v[36:39], v[148:151], a[228:231], v[36:39]\n"
      "ds_write_addtid_b32 v10 offset:512\n"
      "v_cvt_pk_bf16_f32 v12, v16, v17\n"
      "v_mfma_f32_16x16x32_bf16 v[40:43], v[148:151], a[232:235], v[40:43]\n"
      "s_waitcnt lgkmcnt(8)\n"
      "v_mfma_f32_16x16x32_bf16 v[248:251], v[192:195], v[224:227], 0\n"
      "v_mfma_f32_16x16x32_bf16 v[44:47], v[148:151], a[236:239], v[44:47]\n"
      "s_waitcnt lgkmcnt(6)\n"
      "v_mfma_f32_16x16x32_bf16 v[248:251], v[196:199], v[228:231], v[248:251]\n"
      "v_mfma_f32_16x16x32_bf16 v[48:51], v[148:151], a[240:243], v[48:51]\n"
      "s_waitcnt lgkmcnt(4)\n"
      "v_mfma_f32_16x16x32_bf16 v[224:227], v[192:195], v[232:235], 0\n"
      "v_mfma_f32_16x16x32_bf16 v[52:55], v[148:151], a[244:247], v[52:55]\n"
      "s_waitcnt lgkmcnt(2)\n"
      "v_mfma_f32_16x16x32_bf16 v[224:227], v[196:199], v[236:239], v[224:227]\n"
      "v_mfma_f32_16x16x32_bf16 v[56:59], v[148:151], a[248:251], v[56:59]\n"
      "ds_write_addtid_b32 v11 offset:768\n"
      "v_cvt_pk_bf16_f32 v13, v18, v19\n"
      "v_mfma_f32_16x16x32_bf16 v[60:63], v[148:151], a[252:255], v[60:63]\n"
      "global_load_dwordx4 v[148:151], v240, s[74:75] offset:0 sc1\n"
      "ds_write_addtid_b32 v12 offset:1024\n"
      "v_mfma_f32_16x16x32_bf16 v[64:67], v[152:155], a[192:195], v[64:67]\n"
      "s_nop 0\n"
      "v_cndmask_b32 v250, v248, v224, s[64:65]\n"
      "v_mfma_f32_16x16x32_bf16 v[68:71], v[152:155], a[196:199], v[68:71]\n"
      "v_and_b32 v248, s82, v247\n"
      "v_mov_b32 v249, s37\n"
      "v_mfma_f32_16x16x32_bf16 v[72:75], v[152:155], a[200:203], v[72:75]\n"
      "v_and_b32 v250, s82, v250\n"
      "v_mov_b32 v251, s37\n"
      "v_mfma_f32_16x16x32_bf16 v[76:79], v[152:155], a[204:207], v[76:79]\n"
      "s_mov_b64 exec, s[66:67]\n"
      "global_store_dwordx4 v200, v[248:251], s[58:59] sc1\n"
      "s_mov_b64 exec, -1\n"
      "v_mfma_f32_16x16x32_bf16 v[80:83], v[152:155], a[208:211], v[80:83]\n"
      "v_bfe_u32 v248, v243, 1, 3\n"
      "v_lshrrev_b32 v249, 4, v243\n"
      "v_mfma_f32_16x16x32_bf16 v[84:87], v[152:155], a[212:215], v[84:87]\n"
      "v_lshlrev_b32 v249, 1, v249\n"
      "v_sub_u32 v248, v248, v249\n"
      "v_mfma_f32_16x16x32_bf16 v[88:91], v[152:155], a[216:219], v[88:91]\n"
      "v_and_b32 v249, 16, v242\n"
      "v_mov_b32 v254, 0x3f80\n"
      "v_mfma_f32_16x16x32_bf16 v[92:95], v[152:155], a[220:223], v[92:95]\n"
      "global_load_dwordx4 v[152:155], v241, s[14:15] offset:0 sc1\n"
      "v_lshlrev_b32 v249, v249, v254\n"
      "v_mfma_f32_16x16x32_bf16 v[64:67], v[156:159], a[224:227], v[64:67]\n"
      "v_cmp_eq_u32 vcc, 0, v248\n"
      "s_nop 1\n"
      "v_cndmask_b32 v250, 0, v249, vcc\n"
      "v_mfma_f32_16x16x32_bf16 v[68:71], v[156:159], a[228:231], v[68:71]\n"
      "v_cmp_eq_u32 vcc, 1, v248\n"
      "s_nop 1\n"
      "v_cndmask_b32 v251, 0, v249, vcc\n"
      "v_mfma_f32_16x16x32_bf16 v[72:75], v[156:159], a[232:235], v[72:75]\n"
      "v_mov_b32 v252, v250\n"
      "v_mov_b32 v253, v251\n"
      "v_mfma_f32_16x16x32_bf16 v[76:79], v[156:159], a[236:239], v[76:79]\n"
      "v_mov_b32 v248, 0\n"
      "v_mov_b32 v249, 0\n"
      "v_mfma_f32_16x16x32_bf16 v[80:83], v[156:159], a[240:243], v[80:83]\n"
      "v_mov_b32 v254, 0\n"
      "v_mov_b32 v255, 0\n"
      "v_mfma_f32_16x16x32_bf16 v[84:87], v[156:159], a[244:247], v[84:87]\n"
      "s_lshr_b32 s54, s33, 1\n"
      "s_lshl1_add_u32 s54, s80, s54\n"
      "s_lshl_b32 s54, s54, 12\n"
      "v_mfma_f32_16x16x32_bf16 v[88:91], v[156:159], a[248:251], v[88:91]\n"
      "s_add_u32 s54, s54, 262144\n"
      "v_add_u32 v200, s54, v242\n"
      "s_add_u32 s54, s41, 77824\n"
      "v_mfma_f32_16x16x32_bf16 v[92:95], v[156:159], a[252:255], v[92:95]\n"
      "s_waitcnt vmcnt(14)\n"
      "global_load_dwordx4 v[156:159], v241, s[74:75] offset:0 sc1\n"
      "v_add_u32 v201, s54, v201\n"
      "v_mfma_f32_16x16x32_bf16 v[96:99], v[160:163], a[0:3], 0\n"
      "s_add_u32 s48, s34, 1\n"
      "v_and_b32 v202, 15, v243\n"
      "v_lshrrev_b32 v199, 5, v243\n"
      "v_mfma_f32_16x16x32_bf16 v[100:103], v[160:163], a[4:7], 0\n"
      "s_and_b32 s48, s48, 3\n"
      "v_lshl_or_b32 v202, v199, 4, v202\n"
      "s_add_u32 s54, s41, 12288\n"
      "v_mfma_f32_16x16x32_bf16 v[104:107], v[160:163], a[8:11], 0\n"
      "s_mul_i32 s48, s48, 36864\n"
      "v_lshl_add_u32 v202, v202, 1, s54\n"
      "v_and_b32 v199, 48, v243\n"
      "v_mfma_f32_16x16x32_bf16 v[108:111], v[160:163], a[12:15], 0\n"
      "s_add_u32 s48, s48, 0\n"
      "v_add_u32 v199, s54, v199\n"
      "s_and_b32 s54, s33, 1\n"
      "v_mfma_f32_16x16x32_bf16 v[112:115], v[160:163], a[16:19], 0\n"
      "s_lshl1_add_u32 s54, s81, s54\n"
      "s_lshl_b32 s54, s54, 15\n"
      "s_lshr_b32 s55, s33, 1\n"
      "v_mfma_f32_16x16x32_bf16 v[116:119], v[160:163], a[20:23], 0\n"
      "s_lshl1_add_u32 s55, s80, s55\n"
      "s_lshl_b32 s55, s55, 8\n"
      "s_add_u32 s54, s54, s55\n"
      "v_mfma_f32_16x16x32_bf16 v[120:123], v[160:163], a[24:27], 0\n"
      "s_add_u32 s54, s54, 524288\n"
      "v_add_u32 v203, s54, v203\n"
      "s_add_u32 s52, s60, s48\n"
      "s_addc_u32 s53, s61, 0\n"
      "v_mfma_f32_16x16x32_bf16 v[124:127], v[160:163], a[28:31], 0\n"
      "global_load_dwordx4 v[160:163], v240, s[10:11] offset:1024 sc1\n"
      "v_cvt_pk_bf16_f32 v14, v20, v21\n"
      "v_mfma_f32_16x16x32_bf16 v[96:99], v[164:167], a[32:35], v[96:99]\n"
      "ds_write_addtid_b32 v13 offset:1280\n"
      "v_cvt_pk_bf16_f32 v15, v22, v23\n"
      "v_mfma_f32_16x16x32_bf16 v[100:103], v[164:167], a[36:39], v[100:103]\n"
      "ds_write_addtid_b32 v14 offset:1536\n"
      "v_cvt_pk_bf16_f32 v16, v24, v25\n"
      "v_mfma_f32_16x16x32_bf16 v[104:107], v[164:167], a[40:43], v[104:107]\n"
      "ds_write_addtid_b32 v15 offset:1792\n"
      "v_cvt_pk_bf16_f32 v17, v26, v27\n"
      "v_mfma_f32_16x16x32_bf16 v[108:111], v[164:167], a[44:47], v[108:111]\n"
      "ds_write_addtid_b32 v16 offset:2048\n"
      "v_cvt_pk_bf16_f32 v18, v28, v29\n"
      "v_mfma_f32_16x16x32_bf16 v[112:115], v[164:167], a[48:51], v[112:115]\n"
      "ds_write_addtid_b32 v17 offset:2304\n"
      "v_cvt_pk_bf16_f32 v19, v30, v31\n"
      "v_mfma_f32_16x16x32_bf16 v[116:119], v[164:167], a[52:55], v[116:119]\n"
      "ds_write_addtid_b32 v18 offset:2560\n"
      "ds_write_addtid_b32 v19 offset:2816\n"
      "v_mfma_f32_16x16x32_bf16 v[120:123], v[164:167], a[56:59], v[120:123]\n"
      "v_cvt_pk_bf16_f32 v40, v40, v41\n"
      "v_cvt_pk_bf16_f32 v41, v42, v43\n"
      "v_mfma_f32_16x16x32_bf16 v[124:127], v[164:167], a[60:63], v[124:127]\n"
      "s_waitcnt vmcnt(14)\n"
      "global_load_dwordx4 v[164:167], v240, s[72:73] offset:1024 sc1\n"
      "ds_write_addtid_b32 v40 offset:3072\n"
      "v_mfma_f32_16x16x32_bf16 v[96:99], v[168:171], a[64:67], v[96:99]\n"
      "v_cvt_pk_bf16_f32 v42, v44, v45\n"
      "ds_write_addtid_b32 v41 offset:3328\n"
      "v_mfma_f32_16x16x32_bf16 v[100:103], v[168:171], a[68:71], v[100:103]\n"
      "v_cvt_pk_bf16_f32 v43, v46, v47\n"
      "ds_write_addtid_b32 v42 offset:3584\n"
      "v_mfma_f32_16x16x32_bf16 v[104:107], v[168:171], a[72:75], v[104:107]\n"
      "v_cvt_pk_bf16_f32 v44, v48, v49\n"
      "ds_write_addtid_b32 v43 offset:3840\n"
      "v_mfma_f32_16x16x32_bf16 v[108:111], v[168:171], a[76:79], v[108:111]\n"
      "v_cvt_pk_bf16_f32 v45, v50, v51\n"
      "ds_write_addtid_b32 v44 offset:4096\n"
      "v_mfma_f32_16x16x32_bf16 v[112:115], v[168:171], a[80:83], v[112:115]\n"
      "v_cvt_pk_bf16_f32 v46, v52, v53\n"
      "ds_write_addtid_b32 v45 offset:4352\n"
      "v_mfma_f32_16x16x32_bf16 v[116:119], v[168:171], a[84:87], v[116:119]\n"
      "v_cvt_pk_bf16_f32 v47, v54, v55\n"
      "ds_write_addtid_b32 v46 offset:4608\n"
      "v_mfma_f32_16x16x32_bf16 v[120:123], v[168:171], a[88:91], v[120:123]\n"
      "v_cvt_pk_bf16_f32 v48, v56, v57\n"
      "ds_write_addtid_b32 v47 offset:4864\n"
      "v_mfma_f32_16x16x32_bf16 v[124:127], v[168:171], a[92:95], v[124:127]\n"
      "global_load_dwordx4 v[168:171], v241, s[10:11] offset:1024 sc1\n"
      "v_cvt_pk_bf16_f32 v49, v58, v59\n"
      "v_mfma_f32_16x16x32_bf16 v[96:99], v[172:175], a[96:99], v[96:99]\n"
      "ds_write_addtid_b32 v48 offset:5120\n"
      "v_cvt_pk_bf16_f32 v50, v60, v61\n"
      "v_mfma_f32_16x16x32_bf16 v[100:103], v[172:175], a[100:103], v[100:103]\n"
      "ds_write_addtid_b32 v49 offset:5376\n"
      "v_cvt_pk_bf16_f32 v51, v62, v63\n"
      "v_mfma_f32_16x16x32_bf16 v[104:107], v[172:175], a[104:107], v[104:107]\n"
      "ds_write_addtid_b32 v50 offset:5632\n"
      "ds_write_addtid_b32 v51 offset:5888\n"
      "v_mfma_f32_16x16x32_bf16 v[108:111], v[172:175], a[108:111], v[108:111]\n"
      "v_mfma_f32_16x16x32_bf16 v[112:115], v[172:175], a[112:115], v[112:115]\n"
      "v_mfma_f32_16x16x32_bf16 v[116:119], v[172:175], a[116:119], v[116:119]\n"
      "s_waitcnt lgkmcnt(0)\n"
      "s_barrier\n"
      "v_mfma_f32_16x16x32_bf16 v[120:123], v[172:175], a[120:123], v[120:123]\n"
      "ds_read2st64_b32 v[20:21], v244 offset0:0 offset1:1\n"
      "ds_read2st64_b32 v[22:23], v245 offset0:4 offset1:5\n"
      "v_mfma_f32_16x16x32_bf16 v[124:127], v[172:175], a[124:127], v[124:127]\n"
      "s_waitcnt vmcnt(14)\n"
      "global_load_dwordx4 v[172:175], v241, s[72:73] offset:1024 sc1\n"
      "ds_read2st64_b32 v[24:25], v244 offset0:2 offset1:3\n"
      "v_mfma_f32_16x16x32_bf16 v[96:99], v[176:179], a[128:131], v[96:99]\n"
      "ds_read2st64_b32 v[26:27], v245 offset0:6 offset1:7\n"
      "ds_read2st64_b32 v[28:29], v246 offset0:8 offset1:9\n"
      "v_mfma_f32_16x16x32_bf16 v[100:103], v[176:179], a[132:135], v[100:103]\n"
      "ds_read2st64_b32 v[30:31], v246 offset0:10 offset1:11\n"
      "ds_read2st64_b32 v[52:53], v244 offset0:12 offset1:13\n"
      "v_mfma_f32_16x16x32_bf16 v[104:107], v[176:179], a[136:139], v[104:107]\n"
      "ds_read2st64_b32 v[54:55], v245 offset0:16 offset1:17\n"
      "ds_read2st64_b32 v[56:57], v244 offset0:14 offset1:15\n"
      "v_mfma_f32_16x16x32_bf16 v[108:111], v[176:179], a[140:143], v[108:111]\n"
      "ds_read2st64_b32 v[58:59], v245 offset0:18 offset1:19\n"
      "ds_read2st64_b32 v[60:61], v246 offset0:20 offset1:21\n"
      "v_mfma_f32_16x16x32_bf16 v[112:115], v[176:179], a[144:147], v[112:115]\n"
      "ds_read2st64_b32 v[62:63], v246 offset0:22 offset1:23\n"
      "v_cvt_pk_bf16_f32 v72, v72, v73\n"
      "v_mfma_f32_16x16x32_bf16 v[116:119], v[176:179], a[148:151], v[116:119]\n"
      "v_cvt_pk_bf16_f32 v73, v74, v75\n"
      "ds_write_addtid_b32 v72 offset:6144\n"
      "v_mfma_f32_16x16x32_bf16 v[120:123], v[176:179], a[152:155], v[120:123]\n"
      "s_waitcnt lgkmcnt(7)\n"
      "v_mfma_f32_16x16x32_bf16 v[0:3], v[250:253], v[20:23], v[0:3]\n"
      "v_cvt_pk_bf16_f32 v74, v76, v77\n"
      "v_mfma_f32_16x16x32_bf16 v[124:127], v[176:179], a[156:159], v[124:127]\n"
      "global_load_dwordx4 v[176:179], v240, s[14:15] offset:1024 sc1\n"
      "v_mfma_f32_16x16x32_bf16 v[96:99], v[180:183], a[160:163], v[96:99]\n"
      "v_mfma_f32_16x16x32_bf16 v[4:7], v[250:253], v[24:27], v[4:7]\n"
      "v_mfma_f32_16x16x32_bf16 v[100:103], v[180:183], a[164:167], v[100:103]\n"
      "v_mfma_f32_16x16x32_bf16 v[0:3], v[252:255], v[28:31], v[0:3]\n"
      "v_mfma_f32_16x16x32_bf16 v[104:107], v[180:183], a[168:171], v[104:107]\n"
      "v_mfma_f32_16x16x32_bf16 v[4:7], v[248:251], v[28:31], v[4:7]\n"
      "v_mov_b32 v11, s37\n"
      "v_mfma_f32_16x16x32_bf16 v[108:111], v[180:183], a[172:175], v[108:111]\n"
      "ds_write_addtid_b32 v73 offset:6400\n"
      "v_cvt_pk_bf16_f32 v75, v78, v79\n"
      "v_mfma_f32_16x16x32_bf16 v[112:115], v[180:183], a[176:179], v[112:115]\n"
      "ds_write_addtid_b32 v74 offset:6656\n"
      "v_cvt_pk_bf16_f32 v8, v0, v1\n"
      "v_mfma_f32_16x16x32_bf16 v[116:119], v[180:183], a[180:183], v[116:119]\n"
      "v_cvt_pk_bf16_f32 v9, v2, v3\n"
      "v_cvt_pk_bf16_f32 v10, v4, v5\n"
      "v_mfma_f32_16x16x32_bf16 v[120:123], v[180:183], a[184:187], v[120:123]\n"
      "v_cvt_pk_bf16_f32 v247, v6, v7\n"
      "v_cvt_pk_bf16_f32 v76, v80, v81\n"
      "v_mfma_f32_16x16x32_bf16 v[124:127], v[180:183], a[188:191], v[124:127]\n"
      "s_waitcnt vmcnt(14)\n"
      "global_load_dwordx4 v[180:183], v240, s[74:75] offset:1024 sc1\n"
      "v_mfma_f32_16x16x32_bf16 v[96:99], v[184:187], a[192:195], v[96:99]\n"
      "global_store_dwordx4 v242, v[8:11], s[52:53] offset:0 sc1\n"
      "s_waitcnt lgkmcnt(3)\n"
      "v_mfma_f32_16x16x32_bf16 v[100:103], v[184:187], a[196:199], v[100:103]\n"
      "v_mfma_f32_16x16x32_bf16 v[32:35], v[250:253], v[52:55], v[32:35]\n"
      "v_mfma_f32_16x16x32_bf16 v[104:107], v[184:187], a[200:203], v[104:107]\n"
      "v_mfma_f32_16x16x32_bf16 v[36:39], v[250:253], v[56:59], v[36:39]\n"
      "v_mfma_f32_16x16x32_bf16 v[108:111], v[184:187], a[204:207], v[108:111]\n"
      "v_mfma_f32_16x16x32_bf16 v[32:35], v[252:255], v[60:63], v[32:35]\n"
      "v_mfma_f32_16x16x32_bf16 v[112:115], v[184:187], a[208:211], v[112:115]\n"
      "v_mfma_f32_16x16x32_bf16 v[36:39], v[248:251], v[60:63], v[36:39]\n"
      "v_mov_b32 v43, s37\n"
      "v_mfma_f32_16x16x32_bf16 v[116:119], v[184:187], a[212:215], v[116:119]\n"
      "ds_write_addtid_b32 v75 offset:6912\n"
      "v_mov_b32 v44, v247\n"
      "v_mfma_f32_16x16x32_bf16 v[120:123], v[184:187], a[216:219], v[120:123]\n"
      "v_mov_b32 v47, s37\n"
      "v_cvt_pk_bf16_f32 v77, v82, v83\n"
      "v_mfma_f32_16x16x32_bf16 v[124:127], v[184:187], a[220:223], v[124:127]\n"
      "global_load_dwordx4 v[184:187], v241, s[14:15] offset:1024 sc1\n"
      "v_mfma_f32_16x16x32_bf16 v[96:99], v[188:191], a[224:227], v[96:99]\n"
      "ds_write_addtid_b32 v76 offset:7168\n"
      "v_cvt_pk_bf16_f32 v40, v32, v33\n"
      "v_mfma_f32_16x16x32_bf16 v[100:103], v[188:191], a[228:231], v[100:103]\n"
      "v_cvt_pk_bf16_f32 v41, v34, v35\n"
      "v_cvt_pk_bf16_f32 v42, v36, v37\n"
      "v_mfma_f32_16x16x32_bf16 v[104:107], v[188:191], a[232:235], v[104:107]\n"
      "v_cvt_pk_bf16_f32 v45, v38, v39\n"
      "v_cvt_pk_bf16_f32 v78, v84, v85\n"
      "v_mfma_f32_16x16x32_bf16 v[108:111], v[188:191], a[236:239], v[108:111]\n"
      "global_store_dwordx4 v242, v[40:43], s[52:53] offset:1024 sc1\n"
      "v_mfma_f32_16x16x32_bf16 v[112:115], v[188:191], a[240:243], v[112:115]\n"
      "global_store_dwordx4 v242, v[44:47], s[52:53] offset:2048 sc1\n"
      "s_add_u32 s48, s34, 2\n"
      "v_mfma_f32_16x16x32_bf16 v[116:119], v[188:191], a[244:247], v[116:119]\n"
      "s_and_b32 s48, s48, 3\n"
      "ds_write_addtid_b32 v77 offset:7424\n"
      "s_mul_i32 s48, s48, 36864\n"
      "v_mfma_f32_16x16x32_bf16 v[120:123], v[188:191], a[248:251], v[120:123]\n"
      "s_add_u32 s48, s48, 12288\n"
      "s_add_u32 s52, s60, s48\n"
      "s_addc_u32 s53, s61, 0\n"
      "v_mfma_f32_16x16x32_bf16 v[124:127], v[188:191], a[252:255], v[124:127]\n"
      "s_waitcnt vmcnt(17)\n"
      "global_load_dwordx4 v[188:191], v241, s[74:75] offset:1024 sc1\n"
      "v_cvt_pk_bf16_f32 v79, v86, v87\n"
      "v_mfma_f32_16x16x32_bf16 v[0:3], v[128:131], a[0:3], 0\n"
      "ds_write_addtid_b32 v78 offset:7680\n"
      "v_cvt_pk_bf16_f32 v80, v88, v89\n"
      "v_mfma_f32_16x16x32_bf16 v[4:7], v[128:131], a[4:7], 0\n"
      "ds_write_addtid_b32 v79 offset:7936\n"
      "v_cvt_pk_bf16_f32 v81, v90, v91\n"
      "v_mfma_f32_16x16x32_bf16 v[8:11], v[128:131], a[8:11], 0\n"
      "ds_write_addtid_b32 v80 offset:8192\n"
      "v_cvt_pk_bf16_f32 v82, v92, v93\n"
      "v_mfma_f32_16x16x32_bf16 v[12:15], v[128:131], a[12:15], 0\n"
      "ds_write_addtid_b32 v81 offset:8448\n"
      "v_cvt_pk_bf16_f32 v83, v94, v95\n"
      "v_mfma_f32_16x16x32_bf16 v[16:19], v[128:131], a[16:19], 0\n"
      "ds_write_addtid_b32 v82 offset:8704\n"
      "v_mfma_f32_16x16x32_bf16 v[20:23], v[128:131], a[20:23], 0\n"
      "ds_write_addtid_b32 v83 offset:8960\n"
      "v_cvt_pk_bf16_f32 v104, v104, v105\n"
      "v_mfma_f32_16x16x32_bf16 v[24:27], v[128:131], a[24:27], 0\n"
      "v_cvt_pk_bf16_f32 v105, v106, v107\n"
      "v_mfma_f32_16x16x32_bf16 v[28:31], v[128:131], a[28:31], 0\n"
      "global_load_dwordx4 v[128:131], v240, s[10:11] offset:2048 sc1\n"
      "v_mfma_f32_16x16x32_bf16 v[0:3], v[132:135], a[32:35], v[0:3]\n"
      "ds_write_addtid_b32 v104 offset:9216\n"
      "v_cvt_pk_bf16_f32 v106, v108, v109\n"
      "v_mfma_f32_16x16x32_bf16 v[4:7], v[132:135], a[36:39], v[4:7]\n"
      "ds_write_addtid_b32 v105 offset:9472\n"
      "v_cvt_pk_bf16_f32 v107, v110, v111\n"
      "v_mfma_f32_16x16x32_bf16 v[8:11], v[132:135], a[40:43], v[8:11]\n"
      "ds_write_addtid_b32 v106 offset:9728\n"
      "v_cvt_pk_bf16_f32 v108, v112, v113\n"
      "v_mfma_f32_16x16x32_bf16 v[12:15], v[132:135], a[44:47], v[12:15]\n"
      "ds_write_addtid_b32 v107 offset:9984\n"
      "v_cvt_pk_bf16_f32 v109, v114, v115\n"
      "v_mfma_f32_16x16x32_bf16 v[16:19], v[132:135], a[48:51], v[16:19]\n"
      "ds_write_addtid_b32 v108 offset:10240\n"
      "v_cvt_pk_bf16_f32 v110, v116, v117\n"
      "v_mfma_f32_16x16x32_bf16 v[20:23], v[132:135], a[52:55], v[20:23]\n"
      "ds_write_addtid_b32 v109 offset:10496\n"
      "v_cvt_pk_bf16_f32 v111, v118, v119\n"
      "v_mfma_f32_16x16x32_bf16 v[24:27], v[132:135], a[56:59], v[24:27]\n"
      "ds_write_addtid_b32 v110 offset:10752\n"
      "v_cvt_pk_bf16_f32 v112, v120, v121\n"
      "v_mfma_f32_16x16x32_bf16 v[28:31], v[132:135], a[60:63], v[28:31]\n"
      "s_waitcnt vmcnt(17)\n"
      "global_load_dwordx4 v[132:135], v240, s[72:73] offset:2048 sc1\n"
      "v_mfma_f32_16x16x32_bf16 v[0:3], v[136:139], a[64:67], v[0:3]\n"
      "ds_write_addtid_b32 v111 offset:11008\n"
      "v_cvt_pk_bf16_f32 v113, v122, v123\n"
      "v_mfma_f32_16x16x32_bf16 v[4:7], v[136:139], a[68:71], v[4:7]\n"
      "ds_write_addtid_b32 v112 offset:11264\n"
      "v_cvt_pk_bf16_f32 v114, v124, v125\n"
      "v_mfma_f32_16x16x32_bf16 v[8:11], v[136:139], a[72:75], v[8:11]\n"
      "ds_write_addtid_b32 v113 offset:11520\n"
      "v_cvt_pk_bf16_f32 v115, v126, v127\n"
      "v_mfma_f32_16x16x32_bf16 v[12:15], v[136:139], a[76:79], v[12:15]\n"
      "ds_write_addtid_b32 v114 offset:11776\n"
      "v_mfma_f32_16x16x32_bf16 v[16:19], v[136:139], a[80:83], v[16:19]\n"
      "ds_write_addtid_b32 v115 offset:12032\n"
      "v_mfma_f32_16x16x32_bf16 v[20:23], v[136:139], a[84:87], v[20:23]\n"
      "v_mfma_f32_16x16x32_bf16 v[24:27], v[136:139], a[88:91], v[24:27]\n"
      "v_mfma_f32_16x16x32_bf16 v[28:31], v[136:139], a[92:95], v[28:31]\n"
      "global_load_dwordx4 v[136:139], v241, s[10:11] offset:2048 sc1\n"
      "v_mfma_f32_16x16x32_bf16 v[0:3], v[140:143], a[96:99], v[0:3]\n"
      "s_waitcnt lgkmcnt(0)\n"
      "s_barrier\n"
      "v_mfma_f32_16x16x32_bf16 v[4:7], v[140:143], a[100:103], v[4:7]\n"
      "ds_read2st64_b32 v[84:85], v244 offset0:24 offset1:25\n"
      "v_mfma_f32_16x16x32_bf16 v[8:11], v[140:143], a[104:107], v[8:11]\n"
      "ds_read2st64_b32 v[86:87], v245 offset0:28 offset1:29\n"
      "v_mfma_f32_16x16x32_bf16 v[12:15], v[140:143], a[108:111], v[12:15]\n"
      "ds_read2st64_b32 v[88:89], v244 offset0:26 offset1:27\n"
      "v_mfma_f32_16x16x32_bf16 v[16:19], v[140:143], a[112:115], v[16:19]\n"
      "ds_read2st64_b32 v[90:91], v245 offset0:30 offset1:31\n"
      "v_mfma_f32_16x16x32_bf16 v[20:23], v[140:143], a[116:119], v[20:23]\n"
      "ds_read2st64_b32 v[92:93], v246 offset0:32 offset1:33\n"
      "v_mfma_f32_16x16x32_bf16 v[24:27], v[140:143], a[120:123], v[24:27]\n"
      "ds_read2st64_b32 v[94:95], v246 offset0:34 offset1:35\n"
      "v_mfma_f32_16x16x32_bf16 v[28:31], v[140:143], a[124:127], v[28:31]\n"
      "s_waitcnt vmcnt(17)\n"
      "global_load_dwordx4 v[140:143], v241, s[72:73] offset:2048 sc1\n"
      "v_mfma_f32_16x16x32_bf16 v[0:3], v[144:147], a[128:131], v[0:3]\n"
      "ds_read2st64_b32 v[116:117], v244 offset0:36 offset1:37\n"
      "v_mfma_f32_16x16x32_bf16 v[4:7], v[144:147], a[132:135], v[4:7]\n"
      "ds_read2st64_b32 v[118:119], v245 offset0:40 offset1:41\n"
      "v_mfma_f32_16x16x32_bf16 v[8:11], v[144:147], a[136:139], v[8:11]\n"
      "ds_read2st64_b32 v[120:121], v244 offset0:38 offset1:39\n"
      "v_mfma_f32_16x16x32_bf16 v[12:15], v[144:147], a[140:143], v[12:15]\n"
      "s_waitcnt lgkmcnt(3)\n"
      "v_mfma_f32_16x16x32_bf16 v[64:67], v[250:253], v[84:87], v[64:67]\n"
      "v_mfma_f32_16x16x32_bf16 v[16:19], v[144:147], a[144:147], v[16:19]\n"
      "v_mfma_f32_16x16x32_bf16 v[68:71], v[250:253], v[88:91], v[68:71]\n"
      "v_mfma_f32_16x16x32_bf16 v[20:23], v[144:147], a[148:151], v[20:23]\n"
      "v_mfma_f32_16x16x32_bf16 v[64:67], v[252:255], v[92:95], v[64:67]\n"
      "v_mfma_f32_16x16x32_bf16 v[24:27], v[144:147], a[152:155], v[24:27]\n"
      "v_mfma_f32_16x16x32_bf16 v[68:71], v[248:251], v[92:95], v[68:71]\n"
      "v_mov_b32 v75, s37\n"
      "v_mfma_f32_16x16x32_bf16 v[28:31], v[144:147], a[156:159], v[28:31]\n"
      "global_load_dwordx4 v[144:147], v240, s[14:15] offset:2048 sc1\n"
      "v_mfma_f32_16x16x32_bf16 v[0:3], v[148:151], a[160:163], v[0:3]\n"
      "ds_read2st64_b32 v[122:123], v245 offset0:42 offset1:43\n"
      "s_nop 0\n"
      "v_cvt_pk_bf16_f32 v72, v64, v65\n"
      "v_mfma_f32_16x16x32_bf16 v[4:7], v[148:151], a[164:167], v[4:7]\n"
      "ds_read2st64_b32 v[124:125], v246 offset0:44 offset1:45\n"
      "v_cvt_pk_bf16_f32 v73, v66, v67\n"
      "v_mfma_f32_16x16x32_bf16 v[8:11], v[148:151], a[168:171], v[8:11]\n"
      "ds_read2st64_b32 v[126:127], v246 offset0:46 offset1:47\n"
      "v_cvt_pk_bf16_f32 v74, v68, v69\n"
      "v_mfma_f32_16x16x32_bf16 v[12:15], v[148:151], a[172:175], v[12:15]\n"
      "global_load_dwordx4 v[224:227], v200, s[58:59] offset:0 sc1\n"
      "v_cvt_pk_bf16_f32 v247, v70, v71\n"
      "v_mfma_f32_16x16x32_bf16 v[16:19], v[148:151], a[176:179], v[16:19]\n"
      "global_load_dwordx4 v[228:231], v200, s[58:59] offset:1024 sc1\n"
      "v_mfma_f32_16x16x32_bf16 v[20:23], v[148:151], a[180:183], v[20:23]\n"
      "global_load_dwordx4 v[232:235], v200, s[58:59] offset:2048 sc1\n"
      "v_mfma_f32_16x16x32_bf16 v[24:27], v[148:151], a[184:187], v[24:27]\n"
      "global_load_dwordx4 v[236:239], v200, s[58:59] offset:3072 sc1\n"
      "v_mfma_f32_16x16x32_bf16 v[28:31], v[148:151], a[188:191], v[28:31]\n"
      "s_waitcnt vmcnt(20)\n"
      "global_load_dwordx4 v[148:151], v240, s[74:75] offset:2048 sc1\n"
      "v_mfma_f32_16x16x32_bf16 v[0:3], v[152:155], a[192:195], v[0:3]\n"
      "global_store_dwordx4 v242, v[72:75], s[52:53] offset:0 sc1\n"
      "s_waitcnt lgkmcnt(0)\n"
      "v_mfma_f32_16x16x32_bf16 v[4:7], v[152:155], a[196:199], v[4:7]\n"
      "v_mfma_f32_16x16x32_bf16 v[96:99], v[250:253], v[116:119], v[96:99]\n"
      "v_mfma_f32_16x16x32_bf16 v[8:11], v[152:155], a[200:203], v[8:11]\n"
      "v_mfma_f32_16x16x32_bf16 v[100:103], v[250:253], v[120:123], v[100:103]\n"
      "v_mfma_f32_16x16x32_bf16 v[12:15], v[152:155], a[204:207], v[12:15]\n"
      "v_mfma_f32_16x16x32_bf16 v[96:99], v[252:255], v[124:127], v[96:99]\n"
      "v_mfma_f32_16x16x32_bf16 v[16:19], v[152:155], a[208:211], v[16:19]\n"
      "v_mfma_f32_16x16x32_bf16 v[100:103], v[248:251], v[124:127], v[100:103]\n"
      "v_mov_b32 v107, s37\n"
      "v_mfma_f32_16x16x32_bf16 v[20:23], v[152:155], a[212:215], v[20:23]\n"
      "v_mov_b32 v108, v247\n"
      "v_mov_b32 v111, s37\n"
      "v_mfma_f32_16x16x32_bf16 v[24:27], v[152:155], a[216:219], v[24:27]\n"
      "v_mfma_f32_16x16x32_bf16 v[28:31], v[152:155], a[220:223], v[28:31]\n"
      "global_load_dwordx4 v[152:155], v241, s[14:15] offset:2048 sc1\n"
      "v_cvt_pk_bf16_f32 v104, v96, v97\n"
      "v_mfma_f32_16x16x32_bf16 v[0:3], v[156:159], a[224:227], v[0:3]\n"
      "v_cvt_pk_bf16_f32 v105, v98, v99\n"
      "v_cvt_pk_bf16_f32 v106, v100, v101\n"
      "v_mfma_f32_16x16x32_bf16 v[4:7], v[156:159], a[228:231], v[4:7]\n"
      "v_cvt_pk_bf16_f32 v109, v102, v103\n"
      "v_mfma_f32_16x16x32_bf16 v[8:11], v[156:159], a[232:235], v[8:11]\n"
      "global_store_dwordx4 v242, v[104:107], s[52:53] offset:1024 sc1\n"
      "v_mfma_f32_16x16x32_bf16 v[12:15], v[156:159], a[236:239], v[12:15]\n"
      "global_store_dwordx4 v242, v[108:111], s[52:53] offset:2048 sc1\n"
      "s_add_u32 s48, s34, 3\n"
      "v_mfma_f32_16x16x32_bf16 v[16:19], v[156:159], a[240:243], v[16:19]\n"
      "s_and_b32 s48, s48, 3\n"
      "s_mul_i32 s48, s48, 36864\n"
      "s_add_u32 s48, s48, 24576\n"
      "v_mfma_f32_16x16x32_bf16 v[20:23], v[156:159], a[244:247], v[20:23]\n"
      "s_add_u32 s52, s60, s48\n"
      "s_addc_u32 s53, s61, 0\n"
      "v_mfma_f32_16x16x32_bf16 v[24:27], v[156:159], a[248:251], v[24:27]\n"
      "v_mfma_f32_16x16x32_bf16 v[28:31], v[156:159], a[252:255], v[28:31]\n"
      "s_waitcnt vmcnt(23)\n"
      "global_load_dwordx4 v[156:159], v241, s[74:75] offset:2048 sc1\n"
      "v_mfma_f32_16x16x32_bf16 v[32:35], v[160:163], a[0:3], 0\n"
      "v_mfma_f32_16x16x32_bf16 v[36:39], v[160:163], a[4:7], 0\n"
      "v_cvt_pk_bf16_f32 v8, v8, v9\n"
      "v_cvt_pk_bf16_f32 v9, v10, v11\n"
      "v_mfma_f32_16x16x32_bf16 v[40:43], v[160:163], a[8:11], 0\n"
      "ds_write_addtid_b32 v8 offset:0\n"
      "v_cvt_pk_bf16_f32 v10, v12, v13\n"
      "v_mfma_f32_16x16x32_bf16 v[44:47], v[160:163], a[12:15], 0\n"
      "ds_write_addtid_b32 v9 offset:256\n"
      "v_cvt_pk_bf16_f32 v11, v14, v15\n"
      "v_mfma_f32_16x16x32_bf16 v[48:51], v[160:163], a[16:19], 0\n"
      "ds_write_addtid_b32 v10 offset:512\n"
      "v_cvt_pk_bf16_f32 v12, v16, v17\n"
      "v_mfma_f32_16x16x32_bf16 v[52:55], v[160:163], a[20:23], 0\n"
      "ds_write_addtid_b32 v11 offset:768\n"
      "v_cvt_pk_bf16_f32 v13, v18, v19\n"
      "v_mfma_f32_16x16x32_bf16 v[56:59], v[160:163], a[24:27], 0\n"
      "ds_write_addtid_b32 v12 offset:1024\n"
      "v_cvt_pk_bf16_f32 v14, v20, v21\n"
      "v_mfma_f32_16x16x32_bf16 v[60:63], v[160:163], a[28:31], 0\n"
      "global_load_dwordx4 v[160:163], v240, s[10:11] offset:3072 sc1\n"
      "v_mfma_f32_16x16x32_bf16 v[32:35], v[164:167], a[32:35], v[32:35]\n"
      "ds_write_addtid_b32 v13 offset:1280\n"
      "v_cvt_pk_bf16_f32 v15, v22, v23\n"
      "v_mfma_f32_16x16x32_bf16 v[36:39], v[164:167], a[36:39], v[36:39]\n"
      "ds_write_addtid_b32 v14 offset:1536\n"
      "v_cvt_pk_bf16_f32 v16, v24, v25\n"
      "v_mfma_f32_16x16x32_bf16 v[40:43], v[164:167], a[40:43], v[40:43]\n"
      "ds_write_addtid_b32 v15 offset:1792\n"
      "v_cvt_pk_bf16_f32 v17, v26, v27\n"
      "v_mfma_f32_16x16x32_bf16 v[44:47], v[164:167], a[44:47], v[44:47]\n"
      "ds_write_addtid_b32 v16 offset:2048\n"
      "v_cvt_pk_bf16_f32 v18, v28, v29\n"
      "v_mfma_f32_16x16x32_bf16 v[48:51], v[164:167], a[48:51], v[48:51]\n"
      "ds_write_addtid_b32 v17 offset:2304\n"
      "v_cvt_pk_bf16_f32 v19, v30, v31\n"
      "v_mfma_f32_16x16x32_bf16 v[52:55], v[164:167], a[52:55], v[52:55]\n"
      "ds_write_addtid_b32 v18 offset:2560\n"
      "v_mfma_f32_16x16x32_bf16 v[56:59], v[164:167], a[56:59], v[56:59]\n"
      "ds_write_addtid_b32 v19 offset:2816\n"
      "v_mfma_f32_16x16x32_bf16 v[60:63], v[164:167], a[60:63], v[60:63]\n"
      "s_waitcnt vmcnt(23)\n"
      "global_load_dwordx4 v[164:167], v240, s[72:73] offset:3072 sc1\n"
      "v_mfma_f32_16x16x32_bf16 v[32:35], v[168:171], a[64:67], v[32:35]\n"
      "v_mfma_f32_16x16x32_bf16 v[36:39], v[168:171], a[68:71], v[36:39]\n"
      "s_waitcnt lgkmcnt(0)\n"
      "s_barrier\n"
      "v_mfma_f32_16x16x32_bf16 v[40:43], v[168:171], a[72:75], v[40:43]\n"
      "ds_read2st64_b32 v[20:21], v244 offset0:0 offset1:1\n"
      "v_mfma_f32_16x16x32_bf16 v[44:47], v[168:171], a[76:79], v[44:47]\n"
      "ds_read2st64_b32 v[22:23], v245 offset0:4 offset1:5\n"
      "v_mfma_f32_16x16x32_bf16 v[48:51], v[168:171], a[80:83], v[48:51]\n"
      "ds_read2st64_b32 v[24:25], v244 offset0:2 offset1:3\n"
      "v_mfma_f32_16x16x32_bf16 v[52:55], v[168:171], a[84:87], v[52:55]\n"
      "ds_read2st64_b32 v[26:27], v245 offset0:6 offset1:7\n"
      "v_mfma_f32_16x16x32_bf16 v[56:59], v[168:171], a[88:91], v[56:59]\n"
      "ds_read2st64_b32 v[28:29], v246 offset0:8 offset1:9\n"
      "v_mfma_f32_16x16x32_bf16 v[60:63], v[168:171], a[92:95], v[60:63]\n"
      "global_load_dwordx4 v[168:171], v241, s[10:11] offset:3072 sc1\n"
      "v_mfma_f32_16x16x32_bf16 v[32:35], v[172:175], a[96:99], v[32:35]\n"
      "ds_read2st64_b32 v[30:31], v246 offset0:10 offset1:11\n"
      "v_mfma_f32_16x16x32_bf16 v[36:39], v[172:175], a[100:103], v[36:39]\n"
      "v_mfma_f32_16x16x32_bf16 v[40:43], v[172:175], a[104:107], v[40:43]\n"
      "v_mfma_f32_16x16x32_bf16 v[44:47], v[172:175], a[108:111], v[44:47]\n"
      "v_mfma_f32_16x16x32_bf16 v[48:51], v[172:175], a[112:115], v[48:51]\n"
      "v_mfma_f32_16x16x32_bf16 v[52:55], v[172:175], a[116:119], v[52:55]\n"
      "s_waitcnt lgkmcnt(0)\n"
      "v_mfma_f32_16x16x32_bf16 v[0:3], v[250:253], v[20:23], v[0:3]\n"
      "v_mfma_f32_16x16x32_bf16 v[56:59], v[172:175], a[120:123], v[56:59]\n"
      "v_mfma_f32_16x16x32_bf16 v[4:7], v[250:253], v[24:27], v[4:7]\n"
      "v_mfma_f32_16x16x32_bf16 v[60:63], v[172:175], a[124:127], v[60:63]\n"
      "s_waitcnt vmcnt(23)\n"
      "global_load_dwordx4 v[172:175], v241, s[72:73] offset:3072 sc1\n"
      "v_mfma_f32_16x16x32_bf16 v[40:43], v[176:179], a[136:139], v[40:43]\n"
      "s_waitcnt vmcnt(10)\n"
      "v_bitop3_b32 v192, v225, v227, s37 bitop3:0x7e\n"
      "v_bitop3_b32 v192, v192, v229, s37 bitop3:0xf6\n"
      "v_bitop3_b32 v192, v192, v231, s37 bitop3:0xf6\n"
      "v_bitop3_b32 v192, v192, v233, s37 bitop3:0xf6\n"
      "v_bitop3_b32 v192, v192, v235, s37 bitop3:0xf6\n"
      "v_bitop3_b32 v192, v192, v237, s37 bitop3:0xf6\n"
      "v_bitop3_b32 v192, v192, v239, s37 bitop3:0xf6\n"
      "v_cmp_ne_u32 vcc, 0, v192\n"
      "s_nop 1\n"
      "s_cbranch_vccnz .Ls_s17_%=\n"
      ".Ls_o18_%=:\n"
      "v_mfma_f32_16x16x32_bf16 v[44:47], v[176:179], a[140:143], v[44:47]\n"
      "v_mfma_f32_16x16x32_bf16 v[0:3], v[252:255], v[28:31], v[0:3]\n"
      "v_add_f32 v192, v224, v228\n"
      "v_mfma_f32_16x16x32_bf16 v[48:51], v[176:179], a[144:147], v[48:51]\n"
      "v_mfma_f32_16x16x32_bf16 v[4:7], v[248:251], v[28:31], v[4:7]\n"
      "v_add_f32 v193, v226, v230\n"
      "v_mfma_f32_16x16x32_bf16 v[52:55], v[176:179], a[148:151], v[52:55]\n"
      "v_add_f32 v194, v232, v236\n"
      "v_add_f32 v195, v234, v238\n"
      "v_mfma_f32_16x16x32_bf16 v[56:59], v[176:179], a[152:155], v[56:59]\n"
      "ds_read_b64_tr_b16 v[224:225], v201 offset:0\n"
      "v_permlane32_swap_b32 v192, v193\n"
      "v_mfma_f32_16x16x32_bf16 v[60:63], v[176:179], a[156:159], v[60:63]\n"
      "ds_read_b64_tr_b16 v[226:227], v201 offset:512\n"
      "v_permlane32_swap_b32 v194, v195\n"
      "v_mfma_f32_16x16x32_bf16 v[40:43], v[180:183], a[168:171], v[40:43]\n"
      "ds_read_b64_tr_b16 v[228:229], v201 offset:32\n"
      "v_add_f32 v192, v192, v193\n"
      "v_mfma_f32_16x16x32_bf16 v[44:47], v[180:183], a[172:175], v[44:47]\n"
      "ds_read_b64_tr_b16 v[230:231], v201 offset:544\n"
      "v_add_f32 v194, v194, v195\n"
      "v_mfma_f32_16x16x32_bf16 v[48:51], v[180:183], a[176:179], v[48:51]\n"
      "ds_read_b64_tr_b16 v[232:233], v201 offset:64\n"
      "v_mov_b32 v193, v192\n"
      "v_mfma_f32_16x16x32_bf16 v[52:55], v[180:183], a[180:183], v[52:55]\n"
      "ds_read_b64_tr_b16 v[234:235], v201 offset:576\n"
      "v_mov_b32 v195, v194\n"
      "v_mfma_f32_16x16x32_bf16 v[56:59], v[180:183], a[184:187], v[56:59]\n"
      "ds_read_b64_tr_b16 v[236:237], v201 offset:96\n"
      "v_permlane16_swap_b32 v192, v193\n"
      "v_mfma_f32_16x16x32_bf16 v[60:63], v[180:183], a[188:191], v[60:63]\n"
      "ds_read_b64_tr_b16 v[238:239], v201 offset:608\n"
      "v_permlane16_swap_b32 v194, v195\n"
      "v_mfma_f32_16x16x32_bf16 v[40:43], v[184:187], a[200:203], v[40:43]\n"
      "v_add_f32 v192, v192, v193\n"
      "v_add_f32 v194, v194, v195\n"
      "v_mfma_f32_16x16x32_bf16 v[44:47], v[184:187], a[204:207], v[44:47]\n"
      "v_cvt_pk_bf16_f32 v192, v192, v194\n"
      "v_and_b32 v194, 0xffff0000, v192\n"
      "v_lshlrev_b32 v192, 16, v192\n"
      "v_mfma_f32_16x16x32_bf16 v[48:51], v[184:187], a[208:211], v[48:51]\n"
      "v_mul_f32 v193, 0xbfb8aa3b, v192\n"
      "v_exp_f32 v193, v193\n"
      "s_nop 0\n"
      "v_add_f32 v193, 1.0, v193\n"
      "v_mfma_f32_16x16x32_bf16 v[52:55], v[184:187], a[212:215], v[52:55]\n"
      "v_rcp_f32 v193, v193\n"
      "s_nop 0\n"
      "v_mul_f32 v192, v192, v193\n"
      "v_mfma_f32_16x16x32_bf16 v[56:59], v[184:187], a[216:219], v[56:59]\n"
      "v_cvt_pk_bf16_f32 v192, v192, v192\n"
      "v_and_b32 v192, 0xffff0000, v192\n"
      "v_mul_f32 v192, v192, v194\n"
      "v_mfma_f32_16x16x32_bf16 v[60:63], v[184:187], a[220:223], v[60:63]\n"
      "v_cvt_pk_bf16_f32 v192, v192, v192\n"
      "v_mov_b32 v11, s37\n"
      "v_mfma_f32_16x16x32_bf16 v[40:43], v[188:191], a[232:235], v[40:43]\n"
      "ds_write_b16 v202, v192\n"
      "ds_read_b128 v[192:195], v199\n"
      "v_mfma_f32_16x16x32_bf16 v[44:47], v[188:191], a[236:239], v[44:47]\n"
      "v_cvt_pk_bf16_f32 v8, v0, v1\n"
      "v_cvt_pk_bf16_f32 v9, v2, v3\n"
      "v_cvt_pk_bf16_f32 v10, v4, v5\n"
      "v_mfma_f32_16x16x32_bf16 v[48:51], v[188:191], a[240:243], v[48:51]\n"
      "v_cvt_pk_bf16_f32 v247, v6, v7\n"
      "v_mfma_f32_16x16x32_bf16 v[52:55], v[188:191], a[244:247], v[52:55]\n"
      "global_store_dwordx4 v242, v[8:11], s[52:53] offset:0 sc1\n"
      "v_mfma_f32_16x16x32_bf16 v[56:59], v[188:191], a[248:251], v[56:59]\n"
      "v_mfma_f32_16x16x32_bf16 v[60:63], v[188:191], a[252:255], v[60:63]\n"
      "v_mfma_f32_16x16x32_bf16 v[32:35], v[176:179], a[128:131], v[32:35]\n"
      "s_waitcnt lgkmcnt(0)\n"
      "v_mfma_f32_16x16x32_bf16 v[196:199], v[192:195], v[224:227], 0\n"
      "v_mfma_f32_16x16x32_bf16 v[36:39], v[176:179], a[132:135], v[36:39]\n"
      "global_load_dwordx4 v[176:179], v240, s[14:15] offset:3072 sc1\n"
      "v_mfma_f32_16x16x32_bf16 v[224:227], v[192:195], v[228:231], 0\n"
      "v_cvt_pk_bf16_f32 v40, v40, v41\n"
      "v_mfma_f32_16x16x32_bf16 v[32:35], v[180:183], a[160:163], v[32:35]\n"
      "v_mfma_f32_16x16x32_bf16 v[228:231], v[192:195], v[232:235], 0\n"
      "v_mfma_f32_16x16x32_bf16 v[232:235], v[192:195], v[236:239], 0\n"
      "v_mov_b32 v193, s37\n"
      "v_mfma_f32_16x16x32_bf16 v[36:39], v[180:183], a[164:167], v[36:39]\n"
      "global_load_dwordx4 v[180:183], v240, s[74:75] offset:3072 sc1\n"
      "v_mov_b32 v195, s37\n"
      "v_cvt_pk_bf16_f32 v41, v42, v43\n"
      "v_mfma_f32_16x16x32_bf16 v[32:35], v[184:187], a[192:195], v[32:35]\n"
      "ds_write_addtid_b32 v40 offset:3072\n"
      "v_cvt_pk_bf16_f32 v42, v44, v45\n"
      "v_mfma_f32_16x16x32_bf16 v[36:39], v[184:187], a[196:199], v[36:39]\n"
      "global_load_dwordx4 v[184:187], v241, s[14:15] offset:3072 sc1\n"
      "ds_write_addtid_b32 v41 offset:3328\n"
      "v_cndmask_b32 v192, v196, v228, s[64:65]\n"
      "v_mfma_f32_16x16x32_bf16 v[32:35], v[188:191], a[224:227], v[32:35]\n"
      "v_cndmask_b32 v194, v224, v232, s[64:65]\n"
      "v_and_b32 v192, s82, v192\n"
      "v_and_b32 v194, s82, v194\n"
      "v_mfma_f32_16x16x32_bf16 v[36:39], v[188:191], a[228:231], v[36:39]\n"
      "global_load_dwordx4 v[188:191], v241, s[74:75] offset:3072 sc1\n"
      "s_mov_b64 exec, s[66:67]\n"
      "global_store_dwordx4 v203, v[192:195], s[58:59] sc1\n"
      "s_mov_b64 exec, -1\n"
      "v_mfma_f32_16x16x32_bf16 v[72:75], v[128:131], a[8:11], 0\n"
      "s_lshl2_add_u32 s54, s36, s33\n"
      "s_lshl_b32 s55, s54, 15\n"
      "s_lshl2_add_u32 s56, s35, s34\n"
      "s_lshl_b32 s56, s56, 6\n"
      "v_mfma_f32_16x16x32_bf16 v[76:79], v[128:131], a[12:15], 0\n"
      "s_add_u32 s55, s55, s56\n"
      "v_and_b32 v8, 15, v243\n"
      "v_and_b32 v7, 48, v243\n"
      "v_lshl_or_b32 v7, v8, 10, v7\n"
      "v_mfma_f32_16x16x32_bf16 v[80:83], v[128:131], a[16:19], 0\n"
      "v_add_u32 v7, s55, v7\n"
      "v_cvt_pk_bf16_f32 v43, v46, v47\n"
      "v_mfma_f32_16x16x32_bf16 v[84:87], v[128:131], a[20:23], 0\n"
      "ds_write_addtid_b32 v42 offset:3584\n"
      "v_cvt_pk_bf16_f32 v44, v48, v49\n"
      "v_mfma_f32_16x16x32_bf16 v[88:91], v[128:131], a[24:27], 0\n"
      "ds_write_addtid_b32 v43 offset:3840\n"
      "v_cvt_pk_bf16_f32 v45, v50, v51\n"
      "v_mfma_f32_16x16x32_bf16 v[92:95], v[128:131], a[28:31], 0\n"
      "ds_write_addtid_b32 v44 offset:4096\n"
      "v_cvt_pk_bf16_f32 v46, v52, v53\n"
      "v_mfma_f32_16x16x32_bf16 v[72:75], v[132:135], a[40:43], v[72:75]\n"
      "ds_write_addtid_b32 v45 offset:4352\n"
      "v_cvt_pk_bf16_f32 v47, v54, v55\n"
      "v_mfma_f32_16x16x32_bf16 v[76:79], v[132:135], a[44:47], v[76:79]\n"
      "ds_write_addtid_b32 v46 offset:4608\n"
      "v_cvt_pk_bf16_f32 v48, v56, v57\n"
      "v_mfma_f32_16x16x32_bf16 v[80:83], v[132:135], a[48:51], v[80:83]\n"
      "ds_write_addtid_b32 v47 offset:4864\n"
      "v_cvt_pk_bf16_f32 v49, v58, v59\n"
      "v_mfma_f32_16x16x32_bf16 v[84:87], v[132:135], a[52:55], v[84:87]\n"
      "ds_write_addtid_b32 v48 offset:5120\n"
      "v_cvt_pk_bf16_f32 v50, v60, v61\n"
      "v_mfma_f32_16x16x32_bf16 v[88:91], v[132:135], a[56:59], v[88:91]\n"
      "ds_write_addtid_b32 v49 offset:5376\n"
      "v_cvt_pk_bf16_f32 v51, v62, v63\n"
      "v_mfma_f32_16x16x32_bf16 v[92:95], v[132:135], a[60:63], v[92:95]\n"
      "ds_write_addtid_b32 v50 offset:5632\n"
      "ds_write_addtid_b32 v51 offset:5888\n"
      "v_mfma_f32_16x16x32_bf16 v[72:75], v[136:139], a[72:75], v[72:75]\n"
      "v_mfma_f32_16x16x32_bf16 v[76:79], v[136:139], a[76:79], v[76:79]\n"
      "v_mfma_f32_16x16x32_bf16 v[80:83], v[136:139], a[80:83], v[80:83]\n"
      "s_waitcnt lgkmcnt(0)\n"
      "s_barrier\n"
      "v_mfma_f32_16x16x32_bf16 v[84:87], v[136:139], a[84:87], v[84:87]\n"
      "ds_read2st64_b32 v[52:53], v244 offset0:12 offset1:13\n"
      "ds_read2st64_b32 v[54:55], v245 offset0:16 offset1:17\n"
      "v_mfma_f32_16x16x32_bf16 v[88:91], v[136:139], a[88:91], v[88:91]\n"
      "ds_read2st64_b32 v[56:57], v244 offset0:14 offset1:15\n"
      "ds_read2st64_b32 v[58:59], v245 offset0:18 offset1:19\n"
      "v_add_u32 v4, 0, v242\n"
      "v_mfma_f32_16x16x32_bf16 v[92:95], v[136:139], a[92:95], v[92:95]\n"
      "global_load_dwordx4 v[12:15], v4, s[62:63] offset:0 sc1\n"
      "global_load_dwordx4 v[16:19], v4, s[62:63] offset:1024 sc1\n"
      "v_mfma_f32_16x16x32_bf16 v[72:75], v[140:143], a[104:107], v[72:75]\n"
      "global_load_dwordx4 v[20:23], v4, s[62:63] offset:2048 sc1\n"
      "ds_read2st64_b32 v[60:61], v246 offset0:20 offset1:21\n"
      "v_mfma_f32_16x16x32_bf16 v[76:79], v[140:143], a[108:111], v[76:79]\n"
      "ds_read2st64_b32 v[62:63], v246 offset0:22 offset1:23\n"
      "v_mfma_f32_16x16x32_bf16 v[80:83], v[140:143], a[112:115], v[80:83]\n"
      "v_mfma_f32_16x16x32_bf16 v[84:87], v[140:143], a[116:119], v[84:87]\n"
      "v_mfma_f32_16x16x32_bf16 v[88:91], v[140:143], a[120:123], v[88:91]\n"
      "v_mfma_f32_16x16x32_bf16 v[92:95], v[140:143], a[124:127], v[92:95]\n"
      "s_waitcnt vmcnt(18)\n"
      "v_mfma_f32_16x16x32_bf16 v[72:75], v[144:147], a[136:139], v[72:75]\n"
      "s_waitcnt lgkmcnt(0)\n"
      "v_mfma_f32_16x16x32_bf16 v[32:35], v[250:253], v[52:55], v[32:35]\n"
      "v_mfma_f32_16x16x32_bf16 v[36:39], v[250:253], v[56:59], v[36:39]\n"
      "v_mfma_f32_16x16x32_bf16 v[76:79], v[144:147], a[140:143], v[76:79]\n"
      "v_mfma_f32_16x16x32_bf16 v[32:35], v[252:255], v[60:63], v[32:35]\n"
      "v_mfma_f32_16x16x32_bf16 v[36:39], v[248:251], v[60:63], v[36:39]\n"
      "v_mov_b32 v43, s37\n"
      "v_mfma_f32_16x16x32_bf16 v[80:83], v[144:147], a[144:147], v[80:83]\n"
      "v_mov_b32 v44, v247\n"
      "v_mov_b32 v47, s37\n"
      "v_mfma_f32_16x16x32_bf16 v[84:87], v[144:147], a[148:151], v[84:87]\n"
      "v_mfma_f32_16x16x32_bf16 v[88:91], v[144:147], a[152:155], v[88:91]\n"
      "s_nop 0\n"
      "v_cvt_pk_bf16_f32 v40, v32, v33\n"
      "v_mfma_f32_16x16x32_bf16 v[92:95], v[144:147], a[156:159], v[92:95]\n"
      "v_cvt_pk_bf16_f32 v41, v34, v35\n"
      "v_cvt_pk_bf16_f32 v42, v36, v37\n"
      "v_mfma_f32_16x16x32_bf16 v[72:75], v[148:151], a[168:171], v[72:75]\n"
      "v_cvt_pk_bf16_f32 v45, v38, v39\n"
      "v_mfma_f32_16x16x32_bf16 v[76:79], v[148:151], a[172:175], v[76:79]\n"
      "global_store_dwordx4 v242, v[40:43], s[52:53] offset:1024 sc1\n"
      "v_mfma_f32_16x16x32_bf16 v[80:83], v[148:151], a[176:179], v[80:83]\n"
      "global_store_dwordx4 v242, v[44:47], s[52:53] offset:2048 sc1\n"
      "v_mfma_f32_16x16x32_bf16 v[84:87], v[148:151], a[180:183], v[84:87]\n"
      "v_mfma_f32_16x16x32_bf16 v[88:91], v[148:151], a[184:187], v[88:91]\n"
      "v_mfma_f32_16x16x32_bf16 v[92:95], v[148:151], a[188:191], v[92:95]\n"
      "s_waitcnt vmcnt(15)\n"
      "v_mfma_f32_16x16x32_bf16 v[72:75], v[152:155], a[200:203], v[72:75]\n"
      "v_mfma_f32_16x16x32_bf16 v[76:79], v[152:155], a[204:207], v[76:79]\n"
      "v_mfma_f32_16x16x32_bf16 v[80:83], v[152:155], a[208:211], v[80:83]\n"
      "v_mfma_f32_16x16x32_bf16 v[84:87], v[152:155], a[212:215], v[84:87]\n"
      "v_mfma_f32_16x16x32_bf16 v[88:91], v[152:155], a[216:219], v[88:91]\n"
      "v_mfma_f32_16x16x32_bf16 v[92:95], v[152:155], a[220:223], v[92:95]\n"
      "v_mfma_f32_16x16x32_bf16 v[72:75], v[156:159], a[232:235], v[72:75]\n"
      "v_mfma_f32_16x16x32_bf16 v[76:79], v[156:159], a[236:239], v[76:79]\n"
      "v_mfma_f32_16x16x32_bf16 v[80:83], v[156:159], a[240:243], v[80:83]\n"
      "v_mfma_f32_16x16x32_bf16 v[84:87], v[156:159], a[244:247], v[84:87]\n"
      "v_mfma_f32_16x16x32_bf16 v[88:91], v[156:159], a[248:251], v[88:91]\n"
      "v_mfma_f32_16x16x32_bf16 v[92:95], v[156:159], a[252:255], v[92:95]\n"
      "s_waitcnt vmcnt(13)\n"
      "v_mfma_f32_16x16x32_bf16 v[104:107], v[160:163], a[8:11], 0\n"
      "v_mfma_f32_16x16x32_bf16 v[108:111], v[160:163], a[12:15], 0\n"
      "v_cvt_pk_bf16_f32 v72, v72, v73\n"
      "v_cvt_pk_bf16_f32 v73, v74, v75\n"
      "v_mfma_f32_16x16x32_bf16 v[112:115], v[160:163], a[16:19], 0\n"
      "ds_write_addtid_b32 v72 offset:6144\n"
      "v_cvt_pk_bf16_f32 v74, v76, v77\n"
      "v_mfma_f32_16x16x32_bf16 v[116:119], v[160:163], a[20:23], 0\n"
      "ds_write_addtid_b32 v73 offset:6400\n"
      "v_cvt_pk_bf16_f32 v75, v78, v79\n"
      "v_mfma_f32_16x16x32_bf16 v[120:123], v[160:163], a[24:27], 0\n"
      "ds_write_addtid_b32 v74 offset:6656\n"
      "v_cvt_pk_bf16_f32 v76, v80, v81\n"
      "v_mfma_f32_16x16x32_bf16 v[124:127], v[160:163], a[28:31], 0\n"
      "ds_write_addtid_b32 v75 offset:6912\n"
      "v_cvt_pk_bf16_f32 v77, v82, v83\n"
      "v_mfma_f32_16x16x32_bf16 v[104:107], v[164:167], a[40:43], v[104:107]\n"
      "ds_write_addtid_b32 v76 offset:7168\n"
      "v_cvt_pk_bf16_f32 v78, v84, v85\n"
      "v_mfma_f32_16x16x32_bf16 v[108:111], v[164:167], a[44:47], v[108:111]\n"
      "ds_write_addtid_b32 v77 offset:7424\n"
      "v_cvt_pk_bf16_f32 v79, v86, v87\n"
      "v_mfma_f32_16x16x32_bf16 v[112:115], v[164:167], a[48:51], v[112:115]\n"
      "ds_write_addtid_b32 v78 offset:7680\n"
      "v_cvt_pk_bf16_f32 v80, v88, v89\n"
      "v_mfma_f32_16x16x32_bf16 v[116:119], v[164:167], a[52:55], v[116:119]\n"
      "ds_write_addtid_b32 v79 offset:7936\n"
      "v_cvt_pk_bf16_f32 v81, v90, v91\n"
      "v_mfma_f32_16x16x32_bf16 v[120:123], v[164:167], a[56:59], v[120:123]\n"
      "ds_write_addtid_b32 v80 offset:8192\n"
      "v_cvt_pk_bf16_f32 v82, v92, v93\n"
      "v_mfma_f32_16x16x32_bf16 v[124:127], v[164:167], a[60:63], v[124:127]\n"
      "s_waitcnt vmcnt(11)\n"
      "ds_write_addtid_b32 v81 offset:8448\n"
      "v_cvt_pk_bf16_f32 v83, v94, v95\n"
      "v_mfma_f32_16x16x32_bf16 v[104:107], v[168:171], a[72:75], v[104:107]\n"
      "ds_write_addtid_b32 v82 offset:8704\n"
      "v_mfma_f32_16x16x32_bf16 v[108:111], v[168:171], a[76:79], v[108:111]\n"
      "ds_write_addtid_b32 v83 offset:8960\n"
      "v_mfma_f32_16x16x32_bf16 v[112:115], v[168:171], a[80:83], v[112:115]\n"
      "s_waitcnt vmcnt(2)\n"
      "v_bitop3_b32 v22, v15, v19, s37 bitop3:0x7e\n"
      "v_bitop3_b32 v22, v22, v23, s37 bitop3:0xf6\n"
      "v_cmp_ne_u32 vcc, 0, v22\n"
      "s_nop 1\n"
      "s_cbranch_vccnz .Ls_s20_%=\n"
      ".Ls_o21_%=:\n"
      "v_mfma_f32_16x16x32_bf16 v[116:119], v[168:171], a[84:87], v[116:119]\n"
      "v_mov_b32 v15, v20\n"
      "v_mov_b32 v19, v21\n"
      "v_mfma_f32_16x16x32_bf16 v[120:123], v[168:171], a[88:91], v[120:123]\n"
      "s_waitcnt lgkmcnt(0)\n"
      "s_barrier\n"
      "v_mfma_f32_16x16x32_bf16 v[124:127], v[168:171], a[92:95], v[124:127]\n"
      "ds_read2st64_b32 v[84:85], v244 offset0:24 offset1:25\n"
      "v_mfma_f32_16x16x32_bf16 v[104:107], v[172:175], a[104:107], v[104:107]\n"
      "ds_read2st64_b32 v[86:87], v245 offset0:28 offset1:29\n"
      "v_add_u32 v5, 12288, v242\n"
      "v_mfma_f32_16x16x32_bf16 v[108:111], v[172:175], a[108:111], v[108:111]\n"
      "global_load_dwordx4 v[44:47], v5, s[62:63] offset:0 sc1\n"
      "v_mfma_f32_16x16x32_bf16 v[112:115], v[172:175], a[112:115], v[112:115]\n"
      "global_load_dwordx4 v[48:51], v5, s[62:63] offset:1024 sc1\n"
      "v_mfma_f32_16x16x32_bf16 v[116:119], v[172:175], a[116:119], v[116:119]\n"
      "global_load_dwordx4 v[52:55], v5, s[62:63] offset:2048 sc1\n"
      "v_mfma_f32_16x16x32_bf16 v[120:123], v[172:175], a[120:123], v[120:123]\n"
      "ds_read2st64_b32 v[88:89], v244 offset0:26 offset1:27\n"
      "v_mfma_f32_16x16x32_bf16 v[124:127], v[172:175], a[124:127], v[124:127]\n"
      "ds_read2st64_b32 v[90:91], v245 offset0:30 offset1:31\n"
      "v_mfma_f32_16x16x32_bf16 v[104:107], v[176:179], a[136:139], v[104:107]\n"
      "ds_read2st64_b32 v[92:93], v246 offset0:32 offset1:33\n"
      "v_mfma_f32_16x16x32_bf16 v[108:111], v[176:179], a[140:143], v[108:111]\n"
      "ds_read2st64_b32 v[94:95], v246 offset0:34 offset1:35\n"
      "v_mfma_f32_16x16x32_bf16 v[112:115], v[176:179], a[144:147], v[112:115]\n"
      "v_mfma_f32_16x16x32_bf16 v[64:67], v[252:255], v[12:15], 0\n"
      "v_mfma_f32_16x16x32_bf16 v[116:119], v[176:179], a[148:151], v[116:119]\n"
      "v_mfma_f32_16x16x32_bf16 v[68:71], v[248:251], v[12:15], 0\n"
      "v_mfma_f32_16x16x32_bf16 v[120:123], v[176:179], a[152:155], v[120:123]\n"
      "v_mfma_f32_16x16x32_bf16 v[96:99], v[252:255], v[16:19], 0\n"
      "v_mfma_f32_16x16x32_bf16 v[124:127], v[176:179], a[156:159], v[124:127]\n"
      "v_mfma_f32_16x16x32_bf16 v[100:103], v[248:251], v[16:19], 0\n"
      "v_mfma_f32_16x16x32_bf16 v[104:107], v[180:183], a[168:171], v[104:107]\n"
      "s_waitcnt lgkmcnt(0)\n"
      "v_mfma_f32_16x16x32_bf16 v[64:67], v[250:253], v[84:87], v[64:67]\n"
      "v_mfma_f32_16x16x32_bf16 v[108:111], v[180:183], a[172:175], v[108:111]\n"
      "v_mfma_f32_16x16x32_bf16 v[68:71], v[250:253], v[88:91], v[68:71]\n"
      "v_mfma_f32_16x16x32_bf16 v[112:115], v[180:183], a[176:179], v[112:115]\n"
      "v_mfma_f32_16x16x32_bf16 v[64:67], v[252:255], v[92:95], v[64:67]\n"
      "v_mfma_f32_16x16x32_bf16 v[116:119], v[180:183], a[180:183], v[116:119]\n"
      "v_mfma_f32_16x16x32_bf16 v[68:71], v[248:251], v[92:95], v[68:71]\n"
      "v_mfma_f32_16x16x32_bf16 v[120:123], v[180:183], a[184:187], v[120:123]\n"
      "v_mfma_f32_16x16x32_bf16 v[124:127], v[180:183], a[188:191], v[124:127]\n"
      "v_mfma_f32_16x16x32_bf16 v[104:107], v[184:187], a[200:203], v[104:107]\n"
      "v_mfma_f32_16x16x32_bf16 v[108:111], v[184:187], a[204:207], v[108:111]\n"
      "v_mfma_f32_16x16x32_bf16 v[112:115], v[184:187], a[208:211], v[112:115]\n"
      "v_mfma_f32_16x16x32_bf16 v[116:119], v[184:187], a[212:215], v[116:119]\n"
      "v_mfma_f32_16x16x32_bf16 v[120:123], v[184:187], a[216:219], v[120:123]\n"
      "v_mfma_f32_16x16x32_bf16 v[124:127], v[184:187], a[220:223], v[124:127]\n"
      "v_mfma_f32_16x16x32_bf16 v[104:107], v[188:191], a[232:235], v[104:107]\n"
      "v_mfma_f32_16x16x32_bf16 v[108:111], v[188:191], a[236:239], v[108:111]\n"
      "s_lshr_b32 s43, s42, 7\n"
      "s_cmp_eq_u32 s43, s36\n"
      "s_cbranch_scc0 .Ls_e22_%=\n"
      "s_lshl2_add_u32 s44, s35, s34\n"
      "s_lshl2_add_u32 s43, s44, s33\n"
      "s_lshl_b32 s43, s43, 12\n"
      "s_add_u32 s43, s43, 524288\n"
      "v_add_u32 v60, s43, v242\n"
      "global_load_dwordx4 v[24:27], v60, s[58:59] offset:0 sc1\n"
      "global_load_dwordx4 v[28:31], v60, s[58:59] offset:1024 sc1\n"
      "global_load_dwordx4 v[32:35], v60, s[58:59] offset:2048 sc1\n"
      "global_load_dwordx4 v[36:39], v60, s[58:59] offset:3072 sc1\n"
      "v_and_b32 v58, 15, v243\n"
      "v_lshrrev_b32 v59, 5, v243\n"
      "v_lshl_or_b32 v58, v59, 4, v58\n"
      "s_lshl4_add_u32 s44, s42, s44\n"
      "s_lshl_b32 s44, s44, 6\n"
      "v_lshl_add_u32 v59, v58, 1, s44\n"
      "global_load_ushort v57, v59, s[16:17]\n"
      "s_mov_b32 s44, 131072\n"
      "v_lshl_add_u32 v58, v58, 2, s44\n"
      "s_lshl_b32 s43, s33, 13\n"
      "v_add_u32 v59, s43, v58\n"
      ".Ls_e22_%=:\n"
      "v_mfma_f32_16x16x32_bf16 v[112:115], v[188:191], a[240:243], v[112:115]\n"
      "v_mfma_f32_16x16x32_bf16 v[116:119], v[188:191], a[244:247], v[116:119]\n"
      "v_mfma_f32_16x16x32_bf16 v[120:123], v[188:191], a[248:251], v[120:123]\n"
      "v_mfma_f32_16x16x32_bf16 v[124:127], v[188:191], a[252:255], v[124:127]\n"
      "v_mfma_f32_16x16x32_bf16 v[64:67], v[128:131], a[0:3], v[64:67]\n"
      "v_mfma_f32_16x16x32_bf16 v[68:71], v[128:131], a[4:7], v[68:71]\n"
      "v_cvt_pk_bf16_f32 v104, v104, v105\n"
      "v_cvt_pk_bf16_f32 v105, v106, v107\n"
      "v_mfma_f32_16x16x32_bf16 v[64:67], v[132:135], a[32:35], v[64:67]\n"
      "ds_write_addtid_b32 v104 offset:9216\n"
      "v_cvt_pk_bf16_f32 v106, v108, v109\n"
      "v_mfma_f32_16x16x32_bf16 v[68:71], v[132:135], a[36:39], v[68:71]\n"
      "ds_write_addtid_b32 v105 offset:9472\n"
      "v_cvt_pk_bf16_f32 v107, v110, v111\n"
      "v_mfma_f32_16x16x32_bf16 v[64:67], v[136:139], a[64:67], v[64:67]\n"
      "ds_write_addtid_b32 v106 offset:9728\n"
      "v_cvt_pk_bf16_f32 v108, v112, v113\n"
      "v_mfma_f32_16x16x32_bf16 v[68:71], v[136:139], a[68:71], v[68:71]\n"
      "ds_write_addtid_b32 v107 offset:9984\n"
      "v_cvt_pk_bf16_f32 v109, v114, v115\n"
      "v_mfma_f32_16x16x32_bf16 v[64:67], v[140:143], a[96:99], v[64:67]\n"
      "ds_write_addtid_b32 v108 offset:10240\n"
      "v_cvt_pk_bf16_f32 v110, v116, v117\n"
      "v_mfma_f32_16x16x32_bf16 v[68:71], v[140:143], a[100:103], v[68:71]\n"
      "ds_write_addtid_b32 v109 offset:10496\n"
      "v_cvt_pk_bf16_f32 v111, v118, v119\n"
      "v_mfma_f32_16x16x32_bf16 v[64:67], v[144:147], a[128:131], v[64:67]\n"
      "ds_write_addtid_b32 v110 offset:10752\n"
      "v_cvt_pk_bf16_f32 v112, v120, v121\n"
      "v_mfma_f32_16x16x32_bf16 v[68:71], v[144:147], a[132:135], v[68:71]\n"
      "ds_write_addtid_b32 v111 offset:11008\n"
      "v_cvt_pk_bf16_f32 v113, v122, v123\n"
      "v_mfma_f32_16x16x32_bf16 v[64:67], v[148:151], a[160:163], v[64:67]\n"
      "ds_write_addtid_b32 v112 offset:11264\n"
      "v_add_u32 v6, 24576, v242\n"
      "v_cvt_pk_bf16_f32 v114, v124, v125\n"
      "v_mfma_f32_16x16x32_bf16 v[68:71], v[148:151], a[164:167], v[68:71]\n"
      "global_load_dwordx4 v[224:227], v6, s[62:63] offset:0 sc1\n"
      "global_load_dwordx4 v[228:231], v6, s[62:63] offset:1024 sc1\n"
      "v_mfma_f32_16x16x32_bf16 v[64:67], v[152:155], a[192:195], v[64:67]\n"
      "global_load_dwordx4 v[232:235], v6, s[62:63] offset:2048 sc1\n"
      "ds_write_addtid_b32 v113 offset:11520\n"
      "v_cvt_pk_bf16_f32 v115, v126, v127\n"
      "v_mfma_f32_16x16x32_bf16 v[68:71], v[152:155], a[196:199], v[68:71]\n"
      "ds_write_addtid_b32 v114 offset:11776\n"
      "ds_write_addtid_b32 v115 offset:12032\n"
      "v_mfma_f32_16x16x32_bf16 v[64:67], v[156:159], a[224:227], v[64:67]\n"
      "s_waitcnt vmcnt(3)\n"
      "v_bitop3_b32 v54, v47, v51, s37 bitop3:0x7e\n"
      "v_bitop3_b32 v54, v54, v55, s37 bitop3:0xf6\n"
      "v_cmp_ne_u32 vcc, 0, v54\n"
      "s_nop 1\n"
      "s_cbranch_vccnz .Ls_s24_%=\n"
      ".Ls_o25_%=:\n"
      "v_mfma_f32_16x16x32_bf16 v[68:71], v[156:159], a[228:231], v[68:71]\n"
      "v_mov_b32 v47, v52\n"
      "v_mov_b32 v51, v53\n"
      "v_mfma_f32_16x16x32_bf16 v[96:99], v[160:163], a[0:3], v[96:99]\n"
      "s_lshr_b32 s43, s42, 7\n"
      "s_cmp_eq_u32 s43, s36\n"
      "s_cbranch_scc0 .Ls_e26_%=\n"
      "s_waitcnt vmcnt(3)\n"
      "v_bitop3_b32 v61, v25, v27, s37 bitop3:0x7e\n"
      "v_bitop3_b32 v61, v61, v29, s37 bitop3:0xf6\n"
      "v_bitop3_b32 v61, v61, v31, s37 bitop3:0xf6\n"
      "v_bitop3_b32 v61, v61, v33, s37 bitop3:0xf6\n"
      "v_bitop3_b32 v61, v61, v35, s37 bitop3:0xf6\n"
      "v_bitop3_b32 v61, v61, v37, s37 bitop3:0xf6\n"
      "v_bitop3_b32 v61, v61, v39, s37 bitop3:0xf6\n"
      "v_cmp_ne_u32 vcc, 0, v61\n"
      "s_nop 1\n"
      "s_cbranch_vccnz .Ls_s28_%=\n"
      ".Ls_o29_%=:\n"
      "v_add_f32 v24, v24, v28\n"
      "v_add_f32 v26, v26, v30\n"
      "v_add_f32 v32, v32, v36\n"
      "v_add_f32 v34, v34, v38\n"
      "v_add_f32 v24, v24, v32\n"
      "v_add_f32 v26, v26, v34\n"
      "s_nop 1\n"
      "v_permlane32_swap_b32 v24, v26\n"
      "v_add_f32 v24, v24, v26\n"
      "v_mov_b32 v26, v24\n"
      "s_nop 1\n"
      "v_permlane16_swap_b32 v24, v26\n"
      "v_add_f32 v24, v24, v26\n"
      "ds_write_b32 v59, v24\n"
      "s_waitcnt lgkmcnt(0)\n"
      ".Ls_e26_%=:\n"
      "v_mfma_f32_16x16x32_bf16 v[100:103], v[160:163], a[4:7], v[100:103]\n"
      "s_waitcnt lgkmcnt(0)\n"
      "s_barrier\n"
      "v_mfma_f32_16x16x32_bf16 v[96:99], v[164:167], a[32:35], v[96:99]\n"
      "s_lshr_b32 s43, s42, 5\n"
      "s_lshl2_add_u32 s44, s36, s33\n"
      "s_cmp_eq_u32 s43, s44\n"
      "s_cbranch_scc0 .Ls_e30_%=\n"
      "ds_read_b32 v24, v58 offset:0\n"
      "ds_read_b32 v25, v58 offset:8192\n"
      "ds_read_b32 v26, v58 offset:16384\n"
      "ds_read_b32 v27, v58 offset:24576\n"
      "v_mov_b32 v62, 0\n"
      "v_mov_b32 v63, s37\n"
      "s_or_b32 s43, s34, s35\n"
      "s_cmp_eq_u32 s43, 0\n"
      "s_cbranch_scc0 .Ls_z31_%=\n"
      "s_mov_b64 exec, 1\n"
      "global_store_dword v62, v63, s[58:59] sc1\n"
      ".Ls_z31_%=:\n"
      "s_mov_b64 exec, -1\n"
      "s_waitcnt vmcnt(3)\n"
      "v_lshlrev_b32 v57, 16, v57\n"
      "v_and_b32 v56, 48, v243\n"
      "s_mov_b32 s44, 131072\n"
      "v_lshl_add_u32 v56, v56, 1, s44\n"
      "s_waitcnt lgkmcnt(0)\n"
      "v_add_f32 v24, v24, v25\n"
      "s_waitcnt lgkmcnt(0)\n"
      "v_add_f32 v26, v26, v27\n"
      "v_add_f32 v24, v24, v26\n"
      "v_cvt_pk_bf16_f32 v24, v24, v24\n"
      "v_and_b32 v24, 0xffff0000, v24\n"
      "v_mul_f32 v24, s84, v24\n"
      "v_cvt_pk_bf16_f32 v24, v24, v24\n"
      "v_and_b32 v24, 0xffff0000, v24\n"
      "v_add_f32 v24, v24, v57\n"
      "v_cvt_pk_bf16_f32 v24, v24, v24\n"
      "v_and_b32 v24, 0xffff0000, v24\n"
      "v_sub_f32 v24, v24, v57\n"
      "ds_write_b32 v58, v24 offset:1024\n"
      "ds_read_b128 v[28:31], v56 offset:1024\n"
      "ds_read_b128 v[32:35], v56 offset:1040\n"
      ".Ls_e30_%=:\n"
      "v_mfma_f32_16x16x32_bf16 v[100:103], v[164:167], a[36:39], v[100:103]\n"
      "ds_read2st64_b32 v[116:117], v244 offset0:36 offset1:37\n"
      "ds_read2st64_b32 v[118:119], v245 offset0:40 offset1:41\n"
      "v_mfma_f32_16x16x32_bf16 v[96:99], v[168:171], a[64:67], v[96:99]\n"
      "ds_read2st64_b32 v[120:121], v244 offset0:38 offset1:39\n"
      "ds_read2st64_b32 v[122:123], v245 offset0:42 offset1:43\n"
      "v_mfma_f32_16x16x32_bf16 v[100:103], v[168:171], a[68:71], v[100:103]\n"
      "ds_read2st64_b32 v[124:125], v246 offset0:44 offset1:45\n"
      "ds_read2st64_b32 v[126:127], v246 offset0:46 offset1:47\n"
      "v_mfma_f32_16x16x32_bf16 v[96:99], v[172:175], a[96:99], v[96:99]\n"
      "v_mfma_f32_16x16x32_bf16 v[64:67], v[252:255], v[44:47], v[64:67]\n"
      "v_mfma_f32_16x16x32_bf16 v[68:71], v[248:251], v[44:47], v[68:71]\n"
      "v_mfma_f32_16x16x32_bf16 v[100:103], v[172:175], a[100:103], v[100:103]\n"
      "v_mfma_f32_16x16x32_bf16 v[96:99], v[252:255], v[48:51], v[96:99]\n"
      "v_mfma_f32_16x16x32_bf16 v[100:103], v[248:251], v[48:51], v[100:103]\n"
      "v_mfma_f32_16x16x32_bf16 v[96:99], v[176:179], a[128:131], v[96:99]\n"
      "v_mfma_f32_16x16x32_bf16 v[100:103], v[176:179], a[132:135], v[100:103]\n"
      "v_mfma_f32_16x16x32_bf16 v[96:99], v[180:183], a[160:163], v[96:99]\n"
      "s_waitcnt lgkmcnt(0)\n"
      "v_mfma_f32_16x16x32_bf16 v[96:99], v[250:253], v[116:119], v[96:99]\n"
      "v_mfma_f32_16x16x32_bf16 v[100:103], v[250:253], v[120:123], v[100:103]\n"
      "v_mfma_f32_16x16x32_bf16 v[100:103], v[180:183], a[164:167], v[100:103]\n"
      "v_mfma_f32_16x16x32_bf16 v[96:99], v[252:255], v[124:127], v[96:99]\n"
      "v_mfma_f32_16x16x32_bf16 v[100:103], v[248:251], v[124:127], v[100:103]\n"
      "v_mfma_f32_16x16x32_bf16 v[96:99], v[184:187], a[192:195], v[96:99]\n"
      "v_mfma_f32_16x16x32_bf16 v[100:103], v[184:187], a[196:199], v[100:103]\n"
      "v_mfma_f32_16x16x32_bf16 v[96:99], v[188:191], a[224:227], v[96:99]\n"
      "v_mfma_f32_16x16x32_bf16 v[100:103], v[188:191], a[228:231], v[100:103]\n"
      "s_lshr_b32 s43, s42, 5\n"
      "s_lshl2_add_u32 s44, s36, s33\n"
      "s_cmp_eq_u32 s43, s44\n"
      "s_cbranch_scc0 .Ls_e32_%=\n"
      "v_lshrrev_b32 v24, 10, v7\n"
      "v_subrev_u32 v24, s42, v24\n"
      "v_cmp_eq_u32 vcc, 0, v24\n"
      "s_nop 1\n"
      "s_mov_b64 exec, vcc\n"
      "v_fma_f32 v208, v205, v28, v208\n"
      "v_fma_f32 v209, v205, v29, v209\n"
      "v_fma_f32 v210, v205, v30, v210\n"
      "v_fma_f32 v211, v205, v31, v211\n"
      "v_fma_f32 v216, v205, v32, v216\n"
      "v_fma_f32 v217, v205, v33, v217\n"
      "v_fma_f32 v218, v205, v34, v218\n"
      "v_fma_f32 v219, v205, v35, v219\n"
      "s_mov_b64 exec, -1\n"
      "v_cmp_eq_u32 vcc, -16, v24\n"
      "s_nop 1\n"
      "s_mov_b64 exec, vcc\n"
      "v_fma_f32 v212, v207, v28, v212\n"
      "v_fma_f32 v213, v207, v29, v213\n"
      "v_fma_f32 v214, v207, v30, v214\n"
      "v_fma_f32 v215, v207, v31, v215\n"
      "v_fma_f32 v220, v207, v32, v220\n"
      "v_fma_f32 v221, v207, v33, v221\n"
      "v_fma_f32 v222, v207, v34, v222\n"
      "v_fma_f32 v223, v207, v35, v223\n"
      "s_mov_b64 exec, -1\n"
      ".Ls_e32_%=:\n"
      "s_waitcnt vmcnt(0)\n"
      "v_bitop3_b32 v234, v227, v231, s37 bitop3:0x7e\n"
      "v_bitop3_b32 v234, v234, v235, s37 bitop3:0xf6\n"
      "v_cmp_ne_u32 vcc, 0, v234\n"
      "s_nop 1\n"
      "s_cbranch_vccnz .Ls_s34_%=\n"
      ".Ls_o35_%=:\n"
      "v_mov_b32 v227, v232\n"
      "v_mov_b32 v231, v233\n"
      "s_nop 0\n"
      "v_mfma_f32_16x16x32_bf16 v[64:67], v[252:255], v[224:227], v[64:67]\n"
      "v_mfma_f32_16x16x32_bf16 v[96:99], v[252:255], v[228:231], v[96:99]\n"
      "v_mfma_f32_16x16x32_bf16 v[68:71], v[248:251], v[224:227], v[68:71]\n"
      "v_mfma_f32_16x16x32_bf16 v[100:103], v[248:251], v[228:231], v[100:103]\n"
      "s_nop 4\n"
      "v_fma_f32 v208, v204, v64, v208\n"
      "v_fma_f32 v209, v204, v65, v209\n"
      "v_fma_f32 v210, v204, v66, v210\n"
      "v_fma_f32 v211, v204, v67, v211\n"
      "v_fma_f32 v216, v204, v96, v216\n"
      "v_fma_f32 v217, v204, v97, v217\n"
      "v_fma_f32 v218, v204, v98, v218\n"
      "v_fma_f32 v219, v204, v99, v219\n"
      "v_cvt_pk_bf16_f32 v72, v208, v209\n"
      "v_cvt_pk_bf16_f32 v73, v210, v211\n"
      "v_cvt_pk_bf16_f32 v74, v216, v217\n"
      "v_cvt_pk_bf16_f32 v75, v218, v219\n"
      "global_store_dwordx4 v7, v[72:75], s[20:21]\n"
      "v_fma_f32 v212, v206, v68, v212\n"
      "v_fma_f32 v213, v206, v69, v213\n"
      "v_fma_f32 v214, v206, v70, v214\n"
      "v_fma_f32 v215, v206, v71, v215\n"
      "v_fma_f32 v220, v206, v100, v220\n"
      "v_fma_f32 v221, v206, v101, v221\n"
      "v_fma_f32 v222, v206, v102, v222\n"
      "v_fma_f32 v223, v206, v103, v223\n"
      "v_cvt_pk_bf16_f32 v76, v212, v213\n"
      "v_cvt_pk_bf16_f32 v77, v214, v215\n"
      "v_cvt_pk_bf16_f32 v78, v220, v221\n"
      "v_cvt_pk_bf16_f32 v79, v222, v223\n"
      "global_store_dwordx4 v7, v[76:79], s[68:69]\n"
      "s_endpgm\n"
      ".Lprod_%=:\n"
      "s_add_u32 s58, s18, 16777216\n"
      "s_addc_u32 s59, s19, 0\n"
      "v_mov_b32 v235, 0\n"
      "global_load_dword v235, v235, s[58:59] sc1\n"
      "v_and_b32 v238, 15, v243\n"
      "v_lshrrev_b32 v239, 4, v243\n"
      "v_xor_b32 v239, v239, v238\n"
      "v_and_b32 v239, 7, v239\n"
      "v_lshlrev_b32 v239, 4, v239\n"
      "v_lshl_or_b32 v238, v238, 7, v239\n"
      "v_add_u32 v248, s41, v238\n"
      "v_xor_b32 v249, 64, v248\n"
      "v_add_u32 v250, 65536, v238\n"
      "v_xor_b32 v251, 64, v250\n"
      "v_and_b32 v252, 8, v243\n"
      "v_lshlrev_b32 v252, 7, v252\n"
      "v_and_or_b32 v252, v243, 48, v252\n"
      "v_add_u32 v252, s41, v252\n"
      "v_add_u32 v238, s80, v242\n"
      "v_and_b32 v238, 0x3ff, v238\n"
      "v_lshl_or_b32 v244, v236, 12, v237\n"
      "v_add_u32 v245, 31744, v244\n"
      "v_add_u32 v246, 63488, v244\n"
      "v_add_u32 v247, 95232, v244\n"
      "s_bitcmp1_b32 s34, 1\n"
      "s_cselect_b64 s[8:9], s[10:11], s[8:9]\n"
      "s_and_b32 s45, s34, 1\n"
      "s_lshl2_add_u32 s45, s45, s33\n"
      "s_lshl_b32 s45, s45, 9\n"
      "s_lshl_b32 s76, s36, 19\n"
      "s_add_u32 s45, s45, s76\n"
      "s_add_u32 s8, s8, s45\n"
      "s_addc_u32 s9, s9, 0\n"
      "s_lshr_b32 s39, s32, 6\n"
      "s_and_b32 s45, s35, 2\n"
      "s_xor_b32 s39, s39, s45\n"
      "s_lshl_b32 s45, s39, 7\n"
      "s_add_u32 s76, s33, 0\n"
      "s_and_b32 s76, s76, 3\n"
      "s_lshl_b32 s76, s76, 17\n"
      "s_or_b32 s76, s76, s45\n"
      "s_add_u32 s77, s33, 1\n"
      "s_and_b32 s77, s77, 3\n"
      "s_lshl_b32 s77, s77, 17\n"
      "s_or_b32 s77, s77, s45\n"
      "s_add_u32 s78, s33, 2\n"
      "s_and_b32 s78, s78, 3\n"
      "s_lshl_b32 s78, s78, 17\n"
      "s_or_b32 s78, s78, s45\n"
      "s_add_u32 s79, s33, 3\n"
      "s_and_b32 s79, s79, 3\n"
      "s_lshl_b32 s79, s79, 17\n"
      "s_or_b32 s79, s79, s45\n"
      "s_lshl2_add_u32 s43, s34, s35\n"
      "s_and_b32 s44, s40, 31\n"
      "s_lshl_b32 s45, s43, 18\n"
      "s_lshl_b32 s42, s44, 13\n"
      "s_add_u32 s45, s45, s42\n"
      "s_add_u32 s45, s45, 4096\n"
      "s_add_u32 s62, s18, s45\n"
      "s_addc_u32 s63, s19, 0\n"
      "s_bitcmp1_b32 s34, 1\n"
      "s_cselect_b32 s85, 0x80008000, 0\n"
      "s_lshl3_add_u32 s42, s43, s83\n"
      "s_lshl2_add_u32 s42, s42, s33\n"
      "s_lshl_b32 s42, s42, 9\n"
      "s_add_u32 s42, s42, 1048576\n"
      "s_mov_b32 s64, 0xaaaaaaaa\n"
      "s_mov_b32 s65, 0xaaaaaaaa\n"
      "s_mov_b32 s66, 0xcccccccc\n"
      "s_mov_b32 s67, 0xcccccccc\n"
      "s_add_u32 s45, s80, 256\n"
      "s_and_b32 s45, s45, 0x380\n"
      "s_add_u32 s46, s68, s45\n"
      "s_addc_u32 s47, s69, 0\n"
      "s_add_u32 m0, s41, 8192\n"
      "s_nop 0\n"
      "global_load_lds_dwordx4 v240, s[46:47] offset:0\n"
      "global_load_lds_dwordx4 v241, s[46:47] offset:1024\n"
      "global_load_lds_dwordx4 v254, s[46:47] offset:2048\n"
      "global_load_lds_dwordx4 v255, s[46:47] offset:3072\n"
      "s_add_u32 s45, s80, 384\n"
      "s_and_b32 s45, s45, 0x380\n"
      "s_add_u32 s46, s68, s45\n"
      "s_addc_u32 s47, s69, 0\n"
      "s_add_u32 m0, s41, 12288\n"
      "s_nop 0\n"
      "global_load_lds_dwordx4 v240, s[46:47] offset:0\n"
      "global_load_lds_dwordx4 v241, s[46:47] offset:1024\n"
      "global_load_lds_dwordx4 v254, s[46:47] offset:2048\n"
      "global_load_lds_dwordx4 v255, s[46:47] offset:3072\n"
      "s_waitcnt vmcnt(13)\n"
      "s_barrier\n"
      "ds_read_b128 v[0:3], v248 offset:0\n"
      "ds_read_b128 v[4:7], v248 offset:2048\n"
      "ds_read_b128 v[8:11], v249 offset:0\n"
      "ds_read_b128 v[12:15], v249 offset:2048\n"
      "ds_read_b128 a[192:195], v250 offset:0\n"
      "ds_read_b128 a[196:199], v250 offset:2048\n"
      "ds_read_b128 a[200:203], v250 offset:32768\n"
      "ds_read_b128 a[204:207], v251 offset:0\n"
      "ds_read_b128 a[208:211], v251 offset:2048\n"
      "ds_read_b128 a[212:215], v251 offset:32768\n"
      "s_add_u32 s45, s34, 1\n"
      "s_and_b32 s45, s45, 3\n"
      "s_lshl_b32 s45, s45, 15\n"
      "s_or_b32 s45, s45, s81\n"
      "s_xor_b32 s45, s45, 512\n"
      "s_add_u32 s46, s12, s45\n"
      "s_addc_u32 s47, s13, 0\n"
      "s_add_u32 m0, s82, 81920\n"
      "s_nop 0\n"
      "global_load_lds_dwordx4 v240, s[46:47] offset:0\n"
      "global_load_lds_dwordx4 v241, s[46:47] offset:1024\n"
      "global_load_lds_dwordx4 v254, s[46:47] offset:2048\n"
      "global_load_lds_dwordx4 v255, s[46:47] offset:3072\n"
      "s_add_u32 s45, s34, 2\n"
      "s_and_b32 s45, s45, 3\n"
      "s_lshl_b32 s45, s45, 15\n"
      "s_or_b32 s45, s45, s81\n"
      "s_xor_b32 s45, s45, 512\n"
      "s_add_u32 s46, s12, s45\n"
      "s_addc_u32 s47, s13, 0\n"
      "s_add_u32 m0, s82, 114688\n"
      "s_nop 0\n"
      "global_load_lds_dwordx4 v240, s[46:47] offset:0\n"
      "global_load_lds_dwordx4 v241, s[46:47] offset:1024\n"
      "s_waitcnt lgkmcnt(5)\n"
      "v_mfma_f32_16x16x32_bf16 v[192:195], v[0:3], a[192:195], 0\n"
      "s_add_u32 s45, s80, 512\n"
      "s_and_b32 s45, s45, 0x380\n"
      "s_add_u32 s46, s68, s45\n"
      "s_addc_u32 s47, s69, 0\n"
      "s_add_u32 m0, s41, 0\n"
      "s_nop 0\n"
      "global_load_lds_dwordx4 v240, s[46:47] offset:0\n"
      "global_load_lds_dwordx4 v241, s[46:47] offset:1024\n"
      "global_load_lds_dwordx4 v254, s[46:47] offset:2048\n"
      "global_load_lds_dwordx4 v255, s[46:47] offset:3072\n"
      "v_mfma_f32_16x16x32_bf16 v[196:199], v[4:7], a[192:195], 0\n"
      "ds_read_b128 a[216:219], v250 offset:4096\n"
      "ds_read_b128 a[220:223], v250 offset:6144\n"
      "ds_read_b128 a[224:227], v250 offset:36864\n"
      "s_waitcnt lgkmcnt(7)\n"
      "v_mfma_f32_16x16x32_bf16 v[200:203], v[0:3], a[196:199], 0\n"
      "ds_read_b128 a[228:231], v251 offset:4096\n"
      "ds_read_b128 a[232:235], v251 offset:6144\n"
      "ds_read_b128 a[236:239], v251 offset:36864\n"
      "v_mfma_f32_16x16x32_bf16 v[204:207], v[4:7], a[196:199], 0\n"
      "s_waitcnt vmcnt(19)\n"
      "ds_read_b128 v[16:19], v248 offset:4096\n"
      "ds_read_b128 v[20:23], v248 offset:6144\n"
      "s_waitcnt lgkmcnt(11)\n"
      "v_mfma_f32_16x16x32_bf16 v[208:211], v[0:3], a[200:203], 0\n"
      "ds_read_b128 v[24:27], v249 offset:4096\n"
      "ds_read_b128 v[28:31], v249 offset:6144\n"
      "v_mfma_f32_16x16x32_bf16 v[212:215], v[4:7], a[200:203], 0\n"
      "s_waitcnt lgkmcnt(12)\n"
      "v_mfma_f32_16x16x32_bf16 v[192:195], v[8:11], a[204:207], v[192:195]\n"
      "v_mfma_f32_16x16x32_bf16 v[196:199], v[12:15], a[204:207], v[196:199]\n"
      "s_waitcnt lgkmcnt(11)\n"
      "v_mfma_f32_16x16x32_bf16 v[200:203], v[8:11], a[208:211], v[200:203]\n"
      "v_mfma_f32_16x16x32_bf16 v[204:207], v[12:15], a[208:211], v[204:207]\n"
      "s_waitcnt lgkmcnt(10)\n"
      "v_mfma_f32_16x16x32_bf16 v[208:211], v[8:11], a[212:215], v[208:211]\n"
      "v_mfma_f32_16x16x32_bf16 v[212:215], v[12:15], a[212:215], v[212:215]\n"
      "s_waitcnt lgkmcnt(3)\n"
      "v_mfma_f32_16x16x32_bf16 v[192:195], v[16:19], a[216:219], v[192:195]\n"
      "s_waitcnt lgkmcnt(0)\n"
      "s_add_u32 s45, s80, 640\n"
      "s_and_b32 s45, s45, 0x380\n"
      "s_add_u32 s46, s68, s45\n"
      "s_addc_u32 s47, s69, 0\n"
      "s_add_u32 m0, s41, 4096\n"
      "s_nop 0\n"
      "global_load_lds_dwordx4 v240, s[46:47] offset:0\n"
      "global_load_lds_dwordx4 v241, s[46:47] offset:1024\n"
      "global_load_lds_dwordx4 v254, s[46:47] offset:2048\n"
      "global_load_lds_dwordx4 v255, s[46:47] offset:3072\n"
      "v_mfma_f32_16x16x32_bf16 v[196:199], v[20:23], a[216:219], v[196:199]\n"
      "ds_read_b128 a[240:243], v250 offset:8192\n"
      "ds_read_b128 a[244:247], v250 offset:10240\n"
      "ds_read_b128 a[248:251], v250 offset:40960\n"
      "v_mfma_f32_16x16x32_bf16 v[200:203], v[16:19], a[220:223], v[200:203]\n"
      "ds_read_b128 a[252:255], v251 offset:8192\n"
      "ds_read_b128 a[192:195], v251 offset:10240\n"
      "ds_read_b128 a[196:199], v251 offset:40960\n"
      "v_mfma_f32_16x16x32_bf16 v[204:207], v[20:23], a[220:223], v[204:207]\n"
      "s_waitcnt vmcnt(18)\n"
      "ds_read_b128 v[32:35], v248 offset:8192\n"
      "ds_read_b128 v[36:39], v248 offset:10240\n"
      "v_mfma_f32_16x16x32_bf16 v[208:211], v[16:19], a[224:227], v[208:211]\n"
      "ds_read_b128 v[40:43], v249 offset:8192\n"
      "ds_read_b128 v[44:47], v249 offset:10240\n"
      "v_mfma_f32_16x16x32_bf16 v[212:215], v[20:23], a[224:227], v[212:215]\n"
      "v_mfma_f32_16x16x32_bf16 v[192:195], v[24:27], a[228:231], v[192:195]\n"
      "v_mfma_f32_16x16x32_bf16 v[196:199], v[28:31], a[228:231], v[196:199]\n"
      "v_mfma_f32_16x16x32_bf16 v[200:203], v[24:27], a[232:235], v[200:203]\n"
      "v_mfma_f32_16x16x32_bf16 v[204:207], v[28:31], a[232:235], v[204:207]\n"
      "v_mfma_f32_16x16x32_bf16 v[208:211], v[24:27], a[236:239], v[208:211]\n"
      "v_mfma_f32_16x16x32_bf16 v[212:215], v[28:31], a[236:239], v[212:215]\n"
      "s_waitcnt lgkmcnt(3)\n"
      "v_mfma_f32_16x16x32_bf16 v[192:195], v[32:35], a[240:243], v[192:195]\n"
      "s_waitcnt lgkmcnt(0)\n"
      "s_add_u32 s45, s80, 768\n"
      "s_and_b32 s45, s45, 0x380\n"
      "s_add_u32 s46, s68, s45\n"
      "s_addc_u32 s47, s69, 0\n"
      "s_add_u32 m0, s41, 8192\n"
      "s_nop 0\n"
      "global_load_lds_dwordx4 v240, s[46:47] offset:0\n"
      "global_load_lds_dwordx4 v241, s[46:47] offset:1024\n"
      "global_load_lds_dwordx4 v254, s[46:47] offset:2048\n"
      "global_load_lds_dwordx4 v255, s[46:47] offset:3072\n"
      "v_mfma_f32_16x16x32_bf16 v[196:199], v[36:39], a[240:243], v[196:199]\n"
      "v_readfirstlane_b32 s37, v235\n"
      "s_nop 1\n"
      "s_mul_i32 s37, s37, 0x9E3779B1\n"
      "v_mfma_f32_16x16x32_bf16 v[200:203], v[32:35], a[244:247], v[200:203]\n"
      "s_add_u32 s37, s37, 0x7F4A7C15\n"
      "s_mov_b32 s38, 0x8000\n"
      "ds_read_b128 a[200:203], v250 offset:12288\n"
      "ds_read_b128 a[204:207], v250 offset:14336\n"
      "v_mfma_f32_16x16x32_bf16 v[204:207], v[36:39], a[244:247], v[204:207]\n"
      "ds_read_b128 a[208:211], v250 offset:45056\n"
      "ds_read_b128 a[212:215], v251 offset:12288\n"
      "ds_read_b128 a[216:219], v251 offset:14336\n"
      "v_mfma_f32_16x16x32_bf16 v[208:211], v[32:35], a[248:251], v[208:211]\n"
      "ds_read_b128 a[220:223], v251 offset:45056\n"
      "s_waitcnt vmcnt(18)\n"
      "ds_read_b128 v[48:51], v248 offset:12288\n"
      "v_mfma_f32_16x16x32_bf16 v[212:215], v[36:39], a[248:251], v[212:215]\n"
      "ds_read_b128 v[52:55], v248 offset:14336\n"
      "ds_read_b128 v[56:59], v249 offset:12288\n"
      "ds_read_b128 v[60:63], v249 offset:14336\n"
      "v_mfma_f32_16x16x32_bf16 v[192:195], v[40:43], a[252:255], v[192:195]\n"
      "v_mfma_f32_16x16x32_bf16 v[196:199], v[44:47], a[252:255], v[196:199]\n"
      "v_mfma_f32_16x16x32_bf16 v[200:203], v[40:43], a[192:195], v[200:203]\n"
      "v_mfma_f32_16x16x32_bf16 v[204:207], v[44:47], a[192:195], v[204:207]\n"
      "v_mfma_f32_16x16x32_bf16 v[208:211], v[40:43], a[196:199], v[208:211]\n"
      "v_mfma_f32_16x16x32_bf16 v[212:215], v[44:47], a[196:199], v[212:215]\n"
      "s_waitcnt lgkmcnt(3)\n"
      "v_mfma_f32_16x16x32_bf16 v[192:195], v[48:51], a[200:203], v[192:195]\n"
      "s_waitcnt lgkmcnt(0)\n"
      "s_add_u32 s45, s80, 896\n"
      "s_and_b32 s45, s45, 0x380\n"
      "s_add_u32 s46, s68, s45\n"
      "s_addc_u32 s47, s69, 0\n"
      "s_add_u32 m0, s41, 12288\n"
      "s_nop 0\n"
      "global_load_lds_dwordx4 v240, s[46:47] offset:0\n"
      "global_load_lds_dwordx4 v241, s[46:47] offset:1024\n"
      "global_load_lds_dwordx4 v254, s[46:47] offset:2048\n"
      "global_load_lds_dwordx4 v255, s[46:47] offset:3072\n"
      "v_mfma_f32_16x16x32_bf16 v[196:199], v[52:55], a[200:203], v[196:199]\n"
      "s_waitcnt vmcnt(16)\n"
      "s_barrier\n"
      "ds_read_b128 a[224:227], v250 offset:16384\n"
      "v_mfma_f32_16x16x32_bf16 v[200:203], v[48:51], a[204:207], v[200:203]\n"
      "ds_read_b128 a[228:231], v250 offset:18432\n"
      "ds_read_b128 a[232:235], v250 offset:49152\n"
      "ds_read_b128 a[236:239], v251 offset:16384\n"
      "v_mfma_f32_16x16x32_bf16 v[204:207], v[52:55], a[204:207], v[204:207]\n"
      "ds_read_b128 a[240:243], v251 offset:18432\n"
      "ds_read_b128 a[244:247], v251 offset:49152\n"
      "s_waitcnt vmcnt(12)\n"
      "ds_read_b128 v[64:67], v248 offset:0\n"
      "v_mfma_f32_16x16x32_bf16 v[208:211], v[48:51], a[208:211], v[208:211]\n"
      "ds_read_b128 v[68:71], v248 offset:2048\n"
      "ds_read_b128 v[72:75], v249 offset:0\n"
      "ds_read_b128 v[76:79], v249 offset:2048\n"
      "v_mfma_f32_16x16x32_bf16 v[212:215], v[52:55], a[208:211], v[212:215]\n"
      "v_mfma_f32_16x16x32_bf16 v[192:195], v[56:59], a[212:215], v[192:195]\n"
      "v_mfma_f32_16x16x32_bf16 v[196:199], v[60:63], a[212:215], v[196:199]\n"
      "v_mfma_f32_16x16x32_bf16 v[200:203], v[56:59], a[216:219], v[200:203]\n"
      "v_mfma_f32_16x16x32_bf16 v[204:207], v[60:63], a[216:219], v[204:207]\n"
      "v_mfma_f32_16x16x32_bf16 v[208:211], v[56:59], a[220:223], v[208:211]\n"
      "v_mfma_f32_16x16x32_bf16 v[212:215], v[60:63], a[220:223], v[212:215]\n"
      "s_waitcnt lgkmcnt(3)\n"
      "v_mfma_f32_16x16x32_bf16 v[192:195], v[64:67], a[224:227], v[192:195]\n"
      "s_waitcnt lgkmcnt(0)\n"
      "s_add_u32 m0, s41, 0\n"
      "s_nop 0\n"
      "global_load_lds_dwordx4 v238, s[24:25]\n"
      "s_add_u32 m0, s41, 1024\n"
      "s_nop 0\n"
      "global_load_lds_dwordx4 v238, s[22:23]\n"
      "v_mfma_f32_16x16x32_bf16 v[196:199], v[68:71], a[224:227], v[196:199]\n"
      "s_add_u32 s45, s34, 2\n"
      "s_and_b32 s45, s45, 3\n"
      "s_lshl_b32 s45, s45, 15\n"
      "s_or_b32 s45, s45, s81\n"
      "s_add_u32 s46, s12, s45\n"
      "s_addc_u32 s47, s13, 0\n"
      "s_add_u32 m0, s82, 98304\n"
      "s_nop 0\n"
      "global_load_lds_dwordx4 v254, s[46:47] offset:2048\n"
      "global_load_lds_dwordx4 v255, s[46:47] offset:3072\n"
      "v_mfma_f32_16x16x32_bf16 v[200:203], v[64:67], a[228:231], v[200:203]\n"
      "ds_read_b128 a[248:251], v250 offset:20480\n"
      "ds_read_b128 a[252:255], v250 offset:22528\n"
      "ds_read_b128 a[192:195], v250 offset:53248\n"
      "v_mfma_f32_16x16x32_bf16 v[204:207], v[68:71], a[228:231], v[204:207]\n"
      "ds_read_b128 a[196:199], v251 offset:20480\n"
      "ds_read_b128 a[200:203], v251 offset:22528\n"
      "ds_read_b128 a[204:207], v251 offset:53248\n"
      "v_mfma_f32_16x16x32_bf16 v[208:211], v[64:67], a[232:235], v[208:211]\n"
      "s_waitcnt vmcnt(12)\n"
      "ds_read_b128 v[80:83], v248 offset:4096\n"
      "ds_read_b128 v[84:87], v248 offset:6144\n"
      "v_mfma_f32_16x16x32_bf16 v[212:215], v[68:71], a[232:235], v[212:215]\n"
      "ds_read_b128 v[88:91], v249 offset:4096\n"
      "ds_read_b128 v[92:95], v249 offset:6144\n"
      "v_mfma_f32_16x16x32_bf16 v[192:195], v[72:75], a[236:239], v[192:195]\n"
      "v_mfma_f32_16x16x32_bf16 v[196:199], v[76:79], a[236:239], v[196:199]\n"
      "v_mfma_f32_16x16x32_bf16 v[200:203], v[72:75], a[240:243], v[200:203]\n"
      "v_mfma_f32_16x16x32_bf16 v[204:207], v[76:79], a[240:243], v[204:207]\n"
      "v_mfma_f32_16x16x32_bf16 v[208:211], v[72:75], a[244:247], v[208:211]\n"
      "v_mfma_f32_16x16x32_bf16 v[212:215], v[76:79], a[244:247], v[212:215]\n"
      "s_waitcnt lgkmcnt(3)\n"
      "v_mfma_f32_16x16x32_bf16 v[192:195], v[80:83], a[248:251], v[192:195]\n"
      "s_add_u32 s45, s34, 2\n"
      "s_and_b32 s45, s45, 3\n"
      "s_lshl_b32 s45, s45, 15\n"
      "s_or_b32 s45, s45, s81\n"
      "s_xor_b32 s45, s45, 512\n"
      "s_add_u32 s46, s12, s45\n"
      "s_addc_u32 s47, s13, 0\n"
      "s_add_u32 m0, s82, 114688\n"
      "s_nop 0\n"
      "global_load_lds_dwordx4 v254, s[46:47] offset:2048\n"
      "global_load_lds_dwordx4 v255, s[46:47] offset:3072\n"
      "s_waitcnt lgkmcnt(2)\n"
      "v_mfma_f32_16x16x32_bf16 v[196:199], v[84:87], a[248:251], v[196:199]\n"
      "s_add_u32 s45, s34, 3\n"
      "s_and_b32 s45, s45, 3\n"
      "s_lshl_b32 s45, s45, 15\n"
      "s_or_b32 s45, s45, s81\n"
      "s_add_u32 s46, s12, s45\n"
      "s_addc_u32 s47, s13, 0\n"
      "s_add_u32 m0, s82, 131072\n"
      "s_nop 0\n"
      "global_load_lds_dwordx4 v240, s[46:47] offset:0\n"
      "global_load_lds_dwordx4 v241, s[46:47] offset:1024\n"
      "global_load_lds_dwordx4 v254, s[46:47] offset:2048\n"
      "global_load_lds_dwordx4 v255, s[46:47] offset:3072\n"
      "v_mfma_f32_16x16x32_bf16 v[200:203], v[80:83], a[252:255], v[200:203]\n"
      "ds_read_b128 a[208:211], v250 offset:24576\n"
      "ds_read_b128 a[212:215], v250 offset:26624\n"
      "ds_read_b128 a[216:219], v250 offset:57344\n"
      "v_mfma_f32_16x16x32_bf16 v[204:207], v[84:87], a[252:255], v[204:207]\n"
      "ds_read_b128 a[220:223], v251 offset:24576\n"
      "ds_read_b128 a[224:227], v251 offset:26624\n"
      "ds_read_b128 a[228:231], v251 offset:57344\n"
      "v_mfma_f32_16x16x32_bf16 v[208:211], v[80:83], a[192:195], v[208:211]\n"
      "s_waitcnt vmcnt(14)\n"
      "ds_read_b128 v[96:99], v248 offset:8192\n"
      "ds_read_b128 v[100:103], v248 offset:10240\n"
      "v_mfma_f32_16x16x32_bf16 v[212:215], v[84:87], a[192:195], v[212:215]\n"
      "ds_read_b128 v[104:107], v249 offset:8192\n"
      "ds_read_b128 v[108:111], v249 offset:10240\n"
      "s_waitcnt lgkmcnt(11)\n"
      "v_mfma_f32_16x16x32_bf16 v[192:195], v[88:91], a[196:199], v[192:195]\n"
      "s_waitcnt lgkmcnt(10)\n"
      "v_mfma_f32_16x16x32_bf16 v[196:199], v[92:95], a[196:199], v[196:199]\n"
      "v_mfma_f32_16x16x32_bf16 v[200:203], v[88:91], a[200:203], v[200:203]\n"
      "v_mfma_f32_16x16x32_bf16 v[204:207], v[92:95], a[200:203], v[204:207]\n"
      "v_mfma_f32_16x16x32_bf16 v[208:211], v[88:91], a[204:207], v[208:211]\n"
      "v_mfma_f32_16x16x32_bf16 v[212:215], v[92:95], a[204:207], v[212:215]\n"
      "s_waitcnt lgkmcnt(3)\n"
      "v_mfma_f32_16x16x32_bf16 v[192:195], v[96:99], a[208:211], v[192:195]\n"
      "s_add_u32 s45, s34, 3\n"
      "s_and_b32 s45, s45, 3\n"
      "s_lshl_b32 s45, s45, 15\n"
      "s_or_b32 s45, s45, s81\n"
      "s_xor_b32 s45, s45, 512\n"
      "s_add_u32 s46, s12, s45\n"
      "s_addc_u32 s47, s13, 0\n"
      "s_add_u32 m0, s82, 147456\n"
      "s_nop 0\n"
      "global_load_lds_dwordx4 v240, s[46:47] offset:0\n"
      "global_load_lds_dwordx4 v241, s[46:47] offset:1024\n"
      "global_load_lds_dwordx4 v254, s[46:47] offset:2048\n"
      "global_load_lds_dwordx4 v255, s[46:47] offset:3072\n"
      "s_waitcnt lgkmcnt(2)\n"
      "v_mfma_f32_16x16x32_bf16 v[196:199], v[100:103], a[208:211], v[196:199]\n"
      "ds_read_b128 a[232:235], v250 offset:28672\n"
      "ds_read_b128 a[236:239], v250 offset:30720\n"
      "ds_read_b128 a[240:243], v250 offset:61440\n"
      "v_mfma_f32_16x16x32_bf16 v[200:203], v[96:99], a[212:215], v[200:203]\n"
      "ds_read_b128 a[244:247], v251 offset:28672\n"
      "ds_read_b128 a[248:251], v251 offset:30720\n"
      "ds_read_b128 a[252:255], v251 offset:61440\n"
      "v_mfma_f32_16x16x32_bf16 v[204:207], v[100:103], a[212:215], v[204:207]\n"
      "s_waitcnt vmcnt(14)\n"
      "ds_read_b128 v[112:115], v248 offset:12288\n"
      "ds_read_b128 v[116:119], v248 offset:14336\n"
      "v_mfma_f32_16x16x32_bf16 v[208:211], v[96:99], a[216:219], v[208:211]\n"
      "ds_read_b128 v[120:123], v249 offset:12288\n"
      "ds_read_b128 v[124:127], v249 offset:14336\n"
      "v_mfma_f32_16x16x32_bf16 v[212:215], v[100:103], a[216:219], v[212:215]\n"
      "s_waitcnt lgkmcnt(11)\n"
      "v_mfma_f32_16x16x32_bf16 v[192:195], v[104:107], a[220:223], v[192:195]\n"
      "s_waitcnt lgkmcnt(10)\n"
      "v_mfma_f32_16x16x32_bf16 v[196:199], v[108:111], a[220:223], v[196:199]\n"
      "v_mfma_f32_16x16x32_bf16 v[200:203], v[104:107], a[224:227], v[200:203]\n"
      "v_mfma_f32_16x16x32_bf16 v[204:207], v[108:111], a[224:227], v[204:207]\n"
      "v_mfma_f32_16x16x32_bf16 v[208:211], v[104:107], a[228:231], v[208:211]\n"
      "v_mfma_f32_16x16x32_bf16 v[212:215], v[108:111], a[228:231], v[212:215]\n"
      "s_waitcnt lgkmcnt(3)\n"
      "v_mfma_f32_16x16x32_bf16 v[192:195], v[112:115], a[232:235], v[192:195]\n"
      "s_waitcnt vmcnt(12)\n"
      "ds_read_b128 v[136:139], v252 offset:0\n"
      "s_waitcnt lgkmcnt(3)\n"
      "v_mfma_f32_16x16x32_bf16 v[196:199], v[116:119], a[232:235], v[196:199]\n"
      "ds_read_b128 v[140:143], v252 offset:64\n"
      "s_waitcnt lgkmcnt(3)\n"
      "v_mfma_f32_16x16x32_bf16 v[192:195], v[120:123], a[244:247], v[192:195]\n"
      "ds_read_b128 v[144:147], v252 offset:128\n"
      "s_waitcnt lgkmcnt(3)\n"
      "v_mfma_f32_16x16x32_bf16 v[196:199], v[124:127], a[244:247], v[196:199]\n"
      "v_mfma_f32_16x16x32_bf16 v[200:203], v[112:115], a[236:239], v[200:203]\n"
      "ds_read_b128 v[148:151], v252 offset:192\n"
      "v_mfma_f32_16x16x32_bf16 v[204:207], v[116:119], a[236:239], v[204:207]\n"
      "v_mfma_f32_16x16x32_bf16 v[200:203], v[120:123], a[248:251], v[200:203]\n"
      "ds_read_b128 v[152:155], v252 offset:256\n"
      "v_cvt_pk_bf16_f32 v224, v192, v193\n"
      "v_cvt_pk_bf16_f32 v225, v194, v195\n"
      "v_mfma_f32_16x16x32_bf16 v[204:207], v[124:127], a[248:251], v[204:207]\n"
      "v_cvt_pk_bf16_f32 v226, v196, v197\n"
      "v_cvt_pk_bf16_f32 v227, v198, v199\n"
      "v_xor_b32 v224, s85, v224\n"
      "v_mfma_f32_16x16x32_bf16 v[208:211], v[112:115], a[240:243], v[208:211]\n"
      "ds_read_b128 v[156:159], v252 offset:320\n"
      "v_xor_b32 v225, s85, v225\n"
      "v_xor_b32 v226, s85, v226\n"
      "v_mfma_f32_16x16x32_bf16 v[212:215], v[116:119], a[240:243], v[212:215]\n"
      "v_xor_b32 v227, s85, v227\n"
      "v_mfma_f32_16x16x32_bf16 v[208:211], v[120:123], a[252:255], v[208:211]\n"
      "ds_read_b128 v[160:163], v252 offset:384\n"
      "global_store_dwordx4 v242, v[224:227], s[62:63] offset:-4096 sc1\n"
      "v_cvt_pk_bf16_f32 v228, v200, v201\n"
      "v_mfma_f32_16x16x32_bf16 v[212:215], v[124:127], a[252:255], v[212:215]\n"
      "v_cvt_pk_bf16_f32 v229, v202, v203\n"
      "v_cvt_pk_bf16_f32 v230, v204, v205\n"
      "v_cvt_pk_bf16_f32 v231, v206, v207\n"
      "s_waitcnt lgkmcnt(6)\n"
      "v_mfma_f32_16x16x32_bf16 v[128:131], v[0:3], v[136:139], 0\n"
      "ds_read_b128 v[164:167], v252 offset:448\n"
      "v_xor_b32 v228, s85, v228\n"
      "v_xor_b32 v229, s85, v229\n"
      "v_mfma_f32_16x16x32_bf16 v[132:135], v[4:7], v[136:139], 0\n"
      "v_xor_b32 v230, s85, v230\n"
      "v_xor_b32 v231, s85, v231\n"
      "s_waitcnt lgkmcnt(6)\n"
      "v_mfma_f32_16x16x32_bf16 v[128:131], v[8:11], v[140:143], v[128:131]\n"
      "ds_read_b128 v[136:139], v252 offset:512\n"
      "global_store_dwordx4 v242, v[228:231], s[62:63] offset:-3072 sc1\n"
      "v_cvt_pk_bf16_f32 v232, v208, v209\n"
      "v_mfma_f32_16x16x32_bf16 v[132:135], v[12:15], v[140:143], v[132:135]\n"
      "v_cvt_pk_bf16_f32 v233, v210, v211\n"
      "v_cvt_pk_bf16_f32 v234, v212, v213\n"
      "v_cvt_pk_bf16_f32 v235, v214, v215\n"
      "s_waitcnt lgkmcnt(6)\n"
      "v_mfma_f32_16x16x32_bf16 v[128:131], v[16:19], v[144:147], v[128:131]\n"
      "ds_read_b128 v[140:143], v252 offset:576\n"
      "v_xor_b32 v232, s85, v232\n"
      "v_xor_b32 v233, s85, v233\n"
      "v_mfma_f32_16x16x32_bf16 v[132:135], v[20:23], v[144:147], v[132:135]\n"
      "v_xor_b32 v234, s85, v234\n"
      "v_xor_b32 v235, s85, v235\n"
      "s_waitcnt lgkmcnt(6)\n"
      "v_mfma_f32_16x16x32_bf16 v[128:131], v[24:27], v[148:151], v[128:131]\n"
      "ds_read_b128 v[144:147], v252 offset:640\n"
      "global_store_dwordx4 v242, v[232:235], s[62:63] offset:-2048 sc1\n"
      "v_mfma_f32_16x16x32_bf16 v[132:135], v[28:31], v[148:151], v[132:135]\n"
      "s_xor_b32 s70, s76, 0\n"
      "s_add_u32 s70, s8, s70\n"
      "s_addc_u32 s71, s9, 0\n"
      "s_add_u32 m0, s41, 4096\n"
      "s_waitcnt lgkmcnt(6)\n"
      "v_mfma_f32_16x16x32_bf16 v[128:131], v[32:35], v[152:155], v[128:131]\n"
      "s_add_u32 s48, s82, 32768\n"
      "ds_read_b128 v[148:151], v252 offset:704\n"
      "global_load_lds_dwordx4 v244, s[70:71] offset:0\n"
      "v_subrev_u32 v236, s41, v248\n"
      "v_mfma_f32_16x16x32_bf16 v[132:135], v[36:39], v[152:155], v[132:135]\n"
      "s_add_u32 s48, s48, 65536\n"
      "s_waitcnt vmcnt(4)\n"
      "s_barrier\n"
      "s_waitcnt lgkmcnt(6)\n"
      "v_mfma_f32_16x16x32_bf16 v[128:131], v[40:43], v[156:159], v[128:131]\n"
      "ds_read_b128 v[152:155], v252 offset:768\n"
      "ds_read_b128 a[192:195], v250 offset:34816\n"
      "v_add_u32 v236, s48, v236\n"
      "v_mfma_f32_16x16x32_bf16 v[132:135], v[44:47], v[156:159], v[132:135]\n"
      "ds_read_b128 a[196:199], v251 offset:34816\n"
      "ds_read_b128 a[200:203], v250 offset:38912\n"
      "v_xor_b32 v237, 64, v236\n"
      "s_waitcnt lgkmcnt(9)\n"
      "v_mfma_f32_16x16x32_bf16 v[128:131], v[48:51], v[160:163], v[128:131]\n"
      "ds_read_b128 v[156:159], v252 offset:832\n"
      "global_load_lds_dwordx4 v245, s[70:71] offset:1024\n"
      "v_mfma_f32_16x16x32_bf16 v[132:135], v[52:55], v[160:163], v[132:135]\n"
      "ds_read_b128 a[204:207], v251 offset:38912\n"
      "ds_read_b128 a[208:211], v250 offset:43008\n"
      "s_waitcnt lgkmcnt(11)\n"
      "v_mfma_f32_16x16x32_bf16 v[128:131], v[56:59], v[164:167], v[128:131]\n"
      "ds_read_b128 v[160:163], v252 offset:896\n"
      "ds_read_b128 a[212:215], v251 offset:43008\n"
      "v_mfma_f32_16x16x32_bf16 v[132:135], v[60:63], v[164:167], v[132:135]\n"
      "ds_read_b128 a[216:219], v250 offset:47104\n"
      "s_waitcnt lgkmcnt(13)\n"
      "v_mfma_f32_16x16x32_bf16 v[128:131], v[64:67], v[136:139], v[128:131]\n"
      "ds_read_b128 v[164:167], v252 offset:960\n"
      "global_load_lds_dwordx4 v246, s[70:71] offset:2048\n"
      "v_mfma_f32_16x16x32_bf16 v[132:135], v[68:71], v[136:139], v[132:135]\n"
      "ds_read_b128 a[220:223], v251 offset:47104\n"
      "s_waitcnt lgkmcnt(14)\n"
      "v_mfma_f32_16x16x32_bf16 v[128:131], v[72:75], v[140:143], v[128:131]\n"
      "v_mfma_f32_16x16x32_bf16 v[132:135], v[76:79], v[140:143], v[132:135]\n"
      "ds_read_b128 a[224:227], v250 offset:51200\n"
      "s_waitcnt lgkmcnt(14)\n"
      "v_mfma_f32_16x16x32_bf16 v[128:131], v[80:83], v[144:147], v[128:131]\n"
      "global_load_lds_dwordx4 v247, s[70:71] offset:3072\n"
      "v_mfma_f32_16x16x32_bf16 v[132:135], v[84:87], v[144:147], v[132:135]\n"
      "ds_read_b128 a[228:231], v251 offset:51200\n"
      "s_waitcnt lgkmcnt(14)\n"
      "v_mfma_f32_16x16x32_bf16 v[128:131], v[88:91], v[148:151], v[128:131]\n"
      "v_mfma_f32_16x16x32_bf16 v[132:135], v[92:95], v[148:151], v[132:135]\n"
      "s_lshl_b32 s70, s34, 15\n"
      "s_or_b32 s70, s70, s81\n"
      "s_add_u32 s70, s12, s70\n"
      "s_addc_u32 s71, s13, 0\n"
      "s_add_u32 m0, s82, 65536\n"
      "s_waitcnt lgkmcnt(13)\n"
      "v_mfma_f32_16x16x32_bf16 v[128:131], v[96:99], v[152:155], v[128:131]\n"
      "global_load_lds_dwordx4 v240, s[70:71] offset:0\n"
      "ds_read_b128 a[232:235], v250 offset:55296\n"
      "v_mfma_f32_16x16x32_bf16 v[132:135], v[100:103], v[152:155], v[132:135]\n"
      "ds_read_b128 a[236:239], v251 offset:55296\n"
      "s_waitcnt lgkmcnt(11)\n"
      "v_mfma_f32_16x16x32_bf16 v[128:131], v[104:107], v[156:159], v[128:131]\n"
      "v_mfma_f32_16x16x32_bf16 v[132:135], v[108:111], v[156:159], v[132:135]\n"
      "ds_read_b128 a[240:243], v250 offset:59392\n"
      "s_waitcnt lgkmcnt(9)\n"
      "v_mfma_f32_16x16x32_bf16 v[128:131], v[112:115], v[160:163], v[128:131]\n"
      "global_load_lds_dwordx4 v241, s[70:71] offset:1024\n"
      "v_mfma_f32_16x16x32_bf16 v[132:135], v[116:119], v[160:163], v[132:135]\n"
      "ds_read_b128 a[244:247], v251 offset:59392\n"
      "s_waitcnt lgkmcnt(7)\n"
      "v_mfma_f32_16x16x32_bf16 v[128:131], v[120:123], v[164:167], v[128:131]\n"
      "v_mfma_f32_16x16x32_bf16 v[132:135], v[124:127], v[164:167], v[132:135]\n"
      "ds_read_b128 a[248:251], v250 offset:63488\n"
      "v_mfma_f32_16x16x32_bf16 v[216:219], v[0:3], a[192:195], 0\n"
      "global_load_lds_dwordx4 v254, s[70:71] offset:2048\n"
      "v_mfma_f32_16x16x32_bf16 v[220:223], v[4:7], a[192:195], 0\n"
      "ds_read_b128 a[252:255], v251 offset:63488\n"
      "v_xor_b32 v250, 196608, v250\n"
      "v_xor_b32 v251, 196608, v251\n"
      "v_mfma_f32_16x16x32_bf16 v[216:219], v[8:11], a[196:199], v[216:219]\n"
      "s_mov_b32 s22, 0xf0f0f0f0\n"
      "s_mov_b32 s23, 0xf0f0f0f0\n"
      "v_cndmask_b32 v128, v128, v129, s[64:65]\n"
      "v_cndmask_b32 v130, v130, v131, s[64:65]\n"
      "v_mfma_f32_16x16x32_bf16 v[220:223], v[12:15], a[196:199], v[220:223]\n"
      "ds_read_b128 a[192:195], v250 offset:0\n"
      "v_cndmask_b32 v132, v132, v133, s[64:65]\n"
      "v_cndmask_b32 v134, v134, v135, s[64:65]\n"
      "v_mfma_f32_16x16x32_bf16 v[216:219], v[16:19], a[200:203], v[216:219]\n"
      "global_load_lds_dwordx4 v255, s[70:71] offset:3072\n"
      "v_cndmask_b32 v128, v128, v130, s[66:67]\n"
      "v_cndmask_b32 v132, v132, v134, s[66:67]\n"
      "v_mfma_f32_16x16x32_bf16 v[220:223], v[20:23], a[200:203], v[220:223]\n"
      "ds_read_b128 a[196:199], v250 offset:2048\n"
      "v_lshrrev_b32 v133, 4, v243\n"
      "v_cndmask_b32 v128, v128, v132, s[22:23]\n"
      "v_mfma_f32_16x16x32_bf16 v[216:219], v[24:27], a[204:207], v[216:219]\n"
      "v_and_b32 v134, 7, v243\n"
      "v_cvt_pk_bf16_f32 v128, v128, v128\n"
      "s_lshl_b32 s48, s40, 5\n"
      "v_mfma_f32_16x16x32_bf16 v[220:223], v[28:31], a[204:207], v[220:223]\n"
      "ds_read_b128 a[200:203], v251 offset:0\n"
      "v_and_b32 v128, 0xffff0000, v128\n"
      "v_lshl_or_b32 v133, v133, 3, v134\n"
      "v_mfma_f32_16x16x32_bf16 v[216:219], v[32:35], a[208:211], v[216:219]\n"
      "s_lshl_b32 s70, s34, 15\n"
      "s_or_b32 s70, s70, s81\n"
      "s_xor_b32 s70, s70, 512\n"
      "s_add_u32 s70, s12, s70\n"
      "s_addc_u32 s71, s13, 0\n"
      "s_add_u32 m0, s82, 81920\n"
      "v_mfma_f32_16x16x32_bf16 v[220:223], v[36:39], a[208:211], v[220:223]\n"
      "global_load_lds_dwordx4 v240, s[70:71] offset:0\n"
      "ds_read_b128 a[204:207], v251 offset:2048\n"
      "v_add_u32 v133, s48, v133\n"
      "v_mfma_f32_16x16x32_bf16 v[216:219], v[40:43], a[212:215], v[216:219]\n"
      "v_ashrrev_i32 v129, 31, v128\n"
      "v_mul_f32 v130, 0xbfb8aa3b, v128\n"
      "v_or_b32 v129, 0x80000000, v129\n"
      "v_mfma_f32_16x16x32_bf16 v[220:223], v[44:47], a[212:215], v[220:223]\n"
      "ds_read_b128 a[208:211], v250 offset:4096\n"
      "v_exp_f32 v130, v130\n"
      "v_xor_b32 v129, v129, v128\n"
      "v_mfma_f32_16x16x32_bf16 v[216:219], v[48:51], a[216:219], v[216:219]\n"
      "v_sub_u32 v132, 0xfff, v133\n"
      "v_add_f32 v130, 1.0, v130\n"
      "s_mov_b32 s49, 0xffff0000\n"
      "v_mfma_f32_16x16x32_bf16 v[220:223], v[52:55], a[216:219], v[220:223]\n"
      "global_load_lds_dwordx4 v241, s[70:71] offset:1024\n"
      "ds_read_b128 a[212:215], v250 offset:6144\n"
      "v_and_or_b32 v129, v129, s49, v132\n"
      "s_waitcnt lgkmcnt(14)\n"
      "v_mfma_f32_16x16x32_bf16 v[216:219], v[56:59], a[220:223], v[216:219]\n"
      "v_rcp_f32 v130, v130\n"
      "s_mov_b32 s64, 0x00ff00ff\n"
      "s_mov_b32 s65, 0x00ff00ff\n"
      "v_mfma_f32_16x16x32_bf16 v[220:223], v[60:63], a[220:223], v[220:223]\n"
      "ds_read_b128 a[216:219], v251 offset:4096\n"
      "v_cndmask_b32 v129, v129, 0, s[64:65]\n"
      "v_cvt_pk_bf16_f32 v130, v130, v130\n"
      "s_waitcnt lgkmcnt(14)\n"
      "v_mfma_f32_16x16x32_bf16 v[216:219], v[64:67], a[224:227], v[216:219]\n"
      "s_lshr_b32 s48, s34, 1\n"
      "v_add_u32 v238, s42, v242\n"
      "v_mov_b32 v224, s37\n"
      "v_mov_b32 v225, s37\n"
      "v_mov_b32 v226, s37\n"
      "v_mov_b32 v227, s37\n"
      "s_waitcnt vmcnt(10)\n"
      "s_mov_b64 exec, 0xff\n"
      "global_store_dwordx4 v238, v[224:227], s[58:59] offset:0 sc1\n"
      "s_mov_b64 exec, -1\n"
      "v_mfma_f32_16x16x32_bf16 v[220:223], v[68:71], a[224:227], v[220:223]\n"
      "s_lshl2_add_u32 s48, s48, s35\n"
      "ds_read_b128 a[220:223], v251 offset:6144\n"
      "v_max_u32_dpp v129, v129, v129 quad_perm:[1,0,3,2] row_mask:0xf bank_mask:0xf\n"
      "v_and_b32 v130, 0xffff0000, v130\n"
      "s_waitcnt lgkmcnt(14)\n"
      "v_mfma_f32_16x16x32_bf16 v[216:219], v[72:75], a[228:231], v[216:219]\n"
      "s_lshl_b32 s49, s48, 15\n"
      "global_load_lds_dwordx4 v254, s[70:71] offset:2048\n"
      "v_max_u32_dpp v129, v129, v129 quad_perm:[2,3,0,1] row_mask:0xf bank_mask:0xf\n"
      "s_add_u32 s49, s49, 786432\n"
      "v_mfma_f32_16x16x32_bf16 v[220:223], v[76:79], a[228:231], v[220:223]\n"
      "ds_read_b128 a[224:227], v250 offset:8192\n"
      "v_lshl_add_u32 v133, v133, 4, s49\n"
      "v_max_u32_dpp v129, v129, v129 row_half_mirror row_mask:0xf bank_mask:0xf\n"
      "s_waitcnt lgkmcnt(14)\n"
      "v_mfma_f32_16x16x32_bf16 v[216:219], v[80:83], a[232:235], v[216:219]\n"
      "s_lshl_b32 s48, s48, 6\n"
      "s_add_u32 s48, s48, s40\n"
      "v_max_u32_dpp v129, v129, v129 row_mirror row_mask:0xf bank_mask:0xf\n"
      "s_lshl_b32 s48, s48, 6\n"
      "v_mfma_f32_16x16x32_bf16 v[220:223], v[84:87], a[232:235], v[220:223]\n"
      "s_add_u32 s48, s48, 4096\n"
      "ds_read_b128 a[228:231], v250 offset:10240\n"
      "v_mov_b32 v131, v129\n"
      "v_lshl_add_u32 v132, v134, 4, s48\n"
      "s_waitcnt lgkmcnt(14)\n"
      "v_mfma_f32_16x16x32_bf16 v[216:219], v[88:91], a[236:239], v[216:219]\n"
      "global_load_lds_dwordx4 v255, s[70:71] offset:3072\n"
      "v_mov_b32 v137, s37\n"
      "v_permlane16_swap_b32 v129, v131\n"
      "v_mfma_f32_16x16x32_bf16 v[220:223], v[92:95], a[236:239], v[220:223]\n"
      "ds_read_b128 a[232:235], v251 offset:8192\n"
      "v_mov_b32 v138, s37\n"
      "v_mov_b32 v139, s37\n"
      "s_waitcnt lgkmcnt(14)\n"
      "v_mfma_f32_16x16x32_bf16 v[216:219], v[96:99], a[240:243], v[216:219]\n"
      "v_max_u32 v129, v129, v131\n"
      "v_cndmask_b32 v133, v132, v133, s[64:65]\n"
      "v_mov_b32 v131, v129\n"
      "v_mfma_f32_16x16x32_bf16 v[220:223], v[100:103], a[240:243], v[220:223]\n"
      "ds_read_b128 a[236:239], v251 offset:10240\n"
      "v_permlane32_swap_b32 v129, v131\n"
      "v_max_u32 v129, v129, v131\n"
      "s_waitcnt lgkmcnt(14)\n"
      "v_mfma_f32_16x16x32_bf16 v[216:219], v[104:107], a[244:247], v[216:219]\n"
      "s_xor_b32 s70, s77, 0\n"
      "s_add_u32 s70, s8, s70\n"
      "s_addc_u32 s71, s9, 0\n"
      "s_add_u32 m0, s41, 8192\n"
      "v_mfma_f32_16x16x32_bf16 v[220:223], v[108:111], a[244:247], v[220:223]\n"
      "global_load_lds_dwordx4 v244, s[70:71] offset:0\n"
      "ds_read_b128 a[240:243], v250 offset:12288\n"
      "v_cndmask_b32 v136, v129, v130, s[64:65]\n"
      "s_waitcnt lgkmcnt(14)\n"
      "v_mfma_f32_16x16x32_bf16 v[216:219], v[112:115], a[248:251], v[216:219]\n"
      "s_mov_b32 s64, 0x00ff0fff\n"
      "s_mov_b64 exec, s[64:65]\n"
      "global_store_dwordx4 v133, v[136:139], s[58:59] sc1\n"
      "s_mov_b64 exec, -1\n"
      "v_mfma_f32_16x16x32_bf16 v[220:223], v[116:119], a[248:251], v[220:223]\n"
      "ds_read_b128 a[244:247], v250 offset:14336\n"
      "s_waitcnt vmcnt(11)\n"
      "ds_read_b128 a[0:3], v248 offset:4096\n"
      "s_waitcnt lgkmcnt(15)\n"
      "v_mfma_f32_16x16x32_bf16 v[216:219], v[120:123], a[252:255], v[216:219]\n"
      "ds_read_b128 a[4:7], v248 offset:6144\n"
      "ds_read_b128 a[32:35], v249 offset:4096\n"
      "v_mfma_f32_16x16x32_bf16 v[220:223], v[124:127], a[252:255], v[220:223]\n"
      "ds_read_b128 a[248:251], v251 offset:12288\n"
      "ds_read_b128 a[36:39], v249 offset:6144\n"
      "s_waitcnt lgkmcnt(15)\n"
      "v_mfma_f32_16x16x32_bf16 v[192:195], v[0:3], a[192:195], 0\n"
      "global_load_lds_dwordx4 v245, s[70:71] offset:1024\n"
      "v_mfma_f32_16x16x32_bf16 v[196:199], v[4:7], a[192:195], 0\n"
      "ds_read_b128 a[252:255], v251 offset:14336\n"
      "v_mfma_f32_16x16x32_bf16 v[200:203], v[0:3], a[196:199], 0\n"
      "v_cvt_pk_bf16_f32 v224, v216, v217\n"
      "v_cvt_pk_bf16_f32 v225, v218, v219\n"
      "v_cvt_pk_bf16_f32 v226, v220, v221\n"
      "v_mfma_f32_16x16x32_bf16 v[204:207], v[4:7], a[196:199], 0\n"
      "ds_read_b128 a[192:195], v250 offset:16384\n"
      "v_cvt_pk_bf16_f32 v227, v222, v223\n"
      "v_xor_b32 v224, s85, v224\n"
      "v_mfma_f32_16x16x32_bf16 v[192:195], v[8:11], a[200:203], v[192:195]\n"
      "global_load_lds_dwordx4 v246, s[70:71] offset:2048\n"
      "v_xor_b32 v225, s85, v225\n"
      "v_xor_b32 v226, s85, v226\n"
      "v_mfma_f32_16x16x32_bf16 v[196:199], v[12:15], a[200:203], v[196:199]\n"
      "ds_read_b128 a[196:199], v250 offset:18432\n"
      "v_xor_b32 v227, s85, v227\n"
      "s_lshl2_add_u32 s54, s34, s35\n"
      "v_mfma_f32_16x16x32_bf16 v[200:203], v[8:11], a[204:207], v[200:203]\n"
      "s_lshr_b32 s56, s39, 1\n"
      "s_lshl1_add_u32 s56, s33, s56\n"
      "s_lshl_b32 s56, s56, 15\n"
      "s_lshl_b32 s55, s54, 18\n"
      "v_mfma_f32_16x16x32_bf16 v[204:207], v[12:15], a[204:207], v[204:207]\n"
      "s_add_u32 s55, s55, s56\n"
      "ds_read_b128 a[200:203], v251 offset:16384\n"
      "s_add_u32 s55, s55, 4096\n"
      "s_add_u32 s10, s18, s55\n"
      "s_addc_u32 s11, s19, 0\n"
      "s_waitcnt lgkmcnt(15)\n"
      "v_mfma_f32_16x16x32_bf16 v[192:195], v[16:19], a[208:211], v[192:195]\n"
      "s_xor_b32 s55, s55, 0x8000\n"
      "global_load_lds_dwordx4 v247, s[70:71] offset:3072\n"
      "s_add_u32 s14, s18, s55\n"
      "s_addc_u32 s15, s19, 0\n"
      "v_mfma_f32_16x16x32_bf16 v[196:199], v[20:23], a[208:211], v[196:199]\n"
      "s_and_b32 s55, s39, 1\n"
      "ds_read_b128 a[204:207], v251 offset:18432\n"
      "s_lshl_b32 s56, s55, 14\n"
      "v_add_u32 v240, s56, v242\n"
      "v_mfma_f32_16x16x32_bf16 v[200:203], v[16:19], a[212:215], v[200:203]\n"
      "v_xor_b32 v241, 0x4000, v240\n"
      "s_add_u32 s72, s10, 0x2000\n"
      "s_addc_u32 s73, s11, 0\n"
      "v_mfma_f32_16x16x32_bf16 v[204:207], v[20:23], a[212:215], v[204:207]\n"
      "s_add_u32 s74, s14, 0x2000\n"
      "s_addc_u32 s75, s15, 0\n"
      "s_xor_b32 s70, s78, 0\n"
      "s_add_u32 s70, s8, s70\n"
      "s_addc_u32 s71, s9, 0\n"
      "s_add_u32 m0, s41, 12288\n"
      "v_mfma_f32_16x16x32_bf16 v[192:195], v[24:27], a[216:219], v[192:195]\n"
      "s_lshl2_add_u32 s55, s54, s33\n"
      "global_load_lds_dwordx4 v244, s[70:71] offset:0\n"
      "ds_read_b128 a[208:211], v250 offset:20480\n"
      "s_lshl_b32 s55, s55, 12\n"
      "v_mfma_f32_16x16x32_bf16 v[196:199], v[28:31], a[216:219], v[196:199]\n"
      "s_add_u32 s55, s55, 1048576\n"
      "ds_read_b128 a[212:215], v250 offset:22528\n"
      "v_and_b32 v253, 7, v243\n"
      "v_lshl_add_u32 v253, v253, 9, s55\n"
      "v_mfma_f32_16x16x32_bf16 v[200:203], v[24:27], a[220:223], v[200:203]\n"
      "v_mfma_f32_16x16x32_bf16 v[204:207], v[28:31], a[220:223], v[204:207]\n"
      "ds_read_b128 a[216:219], v251 offset:20480\n"
      "s_waitcnt lgkmcnt(15)\n"
      "v_mfma_f32_16x16x32_bf16 v[192:195], v[32:35], a[224:227], v[192:195]\n"
      "global_load_lds_dwordx4 v245, s[70:71] offset:1024\n"
      "v_mfma_f32_16x16x32_bf16 v[196:199], v[36:39], a[224:227], v[196:199]\n"
      "ds_read_b128 a[220:223], v251 offset:22528\n"
      "s_waitcnt vmcnt(7)\n"
      "s_barrier\n"
      "v_mfma_f32_16x16x32_bf16 v[200:203], v[32:35], a[228:231], v[200:203]\n"
      "v_mfma_f32_16x16x32_bf16 v[204:207], v[36:39], a[228:231], v[204:207]\n"
      "ds_read_b128 a[224:227], v250 offset:24576\n"
      "v_mfma_f32_16x16x32_bf16 v[192:195], v[40:43], a[232:235], v[192:195]\n"
      "global_load_lds_dwordx4 v246, s[70:71] offset:2048\n"
      "v_mfma_f32_16x16x32_bf16 v[196:199], v[44:47], a[232:235], v[196:199]\n"
      "ds_read_b128 a[228:231], v250 offset:26624\n"
      "v_mfma_f32_16x16x32_bf16 v[200:203], v[40:43], a[236:239], v[200:203]\n"
      "v_mfma_f32_16x16x32_bf16 v[204:207], v[44:47], a[236:239], v[204:207]\n"
      "ds_read_b128 a[232:235], v251 offset:24576\n"
      "s_waitcnt lgkmcnt(15)\n"
      "v_mfma_f32_16x16x32_bf16 v[192:195], v[48:51], a[240:243], v[192:195]\n"
      "global_load_lds_dwordx4 v247, s[70:71] offset:3072\n"
      "v_mfma_f32_16x16x32_bf16 v[196:199], v[52:55], a[240:243], v[196:199]\n"
      "ds_read_b128 a[236:239], v251 offset:26624\n"
      "v_mfma_f32_16x16x32_bf16 v[200:203], v[48:51], a[244:247], v[200:203]\n"
      "v_mfma_f32_16x16x32_bf16 v[204:207], v[52:55], a[244:247], v[204:207]\n"
      "s_xor_b32 s70, s79, 0\n"
      "s_add_u32 s70, s8, s70\n"
      "s_addc_u32 s71, s9, 0\n"
      "s_add_u32 m0, s82, 98304\n"
      "s_waitcnt lgkmcnt(14)\n"
      "v_mfma_f32_16x16x32_bf16 v[192:195], v[56:59], a[248:251], v[192:195]\n"
      "global_load_lds_dwordx4 v244, s[70:71] offset:0\n"
      "ds_read_b128 a[240:243], v250 offset:28672\n"
      "v_mfma_f32_16x16x32_bf16 v[196:199], v[60:63], a[248:251], v[196:199]\n"
      "ds_read_b128 a[244:247], v250 offset:30720\n"
      "s_waitcnt lgkmcnt(14)\n"
      "v_mfma_f32_16x16x32_bf16 v[200:203], v[56:59], a[252:255], v[200:203]\n"
      "v_mfma_f32_16x16x32_bf16 v[204:207], v[60:63], a[252:255], v[204:207]\n"
      "ds_read_b128 a[248:251], v251 offset:28672\n"
      "s_waitcnt lgkmcnt(14)\n"
      "v_mfma_f32_16x16x32_bf16 v[192:195], v[64:67], a[192:195], v[192:195]\n"
      "global_load_lds_dwordx4 v245, s[70:71] offset:1024\n"
      "v_mfma_f32_16x16x32_bf16 v[196:199], v[68:71], a[192:195], v[196:199]\n"
      "ds_read_b128 a[252:255], v251 offset:30720\n"
      "v_xor_b32 v250, 196608, v250\n"
      "v_xor_b32 v251, 196608, v251\n"
      "s_waitcnt lgkmcnt(14)\n"
      "v_mfma_f32_16x16x32_bf16 v[200:203], v[64:67], a[196:199], v[200:203]\n"
      "v_mfma_f32_16x16x32_bf16 v[204:207], v[68:71], a[196:199], v[204:207]\n"
      "ds_read_b128 a[192:195], v250 offset:0\n"
      "s_waitcnt lgkmcnt(14)\n"
      "v_mfma_f32_16x16x32_bf16 v[192:195], v[72:75], a[200:203], v[192:195]\n"
      "global_load_lds_dwordx4 v246, s[70:71] offset:2048\n"
      "v_mfma_f32_16x16x32_bf16 v[196:199], v[76:79], a[200:203], v[196:199]\n"
      "ds_read_b128 a[196:199], v250 offset:2048\n"
      "s_waitcnt lgkmcnt(14)\n"
      "v_mfma_f32_16x16x32_bf16 v[200:203], v[72:75], a[204:207], v[200:203]\n"
      "s_waitcnt vmcnt(7)\n"
      "ds_read_b128 a[8:11], v248 offset:8192\n"
      "v_mfma_f32_16x16x32_bf16 v[204:207], v[76:79], a[204:207], v[204:207]\n"
      "ds_read_b128 a[200:203], v251 offset:0\n"
      "ds_read_b128 a[12:15], v248 offset:10240\n"
      "s_waitcnt lgkmcnt(15)\n"
      "v_mfma_f32_16x16x32_bf16 v[192:195], v[80:83], a[208:211], v[192:195]\n"
      "global_load_lds_dwordx4 v247, s[70:71] offset:3072\n"
      "ds_read_b128 a[40:43], v249 offset:8192\n"
      "v_mfma_f32_16x16x32_bf16 v[196:199], v[84:87], a[208:211], v[196:199]\n"
      "ds_read_b128 a[204:207], v251 offset:2048\n"
      "ds_read_b128 a[44:47], v249 offset:10240\n"
      "v_mfma_f32_16x16x32_bf16 v[200:203], v[80:83], a[212:215], v[200:203]\n"
      "v_mfma_f32_16x16x32_bf16 v[204:207], v[84:87], a[212:215], v[204:207]\n"
      "s_xor_b32 s70, s76, 256\n"
      "s_add_u32 s70, s8, s70\n"
      "s_addc_u32 s71, s9, 0\n"
      "s_add_u32 m0, s82, 114688\n"
      "s_waitcnt lgkmcnt(15)\n"
      "v_mfma_f32_16x16x32_bf16 v[192:195], v[88:91], a[216:219], v[192:195]\n"
      "global_load_lds_dwordx4 v244, s[70:71] offset:0\n"
      "ds_read_b128 a[208:211], v250 offset:4096\n"
      "v_mfma_f32_16x16x32_bf16 v[196:199], v[92:95], a[216:219], v[196:199]\n"
      "ds_read_b128 a[212:215], v250 offset:6144\n"
      "v_mfma_f32_16x16x32_bf16 v[200:203], v[88:91], a[220:223], v[200:203]\n"
      "v_mfma_f32_16x16x32_bf16 v[204:207], v[92:95], a[220:223], v[204:207]\n"
      "ds_read_b128 a[216:219], v251 offset:4096\n"
      "v_mfma_f32_16x16x32_bf16 v[192:195], v[96:99], a[224:227], v[192:195]\n"
      "global_load_lds_dwordx4 v245, s[70:71] offset:1024\n"
      "v_mfma_f32_16x16x32_bf16 v[196:199], v[100:103], a[224:227], v[196:199]\n"
      "ds_read_b128 a[220:223], v251 offset:6144\n"
      "s_waitcnt lgkmcnt(15)\n"
      "v_mfma_f32_16x16x32_bf16 v[200:203], v[96:99], a[228:231], v[200:203]\n"
      "v_mfma_f32_16x16x32_bf16 v[204:207], v[100:103], a[228:231], v[204:207]\n"
      "ds_read_b128 a[224:227], v250 offset:8192\n"
      "v_mfma_f32_16x16x32_bf16 v[192:195], v[104:107], a[232:235], v[192:195]\n"
      "global_load_lds_dwordx4 v246, s[70:71] offset:2048\n"
      "v_mfma_f32_16x16x32_bf16 v[196:199], v[108:111], a[232:235], v[196:199]\n"
      "ds_read_b128 a[228:231], v250 offset:10240\n"
      "v_mfma_f32_16x16x32_bf16 v[200:203], v[104:107], a[236:239], v[200:203]\n"
      "s_waitcnt vmcnt(7)\n"
      "ds_read_b128 a[16:19], v248 offset:12288\n"
      "v_mfma_f32_16x16x32_bf16 v[204:207], v[108:111], a[236:239], v[204:207]\n"
      "ds_read_b128 a[232:235], v251 offset:8192\n"
      "ds_read_b128 a[20:23], v248 offset:14336\n"
      "v_mfma_f32_16x16x32_bf16 v[192:195], v[112:115], a[240:243], v[192:195]\n"
      "global_load_lds_dwordx4 v247, s[70:71] offset:3072\n"
      "ds_read_b128 a[48:51], v249 offset:12288\n"
      "v_mfma_f32_16x16x32_bf16 v[196:199], v[116:119], a[240:243], v[196:199]\n"
      "ds_read_b128 a[236:239], v251 offset:10240\n"
      "s_waitcnt lgkmcnt(15)\n"
      "s_barrier\n"
      "v_mfma_f32_16x16x32_bf16 v[200:203], v[112:115], a[244:247], v[200:203]\n"
      "ds_read_b128 a[52:55], v249 offset:14336\n"
      "v_mfma_f32_16x16x32_bf16 v[204:207], v[116:119], a[244:247], v[204:207]\n"
      "s_xor_b32 s70, s77, 256\n"
      "s_add_u32 s70, s8, s70\n"
      "s_addc_u32 s71, s9, 0\n"
      "s_add_u32 m0, s82, 131072\n"
      "v_mfma_f32_16x16x32_bf16 v[192:195], v[120:123], a[248:251], v[192:195]\n"
      "global_load_lds_dwordx4 v244, s[70:71] offset:0\n"
      "ds_read_b128 a[240:243], v250 offset:12288\n"
      "v_mfma_f32_16x16x32_bf16 v[196:199], v[124:127], a[248:251], v[196:199]\n"
      "ds_read_b128 a[244:247], v250 offset:14336\n"
      "v_mfma_f32_16x16x32_bf16 v[200:203], v[120:123], a[252:255], v[200:203]\n"
      "v_mfma_f32_16x16x32_bf16 v[204:207], v[124:127], a[252:255], v[204:207]\n"
      "ds_read_b128 a[248:251], v251 offset:12288\n"
      "v_mfma_f32_16x16x32_bf16 v[208:211], v[0:3], a[192:195], 0\n"
      "global_load_lds_dwordx4 v245, s[70:71] offset:1024\n"
      "v_mfma_f32_16x16x32_bf16 v[212:215], v[4:7], a[192:195], 0\n"
      "ds_read_b128 a[252:255], v251 offset:14336\n"
      "v_mfma_f32_16x16x32_bf16 v[216:219], v[0:3], a[196:199], 0\n"
      "v_cvt_pk_bf16_f32 v228, v192, v193\n"
      "v_cvt_pk_bf16_f32 v229, v194, v195\n"
      "v_cvt_pk_bf16_f32 v230, v196, v197\n"
      "v_mfma_f32_16x16x32_bf16 v[220:223], v[4:7], a[196:199], 0\n"
      "ds_read_b128 a[192:195], v250 offset:16384\n"
      "v_cvt_pk_bf16_f32 v231, v198, v199\n"
      "v_xor_b32 v228, s85, v228\n"
      "v_mfma_f32_16x16x32_bf16 v[208:211], v[8:11], a[200:203], v[208:211]\n"
      "global_load_lds_dwordx4 v246, s[70:71] offset:2048\n"
      "v_xor_b32 v229, s85, v229\n"
      "v_xor_b32 v230, s85, v230\n"
      "v_mfma_f32_16x16x32_bf16 v[212:215], v[12:15], a[200:203], v[212:215]\n"
      "ds_read_b128 a[196:199], v250 offset:18432\n"
      "v_xor_b32 v231, s85, v231\n"
      "v_cvt_pk_bf16_f32 v232, v200, v201\n"
      "s_waitcnt lgkmcnt(15)\n"
      "v_mfma_f32_16x16x32_bf16 v[216:219], v[8:11], a[204:207], v[216:219]\n"
      "s_waitcnt vmcnt(7)\n"
      "ds_read_b128 a[24:27], v236 offset:0\n"
      "v_mfma_f32_16x16x32_bf16 v[220:223], v[12:15], a[204:207], v[220:223]\n"
      "ds_read_b128 a[200:203], v251 offset:16384\n"
      "ds_read_b128 a[28:31], v236 offset:2048\n"
      "v_cvt_pk_bf16_f32 v233, v202, v203\n"
      "v_mfma_f32_16x16x32_bf16 v[208:211], v[16:19], a[208:211], v[208:211]\n"
      "global_load_lds_dwordx4 v247, s[70:71] offset:3072\n"
      "ds_read_b128 a[56:59], v237 offset:0\n"
      "v_cvt_pk_bf16_f32 v234, v204, v205\n"
      "v_mfma_f32_16x16x32_bf16 v[212:215], v[20:23], a[208:211], v[212:215]\n"
      "ds_read_b128 a[204:207], v251 offset:18432\n"
      "ds_read_b128 a[60:63], v237 offset:2048\n"
      "v_cvt_pk_bf16_f32 v235, v206, v207\n"
      "v_mfma_f32_16x16x32_bf16 v[216:219], v[16:19], a[212:215], v[216:219]\n"
      "v_xor_b32 v232, s85, v232\n"
      "v_xor_b32 v233, s85, v233\n"
      "v_xor_b32 v234, s85, v234\n"
      "v_mfma_f32_16x16x32_bf16 v[220:223], v[20:23], a[212:215], v[220:223]\n"
      "s_xor_b32 s70, s78, 256\n"
      "s_add_u32 s70, s8, s70\n"
      "s_addc_u32 s71, s9, 0\n"
      "s_add_u32 m0, s82, 147456\n"
      "v_mfma_f32_16x16x32_bf16 v[208:211], v[24:27], a[216:219], v[208:211]\n"
      "global_load_lds_dwordx4 v244, s[70:71] offset:0\n"
      "ds_read_b128 a[208:211], v250 offset:20480\n"
      "v_xor_b32 v235, s85, v235\n"
      "v_mfma_f32_16x16x32_bf16 v[212:215], v[28:31], a[216:219], v[212:215]\n"
      "ds_read_b128 a[212:215], v250 offset:22528\n"
      "s_waitcnt lgkmcnt(15)\n"
      "v_mfma_f32_16x16x32_bf16 v[216:219], v[24:27], a[220:223], v[216:219]\n"
      "v_mfma_f32_16x16x32_bf16 v[220:223], v[28:31], a[220:223], v[220:223]\n"
      "ds_read_b128 a[216:219], v251 offset:20480\n"
      "v_mfma_f32_16x16x32_bf16 v[208:211], v[32:35], a[224:227], v[208:211]\n"
      "global_load_lds_dwordx4 v245, s[70:71] offset:1024\n"
      "v_mfma_f32_16x16x32_bf16 v[212:215], v[36:39], a[224:227], v[212:215]\n"
      "ds_read_b128 a[220:223], v251 offset:22528\n"
      "v_mfma_f32_16x16x32_bf16 v[216:219], v[32:35], a[228:231], v[216:219]\n"
      "v_mfma_f32_16x16x32_bf16 v[220:223], v[36:39], a[228:231], v[220:223]\n"
      "ds_read_b128 a[224:227], v250 offset:24576\n"
      "v_mfma_f32_16x16x32_bf16 v[208:211], v[40:43], a[232:235], v[208:211]\n"
      "global_load_lds_dwordx4 v246, s[70:71] offset:2048\n"
      "v_mfma_f32_16x16x32_bf16 v[212:215], v[44:47], a[232:235], v[212:215]\n"
      "ds_read_b128 a[228:231], v250 offset:26624\n"
      "v_mfma_f32_16x16x32_bf16 v[216:219], v[40:43], a[236:239], v[216:219]\n"
      "s_waitcnt vmcnt(7)\n"
      "ds_read_b128 a[64:67], v236 offset:16384\n"
      "v_mfma_f32_16x16x32_bf16 v[220:223], v[44:47], a[236:239], v[220:223]\n"
      "ds_read_b128 a[232:235], v251 offset:24576\n"
      "ds_read_b128 a[68:71], v236 offset:18432\n"
      "s_waitcnt lgkmcnt(15)\n"
      "v_mfma_f32_16x16x32_bf16 v[208:211], v[48:51], a[240:243], v[208:211]\n"
      "global_load_lds_dwordx4 v247, s[70:71] offset:3072\n"
      "ds_read_b128 a[96:99], v237 offset:16384\n"
      "v_mfma_f32_16x16x32_bf16 v[212:215], v[52:55], a[240:243], v[212:215]\n"
      "ds_read_b128 a[236:239], v251 offset:26624\n"
      "ds_read_b128 a[100:103], v237 offset:18432\n"
      "v_mfma_f32_16x16x32_bf16 v[216:219], v[48:51], a[244:247], v[216:219]\n"
      "v_mfma_f32_16x16x32_bf16 v[220:223], v[52:55], a[244:247], v[220:223]\n"
      "global_load_dword v252, v253, s[58:59] offset:0 sc1\n"
      "s_xor_b32 s70, s79, 256\n"
      "s_add_u32 s70, s8, s70\n"
      "s_addc_u32 s71, s9, 0\n"
      "s_add_u32 m0, s41, 0\n"
      "v_mfma_f32_16x16x32_bf16 v[208:211], v[56:59], a[248:251], v[208:211]\n"
      "global_load_lds_dwordx4 v244, s[70:71] offset:0\n"
      "ds_read_b128 a[240:243], v250 offset:28672\n"
      "v_mfma_f32_16x16x32_bf16 v[212:215], v[60:63], a[248:251], v[212:215]\n"
      "ds_read_b128 a[244:247], v250 offset:30720\n"
      "v_mfma_f32_16x16x32_bf16 v[216:219], v[56:59], a[252:255], v[216:219]\n"
      "v_mfma_f32_16x16x32_bf16 v[220:223], v[60:63], a[252:255], v[220:223]\n"
      "ds_read_b128 a[248:251], v251 offset:28672\n"
      "v_mfma_f32_16x16x32_bf16 v[208:211], v[64:67], a[192:195], v[208:211]\n"
      "global_load_lds_dwordx4 v245, s[70:71] offset:1024\n"
      "v_mfma_f32_16x16x32_bf16 v[212:215], v[68:71], a[192:195], v[212:215]\n"
      "ds_read_b128 a[252:255], v251 offset:30720\n"
      "v_mfma_f32_16x16x32_bf16 v[216:219], v[64:67], a[196:199], v[216:219]\n"
      "v_mfma_f32_16x16x32_bf16 v[220:223], v[68:71], a[196:199], v[220:223]\n"
      "s_waitcnt lgkmcnt(15)\n"
      "v_mfma_f32_16x16x32_bf16 v[208:211], v[72:75], a[200:203], v[208:211]\n"
      "global_load_lds_dwordx4 v246, s[70:71] offset:2048\n"
      "v_mfma_f32_16x16x32_bf16 v[212:215], v[76:79], a[200:203], v[212:215]\n"
      "v_mfma_f32_16x16x32_bf16 v[216:219], v[72:75], a[204:207], v[216:219]\n"
      "s_waitcnt vmcnt(8)\n"
      "ds_read_b128 a[72:75], v236 offset:32768\n"
      "v_mfma_f32_16x16x32_bf16 v[220:223], v[76:79], a[204:207], v[220:223]\n"
      "ds_read_b128 a[76:79], v236 offset:34816\n"
      "ds_read_b128 a[104:107], v237 offset:32768\n"
      "v_mfma_f32_16x16x32_bf16 v[208:211], v[80:83], a[208:211], v[208:211]\n"
      "global_load_lds_dwordx4 v247, s[70:71] offset:3072\n"
      "ds_read_b128 a[108:111], v237 offset:34816\n"
      "v_mfma_f32_16x16x32_bf16 v[212:215], v[84:87], a[208:211], v[212:215]\n"
      "s_waitcnt lgkmcnt(15)\n"
      "v_mfma_f32_16x16x32_bf16 v[216:219], v[80:83], a[212:215], v[216:219]\n"
      "v_mfma_f32_16x16x32_bf16 v[220:223], v[84:87], a[212:215], v[220:223]\n"
      "s_waitcnt vmcnt(4)\n"
      "v_cmp_ne_u32 vcc, s37, v252\n"
      "s_nop 1\n"
      "s_cbranch_vccnz .Lp_s2_%=\n"
      ".Lp_o3_%=:\n"
      "v_mfma_f32_16x16x32_bf16 v[208:211], v[88:91], a[216:219], v[208:211]\n"
      "global_load_dwordx4 v[128:131], v240, s[10:11] offset:-4096 sc1\n"
      "v_mfma_f32_16x16x32_bf16 v[212:215], v[92:95], a[216:219], v[212:215]\n"
      "v_mfma_f32_16x16x32_bf16 v[216:219], v[88:91], a[220:223], v[216:219]\n"
      "v_mfma_f32_16x16x32_bf16 v[220:223], v[92:95], a[220:223], v[220:223]\n"
      "v_mfma_f32_16x16x32_bf16 v[208:211], v[96:99], a[224:227], v[208:211]\n"
      "global_load_dwordx4 v[132:135], v240, s[10:11] offset:-3072 sc1\n"
      "v_mfma_f32_16x16x32_bf16 v[212:215], v[100:103], a[224:227], v[212:215]\n"
      "s_waitcnt lgkmcnt(14)\n"
      "v_mfma_f32_16x16x32_bf16 v[216:219], v[96:99], a[228:231], v[216:219]\n"
      "v_mfma_f32_16x16x32_bf16 v[220:223], v[100:103], a[228:231], v[220:223]\n"
      "s_waitcnt lgkmcnt(12)\n"
      "v_mfma_f32_16x16x32_bf16 v[208:211], v[104:107], a[232:235], v[208:211]\n"
      "global_load_dwordx4 v[136:139], v240, s[10:11] offset:-2048 sc1\n"
      "v_mfma_f32_16x16x32_bf16 v[212:215], v[108:111], a[232:235], v[212:215]\n"
      "s_waitcnt lgkmcnt(9)\n"
      "v_mfma_f32_16x16x32_bf16 v[216:219], v[104:107], a[236:239], v[216:219]\n"
      "ds_read_b128 a[80:83], v236 offset:49152\n"
      "ds_read_b128 a[84:87], v236 offset:51200\n"
      "v_mfma_f32_16x16x32_bf16 v[220:223], v[108:111], a[236:239], v[220:223]\n"
      "ds_read_b128 a[112:115], v237 offset:49152\n"
      "ds_read_b128 a[116:119], v237 offset:51200\n"
      "s_waitcnt lgkmcnt(11)\n"
      "v_mfma_f32_16x16x32_bf16 v[208:211], v[112:115], a[240:243], v[208:211]\n"
      "global_store_dwordx4 v242, v[224:227], s[62:63] offset:-1024 sc1\n"
      "global_store_dwordx4 v242, v[228:231], s[62:63] offset:0 sc1\n"
      "v_mfma_f32_16x16x32_bf16 v[212:215], v[116:119], a[240:243], v[212:215]\n"
      "global_store_dwordx4 v242, v[232:235], s[62:63] offset:1024 sc1\n"
      "global_load_dwordx4 v[140:143], v240, s[72:73] offset:-4096 sc1\n"
      "s_waitcnt lgkmcnt(10)\n"
      "v_mfma_f32_16x16x32_bf16 v[216:219], v[112:115], a[244:247], v[216:219]\n"
      "v_mfma_f32_16x16x32_bf16 v[220:223], v[116:119], a[244:247], v[220:223]\n"
      "s_waitcnt lgkmcnt(9)\n"
      "v_mfma_f32_16x16x32_bf16 v[208:211], v[120:123], a[248:251], v[208:211]\n"
      "v_mfma_f32_16x16x32_bf16 v[212:215], v[124:127], a[248:251], v[212:215]\n"
      "global_load_dwordx4 v[144:147], v240, s[72:73] offset:-3072 sc1\n"
      "s_waitcnt lgkmcnt(8)\n"
      "v_mfma_f32_16x16x32_bf16 v[216:219], v[120:123], a[252:255], v[216:219]\n"
      "v_mfma_f32_16x16x32_bf16 v[220:223], v[124:127], a[252:255], v[220:223]\n"
      "s_waitcnt vmcnt(7)\n"
      "v_mfma_f32_16x16x32_bf16 v[0:3], v[128:131], a[0:3], 0\n"
      "v_mfma_f32_16x16x32_bf16 v[4:7], v[128:131], a[4:7], 0\n"
      "global_load_dwordx4 v[148:151], v240, s[72:73] offset:-2048 sc1\n"
      "v_mfma_f32_16x16x32_bf16 v[8:11], v[128:131], a[8:11], 0\n"
      "v_cvt_pk_bf16_f32 v192, v208, v209\n"
      "v_cvt_pk_bf16_f32 v193, v210, v211\n"
      "v_mfma_f32_16x16x32_bf16 v[12:15], v[128:131], a[12:15], 0\n"
      "v_cvt_pk_bf16_f32 v194, v212, v213\n"
      "v_cvt_pk_bf16_f32 v195, v214, v215\n"
      "v_mfma_f32_16x16x32_bf16 v[16:19], v[128:131], a[16:19], 0\n"
      "v_xor_b32 v192, s85, v192\n"
      "v_xor_b32 v193, s85, v193\n"
      "v_mfma_f32_16x16x32_bf16 v[20:23], v[128:131], a[20:23], 0\n"
      "v_xor_b32 v194, s85, v194\n"
      "v_xor_b32 v195, s85, v195\n"
      "v_mfma_f32_16x16x32_bf16 v[24:27], v[128:131], a[24:27], 0\n"
      "ds_read_b128 a[88:91], v248 offset:0\n"
      "v_cvt_pk_bf16_f32 v196, v216, v217\n"
      "v_mfma_f32_16x16x32_bf16 v[28:31], v[128:131], a[28:31], 0\n"
      "ds_read_b128 a[92:95], v248 offset:2048\n"
      "v_cvt_pk_bf16_f32 v197, v218, v219\n"
      "s_waitcnt vmcnt(7)\n"
      "v_mfma_f32_16x16x32_bf16 v[32:35], v[132:135], a[0:3], 0\n"
      "s_xor_b32 s70, s76, 128\n"
      "s_add_u32 s70, s8, s70\n"
      "s_addc_u32 s71, s9, 0\n"
      "s_add_u32 m0, s41, 4096\n"
      "v_mfma_f32_16x16x32_bf16 v[36:39], v[132:135], a[4:7], 0\n"
      "global_load_lds_dwordx4 v244, s[70:71] offset:0\n"
      "v_cvt_pk_bf16_f32 v198, v220, v221\n"
      "v_mfma_f32_16x16x32_bf16 v[40:43], v[132:135], a[8:11], 0\n"
      "ds_read_b128 a[120:123], v249 offset:0\n"
      "v_cvt_pk_bf16_f32 v199, v222, v223\n"
      "v_mfma_f32_16x16x32_bf16 v[44:47], v[132:135], a[12:15], 0\n"
      "ds_read_b128 a[124:127], v249 offset:2048\n"
      "v_xor_b32 v196, s85, v196\n"
      "v_mfma_f32_16x16x32_bf16 v[48:51], v[132:135], a[16:19], 0\n"
      "global_load_lds_dwordx4 v245, s[70:71] offset:1024\n"
      "v_xor_b32 v197, s85, v197\n"
      "v_mfma_f32_16x16x32_bf16 v[52:55], v[132:135], a[20:23], 0\n"
      "v_xor_b32 v198, s85, v198\n"
      "v_xor_b32 v199, s85, v199\n"
      "v_mfma_f32_16x16x32_bf16 v[56:59], v[132:135], a[24:27], 0\n"
      "global_store_dwordx4 v242, v[192:195], s[62:63] offset:2048 sc1\n"
      "global_store_dwordx4 v242, v[196:199], s[62:63] offset:3072 sc1\n"
      "v_mfma_f32_16x16x32_bf16 v[60:63], v[132:135], a[28:31], 0\n"
      "s_waitcnt vmcnt(6)\n"
      "global_load_lds_dwordx4 v246, s[70:71] offset:2048\n"
      "v_mfma_f32_16x16x32_bf16 v[64:67], v[136:139], a[0:3], 0\n"
      "v_mfma_f32_16x16x32_bf16 v[68:71], v[136:139], a[4:7], 0\n"
      "v_mfma_f32_16x16x32_bf16 v[72:75], v[136:139], a[8:11], 0\n"
      "global_load_lds_dwordx4 v247, s[70:71] offset:3072\n"
      "v_mfma_f32_16x16x32_bf16 v[76:79], v[136:139], a[12:15], 0\n"
      "v_mfma_f32_16x16x32_bf16 v[80:83], v[136:139], a[16:19], 0\n"
      "global_load_dwordx4 v[152:155], v240, s[14:15] offset:-4096 sc1\n"
      "v_mfma_f32_16x16x32_bf16 v[84:87], v[136:139], a[20:23], 0\n"
      "v_mfma_f32_16x16x32_bf16 v[88:91], v[136:139], a[24:27], 0\n"
      "v_mfma_f32_16x16x32_bf16 v[92:95], v[136:139], a[28:31], 0\n"
      "global_load_dwordx4 v[156:159], v240, s[14:15] offset:-3072 sc1\n"
      "v_mfma_f32_16x16x32_bf16 v[0:3], v[140:143], a[32:35], v[0:3]\n"
      "v_mfma_f32_16x16x32_bf16 v[4:7], v[140:143], a[36:39], v[4:7]\n"
      "v_mfma_f32_16x16x32_bf16 v[8:11], v[140:143], a[40:43], v[8:11]\n"
      "global_load_dwordx4 v[160:163], v240, s[14:15] offset:-2048 sc1\n"
      "v_mfma_f32_16x16x32_bf16 v[12:15], v[140:143], a[44:47], v[12:15]\n"
      "v_mfma_f32_16x16x32_bf16 v[16:19], v[140:143], a[48:51], v[16:19]\n"
      "v_mfma_f32_16x16x32_bf16 v[20:23], v[140:143], a[52:55], v[20:23]\n"
      "s_xor_b32 s70, s77, 128\n"
      "s_add_u32 s70, s8, s70\n"
      "s_addc_u32 s71, s9, 0\n"
      "s_add_u32 m0, s41, 8192\n"
      "v_mfma_f32_16x16x32_bf16 v[24:27], v[140:143], a[56:59], v[24:27]\n"
      "global_load_lds_dwordx4 v244, s[70:71] offset:0\n"
      "v_mfma_f32_16x16x32_bf16 v[28:31], v[140:143], a[60:63], v[28:31]\n"
      "s_waitcnt vmcnt(10)\n"
      "v_mfma_f32_16x16x32_bf16 v[32:35], v[144:147], a[32:35], v[32:35]\n"
      "v_mfma_f32_16x16x32_bf16 v[36:39], v[144:147], a[36:39], v[36:39]\n"
      "global_load_lds_dwordx4 v245, s[70:71] offset:1024\n"
      "v_mfma_f32_16x16x32_bf16 v[40:43], v[144:147], a[40:43], v[40:43]\n"
      "v_mfma_f32_16x16x32_bf16 v[44:47], v[144:147], a[44:47], v[44:47]\n"
      "v_mfma_f32_16x16x32_bf16 v[48:51], v[144:147], a[48:51], v[48:51]\n"
      "global_load_lds_dwordx4 v246, s[70:71] offset:2048\n"
      "v_mfma_f32_16x16x32_bf16 v[52:55], v[144:147], a[52:55], v[52:55]\n"
      "v_mfma_f32_16x16x32_bf16 v[56:59], v[144:147], a[56:59], v[56:59]\n"
      "v_mfma_f32_16x16x32_bf16 v[60:63], v[144:147], a[60:63], v[60:63]\n"
      "global_load_lds_dwordx4 v247, s[70:71] offset:3072\n"
      "v_mfma_f32_16x16x32_bf16 v[64:67], v[148:151], a[32:35], v[64:67]\n"
      "v_mfma_f32_16x16x32_bf16 v[68:71], v[148:151], a[36:39], v[68:71]\n"
      "s_xor_b32 s70, s78, 128\n"
      "s_add_u32 s70, s8, s70\n"
      "s_addc_u32 s71, s9, 0\n"
      "s_add_u32 m0, s41, 12288\n"
      "v_mfma_f32_16x16x32_bf16 v[72:75], v[148:151], a[40:43], v[72:75]\n"
      "global_load_lds_dwordx4 v244, s[70:71] offset:0\n"
      "v_add_u32 v238, s42, v242\n"
      "v_mov_b32 v200, s37\n"
      "v_mov_b32 v201, s37\n"
      "v_mov_b32 v202, s37\n"
      "v_mov_b32 v203, s37\n"
      "s_waitcnt vmcnt(10)\n"
      "s_mov_b64 exec, 0xff\n"
      "global_store_dwordx4 v238, v[200:203], s[58:59] offset:128 sc1\n"
      "s_mov_b64 exec, -1\n"
      "v_mfma_f32_16x16x32_bf16 v[76:79], v[148:151], a[44:47], v[76:79]\n"
      "s_lshl2_add_u32 s48, s36, s35\n"
      "s_mul_i32 s48, s48, 147456\n"
      "s_mul_i32 s49, s33, 3072\n"
      "v_mfma_f32_16x16x32_bf16 v[80:83], v[148:151], a[48:51], v[80:83]\n"
      "s_add_u32 s48, s48, s49\n"
      "s_add_u32 s48, s48, 6291456\n"
      "s_add_u32 s60, s18, s48\n"
      "s_addc_u32 s61, s19, 0\n"
      "v_mfma_f32_16x16x32_bf16 v[84:87], v[148:151], a[52:55], v[84:87]\n"
      "s_mul_i32 s49, s34, 36864\n"
      "global_load_lds_dwordx4 v245, s[70:71] offset:1024\n"
      "s_add_u32 s62, s60, s49\n"
      "s_addc_u32 s63, s61, 0\n"
      "v_mfma_f32_16x16x32_bf16 v[88:91], v[148:151], a[56:59], v[88:91]\n"
      "v_mfma_f32_16x16x32_bf16 v[92:95], v[148:151], a[60:63], v[92:95]\n"
      "s_waitcnt vmcnt(8)\n"
      "v_mfma_f32_16x16x32_bf16 v[0:3], v[152:155], a[64:67], v[0:3]\n"
      "global_load_lds_dwordx4 v246, s[70:71] offset:2048\n"
      "ds_read_b128 a[128:131], v248 offset:4096\n"
      "v_mfma_f32_16x16x32_bf16 v[4:7], v[152:155], a[68:71], v[4:7]\n"
      "ds_read_b128 a[132:135], v248 offset:6144\n"
      "ds_read_b128 a[160:163], v249 offset:4096\n"
      "s_waitcnt lgkmcnt(14)\n"
      "v_mfma_f32_16x16x32_bf16 v[8:11], v[152:155], a[72:75], v[8:11]\n"
      "ds_read_b128 a[164:167], v249 offset:6144\n"
      "s_waitcnt lgkmcnt(14)\n"
      "v_mfma_f32_16x16x32_bf16 v[12:15], v[152:155], a[76:79], v[12:15]\n"
      "global_load_lds_dwordx4 v247, s[70:71] offset:3072\n"
      "s_waitcnt lgkmcnt(11)\n"
      "v_mfma_f32_16x16x32_bf16 v[16:19], v[152:155], a[80:83], v[16:19]\n"
      "s_waitcnt lgkmcnt(10)\n"
      "v_mfma_f32_16x16x32_bf16 v[20:23], v[152:155], a[84:87], v[20:23]\n"
      "global_load_dwordx4 v[164:167], v240, s[74:75] offset:-4096 sc1\n"
      "s_waitcnt lgkmcnt(7)\n"
      "v_mfma_f32_16x16x32_bf16 v[24:27], v[152:155], a[88:91], v[24:27]\n"
      "s_waitcnt lgkmcnt(6)\n"
      "v_mfma_f32_16x16x32_bf16 v[28:31], v[152:155], a[92:95], v[28:31]\n"
      "v_mfma_f32_16x16x32_bf16 v[32:35], v[156:159], a[64:67], v[32:35]\n"
      "global_load_dwordx4 v[168:171], v240, s[74:75] offset:-3072 sc1\n"
      "v_mfma_f32_16x16x32_bf16 v[36:39], v[156:159], a[68:71], v[36:39]\n"
      "v_mfma_f32_16x16x32_bf16 v[40:43], v[156:159], a[72:75], v[40:43]\n"
      "v_mfma_f32_16x16x32_bf16 v[44:47], v[156:159], a[76:79], v[44:47]\n"
      "global_load_dwordx4 v[172:175], v240, s[74:75] offset:-2048 sc1\n"
      "v_mfma_f32_16x16x32_bf16 v[48:51], v[156:159], a[80:83], v[48:51]\n"
      "v_mfma_f32_16x16x32_bf16 v[52:55], v[156:159], a[84:87], v[52:55]\n"
      "v_mfma_f32_16x16x32_bf16 v[56:59], v[156:159], a[88:91], v[56:59]\n"
      "s_xor_b32 s70, s79, 128\n"
      "s_add_u32 s70, s8, s70\n"
      "s_addc_u32 s71, s9, 0\n"
      "s_add_u32 m0, s82, 98304\n"
      "v_mfma_f32_16x16x32_bf16 v[60:63], v[156:159], a[92:95], v[60:63]\n"
      "s_waitcnt vmcnt(2)\n"
      "global_load_lds_dwordx4 v244, s[70:71] offset:0\n"
      "v_mfma_f32_16x16x32_bf16 v[64:67], v[160:163], a[64:67], v[64:67]\n"
      "v_mfma_f32_16x16x32_bf16 v[68:71], v[160:163], a[68:71], v[68:71]\n"
      "v_mfma_f32_16x16x32_bf16 v[72:75], v[160:163], a[72:75], v[72:75]\n"
      "global_load_lds_dwordx4 v245, s[70:71] offset:1024\n"
      "v_mfma_f32_16x16x32_bf16 v[76:79], v[160:163], a[76:79], v[76:79]\n"
      "v_mfma_f32_16x16x32_bf16 v[80:83], v[160:163], a[80:83], v[80:83]\n"
      "v_mfma_f32_16x16x32_bf16 v[84:87], v[160:163], a[84:87], v[84:87]\n"
      "global_load_lds_dwordx4 v246, s[70:71] offset:2048\n"
      "ds_read_b128 a[136:139], v248 offset:8192\n"
      "v_mfma_f32_16x16x32_bf16 v[88:91], v[160:163], a[88:91], v[88:91]\n"
      "ds_read_b128 a[140:143], v248 offset:10240\n"
      "ds_read_b128 a[168:171], v249 offset:8192\n"
      "v_mfma_f32_16x16x32_bf16 v[92:95], v[160:163], a[92:95], v[92:95]\n"
      "ds_read_b128 a[172:175], v249 offset:10240\n"
      "v_mfma_f32_16x16x32_bf16 v[0:3], v[164:167], a[96:99], v[0:3]\n"
      "global_load_lds_dwordx4 v247, s[70:71] offset:3072\n"
      "v_mfma_f32_16x16x32_bf16 v[4:7], v[164:167], a[100:103], v[4:7]\n"
      "v_mfma_f32_16x16x32_bf16 v[8:11], v[164:167], a[104:107], v[8:11]\n"
      "global_load_dwordx4 v[176:179], v241, s[10:11] offset:-4096 sc1\n"
      "v_mfma_f32_16x16x32_bf16 v[12:15], v[164:167], a[108:111], v[12:15]\n"
      "v_mfma_f32_16x16x32_bf16 v[16:19], v[164:167], a[112:115], v[16:19]\n"
      "v_mfma_f32_16x16x32_bf16 v[20:23], v[164:167], a[116:119], v[20:23]\n"
      "global_load_dwordx4 v[180:183], v241, s[10:11] offset:-3072 sc1\n"
      "s_waitcnt lgkmcnt(9)\n"
      "v_mfma_f32_16x16x32_bf16 v[24:27], v[164:167], a[120:123], v[24:27]\n"
      "s_waitcnt lgkmcnt(8)\n"
      "v_mfma_f32_16x16x32_bf16 v[28:31], v[164:167], a[124:127], v[28:31]\n"
      "s_waitcnt vmcnt(6)\n"
      "v_mfma_f32_16x16x32_bf16 v[32:35], v[168:171], a[96:99], v[32:35]\n"
      "global_load_dwordx4 v[184:187], v241, s[10:11] offset:-2048 sc1\n"
      "global_load_dwordx4 v[188:191], v241, s[72:73] offset:-4096 sc1\n"
      "v_mfma_f32_16x16x32_bf16 v[36:39], v[168:171], a[100:103], v[36:39]\n"
      "global_load_dwordx4 v[128:131], v241, s[72:73] offset:-3072 sc1\n"
      "global_load_dwordx4 v[132:135], v241, s[72:73] offset:-2048 sc1\n"
      "v_mfma_f32_16x16x32_bf16 v[40:43], v[168:171], a[104:107], v[40:43]\n"
      "global_load_dwordx4 v[136:139], v241, s[14:15] offset:-4096 sc1\n"
      "global_load_dwordx4 v[140:143], v241, s[74:75] offset:-4096 sc1\n"
      "v_mfma_f32_16x16x32_bf16 v[44:47], v[168:171], a[108:111], v[44:47]\n"
      "s_xor_b32 s70, s76, 384\n"
      "s_add_u32 s70, s8, s70\n"
      "s_addc_u32 s71, s9, 0\n"
      "s_add_u32 m0, s82, 114688\n"
      "v_mfma_f32_16x16x32_bf16 v[48:51], v[168:171], a[112:115], v[48:51]\n"
      "global_load_lds_dwordx4 v244, s[70:71] offset:0\n"
      "global_load_dwordx4 v[144:147], v241, s[14:15] offset:-3072 sc1\n"
      "v_mfma_f32_16x16x32_bf16 v[52:55], v[168:171], a[116:119], v[52:55]\n"
      "global_load_dwordx4 v[148:151], v241, s[74:75] offset:-3072 sc1\n"
      "global_load_dwordx4 v[152:155], v241, s[14:15] offset:-2048 sc1\n"
      "v_mfma_f32_16x16x32_bf16 v[56:59], v[168:171], a[120:123], v[56:59]\n"
      "global_load_dwordx4 v[156:159], v241, s[74:75] offset:-2048 sc1\n"
      "global_load_dword v252, v253, s[58:59] offset:128 sc1\n"
      "v_mfma_f32_16x16x32_bf16 v[60:63], v[168:171], a[124:127], v[60:63]\n"
      "global_load_lds_dwordx4 v245, s[70:71] offset:1024\n"
      "ds_read_b128 a[144:147], v248 offset:12288\n"
      "v_mfma_f32_16x16x32_bf16 v[64:67], v[172:175], a[96:99], v[64:67]\n"
      "ds_read_b128 a[148:151], v248 offset:14336\n"
      "ds_read_b128 a[176:179], v249 offset:12288\n"
      "v_mfma_f32_16x16x32_bf16 v[68:71], v[172:175], a[100:103], v[68:71]\n"
      "ds_read_b128 a[180:183], v249 offset:14336\n"
      "v_mfma_f32_16x16x32_bf16 v[72:75], v[172:175], a[104:107], v[72:75]\n"
      "global_load_lds_dwordx4 v246, s[70:71] offset:2048\n"
      "v_mfma_f32_16x16x32_bf16 v[76:79], v[172:175], a[108:111], v[76:79]\n"
      "v_mfma_f32_16x16x32_bf16 v[80:83], v[172:175], a[112:115], v[80:83]\n"
      "v_mfma_f32_16x16x32_bf16 v[84:87], v[172:175], a[116:119], v[84:87]\n"
      "global_load_lds_dwordx4 v247, s[70:71] offset:3072\n"
      "v_mfma_f32_16x16x32_bf16 v[88:91], v[172:175], a[120:123], v[88:91]\n"
      "v_mfma_f32_16x16x32_bf16 v[92:95], v[172:175], a[124:127], v[92:95]\n"
      "s_waitcnt vmcnt(15)\n"
      "s_xor_b32 s70, s77, 384\n"
      "s_add_u32 s70, s8, s70\n"
      "s_addc_u32 s71, s9, 0\n"
      "s_add_u32 m0, s82, 131072\n"
      "s_waitcnt lgkmcnt(11)\n"
      "v_mfma_f32_16x16x32_bf16 v[0:3], v[176:179], a[128:131], v[0:3]\n"
      "global_load_lds_dwordx4 v244, s[70:71] offset:0\n"
      "s_waitcnt lgkmcnt(10)\n"
      "v_mfma_f32_16x16x32_bf16 v[4:7], v[176:179], a[132:135], v[4:7]\n"
      "s_waitcnt lgkmcnt(7)\n"
      "v_mfma_f32_16x16x32_bf16 v[8:11], v[176:179], a[136:139], v[8:11]\n"
      "s_waitcnt lgkmcnt(6)\n"
      "v_mfma_f32_16x16x32_bf16 v[12:15], v[176:179], a[140:143], v[12:15]\n"
      "global_load_lds_dwordx4 v245, s[70:71] offset:1024\n"
      "s_waitcnt lgkmcnt(3)\n"
      "v_mfma_f32_16x16x32_bf16 v[16:19], v[176:179], a[144:147], v[16:19]\n"
      "s_lshr_b32 s54, s34, 1\n"
      "s_lshl2_add_u32 s54, s54, s35\n"
      "s_lshl_b32 s54, s54, 12\n"
      "s_waitcnt lgkmcnt(2)\n"
      "v_mfma_f32_16x16x32_bf16 v[20:23], v[176:179], a[148:151], v[20:23]\n"
      "s_add_u32 s54, s54, 4096\n"
      "v_lshl_add_u32 v200, v243, 6, s54\n"
      "global_load_dwordx2 v[202:203], v200, s[58:59] sc1\n"
      "ds_read_b128 a[152:155], v236 offset:0\n"
      "ds_read_b128 a[156:159], v236 offset:2048\n"
      "ds_read_b128 a[184:187], v237 offset:0\n"
      "ds_read_b128 a[188:191], v237 offset:2048\n"
      "s_waitcnt lgkmcnt(3)\n"
      "v_mfma_f32_16x16x32_bf16 v[24:27], v[176:179], a[152:155], v[24:27]\n"
      "global_load_lds_dwordx4 v246, s[70:71] offset:2048\n"
      "s_waitcnt vmcnt(4)\n"
      "ds_read_b128 a[192:195], v236 offset:16384\n"
      "s_waitcnt lgkmcnt(3)\n"
      "v_mfma_f32_16x16x32_bf16 v[28:31], v[176:179], a[156:159], v[28:31]\n"
      "ds_read_b128 a[196:199], v236 offset:18432\n"
      "ds_read_b128 a[224:227], v237 offset:16384\n"
      "v_mfma_f32_16x16x32_bf16 v[32:35], v[180:183], a[128:131], v[32:35]\n"
      "ds_read_b128 a[228:231], v237 offset:18432\n"
      "v_mfma_f32_16x16x32_bf16 v[36:39], v[180:183], a[132:135], v[36:39]\n"
      "global_load_lds_dwordx4 v247, s[70:71] offset:3072\n"
      "v_mfma_f32_16x16x32_bf16 v[40:43], v[180:183], a[136:139], v[40:43]\n"
      "v_mfma_f32_16x16x32_bf16 v[44:47], v[180:183], a[140:143], v[44:47]\n"
      "s_xor_b32 s70, s78, 384\n"
      "s_add_u32 s70, s8, s70\n"
      "s_addc_u32 s71, s9, 0\n"
      "s_add_u32 m0, s82, 147456\n"
      "v_mfma_f32_16x16x32_bf16 v[48:51], v[180:183], a[144:147], v[48:51]\n"
      "global_load_lds_dwordx4 v244, s[70:71] offset:0\n"
      "v_mfma_f32_16x16x32_bf16 v[52:55], v[180:183], a[148:151], v[52:55]\n"
      "v_mfma_f32_16x16x32_bf16 v[56:59], v[180:183], a[152:155], v[56:59]\n"
      "v_mfma_f32_16x16x32_bf16 v[60:63], v[180:183], a[156:159], v[60:63]\n"
      "global_load_lds_dwordx4 v245, s[70:71] offset:1024\n"
      "v_mfma_f32_16x16x32_bf16 v[64:67], v[184:187], a[128:131], v[64:67]\n"
      "v_mfma_f32_16x16x32_bf16 v[68:71], v[184:187], a[132:135], v[68:71]\n"
      "v_mfma_f32_16x16x32_bf16 v[72:75], v[184:187], a[136:139], v[72:75]\n"
      "global_load_lds_dwordx4 v246, s[70:71] offset:2048\n"
      "v_mfma_f32_16x16x32_bf16 v[76:79], v[184:187], a[140:143], v[76:79]\n"
      "v_mfma_f32_16x16x32_bf16 v[80:83], v[184:187], a[144:147], v[80:83]\n"
      "v_mfma_f32_16x16x32_bf16 v[84:87], v[184:187], a[148:151], v[84:87]\n"
      "global_load_lds_dwordx4 v247, s[70:71] offset:3072\n"
      "v_mfma_f32_16x16x32_bf16 v[88:91], v[184:187], a[152:155], v[88:91]\n"
      "v_mfma_f32_16x16x32_bf16 v[92:95], v[184:187], a[156:159], v[92:95]\n"
      "s_xor_b32 s70, s79, 384\n"
      "s_add_u32 s70, s8, s70\n"
      "s_addc_u32 s71, s9, 0\n"
      "s_add_u32 m0, s41, 0\n"
      "v_mfma_f32_16x16x32_bf16 v[0:3], v[188:191], a[160:163], v[0:3]\n"
      "global_load_lds_dwordx4 v244, s[70:71] offset:0\n"
      "v_mfma_f32_16x16x32_bf16 v[4:7], v[188:191], a[164:167], v[4:7]\n"
      "v_mfma_f32_16x16x32_bf16 v[8:11], v[188:191], a[168:171], v[8:11]\n"
      "v_mfma_f32_16x16x32_bf16 v[12:15], v[188:191], a[172:175], v[12:15]\n"
      "global_load_lds_dwordx4 v245, s[70:71] offset:1024\n"
      "v_mfma_f32_16x16x32_bf16 v[16:19], v[188:191], a[176:179], v[16:19]\n"
      "v_mfma_f32_16x16x32_bf16 v[20:23], v[188:191], a[180:183], v[20:23]\n"
      "s_waitcnt lgkmcnt(5)\n"
      "v_mfma_f32_16x16x32_bf16 v[24:27], v[188:191], a[184:187], v[24:27]\n"
      "global_load_lds_dwordx4 v246, s[70:71] offset:2048\n"
      "s_waitcnt lgkmcnt(4)\n"
      "v_mfma_f32_16x16x32_bf16 v[28:31], v[188:191], a[188:191], v[28:31]\n"
      "v_mfma_f32_16x16x32_bf16 v[32:35], v[128:131], a[160:163], v[32:35]\n"
      "v_mfma_f32_16x16x32_bf16 v[36:39], v[128:131], a[164:167], v[36:39]\n"
      "global_load_lds_dwordx4 v247, s[70:71] offset:3072\n"
      "v_mfma_f32_16x16x32_bf16 v[40:43], v[128:131], a[168:171], v[40:43]\n"
      "v_mfma_f32_16x16x32_bf16 v[44:47], v[128:131], a[172:175], v[44:47]\n"
      "v_mfma_f32_16x16x32_bf16 v[48:51], v[128:131], a[176:179], v[48:51]\n"
      "v_mfma_f32_16x16x32_bf16 v[52:55], v[128:131], a[180:183], v[52:55]\n"
      "v_mfma_f32_16x16x32_bf16 v[56:59], v[128:131], a[184:187], v[56:59]\n"
      "v_mfma_f32_16x16x32_bf16 v[60:63], v[128:131], a[188:191], v[60:63]\n"
      "s_waitcnt vmcnt(8)\n"
      "ds_read_b128 a[200:203], v236 offset:32768\n"
      "v_mfma_f32_16x16x32_bf16 v[64:67], v[132:135], a[160:163], v[64:67]\n"
      "ds_read_b128 a[204:207], v236 offset:34816\n"
      "ds_read_b128 a[232:235], v237 offset:32768\n"
      "v_mfma_f32_16x16x32_bf16 v[68:71], v[132:135], a[164:167], v[68:71]\n"
      "ds_read_b128 a[236:239], v237 offset:34816\n"
      "v_mfma_f32_16x16x32_bf16 v[72:75], v[132:135], a[168:171], v[72:75]\n"
      "v_cmp_ne_u32 vcc, s37, v252\n"
      "s_nop 1\n"
      "s_cbranch_vccnz .Lp_s5_%=\n"
      ".Lp_o6_%=:\n"
      "v_mfma_f32_16x16x32_bf16 v[76:79], v[132:135], a[172:175], v[76:79]\n"
      "v_mfma_f32_16x16x32_bf16 v[80:83], v[132:135], a[176:179], v[80:83]\n"
      "global_load_dwordx4 v[160:163], v240, s[10:11] offset:-1024 sc1\n"
      "global_load_dwordx4 v[164:167], v240, s[72:73] offset:-1024 sc1\n"
      "v_mfma_f32_16x16x32_bf16 v[84:87], v[132:135], a[180:183], v[84:87]\n"
      "global_load_dwordx4 v[168:171], v240, s[14:15] offset:-1024 sc1\n"
      "global_load_dwordx4 v[172:175], v240, s[74:75] offset:-1024 sc1\n"
      "v_mfma_f32_16x16x32_bf16 v[88:91], v[132:135], a[184:187], v[88:91]\n"
      "global_load_dwordx4 v[176:179], v241, s[10:11] offset:-1024 sc1\n"
      "global_load_dwordx4 v[180:183], v241, s[72:73] offset:-1024 sc1\n"
      "v_mfma_f32_16x16x32_bf16 v[92:95], v[132:135], a[188:191], v[92:95]\n"
      "global_load_dwordx4 v[184:187], v241, s[14:15] offset:-1024 sc1\n"
      "global_load_dwordx4 v[188:191], v241, s[74:75] offset:-1024 sc1\n"
      "s_waitcnt lgkmcnt(7)\n"
      "v_mfma_f32_16x16x32_bf16 v[0:3], v[136:139], a[192:195], v[0:3]\n"
      "global_load_dwordx4 v[128:131], v240, s[10:11] offset:0 sc1\n"
      "global_load_dwordx4 v[132:135], v240, s[72:73] offset:0 sc1\n"
      "s_waitcnt lgkmcnt(6)\n"
      "v_mfma_f32_16x16x32_bf16 v[4:7], v[136:139], a[196:199], v[4:7]\n"
      "s_waitcnt lgkmcnt(3)\n"
      "v_mfma_f32_16x16x32_bf16 v[8:11], v[136:139], a[200:203], v[8:11]\n"
      "s_waitcnt lgkmcnt(2)\n"
      "v_mfma_f32_16x16x32_bf16 v[12:15], v[136:139], a[204:207], v[12:15]\n"
      "s_waitcnt vmcnt(14)\n"
      "ds_read_b128 a[208:211], v236 offset:49152\n"
      "ds_read_b128 a[212:215], v236 offset:51200\n"
      "ds_read_b128 a[240:243], v237 offset:49152\n"
      "ds_read_b128 a[244:247], v237 offset:51200\n"
      "s_waitcnt lgkmcnt(3)\n"
      "v_mfma_f32_16x16x32_bf16 v[16:19], v[136:139], a[208:211], v[16:19]\n"
      "s_waitcnt vmcnt(10)\n"
      "ds_read_b128 a[216:219], v248 offset:0\n"
      "s_waitcnt lgkmcnt(3)\n"
      "v_mfma_f32_16x16x32_bf16 v[20:23], v[136:139], a[212:215], v[20:23]\n"
      "ds_read_b128 a[220:223], v248 offset:2048\n"
      "ds_read_b128 a[248:251], v249 offset:0\n"
      "ds_read_b128 a[252:255], v249 offset:2048\n"
      "s_waitcnt lgkmcnt(3)\n"
      "v_mfma_f32_16x16x32_bf16 v[24:27], v[136:139], a[216:219], v[24:27]\n"
      "s_sub_u32 s48, s33, 1\n"
      "s_and_b32 s48, s48, 3\n"
      "s_lshl_b32 s48, s48, 14\n"
      "s_waitcnt lgkmcnt(2)\n"
      "v_mfma_f32_16x16x32_bf16 v[28:31], v[136:139], a[220:223], v[28:31]\n"
      "global_load_dwordx4 v[136:139], v240, s[14:15] offset:0 sc1\n"
      "v_lshl_add_u32 v244, v243, 2, s48\n"
      "v_mfma_f32_16x16x32_bf16 v[0:3], v[140:143], a[224:227], v[0:3]\n"
      "s_sub_u32 s48, s33, 2\n"
      "s_and_b32 s48, s48, 3\n"
      "s_lshl_b32 s48, s48, 14\n"
      "v_mfma_f32_16x16x32_bf16 v[4:7], v[140:143], a[228:231], v[4:7]\n"
      "v_lshl_add_u32 v245, v243, 2, s48\n"
      "s_sub_u32 s48, s33, 3\n"
      "v_mfma_f32_16x16x32_bf16 v[8:11], v[140:143], a[232:235], v[8:11]\n"
      "s_and_b32 s48, s48, 3\n"
      "s_lshl_b32 s48, s48, 14\n"
      "v_lshl_add_u32 v246, v243, 2, s48\n"
      "v_mfma_f32_16x16x32_bf16 v[12:15], v[140:143], a[236:239], v[12:15]\n"
      "v_mfma_f32_16x16x32_bf16 v[16:19], v[140:143], a[240:243], v[16:19]\n"
      "v_mfma_f32_16x16x32_bf16 v[20:23], v[140:143], a[244:247], v[20:23]\n"
      "s_waitcnt lgkmcnt(1)\n"
      "v_mfma_f32_16x16x32_bf16 v[24:27], v[140:143], a[248:251], v[24:27]\n"
      "s_waitcnt lgkmcnt(0)\n"
      "v_mfma_f32_16x16x32_bf16 v[28:31], v[140:143], a[252:255], v[28:31]\n"
      "global_load_dwordx4 v[140:143], v240, s[74:75] offset:0 sc1\n"
      "v_mfma_f32_16x16x32_bf16 v[32:35], v[144:147], a[192:195], v[32:35]\n"
      "v_cmp_ne_u32 vcc, s37, v203\n"
      "s_nop 1\n"
      "s_cbranch_vccnz .Lp_s8_%=\n"
      ".Lp_o9_%=:\n"
      "v_mfma_f32_16x16x32_bf16 v[36:39], v[144:147], a[196:199], v[36:39]\n"
      "s_mov_b32 m0, s41\n"
      "v_cvt_pk_bf16_f32 v8, v8, v9\n"
      "v_cvt_pk_bf16_f32 v9, v10, v11\n"
      "v_mfma_f32_16x16x32_bf16 v[40:43], v[144:147], a[200:203], v[40:43]\n"
      "ds_write_addtid_b32 v8 offset:0\n"
      "v_cvt_pk_bf16_f32 v10, v12, v13\n"
      "v_mfma_f32_16x16x32_bf16 v[44:47], v[144:147], a[204:207], v[44:47]\n"
      "ds_write_addtid_b32 v9 offset:256\n"
      "v_cvt_pk_bf16_f32 v11, v14, v15\n"
      "v_mfma_f32_16x16x32_bf16 v[48:51], v[144:147], a[208:211], v[48:51]\n"
      "ds_write_addtid_b32 v10 offset:512\n"
      "v_cvt_pk_bf16_f32 v12, v16, v17\n"
      "v_mfma_f32_16x16x32_bf16 v[52:55], v[144:147], a[212:215], v[52:55]\n"
      "ds_write_addtid_b32 v11 offset:768\n"
      "v_cvt_pk_bf16_f32 v13, v18, v19\n"
      "v_mfma_f32_16x16x32_bf16 v[56:59], v[144:147], a[216:219], v[56:59]\n"
      "ds_write_addtid_b32 v12 offset:1024\n"
      "v_cvt_pk_bf16_f32 v14, v20, v21\n"
      "v_mfma_f32_16x16x32_bf16 v[60:63], v[144:147], a[220:223], v[60:63]\n"
      "global_load_dwordx4 v[144:147], v241, s[10:11] offset:0 sc1\n"
      "v_max_u32_dpp v202, v202, v202 quad_perm:[1,0,3,2] row_mask:0xf bank_mask:0xf\n"
      "v_mfma_f32_16x16x32_bf16 v[32:35], v[148:151], a[224:227], v[32:35]\n"
      "ds_write_addtid_b32 v13 offset:1280\n"
      "v_cvt_pk_bf16_f32 v15, v22, v23\n"
      "v_mfma_f32_16x16x32_bf16 v[36:39], v[148:151], a[228:231], v[36:39]\n"
      "ds_write_addtid_b32 v14 offset:1536\n"
      "v_cvt_pk_bf16_f32 v16, v24, v25\n"
      "v_mfma_f32_16x16x32_bf16 v[40:43], v[148:151], a[232:235], v[40:43]\n"
      "ds_write_addtid_b32 v15 offset:1792\n"
      "v_cvt_pk_bf16_f32 v17, v26, v27\n"
      "v_mfma_f32_16x16x32_bf16 v[44:47], v[148:151], a[236:239], v[44:47]\n"
      "ds_write_addtid_b32 v16 offset:2048\n"
      "v_cvt_pk_bf16_f32 v18, v28, v29\n"
      "v_mfma_f32_16x16x32_bf16 v[48:51], v[148:151], a[240:243], v[48:51]\n"
      "ds_write_addtid_b32 v17 offset:2304\n"
      "v_cvt_pk_bf16_f32 v19, v30, v31\n"
      "v_mfma_f32_16x16x32_bf16 v[52:55], v[148:151], a[244:247], v[52:55]\n"
      "ds_write_addtid_b32 v18 offset:2560\n"
      "v_max_u32_dpp v202, v202, v202 quad_perm:[2,3,0,1] row_mask:0xf bank_mask:0xf\n"
      "v_mfma_f32_16x16x32_bf16 v[56:59], v[148:151], a[248:251], v[56:59]\n"
      "ds_write_addtid_b32 v19 offset:2816\n"
      "v_max_u32_dpp v202, v202, v202 row_half_mirror row_mask:0xf bank_mask:0xf\n"
      "v_mfma_f32_16x16x32_bf16 v[60:63], v[148:151], a[252:255], v[60:63]\n"
      "global_load_dwordx4 v[148:151], v241, s[72:73] offset:0 sc1\n"
      "v_max_u32_dpp v202, v202, v202 row_mirror row_mask:0xf bank_mask:0xf\n"
      "v_mfma_f32_16x16x32_bf16 v[64:67], v[152:155], a[192:195], v[64:67]\n"
      "v_mov_b32 v201, v202\n"
      "s_nop 1\n"
      "v_permlane16_swap_b32 v202, v201\n"
      "v_mfma_f32_16x16x32_bf16 v[68:71], v[152:155], a[196:199], v[68:71]\n"
      "v_cvt_pk_bf16_f32 v40, v40, v41\n"
      "v_cvt_pk_bf16_f32 v41, v42, v43\n"
      "v_mfma_f32_16x16x32_bf16 v[72:75], v[152:155], a[200:203], v[72:75]\n"
      "ds_write_addtid_b32 v40 offset:3072\n"
      "v_cvt_pk_bf16_f32 v42, v44, v45\n"
      "v_mfma_f32_16x16x32_bf16 v[76:79], v[152:155], a[204:207], v[76:79]\n"
      "ds_write_addtid_b32 v41 offset:3328\n"
      "v_cvt_pk_bf16_f32 v43, v46, v47\n"
      "v_mfma_f32_16x16x32_bf16 v[80:83], v[152:155], a[208:211], v[80:83]\n"
      "ds_write_addtid_b32 v42 offset:3584\n"
      "v_cvt_pk_bf16_f32 v44, v48, v49\n"
      "v_mfma_f32_16x16x32_bf16 v[84:87], v[152:155], a[212:215], v[84:87]\n"
      "ds_write_addtid_b32 v43 offset:3840\n"
      "v_cvt_pk_bf16_f32 v45, v50, v51\n"
      "v_mfma_f32_16x16x32_bf16 v[88:91], v[152:155], a[216:219], v[88:91]\n"
      "ds_write_addtid_b32 v44 offset:4096\n"
      "v_cvt_pk_bf16_f32 v46, v52, v53\n"
      "v_mfma_f32_16x16x32_bf16 v[92:95], v[152:155], a[220:223], v[92:95]\n"
      "global_load_dwordx4 v[152:155], v241, s[14:15] offset:0 sc1\n"
      "v_max_u32 v202, v202, v201\n"
      "v_mfma_f32_16x16x32_bf16 v[64:67], v[156:159], a[224:227], v[64:67]\n"
      "ds_write_addtid_b32 v45 offset:4352\n"
      "v_cvt_pk_bf16_f32 v47, v54, v55\n"
      "v_mfma_f32_16x16x32_bf16 v[68:71], v[156:159], a[228:231], v[68:71]\n"
      "ds_write_addtid_b32 v46 offset:4608\n"
      "v_cvt_pk_bf16_f32 v48, v56, v57\n"
      "v_mfma_f32_16x16x32_bf16 v[72:75], v[156:159], a[232:235], v[72:75]\n"
      "ds_write_addtid_b32 v47 offset:4864\n"
      "v_cvt_pk_bf16_f32 v49, v58, v59\n"
      "v_mfma_f32_16x16x32_bf16 v[76:79], v[156:159], a[236:239], v[76:79]\n"
      "ds_write_addtid_b32 v48 offset:5120\n"
      "v_cvt_pk_bf16_f32 v50, v60, v61\n"
      "v_mfma_f32_16x16x32_bf16 v[80:83], v[156:159], a[240:243], v[80:83]\n"
      "ds_write_addtid_b32 v49 offset:5376\n"
      "v_cvt_pk_bf16_f32 v51, v62, v63\n"
      "v_mfma_f32_16x16x32_bf16 v[84:87], v[156:159], a[244:247], v[84:87]\n"
      "ds_write_addtid_b32 v50 offset:5632\n"
      "v_mov_b32 v201, v202\n"
      "v_mfma_f32_16x16x32_bf16 v[88:91], v[156:159], a[248:251], v[88:91]\n"
      "ds_write_addtid_b32 v51 offset:5888\n"
      "v_permlane32_swap_b32 v202, v201\n"
      "v_mfma_f32_16x16x32_bf16 v[92:95], v[156:159], a[252:255], v[92:95]\n"
      "s_waitcnt vmcnt(13)\n"
      "global_load_dwordx4 v[156:159], v241, s[74:75] offset:0 sc1\n"
      "v_max_u32 v202, v202, v201\n"
      "v_mfma_f32_16x16x32_bf16 v[96:99], v[160:163], a[0:3], 0\n"
      "v_readfirstlane_b32 s40, v202\n"
      "s_nop 1\n"
      "s_andn2_b32 s42, 0x7ff, s40\n"
      "v_mfma_f32_16x16x32_bf16 v[100:103], v[160:163], a[4:7], 0\n"
      "s_waitcnt lgkmcnt(0)\n"
      "s_barrier\n"
      "v_mfma_f32_16x16x32_bf16 v[104:107], v[160:163], a[8:11], 0\n"
      "ds_read2st64_b32 v[20:21], v244 offset0:0 offset1:1\n"
      "v_bfe_u32 v248, v243, 1, 3\n"
      "v_mfma_f32_16x16x32_bf16 v[108:111], v[160:163], a[12:15], 0\n"
      "ds_read2st64_b32 v[22:23], v245 offset0:4 offset1:5\n"
      "v_lshrrev_b32 v249, 4, v243\n"
      "v_mfma_f32_16x16x32_bf16 v[112:115], v[160:163], a[16:19], 0\n"
      "ds_read2st64_b32 v[24:25], v244 offset0:2 offset1:3\n"
      "v_lshlrev_b32 v249, 1, v249\n"
      "v_mfma_f32_16x16x32_bf16 v[116:119], v[160:163], a[20:23], 0\n"
      "ds_read2st64_b32 v[26:27], v245 offset0:6 offset1:7\n"
      "v_sub_u32 v248, v248, v249\n"
      "v_mfma_f32_16x16x32_bf16 v[120:123], v[160:163], a[24:27], 0\n"
      "ds_read2st64_b32 v[28:29], v246 offset0:8 offset1:9\n"
      "v_and_b32 v249, 16, v242\n"
      "v_mfma_f32_16x16x32_bf16 v[124:127], v[160:163], a[28:31], 0\n"
      "global_load_dwordx4 v[160:163], v240, s[10:11] offset:1024 sc1\n"
      "v_mov_b32 v254, 0x3f80\n"
      "v_mfma_f32_16x16x32_bf16 v[96:99], v[164:167], a[32:35], v[96:99]\n"
      "ds_read2st64_b32 v[30:31], v246 offset0:10 offset1:11\n"
      "v_lshlrev_b32 v249, v249, v254\n"
      "v_mfma_f32_16x16x32_bf16 v[100:103], v[164:167], a[36:39], v[100:103]\n"
      "ds_read2st64_b32 v[52:53], v244 offset0:12 offset1:13\n"
      "ds_read2st64_b32 v[54:55], v245 offset0:16 offset1:17\n"
      "v_mfma_f32_16x16x32_bf16 v[104:107], v[164:167], a[40:43], v[104:107]\n"
      "ds_read2st64_b32 v[56:57], v244 offset0:14 offset1:15\n"
      "ds_read2st64_b32 v[58:59], v245 offset0:18 offset1:19\n"
      "v_mfma_f32_16x16x32_bf16 v[108:111], v[164:167], a[44:47], v[108:111]\n"
      "ds_read2st64_b32 v[60:61], v246 offset0:20 offset1:21\n"
      "ds_read2st64_b32 v[62:63], v246 offset0:22 offset1:23\n"
      "v_mfma_f32_16x16x32_bf16 v[112:115], v[164:167], a[48:51], v[112:115]\n"
      "v_cmp_eq_u32 vcc, 0, v248\n"
      "s_nop 1\n"
      "v_cndmask_b32 v250, 0, v249, vcc\n"
      "v_mfma_f32_16x16x32_bf16 v[116:119], v[164:167], a[52:55], v[116:119]\n"
      "v_cmp_eq_u32 vcc, 1, v248\n"
      "s_nop 1\n"
      "v_cndmask_b32 v251, 0, v249, vcc\n"
      "v_mfma_f32_16x16x32_bf16 v[120:123], v[164:167], a[56:59], v[120:123]\n"
      "v_cvt_pk_bf16_f32 v72, v72, v73\n"
      "v_cvt_pk_bf16_f32 v73, v74, v75\n"
      "v_mfma_f32_16x16x32_bf16 v[124:127], v[164:167], a[60:63], v[124:127]\n"
      "s_waitcnt vmcnt(13)\n"
      "global_load_dwordx4 v[164:167], v240, s[72:73] offset:1024 sc1\n"
      "v_mov_b32 v252, v250\n"
      "v_mfma_f32_16x16x32_bf16 v[96:99], v[168:171], a[64:67], v[96:99]\n"
      "ds_write_addtid_b32 v72 offset:6144\n"
      "v_cvt_pk_bf16_f32 v74, v76, v77\n"
      "v_mfma_f32_16x16x32_bf16 v[100:103], v[168:171], a[68:71], v[100:103]\n"
      "ds_write_addtid_b32 v73 offset:6400\n"
      "v_cvt_pk_bf16_f32 v75, v78, v79\n"
      "v_mfma_f32_16x16x32_bf16 v[104:107], v[168:171], a[72:75], v[104:107]\n"
      "ds_write_addtid_b32 v74 offset:6656\n"
      "v_cvt_pk_bf16_f32 v76, v80, v81\n"
      "v_mfma_f32_16x16x32_bf16 v[108:111], v[168:171], a[76:79], v[108:111]\n"
      "ds_write_addtid_b32 v75 offset:6912\n"
      "v_cvt_pk_bf16_f32 v77, v82, v83\n"
      "v_mfma_f32_16x16x32_bf16 v[112:115], v[168:171], a[80:83], v[112:115]\n"
      "ds_write_addtid_b32 v76 offset:7168\n"
      "v_cvt_pk_bf16_f32 v78, v84, v85\n"
      "v_mfma_f32_16x16x32_bf16 v[116:119], v[168:171], a[84:87], v[116:119]\n"
      "ds_write_addtid_b32 v77 offset:7424\n"
      "v_cvt_pk_bf16_f32 v79, v86, v87\n"
      "v_mfma_f32_16x16x32_bf16 v[120:123], v[168:171], a[88:91], v[120:123]\n"
      "ds_write_addtid_b32 v78 offset:7680\n"
      "v_cvt_pk_bf16_f32 v80, v88, v89\n"
      "v_mfma_f32_16x16x32_bf16 v[124:127], v[168:171], a[92:95], v[124:127]\n"
      "global_load_dwordx4 v[168:171], v240, s[14:15] offset:1024 sc1\n"
      "v_mov_b32 v253, v251\n"
      "v_mfma_f32_16x16x32_bf16 v[96:99], v[172:175], a[96:99], v[96:99]\n"
      "ds_write_addtid_b32 v79 offset:7936\n"
      "v_cvt_pk_bf16_f32 v81, v90, v91\n"
      "v_mfma_f32_16x16x32_bf16 v[100:103], v[172:175], a[100:103], v[100:103]\n"
      "ds_write_addtid_b32 v80 offset:8192\n"
      "v_cvt_pk_bf16_f32 v82, v92, v93\n"
      "v_mfma_f32_16x16x32_bf16 v[104:107], v[172:175], a[104:107], v[104:107]\n"
      "ds_write_addtid_b32 v81 offset:8448\n"
      "v_cvt_pk_bf16_f32 v83, v94, v95\n"
      "v_mfma_f32_16x16x32_bf16 v[108:111], v[172:175], a[108:111], v[108:111]\n"
      "ds_write_addtid_b32 v82 offset:8704\n"
      "v_mov_b32 v248, 0\n"
      "v_mfma_f32_16x16x32_bf16 v[112:115], v[172:175], a[112:115], v[112:115]\n"
      "ds_write_addtid_b32 v83 offset:8960\n"
      "v_mov_b32 v249, 0\n"
      "v_mfma_f32_16x16x32_bf16 v[116:119], v[172:175], a[116:119], v[116:119]\n"
      "v_mov_b32 v254, 0\n"
      "v_mov_b32 v255, 0\n"
      "v_mfma_f32_16x16x32_bf16 v[120:123], v[172:175], a[120:123], v[120:123]\n"
      "s_add_u32 s48, s34, 1\n"
      "s_lshr_b32 s43, s42, 5\n"
      "s_lshl2_add_u32 s44, s36, s33\n"
      "s_cmp_eq_u32 s43, s44\n"
      "s_cbranch_scc0 .Lp_e10_%=\n"
      "s_lshl_b32 s43, s42, 10\n"
      "v_add_u32 v197, s43, v242\n"
      "global_load_dwordx4 v[232:235], v197, s[16:17]\n"
      "global_load_dwordx4 v[236:239], v242, s[24:25]\n"
      ".Lp_e10_%=:\n"
      "v_mfma_f32_16x16x32_bf16 v[124:127], v[172:175], a[124:127], v[124:127]\n"
      "s_waitcnt vmcnt(13)\n"
      "global_load_dwordx4 v[172:175], v240, s[74:75] offset:1024 sc1\n"
      "s_and_b32 s48, s48, 3\n"
      "v_mfma_f32_16x16x32_bf16 v[96:99], v[176:179], a[128:131], v[96:99]\n"
      "s_mul_i32 s48, s48, 36864\n"
      "s_add_u32 s48, s48, 0\n"
      "s_add_u32 s52, s60, s48\n"
      "s_addc_u32 s53, s61, 0\n"
      "v_mfma_f32_16x16x32_bf16 v[100:103], v[176:179], a[132:135], v[100:103]\n"
      "s_waitcnt lgkmcnt(15)\n"
      "v_mfma_f32_16x16x32_bf16 v[0:3], v[250:253], v[20:23], v[0:3]\n"
      "s_lshl2_add_u32 s54, s36, s33\n"
      "v_mfma_f32_16x16x32_bf16 v[104:107], v[176:179], a[136:139], v[104:107]\n"
      "s_lshl_b32 s55, s54, 15\n"
      "v_mfma_f32_16x16x32_bf16 v[4:7], v[250:253], v[24:27], v[4:7]\n"
      "s_lshl2_add_u32 s56, s35, s34\n"
      "v_mfma_f32_16x16x32_bf16 v[108:111], v[176:179], a[140:143], v[108:111]\n"
      "s_lshl_b32 s56, s56, 6\n"
      "v_mfma_f32_16x16x32_bf16 v[0:3], v[252:255], v[28:31], v[0:3]\n"
      "s_add_u32 s55, s55, s56\n"
      "v_mfma_f32_16x16x32_bf16 v[112:115], v[176:179], a[144:147], v[112:115]\n"
      "v_mfma_f32_16x16x32_bf16 v[4:7], v[248:251], v[28:31], v[4:7]\n"
      "v_mov_b32 v11, s37\n"
      "v_mfma_f32_16x16x32_bf16 v[116:119], v[176:179], a[148:151], v[116:119]\n"
      "v_and_b32 v198, 15, v243\n"
      "v_and_b32 v199, 48, v243\n"
      "v_mfma_f32_16x16x32_bf16 v[120:123], v[176:179], a[152:155], v[120:123]\n"
      "v_cvt_pk_bf16_f32 v8, v0, v1\n"
      "v_cvt_pk_bf16_f32 v9, v2, v3\n"
      "v_mfma_f32_16x16x32_bf16 v[124:127], v[176:179], a[156:159], v[124:127]\n"
      "global_load_dwordx4 v[176:179], v241, s[10:11] offset:1024 sc1\n"
      "v_cvt_pk_bf16_f32 v10, v4, v5\n"
      "v_mfma_f32_16x16x32_bf16 v[96:99], v[180:183], a[160:163], v[96:99]\n"
      "v_cvt_pk_bf16_f32 v247, v6, v7\n"
      "v_lshl_or_b32 v199, v198, 10, v199\n"
      "v_mfma_f32_16x16x32_bf16 v[100:103], v[180:183], a[164:167], v[100:103]\n"
      "global_store_dwordx4 v242, v[8:11], s[52:53] offset:0 sc1\n"
      "s_waitcnt lgkmcnt(12)\n"
      "v_mfma_f32_16x16x32_bf16 v[104:107], v[180:183], a[168:171], v[104:107]\n"
      "v_mfma_f32_16x16x32_bf16 v[32:35], v[250:253], v[52:55], v[32:35]\n"
      "v_add_u32 v199, s55, v199\n"
      "v_mfma_f32_16x16x32_bf16 v[108:111], v[180:183], a[172:175], v[108:111]\n"
      "v_mfma_f32_16x16x32_bf16 v[36:39], v[250:253], v[56:59], v[36:39]\n"
      "v_add_u32 v197, 16384, v199\n"
      "v_mfma_f32_16x16x32_bf16 v[112:115], v[180:183], a[176:179], v[112:115]\n"
      "v_mfma_f32_16x16x32_bf16 v[32:35], v[252:255], v[60:63], v[32:35]\n"
      "v_mfma_f32_16x16x32_bf16 v[116:119], v[180:183], a[180:183], v[116:119]\n"
      "v_mfma_f32_16x16x32_bf16 v[36:39], v[248:251], v[60:63], v[36:39]\n"
      "v_mov_b32 v43, s37\n"
      "v_mfma_f32_16x16x32_bf16 v[120:123], v[180:183], a[184:187], v[120:123]\n"
      "global_load_dwordx4 v[224:227], v199, s[16:17]\n"
      "v_mov_b32 v44, v247\n"
      "v_mfma_f32_16x16x32_bf16 v[124:127], v[180:183], a[188:191], v[124:127]\n"
      "s_waitcnt vmcnt(15)\n"
      "global_load_dwordx4 v[180:183], v241, s[72:73] offset:1024 sc1\n"
      "v_mov_b32 v47, s37\n"
      "v_mfma_f32_16x16x32_bf16 v[96:99], v[184:187], a[192:195], v[96:99]\n"
      "global_load_dwordx4 v[228:231], v197, s[16:17]\n"
      "s_lshr_b32 s55, s34, 1\n"
      "v_mfma_f32_16x16x32_bf16 v[100:103], v[184:187], a[196:199], v[100:103]\n"
      "s_lshl2_add_u32 s55, s55, s35\n"
      "v_cvt_pk_bf16_f32 v40, v32, v33\n"
      "v_cvt_pk_bf16_f32 v41, v34, v35\n"
      "v_mfma_f32_16x16x32_bf16 v[104:107], v[184:187], a[200:203], v[104:107]\n"
      "s_lshl_b32 s55, s55, 15\n"
      "v_cvt_pk_bf16_f32 v42, v36, v37\n"
      "v_cvt_pk_bf16_f32 v45, v38, v39\n"
      "v_mfma_f32_16x16x32_bf16 v[108:111], v[184:187], a[204:207], v[108:111]\n"
      "s_lshl_b32 s54, s54, 9\n"
      "global_store_dwordx4 v242, v[40:43], s[52:53] offset:1024 sc1\n"
      "s_add_u32 s54, s54, s55\n"
      "v_mfma_f32_16x16x32_bf16 v[112:115], v[184:187], a[208:211], v[112:115]\n"
      "s_add_u32 s54, s54, 786432\n"
      "global_store_dwordx4 v242, v[44:47], s[52:53] offset:2048 sc1\n"
      "s_add_u32 s48, s34, 2\n"
      "v_mfma_f32_16x16x32_bf16 v[116:119], v[184:187], a[212:215], v[116:119]\n"
      "s_and_b32 s48, s48, 3\n"
      "s_mul_i32 s48, s48, 36864\n"
      "s_add_u32 s48, s48, 12288\n"
      "v_mfma_f32_16x16x32_bf16 v[120:123], v[184:187], a[216:219], v[120:123]\n"
      "s_add_u32 s52, s60, s48\n"
      "s_addc_u32 s53, s61, 0\n"
      "v_lshl_add_u32 v198, v198, 4, s54\n"
      "v_mfma_f32_16x16x32_bf16 v[124:127], v[184:187], a[220:223], v[124:127]\n"
      "global_load_dwordx4 v[184:187], v241, s[14:15] offset:1024 sc1\n"
      "v_mfma_f32_16x16x32_bf16 v[96:99], v[188:191], a[224:227], v[96:99]\n"
      "global_load_dwordx2 v[204:205], v198, s[58:59] offset:0 sc1\n"
      "v_mfma_f32_16x16x32_bf16 v[100:103], v[188:191], a[228:231], v[100:103]\n"
      "global_load_dwordx2 v[206:207], v198, s[58:59] offset:256 sc1\n"
      "s_add_u32 s68, s20, 16384\n"
      "s_addc_u32 s69, s21, 0\n"
      "v_mfma_f32_16x16x32_bf16 v[104:107], v[188:191], a[232:235], v[104:107]\n"
      "s_ashr_i32 s54, s40, 31\n"
      "s_not_b32 s54, s54\n"
      "s_or_b32 s54, s54, 0x80000000\n"
      "v_mfma_f32_16x16x32_bf16 v[108:111], v[188:191], a[236:239], v[108:111]\n"
      "s_xor_b32 s54, s54, s40\n"
      "s_and_b32 s54, s54, 0xffff0000\n"
      "v_mov_b32 v192, s54\n"
      "v_mfma_f32_16x16x32_bf16 v[112:115], v[188:191], a[240:243], v[112:115]\n"
      "v_mul_f32 v192, 0xbfb8aa3b, v192\n"
      "v_exp_f32 v192, v192\n"
      "v_mfma_f32_16x16x32_bf16 v[116:119], v[188:191], a[244:247], v[116:119]\n"
      "v_add_f32 v192, 1.0, v192\n"
      "v_rcp_f32 v192, v192\n"
      "v_mfma_f32_16x16x32_bf16 v[120:123], v[188:191], a[248:251], v[120:123]\n"
      "v_cvt_pk_bf16_f32 v192, v192, v192\n"
      "v_and_b32 v192, 0xffff0000, v192\n"
      "v_mfma_f32_16x16x32_bf16 v[124:127], v[188:191], a[252:255], v[124:127]\n"
      "s_waitcnt vmcnt(20)\n"
      "global_load_dwordx4 v[188:191], v241, s[74:75] offset:1024 sc1\n"
      "v_readfirstlane_b32 s84, v192\n"
      "v_mfma_f32_16x16x32_bf16 v[0:3], v[128:131], a[0:3], 0\n"
      "v_mfma_f32_16x16x32_bf16 v[4:7], v[128:131], a[4:7], 0\n"
      "v_cvt_pk_bf16_f32 v104, v104, v105\n"
      "v_cvt_pk_bf16_f32 v105, v106, v107\n"
      "v_mfma_f32_16x16x32_bf16 v[8:11], v[128:131], a[8:11], 0\n"
      "ds_write_addtid_b32 v104 offset:9216\n"
      "v_cvt_pk_bf16_f32 v106, v108, v109\n"
      "v_mfma_f32_16x16x32_bf16 v[12:15], v[128:131], a[12:15], 0\n"
      "ds_write_addtid_b32 v105 offset:9472\n"
      "v_cvt_pk_bf16_f32 v107, v110, v111\n"
      "v_mfma_f32_16x16x32_bf16 v[16:19], v[128:131], a[16:19], 0\n"
      "ds_write_addtid_b32 v106 offset:9728\n"
      "v_cvt_pk_bf16_f32 v108, v112, v113\n"
      "v_mfma_f32_16x16x32_bf16 v[20:23], v[128:131], a[20:23], 0\n"
      "ds_write_addtid_b32 v107 offset:9984\n"
      "v_cvt_pk_bf16_f32 v109, v114, v115\n"
      "v_mfma_f32_16x16x32_bf16 v[24:27], v[128:131], a[24:27], 0\n"
      "ds_write_addtid_b32 v108 offset:10240\n"
      "v_cvt_pk_bf16_f32 v110, v116, v117\n"
      "v_mfma_f32_16x16x32_bf16 v[28:31], v[128:131], a[28:31], 0\n"
      "global_load_dwordx4 v[128:131], v240, s[10:11] offset:2048 sc1\n"
      "v_mfma_f32_16x16x32_bf16 v[0:3], v[132:135], a[32:35], v[0:3]\n"
      "ds_write_addtid_b32 v109 offset:10496\n"
      "v_cvt_pk_bf16_f32 v111, v118, v119\n"
      "v_mfma_f32_16x16x32_bf16 v[4:7], v[132:135], a[36:39], v[4:7]\n"
      "ds_write_addtid_b32 v110 offset:10752\n"
      "v_cvt_pk_bf16_f32 v112, v120, v121\n"
      "v_mfma_f32_16x16x32_bf16 v[8:11], v[132:135], a[40:43], v[8:11]\n"
      "ds_write_addtid_b32 v111 offset:11008\n"
      "v_cvt_pk_bf16_f32 v113, v122, v123\n"
      "v_mfma_f32_16x16x32_bf16 v[12:15], v[132:135], a[44:47], v[12:15]\n"
      "ds_write_addtid_b32 v112 offset:11264\n"
      "v_cvt_pk_bf16_f32 v114, v124, v125\n"
      "v_mfma_f32_16x16x32_bf16 v[16:19], v[132:135], a[48:51], v[16:19]\n"
      "ds_write_addtid_b32 v113 offset:11520\n"
      "v_cvt_pk_bf16_f32 v115, v126, v127\n"
      "v_mfma_f32_16x16x32_bf16 v[20:23], v[132:135], a[52:55], v[20:23]\n"
      "ds_write_addtid_b32 v114 offset:11776\n"
      "v_mfma_f32_16x16x32_bf16 v[24:27], v[132:135], a[56:59], v[24:27]\n"
      "ds_write_addtid_b32 v115 offset:12032\n"
      "v_mfma_f32_16x16x32_bf16 v[28:31], v[132:135], a[60:63], v[28:31]\n"
      "s_waitcnt vmcnt(20)\n"
      "global_load_dwordx4 v[132:135], v240, s[72:73] offset:2048 sc1\n"
      "v_mfma_f32_16x16x32_bf16 v[0:3], v[136:139], a[64:67], v[0:3]\n"
      "v_mfma_f32_16x16x32_bf16 v[4:7], v[136:139], a[68:71], v[4:7]\n"
      "s_waitcnt lgkmcnt(0)\n"
      "s_barrier\n"
      "v_mfma_f32_16x16x32_bf16 v[8:11], v[136:139], a[72:75], v[8:11]\n"
      "ds_read2st64_b32 v[84:85], v244 offset0:24 offset1:25\n"
      "v_mfma_f32_16x16x32_bf16 v[12:15], v[136:139], a[76:79], v[12:15]\n"
      "ds_read2st64_b32 v[86:87], v245 offset0:28 offset1:29\n"
      "v_mfma_f32_16x16x32_bf16 v[16:19], v[136:139], a[80:83], v[16:19]\n"
      "ds_read2st64_b32 v[88:89], v244 offset0:26 offset1:27\n"
      "v_mfma_f32_16x16x32_bf16 v[20:23], v[136:139], a[84:87], v[20:23]\n"
      "ds_read2st64_b32 v[90:91], v245 offset0:30 offset1:31\n"
      "v_mfma_f32_16x16x32_bf16 v[24:27], v[136:139], a[88:91], v[24:27]\n"
      "ds_read2st64_b32 v[92:93], v246 offset0:32 offset1:33\n"
      "v_mfma_f32_16x16x32_bf16 v[28:31], v[136:139], a[92:95], v[28:31]\n"
      "global_load_dwordx4 v[136:139], v240, s[14:15] offset:2048 sc1\n"
      "v_mfma_f32_16x16x32_bf16 v[0:3], v[140:143], a[96:99], v[0:3]\n"
      "ds_read2st64_b32 v[94:95], v246 offset0:34 offset1:35\n"
      "v_mfma_f32_16x16x32_bf16 v[4:7], v[140:143], a[100:103], v[4:7]\n"
      "ds_read2st64_b32 v[116:117], v244 offset0:36 offset1:37\n"
      "v_mfma_f32_16x16x32_bf16 v[8:11], v[140:143], a[104:107], v[8:11]\n"
      "ds_read2st64_b32 v[118:119], v245 offset0:40 offset1:41\n"
      "v_mfma_f32_16x16x32_bf16 v[12:15], v[140:143], a[108:111], v[12:15]\n"
      "ds_read2st64_b32 v[120:121], v244 offset0:38 offset1:39\n"
      "v_mfma_f32_16x16x32_bf16 v[16:19], v[140:143], a[112:115], v[16:19]\n"
      "ds_read2st64_b32 v[122:123], v245 offset0:42 offset1:43\n"
      "v_mfma_f32_16x16x32_bf16 v[20:23], v[140:143], a[116:119], v[20:23]\n"
      "s_waitcnt lgkmcnt(4)\n"
      "v_mfma_f32_16x16x32_bf16 v[64:67], v[250:253], v[84:87], v[64:67]\n"
      "v_mfma_f32_16x16x32_bf16 v[24:27], v[140:143], a[120:123], v[24:27]\n"
      "v_mfma_f32_16x16x32_bf16 v[68:71], v[250:253], v[88:91], v[68:71]\n"
      "v_mfma_f32_16x16x32_bf16 v[28:31], v[140:143], a[124:127], v[28:31]\n"
      "s_waitcnt vmcnt(20)\n"
      "global_load_dwordx4 v[140:143], v240, s[74:75] offset:2048 sc1\n"
      "v_mfma_f32_16x16x32_bf16 v[0:3], v[144:147], a[128:131], v[0:3]\n"
      "v_mfma_f32_16x16x32_bf16 v[64:67], v[252:255], v[92:95], v[64:67]\n"
      "v_mfma_f32_16x16x32_bf16 v[4:7], v[144:147], a[132:135], v[4:7]\n"
      "v_mfma_f32_16x16x32_bf16 v[68:71], v[248:251], v[92:95], v[68:71]\n"
      "v_mov_b32 v75, s37\n"
      "v_mfma_f32_16x16x32_bf16 v[8:11], v[144:147], a[136:139], v[8:11]\n"
      "ds_read2st64_b32 v[124:125], v246 offset0:44 offset1:45\n"
      "v_mfma_f32_16x16x32_bf16 v[12:15], v[144:147], a[140:143], v[12:15]\n"
      "ds_read2st64_b32 v[126:127], v246 offset0:46 offset1:47\n"
      "s_nop 0\n"
      "v_cvt_pk_bf16_f32 v72, v64, v65\n"
      "v_mfma_f32_16x16x32_bf16 v[16:19], v[144:147], a[144:147], v[16:19]\n"
      "v_cvt_pk_bf16_f32 v73, v66, v67\n"
      "v_cvt_pk_bf16_f32 v74, v68, v69\n"
      "v_mfma_f32_16x16x32_bf16 v[20:23], v[144:147], a[148:151], v[20:23]\n"
      "v_cvt_pk_bf16_f32 v247, v70, v71\n"
      "v_mfma_f32_16x16x32_bf16 v[24:27], v[144:147], a[152:155], v[24:27]\n"
      "global_store_dwordx4 v242, v[72:75], s[52:53] offset:0 sc1\n"
      "v_mfma_f32_16x16x32_bf16 v[28:31], v[144:147], a[156:159], v[28:31]\n"
      "global_load_dwordx4 v[144:147], v241, s[10:11] offset:2048 sc1\n"
      "v_mfma_f32_16x16x32_bf16 v[0:3], v[148:151], a[160:163], v[0:3]\n"
      "s_waitcnt lgkmcnt(0)\n"
      "v_mfma_f32_16x16x32_bf16 v[96:99], v[250:253], v[116:119], v[96:99]\n"
      "v_mfma_f32_16x16x32_bf16 v[4:7], v[148:151], a[164:167], v[4:7]\n"
      "v_mfma_f32_16x16x32_bf16 v[100:103], v[250:253], v[120:123], v[100:103]\n"
      "v_mfma_f32_16x16x32_bf16 v[8:11], v[148:151], a[168:171], v[8:11]\n"
      "v_mfma_f32_16x16x32_bf16 v[96:99], v[252:255], v[124:127], v[96:99]\n"
      "v_mfma_f32_16x16x32_bf16 v[12:15], v[148:151], a[172:175], v[12:15]\n"
      "v_mfma_f32_16x16x32_bf16 v[100:103], v[248:251], v[124:127], v[100:103]\n"
      "v_mov_b32 v107, s37\n"
      "v_mfma_f32_16x16x32_bf16 v[16:19], v[148:151], a[176:179], v[16:19]\n"
      "v_mov_b32 v108, v247\n"
      "v_mov_b32 v111, s37\n"
      "v_mfma_f32_16x16x32_bf16 v[20:23], v[148:151], a[180:183], v[20:23]\n"
      "v_mfma_f32_16x16x32_bf16 v[24:27], v[148:151], a[184:187], v[24:27]\n"
      "v_cvt_pk_bf16_f32 v104, v96, v97\n"
      "v_cvt_pk_bf16_f32 v105, v98, v99\n"
      "v_mfma_f32_16x16x32_bf16 v[28:31], v[148:151], a[188:191], v[28:31]\n"
      "s_waitcnt vmcnt(21)\n"
      "global_load_dwordx4 v[148:151], v241, s[72:73] offset:2048 sc1\n"
      "v_cvt_pk_bf16_f32 v106, v100, v101\n"
      "v_mfma_f32_16x16x32_bf16 v[0:3], v[152:155], a[192:195], v[0:3]\n"
      "v_cvt_pk_bf16_f32 v109, v102, v103\n"
      "v_mfma_f32_16x16x32_bf16 v[4:7], v[152:155], a[196:199], v[4:7]\n"
      "global_store_dwordx4 v242, v[104:107], s[52:53] offset:1024 sc1\n"
      "v_mfma_f32_16x16x32_bf16 v[8:11], v[152:155], a[200:203], v[8:11]\n"
      "global_store_dwordx4 v242, v[108:111], s[52:53] offset:2048 sc1\n"
      "s_add_u32 s48, s34, 3\n"
      "v_mfma_f32_16x16x32_bf16 v[12:15], v[152:155], a[204:207], v[12:15]\n"
      "s_and_b32 s48, s48, 3\n"
      "s_waitcnt vmcnt(10)\n"
      "v_bitop3_b32 v197, v205, v207, s37 bitop3:0x7e\n"
      "v_cmp_ne_u32 vcc, 0, v197\n"
      "s_nop 1\n"
      "s_cbranch_vccnz .Lp_s12_%=\n"
      ".Lp_o13_%=:\n"
      "v_mfma_f32_16x16x32_bf16 v[16:19], v[152:155], a[208:211], v[16:19]\n"
      "s_mul_i32 s48, s48, 36864\n"
      "s_lshr_b32 s43, s42, 5\n"
      "s_lshl2_add_u32 s44, s36, s33\n"
      "s_cmp_eq_u32 s43, s44\n"
      "s_cbranch_scc0 .Lp_e14_%=\n"
      "v_lshlrev_b32 v192, 16, v232\n"
      "v_lshlrev_b32 v193, 16, v236\n"
      "v_and_b32 v194, 0xffff0000, v232\n"
      "v_and_b32 v195, 0xffff0000, v236\n"
      "v_mul_f32 v192, v192, v193\n"
      "v_mul_f32 v194, v194, v195\n"
      "v_cvt_pk_bf16_f32 v192, v192, v194\n"
      "v_lshlrev_b32 v193, 16, v192\n"
      "v_and_b32 v192, 0xffff0000, v192\n"
      "v_add_f32 v196, v192, v193\n"
      "v_lshlrev_b32 v192, 16, v233\n"
      "v_lshlrev_b32 v193, 16, v237\n"
      "v_and_b32 v194, 0xffff0000, v233\n"
      "v_and_b32 v195, 0xffff0000, v237\n"
      "v_mul_f32 v192, v192, v193\n"
      "v_mul_f32 v194, v194, v195\n"
      "v_cvt_pk_bf16_f32 v192, v192, v194\n"
      "v_lshlrev_b32 v193, 16, v192\n"
      "v_and_b32 v192, 0xffff0000, v192\n"
      "v_add_f32 v192, v192, v193\n"
      "v_add_f32 v196, v196, v192\n"
      "v_lshlrev_b32 v192, 16, v234\n"
      "v_lshlrev_b32 v193, 16, v238\n"
      "v_and_b32 v194, 0xffff0000, v234\n"
      "v_and_b32 v195, 0xffff0000, v238\n"
      "v_mul_f32 v192, v192, v193\n"
      "v_mul_f32 v194, v194, v195\n"
      "v_cvt_pk_bf16_f32 v192, v192, v194\n"
      "v_lshlrev_b32 v193, 16, v192\n"
      "v_and_b32 v192, 0xffff0000, v192\n"
      "v_add_f32 v192, v192, v193\n"
      "v_add_f32 v196, v196, v192\n"
      "v_lshlrev_b32 v192, 16, v235\n"
      "v_lshlrev_b32 v193, 16, v239\n"
      "v_and_b32 v194, 0xffff0000, v235\n"
      "v_and_b32 v195, 0xffff0000, v239\n"
      "v_mul_f32 v192, v192, v193\n"
      "v_mul_f32 v194, v194, v195\n"
      "v_cvt_pk_bf16_f32 v192, v192, v194\n"
      "v_lshlrev_b32 v193, 16, v192\n"
      "v_and_b32 v192, 0xffff0000, v192\n"
      "v_add_f32 v192, v192, v193\n"
      "v_add_f32 v196, v196, v192\n"
      "s_nop 1\n"
      "v_add_f32_dpp v196, v196, v196 quad_perm:[1,0,3,2] row_mask:0xf bank_mask:0xf\n"
      "s_nop 1\n"
      "v_add_f32_dpp v196, v196, v196 quad_perm:[2,3,0,1] row_mask:0xf bank_mask:0xf\n"
      "s_nop 1\n"
      "v_add_f32_dpp v196, v196, v196 row_half_mirror row_mask:0xf bank_mask:0xf\n"
      "s_nop 1\n"
      "v_add_f32_dpp v196, v196, v196 row_mirror row_mask:0xf bank_mask:0xf\n"
      "v_mov_b32 v192, v196\n"
      "s_nop 1\n"
      "v_permlane16_swap_b32 v196, v192\n"
      "v_add_f32 v196, v196, v192\n"
      "v_mov_b32 v192, v196\n"
      "s_nop 1\n"
      "v_permlane32_swap_b32 v196, v192\n"
      "v_add_f32 v196, v196, v192\n"
      "v_cvt_pk_bf16_f32 v196, v196, v196\n"
      "v_and_b32 v196, 0xffff0000, v196\n"
      "v_mul_f32 v196, 0xbfb8aa3b, v196\n"
      "v_exp_f32 v196, v196\n"
      "v_lshrrev_b32 v192, 10, v199\n"
      "v_add_f32 v196, 1.0, v196\n"
      "v_rcp_f32 v196, v196\n"
      "v_subrev_u32 v192, s42, v192\n"
      "v_cvt_pk_bf16_f32 v196, v196, v196\n"
      "v_and_b32 v196, 0xffff0000, v196\n"
      "v_cmp_eq_u32 vcc, 0, v192\n"
      "s_nop 1\n"
      "v_cndmask_b32 v204, v204, v196, vcc\n"
      "v_cmp_eq_u32 vcc, -16, v192\n"
      "s_nop 1\n"
      "v_cndmask_b32 v206, v206, v196, vcc\n"
      ".Lp_e14_%=:\n"
      "v_mfma_f32_16x16x32_bf16 v[20:23], v[152:155], a[212:215], v[20:23]\n"
      "s_add_u32 s48, s48, 24576\n"
      "s_add_u32 s52, s60, s48\n"
      "s_addc_u32 s53, s61, 0\n"
      "v_mfma_f32_16x16x32_bf16 v[24:27], v[152:155], a[216:219], v[24:27]\n"
      "v_sub_f32 v205, 1.0, v204\n"
      "v_cvt_pk_bf16_f32 v205, v205, v205\n"
      "v_mfma_f32_16x16x32_bf16 v[28:31], v[152:155], a[220:223], v[28:31]\n"
      "global_load_dwordx4 v[152:155], v241, s[14:15] offset:2048 sc1\n"
      "v_and_b32 v205, 0xffff0000, v205\n"
      "v_mfma_f32_16x16x32_bf16 v[0:3], v[156:159], a[224:227], v[0:3]\n"
      "v_lshlrev_b32 v208, 16, v224\n"
      "v_and_b32 v209, 0xffff0000, v224\n"
      "v_mfma_f32_16x16x32_bf16 v[4:7], v[156:159], a[228:231], v[4:7]\n"
      "v_mul_f32 v208, v205, v208\n"
      "v_mul_f32 v209, v205, v209\n"
      "v_mfma_f32_16x16x32_bf16 v[8:11], v[156:159], a[232:235], v[8:11]\n"
      "v_lshlrev_b32 v210, 16, v225\n"
      "v_and_b32 v211, 0xffff0000, v225\n"
      "v_mfma_f32_16x16x32_bf16 v[12:15], v[156:159], a[236:239], v[12:15]\n"
      "v_mul_f32 v210, v205, v210\n"
      "v_mul_f32 v211, v205, v211\n"
      "v_mfma_f32_16x16x32_bf16 v[16:19], v[156:159], a[240:243], v[16:19]\n"
      "v_lshlrev_b32 v216, 16, v226\n"
      "v_and_b32 v217, 0xffff0000, v226\n"
      "v_mfma_f32_16x16x32_bf16 v[20:23], v[156:159], a[244:247], v[20:23]\n"
      "v_mul_f32 v216, v205, v216\n"
      "v_mul_f32 v217, v205, v217\n"
      "v_mfma_f32_16x16x32_bf16 v[24:27], v[156:159], a[248:251], v[24:27]\n"
      "v_lshlrev_b32 v218, 16, v227\n"
      "v_and_b32 v219, 0xffff0000, v227\n"
      "v_mfma_f32_16x16x32_bf16 v[28:31], v[156:159], a[252:255], v[28:31]\n"
      "global_load_dwordx4 v[156:159], v241, s[74:75] offset:2048 sc1\n"
      "v_mul_f32 v218, v205, v218\n"
      "v_mfma_f32_16x16x32_bf16 v[32:35], v[160:163], a[0:3], 0\n"
      "v_mul_f32 v219, v205, v219\n"
      "v_sub_f32 v207, 1.0, v206\n"
      "v_mfma_f32_16x16x32_bf16 v[36:39], v[160:163], a[4:7], 0\n"
      "v_cvt_pk_bf16_f32 v8, v8, v9\n"
      "v_cvt_pk_bf16_f32 v9, v10, v11\n"
      "v_mfma_f32_16x16x32_bf16 v[40:43], v[160:163], a[8:11], 0\n"
      "ds_write_addtid_b32 v8 offset:0\n"
      "v_cvt_pk_bf16_f32 v10, v12, v13\n"
      "v_mfma_f32_16x16x32_bf16 v[44:47], v[160:163], a[12:15], 0\n"
      "ds_write_addtid_b32 v9 offset:256\n"
      "v_cvt_pk_bf16_f32 v11, v14, v15\n"
      "v_mfma_f32_16x16x32_bf16 v[48:51], v[160:163], a[16:19], 0\n"
      "ds_write_addtid_b32 v10 offset:512\n"
      "v_cvt_pk_bf16_f32 v12, v16, v17\n"
      "v_mfma_f32_16x16x32_bf16 v[52:55], v[160:163], a[20:23], 0\n"
      "ds_write_addtid_b32 v11 offset:768\n"
      "v_cvt_pk_bf16_f32 v13, v18, v19\n"
      "v_mfma_f32_16x16x32_bf16 v[56:59], v[160:163], a[24:27], 0\n"
      "ds_write_addtid_b32 v12 offset:1024\n"
      "v_cvt_pk_bf16_f32 v14, v20, v21\n"
      "v_mfma_f32_16x16x32_bf16 v[60:63], v[160:163], a[28:31], 0\n"
      "global_load_dwordx4 v[160:163], v240, s[10:11] offset:3072 sc1\n"
      "v_cvt_pk_bf16_f32 v207, v207, v207\n"
      "v_mfma_f32_16x16x32_bf16 v[32:35], v[164:167], a[32:35], v[32:35]\n"
      "ds_write_addtid_b32 v13 offset:1280\n"
      "v_cvt_pk_bf16_f32 v15, v22, v23\n"
      "v_mfma_f32_16x16x32_bf16 v[36:39], v[164:167], a[36:39], v[36:39]\n"
      "ds_write_addtid_b32 v14 offset:1536\n"
      "v_cvt_pk_bf16_f32 v16, v24, v25\n"
      "v_mfma_f32_16x16x32_bf16 v[40:43], v[164:167], a[40:43], v[40:43]\n"
      "ds_write_addtid_b32 v15 offset:1792\n"
      "v_cvt_pk_bf16_f32 v17, v26, v27\n"
      "v_mfma_f32_16x16x32_bf16 v[44:47], v[164:167], a[44:47], v[44:47]\n"
      "ds_write_addtid_b32 v16 offset:2048\n"
      "v_cvt_pk_bf16_f32 v18, v28, v29\n"
      "v_mfma_f32_16x16x32_bf16 v[48:51], v[164:167], a[48:51], v[48:51]\n"
      "ds_write_addtid_b32 v17 offset:2304\n"
      "v_cvt_pk_bf16_f32 v19, v30, v31\n"
      "v_mfma_f32_16x16x32_bf16 v[52:55], v[164:167], a[52:55], v[52:55]\n"
      "ds_write_addtid_b32 v18 offset:2560\n"
      "v_and_b32 v207, 0xffff0000, v207\n"
      "v_mfma_f32_16x16x32_bf16 v[56:59], v[164:167], a[56:59], v[56:59]\n"
      "ds_write_addtid_b32 v19 offset:2816\n"
      "v_lshlrev_b32 v212, 16, v228\n"
      "v_mfma_f32_16x16x32_bf16 v[60:63], v[164:167], a[60:63], v[60:63]\n"
      "global_load_dwordx4 v[164:167], v240, s[72:73] offset:3072 sc1\n"
      "v_and_b32 v213, 0xffff0000, v228\n"
      "v_mfma_f32_16x16x32_bf16 v[32:35], v[168:171], a[64:67], v[32:35]\n"
      "v_mul_f32 v212, v207, v212\n"
      "v_mul_f32 v213, v207, v213\n"
      "v_mfma_f32_16x16x32_bf16 v[36:39], v[168:171], a[68:71], v[36:39]\n"
      "s_waitcnt lgkmcnt(0)\n"
      "s_barrier\n"
      "v_mfma_f32_16x16x32_bf16 v[40:43], v[168:171], a[72:75], v[40:43]\n"
      "ds_read2st64_b32 v[20:21], v244 offset0:0 offset1:1\n"
      "v_lshlrev_b32 v214, 16, v229\n"
      "v_mfma_f32_16x16x32_bf16 v[44:47], v[168:171], a[76:79], v[44:47]\n"
      "ds_read2st64_b32 v[22:23], v245 offset0:4 offset1:5\n"
      "v_and_b32 v215, 0xffff0000, v229\n"
      "v_mfma_f32_16x16x32_bf16 v[48:51], v[168:171], a[80:83], v[48:51]\n"
      "ds_read2st64_b32 v[24:25], v244 offset0:2 offset1:3\n"
      "v_mul_f32 v214, v207, v214\n"
      "v_mfma_f32_16x16x32_bf16 v[52:55], v[168:171], a[84:87], v[52:55]\n"
      "ds_read2st64_b32 v[26:27], v245 offset0:6 offset1:7\n"
      "v_mul_f32 v215, v207, v215\n"
      "v_mfma_f32_16x16x32_bf16 v[56:59], v[168:171], a[88:91], v[56:59]\n"
      "ds_read2st64_b32 v[28:29], v246 offset0:8 offset1:9\n"
      "v_lshlrev_b32 v220, 16, v230\n"
      "v_mfma_f32_16x16x32_bf16 v[60:63], v[168:171], a[92:95], v[60:63]\n"
      "global_load_dwordx4 v[168:171], v240, s[14:15] offset:3072 sc1\n"
      "v_and_b32 v221, 0xffff0000, v230\n"
      "v_mfma_f32_16x16x32_bf16 v[32:35], v[172:175], a[96:99], v[32:35]\n"
      "ds_read2st64_b32 v[30:31], v246 offset0:10 offset1:11\n"
      "v_mul_f32 v220, v207, v220\n"
      "v_mfma_f32_16x16x32_bf16 v[36:39], v[172:175], a[100:103], v[36:39]\n"
      "v_mul_f32 v221, v207, v221\n"
      "v_lshlrev_b32 v222, 16, v231\n"
      "v_mfma_f32_16x16x32_bf16 v[40:43], v[172:175], a[104:107], v[40:43]\n"
      "v_and_b32 v223, 0xffff0000, v231\n"
      "v_mul_f32 v222, v207, v222\n"
      "v_mfma_f32_16x16x32_bf16 v[44:47], v[172:175], a[108:111], v[44:47]\n"
      "v_mul_f32 v223, v207, v223\n"
      "v_mfma_f32_16x16x32_bf16 v[48:51], v[172:175], a[112:115], v[48:51]\n"
      "v_mfma_f32_16x16x32_bf16 v[52:55], v[172:175], a[116:119], v[52:55]\n"
      "s_waitcnt lgkmcnt(0)\n"
      "v_mfma_f32_16x16x32_bf16 v[0:3], v[250:253], v[20:23], v[0:3]\n"
      "v_mfma_f32_16x16x32_bf16 v[56:59], v[172:175], a[120:123], v[56:59]\n"
      "v_mfma_f32_16x16x32_bf16 v[4:7], v[250:253], v[24:27], v[4:7]\n"
      "v_mfma_f32_16x16x32_bf16 v[60:63], v[172:175], a[124:127], v[60:63]\n"
      "global_load_dwordx4 v[172:175], v240, s[74:75] offset:3072 sc1\n"
      "v_mfma_f32_16x16x32_bf16 v[40:43], v[176:179], a[136:139], v[40:43]\n"
      "v_mfma_f32_16x16x32_bf16 v[0:3], v[252:255], v[28:31], v[0:3]\n"
      "v_mfma_f32_16x16x32_bf16 v[44:47], v[176:179], a[140:143], v[44:47]\n"
      "v_mfma_f32_16x16x32_bf16 v[4:7], v[248:251], v[28:31], v[4:7]\n"
      "v_mov_b32 v11, s37\n"
      "v_mfma_f32_16x16x32_bf16 v[48:51], v[176:179], a[144:147], v[48:51]\n"
      "v_mfma_f32_16x16x32_bf16 v[52:55], v[176:179], a[148:151], v[52:55]\n"
      "s_nop 2\n"
      "v_cvt_pk_bf16_f32 v8, v0, v1\n"
      "v_mfma_f32_16x16x32_bf16 v[56:59], v[176:179], a[152:155], v[56:59]\n"
      "v_cvt_pk_bf16_f32 v9, v2, v3\n"
      "v_cvt_pk_bf16_f32 v10, v4, v5\n"
      "v_mfma_f32_16x16x32_bf16 v[60:63], v[176:179], a[156:159], v[60:63]\n"
      "v_cvt_pk_bf16_f32 v247, v6, v7\n"
      "v_mfma_f32_16x16x32_bf16 v[40:43], v[180:183], a[168:171], v[40:43]\n"
      "global_store_dwordx4 v242, v[8:11], s[52:53] offset:0 sc1\n"
      "s_lshl2_add_u32 s54, s36, s33\n"
      "v_mfma_f32_16x16x32_bf16 v[44:47], v[180:183], a[172:175], v[44:47]\n"
      "s_lshl_b32 s55, s54, 15\n"
      "s_lshl2_add_u32 s56, s35, s34\n"
      "s_lshl_b32 s56, s56, 6\n"
      "v_mfma_f32_16x16x32_bf16 v[48:51], v[180:183], a[176:179], v[48:51]\n"
      "s_add_u32 s55, s55, s56\n"
      "v_and_b32 v8, 15, v243\n"
      "v_and_b32 v7, 48, v243\n"
      "v_mfma_f32_16x16x32_bf16 v[52:55], v[180:183], a[180:183], v[52:55]\n"
      "v_lshl_or_b32 v7, v8, 10, v7\n"
      "v_add_u32 v7, s55, v7\n"
      "v_mfma_f32_16x16x32_bf16 v[56:59], v[180:183], a[184:187], v[56:59]\n"
      "v_mfma_f32_16x16x32_bf16 v[60:63], v[180:183], a[188:191], v[60:63]\n"
      "s_waitcnt vmcnt(16)\n"
      "v_mfma_f32_16x16x32_bf16 v[40:43], v[184:187], a[200:203], v[40:43]\n"
      "v_mfma_f32_16x16x32_bf16 v[44:47], v[184:187], a[204:207], v[44:47]\n"
      "v_mfma_f32_16x16x32_bf16 v[48:51], v[184:187], a[208:211], v[48:51]\n"
      "v_mfma_f32_16x16x32_bf16 v[52:55], v[184:187], a[212:215], v[52:55]\n"
      "v_mfma_f32_16x16x32_bf16 v[56:59], v[184:187], a[216:219], v[56:59]\n"
      "v_mfma_f32_16x16x32_bf16 v[60:63], v[184:187], a[220:223], v[60:63]\n"
      "v_mfma_f32_16x16x32_bf16 v[40:43], v[188:191], a[232:235], v[40:43]\n"
      "v_mfma_f32_16x16x32_bf16 v[44:47], v[188:191], a[236:239], v[44:47]\n"
      "v_mfma_f32_16x16x32_bf16 v[48:51], v[188:191], a[240:243], v[48:51]\n"
      "v_mfma_f32_16x16x32_bf16 v[52:55], v[188:191], a[244:247], v[52:55]\n"
      "v_mfma_f32_16x16x32_bf16 v[56:59], v[188:191], a[248:251], v[56:59]\n"
      "v_mfma_f32_16x16x32_bf16 v[60:63], v[188:191], a[252:255], v[60:63]\n"
      "v_mfma_f32_16x16x32_bf16 v[32:35], v[176:179], a[128:131], v[32:35]\n"
      "v_mfma_f32_16x16x32_bf16 v[36:39], v[176:179], a[132:135], v[36:39]\n"
      "global_load_dwordx4 v[176:179], v241, s[10:11] offset:3072 sc1\n"
      "v_cvt_pk_bf16_f32 v40, v40, v41\n"
      "v_cvt_pk_bf16_f32 v41, v42, v43\n"
      "v_mfma_f32_16x16x32_bf16 v[32:35], v[180:183], a[160:163], v[32:35]\n"
      "ds_write_addtid_b32 v40 offset:3072\n"
      "v_cvt_pk_bf16_f32 v42, v44, v45\n"
      "v_mfma_f32_16x16x32_bf16 v[36:39], v[180:183], a[164:167], v[36:39]\n"
      "global_load_dwordx4 v[180:183], v241, s[72:73] offset:3072 sc1\n"
      "ds_write_addtid_b32 v41 offset:3328\n"
      "v_cvt_pk_bf16_f32 v43, v46, v47\n"
      "v_mfma_f32_16x16x32_bf16 v[32:35], v[184:187], a[192:195], v[32:35]\n"
      "ds_write_addtid_b32 v42 offset:3584\n"
      "v_cvt_pk_bf16_f32 v44, v48, v49\n"
      "v_mfma_f32_16x16x32_bf16 v[36:39], v[184:187], a[196:199], v[36:39]\n"
      "global_load_dwordx4 v[184:187], v241, s[14:15] offset:3072 sc1\n"
      "ds_write_addtid_b32 v43 offset:3840\n"
      "v_cvt_pk_bf16_f32 v45, v50, v51\n"
      "v_mfma_f32_16x16x32_bf16 v[32:35], v[188:191], a[224:227], v[32:35]\n"
      "ds_write_addtid_b32 v44 offset:4096\n"
      "v_cvt_pk_bf16_f32 v46, v52, v53\n"
      "v_mfma_f32_16x16x32_bf16 v[36:39], v[188:191], a[228:231], v[36:39]\n"
      "s_waitcnt vmcnt(17)\n"
      "global_load_dwordx4 v[188:191], v241, s[74:75] offset:3072 sc1\n"
      "ds_write_addtid_b32 v45 offset:4352\n"
      "v_cvt_pk_bf16_f32 v47, v54, v55\n"
      "v_mfma_f32_16x16x32_bf16 v[72:75], v[128:131], a[8:11], 0\n"
      "ds_write_addtid_b32 v46 offset:4608\n"
      "v_cvt_pk_bf16_f32 v48, v56, v57\n"
      "v_mfma_f32_16x16x32_bf16 v[76:79], v[128:131], a[12:15], 0\n"
      "ds_write_addtid_b32 v47 offset:4864\n"
      "v_cvt_pk_bf16_f32 v49, v58, v59\n"
      "v_mfma_f32_16x16x32_bf16 v[80:83], v[128:131], a[16:19], 0\n"
      "ds_write_addtid_b32 v48 offset:5120\n"
      "v_cvt_pk_bf16_f32 v50, v60, v61\n"
      "v_mfma_f32_16x16x32_bf16 v[84:87], v[128:131], a[20:23], 0\n"
      "ds_write_addtid_b32 v49 offset:5376\n"
      "v_cvt_pk_bf16_f32 v51, v62, v63\n"
      "v_mfma_f32_16x16x32_bf16 v[88:91], v[128:131], a[24:27], 0\n"
      "ds_write_addtid_b32 v50 offset:5632\n"
      "ds_write_addtid_b32 v51 offset:5888\n"
      "v_mfma_f32_16x16x32_bf16 v[92:95], v[128:131], a[28:31], 0\n"
      "v_mfma_f32_16x16x32_bf16 v[72:75], v[132:135], a[40:43], v[72:75]\n"
      "v_mfma_f32_16x16x32_bf16 v[76:79], v[132:135], a[44:47], v[76:79]\n"
      "s_waitcnt lgkmcnt(0)\n"
      "s_barrier\n"
      "v_mfma_f32_16x16x32_bf16 v[80:83], v[132:135], a[48:51], v[80:83]\n"
      "ds_read2st64_b32 v[52:53], v244 offset0:12 offset1:13\n"
      "ds_read2st64_b32 v[54:55], v245 offset0:16 offset1:17\n"
      "v_mfma_f32_16x16x32_bf16 v[84:87], v[132:135], a[52:55], v[84:87]\n"
      "ds_read2st64_b32 v[56:57], v244 offset0:14 offset1:15\n"
      "ds_read2st64_b32 v[58:59], v245 offset0:18 offset1:19\n"
      "v_mfma_f32_16x16x32_bf16 v[88:91], v[132:135], a[56:59], v[88:91]\n"
      "ds_read2st64_b32 v[60:61], v246 offset0:20 offset1:21\n"
      "ds_read2st64_b32 v[62:63], v246 offset0:22 offset1:23\n"
      "v_mfma_f32_16x16x32_bf16 v[92:95], v[132:135], a[60:63], v[92:95]\n"
      "s_waitcnt vmcnt(16)\n"
      "v_mfma_f32_16x16x32_bf16 v[72:75], v[136:139], a[72:75], v[72:75]\n"
      "v_mfma_f32_16x16x32_bf16 v[76:79], v[136:139], a[76:79], v[76:79]\n"
      "v_mfma_f32_16x16x32_bf16 v[80:83], v[136:139], a[80:83], v[80:83]\n"
      "v_mfma_f32_16x16x32_bf16 v[84:87], v[136:139], a[84:87], v[84:87]\n"
      "s_waitcnt lgkmcnt(0)\n"
      "v_mfma_f32_16x16x32_bf16 v[32:35], v[250:253], v[52:55], v[32:35]\n"
      "v_mfma_f32_16x16x32_bf16 v[36:39], v[250:253], v[56:59], v[36:39]\n"
      "v_mfma_f32_16x16x32_bf16 v[88:91], v[136:139], a[88:91], v[88:91]\n"
      "v_mfma_f32_16x16x32_bf16 v[32:35], v[252:255], v[60:63], v[32:35]\n"
      "v_mfma_f32_16x16x32_bf16 v[36:39], v[248:251], v[60:63], v[36:39]\n"
      "v_add_u32 v4, 0, v242\n"
      "v_mfma_f32_16x16x32_bf16 v[92:95], v[136:139], a[92:95], v[92:95]\n"
      "global_load_dwordx4 v[12:15], v4, s[62:63] offset:0 sc1\n"
      "global_load_dwordx4 v[16:19], v4, s[62:63] offset:1024 sc1\n"
      "v_mov_b32 v43, s37\n"
      "v_mfma_f32_16x16x32_bf16 v[72:75], v[140:143], a[104:107], v[72:75]\n"
      "global_load_dwordx4 v[20:23], v4, s[62:63] offset:2048 sc1\n"
      "v_mov_b32 v44, v247\n"
      "v_mov_b32 v47, s37\n"
      "v_mfma_f32_16x16x32_bf16 v[76:79], v[140:143], a[108:111], v[76:79]\n"
      "v_mfma_f32_16x16x32_bf16 v[80:83], v[140:143], a[112:115], v[80:83]\n"
      "v_cvt_pk_bf16_f32 v40, v32, v33\n"
      "v_cvt_pk_bf16_f32 v41, v34, v35\n"
      "v_cvt_pk_bf16_f32 v42, v36, v37\n"
      "v_mfma_f32_16x16x32_bf16 v[84:87], v[140:143], a[116:119], v[84:87]\n"
      "v_cvt_pk_bf16_f32 v45, v38, v39\n"
      "v_mfma_f32_16x16x32_bf16 v[88:91], v[140:143], a[120:123], v[88:91]\n"
      "global_store_dwordx4 v242, v[40:43], s[52:53] offset:1024 sc1\n"
      "global_store_dwordx4 v242, v[44:47], s[52:53] offset:2048 sc1\n"
      "v_mfma_f32_16x16x32_bf16 v[92:95], v[140:143], a[124:127], v[92:95]\n"
      "s_waitcnt vmcnt(18)\n"
      "v_mfma_f32_16x16x32_bf16 v[72:75], v[144:147], a[136:139], v[72:75]\n"
      "v_mfma_f32_16x16x32_bf16 v[76:79], v[144:147], a[140:143], v[76:79]\n"
      "v_mfma_f32_16x16x32_bf16 v[80:83], v[144:147], a[144:147], v[80:83]\n"
      "v_mfma_f32_16x16x32_bf16 v[84:87], v[144:147], a[148:151], v[84:87]\n"
      "v_mfma_f32_16x16x32_bf16 v[88:91], v[144:147], a[152:155], v[88:91]\n"
      "v_mfma_f32_16x16x32_bf16 v[92:95], v[144:147], a[156:159], v[92:95]\n"
      "v_mfma_f32_16x16x32_bf16 v[72:75], v[148:151], a[168:171], v[72:75]\n"
      "v_mfma_f32_16x16x32_bf16 v[76:79], v[148:151], a[172:175], v[76:79]\n"
      "v_mfma_f32_16x16x32_bf16 v[80:83], v[148:151], a[176:179], v[80:83]\n"
      "v_mfma_f32_16x16x32_bf16 v[84:87], v[148:151], a[180:183], v[84:87]\n"
      "v_mfma_f32_16x16x32_bf16 v[88:91], v[148:151], a[184:187], v[88:91]\n"
      "v_mfma_f32_16x16x32_bf16 v[92:95], v[148:151], a[188:191], v[92:95]\n"
      "s_waitcnt vmcnt(14)\n"
      "v_mfma_f32_16x16x32_bf16 v[72:75], v[152:155], a[200:203], v[72:75]\n"
      "v_mfma_f32_16x16x32_bf16 v[76:79], v[152:155], a[204:207], v[76:79]\n"
      "v_mfma_f32_16x16x32_bf16 v[80:83], v[152:155], a[208:211], v[80:83]\n"
      "v_mfma_f32_16x16x32_bf16 v[84:87], v[152:155], a[212:215], v[84:87]\n"
      "v_mfma_f32_16x16x32_bf16 v[88:91], v[152:155], a[216:219], v[88:91]\n"
      "v_mfma_f32_16x16x32_bf16 v[92:95], v[152:155], a[220:223], v[92:95]\n"
      "v_mfma_f32_16x16x32_bf16 v[72:75], v[156:159], a[232:235], v[72:75]\n"
      "v_mfma_f32_16x16x32_bf16 v[76:79], v[156:159], a[236:239], v[76:79]\n"
      "v_mfma_f32_16x16x32_bf16 v[80:83], v[156:159], a[240:243], v[80:83]\n"
      "v_mfma_f32_16x16x32_bf16 v[84:87], v[156:159], a[244:247], v[84:87]\n"
      "v_mfma_f32_16x16x32_bf16 v[88:91], v[156:159], a[248:251], v[88:91]\n"
      "v_mfma_f32_16x16x32_bf16 v[92:95], v[156:159], a[252:255], v[92:95]\n"
      "s_waitcnt vmcnt(12)\n"
      "v_mfma_f32_16x16x32_bf16 v[104:107], v[160:163], a[8:11], 0\n"
      "v_mfma_f32_16x16x32_bf16 v[108:111], v[160:163], a[12:15], 0\n"
      "v_cvt_pk_bf16_f32 v72, v72, v73\n"
      "v_cvt_pk_bf16_f32 v73, v74, v75\n"
      "v_mfma_f32_16x16x32_bf16 v[112:115], v[160:163], a[16:19], 0\n"
      "ds_write_addtid_b32 v72 offset:6144\n"
      "v_cvt_pk_bf16_f32 v74, v76, v77\n"
      "v_mfma_f32_16x16x32_bf16 v[116:119], v[160:163], a[20:23], 0\n"
      "ds_write_addtid_b32 v73 offset:6400\n"
      "v_cvt_pk_bf16_f32 v75, v78, v79\n"
      "v_mfma_f32_16x16x32_bf16 v[120:123], v[160:163], a[24:27], 0\n"
      "ds_write_addtid_b32 v74 offset:6656\n"
      "v_cvt_pk_bf16_f32 v76, v80, v81\n"
      "v_mfma_f32_16x16x32_bf16 v[124:127], v[160:163], a[28:31], 0\n"
      "ds_write_addtid_b32 v75 offset:6912\n"
      "v_cvt_pk_bf16_f32 v77, v82, v83\n"
      "v_mfma_f32_16x16x32_bf16 v[104:107], v[164:167], a[40:43], v[104:107]\n"
      "ds_write_addtid_b32 v76 offset:7168\n"
      "v_cvt_pk_bf16_f32 v78, v84, v85\n"
      "v_mfma_f32_16x16x32_bf16 v[108:111], v[164:167], a[44:47], v[108:111]\n"
      "ds_write_addtid_b32 v77 offset:7424\n"
      "v_cvt_pk_bf16_f32 v79, v86, v87\n"
      "v_mfma_f32_16x16x32_bf16 v[112:115], v[164:167], a[48:51], v[112:115]\n"
      "ds_write_addtid_b32 v78 offset:7680\n"
      "v_cvt_pk_bf16_f32 v80, v88, v89\n"
      "v_mfma_f32_16x16x32_bf16 v[116:119], v[164:167], a[52:55], v[116:119]\n"
      "ds_write_addtid_b32 v79 offset:7936\n"
      "v_cvt_pk_bf16_f32 v81, v90, v91\n"
      "v_mfma_f32_16x16x32_bf16 v[120:123], v[164:167], a[56:59], v[120:123]\n"
      "ds_write_addtid_b32 v80 offset:8192\n"
      "v_cvt_pk_bf16_f32 v82, v92, v93\n"
      "v_mfma_f32_16x16x32_bf16 v[124:127], v[164:167], a[60:63], v[124:127]\n"
      "s_waitcnt vmcnt(10)\n"
      "ds_write_addtid_b32 v81 offset:8448\n"
      "v_cvt_pk_bf16_f32 v83, v94, v95\n"
      "v_mfma_f32_16x16x32_bf16 v[104:107], v[168:171], a[72:75], v[104:107]\n"
      "ds_write_addtid_b32 v82 offset:8704\n"
      "v_mfma_f32_16x16x32_bf16 v[108:111], v[168:171], a[76:79], v[108:111]\n"
      "ds_write_addtid_b32 v83 offset:8960\n"
      "v_mfma_f32_16x16x32_bf16 v[112:115], v[168:171], a[80:83], v[112:115]\n"
      "s_waitcnt vmcnt(2)\n"
      "v_bitop3_b32 v22, v15, v19, s37 bitop3:0x7e\n"
      "v_bitop3_b32 v22, v22, v23, s37 bitop3:0xf6\n"
      "v_cmp_ne_u32 vcc, 0, v22\n"
      "s_nop 1\n"
      "s_cbranch_vccnz .Lp_s16_%=\n"
      ".Lp_o17_%=:\n"
      "v_mfma_f32_16x16x32_bf16 v[116:119], v[168:171], a[84:87], v[116:119]\n"
      "v_mov_b32 v15, v20\n"
      "v_mov_b32 v19, v21\n"
      "v_mfma_f32_16x16x32_bf16 v[120:123], v[168:171], a[88:91], v[120:123]\n"
      "s_waitcnt lgkmcnt(0)\n"
      "s_barrier\n"
      "v_mfma_f32_16x16x32_bf16 v[124:127], v[168:171], a[92:95], v[124:127]\n"
      "ds_read2st64_b32 v[84:85], v244 offset0:24 offset1:25\n"
      "v_mfma_f32_16x16x32_bf16 v[104:107], v[172:175], a[104:107], v[104:107]\n"
      "ds_read2st64_b32 v[86:87], v245 offset0:28 offset1:29\n"
      "v_add_u32 v5, 12288, v242\n"
      "v_mfma_f32_16x16x32_bf16 v[108:111], v[172:175], a[108:111], v[108:111]\n"
      "global_load_dwordx4 v[44:47], v5, s[62:63] offset:0 sc1\n"
      "v_mfma_f32_16x16x32_bf16 v[112:115], v[172:175], a[112:115], v[112:115]\n"
      "global_load_dwordx4 v[48:51], v5, s[62:63] offset:1024 sc1\n"
      "v_mfma_f32_16x16x32_bf16 v[116:119], v[172:175], a[116:119], v[116:119]\n"
      "global_load_dwordx4 v[52:55], v5, s[62:63] offset:2048 sc1\n"
      "v_mfma_f32_16x16x32_bf16 v[120:123], v[172:175], a[120:123], v[120:123]\n"
      "ds_read2st64_b32 v[88:89], v244 offset0:26 offset1:27\n"
      "v_mfma_f32_16x16x32_bf16 v[124:127], v[172:175], a[124:127], v[124:127]\n"
      "ds_read2st64_b32 v[90:91], v245 offset0:30 offset1:31\n"
      "v_mfma_f32_16x16x32_bf16 v[104:107], v[176:179], a[136:139], v[104:107]\n"
      "ds_read2st64_b32 v[92:93], v246 offset0:32 offset1:33\n"
      "v_mfma_f32_16x16x32_bf16 v[108:111], v[176:179], a[140:143], v[108:111]\n"
      "ds_read2st64_b32 v[94:95], v246 offset0:34 offset1:35\n"
      "v_mfma_f32_16x16x32_bf16 v[112:115], v[176:179], a[144:147], v[112:115]\n"
      "v_mfma_f32_16x16x32_bf16 v[64:67], v[252:255], v[12:15], 0\n"
      "v_mfma_f32_16x16x32_bf16 v[116:119], v[176:179], a[148:151], v[116:119]\n"
      "v_mfma_f32_16x16x32_bf16 v[68:71], v[248:251], v[12:15], 0\n"
      "v_mfma_f32_16x16x32_bf16 v[120:123], v[176:179], a[152:155], v[120:123]\n"
      "v_mfma_f32_16x16x32_bf16 v[96:99], v[252:255], v[16:19], 0\n"
      "v_mfma_f32_16x16x32_bf16 v[124:127], v[176:179], a[156:159], v[124:127]\n"
      "v_mfma_f32_16x16x32_bf16 v[100:103], v[248:251], v[16:19], 0\n"
      "v_mfma_f32_16x16x32_bf16 v[104:107], v[180:183], a[168:171], v[104:107]\n"
      "s_waitcnt lgkmcnt(0)\n"
      "v_mfma_f32_16x16x32_bf16 v[64:67], v[250:253], v[84:87], v[64:67]\n"
      "v_mfma_f32_16x16x32_bf16 v[108:111], v[180:183], a[172:175], v[108:111]\n"
      "v_mfma_f32_16x16x32_bf16 v[68:71], v[250:253], v[88:91], v[68:71]\n"
      "v_mfma_f32_16x16x32_bf16 v[112:115], v[180:183], a[176:179], v[112:115]\n"
      "v_mfma_f32_16x16x32_bf16 v[64:67], v[252:255], v[92:95], v[64:67]\n"
      "v_mfma_f32_16x16x32_bf16 v[116:119], v[180:183], a[180:183], v[116:119]\n"
      "v_mfma_f32_16x16x32_bf16 v[68:71], v[248:251], v[92:95], v[68:71]\n"
      "v_mfma_f32_16x16x32_bf16 v[120:123], v[180:183], a[184:187], v[120:123]\n"
      "v_mfma_f32_16x16x32_bf16 v[124:127], v[180:183], a[188:191], v[124:127]\n"
      "v_mfma_f32_16x16x32_bf16 v[104:107], v[184:187], a[200:203], v[104:107]\n"
      "v_mfma_f32_16x16x32_bf16 v[108:111], v[184:187], a[204:207], v[108:111]\n"
      "v_mfma_f32_16x16x32_bf16 v[112:115], v[184:187], a[208:211], v[112:115]\n"
      "v_mfma_f32_16x16x32_bf16 v[116:119], v[184:187], a[212:215], v[116:119]\n"
      "v_mfma_f32_16x16x32_bf16 v[120:123], v[184:187], a[216:219], v[120:123]\n"
      "v_mfma_f32_16x16x32_bf16 v[124:127], v[184:187], a[220:223], v[124:127]\n"
      "v_mfma_f32_16x16x32_bf16 v[104:107], v[188:191], a[232:235], v[104:107]\n"
      "v_mfma_f32_16x16x32_bf16 v[108:111], v[188:191], a[236:239], v[108:111]\n"
      "s_lshr_b32 s43, s42, 7\n"
      "s_cmp_eq_u32 s43, s36\n"
      "s_cbranch_scc0 .Lp_e18_%=\n"
      "s_lshl2_add_u32 s44, s35, s34\n"
      "s_lshl2_add_u32 s43, s44, s33\n"
      "s_lshl_b32 s43, s43, 12\n"
      "s_add_u32 s43, s43, 524288\n"
      "v_add_u32 v60, s43, v242\n"
      "global_load_dwordx4 v[24:27], v60, s[58:59] offset:0 sc1\n"
      "global_load_dwordx4 v[28:31], v60, s[58:59] offset:1024 sc1\n"
      "global_load_dwordx4 v[32:35], v60, s[58:59] offset:2048 sc1\n"
      "global_load_dwordx4 v[36:39], v60, s[58:59] offset:3072 sc1\n"
      "v_and_b32 v58, 15, v243\n"
      "v_lshrrev_b32 v59, 5, v243\n"
      "v_lshl_or_b32 v58, v59, 4, v58\n"
      "s_lshl4_add_u32 s44, s42, s44\n"
      "s_lshl_b32 s44, s44, 6\n"
      "v_lshl_add_u32 v59, v58, 1, s44\n"
      "global_load_ushort v57, v59, s[16:17]\n"
      "s_mov_b32 s44, 131072\n"
      "v_lshl_add_u32 v58, v58, 2, s44\n"
      "s_lshl_b32 s43, s33, 13\n"
      "v_add_u32 v59, s43, v58\n"
      ".Lp_e18_%=:\n"
      "v_mfma_f32_16x16x32_bf16 v[112:115], v[188:191], a[240:243], v[112:115]\n"
      "v_mfma_f32_16x16x32_bf16 v[116:119], v[188:191], a[244:247], v[116:119]\n"
      "v_mfma_f32_16x16x32_bf16 v[120:123], v[188:191], a[248:251], v[120:123]\n"
      "v_mfma_f32_16x16x32_bf16 v[124:127], v[188:191], a[252:255], v[124:127]\n"
      "v_mfma_f32_16x16x32_bf16 v[64:67], v[128:131], a[0:3], v[64:67]\n"
      "v_mfma_f32_16x16x32_bf16 v[68:71], v[128:131], a[4:7], v[68:71]\n"
      "v_cvt_pk_bf16_f32 v104, v104, v105\n"
      "v_cvt_pk_bf16_f32 v105, v106, v107\n"
      "v_mfma_f32_16x16x32_bf16 v[64:67], v[132:135], a[32:35], v[64:67]\n"
      "ds_write_addtid_b32 v104 offset:9216\n"
      "v_cvt_pk_bf16_f32 v106, v108, v109\n"
      "v_mfma_f32_16x16x32_bf16 v[68:71], v[132:135], a[36:39], v[68:71]\n"
      "ds_write_addtid_b32 v105 offset:9472\n"
      "v_cvt_pk_bf16_f32 v107, v110, v111\n"
      "v_mfma_f32_16x16x32_bf16 v[64:67], v[136:139], a[64:67], v[64:67]\n"
      "ds_write_addtid_b32 v106 offset:9728\n"
      "v_cvt_pk_bf16_f32 v108, v112, v113\n"
      "v_mfma_f32_16x16x32_bf16 v[68:71], v[136:139], a[68:71], v[68:71]\n"
      "ds_write_addtid_b32 v107 offset:9984\n"
      "v_cvt_pk_bf16_f32 v109, v114, v115\n"
      "v_mfma_f32_16x16x32_bf16 v[64:67], v[140:143], a[96:99], v[64:67]\n"
      "ds_write_addtid_b32 v108 offset:10240\n"
      "v_cvt_pk_bf16_f32 v110, v116, v117\n"
      "v_mfma_f32_16x16x32_bf16 v[68:71], v[140:143], a[100:103], v[68:71]\n"
      "ds_write_addtid_b32 v109 offset:10496\n"
      "v_cvt_pk_bf16_f32 v111, v118, v119\n"
      "v_mfma_f32_16x16x32_bf16 v[64:67], v[144:147], a[128:131], v[64:67]\n"
      "ds_write_addtid_b32 v110 offset:10752\n"
      "v_cvt_pk_bf16_f32 v112, v120, v121\n"
      "v_mfma_f32_16x16x32_bf16 v[68:71], v[144:147], a[132:135], v[68:71]\n"
      "ds_write_addtid_b32 v111 offset:11008\n"
      "v_cvt_pk_bf16_f32 v113, v122, v123\n"
      "v_mfma_f32_16x16x32_bf16 v[64:67], v[148:151], a[160:163], v[64:67]\n"
      "ds_write_addtid_b32 v112 offset:11264\n"
      "v_add_u32 v6, 24576, v242\n"
      "v_cvt_pk_bf16_f32 v114, v124, v125\n"
      "v_mfma_f32_16x16x32_bf16 v[68:71], v[148:151], a[164:167], v[68:71]\n"
      "global_load_dwordx4 v[224:227], v6, s[62:63] offset:0 sc1\n"
      "global_load_dwordx4 v[228:231], v6, s[62:63] offset:1024 sc1\n"
      "v_mfma_f32_16x16x32_bf16 v[64:67], v[152:155], a[192:195], v[64:67]\n"
      "global_load_dwordx4 v[232:235], v6, s[62:63] offset:2048 sc1\n"
      "ds_write_addtid_b32 v113 offset:11520\n"
      "v_cvt_pk_bf16_f32 v115, v126, v127\n"
      "v_mfma_f32_16x16x32_bf16 v[68:71], v[152:155], a[196:199], v[68:71]\n"
      "ds_write_addtid_b32 v114 offset:11776\n"
      "ds_write_addtid_b32 v115 offset:12032\n"
      "v_mfma_f32_16x16x32_bf16 v[64:67], v[156:159], a[224:227], v[64:67]\n"
      "s_waitcnt vmcnt(3)\n"
      "v_bitop3_b32 v54, v47, v51, s37 bitop3:0x7e\n"
      "v_bitop3_b32 v54, v54, v55, s37 bitop3:0xf6\n"
      "v_cmp_ne_u32 vcc, 0, v54\n"
      "s_nop 1\n"
      "s_cbranch_vccnz .Lp_s20_%=\n"
      ".Lp_o21_%=:\n"
      "v_mfma_f32_16x16x32_bf16 v[68:71], v[156:159], a[228:231], v[68:71]\n"
      "v_mov_b32 v47, v52\n"
      "v_mov_b32 v51, v53\n"
      "v_mfma_f32_16x16x32_bf16 v[96:99], v[160:163], a[0:3], v[96:99]\n"
      "s_lshr_b32 s43, s42, 7\n"
      "s_cmp_eq_u32 s43, s36\n"
      "s_cbranch_scc0 .Lp_e22_%=\n"
      "s_waitcnt vmcnt(3)\n"
      "v_bitop3_b32 v61, v25, v27, s37 bitop3:0x7e\n"
      "v_bitop3_b32 v61, v61, v29, s37 bitop3:0xf6\n"
      "v_bitop3_b32 v61, v61, v31, s37 bitop3:0xf6\n"
      "v_bitop3_b32 v61, v61, v33, s37 bitop3:0xf6\n"
      "v_bitop3_b32 v61, v61, v35, s37 bitop3:0xf6\n"
      "v_bitop3_b32 v61, v61, v37, s37 bitop3:0xf6\n"
      "v_bitop3_b32 v61, v61, v39, s37 bitop3:0xf6\n"
      "v_cmp_ne_u32 vcc, 0, v61\n"
      "s_nop 1\n"
      "s_cbranch_vccnz .Lp_s24_%=\n"
      ".Lp_o25_%=:\n"
      "v_add_f32 v24, v24, v28\n"
      "v_add_f32 v26, v26, v30\n"
      "v_add_f32 v32, v32, v36\n"
      "v_add_f32 v34, v34, v38\n"
      "v_add_f32 v24, v24, v32\n"
      "v_add_f32 v26, v26, v34\n"
      "s_nop 1\n"
      "v_permlane32_swap_b32 v24, v26\n"
      "v_add_f32 v24, v24, v26\n"
      "v_mov_b32 v26, v24\n"
      "s_nop 1\n"
      "v_permlane16_swap_b32 v24, v26\n"
      "v_add_f32 v24, v24, v26\n"
      "ds_write_b32 v59, v24\n"
      "s_waitcnt lgkmcnt(0)\n"
      ".Lp_e22_%=:\n"
      "v_mfma_f32_16x16x32_bf16 v[100:103], v[160:163], a[4:7], v[100:103]\n"
      "s_waitcnt lgkmcnt(0)\n"
      "s_barrier\n"
      "v_mfma_f32_16x16x32_bf16 v[96:99], v[164:167], a[32:35], v[96:99]\n"
      "s_lshr_b32 s43, s42, 5\n"
      "s_lshl2_add_u32 s44, s36, s33\n"
      "s_cmp_eq_u32 s43, s44\n"
      "s_cbranch_scc0 .Lp_e26_%=\n"
      "ds_read_b32 v24, v58 offset:0\n"
      "ds_read_b32 v25, v58 offset:8192\n"
      "ds_read_b32 v26, v58 offset:16384\n"
      "ds_read_b32 v27, v58 offset:24576\n"
      "v_mov_b32 v62, 0\n"
      "v_mov_b32 v63, s37\n"
      "s_or_b32 s43, s34, s35\n"
      "s_cmp_eq_u32 s43, 0\n"
      "s_cbranch_scc0 .Lp_z27_%=\n"
      "s_mov_b64 exec, 1\n"
      "global_store_dword v62, v63, s[58:59] sc1\n"
      ".Lp_z27_%=:\n"
      "s_mov_b64 exec, -1\n"
      "s_waitcnt vmcnt(3)\n"
      "v_lshlrev_b32 v57, 16, v57\n"
      "v_and_b32 v56, 48, v243\n"
      "s_mov_b32 s44, 131072\n"
      "v_lshl_add_u32 v56, v56, 1, s44\n"
      "s_waitcnt lgkmcnt(0)\n"
      "v_add_f32 v24, v24, v25\n"
      "s_waitcnt lgkmcnt(0)\n"
      "v_add_f32 v26, v26, v27\n"
      "v_add_f32 v24, v24, v26\n"
      "v_cvt_pk_bf16_f32 v24, v24, v24\n"
      "v_and_b32 v24, 0xffff0000, v24\n"
      "v_mul_f32 v24, s84, v24\n"
      "v_cvt_pk_bf16_f32 v24, v24, v24\n"
      "v_and_b32 v24, 0xffff0000, v24\n"
      "v_add_f32 v24, v24, v57\n"
      "v_cvt_pk_bf16_f32 v24, v24, v24\n"
      "v_and_b32 v24, 0xffff0000, v24\n"
      "v_sub_f32 v24, v24, v57\n"
      "ds_write_b32 v58, v24 offset:1024\n"
      "ds_read_b128 v[28:31], v56 offset:1024\n"
      "ds_read_b128 v[32:35], v56 offset:1040\n"
      ".Lp_e26_%=:\n"
      "v_mfma_f32_16x16x32_bf16 v[100:103], v[164:167], a[36:39], v[100:103]\n"
      "ds_read2st64_b32 v[116:117], v244 offset0:36 offset1:37\n"
      "ds_read2st64_b32 v[118:119], v245 offset0:40 offset1:41\n"
      "v_mfma_f32_16x16x32_bf16 v[96:99], v[168:171], a[64:67], v[96:99]\n"
      "ds_read2st64_b32 v[120:121], v244 offset0:38 offset1:39\n"
      "ds_read2st64_b32 v[122:123], v245 offset0:42 offset1:43\n"
      "v_mfma_f32_16x16x32_bf16 v[100:103], v[168:171], a[68:71], v[100:103]\n"
      "ds_read2st64_b32 v[124:125], v246 offset0:44 offset1:45\n"
      "ds_read2st64_b32 v[126:127], v246 offset0:46 offset1:47\n"
      "v_mfma_f32_16x16x32_bf16 v[96:99], v[172:175], a[96:99], v[96:99]\n"
      "v_mfma_f32_16x16x32_bf16 v[64:67], v[252:255], v[44:47], v[64:67]\n"
      "v_mfma_f32_16x16x32_bf16 v[68:71], v[248:251], v[44:47], v[68:71]\n"
      "v_mfma_f32_16x16x32_bf16 v[100:103], v[172:175], a[100:103], v[100:103]\n"
      "v_mfma_f32_16x16x32_bf16 v[96:99], v[252:255], v[48:51], v[96:99]\n"
      "v_mfma_f32_16x16x32_bf16 v[100:103], v[248:251], v[48:51], v[100:103]\n"
      "v_mfma_f32_16x16x32_bf16 v[96:99], v[176:179], a[128:131], v[96:99]\n"
      "v_mfma_f32_16x16x32_bf16 v[100:103], v[176:179], a[132:135], v[100:103]\n"
      "v_mfma_f32_16x16x32_bf16 v[96:99], v[180:183], a[160:163], v[96:99]\n"
      "s_waitcnt lgkmcnt(0)\n"
      "v_mfma_f32_16x16x32_bf16 v[96:99], v[250:253], v[116:119], v[96:99]\n"
      "v_mfma_f32_16x16x32_bf16 v[100:103], v[250:253], v[120:123], v[100:103]\n"
      "v_mfma_f32_16x16x32_bf16 v[100:103], v[180:183], a[164:167], v[100:103]\n"
      "v_mfma_f32_16x16x32_bf16 v[96:99], v[252:255], v[124:127], v[96:99]\n"
      "v_mfma_f32_16x16x32_bf16 v[100:103], v[248:251], v[124:127], v[100:103]\n"
      "v_mfma_f32_16x16x32_bf16 v[96:99], v[184:187], a[192:195], v[96:99]\n"
      "v_mfma_f32_16x16x32_bf16 v[100:103], v[184:187], a[196:199], v[100:103]\n"
      "v_mfma_f32_16x16x32_bf16 v[96:99], v[188:191], a[224:227], v[96:99]\n"
      "v_mfma_f32_16x16x32_bf16 v[100:103], v[188:191], a[228:231], v[100:103]\n"
      "s_lshr_b32 s43, s42, 5\n"
      "s_lshl2_add_u32 s44, s36, s33\n"
      "s_cmp_eq_u32 s43, s44\n"
      "s_cbranch_scc0 .Lp_e28_%=\n"
      "v_lshrrev_b32 v24, 10, v7\n"
      "v_subrev_u32 v24, s42, v24\n"
      "v_cmp_eq_u32 vcc, 0, v24\n"
      "s_nop 1\n"
      "s_mov_b64 exec, vcc\n"
      "v_fma_f32 v208, v205, v28, v208\n"
      "v_fma_f32 v209, v205, v29, v209\n"
      "v_fma_f32 v210, v205, v30, v210\n"
      "v_fma_f32 v211, v205, v31, v211\n"
      "v_fma_f32 v216, v205, v32, v216\n"
      "v_fma_f32 v217, v205, v33, v217\n"
      "v_fma_f32 v218, v205, v34, v218\n"
      "v_fma_f32 v219, v205, v35, v219\n"
      "s_mov_b64 exec, -1\n"
      "v_cmp_eq_u32 vcc, -16, v24\n"
      "s_nop 1\n"
      "s_mov_b64 exec, vcc\n"
      "v_fma_f32 v212, v207, v28, v212\n"
      "v_fma_f32 v213, v207, v29, v213\n"
      "v_fma_f32 v214, v207, v30, v214\n"
      "v_fma_f32 v215, v207, v31, v215\n"
      "v_fma_f32 v220, v207, v32, v220\n"
      "v_fma_f32 v221, v207, v33, v221\n"
      "v_fma_f32 v222, v207, v34, v222\n"
      "v_fma_f32 v223, v207, v35, v223\n"
      "s_mov_b64 exec, -1\n"
      ".Lp_e28_%=:\n"
      "s_waitcnt vmcnt(0)\n"
      "v_bitop3_b32 v234, v227, v231, s37 bitop3:0x7e\n"
      "v_bitop3_b32 v234, v234, v235, s37 bitop3:0xf6\n"
      "v_cmp_ne_u32 vcc, 0, v234\n"
      "s_nop 1\n"
      "s_cbranch_vccnz .Lp_s30_%=\n"
      ".Lp_o31_%=:\n"
      "v_mov_b32 v227, v232\n"
      "v_mov_b32 v231, v233\n"
      "s_nop 0\n"
      "v_mfma_f32_16x16x32_bf16 v[64:67], v[252:255], v[224:227], v[64:67]\n"
      "v_mfma_f32_16x16x32_bf16 v[96:99], v[252:255], v[228:231], v[96:99]\n"
      "v_mfma_f32_16x16x32_bf16 v[68:71], v[248:251], v[224:227], v[68:71]\n"
      "v_mfma_f32_16x16x32_bf16 v[100:103], v[248:251], v[228:231], v[100:103]\n"
      "s_nop 4\n"
      "v_fma_f32 v208, v204, v64, v208\n"
      "v_fma_f32 v209, v204, v65, v209\n"
      "v_fma_f32 v210, v204, v66, v210\n"
      "v_fma_f32 v211, v204, v67, v211\n"
      "v_fma_f32 v216, v204, v96, v216\n"
      "v_fma_f32 v217, v204, v97, v217\n"
      "v_fma_f32 v218, v204, v98, v218\n"
      "v_fma_f32 v219, v204, v99, v219\n"
      "v_cvt_pk_bf16_f32 v72, v208, v209\n"
      "v_cvt_pk_bf16_f32 v73, v210, v211\n"
      "v_cvt_pk_bf16_f32 v74, v216, v217\n"
      "v_cvt_pk_bf16_f32 v75, v218, v219\n"
      "global_store_dwordx4 v7, v[72:75], s[20:21]\n"
      "v_fma_f32 v212, v206, v68, v212\n"
      "v_fma_f32 v213, v206, v69, v213\n"
      "v_fma_f32 v214, v206, v70, v214\n"
      "v_fma_f32 v215, v206, v71, v215\n"
      "v_fma_f32 v220, v206, v100, v220\n"
      "v_fma_f32 v221, v206, v101, v221\n"
      "v_fma_f32 v222, v206, v102, v222\n"
      "v_fma_f32 v223, v206, v103, v223\n"
      "v_cvt_pk_bf16_f32 v76, v212, v213\n"
      "v_cvt_pk_bf16_f32 v77, v214, v215\n"
      "v_cvt_pk_bf16_f32 v78, v220, v221\n"
      "v_cvt_pk_bf16_f32 v79, v222, v223\n"
      "global_store_dwordx4 v7, v[76:79], s[68:69]\n"
      "s_endpgm\n"
      ".Lp_s2_%=:\n"
      "s_cmp_eq_u32 s38, 0\n"
      "s_cbranch_scc1 .Lp_o3_%=\n"
      "s_sub_u32 s38, s38, 1\n"
      "s_sleep 4\n"
      "s_nop 7\n"
      "global_load_dword v252, v253, s[58:59] offset:0 sc1\n"
      "s_waitcnt vmcnt(0)\n"
      "v_cmp_ne_u32 vcc, s37, v252\n"
      "s_nop 1\n"
      "s_cbranch_vccnz .Lp_s2_%=\n"
      "s_nop 7\n"
      "s_branch .Lp_o3_%=\n"
      ".Lp_s5_%=:\n"
      "s_cmp_eq_u32 s38, 0\n"
      "s_cbranch_scc1 .Lp_o6_%=\n"
      "s_sub_u32 s38, s38, 1\n"
      "s_sleep 4\n"
      "s_nop 7\n"
      "global_load_dword v252, v253, s[58:59] offset:128 sc1\n"
      "s_waitcnt vmcnt(0)\n"
      "v_cmp_ne_u32 vcc, s37, v252\n"
      "s_nop 1\n"
      "s_cbranch_vccnz .Lp_s5_%=\n"
      "s_nop 7\n"
      "s_branch .Lp_o6_%=\n"
      ".Lp_s8_%=:\n"
      "s_cmp_eq_u32 s38, 0\n"
      "s_cbranch_scc1 .Lp_o9_%=\n"
      "s_sub_u32 s38, s38, 1\n"
      "s_sleep 2\n"
      "s_nop 7\n"
      "global_load_dwordx2 v[202:203], v200, s[58:59] sc1\n"
      "s_waitcnt vmcnt(0)\n"
      "v_cmp_ne_u32 vcc, s37, v203\n"
      "s_nop 1\n"
      "s_cbranch_vccnz .Lp_s8_%=\n"
      "s_nop 7\n"
      "s_branch .Lp_o9_%=\n"
      ".Lp_s12_%=:\n"
      "s_cmp_eq_u32 s38, 0\n"
      "s_cbranch_scc1 .Lp_o13_%=\n"
      "s_sub_u32 s38, s38, 1\n"
      "s_sleep 1\n"
      "s_nop 7\n"
      "global_load_dwordx2 v[204:205], v198, s[58:59] offset:0 sc1\n"
      "global_load_dwordx2 v[206:207], v198, s[58:59] offset:256 sc1\n"
      "s_waitcnt vmcnt(0)\n"
      "v_bitop3_b32 v197, v205, v207, s37 bitop3:0x7e\n"
      "v_cmp_ne_u32 vcc, 0, v197\n"
      "s_nop 1\n"
      "s_cbranch_vccnz .Lp_s12_%=\n"
      "s_nop 7\n"
      "s_branch .Lp_o13_%=\n"
      ".Lp_s16_%=:\n"
      "s_cmp_eq_u32 s38, 0\n"
      "s_cbranch_scc1 .Lp_o17_%=\n"
      "s_sub_u32 s38, s38, 1\n"
      "s_nop 7\n"
      "global_load_dwordx4 v[12:15], v4, s[62:63] offset:0 sc1\n"
      "global_load_dwordx4 v[16:19], v4, s[62:63] offset:1024 sc1\n"
      "global_load_dwordx4 v[20:23], v4, s[62:63] offset:2048 sc1\n"
      "s_waitcnt vmcnt(0)\n"
      "v_bitop3_b32 v22, v15, v19, s37 bitop3:0x7e\n"
      "v_bitop3_b32 v22, v22, v23, s37 bitop3:0xf6\n"
      "v_cmp_ne_u32 vcc, 0, v22\n"
      "s_nop 1\n"
      "s_cbranch_vccnz .Lp_s16_%=\n"
      "s_nop 7\n"
      "s_branch .Lp_o17_%=\n"
      ".Lp_s20_%=:\n"
      "s_cmp_eq_u32 s38, 0\n"
      "s_cbranch_scc1 .Lp_o21_%=\n"
      "s_sub_u32 s38, s38, 1\n"
      "s_nop 7\n"
      "global_load_dwordx4 v[44:47], v5, s[62:63] offset:0 sc1\n"
      "global_load_dwordx4 v[48:51], v5, s[62:63] offset:1024 sc1\n"
      "global_load_dwordx4 v[52:55], v5, s[62:63] offset:2048 sc1\n"
      "s_waitcnt vmcnt(0)\n"
      "v_bitop3_b32 v54, v47, v51, s37 bitop3:0x7e\n"
      "v_bitop3_b32 v54, v54, v55, s37 bitop3:0xf6\n"
      "v_cmp_ne_u32 vcc, 0, v54\n"
      "s_nop 1\n"
      "s_cbranch_vccnz .Lp_s20_%=\n"
      "s_nop 7\n"
      "s_branch .Lp_o21_%=\n"
      ".Lp_s24_%=:\n"
      "s_cmp_eq_u32 s38, 0\n"
      "s_cbranch_scc1 .Lp_o25_%=\n"
      "s_sub_u32 s38, s38, 1\n"
      "s_sleep 2\n"
      "s_nop 7\n"
      "global_load_dwordx4 v[24:27], v60, s[58:59] offset:0 sc1\n"
      "global_load_dwordx4 v[28:31], v60, s[58:59] offset:1024 sc1\n"
      "global_load_dwordx4 v[32:35], v60, s[58:59] offset:2048 sc1\n"
      "global_load_dwordx4 v[36:39], v60, s[58:59] offset:3072 sc1\n"
      "s_waitcnt vmcnt(0)\n"
      "v_bitop3_b32 v61, v25, v27, s37 bitop3:0x7e\n"
      "v_bitop3_b32 v61, v61, v29, s37 bitop3:0xf6\n"
      "v_bitop3_b32 v61, v61, v31, s37 bitop3:0xf6\n"
      "v_bitop3_b32 v61, v61, v33, s37 bitop3:0xf6\n"
      "v_bitop3_b32 v61, v61, v35, s37 bitop3:0xf6\n"
      "v_bitop3_b32 v61, v61, v37, s37 bitop3:0xf6\n"
      "v_bitop3_b32 v61, v61, v39, s37 bitop3:0xf6\n"
      "v_cmp_ne_u32 vcc, 0, v61\n"
      "s_nop 1\n"
      "s_cbranch_vccnz .Lp_s24_%=\n"
      "s_nop 7\n"
      "s_branch .Lp_o25_%=\n"
      ".Lp_s30_%=:\n"
      "s_cmp_eq_u32 s38, 0\n"
      "s_cbranch_scc1 .Lp_o31_%=\n"
      "s_sub_u32 s38, s38, 1\n"
      "s_nop 7\n"
      "global_load_dwordx4 v[224:227], v6, s[62:63] offset:0 sc1\n"
      "global_load_dwordx4 v[228:231], v6, s[62:63] offset:1024 sc1\n"
      "global_load_dwordx4 v[232:235], v6, s[62:63] offset:2048 sc1\n"
      "s_waitcnt vmcnt(0)\n"
      "v_bitop3_b32 v234, v227, v231, s37 bitop3:0x7e\n"
      "v_bitop3_b32 v234, v234, v235, s37 bitop3:0xf6\n"
      "v_cmp_ne_u32 vcc, 0, v234\n"
      "s_nop 1\n"
      "s_cbranch_vccnz .Lp_s30_%=\n"
      "s_nop 7\n"
      "s_branch .Lp_o31_%=\n"
      ".Ls_s3_%=:\n"
      "s_cmp_eq_u32 s38, 0\n"
      "s_cbranch_scc1 .Ls_o4_%=\n"
      "s_sub_u32 s38, s38, 1\n"
      "s_sleep 4\n"
      "s_nop 7\n"
      "global_load_dword v252, v253, s[58:59] offset:0 sc1\n"
      "s_waitcnt vmcnt(0)\n"
      "v_cmp_ne_u32 vcc, s37, v252\n"
      "s_nop 1\n"
      "s_cbranch_vccnz .Ls_s3_%=\n"
      "s_nop 7\n"
      "s_branch .Ls_o4_%=\n"
      ".Ls_s6_%=:\n"
      "s_cmp_eq_u32 s38, 0\n"
      "s_cbranch_scc1 .Ls_o7_%=\n"
      "s_sub_u32 s38, s38, 1\n"
      "s_sleep 2\n"
      "s_nop 7\n"
      "global_load_dwordx2 v[206:207], v204, s[58:59] sc1\n"
      "s_waitcnt vmcnt(0)\n"
      "v_cmp_ne_u32 vcc, s37, v207\n"
      "s_nop 1\n"
      "s_cbranch_vccnz .Ls_s6_%=\n"
      "s_nop 7\n"
      "s_branch .Ls_o7_%=\n"
      ".Ls_s10_%=:\n"
      "s_cmp_eq_u32 s38, 0\n"
      "s_cbranch_scc1 .Ls_o11_%=\n"
      "s_sub_u32 s38, s38, 1\n"
      "s_sleep 1\n"
      "s_nop 7\n"
      "global_load_dwordx2 v[204:205], v198, s[58:59] offset:0 sc1\n"
      "global_load_dwordx2 v[206:207], v198, s[58:59] offset:256 sc1\n"
      "s_waitcnt vmcnt(0)\n"
      "v_bitop3_b32 v197, v205, v207, s37 bitop3:0x7e\n"
      "v_cmp_ne_u32 vcc, 0, v197\n"
      "s_nop 1\n"
      "s_cbranch_vccnz .Ls_s10_%=\n"
      "s_nop 7\n"
      "s_branch .Ls_o11_%=\n"
      ".Ls_s14_%=:\n"
      "s_cmp_eq_u32 s38, 0\n"
      "s_cbranch_scc1 .Ls_o15_%=\n"
      "s_sub_u32 s38, s38, 1\n"
      "s_sleep 4\n"
      "s_nop 7\n"
      "global_load_dword v252, v253, s[58:59] offset:128 sc1\n"
      "s_waitcnt vmcnt(0)\n"
      "v_cmp_ne_u32 vcc, s37, v252\n"
      "s_nop 1\n"
      "s_cbranch_vccnz .Ls_s14_%=\n"
      "s_nop 7\n"
      "s_branch .Ls_o15_%=\n"
      ".Ls_s17_%=:\n"
      "s_cmp_eq_u32 s38, 0\n"
      "s_cbranch_scc1 .Ls_o18_%=\n"
      "s_sub_u32 s38, s38, 1\n"
      "s_sleep 8\n"
      "s_nop 7\n"
      "global_load_dwordx4 v[224:227], v200, s[58:59] offset:0 sc1\n"
      "global_load_dwordx4 v[228:231], v200, s[58:59] offset:1024 sc1\n"
      "global_load_dwordx4 v[232:235], v200, s[58:59] offset:2048 sc1\n"
      "global_load_dwordx4 v[236:239], v200, s[58:59] offset:3072 sc1\n"
      "s_waitcnt vmcnt(0)\n"
      "v_bitop3_b32 v192, v225, v227, s37 bitop3:0x7e\n"
      "v_bitop3_b32 v192, v192, v229, s37 bitop3:0xf6\n"
      "v_bitop3_b32 v192, v192, v231, s37 bitop3:0xf6\n"
      "v_bitop3_b32 v192, v192, v233, s37 bitop3:0xf6\n"
      "v_bitop3_b32 v192, v192, v235, s37 bitop3:0xf6\n"
      "v_bitop3_b32 v192, v192, v237, s37 bitop3:0xf6\n"
      "v_bitop3_b32 v192, v192, v239, s37 bitop3:0xf6\n"
      "v_cmp_ne_u32 vcc, 0, v192\n"
      "s_nop 1\n"
      "s_cbranch_vccnz .Ls_s17_%=\n"
      "s_nop 7\n"
      "s_branch .Ls_o18_%=\n"
      ".Ls_s20_%=:\n"
      "s_cmp_eq_u32 s38, 0\n"
      "s_cbranch_scc1 .Ls_o21_%=\n"
      "s_sub_u32 s38, s38, 1\n"
      "s_nop 7\n"
      "global_load_dwordx4 v[12:15], v4, s[62:63] offset:0 sc1\n"
      "global_load_dwordx4 v[16:19], v4, s[62:63] offset:1024 sc1\n"
      "global_load_dwordx4 v[20:23], v4, s[62:63] offset:2048 sc1\n"
      "s_waitcnt vmcnt(0)\n"
      "v_bitop3_b32 v22, v15, v19, s37 bitop3:0x7e\n"
      "v_bitop3_b32 v22, v22, v23, s37 bitop3:0xf6\n"
      "v_cmp_ne_u32 vcc, 0, v22\n"
      "s_nop 1\n"
      "s_cbranch_vccnz .Ls_s20_%=\n"
      "s_nop 7\n"
      "s_branch .Ls_o21_%=\n"
      ".Ls_s24_%=:\n"
      "s_cmp_eq_u32 s38, 0\n"
      "s_cbranch_scc1 .Ls_o25_%=\n"
      "s_sub_u32 s38, s38, 1\n"
      "s_nop 7\n"
      "global_load_dwordx4 v[44:47], v5, s[62:63] offset:0 sc1\n"
      "global_load_dwordx4 v[48:51], v5, s[62:63] offset:1024 sc1\n"
      "global_load_dwordx4 v[52:55], v5, s[62:63] offset:2048 sc1\n"
      "s_waitcnt vmcnt(0)\n"
      "v_bitop3_b32 v54, v47, v51, s37 bitop3:0x7e\n"
      "v_bitop3_b32 v54, v54, v55, s37 bitop3:0xf6\n"
      "v_cmp_ne_u32 vcc, 0, v54\n"
      "s_nop 1\n"
      "s_cbranch_vccnz .Ls_s24_%=\n"
      "s_nop 7\n"
      "s_branch .Ls_o25_%=\n"
      ".Ls_s28_%=:\n"
      "s_cmp_eq_u32 s38, 0\n"
      "s_cbranch_scc1 .Ls_o29_%=\n"
      "s_sub_u32 s38, s38, 1\n"
      "s_sleep 2\n"
      "s_nop 7\n"
      "global_load_dwordx4 v[24:27], v60, s[58:59] offset:0 sc1\n"
      "global_load_dwordx4 v[28:31], v60, s[58:59] offset:1024 sc1\n"
      "global_load_dwordx4 v[32:35], v60, s[58:59] offset:2048 sc1\n"
      "global_load_dwordx4 v[36:39], v60, s[58:59] offset:3072 sc1\n"
      "s_waitcnt vmcnt(0)\n"
      "v_bitop3_b32 v61, v25, v27, s37 bitop3:0x7e\n"
      "v_bitop3_b32 v61, v61, v29, s37 bitop3:0xf6\n"
      "v_bitop3_b32 v61, v61, v31, s37 bitop3:0xf6\n"
      "v_bitop3_b32 v61, v61, v33, s37 bitop3:0xf6\n"
      "v_bitop3_b32 v61, v61, v35, s37 bitop3:0xf6\n"
      "v_bitop3_b32 v61, v61, v37, s37 bitop3:0xf6\n"
      "v_bitop3_b32 v61, v61, v39, s37 bitop3:0xf6\n"
      "v_cmp_ne_u32 vcc, 0, v61\n"
      "s_nop 1\n"
      "s_cbranch_vccnz .Ls_s28_%=\n"
      "s_nop 7\n"
      "s_branch .Ls_o29_%=\n"
      ".Ls_s34_%=:\n"
      "s_cmp_eq_u32 s38, 0\n"
      "s_cbranch_scc1 .Ls_o35_%=\n"
      "s_sub_u32 s38, s38, 1\n"
      "s_nop 7\n"
      "global_load_dwordx4 v[224:227], v6, s[62:63] offset:0 sc1\n"
      "global_load_dwordx4 v[228:231], v6, s[62:63] offset:1024 sc1\n"
      "global_load_dwordx4 v[232:235], v6, s[62:63] offset:2048 sc1\n"
      "s_waitcnt vmcnt(0)\n"
      "v_bitop3_b32 v234, v227, v231, s37 bitop3:0x7e\n"
      "v_bitop3_b32 v234, v234, v235, s37 bitop3:0xf6\n"
      "v_cmp_ne_u32 vcc, 0, v234\n"
      "s_nop 1\n"
      "s_cbranch_vccnz .Ls_s34_%=\n"
      "s_nop 7\n"
      "s_branch .Ls_o35_%=\n"
      ".Lend_%=:\n"
      :
      : "s"(__builtin_amdgcn_kernarg_segment_ptr()), "s"(blockIdx.x), "s"(__builtin_amdgcn_readfirstlane(threadIdx.x >> 6)),
        "s"((unsigned)(size_t)smem)
      : "memory", "scc", "vcc",
        "v0", "v1", "v2", "v3", "v4", "v5", "v6", "v7", "v8", "v9", "v10", "v11", "v12", "v13", "v14", "v15", "v16", "v17", "v18", "v19", "v20", "v21", "v22", "v23", "v24", "v25", "v26", "v27", "v28", "v29", "v30", "v31", "v32", "v33", "v34", "v35", "v36", "v37", "v38", "v39", "v40", "v41", "v42", "v43", "v44", "v45", "v46", "v47", "v48", "v49", "v50", "v51", "v52", "v53", "v54", "v55", "v56", "v57", "v58", "v59", "v60", "v61", "v62", "v63", "v64", "v65", "v66", "v67", "v68", "v69", "v70", "v71", "v72", "v73", "v74", "v75", "v76", "v77", "v78", "v79", "v80", "v81", "v82", "v83", "v84", "v85", "v86", "v87", "v88", "v89", "v90", "v91", "v92", "v93", "v94", "v95", "v96", "v97", "v98", "v99", "v100", "v101", "v102", "v103", "v104", "v105", "v106", "v107", "v108", "v109", "v110", "v111", "v112", "v113", "v114", "v115", "v116", "v117", "v118", "v119", "v120", "v121", "v122", "v123", "v124", "v125", "v126", "v127", "v128", "v129", "v130", "v131", "v132", "v133", "v134", "v135", "v136", "v137", "v138", "v139", "v140", "v141", "v142", "v143", "v144", "v145", "v146", "v147", "v148", "v149", "v150", "v151", "v152", "v153", "v154", "v155", "v156", "v157", "v158", "v159", "v160", "v161", "v162", "v163", "v164", "v165", "v166", "v167", "v168", "v169", "v170", "v171", "v172", "v173", "v174", "v175", "v176", "v177", "v178", "v179", "v180", "v181", "v182", "v183", "v184", "v185", "v186", "v187", "v188", "v189", "v190", "v191", "v192", "v193", "v194", "v195", "v196", "v197", "v198", "v199", "v200", "v201", "v202", "v203", "v204", "v205", "v206", "v207", "v208", "v209", "v210", "v211", "v212", "v213", "v214", "v215", "v216", "v217", "v218", "v219", "v220", "v221", "v222", "v223", "v224", "v225", "v226", "v227", "v228", "v229", "v230", "v231", "v232", "v233", "v234", "v235", "v236", "v237", "v238", "v239", "v240", "v241", "v242", "v243", "v244", "v245", "v246", "v247", "v248", "v249", "v250", "v251", "v252", "v253", "v254", "v255",
        "a0", "a1", "a2", "a3", "a4", "a5", "a6", "a7", "a8", "a9", "a10", "a11", "a12", "a13", "a14", "a15", "a16", "a17", "a18", "a19", "a20", "a21", "a22", "a23", "a24", "a25", "a26", "a27", "a28", "a29", "a30", "a31", "a32", "a33", "a34", "a35", "a36", "a37", "a38", "a39", "a40", "a41", "a42", "a43", "a44", "a45", "a46", "a47", "a48", "a49", "a50", "a51", "a52", "a53", "a54", "a55", "a56", "a57", "a58", "a59", "a60", "a61", "a62", "a63", "a64", "a65", "a66", "a67", "a68", "a69", "a70", "a71", "a72", "a73", "a74", "a75", "a76", "a77", "a78", "a79", "a80", "a81", "a82", "a83", "a84", "a85", "a86", "a87", "a88", "a89", "a90", "a91", "a92", "a93", "a94", "a95", "a96", "a97", "a98", "a99", "a100", "a101", "a102", "a103", "a104", "a105", "a106", "a107", "a108", "a109", "a110", "a111", "a112", "a113", "a114", "a115", "a116", "a117", "a118", "a119", "a120", "a121", "a122", "a123", "a124", "a125", "a126", "a127", "a128", "a129", "a130", "a131", "a132", "a133", "a134", "a135", "a136", "a137", "a138", "a139", "a140", "a141", "a142", "a143", "a144", "a145", "a146", "a147", "a148", "a149", "a150", "a151", "a152", "a153", "a154", "a155", "a156", "a157", "a158", "a159", "a160", "a161", "a162", "a163", "a164", "a165", "a166", "a167", "a168", "a169", "a170", "a171", "a172", "a173", "a174", "a175", "a176", "a177", "a178", "a179", "a180", "a181", "a182", "a183", "a184", "a185", "a186", "a187", "a188", "a189", "a190", "a191", "a192", "a193", "a194", "a195", "a196", "a197", "a198", "a199", "a200", "a201", "a202", "a203", "a204", "a205", "a206", "a207", "a208", "a209", "a210", "a211", "a212", "a213", "a214", "a215", "a216", "a217", "a218", "a219", "a220", "a221", "a222", "a223", "a224", "a225", "a226", "a227", "a228", "a229", "a230", "a231", "a232", "a233", "a234", "a235", "a236", "a237", "a238", "a239", "a240", "a241", "a242", "a243", "a244", "a245", "a246", "a247", "a248", "a249", "a250", "a251", "a252", "a253", "a254", "a255",
        "s8", "s9", "s10", "s11", "s12", "s13", "s14", "s15", "s16", "s17", "s18", "s19", "s20", "s21", "s22", "s23", "s24", "s25", "s26", "s27", "s28", "s29", "s30", "s31", "s32", "s33", "s34", "s35", "s36", "s37", "s38", "s39", "s40", "s41", "s42", "s43", "s44", "s45", "s46", "s47", "s48", "s49", "s50", "s51", "s52", "s53", "s54", "s55", "s56", "s57", "s58", "s59", "s60", "s61", "s62", "s63", "s64", "s65", "s66", "s67", "s68", "s69", "s70", "s71", "s72", "s73", "s74", "s75", "s76", "s77", "s78", "s79", "s80", "s81", "s82", "s83", "s84", "s85");
}

extern "C" void kernel_launch(void* const* d_in, const int* in_sizes, int n_in, void* d_out, int out_size, void* d_ws, size_t ws_size,
                              hipStream_t stream) {
  fused_k<<<256, 256, 0, stream>>>((const u16*)d_in[1], (const u16*)d_in[2], (const u16*)d_in[3], (const u16*)d_in[4], (const u16*)d_in[0],
                                   (char*)d_ws, (u16*)d_out, (const u16*)d_in[6], (const u16*)d_in[10], (const u16*)d_in[7],
                                   (const u16*)d_in[8], (const u16*)d_in[9]);
}
```

```cpp
#include <hip/hip_runtime.h>

typedef unsigned short u16;

__global__ __launch_bounds__(256) void fused_k(const u16* wsre, const u16* wsim, const u16* wdre, const u16* wdim, const u16* x, char* ws,
                                               u16* out, const u16* w_r, const u16* gate_w, const u16* w_gate, const u16* w_up,
                                               const u16* w_down) {
  __shared__ __attribute__((aligned(16))) char smem[163840];
  asm volatile(
      ".Lbegin_%=:\n"
      "s_load_dwordx16 s[8:23], %0, 0\n"
      "s_load_dwordx8 s[24:31], %0, 64\n"
      "v_mbcnt_lo_u32_b32 v243, -1, 0\n"
      "s_mov_b32 s32, %1\n"
      "v_mbcnt_hi_u32_b32 v243, -1, v243\n"
      "s_mov_b32 s33, %2\n"
      "s_bfe_u32 s34, s32, 0x20001\n"
      "s_bfe_u32 s35, s32, 0x20003\n"
      "v_lshrrev_b32 v236, 3, v243\n"
      "s_lshr_b32 s43, s32, 5\n"
      "v_xor_b32 v237, v243, v236\n"
      "s_and_b32 s36, s32, 1\n"
      "v_and_b32 v237, 7, v237\n"
      "s_lshl3_add_u32 s36, s36, s43\n"
      "v_lshlrev_b32 v237, 4, v237\n"
      "s_add_u32 s44, s43, s35\n"
      "v_lshlrev_b32 v242, 4, v243\n"
      "s_lshl_b32 s41, s33, 14\n"
      "s_bitcmp1_b32 s44, 0\n"
      "s_cbranch_scc1 .Lstream_%=\n"
      "v_and_b32 v238, 3, v236\n"
      "s_lshr_b32 s43, s43, 1\n"
      "v_and_b32 v239, 4, v236\n"
      "s_add_u32 s80, s43, s35\n"
      "v_lshl_or_b32 v238, v239, 1, v238\n"
      "s_lshl_b32 s80, s80, 8\n"
      "v_lshl_or_b32 v240, v238, 10, v237\n"
      "s_and_b32 s80, s80, 0x300\n"
      "v_add_u32 v241, 15360, v240\n"
      "s_lshr_b32 s83, s36, 1\n"
      "v_add_u32 v254, 2048, v240\n"
      "s_and_b32 s44, s34, 1\n"
      "v_add_u32 v255, 17408, v240\n"
      "s_lshl3_add_u32 s44, s44, s83\n"
      "s_lshl2_add_u32 s40, s44, s33\n"
      "s_lshl_b32 s44, s40, 15\n"
      "s_lshl_b32 s81, s33, 7\n"
      "s_add_u32 s81, s81, s80\n"
      "s_and_b32 s81, s81, 0x380\n"
      "s_lshl_b32 s45, s35, 17\n"
      "s_lshl_b32 s82, s33, 12\n"
      "s_bitcmp1_b32 s34, 1\n"
      "s_waitcnt lgkmcnt(0)\n"
      "s_cselect_b64 s[12:13], s[14:15], s[12:13]\n"
      "s_add_u32 s68, s16, s44\n"
      "s_addc_u32 s69, s17, 0\n"
      "s_add_u32 s12, s12, s45\n"
      "s_addc_u32 s13, s13, 0\n"
      "s_add_u32 s45, s34, 1\n"
      "s_and_b32 s45, s45, 3\n"
      "s_lshl_b32 s45, s45, 15\n"
      "s_or_b32 s45, s45, s81\n"
      "s_add_u32 s46, s12, s45\n"
      "s_addc_u32 s47, s13, 0\n"
      "s_add_u32 m0, s82, 65536\n"
      "s_nop 0\n"
      "global_load_lds_dwordx4 v240, s[46:47] offset:0\n"
      "global_load_lds_dwordx4 v241, s[46:47] offset:1024\n"
      "global_load_lds_dwordx4 v254, s[46:47] offset:2048\n"
      "global_load_lds_dwordx4 v255, s[46:47] offset:3072\n"
      "s_add_u32 s46, s68, s80\n"
      "s_addc_u32 s47, s69, 0\n"
      "s_add_u32 m0, s41, 0\n"
      "s_nop 0\n"
      "global_load_lds_dwordx4 v240, s[46:47] offset:0\n"
      "global_load_lds_dwordx4 v241, s[46:47] offset:1024\n"
      "global_load_lds_dwordx4 v254, s[46:47] offset:2048\n"
      "global_load_lds_dwordx4 v255, s[46:47] offset:3072\n"
      "s_add_u32 s45, s34, 2\n"
      "s_and_b32 s45, s45, 3\n"
      "s_lshl_b32 s45, s45, 15\n"
      "s_or_b32 s45, s45, s81\n"
      "s_add_u32 s46, s12, s45\n"
      "s_addc_u32 s47, s13, 0\n"
      "s_add_u32 m0, s82, 98304\n"
      "s_nop 0\n"
      "global_load_lds_dwordx4 v240, s[46:47] offset:0\n"
      "global_load_lds_dwordx4 v241, s[46:47] offset:1024\n"
      "s_add_u32 s45, s80, 128\n"
      "s_and_b32 s45, s45, 0x380\n"
      "s_add_u32 s46, s68, s45\n"
      "s_addc_u32 s47, s69, 0\n"
      "s_add_u32 m0, s41, 4096\n"
      "s_nop 0\n"
      "global_load_lds_dwordx4 v240, s[46:47] offset:0\n"
      "global_load_lds_dwordx4 v241, s[46:47] offset:1024\n"
      "global_load_lds_dwordx4 v254, s[46:47] offset:2048\n"
      "global_load_lds_dwordx4 v255, s[46:47] offset:3072\n"
      "s_branch .Lprod_%=\n"
      ".Lstream_%=:\n"
      "v_lshl_or_b32 v244, v236, 12, v237\n"
      "v_add_u32 v245, 31744, v244\n"
      "v_add_u32 v246, 63488, v244\n"
      "v_add_u32 v247, 95232, v244\n"
      "s_getpc_b64 s[46:47]\n"
      ".Lhere_%=:\n"
      "s_lshr_b32 s44, s32, 4\n"
      "s_lshl2_add_u32 s44, s44, s33\n"
      "s_lshl_b32 s44, s44, 10\n"
      "s_sub_u32 s46, s46, .Lhere_%=-.Lbegin_%=\n"
      "s_subb_u32 s47, s47, 0\n"
      "v_add_u32 v238, s44, v242\n"
      "v_min_u32 v238, .Lend_%=-.Lbegin_%=-16, v238\n"
      "global_load_dwordx4 v[192:195], v238, s[46:47]\n"
      "s_waitcnt lgkmcnt(0)\n"
      "s_bitcmp1_b32 s34, 1\n"
      "s_cselect_b64 s[8:9], s[10:11], s[8:9]\n"
      "s_and_b32 s45, s34, 1\n"
      "s_lshl2_add_u32 s45, s45, s33\n"
      "s_lshl_b32 s45, s45, 9\n"
      "s_lshl_b32 s76, s36, 19\n"
      "s_add_u32 s45, s45, s76\n"
      "s_add_u32 s8, s8, s45\n"
      "s_addc_u32 s9, s9, 0\n"
      "s_lshr_b32 s39, s32, 6\n"
      "s_and_b32 s45, s35, 2\n"
      "s_xor_b32 s39, s39, s45\n"
      "s_lshl_b32 s45, s39, 7\n"
      "s_add_u32 s76, s33, 0\n"
      "s_and_b32 s76, s76, 3\n"
      "s_lshl_b32 s76, s76, 17\n"
      "s_or_b32 s76, s76, s45\n"
      "s_add_u32 s77, s33, 1\n"
      "s_and_b32 s77, s77, 3\n"
      "s_lshl_b32 s77, s77, 17\n"
      "s_or_b32 s77, s77, s45\n"
      "s_add_u32 s78, s33, 2\n"
      "s_and_b32 s78, s78, 3\n"
      "s_lshl_b32 s78, s78, 17\n"
      "s_or_b32 s78, s78, s45\n"
      "s_add_u32 s79, s33, 3\n"
      "s_and_b32 s79, s79, 3\n"
      "s_lshl_b32 s79, s79, 17\n"
      "s_or_b32 s79, s79, s45\n"
      "s_lshl2_add_u32 s43, s34, s35\n"
      "s_mul_i32 s43, s43, 266240\n"
      "s_add_u32 s43, s43, 262144\n"
      "s_lshl2_add_u32 s44, s36, s35\n"
      "s_mul_i32 s44, s44, 147712\n"
      "s_add_u32 s44, s44, 6438912\n"
      "s_bitcmp1_b32 s33, 0\n"
      "s_cselect_b32 s43, s43, s44\n"
      "s_cselect_b64 s[46:47], s[22:23], s[24:25]\n"
      "s_cselect_b64 s[70:71], s[28:29], s[26:27]\n"
      "s_lshl_b32 s44, s36, 17\n"
      "s_mul_i32 s45, s32, 7168\n"
      "s_bitcmp1_b32 s33, 1\n"
      "s_cselect_b64 s[46:47], s[46:47], s[18:19]\n"
      "s_cselect_b32 s43, 0, s43\n"
      "v_mov_b32 v238, s43\n"
      "global_load_dword v197, v238, s[46:47] sc1\n"
      "s_cmp_eq_u32 s33, 2\n"
      "s_cselect_b64 s[70:71], s[30:31], s[70:71]\n"
      "s_cmp_eq_u32 s33, 3\n"
      "s_cselect_b64 s[70:71], s[20:21], s[70:71]\n"
      "s_cselect_b32 s44, s44, s45\n"
      "v_mov_b32 v239, s44\n"
      "global_load_dword v198, v239, s[70:71] sc1\n"
      "s_sleep 20\n"
      "s_bitcmp1_b32 s34, 1\n"
      "s_cselect_b64 s[46:47], s[14:15], s[12:13]\n"
      "s_min_u32 s44, s33, 2\n"
      "s_add_u32 s44, s44, s34\n"
      "s_add_u32 s44, s44, 2\n"
      "s_and_b32 s44, s44, 3\n"
      "s_lshl2_add_u32 s44, s35, s44\n"
      "s_lshr_b32 s45, s32, 6\n"
      "s_lshl2_add_u32 s44, s44, s45\n"
      "s_lshl_b32 s44, s44, 13\n"
      "v_lshl_add_u32 v238, v243, 7, s44\n"
      "s_cmp_eq_u32 s33, 3\n"
      "s_cbranch_scc1 .Ls_e1_%=\n"
      "global_load_dword v196, v238, s[46:47]\n"
      ".Ls_e1_%=:\n"
      "s_xor_b32 s45, s76, 0\n"
      "s_add_u32 s46, s8, s45\n"
      "s_addc_u32 s47, s9, 0\n"
      "s_add_u32 m0, s41, 0\n"
      "s_nop 0\n"
      "global_load_lds_dwordx4 v244, s[46:47] offset:0\n"
      "global_load_lds_dwordx4 v245, s[46:47] offset:1024\n"
      "global_load_lds_dwordx4 v246, s[46:47] offset:2048\n"
      "global_load_lds_dwordx4 v247, s[46:47] offset:3072\n"
      "s_xor_b32 s45, s77, 0\n"
      "s_add_u32 s46, s8, s45\n"
      "s_addc_u32 s47, s9, 0\n"
      "s_add_u32 m0, s41, 4096\n"
      "s_nop 0\n"
      "global_load_lds_dwordx4 v244, s[46:47] offset:0\n"
      "global_load_lds_dwordx4 v245, s[46:47] offset:1024\n"
      "global_load_lds_dwordx4 v246, s[46:47] offset:2048\n"
      "global_load_lds_dwordx4 v247, s[46:47] offset:3072\n"
      "s_xor_b32 s45, s78, 0\n"
      "s_add_u32 s46, s8, s45\n"
      "s_addc_u32 s47, s9, 0\n"
      "s_add_u32 m0, s41, 8192\n"
      "s_nop 0\n"
      "global_load_lds_dwordx4 v244, s[46:47] offset:0\n"
      "global_load_lds_dwordx4 v245, s[46:47] offset:1024\n"
      "global_load_lds_dwordx4 v246, s[46:47] offset:2048\n"
      "global_load_lds_dwordx4 v247, s[46:47] offset:3072\n"
      "s_add_u32 s58, s18, 4718592\n"
      "s_addc_u32 s59, s19, 0\n"
      "v_mov_b32 v235, 0\n"
      "global_load_dword v235, v235, s[58:59] sc1\n"
      "v_and_b32 v238, 15, v243\n"
      "v_lshrrev_b32 v239, 4, v243\n"
      "v_xor_b32 v239, v239, v238\n"
      "v_and_b32 v239, 7, v239\n"
      "v_lshlrev_b32 v239, 4, v239\n"
      "v_lshl_or_b32 v238, v238, 7, v239\n"
      "v_add_u32 v248, s41, v238\n"
      "v_xor_b32 v249, 64, v248\n"
      "s_lshl2_add_u32 s43, s34, s35\n"
      "s_lshr_b32 s45, s39, 1\n"
      "s_lshl1_add_u32 s45, s33, s45\n"
      "s_lshl_b32 s45, s45, 15\n"
      "s_mul_i32 s44, s43, 266240\n"
      "s_add_u32 s44, s44, 4096\n"
      "s_add_u32 s14, s18, s44\n"
      "s_addc_u32 s15, s19, 0\n"
      "s_add_u32 s10, s14, s45\n"
      "s_addc_u32 s11, s15, 0\n"
      "s_xor_b32 s45, s45, 0x8000\n"
      "s_add_u32 s14, s14, s45\n"
      "s_addc_u32 s15, s15, 0\n"
      "s_and_b32 s44, s39, 1\n"
      "s_lshl_b32 s45, s44, 14\n"
      "v_add_u32 v240, s45, v242\n"
      "v_xor_b32 v241, 0x4000, v240\n"
      "s_add_u32 s72, s10, 0x2000\n"
      "s_addc_u32 s73, s11, 0\n"
      "s_add_u32 s74, s14, 0x2000\n"
      "s_addc_u32 s75, s15, 0\n"
      "s_lshl2_add_u32 s44, s43, s33\n"
      "s_lshl_b32 s44, s44, 12\n"
      "s_add_u32 s44, s44, 1048576\n"
      "v_and_b32 v253, 7, v243\n"
      "v_lshl_add_u32 v253, v253, 9, s44\n"
      "s_lshl2_add_u32 s43, s36, s35\n"
      "s_mul_i32 s43, s43, 147712\n"
      "s_mul_i32 s44, s33, 3072\n"
      "s_add_u32 s43, s43, s44\n"
      "s_add_u32 s43, s43, 6291456\n"
      "s_add_u32 s60, s18, s43\n"
      "s_addc_u32 s61, s19, 0\n"
      "s_mul_i32 s44, s34, 36864\n"
      "s_add_u32 s62, s60, s44\n"
      "s_addc_u32 s63, s61, 0\n"
      "s_lshr_b32 s43, s34, 1\n"
      "s_lshl2_add_u32 s43, s43, s35\n"
      "s_lshl_b32 s43, s43, 12\n"
      "s_add_u32 s43, s43, 4096\n"
      "v_lshl_add_u32 v204, v243, 6, s43\n"
      "s_lshr_b32 s80, s32, 4\n"
      "s_bfe_u32 s43, s32, 0x10002\n"
      "s_lshl1_add_u32 s80, s80, s43\n"
      "s_and_b32 s81, s32, 3\n"
      "s_cmp_lt_u32 s80, 28\n"
      "s_cselect_b32 s82, -1, 0\n"
      "s_lshr_b32 s83, s33, 1\n"
      "s_lshl1_add_u32 s83, s81, s83\n"
      "v_add_u32 v250, 0x10000, v248\n"
      "v_add_u32 v251, 0x10000, v249\n"
      "s_xor_b32 s45, s79, 0\n"
      "s_add_u32 s46, s8, s45\n"
      "s_addc_u32 s47, s9, 0\n"
      "s_add_u32 m0, s41, 12288\n"
      "s_nop 0\n"
      "global_load_lds_dwordx4 v244, s[46:47] offset:0\n"
      "global_load_lds_dwordx4 v245, s[46:47] offset:1024\n"
      "global_load_lds_dwordx4 v246, s[46:47] offset:2048\n"
      "global_load_lds_dwordx4 v247, s[46:47] offset:3072\n"
      "s_xor_b32 s45, s76, 128\n"
      "s_add_u32 s46, s8, s45\n"
      "s_addc_u32 s47, s9, 0\n"
      "s_add_u32 m0, s41, 65536\n"
      "s_nop 0\n"
      "global_load_lds_dwordx4 v244, s[46:47] offset:0\n"
      "global_load_lds_dwordx4 v245, s[46:47] offset:1024\n"
      "global_load_lds_dwordx4 v246, s[46:47] offset:2048\n"
      "global_load_lds_dwordx4 v247, s[46:47] offset:3072\n"
      "s_waitcnt vmcnt(17)\n"
      "ds_read_b128 a[0:3], v248 offset:0\n"
      "ds_read_b128 a[4:7], v248 offset:2048\n"
      "ds_read_b128 a[32:35], v249 offset:0\n"
      "ds_read_b128 a[36:39], v249 offset:2048\n"
      "s_xor_b32 s45, s77, 128\n"
      "s_add_u32 s46, s8, s45\n"
      "s_addc_u32 s47, s9, 0\n"
      "s_add_u32 m0, s41, 69632\n"
      "s_nop 0\n"
      "global_load_lds_dwordx4 v244, s[46:47] offset:0\n"
      "global_load_lds_dwordx4 v245, s[46:47] offset:1024\n"
      "global_load_lds_dwordx4 v246, s[46:47] offset:2048\n"
      "global_load_lds_dwordx4 v247, s[46:47] offset:3072\n"
      "s_waitcnt vmcnt(17)\n"
      "ds_read_b128 a[8:11], v248 offset:4096\n"
      "ds_read_b128 a[12:15], v248 offset:6144\n"
      "ds_read_b128 a[40:43], v249 offset:4096\n"
      "ds_read_b128 a[44:47], v249 offset:6144\n"
      "s_xor_b32 s45, s78, 128\n"
      "s_add_u32 s46, s8, s45\n"
      "s_addc_u32 s47, s9, 0\n"
      "s_add_u32 m0, s41, 73728\n"
      "s_nop 0\n"
      "global_load_lds_dwordx4 v244, s[46:47] offset:0\n"
      "global_load_lds_dwordx4 v245, s[46:47] offset:1024\n"
      "global_load_lds_dwordx4 v246, s[46:47] offset:2048\n"
      "global_load_lds_dwordx4 v247, s[46:47] offset:3072\n"
      "s_waitcnt vmcnt(17)\n"
      "ds_read_b128 a[16:19], v248 offset:8192\n"
      "ds_read_b128 a[20:23], v248 offset:10240\n"
      "ds_read_b128 a[48:51], v249 offset:8192\n"
      "ds_read_b128 a[52:55], v249 offset:10240\n"
      "s_waitcnt vmcnt(16)\n"
      "v_readfirstlane_b32 s37, v235\n"
      "s_nop 1\n"
      "s_mul_i32 s37, s37, 0x9E3779B1\n"
      "s_add_u32 s37, s37, 0x7F4A7C15\n"
      "s_mov_b32 s38, 0x8000\n"
      "s_waitcnt lgkmcnt(8)\n"
      "s_xor_b32 s45, s79, 128\n"
      "s_add_u32 s46, s8, s45\n"
      "s_addc_u32 s47, s9, 0\n"
      "s_add_u32 m0, s41, 0\n"
      "s_nop 0\n"
      "global_load_lds_dwordx4 v244, s[46:47] offset:0\n"
      "global_load_lds_dwordx4 v245, s[46:47] offset:1024\n"
      "global_load_lds_dwordx4 v246, s[46:47] offset:2048\n"
      "global_load_lds_dwordx4 v247, s[46:47] offset:3072\n"
      "s_min_u32 s43, s80, 27\n"
      "v_lshrrev_b32 v202, 3, v243\n"
      "v_and_b32 v203, 0x70, v242\n"
      "v_lshl_or_b32 v200, v202, 10, v203\n"
      "v_add_u32 v201, 7168, v200\n"
      "v_add_u32 v202, 14336, v200\n"
      "v_add_u32 v203, 21504, v200\n"
      "s_lshr_b32 s44, s33, 1\n"
      "s_lshl1_add_u32 s44, s43, s44\n"
      "s_lshl_b32 s44, s44, 15\n"
      "s_and_b32 s45, s33, 1\n"
      "s_lshl1_add_u32 s45, s81, s45\n"
      "s_lshl_b32 s45, s45, 7\n"
      "s_add_u32 s44, s44, s45\n"
      "s_add_u32 s70, s30, s44\n"
      "s_addc_u32 s71, s31, 0\n"
      "s_add_u32 m0, s41, 77824\n"
      "s_nop 0\n"
      "global_load_lds_dwordx4 v200, s[70:71] offset:0\n"
      "global_load_lds_dwordx4 v201, s[70:71] offset:1024\n"
      "global_load_lds_dwordx4 v202, s[70:71] offset:2048\n"
      "global_load_lds_dwordx4 v203, s[70:71] offset:3072\n"
      "s_waitcnt vmcnt(20)\n"
      "ds_read_b128 a[24:27], v248 offset:12288\n"
      "ds_read_b128 a[28:31], v248 offset:14336\n"
      "ds_read_b128 a[56:59], v249 offset:12288\n"
      "ds_read_b128 a[60:63], v249 offset:14336\n"
      "s_waitcnt lgkmcnt(8)\n"
      "s_xor_b32 s45, s76, 256\n"
      "s_add_u32 s46, s8, s45\n"
      "s_addc_u32 s47, s9, 0\n"
      "s_add_u32 m0, s41, 4096\n"
      "s_nop 0\n"
      "global_load_lds_dwordx4 v244, s[46:47] offset:0\n"
      "global_load_lds_dwordx4 v245, s[46:47] offset:1024\n"
      "global_load_lds_dwordx4 v246, s[46:47] offset:2048\n"
      "global_load_lds_dwordx4 v247, s[46:47] offset:3072\n"
      "s_waitcnt vmcnt(20)\n"
      "ds_read_b128 a[64:67], v250 offset:0\n"
      "ds_read_b128 a[68:71], v250 offset:2048\n"
      "ds_read_b128 a[96:99], v251 offset:0\n"
      "ds_read_b128 a[100:103], v251 offset:2048\n"
      "s_waitcnt lgkmcnt(8)\n"
      "s_xor_b32 s45, s77, 256\n"
      "s_add_u32 s46, s8, s45\n"
      "s_addc_u32 s47, s9, 0\n"
      "s_add_u32 m0, s41, 8192\n"
      "s_nop 0\n"
      "global_load_lds_dwordx4 v244, s[46:47] offset:0\n"
      "global_load_lds_dwordx4 v245, s[46:47] offset:1024\n"
      "global_load_lds_dwordx4 v246, s[46:47] offset:2048\n"
      "global_load_lds_dwordx4 v247, s[46:47] offset:3072\n"
      "s_waitcnt vmcnt(20)\n"
      "ds_read_b128 a[72:75], v250 offset:4096\n"
      "ds_read_b128 a[76:79], v250 offset:6144\n"
      "ds_read_b128 a[104:107], v251 offset:4096\n"
      "ds_read_b128 a[108:111], v251 offset:6144\n"
      "s_waitcnt lgkmcnt(8)\n"
      "s_xor_b32 s45, s78, 256\n"
      "s_add_u32 s46, s8, s45\n"
      "s_addc_u32 s47, s9, 0\n"
      "s_add_u32 m0, s41, 12288\n"
      "s_nop 0\n"
      "global_load_lds_dwordx4 v244, s[46:47] offset:0\n"
      "global_load_lds_dwordx4 v245, s[46:47] offset:1024\n"
      "global_load_lds_dwordx4 v246, s[46:47] offset:2048\n"
      "global_load_lds_dwordx4 v247, s[46:47] offset:3072\n"
      "s_waitcnt vmcnt(20)\n"
      "ds_read_b128 a[80:83], v250 offset:8192\n"
      "ds_read_b128 a[84:87], v250 offset:10240\n"
      "ds_read_b128 a[112:115], v251 offset:8192\n"
      "ds_read_b128 a[116:119], v251 offset:10240\n"
      "s_waitcnt vmcnt(16)\n"
      "ds_read_b128 a[88:91], v248 offset:0\n"
      "ds_read_b128 a[92:95], v248 offset:2048\n"
      "ds_read_b128 a[120:123], v249 offset:0\n"
      "ds_read_b128 a[124:127], v249 offset:2048\n"
      "global_load_dword v252, v253, s[58:59] offset:0 sc1\n"
      "s_waitcnt lgkmcnt(12)\n"
      "s_xor_b32 s45, s79, 256\n"
      "s_add_u32 s46, s8, s45\n"
      "s_addc_u32 s47, s9, 0\n"
      "s_add_u32 m0, s41, 65536\n"
      "s_nop 0\n"
      "global_load_lds_dwordx4 v244, s[46:47] offset:0\n"
      "global_load_lds_dwordx4 v245, s[46:47] offset:1024\n"
      "global_load_lds_dwordx4 v246, s[46:47] offset:2048\n"
      "global_load_lds_dwordx4 v247, s[46:47] offset:3072\n"
      "s_waitcnt lgkmcnt(8)\n"
      "s_xor_b32 s45, s76, 384\n"
      "s_add_u32 s46, s8, s45\n"
      "s_addc_u32 s47, s9, 0\n"
      "s_add_u32 m0, s41, 69632\n"
      "s_nop 0\n"
      "global_load_lds_dwordx4 v244, s[46:47] offset:0\n"
      "global_load_lds_dwordx4 v245, s[46:47] offset:1024\n"
      "global_load_lds_dwordx4 v246, s[46:47] offset:2048\n"
      "global_load_lds_dwordx4 v247, s[46:47] offset:3072\n"
      "s_waitcnt vmcnt(17)\n"
      "ds_read_b128 a[128:131], v248 offset:4096\n"
      "ds_read_b128 a[132:135], v248 offset:6144\n"
      "ds_read_b128 a[160:163], v249 offset:4096\n"
      "ds_read_b128 a[164:167], v249 offset:6144\n"
      "s_waitcnt vmcnt(13)\n"
      "ds_read_b128 a[136:139], v248 offset:8192\n"
      "ds_read_b128 a[140:143], v248 offset:10240\n"
      "ds_read_b128 a[168:171], v249 offset:8192\n"
      "ds_read_b128 a[172:175], v249 offset:10240\n"
      "s_waitcnt vmcnt(8)\n"
      "v_cmp_ne_u32 vcc, s37, v252\n"
      "s_nop 1\n"
      "s_cbranch_vccnz .Ls_s3_%=\n"
      ".Ls_o4_%=:\n"
      "global_load_dwordx2 v[206:207], v204, s[58:59] sc1\n"
      "global_load_dwordx4 v[128:131], v240, s[10:11] offset:-4096 sc1\n"
      "global_load_dwordx4 v[132:135], v240, s[10:11] offset:-3072 sc1\n"
      "global_load_dwordx4 v[136:139], v240, s[10:11] offset:-2048 sc1\n"
      "global_load_dwordx4 v[140:143], v240, s[72:73] offset:-4096 sc1\n"
      "global_load_dwordx4 v[144:147], v240, s[72:73] offset:-3072 sc1\n"
      "global_load_dwordx4 v[148:151], v240, s[72:73] offset:-2048 sc1\n"
      "s_waitcnt vmcnt(5)\n"
      "v_mfma_f32_16x16x32_bf16 v[0:3], v[128:131], a[0:3], 0\n"
      "global_load_dwordx4 v[152:155], v241, s[10:11] offset:-4096 sc1\n"
      "v_cmp_ne_u32 vcc, s37, v207\n"
      "s_nop 1\n"
      "s_cbranch_vccnz .Ls_s6_%=\n"
      ".Ls_o7_%=:\n"
      "v_mfma_f32_16x16x32_bf16 v[4:7], v[128:131], a[4:7], 0\n"
      "v_max_u32_dpp v206, v206, v206 quad_perm:[1,0,3,2] row_mask:0xf bank_mask:0xf\n"
      "s_nop 1\n"
      "v_max_u32_dpp v206, v206, v206 quad_perm:[2,3,0,1] row_mask:0xf bank_mask:0xf\n"
      "v_mfma_f32_16x16x32_bf16 v[8:11], v[128:131], a[8:11], 0\n"
      "global_load_dwordx4 v[156:159], v241, s[10:11] offset:-3072 sc1\n"
      "v_max_u32_dpp v206, v206, v206 row_half_mirror row_mask:0xf bank_mask:0xf\n"
      "v_mfma_f32_16x16x32_bf16 v[12:15], v[128:131], a[12:15], 0\n"
      "s_nop 0\n"
      "v_max_u32_dpp v206, v206, v206 row_mirror row_mask:0xf bank_mask:0xf\n"
      "v_mfma_f32_16x16x32_bf16 v[16:19], v[128:131], a[16:19], 0\n"
      "global_load_dwordx4 v[160:163], v241, s[10:11] offset:-2048 sc1\n"
      "v_mov_b32 v205, v206\n"
      "v_mfma_f32_16x16x32_bf16 v[20:23], v[128:131], a[20:23], 0\n"
      "s_nop 0\n"
      "v_permlane16_swap_b32 v206, v205\n"
      "v_mfma_f32_16x16x32_bf16 v[24:27], v[128:131], a[24:27], 0\n"
      "global_load_dwordx4 v[164:167], v241, s[72:73] offset:-4096 sc1\n"
      "v_max_u32 v206, v206, v205\n"
      "v_mfma_f32_16x16x32_bf16 v[28:31], v[128:131], a[28:31], 0\n"
      "v_mov_b32 v205, v206\n"
      "s_nop 1\n"
      "v_permlane32_swap_b32 v206, v205\n"
      "s_waitcnt vmcnt(8)\n"
      "v_mfma_f32_16x16x32_bf16 v[32:35], v[132:135], a[0:3], 0\n"
      "global_load_dwordx4 v[168:171], v241, s[72:73] offset:-3072 sc1\n"
      "v_max_u32 v206, v206, v205\n"
      "v_mfma_f32_16x16x32_bf16 v[36:39], v[132:135], a[4:7], 0\n"
      "v_readfirstlane_b32 s40, v206\n"
      "s_nop 1\n"
      "s_andn2_b32 s42, 0x7ff, s40\n"
      "v_mfma_f32_16x16x32_bf16 v[40:43], v[132:135], a[8:11], 0\n"
      "global_load_dwordx4 v[172:175], v241, s[72:73] offset:-2048 sc1\n"
      "s_lshr_b32 s43, s42, 5\n"
      "s_lshl2_add_u32 s44, s36, s33\n"
      "s_cmp_eq_u32 s43, s44\n"
      "s_cbranch_scc0 .Ls_e8_%=\n"
      "s_lshl_b32 s43, s42, 10\n"
      "v_add_u32 v197, s43, v242\n"
      "global_load_dwordx4 v[232:235], v197, s[16:17]\n"
      "global_load_dwordx4 v[236:239], v242, s[24:25]\n"
      ".Ls_e8_%=:\n"
      "v_mfma_f32_16x16x32_bf16 v[44:47], v[132:135], a[12:15], 0\n"
      "s_lshl2_add_u32 s54, s36, s33\n"
      "s_lshl_b32 s55, s54, 15\n"
      "s_lshl2_add_u32 s56, s35, s34\n"
      "v_mfma_f32_16x16x32_bf16 v[48:51], v[132:135], a[16:19], 0\n"
      "s_lshl_b32 s56, s56, 6\n"
      "global_load_dwordx4 v[176:179], v240, s[14:15] offset:-4096 sc1\n"
      "s_add_u32 s55, s55, s56\n"
      "v_mfma_f32_16x16x32_bf16 v[52:55], v[132:135], a[20:23], 0\n"
      "v_and_b32 v198, 15, v243\n"
      "v_and_b32 v199, 48, v243\n"
      "v_mfma_f32_16x16x32_bf16 v[56:59], v[132:135], a[24:27], 0\n"
      "global_load_dwordx4 v[180:183], v240, s[14:15] offset:-3072 sc1\n"
      "v_lshl_or_b32 v199, v198, 10, v199\n"
      "v_mfma_f32_16x16x32_bf16 v[60:63], v[132:135], a[28:31], 0\n"
      "s_waitcnt vmcnt(10)\n"
      "v_add_u32 v199, s55, v199\n"
      "v_add_u32 v197, 16384, v199\n"
      "v_mfma_f32_16x16x32_bf16 v[64:67], v[136:139], a[0:3], 0\n"
      "global_load_dwordx4 v[184:187], v240, s[14:15] offset:-2048 sc1\n"
      "global_load_dwordx4 v[188:191], v240, s[74:75] offset:-4096 sc1\n"
      "v_mfma_f32_16x16x32_bf16 v[68:71], v[136:139], a[4:7], 0\n"
      "global_load_dwordx4 v[128:131], v240, s[74:75] offset:-3072 sc1\n"
      "global_load_dwordx4 v[132:135], v240, s[74:75] offset:-2048 sc1\n"
      "v_mfma_f32_16x16x32_bf16 v[72:75], v[136:139], a[8:11], 0\n"
      "s_waitcnt lgkmcnt(12)\n"
      "s_xor_b32 s70, s77, 384\n"
      "s_add_u32 s70, s8, s70\n"
      "s_addc_u32 s71, s9, 0\n"
      "s_add_u32 m0, s41, 73728\n"
      "v_mfma_f32_16x16x32_bf16 v[76:79], v[136:139], a[12:15], 0\n"
      "global_load_lds_dwordx4 v244, s[70:71] offset:0\n"
      "global_load_dwordx4 v[224:227], v199, s[16:17]\n"
      "v_mfma_f32_16x16x32_bf16 v[80:83], v[136:139], a[16:19], 0\n"
      "global_load_dwordx4 v[228:231], v197, s[16:17]\n"
      "s_lshr_b32 s55, s34, 1\n"
      "v_mfma_f32_16x16x32_bf16 v[84:87], v[136:139], a[20:23], 0\n"
      "s_lshl2_add_u32 s55, s55, s35\n"
      "global_load_lds_dwordx4 v245, s[70:71] offset:1024\n"
      "s_lshl_b32 s55, s55, 15\n"
      "v_mfma_f32_16x16x32_bf16 v[88:91], v[136:139], a[24:27], 0\n"
      "s_lshl_b32 s54, s54, 9\n"
      "s_add_u32 s54, s54, s55\n"
      "s_add_u32 s54, s54, 786432\n"
      "v_mfma_f32_16x16x32_bf16 v[92:95], v[136:139], a[28:31], 0\n"
      "global_load_lds_dwordx4 v246, s[70:71] offset:2048\n"
      "v_lshl_add_u32 v198, v198, 4, s54\n"
      "v_mfma_f32_16x16x32_bf16 v[0:3], v[140:143], a[32:35], v[0:3]\n"
      "global_load_dwordx4 v[136:139], v241, s[14:15] offset:-4096 sc1\n"
      "global_load_dwordx2 v[204:205], v198, s[58:59] offset:0 sc1\n"
      "v_mfma_f32_16x16x32_bf16 v[4:7], v[140:143], a[36:39], v[4:7]\n"
      "global_load_lds_dwordx4 v247, s[70:71] offset:3072\n"
      "global_load_dwordx2 v[206:207], v198, s[58:59] offset:256 sc1\n"
      "v_mfma_f32_16x16x32_bf16 v[8:11], v[140:143], a[40:43], v[8:11]\n"
      "s_add_u32 s68, s20, 16384\n"
      "s_addc_u32 s69, s21, 0\n"
      "s_waitcnt lgkmcnt(8)\n"
      "s_xor_b32 s70, s78, 384\n"
      "s_add_u32 s70, s8, s70\n"
      "s_addc_u32 s71, s9, 0\n"
      "s_add_u32 m0, s41, 0\n"
      "v_mfma_f32_16x16x32_bf16 v[12:15], v[140:143], a[44:47], v[12:15]\n"
      "s_ashr_i32 s54, s40, 31\n"
      "global_load_lds_dwordx4 v244, s[70:71] offset:0\n"
      "s_not_b32 s54, s54\n"
      "v_mfma_f32_16x16x32_bf16 v[16:19], v[140:143], a[48:51], v[16:19]\n"
      "s_or_b32 s54, s54, 0x80000000\n"
      "s_xor_b32 s54, s54, s40\n"
      "s_and_b32 s54, s54, 0xffff0000\n"
      "v_mfma_f32_16x16x32_bf16 v[20:23], v[140:143], a[52:55], v[20:23]\n"
      "global_load_lds_dwordx4 v245, s[70:71] offset:1024\n"
      "v_mov_b32 v192, s54\n"
      "v_mfma_f32_16x16x32_bf16 v[24:27], v[140:143], a[56:59], v[24:27]\n"
      "ds_read_b128 a[144:147], v248 offset:12288\n"
      "v_mul_f32 v192, 0xbfb8aa3b, v192\n"
      "v_mfma_f32_16x16x32_bf16 v[28:31], v[140:143], a[60:63], v[28:31]\n"
      "s_waitcnt vmcnt(23)\n"
      "global_load_lds_dwordx4 v246, s[70:71] offset:2048\n"
      "v_exp_f32 v192, v192\n"
      "v_mfma_f32_16x16x32_bf16 v[32:35], v[144:147], a[32:35], v[32:35]\n"
      "global_load_dwordx4 v[140:143], v241, s[74:75] offset:-4096 sc1\n"
      "v_add_f32 v192, 1.0, v192\n"
      "v_mfma_f32_16x16x32_bf16 v[36:39], v[144:147], a[36:39], v[36:39]\n"
      "global_load_lds_dwordx4 v247, s[70:71] offset:3072\n"
      "v_rcp_f32 v192, v192\n"
      "v_mfma_f32_16x16x32_bf16 v[40:43], v[144:147], a[40:43], v[40:43]\n"
      "s_waitcnt lgkmcnt(5)\n"
      "s_xor_b32 s70, s79, 384\n"
      "s_add_u32 s70, s8, s70\n"
      "s_addc_u32 s71, s9, 0\n"
      "s_add_u32 m0, s41, 4096\n"
      "v_mfma_f32_16x16x32_bf16 v[44:47], v[144:147], a[44:47], v[44:47]\n"
      "global_load_lds_dwordx4 v244, s[70:71] offset:0\n"
      "v_cvt_pk_bf16_f32 v192, v192, v192\n"
      "v_mfma_f32_16x16x32_bf16 v[48:51], v[144:147], a[48:51], v[48:51]\n"
      "ds_read_b128 a[148:151], v248 offset:14336\n"
      "v_and_b32 v192, 0xffff0000, v192\n"
      "v_mfma_f32_16x16x32_bf16 v[52:55], v[144:147], a[52:55], v[52:55]\n"
      "global_load_lds_dwordx4 v245, s[70:71] offset:1024\n"
      "v_readfirstlane_b32 s84, v192\n"
      "v_mfma_f32_16x16x32_bf16 v[56:59], v[144:147], a[56:59], v[56:59]\n"
      "ds_read_b128 a[176:179], v249 offset:12288\n"
      "ds_read_b128 a[180:183], v249 offset:14336\n"
      "v_mfma_f32_16x16x32_bf16 v[60:63], v[144:147], a[60:63], v[60:63]\n"
      "global_load_lds_dwordx4 v246, s[70:71] offset:2048\n"
      "global_load_dwordx4 v[144:147], v241, s[14:15] offset:-3072 sc1\n"
      "v_mfma_f32_16x16x32_bf16 v[64:67], v[148:151], a[32:35], v[64:67]\n"
      "ds_read_b128 a[152:155], v250 offset:0\n"
      "ds_read_b128 a[156:159], v250 offset:2048\n"
      "v_mfma_f32_16x16x32_bf16 v[68:71], v[148:151], a[36:39], v[68:71]\n"
      "global_load_lds_dwordx4 v247, s[70:71] offset:3072\n"
      "ds_read_b128 a[184:187], v251 offset:0\n"
      "v_mfma_f32_16x16x32_bf16 v[72:75], v[148:151], a[40:43], v[72:75]\n"
      "ds_read_b128 a[188:191], v251 offset:2048\n"
      "ds_read_b128 a[192:195], v250 offset:4096\n"
      "v_mfma_f32_16x16x32_bf16 v[76:79], v[148:151], a[44:47], v[76:79]\n"
      "ds_read_b128 a[196:199], v250 offset:6144\n"
      "ds_read_b128 a[224:227], v251 offset:4096\n"
      "v_mfma_f32_16x16x32_bf16 v[80:83], v[148:151], a[48:51], v[80:83]\n"
      "ds_read_b128 a[228:231], v251 offset:6144\n"
      "v_mfma_f32_16x16x32_bf16 v[84:87], v[148:151], a[52:55], v[84:87]\n"
      "v_mfma_f32_16x16x32_bf16 v[88:91], v[148:151], a[56:59], v[88:91]\n"
      "v_mfma_f32_16x16x32_bf16 v[92:95], v[148:151], a[60:63], v[92:95]\n"
      "s_waitcnt vmcnt(29)\n"
      "global_load_dwordx4 v[148:151], v241, s[74:75] offset:-3072 sc1\n"
      "v_mfma_f32_16x16x32_bf16 v[0:3], v[152:155], a[64:67], v[0:3]\n"
      "v_mfma_f32_16x16x32_bf16 v[4:7], v[152:155], a[68:71], v[4:7]\n"
      "v_mfma_f32_16x16x32_bf16 v[8:11], v[152:155], a[72:75], v[8:11]\n"
      "v_mfma_f32_16x16x32_bf16 v[12:15], v[152:155], a[76:79], v[12:15]\n"
      "v_mfma_f32_16x16x32_bf16 v[16:19], v[152:155], a[80:83], v[16:19]\n"
      "v_mfma_f32_16x16x32_bf16 v[20:23], v[152:155], a[84:87], v[20:23]\n"
      "v_mfma_f32_16x16x32_bf16 v[24:27], v[152:155], a[88:91], v[24:27]\n"
      "v_mfma_f32_16x16x32_bf16 v[28:31], v[152:155], a[92:95], v[28:31]\n"
      "global_load_dwordx4 v[152:155], v241, s[14:15] offset:-2048 sc1\n"
      "s_waitcnt vmcnt(13)\n"
      "ds_read_b128 a[200:203], v250 offset:8192\n"
      "v_mfma_f32_16x16x32_bf16 v[32:35], v[156:159], a[64:67], v[32:35]\n"
      "ds_read_b128 a[204:207], v250 offset:10240\n"
      "ds_read_b128 a[232:235], v251 offset:8192\n"
      "v_mfma_f32_16x16x32_bf16 v[36:39], v[156:159], a[68:71], v[36:39]\n"
      "ds_read_b128 a[236:239], v251 offset:10240\n"
      "v_mfma_f32_16x16x32_bf16 v[40:43], v[156:159], a[72:75], v[40:43]\n"
      "v_mfma_f32_16x16x32_bf16 v[44:47], v[156:159], a[76:79], v[44:47]\n"
      "v_mfma_f32_16x16x32_bf16 v[48:51], v[156:159], a[80:83], v[48:51]\n"
      "v_mfma_f32_16x16x32_bf16 v[52:55], v[156:159], a[84:87], v[52:55]\n"
      "v_mfma_f32_16x16x32_bf16 v[56:59], v[156:159], a[88:91], v[56:59]\n"
      "v_mfma_f32_16x16x32_bf16 v[60:63], v[156:159], a[92:95], v[60:63]\n"
      "global_load_dwordx4 v[156:159], v241, s[74:75] offset:-2048 sc1\n"
      "s_waitcnt vmcnt(8)\n"
      "ds_read_b128 a[208:211], v248 offset:0\n"
      "v_mfma_f32_16x16x32_bf16 v[64:67], v[160:163], a[64:67], v[64:67]\n"
      "ds_read_b128 a[212:215], v248 offset:2048\n"
      "ds_read_b128 a[240:243], v249 offset:0\n"
      "v_mfma_f32_16x16x32_bf16 v[68:71], v[160:163], a[68:71], v[68:71]\n"
      "ds_read_b128 a[244:247], v249 offset:2048\n"
      "v_mfma_f32_16x16x32_bf16 v[72:75], v[160:163], a[72:75], v[72:75]\n"
      "v_mfma_f32_16x16x32_bf16 v[76:79], v[160:163], a[76:79], v[76:79]\n"
      "v_mfma_f32_16x16x32_bf16 v[80:83], v[160:163], a[80:83], v[80:83]\n"
      "v_mfma_f32_16x16x32_bf16 v[84:87], v[160:163], a[84:87], v[84:87]\n"
      "v_mfma_f32_16x16x32_bf16 v[88:91], v[160:163], a[88:91], v[88:91]\n"
      "s_min_u32 s43, s80, 27\n"
      "v_lshrrev_b32 v202, 3, v243\n"
      "v_and_b32 v203, 0x70, v242\n"
      "s_movk_i32 s44, 3584\n"
      "v_mad_u32_u24 v200, v202, s44, v203\n"
      "v_add_u32 v201, 27648, v200\n"
      "v_add_u32 v202, 55296, v200\n"
      "v_add_u32 v203, 82944, v200\n"
      "s_bitcmp1_b32 s33, 0\n"
      "s_cselect_b64 s[70:71], s[28:29], s[26:27]\n"
      "s_mul_i32 s44, s83, 229376\n"
      "s_lshl_b32 s45, s43, 7\n"
      "s_add_u32 s44, s44, s45\n"
      "s_add_u32 s70, s70, s44\n"
      "s_addc_u32 s71, s71, 0\n"
      "s_lshl_b32 s45, s33, 13\n"
      "s_add_u32 m0, s45, 131072\n"
      "v_mfma_f32_16x16x32_bf16 v[92:95], v[160:163], a[92:95], v[92:95]\n"
      "global_load_lds_dwordx4 v200, s[70:71] offset:0\n"
      "s_waitcnt vmcnt(4)\n"
      "ds_read_b128 a[216:219], v248 offset:4096\n"
      "v_mfma_f32_16x16x32_bf16 v[0:3], v[164:167], a[96:99], v[0:3]\n"
      "global_load_lds_dwordx4 v201, s[70:71] offset:1024\n"
      "ds_read_b128 a[220:223], v248 offset:6144\n"
      "v_mfma_f32_16x16x32_bf16 v[4:7], v[164:167], a[100:103], v[4:7]\n"
      "global_load_lds_dwordx4 v202, s[70:71] offset:2048\n"
      "ds_read_b128 a[248:251], v249 offset:4096\n"
      "v_mfma_f32_16x16x32_bf16 v[8:11], v[164:167], a[104:107], v[8:11]\n"
      "global_load_lds_dwordx4 v203, s[70:71] offset:3072\n"
      "ds_read_b128 a[252:255], v249 offset:6144\n"
      "v_mfma_f32_16x16x32_bf16 v[12:15], v[164:167], a[108:111], v[12:15]\n"
      "s_add_u32 s70, s70, 114688\n"
      "s_addc_u32 s71, s71, 0\n"
      "s_add_u32 m0, m0, 4096\n"
      "v_mfma_f32_16x16x32_bf16 v[16:19], v[164:167], a[112:115], v[16:19]\n"
      "global_load_lds_dwordx4 v200, s[70:71] offset:0\n"
      "v_mfma_f32_16x16x32_bf16 v[20:23], v[164:167], a[116:119], v[20:23]\n"
      "global_load_lds_dwordx4 v201, s[70:71] offset:1024\n"
      "v_mfma_f32_16x16x32_bf16 v[24:27], v[164:167], a[120:123], v[24:27]\n"
      "global_load_lds_dwordx4 v202, s[70:71] offset:2048\n"
      "v_mfma_f32_16x16x32_bf16 v[28:31], v[164:167], a[124:127], v[28:31]\n"
      "global_load_lds_dwordx4 v203, s[70:71] offset:3072\n"
      "s_sub_u32 s48, s33, 1\n"
      "v_mfma_f32_16x16x32_bf16 v[32:35], v[168:171], a[96:99], v[32:35]\n"
      "s_and_b32 s48, s48, 3\n"
      "global_load_dword v252, v253, s[58:59] offset:128 sc1\n"
      "s_lshl_b32 s48, s48, 14\n"
      "v_mfma_f32_16x16x32_bf16 v[36:39], v[168:171], a[100:103], v[36:39]\n"
      "v_lshl_add_u32 v244, v243, 2, s48\n"
      "s_sub_u32 s48, s33, 2\n"
      "v_mfma_f32_16x16x32_bf16 v[40:43], v[168:171], a[104:107], v[40:43]\n"
      "s_and_b32 s48, s48, 3\n"
      "s_lshl_b32 s48, s48, 14\n"
      "v_lshl_add_u32 v245, v243, 2, s48\n"
      "v_mfma_f32_16x16x32_bf16 v[44:47], v[168:171], a[108:111], v[44:47]\n"
      "s_sub_u32 s48, s33, 3\n"
      "s_and_b32 s48, s48, 3\n"
      "s_lshl_b32 s48, s48, 14\n"
      "v_mfma_f32_16x16x32_bf16 v[48:51], v[168:171], a[112:115], v[48:51]\n"
      "v_lshl_add_u32 v246, v243, 2, s48\n"
      "v_mfma_f32_16x16x32_bf16 v[52:55], v[168:171], a[116:119], v[52:55]\n"
      "v_mfma_f32_16x16x32_bf16 v[56:59], v[168:171], a[120:123], v[56:59]\n"
      "v_mfma_f32_16x16x32_bf16 v[60:63], v[168:171], a[124:127], v[60:63]\n"
      "v_bitop3_b32 v197, v205, v207, s37 bitop3:0x7e\n"
      "v_cmp_ne_u32 vcc, 0, v197\n"
      "s_nop 1\n"
      "s_cbranch_vccnz .Ls_s10_%=\n"
      ".Ls_o11_%=:\n"
      "v_mfma_f32_16x16x32_bf16 v[64:67], v[172:175], a[96:99], v[64:67]\n"
      "s_lshr_b32 s43, s42, 5\n"
      "s_lshl2_add_u32 s44, s36, s33\n"
      "s_cmp_eq_u32 s43, s44\n"
      "s_cbranch_scc0 .Ls_e12_%=\n"
      "v_lshlrev_b32 v192, 16, v232\n"
      "v_lshlrev_b32 v193, 16, v236\n"
      "v_and_b32 v194, 0xffff0000, v232\n"
      "v_and_b32 v195, 0xffff0000, v236\n"
      "v_mul_f32 v192, v192, v193\n"
      "v_mul_f32 v194, v194, v195\n"
      "v_cvt_pk_bf16_f32 v192, v192, v194\n"
      "v_lshlrev_b32 v193, 16, v192\n"
      "v_and_b32 v192, 0xffff0000, v192\n"
      "v_add_f32 v196, v192, v193\n"
      "v_lshlrev_b32 v192, 16, v233\n"
      "v_lshlrev_b32 v193, 16, v237\n"
      "v_and_b32 v194, 0xffff0000, v233\n"
      "v_and_b32 v195, 0xffff0000, v237\n"
      "v_mul_f32 v192, v192, v193\n"
      "v_mul_f32 v194, v194, v195\n"
      "v_cvt_pk_bf16_f32 v192, v192, v194\n"
      "v_lshlrev_b32 v193, 16, v192\n"
      "v_and_b32 v192, 0xffff0000, v192\n"
      "v_add_f32 v192, v192, v193\n"
      "v_add_f32 v196, v196, v192\n"
      "v_lshlrev_b32 v192, 16, v234\n"
      "v_lshlrev_b32 v193, 16, v238\n"
      "v_and_b32 v194, 0xffff0000, v234\n"
      "v_and_b32 v195, 0xffff0000, v238\n"
      "v_mul_f32 v192, v192, v193\n"
      "v_mul_f32 v194, v194, v195\n"
      "v_cvt_pk_bf16_f32 v192, v192, v194\n"
      "v_lshlrev_b32 v193, 16, v192\n"
      "v_and_b32 v192, 0xffff0000, v192\n"
      "v_add_f32 v192, v192, v193\n"
      "v_add_f32 v196, v196, v192\n"
      "v_lshlrev_b32 v192, 16, v235\n"
      "v_lshlrev_b32 v193, 16, v239\n"
      "v_and_b32 v194, 0xffff0000, v235\n"
      "v_and_b32 v195, 0xffff0000, v239\n"
      "v_mul_f32 v192, v192, v193\n"
      "v_mul_f32 v194, v194, v195\n"
      "v_cvt_pk_bf16_f32 v192, v192, v194\n"
      "v_lshlrev_b32 v193, 16, v192\n"
      "v_and_b32 v192, 0xffff0000, v192\n"
      "v_add_f32 v192, v192, v193\n"
      "v_add_f32 v196, v196, v192\n"
      "s_nop 1\n"
      "v_add_f32_dpp v196, v196, v196 quad_perm:[1,0,3,2] row_mask:0xf bank_mask:0xf\n"
      "s_nop 1\n"
      "v_add_f32_dpp v196, v196, v196 quad_perm:[2,3,0,1] row_mask:0xf bank_mask:0xf\n"
      "s_nop 1\n"
      "v_add_f32_dpp v196, v196, v196 row_half_mirror row_mask:0xf bank_mask:0xf\n"
      "s_nop 1\n"
      "v_add_f32_dpp v196, v196, v196 row_mirror row_mask:0xf bank_mask:0xf\n"
      "v_mov_b32 v192, v196\n"
      "s_nop 1\n"
      "v_permlane16_swap_b32 v196, v192\n"
      "v_add_f32 v196, v196, v192\n"
      "v_mov_b32 v192, v196\n"
      "s_nop 1\n"
      "v_permlane32_swap_b32 v196, v192\n"
      "v_add_f32 v196, v196, v192\n"
      "v_cvt_pk_bf16_f32 v196, v196, v196\n"
      "v_and_b32 v196, 0xffff0000, v196\n"
      "v_mul_f32 v196, 0xbfb8aa3b, v196\n"
      "v_exp_f32 v196, v196\n"
      "v_lshrrev_b32 v192, 10, v199\n"
      "v_add_f32 v196, 1.0, v196\n"
      "v_rcp_f32 v196, v196\n"
      "v_subrev_u32 v192, s42, v192\n"
      "v_cvt_pk_bf16_f32 v196, v196, v196\n"
      "v_and_b32 v196, 0xffff0000, v196\n"
      "v_cmp_eq_u32 vcc, 0, v192\n"
      "s_nop 1\n"
      "v_cndmask_b32 v204, v204, v196, vcc\n"
      "v_cmp_eq_u32 vcc, -16, v192\n"
      "s_nop 1\n"
      "v_cndmask_b32 v206, v206, v196, vcc\n"
      ".Ls_e12_%=:\n"
      "v_mfma_f32_16x16x32_bf16 v[68:71], v[172:175], a[100:103], v[68:71]\n"
      "v_sub_f32 v205, 1.0, v204\n"
      "v_cvt_pk_bf16_f32 v205, v205, v205\n"
      "v_mfma_f32_16x16x32_bf16 v[72:75], v[172:175], a[104:107], v[72:75]\n"
      "v_and_b32 v205, 0xffff0000, v205\n"
      "v_lshlrev_b32 v208, 16, v224\n"
      "v_mfma_f32_16x16x32_bf16 v[76:79], v[172:175], a[108:111], v[76:79]\n"
      "v_and_b32 v209, 0xffff0000, v224\n"
      "v_mul_f32 v208, v205, v208\n"
      "v_mfma_f32_16x16x32_bf16 v[80:83], v[172:175], a[112:115], v[80:83]\n"
      "v_mul_f32 v209, v205, v209\n"
      "v_lshlrev_b32 v210, 16, v225\n"
      "v_mfma_f32_16x16x32_bf16 v[84:87], v[172:175], a[116:119], v[84:87]\n"
      "v_and_b32 v211, 0xffff0000, v225\n"
      "v_mul_f32 v210, v205, v210\n"
      "v_mfma_f32_16x16x32_bf16 v[88:91], v[172:175], a[120:123], v[88:91]\n"
      "v_mul_f32 v211, v205, v211\n"
      "v_lshlrev_b32 v216, 16, v226\n"
      "v_mfma_f32_16x16x32_bf16 v[92:95], v[172:175], a[124:127], v[92:95]\n"
      "v_and_b32 v217, 0xffff0000, v226\n"
      "v_mul_f32 v216, v205, v216\n"
      "v_mfma_f32_16x16x32_bf16 v[0:3], v[176:179], a[128:131], v[0:3]\n"
      "v_mul_f32 v217, v205, v217\n"
      "v_lshlrev_b32 v218, 16, v227\n"
      "v_mfma_f32_16x16x32_bf16 v[4:7], v[176:179], a[132:135], v[4:7]\n"
      "v_and_b32 v219, 0xffff0000, v227\n"
      "v_mul_f32 v218, v205, v218\n"
      "s_waitcnt lgkmcnt(15)\n"
      "v_mfma_f32_16x16x32_bf16 v[8:11], v[176:179], a[136:139], v[8:11]\n"
      "v_mul_f32 v219, v205, v219\n"
      "v_sub_f32 v207, 1.0, v206\n"
      "v_mfma_f32_16x16x32_bf16 v[12:15], v[176:179], a[140:143], v[12:15]\n"
      "v_cvt_pk_bf16_f32 v207, v207, v207\n"
      "v_and_b32 v207, 0xffff0000, v207\n"
      "v_mfma_f32_16x16x32_bf16 v[16:19], v[176:179], a[144:147], v[16:19]\n"
      "v_lshlrev_b32 v212, 16, v228\n"
      "v_and_b32 v213, 0xffff0000, v228\n"
      "v_mfma_f32_16x16x32_bf16 v[20:23], v[176:179], a[148:151], v[20:23]\n"
      "v_mul_f32 v212, v207, v212\n"
      "v_mul_f32 v213, v207, v213\n"
      "v_mfma_f32_16x16x32_bf16 v[24:27], v[176:179], a[152:155], v[24:27]\n"
      "v_lshlrev_b32 v214, 16, v229\n"
      "v_and_b32 v215, 0xffff0000, v229\n"
      "v_mfma_f32_16x16x32_bf16 v[28:31], v[176:179], a[156:159], v[28:31]\n"
      "v_mul_f32 v214, v207, v214\n"
      "v_mul_f32 v215, v207, v215\n"
      "v_mfma_f32_16x16x32_bf16 v[32:35], v[180:183], a[128:131], v[32:35]\n"
      "v_lshlrev_b32 v220, 16, v230\n"
      "v_and_b32 v221, 0xffff0000, v230\n"
      "v_mfma_f32_16x16x32_bf16 v[36:39], v[180:183], a[132:135], v[36:39]\n"
      "v_mul_f32 v220, v207, v220\n"
      "v_mul_f32 v221, v207, v221\n"
      "v_mfma_f32_16x16x32_bf16 v[40:43], v[180:183], a[136:139], v[40:43]\n"
      "v_lshlrev_b32 v222, 16, v231\n"
      "v_and_b32 v223, 0xffff0000, v231\n"
      "v_mfma_f32_16x16x32_bf16 v[44:47], v[180:183], a[140:143], v[44:47]\n"
      "v_mul_f32 v222, v207, v222\n"
      "v_mul_f32 v223, v207, v223\n"
      "v_mfma_f32_16x16x32_bf16 v[48:51], v[180:183], a[144:147], v[48:51]\n"
      "s_lshl_b32 s54, s42, 10\n"
      "s_lshl_b32 s55, s83, 7\n"
      "s_add_u32 s54, s54, s55\n"
      "v_mfma_f32_16x16x32_bf16 v[52:55], v[180:183], a[148:151], v[52:55]\n"
      "v_and_b32 v224, 48, v243\n"
      "v_add_u32 v224, s54, v224\n"
      "v_mfma_f32_16x16x32_bf16 v[56:59], v[180:183], a[152:155], v[56:59]\n"
      "global_load_dwordx4 v[192:195], v224, s[16:17]\n"
      "global_load_dwordx4 v[196:199], v224, s[16:17] offset:64\n"
      "v_mfma_f32_16x16x32_bf16 v[60:63], v[180:183], a[156:159], v[60:63]\n"
      "v_lshrrev_b32 v201, 4, v243\n"
      "v_bfe_u32 v224, v243, 2, 2\n"
      "v_mfma_f32_16x16x32_bf16 v[64:67], v[184:187], a[128:131], v[64:67]\n"
      "v_lshl_or_b32 v201, v201, 3, v224\n"
      "v_and_b32 v224, 3, v243\n"
      "v_mfma_f32_16x16x32_bf16 v[68:71], v[184:187], a[132:135], v[68:71]\n"
      "v_lshlrev_b32 v224, 3, v224\n"
      "v_lshl_or_b32 v201, v201, 7, v224\n"
      "v_mfma_f32_16x16x32_bf16 v[72:75], v[184:187], a[136:139], v[72:75]\n"
      "s_mov_b32 s64, 0xffff0000\n"
      "s_mov_b32 s65, 0xffff0000\n"
      "s_mov_b32 s66, -1\n"
      "v_mfma_f32_16x16x32_bf16 v[76:79], v[184:187], a[140:143], v[76:79]\n"
      "s_mov_b32 s67, 0\n"
      "s_lshl_b32 s54, s33, 13\n"
      "s_add_u32 s54, s54, 131072\n"
      "v_mfma_f32_16x16x32_bf16 v[80:83], v[184:187], a[144:147], v[80:83]\n"
      "v_add_u32 v202, s54, v201\n"
      "s_and_b32 s54, s33, 1\n"
      "v_mfma_f32_16x16x32_bf16 v[84:87], v[184:187], a[148:151], v[84:87]\n"
      "s_lshl3_add_u32 s54, s54, s83\n"
      "s_lshl_b32 s54, s54, 8\n"
      "s_lshl_b32 s55, s80, 13\n"
      "v_mfma_f32_16x16x32_bf16 v[88:91], v[184:187], a[152:155], v[88:91]\n"
      "s_add_u32 s55, s55, 262144\n"
      "s_add_u32 s54, s54, s55\n"
      "v_bfe_u32 v224, v243, 4, 1\n"
      "v_mfma_f32_16x16x32_bf16 v[92:95], v[184:187], a[156:159], v[92:95]\n"
      "v_and_b32 v203, 0xf0, v242\n"
      "v_lshl_or_b32 v200, v224, 12, v203\n"
      "v_mfma_f32_16x16x32_bf16 v[0:3], v[188:191], a[160:163], v[0:3]\n"
      "v_add_u32 v200, s54, v200\n"
      "v_lshl_or_b32 v203, v224, 14, v203\n"
      "v_mfma_f32_16x16x32_bf16 v[4:7], v[188:191], a[164:167], v[4:7]\n"
      "v_mfma_f32_16x16x32_bf16 v[8:11], v[188:191], a[168:171], v[8:11]\n"
      "v_mfma_f32_16x16x32_bf16 v[12:15], v[188:191], a[172:175], v[12:15]\n"
      "v_mfma_f32_16x16x32_bf16 v[16:19], v[188:191], a[176:179], v[16:19]\n"
      "v_mfma_f32_16x16x32_bf16 v[20:23], v[188:191], a[180:183], v[20:23]\n"
      "v_mfma_f32_16x16x32_bf16 v[24:27], v[188:191], a[184:187], v[24:27]\n"
      "v_mfma_f32_16x16x32_bf16 v[28:31], v[188:191], a[188:191], v[28:31]\n"
      "v_mfma_f32_16x16x32_bf16 v[32:35], v[128:131], a[160:163], v[32:35]\n"
      "v_mfma_f32_16x16x32_bf16 v[36:39], v[128:131], a[164:167], v[36:39]\n"
      "v_mfma_f32_16x16x32_bf16 v[40:43], v[128:131], a[168:171], v[40:43]\n"
      "v_mfma_f32_16x16x32_bf16 v[44:47], v[128:131], a[172:175], v[44:47]\n"
      "v_mfma_f32_16x16x32_bf16 v[48:51], v[128:131], a[176:179], v[48:51]\n"
      "v_mfma_f32_16x16x32_bf16 v[52:55], v[128:131], a[180:183], v[52:55]\n"
      "v_mfma_f32_16x16x32_bf16 v[56:59], v[128:131], a[184:187], v[56:59]\n"
      "v_mfma_f32_16x16x32_bf16 v[60:63], v[128:131], a[188:191], v[60:63]\n"
      "v_mfma_f32_16x16x32_bf16 v[64:67], v[132:135], a[160:163], v[64:67]\n"
      "v_mfma_f32_16x16x32_bf16 v[68:71], v[132:135], a[164:167], v[68:71]\n"
      "v_mfma_f32_16x16x32_bf16 v[72:75], v[132:135], a[168:171], v[72:75]\n"
      "s_waitcnt vmcnt(2)\n"
      "v_cmp_ne_u32 vcc, s37, v252\n"
      "s_nop 1\n"
      "s_cbranch_vccnz .Ls_s14_%=\n"
      ".Ls_o15_%=:\n"
      "v_mfma_f32_16x16x32_bf16 v[76:79], v[132:135], a[172:175], v[76:79]\n"
      "v_mfma_f32_16x16x32_bf16 v[80:83], v[132:135], a[176:179], v[80:83]\n"
      "global_load_dwordx4 v[160:163], v240, s[10:11] offset:-1024 sc1\n"
      "global_load_dwordx4 v[164:167], v240, s[72:73] offset:-1024 sc1\n"
      "v_mfma_f32_16x16x32_bf16 v[84:87], v[132:135], a[180:183], v[84:87]\n"
      "global_load_dwordx4 v[168:171], v241, s[10:11] offset:-1024 sc1\n"
      "global_load_dwordx4 v[172:175], v241, s[72:73] offset:-1024 sc1\n"
      "v_mfma_f32_16x16x32_bf16 v[88:91], v[132:135], a[184:187], v[88:91]\n"
      "global_load_dwordx4 v[176:179], v240, s[14:15] offset:-1024 sc1\n"
      "global_load_dwordx4 v[180:183], v240, s[74:75] offset:-1024 sc1\n"
      "v_mfma_f32_16x16x32_bf16 v[92:95], v[132:135], a[188:191], v[92:95]\n"
      "global_load_dwordx4 v[184:187], v241, s[14:15] offset:-1024 sc1\n"
      "global_load_dwordx4 v[188:191], v241, s[74:75] offset:-1024 sc1\n"
      "v_mfma_f32_16x16x32_bf16 v[0:3], v[136:139], a[192:195], v[0:3]\n"
      "global_load_dwordx4 v[128:131], v240, s[10:11] offset:0 sc1\n"
      "global_load_dwordx4 v[132:135], v240, s[72:73] offset:0 sc1\n"
      "s_waitcnt lgkmcnt(14)\n"
      "v_mfma_f32_16x16x32_bf16 v[4:7], v[136:139], a[196:199], v[4:7]\n"
      "s_waitcnt lgkmcnt(11)\n"
      "v_mfma_f32_16x16x32_bf16 v[8:11], v[136:139], a[200:203], v[8:11]\n"
      "s_waitcnt lgkmcnt(10)\n"
      "v_mfma_f32_16x16x32_bf16 v[12:15], v[136:139], a[204:207], v[12:15]\n"
      "s_waitcnt lgkmcnt(7)\n"
      "v_mfma_f32_16x16x32_bf16 v[16:19], v[136:139], a[208:211], v[16:19]\n"
      "s_waitcnt lgkmcnt(6)\n"
      "v_mfma_f32_16x16x32_bf16 v[20:23], v[136:139], a[212:215], v[20:23]\n"
      "ds_read_b64_tr_b16 v[224:225], v202 offset:0\n"
      "ds_read_b64_tr_b16 v[226:227], v202 offset:512\n"
      "s_waitcnt lgkmcnt(5)\n"
      "v_mfma_f32_16x16x32_bf16 v[24:27], v[136:139], a[216:219], v[24:27]\n"
      "ds_read_b64_tr_b16 v[228:229], v202 offset:4096\n"
      "ds_read_b64_tr_b16 v[230:231], v202 offset:4608\n"
      "s_waitcnt lgkmcnt(6)\n"
      "v_mfma_f32_16x16x32_bf16 v[28:31], v[136:139], a[220:223], v[28:31]\n"
      "global_load_dwordx4 v[136:139], v241, s[10:11] offset:0 sc1\n"
      "ds_read_b64_tr_b16 v[232:233], v202 offset:64\n"
      "v_mfma_f32_16x16x32_bf16 v[0:3], v[140:143], a[224:227], v[0:3]\n"
      "ds_read_b64_tr_b16 v[234:235], v202 offset:576\n"
      "ds_read_b64_tr_b16 v[236:237], v202 offset:4160\n"
      "v_mfma_f32_16x16x32_bf16 v[4:7], v[140:143], a[228:231], v[4:7]\n"
      "ds_read_b64_tr_b16 v[238:239], v202 offset:4672\n"
      "v_mfma_f32_16x16x32_bf16 v[8:11], v[140:143], a[232:235], v[8:11]\n"
      "v_mfma_f32_16x16x32_bf16 v[12:15], v[140:143], a[236:239], v[12:15]\n"
      "v_mfma_f32_16x16x32_bf16 v[16:19], v[140:143], a[240:243], v[16:19]\n"
      "s_waitcnt vmcnt(12) lgkmcnt(6)\n"
      "v_mfma_f32_16x16x32_bf16 v[248:251], v[192:195], v[224:227], 0\n"
      "v_mfma_f32_16x16x32_bf16 v[20:23], v[140:143], a[244:247], v[20:23]\n"
      "s_waitcnt vmcnt(11) lgkmcnt(4)\n"
      "v_mfma_f32_16x16x32_bf16 v[248:251], v[196:199], v[228:231], v[248:251]\n"
      "v_mfma_f32_16x16x32_bf16 v[24:27], v[140:143], a[248:251], v[24:27]\n"
      "s_waitcnt lgkmcnt(2)\n"
      "v_mfma_f32_16x16x32_bf16 v[224:227], v[192:195], v[232:235], 0\n"
      "v_mfma_f32_16x16x32_bf16 v[28:31], v[140:143], a[252:255], v[28:31]\n"
      "global_load_dwordx4 v[140:143], v241, s[72:73] offset:0 sc1\n"
      "s_waitcnt lgkmcnt(0)\n"
      "v_mfma_f32_16x16x32_bf16 v[224:227], v[196:199], v[236:239], v[224:227]\n"
      "v_mfma_f32_16x16x32_bf16 v[32:35], v[144:147], a[192:195], v[32:35]\n"
      "v_mfma_f32_16x16x32_bf16 v[36:39], v[144:147], a[196:199], v[36:39]\n"
      "s_mov_b32 m0, s41\n"
      "v_cvt_pk_bf16_f32 v8, v8, v9\n"
      "v_cvt_pk_bf16_f32 v9, v10, v11\n"
      "v_mfma_f32_16x16x32_bf16 v[40:43], v[144:147], a[200:203], v[40:43]\n"
      "ds_write_addtid_b32 v8 offset:0\n"
      "s_nop 0\n"
      "v_cndmask_b32 v247, v248, v224, s[64:65]\n"
      "v_mfma_f32_16x16x32_bf16 v[44:47], v[144:147], a[204:207], v[44:47]\n"
      "ds_read_b64_tr_b16 v[224:225], v202 offset:32\n"
      "v_cvt_pk_bf16_f32 v10, v12, v13\n"
      "v_mfma_f32_16x16x32_bf16 v[48:51], v[144:147], a[208:211], v[48:51]\n"
      "ds_read_b64_tr_b16 v[226:227], v202 offset:544\n"
      "ds_read_b64_tr_b16 v[228:229], v202 offset:4128\n"
      "v_mfma_f32_16x16x32_bf16 v[52:55], v[144:147], a[212:215], v[52:55]\n"
      "ds_read_b64_tr_b16 v[230:231], v202 offset:4640\n"
      "ds_read_b64_tr_b16 v[232:233], v202 offset:96\n"
      "v_mfma_f32_16x16x32_bf16 v[56:59], v[144:147], a[216:219], v[56:59]\n"
      "ds_read_b64_tr_b16 v[234:235], v202 offset:608\n"
      "ds_read_b64_tr_b16 v[236:237], v202 offset:4192\n"
      "v_mfma_f32_16x16x32_bf16 v[60:63], v[144:147], a[220:223], v[60:63]\n"
      "global_load_dwordx4 v[144:147], v240, s[14:15] offset:0 sc1\n"
      "ds_read_b64_tr_b16 v[238:239], v202 offset:4704\n"
      "v_mfma_f32_16x16x32_bf16 v[32:35], v[148:151], a[224:227], v[32:35]\n"
      "ds_write_addtid_b32 v9 offset:256\n"
      "v_cvt_pk_bf16_f32 v11, v14, v15\n"
      "v_mfma_f32_16x16x32_bf16 v[36:39], v[148:151], a[228:231], v[36:39]\n"
      "ds_write_addtid_b32 v10 offset:512\n"
      "v_cvt_pk_bf16_f32 v12, v16, v17\n"
      "v_mfma_f32_16x16x32_bf16 v[40:43], v[148:151], a[232:235], v[40:43]\n"
      "s_waitcnt lgkmcnt(8)\n"
      "v_mfma_f32_16x16x32_bf16 v[248:251], v[192:195], v[224:227], 0\n"
      "v_mfma_f32_16x16x32_bf16 v[44:47], v[148:151], a[236:239], v[44:47]\n"
      "s_waitcnt lgkmcnt(6)\n"
      "v_mfma_f32_16x16x32_bf16 v[248:251], v[196:199], v[228:231], v[248:251]\n"
      "v_mfma_f32_16x16x32_bf16 v[48:51], v[148:151], a[240:243], v[48:51]\n"
      "s_waitcnt lgkmcnt(4)\n"
      "v_mfma_f32_16x16x32_bf16 v[224:227], v[192:195], v[232:235], 0\n"
      "v_mfma_f32_16x16x32_bf16 v[52:55], v[148:151], a[244:247], v[52:55]\n"
      "s_waitcnt lgkmcnt(2)\n"
      "v_mfma_f32_16x16x32_bf16 v[224:227], v[196:199], v[236:239], v[224:227]\n"
      "v_mfma_f32_16x16x32_bf16 v[56:59], v[148:151], a[248:251], v[56:59]\n"
      "ds_write_addtid_b32 v11 offset:768\n"
      "v_cvt_pk_bf16_f32 v13, v18, v19\n"
      "v_mfma_f32_16x16x32_bf16 v[60:63], v[148:151], a[252:255], v[60:63]\n"
      "global_load_dwordx4 v[148:151], v240, s[74:75] offset:0 sc1\n"
      "ds_write_addtid_b32 v12 offset:1024\n"
      "v_mfma_f32_16x16x32_bf16 v[64:67], v[152:155], a[192:195], v[64:67]\n"
      "s_nop 0\n"
      "v_cndmask_b32 v250, v248, v224, s[64:65]\n"
      "v_mfma_f32_16x16x32_bf16 v[68:71], v[152:155], a[196:199], v[68:71]\n"
      "v_and_b32 v248, s82, v247\n"
      "v_mov_b32 v249, s37\n"
      "v_mfma_f32_16x16x32_bf16 v[72:75], v[152:155], a[200:203], v[72:75]\n"
      "v_and_b32 v250, s82, v250\n"
      "v_mov_b32 v251, s37\n"
      "v_mfma_f32_16x16x32_bf16 v[76:79], v[152:155], a[204:207], v[76:79]\n"
      "s_mov_b64 exec, s[66:67]\n"
      "global_store_dwordx4 v200, v[248:251], s[58:59] sc1\n"
      "s_mov_b64 exec, -1\n"
      "v_mfma_f32_16x16x32_bf16 v[80:83], v[152:155], a[208:211], v[80:83]\n"
      "v_bfe_u32 v248, v243, 1, 3\n"
      "v_lshrrev_b32 v249, 4, v243\n"
      "v_mfma_f32_16x16x32_bf16 v[84:87], v[152:155], a[212:215], v[84:87]\n"
      "v_lshlrev_b32 v249, 1, v249\n"
      "v_sub_u32 v248, v248, v249\n"
      "v_mfma_f32_16x16x32_bf16 v[88:91], v[152:155], a[216:219], v[88:91]\n"
      "v_and_b32 v249, 16, v242\n"
      "v_mov_b32 v254, 0x3f80\n"
      "v_mfma_f32_16x16x32_bf16 v[92:95], v[152:155], a[220:223], v[92:95]\n"
      "global_load_dwordx4 v[152:155], v241, s[14:15] offset:0 sc1\n"
      "v_lshlrev_b32 v249, v249, v254\n"
      "v_mfma_f32_16x16x32_bf16 v[64:67], v[156:159], a[224:227], v[64:67]\n"
      "v_cmp_eq_u32 vcc, 0, v248\n"
      "s_nop 1\n"
      "v_cndmask_b32 v250, 0, v249, vcc\n"
      "v_mfma_f32_16x16x32_bf16 v[68:71], v[156:159], a[228:231], v[68:71]\n"
      "v_cmp_eq_u32 vcc, 1, v248\n"
      "s_nop 1\n"
      "v_cndmask_b32 v251, 0, v249, vcc\n"
      "v_mfma_f32_16x16x32_bf16 v[72:75], v[156:159], a[232:235], v[72:75]\n"
      "v_mov_b32 v252, v250\n"
      "v_mov_b32 v253, v251\n"
      "v_mfma_f32_16x16x32_bf16 v[76:79], v[156:159], a[236:239], v[76:79]\n"
      "v_mov_b32 v248, 0\n"
      "v_mov_b32 v249, 0\n"
      "v_mfma_f32_16x16x32_bf16 v[80:83], v[156:159], a[240:243], v[80:83]\n"
      "v_mov_b32 v254, 0\n"
      "v_mov_b32 v255, 0\n"
      "v_mfma_f32_16x16x32_bf16 v[84:87], v[156:159], a[244:247], v[84:87]\n"
      "s_lshr_b32 s54, s33, 1\n"
      "s_lshl1_add_u32 s54, s80, s54\n"
      "s_lshl_b32 s54, s54, 12\n"
      "v_mfma_f32_16x16x32_bf16 v[88:91], v[156:159], a[248:251], v[88:91]\n"
      "s_add_u32 s54, s54, 262144\n"
      "v_add_u32 v200, s54, v242\n"
      "s_add_u32 s54, s41, 77824\n"
      "v_mfma_f32_16x16x32_bf16 v[92:95], v[156:159], a[252:255], v[92:95]\n"
      "s_waitcnt vmcnt(14)\n"
      "global_load_dwordx4 v[156:159], v241, s[74:75] offset:0 sc1\n"
      "v_add_u32 v201, s54, v201\n"
      "v_mfma_f32_16x16x32_bf16 v[96:99], v[160:163], a[0:3], 0\n"
      "s_add_u32 s48, s34, 1\n"
      "v_and_b32 v202, 15, v243\n"
      "v_lshrrev_b32 v199, 5, v243\n"
      "v_mfma_f32_16x16x32_bf16 v[100:103], v[160:163], a[4:7], 0\n"
      "s_and_b32 s48, s48, 3\n"
      "v_lshl_or_b32 v202, v199, 4, v202\n"
      "s_add_u32 s54, s41, 12288\n"
      "v_mfma_f32_16x16x32_bf16 v[104:107], v[160:163], a[8:11], 0\n"
      "s_mul_i32 s48, s48, 36864\n"
      "v_lshl_add_u32 v202, v202, 1, s54\n"
      "v_and_b32 v199, 48, v243\n"
      "v_mfma_f32_16x16x32_bf16 v[108:111], v[160:163], a[12:15], 0\n"
      "s_add_u32 s48, s48, 0\n"
      "v_add_u32 v199, s54, v199\n"
      "s_and_b32 s54, s33, 1\n"
      "v_mfma_f32_16x16x32_bf16 v[112:115], v[160:163], a[16:19], 0\n"
      "s_lshl1_add_u32 s54, s81, s54\n"
      "s_lshl_b32 s54, s54, 15\n"
      "s_lshr_b32 s55, s33, 1\n"
      "v_mfma_f32_16x16x32_bf16 v[116:119], v[160:163], a[20:23], 0\n"
      "s_lshl1_add_u32 s55, s80, s55\n"
      "s_lshl_b32 s55, s55, 8\n"
      "s_add_u32 s54, s54, s55\n"
      "v_mfma_f32_16x16x32_bf16 v[120:123], v[160:163], a[24:27], 0\n"
      "s_add_u32 s54, s54, 524288\n"
      "v_add_u32 v203, s54, v203\n"
      "s_add_u32 s52, s60, s48\n"
      "s_addc_u32 s53, s61, 0\n"
      "v_mfma_f32_16x16x32_bf16 v[124:127], v[160:163], a[28:31], 0\n"
      "global_load_dwordx4 v[160:163], v240, s[10:11] offset:1024 sc1\n"
      "v_cvt_pk_bf16_f32 v14, v20, v21\n"
      "v_mfma_f32_16x16x32_bf16 v[96:99], v[164:167], a[32:35], v[96:99]\n"
      "ds_write_addtid_b32 v13 offset:1280\n"
      "v_cvt_pk_bf16_f32 v15, v22, v23\n"
      "v_mfma_f32_16x16x32_bf16 v[100:103], v[164:167], a[36:39], v[100:103]\n"
      "ds_write_addtid_b32 v14 offset:1536\n"
      "v_cvt_pk_bf16_f32 v16, v24, v25\n"
      "v_mfma_f32_16x16x32_bf16 v[104:107], v[164:167], a[40:43], v[104:107]\n"
      "ds_write_addtid_b32 v15 offset:1792\n"
      "v_cvt_pk_bf16_f32 v17, v26, v27\n"
      "v_mfma_f32_16x16x32_bf16 v[108:111], v[164:167], a[44:47], v[108:111]\n"
      "ds_write_addtid_b32 v16 offset:2048\n"
      "v_cvt_pk_bf16_f32 v18, v28, v29\n"
      "v_mfma_f32_16x16x32_bf16 v[112:115], v[164:167], a[48:51], v[112:115]\n"
      "ds_write_addtid_b32 v17 offset:2304\n"
      "v_cvt_pk_bf16_f32 v19, v30, v31\n"
      "v_mfma_f32_16x16x32_bf16 v[116:119], v[164:167], a[52:55], v[116:119]\n"
      "ds_write_addtid_b32 v18 offset:2560\n"
      "ds_write_addtid_b32 v19 offset:2816\n"
      "v_mfma_f32_16x16x32_bf16 v[120:123], v[164:167], a[56:59], v[120:123]\n"
      "v_cvt_pk_bf16_f32 v40, v40, v41\n"
      "v_cvt_pk_bf16_f32 v41, v42, v43\n"
      "v_mfma_f32_16x16x32_bf16 v[124:127], v[164:167], a[60:63], v[124:127]\n"
      "s_waitcnt vmcnt(14)\n"
      "global_load_dwordx4 v[164:167], v240, s[72:73] offset:1024 sc1\n"
      "ds_write_addtid_b32 v40 offset:3072\n"
      "v_mfma_f32_16x16x32_bf16 v[96:99], v[168:171], a[64:67], v[96:99]\n"
      "v_cvt_pk_bf16_f32 v42, v44, v45\n"
      "ds_write_addtid_b32 v41 offset:3328\n"
      "v_mfma_f32_16x16x32_bf16 v[100:103], v[168:171], a[68:71], v[100:103]\n"
      "v_cvt_pk_bf16_f32 v43, v46, v47\n"
      "ds_write_addtid_b32 v42 offset:3584\n"
      "v_mfma_f32_16x16x32_bf16 v[104:107], v[168:171], a[72:75], v[104:107]\n"
      "v_cvt_pk_bf16_f32 v44, v48, v49\n"
      "ds_write_addtid_b32 v43 offset:3840\n"
      "v_mfma_f32_16x16x32_bf16 v[108:111], v[168:171], a[76:79], v[108:111]\n"
      "v_cvt_pk_bf16_f32 v45, v50, v51\n"
      "ds_write_addtid_b32 v44 offset:4096\n"
      "v_mfma_f32_16x16x32_bf16 v[112:115], v[168:171], a[80:83], v[112:115]\n"
      "v_cvt_pk_bf16_f32 v46, v52, v53\n"
      "ds_write_addtid_b32 v45 offset:4352\n"
      "v_mfma_f32_16x16x32_bf16 v[116:119], v[168:171], a[84:87], v[116:119]\n"
      "v_cvt_pk_bf16_f32 v47, v54, v55\n"
      "ds_write_addtid_b32 v46 offset:4608\n"
      "v_mfma_f32_16x16x32_bf16 v[120:123], v[168:171], a[88:91], v[120:123]\n"
      "v_cvt_pk_bf16_f32 v48, v56, v57\n"
      "ds_write_addtid_b32 v47 offset:4864\n"
      "v_mfma_f32_16x16x32_bf16 v[124:127], v[168:171], a[92:95], v[124:127]\n"
      "global_load_dwordx4 v[168:171], v241, s[10:11] offset:1024 sc1\n"
      "v_cvt_pk_bf16_f32 v49, v58, v59\n"
      "v_mfma_f32_16x16x32_bf16 v[96:99], v[172:175], a[96:99], v[96:99]\n"
      "ds_write_addtid_b32 v48 offset:5120\n"
      "v_cvt_pk_bf16_f32 v50, v60, v61\n"
      "v_mfma_f32_16x16x32_bf16 v[100:103], v[172:175], a[100:103], v[100:103]\n"
      "ds_write_addtid_b32 v49 offset:5376\n"
      "v_cvt_pk_bf16_f32 v51, v62, v63\n"
      "v_mfma_f32_16x16x32_bf16 v[104:107], v[172:175], a[104:107], v[104:107]\n"
      "ds_write_addtid_b32 v50 offset:5632\n"
      "ds_write_addtid_b32 v51 offset:5888\n"
      "v_mfma_f32_16x16x32_bf16 v[108:111], v[172:175], a[108:111], v[108:111]\n"
      "v_mfma_f32_16x16x32_bf16 v[112:115], v[172:175], a[112:115], v[112:115]\n"
      "v_mfma_f32_16x16x32_bf16 v[116:119], v[172:175], a[116:119], v[116:119]\n"
      "s_waitcnt lgkmcnt(0)\n"
      "s_barrier\n"
      "v_mfma_f32_16x16x32_bf16 v[120:123], v[172:175], a[120:123], v[120:123]\n"
      "ds_read2st64_b32 v[20:21], v244 offset0:0 offset1:1\n"
      "ds_read2st64_b32 v[22:23], v245 offset0:4 offset1:5\n"
      "v_mfma_f32_16x16x32_bf16 v[124:127], v[172:175], a[124:127], v[124:127]\n"
      "s_waitcnt vmcnt(14)\n"
      "global_load_dwordx4 v[172:175], v241, s[72:73] offset:1024 sc1\n"
      "ds_read2st64_b32 v[24:25], v244 offset0:2 offset1:3\n"
      "v_mfma_f32_16x16x32_bf16 v[96:99], v[176:179], a[128:131], v[96:99]\n"
      "ds_read2st64_b32 v[26:27], v245 offset0:6 offset1:7\n"
      "ds_read2st64_b32 v[28:29], v246 offset0:8 offset1:9\n"
      "v_mfma_f32_16x16x32_bf16 v[100:103], v[176:179], a[132:135], v[100:103]\n"
      "ds_read2st64_b32 v[30:31], v246 offset0:10 offset1:11\n"
      "ds_read2st64_b32 v[52:53], v244 offset0:12 offset1:13\n"
      "v_mfma_f32_16x16x32_bf16 v[104:107], v[176:179], a[136:139], v[104:107]\n"
      "ds_read2st64_b32 v[54:55], v245 offset0:16 offset1:17\n"
      "ds_read2st64_b32 v[56:57], v244 offset0:14 offset1:15\n"
      "v_mfma_f32_16x16x32_bf16 v[108:111], v[176:179], a[140:143], v[108:111]\n"
      "ds_read2st64_b32 v[58:59], v245 offset0:18 offset1:19\n"
      "ds_read2st64_b32 v[60:61], v246 offset0:20 offset1:21\n"
      "v_mfma_f32_16x16x32_bf16 v[112:115], v[176:179], a[144:147], v[112:115]\n"
      "ds_read2st64_b32 v[62:63], v246 offset0:22 offset1:23\n"
      "v_cvt_pk_bf16_f32 v72, v72, v73\n"
      "v_mfma_f32_16x16x32_bf16 v[116:119], v[176:179], a[148:151], v[116:119]\n"
      "v_cvt_pk_bf16_f32 v73, v74, v75\n"
      "ds_write_addtid_b32 v72 offset:6144\n"
      "v_mfma_f32_16x16x32_bf16 v[120:123], v[176:179], a[152:155], v[120:123]\n"
      "s_waitcnt lgkmcnt(7)\n"
      "v_mfma_f32_16x16x32_bf16 v[0:3], v[250:253], v[20:23], v[0:3]\n"
      "v_cvt_pk_bf16_f32 v74, v76, v77\n"
      "v_mfma_f32_16x16x32_bf16 v[124:127], v[176:179], a[156:159], v[124:127]\n"
      "global_load_dwordx4 v[176:179], v240, s[14:15] offset:1024 sc1\n"
      "v_mfma_f32_16x16x32_bf16 v[96:99], v[180:183], a[160:163], v[96:99]\n"
      "v_mfma_f32_16x16x32_bf16 v[4:7], v[250:253], v[24:27], v[4:7]\n"
      "v_mfma_f32_16x16x32_bf16 v[100:103], v[180:183], a[164:167], v[100:103]\n"
      "v_mfma_f32_16x16x32_bf16 v[0:3], v[252:255], v[28:31], v[0:3]\n"
      "v_mfma_f32_16x16x32_bf16 v[104:107], v[180:183], a[168:171], v[104:107]\n"
      "v_mfma_f32_16x16x32_bf16 v[4:7], v[248:251], v[28:31], v[4:7]\n"
      "v_mov_b32 v11, s37\n"
      "v_mfma_f32_16x16x32_bf16 v[108:111], v[180:183], a[172:175], v[108:111]\n"
      "ds_write_addtid_b32 v73 offset:6400\n"
      "v_cvt_pk_bf16_f32 v75, v78, v79\n"
      "v_mfma_f32_16x16x32_bf16 v[112:115], v[180:183], a[176:179], v[112:115]\n"
      "ds_write_addtid_b32 v74 offset:6656\n"
      "v_cvt_pk_bf16_f32 v8, v0, v1\n"
      "v_mfma_f32_16x16x32_bf16 v[116:119], v[180:183], a[180:183], v[116:119]\n"
      "v_cvt_pk_bf16_f32 v9, v2, v3\n"
      "v_cvt_pk_bf16_f32 v10, v4, v5\n"
      "v_mfma_f32_16x16x32_bf16 v[120:123], v[180:183], a[184:187], v[120:123]\n"
      "v_cvt_pk_bf16_f32 v247, v6, v7\n"
      "v_cvt_pk_bf16_f32 v76, v80, v81\n"
      "v_mfma_f32_16x16x32_bf16 v[124:127], v[180:183], a[188:191], v[124:127]\n"
      "s_waitcnt vmcnt(14)\n"
      "global_load_dwordx4 v[180:183], v240, s[74:75] offset:1024 sc1\n"
      "v_mfma_f32_16x16x32_bf16 v[96:99], v[184:187], a[192:195], v[96:99]\n"
      "global_store_dwordx4 v242, v[8:11], s[52:53] offset:0 sc1\n"
      "s_waitcnt lgkmcnt(3)\n"
      "v_mfma_f32_16x16x32_bf16 v[100:103], v[184:187], a[196:199], v[100:103]\n"
      "v_mfma_f32_16x16x32_bf16 v[32:35], v[250:253], v[52:55], v[32:35]\n"
      "v_mfma_f32_16x16x32_bf16 v[104:107], v[184:187], a[200:203], v[104:107]\n"
      "v_mfma_f32_16x16x32_bf16 v[36:39], v[250:253], v[56:59], v[36:39]\n"
      "v_mfma_f32_16x16x32_bf16 v[108:111], v[184:187], a[204:207], v[108:111]\n"
      "v_mfma_f32_16x16x32_bf16 v[32:35], v[252:255], v[60:63], v[32:35]\n"
      "v_mfma_f32_16x16x32_bf16 v[112:115], v[184:187], a[208:211], v[112:115]\n"
      "v_mfma_f32_16x16x32_bf16 v[36:39], v[248:251], v[60:63], v[36:39]\n"
      "v_mov_b32 v43, s37\n"
      "v_mfma_f32_16x16x32_bf16 v[116:119], v[184:187], a[212:215], v[116:119]\n"
      "ds_write_addtid_b32 v75 offset:6912\n"
      "v_mov_b32 v44, v247\n"
      "v_mfma_f32_16x16x32_bf16 v[120:123], v[184:187], a[216:219], v[120:123]\n"
      "v_mov_b32 v47, s37\n"
      "v_cvt_pk_bf16_f32 v77, v82, v83\n"
      "v_mfma_f32_16x16x32_bf16 v[124:127], v[184:187], a[220:223], v[124:127]\n"
      "global_load_dwordx4 v[184:187], v241, s[14:15] offset:1024 sc1\n"
      "v_mfma_f32_16x16x32_bf16 v[96:99], v[188:191], a[224:227], v[96:99]\n"
      "ds_write_addtid_b32 v76 offset:7168\n"
      "v_cvt_pk_bf16_f32 v40, v32, v33\n"
      "v_mfma_f32_16x16x32_bf16 v[100:103], v[188:191], a[228:231], v[100:103]\n"
      "v_cvt_pk_bf16_f32 v41, v34, v35\n"
      "v_cvt_pk_bf16_f32 v42, v36, v37\n"
      "v_mfma_f32_16x16x32_bf16 v[104:107], v[188:191], a[232:235], v[104:107]\n"
      "v_cvt_pk_bf16_f32 v45, v38, v39\n"
      "v_cvt_pk_bf16_f32 v78, v84, v85\n"
      "v_mfma_f32_16x16x32_bf16 v[108:111], v[188:191], a[236:239], v[108:111]\n"
      "global_store_dwordx4 v242, v[40:43], s[52:53] offset:1024 sc1\n"
      "v_mfma_f32_16x16x32_bf16 v[112:115], v[188:191], a[240:243], v[112:115]\n"
      "global_store_dwordx4 v242, v[44:47], s[52:53] offset:2048 sc1\n"
      "s_add_u32 s48, s34, 2\n"
      "v_mfma_f32_16x16x32_bf16 v[116:119], v[188:191], a[244:247], v[116:119]\n"
      "s_and_b32 s48, s48, 3\n"
      "ds_write_addtid_b32 v77 offset:7424\n"
      "s_mul_i32 s48, s48, 36864\n"
      "v_mfma_f32_16x16x32_bf16 v[120:123], v[188:191], a[248:251], v[120:123]\n"
      "s_add_u32 s48, s48, 12288\n"
      "s_add_u32 s52, s60, s48\n"
      "s_addc_u32 s53, s61, 0\n"
      "v_mfma_f32_16x16x32_bf16 v[124:127], v[188:191], a[252:255], v[124:127]\n"
      "s_waitcnt vmcnt(17)\n"
      "global_load_dwordx4 v[188:191], v241, s[74:75] offset:1024 sc1\n"
      "v_cvt_pk_bf16_f32 v79, v86, v87\n"
      "v_mfma_f32_16x16x32_bf16 v[0:3], v[128:131], a[0:3], 0\n"
      "ds_write_addtid_b32 v78 offset:7680\n"
      "v_cvt_pk_bf16_f32 v80, v88, v89\n"
      "v_mfma_f32_16x16x32_bf16 v[4:7], v[128:131], a[4:7], 0\n"
      "ds_write_addtid_b32 v79 offset:7936\n"
      "v_cvt_pk_bf16_f32 v81, v90, v91\n"
      "v_mfma_f32_16x16x32_bf16 v[8:11], v[128:131], a[8:11], 0\n"
      "ds_write_addtid_b32 v80 offset:8192\n"
      "v_cvt_pk_bf16_f32 v82, v92, v93\n"
      "v_mfma_f32_16x16x32_bf16 v[12:15], v[128:131], a[12:15], 0\n"
      "ds_write_addtid_b32 v81 offset:8448\n"
      "v_cvt_pk_bf16_f32 v83, v94, v95\n"
      "v_mfma_f32_16x16x32_bf16 v[16:19], v[128:131], a[16:19], 0\n"
      "ds_write_addtid_b32 v82 offset:8704\n"
      "v_mfma_f32_16x16x32_bf16 v[20:23], v[128:131], a[20:23], 0\n"
      "ds_write_addtid_b32 v83 offset:8960\n"
      "v_cvt_pk_bf16_f32 v104, v104, v105\n"
      "v_mfma_f32_16x16x32_bf16 v[24:27], v[128:131], a[24:27], 0\n"
      "v_cvt_pk_bf16_f32 v105, v106, v107\n"
      "v_mfma_f32_16x16x32_bf16 v[28:31], v[128:131], a[28:31], 0\n"
      "global_load_dwordx4 v[128:131], v240, s[10:11] offset:2048 sc1\n"
      "v_mfma_f32_16x16x32_bf16 v[0:3], v[132:135], a[32:35], v[0:3]\n"
      "ds_write_addtid_b32 v104 offset:9216\n"
      "v_cvt_pk_bf16_f32 v106, v108, v109\n"
      "v_mfma_f32_16x16x32_bf16 v[4:7], v[132:135], a[36:39], v[4:7]\n"
      "ds_write_addtid_b32 v105 offset:9472\n"
      "v_cvt_pk_bf16_f32 v107, v110, v111\n"
      "v_mfma_f32_16x16x32_bf16 v[8:11], v[132:135], a[40:43], v[8:11]\n"
      "ds_write_addtid_b32 v106 offset:9728\n"
      "v_cvt_pk_bf16_f32 v108, v112, v113\n"
      "v_mfma_f32_16x16x32_bf16 v[12:15], v[132:135], a[44:47], v[12:15]\n"
      "ds_write_addtid_b32 v107 offset:9984\n"
      "v_cvt_pk_bf16_f32 v109, v114, v115\n"
      "v_mfma_f32_16x16x32_bf16 v[16:19], v[132:135], a[48:51], v[16:19]\n"
      "ds_write_addtid_b32 v108 offset:10240\n"
      "v_cvt_pk_bf16_f32 v110, v116, v117\n"
      "v_mfma_f32_16x16x32_bf16 v[20:23], v[132:135], a[52:55], v[20:23]\n"
      "ds_write_addtid_b32 v109 offset:10496\n"
      "v_cvt_pk_bf16_f32 v111, v118, v119\n"
      "v_mfma_f32_16x16x32_bf16 v[24:27], v[132:135], a[56:59], v[24:27]\n"
      "ds_write_addtid_b32 v110 offset:10752\n"
      "v_cvt_pk_bf16_f32 v112, v120, v121\n"
      "v_mfma_f32_16x16x32_bf16 v[28:31], v[132:135], a[60:63], v[28:31]\n"
      "s_waitcnt vmcnt(17)\n"
      "global_load_dwordx4 v[132:135], v240, s[72:73] offset:2048 sc1\n"
      "v_mfma_f32_16x16x32_bf16 v[0:3], v[136:139], a[64:67], v[0:3]\n"
      "ds_write_addtid_b32 v111 offset:11008\n"
      "v_cvt_pk_bf16_f32 v113, v122, v123\n"
      "v_mfma_f32_16x16x32_bf16 v[4:7], v[136:139], a[68:71], v[4:7]\n"
      "ds_write_addtid_b32 v112 offset:11264\n"
      "v_cvt_pk_bf16_f32 v114, v124, v125\n"
      "v_mfma_f32_16x16x32_bf16 v[8:11], v[136:139], a[72:75], v[8:11]\n"
      "ds_write_addtid_b32 v113 offset:11520\n"
      "v_cvt_pk_bf16_f32 v115, v126, v127\n"
      "v_mfma_f32_16x16x32_bf16 v[12:15], v[136:139], a[76:79], v[12:15]\n"
      "ds_write_addtid_b32 v114 offset:11776\n"
      "v_mfma_f32_16x16x32_bf16 v[16:19], v[136:139], a[80:83], v[16:19]\n"
      "ds_write_addtid_b32 v115 offset:12032\n"
      "v_mfma_f32_16x16x32_bf16 v[20:23], v[136:139], a[84:87], v[20:23]\n"
      "v_mfma_f32_16x16x32_bf16 v[24:27], v[136:139], a[88:91], v[24:27]\n"
      "v_mfma_f32_16x16x32_bf16 v[28:31], v[136:139], a[92:95], v[28:31]\n"
      "global_load_dwordx4 v[136:139], v241, s[10:11] offset:2048 sc1\n"
      "v_mfma_f32_16x16x32_bf16 v[0:3], v[140:143], a[96:99], v[0:3]\n"
      "s_waitcnt lgkmcnt(0)\n"
      "s_barrier\n"
      "v_mfma_f32_16x16x32_bf16 v[4:7], v[140:143], a[100:103], v[4:7]\n"
      "ds_read2st64_b32 v[84:85], v244 offset0:24 offset1:25\n"
      "v_mfma_f32_16x16x32_bf16 v[8:11], v[140:143], a[104:107], v[8:11]\n"
      "ds_read2st64_b32 v[86:87], v245 offset0:28 offset1:29\n"
      "v_mfma_f32_16x16x32_bf16 v[12:15], v[140:143], a[108:111], v[12:15]\n"
      "ds_read2st64_b32 v[88:89], v244 offset0:26 offset1:27\n"
      "v_mfma_f32_16x16x32_bf16 v[16:19], v[140:143], a[112:115], v[16:19]\n"
      "ds_read2st64_b32 v[90:91], v245 offset0:30 offset1:31\n"
      "v_mfma_f32_16x16x32_bf16 v[20:23], v[140:143], a[116:119], v[20:23]\n"
      "ds_read2st64_b32 v[92:93], v246 offset0:32 offset1:33\n"
      "v_mfma_f32_16x16x32_bf16 v[24:27], v[140:143], a[120:123], v[24:27]\n"
      "ds_read2st64_b32 v[94:95], v246 offset0:34 offset1:35\n"
      "v_mfma_f32_16x16x32_bf16 v[28:31], v[140:143], a[124:127], v[28:31]\n"
      "s_waitcnt vmcnt(17)\n"
      "global_load_dwordx4 v[140:143], v241, s[72:73] offset:2048 sc1\n"
      "v_mfma_f32_16x16x32_bf16 v[0:3], v[144:147], a[128:131], v[0:3]\n"
      "ds_read2st64_b32 v[116:117], v244 offset0:36 offset1:37\n"
      "v_mfma_f32_16x16x32_bf16 v[4:7], v[144:147], a[132:135], v[4:7]\n"
      "ds_read2st64_b32 v[118:119], v245 offset0:40 offset1:41\n"
      "v_mfma_f32_16x16x32_bf16 v[8:11], v[144:147], a[136:139], v[8:11]\n"
      "ds_read2st64_b32 v[120:121], v244 offset0:38 offset1:39\n"
      "v_mfma_f32_16x16x32_bf16 v[12:15], v[144:147], a[140:143], v[12:15]\n"
      "s_waitcnt lgkmcnt(3)\n"
      "v_mfma_f32_16x16x32_bf16 v[64:67], v[250:253], v[84:87], v[64:67]\n"
      "v_mfma_f32_16x16x32_bf16 v[16:19], v[144:147], a[144:147], v[16:19]\n"
      "v_mfma_f32_16x16x32_bf16 v[68:71], v[250:253], v[88:91], v[68:71]\n"
      "v_mfma_f32_16x16x32_bf16 v[20:23], v[144:147], a[148:151], v[20:23]\n"
      "v_mfma_f32_16x16x32_bf16 v[64:67], v[252:255], v[92:95], v[64:67]\n"
      "v_mfma_f32_16x16x32_bf16 v[24:27], v[144:147], a[152:155], v[24:27]\n"
      "v_mfma_f32_16x16x32_bf16 v[68:71], v[248:251], v[92:95], v[68:71]\n"
      "v_mov_b32 v75, s37\n"
      "v_mfma_f32_16x16x32_bf16 v[28:31], v[144:147], a[156:159], v[28:31]\n"
      "global_load_dwordx4 v[144:147], v240, s[14:15] offset:2048 sc1\n"
      "v_mfma_f32_16x16x32_bf16 v[0:3], v[148:151], a[160:163], v[0:3]\n"
      "ds_read2st64_b32 v[122:123], v245 offset0:42 offset1:43\n"
      "s_nop 0\n"
      "v_cvt_pk_bf16_f32 v72, v64, v65\n"
      "v_mfma_f32_16x16x32_bf16 v[4:7], v[148:151], a[164:167], v[4:7]\n"
      "ds_read2st64_b32 v[124:125], v246 offset0:44 offset1:45\n"
      "v_cvt_pk_bf16_f32 v73, v66, v67\n"
      "v_mfma_f32_16x16x32_bf16 v[8:11], v[148:151], a[168:171], v[8:11]\n"
      "ds_read2st64_b32 v[126:127], v246 offset0:46 offset1:47\n"
      "v_cvt_pk_bf16_f32 v74, v68, v69\n"
      "v_mfma_f32_16x16x32_bf16 v[12:15], v[148:151], a[172:175], v[12:15]\n"
      "global_load_dwordx4 v[224:227], v200, s[58:59] offset:0 sc1\n"
      "v_cvt_pk_bf16_f32 v247, v70, v71\n"
      "v_mfma_f32_16x16x32_bf16 v[16:19], v[148:151], a[176:179], v[16:19]\n"
      "global_load_dwordx4 v[228:231], v200, s[58:59] offset:1024 sc1\n"
      "v_mfma_f32_16x16x32_bf16 v[20:23], v[148:151], a[180:183], v[20:23]\n"
      "global_load_dwordx4 v[232:235], v200, s[58:59] offset:2048 sc1\n"
      "v_mfma_f32_16x16x32_bf16 v[24:27], v[148:151], a[184:187], v[24:27]\n"
      "global_load_dwordx4 v[236:239], v200, s[58:59] offset:3072 sc1\n"
      "v_mfma_f32_16x16x32_bf16 v[28:31], v[148:151], a[188:191], v[28:31]\n"
      "s_waitcnt vmcnt(20)\n"
      "global_load_dwordx4 v[148:151], v240, s[74:75] offset:2048 sc1\n"
      "v_mfma_f32_16x16x32_bf16 v[0:3], v[152:155], a[192:195], v[0:3]\n"
      "global_store_dwordx4 v242, v[72:75], s[52:53] offset:0 sc1\n"
      "s_waitcnt lgkmcnt(0)\n"
      "v_mfma_f32_16x16x32_bf16 v[4:7], v[152:155], a[196:199], v[4:7]\n"
      "v_mfma_f32_16x16x32_bf16 v[96:99], v[250:253], v[116:119], v[96:99]\n"
      "v_mfma_f32_16x16x32_bf16 v[8:11], v[152:155], a[200:203], v[8:11]\n"
      "v_mfma_f32_16x16x32_bf16 v[100:103], v[250:253], v[120:123], v[100:103]\n"
      "v_mfma_f32_16x16x32_bf16 v[12:15], v[152:155], a[204:207], v[12:15]\n"
      "v_mfma_f32_16x16x32_bf16 v[96:99], v[252:255], v[124:127], v[96:99]\n"
      "v_mfma_f32_16x16x32_bf16 v[16:19], v[152:155], a[208:211], v[16:19]\n"
      "v_mfma_f32_16x16x32_bf16 v[100:103], v[248:251], v[124:127], v[100:103]\n"
      "v_mov_b32 v107, s37\n"
      "v_mfma_f32_16x16x32_bf16 v[20:23], v[152:155], a[212:215], v[20:23]\n"
      "v_mov_b32 v108, v247\n"
      "v_mov_b32 v111, s37\n"
      "v_mfma_f32_16x16x32_bf16 v[24:27], v[152:155], a[216:219], v[24:27]\n"
      "v_mfma_f32_16x16x32_bf16 v[28:31], v[152:155], a[220:223], v[28:31]\n"
      "global_load_dwordx4 v[152:155], v241, s[14:15] offset:2048 sc1\n"
      "v_cvt_pk_bf16_f32 v104, v96, v97\n"
      "v_mfma_f32_16x16x32_bf16 v[0:3], v[156:159], a[224:227], v[0:3]\n"
      "v_cvt_pk_bf16_f32 v105, v98, v99\n"
      "v_cvt_pk_bf16_f32 v106, v100, v101\n"
      "v_mfma_f32_16x16x32_bf16 v[4:7], v[156:159], a[228:231], v[4:7]\n"
      "v_cvt_pk_bf16_f32 v109, v102, v103\n"
      "v_mfma_f32_16x16x32_bf16 v[8:11], v[156:159], a[232:235], v[8:11]\n"
      "global_store_dwordx4 v242, v[104:107], s[52:53] offset:1024 sc1\n"
      "v_mfma_f32_16x16x32_bf16 v[12:15], v[156:159], a[236:239], v[12:15]\n"
      "global_store_dwordx4 v242, v[108:111], s[52:53] offset:2048 sc1\n"
      "s_add_u32 s48, s34, 3\n"
      "v_mfma_f32_16x16x32_bf16 v[16:19], v[156:159], a[240:243], v[16:19]\n"
      "s_and_b32 s48, s48, 3\n"
      "s_mul_i32 s48, s48, 36864\n"
      "s_add_u32 s48, s48, 24576\n"
      "v_mfma_f32_16x16x32_bf16 v[20:23], v[156:159], a[244:247], v[20:23]\n"
      "s_add_u32 s52, s60, s48\n"
      "s_addc_u32 s53, s61, 0\n"
      "v_mfma_f32_16x16x32_bf16 v[24:27], v[156:159], a[248:251], v[24:27]\n"
      "v_mfma_f32_16x16x32_bf16 v[28:31], v[156:159], a[252:255], v[28:31]\n"
      "s_waitcnt vmcnt(23)\n"
      "global_load_dwordx4 v[156:159], v241, s[74:75] offset:2048 sc1\n"
      "v_mfma_f32_16x16x32_bf16 v[32:35], v[160:163], a[0:3], 0\n"
      "v_mfma_f32_16x16x32_bf16 v[36:39], v[160:163], a[4:7], 0\n"
      "v_cvt_pk_bf16_f32 v8, v8, v9\n"
      "v_cvt_pk_bf16_f32 v9, v10, v11\n"
      "v_mfma_f32_16x16x32_bf16 v[40:43], v[160:163], a[8:11], 0\n"
      "ds_write_addtid_b32 v8 offset:0\n"
      "v_cvt_pk_bf16_f32 v10, v12, v13\n"
      "v_mfma_f32_16x16x32_bf16 v[44:47], v[160:163], a[12:15], 0\n"
      "ds_write_addtid_b32 v9 offset:256\n"
      "v_cvt_pk_bf16_f32 v11, v14, v15\n"
      "v_mfma_f32_16x16x32_bf16 v[48:51], v[160:163], a[16:19], 0\n"
      "ds_write_addtid_b32 v10 offset:512\n"
      "v_cvt_pk_bf16_f32 v12, v16, v17\n"
      "v_mfma_f32_16x16x32_bf16 v[52:55], v[160:163], a[20:23], 0\n"
      "ds_write_addtid_b32 v11 offset:768\n"
      "v_cvt_pk_bf16_f32 v13, v18, v19\n"
      "v_mfma_f32_16x16x32_bf16 v[56:59], v[160:163], a[24:27], 0\n"
      "ds_write_addtid_b32 v12 offset:1024\n"
      "v_cvt_pk_bf16_f32 v14, v20, v21\n"
      "v_mfma_f32_16x16x32_bf16 v[60:63], v[160:163], a[28:31], 0\n"
      "global_load_dwordx4 v[160:163], v240, s[10:11] offset:3072 sc1\n"
      "v_mfma_f32_16x16x32_bf16 v[32:35], v[164:167], a[32:35], v[32:35]\n"
      "ds_write_addtid_b32 v13 offset:1280\n"
      "v_cvt_pk_bf16_f32 v15, v22, v23\n"
      "v_mfma_f32_16x16x32_bf16 v[36:39], v[164:167], a[36:39], v[36:39]\n"
      "ds_write_addtid_b32 v14 offset:1536\n"
      "v_cvt_pk_bf16_f32 v16, v24, v25\n"
      "v_mfma_f32_16x16x32_bf16 v[40:43], v[164:167], a[40:43], v[40:43]\n"
      "ds_write_addtid_b32 v15 offset:1792\n"
      "v_cvt_pk_bf16_f32 v17, v26, v27\n"
      "v_mfma_f32_16x16x32_bf16 v[44:47], v[164:167], a[44:47], v[44:47]\n"
      "ds_write_addtid_b32 v16 offset:2048\n"
      "v_cvt_pk_bf16_f32 v18, v28, v29\n"
      "v_mfma_f32_16x16x32_bf16 v[48:51], v[164:167], a[48:51], v[48:51]\n"
      "ds_write_addtid_b32 v17 offset:2304\n"
      "v_cvt_pk_bf16_f32 v19, v30, v31\n"
      "v_mfma_f32_16x16x32_bf16 v[52:55], v[164:167], a[52:55], v[52:55]\n"
      "ds_write_addtid_b32 v18 offset:2560\n"
      "v_mfma_f32_16x16x32_bf16 v[56:59], v[164:167], a[56:59], v[56:59]\n"
      "ds_write_addtid_b32 v19 offset:2816\n"
      "v_mfma_f32_16x16x32_bf16 v[60:63], v[164:167], a[60:63], v[60:63]\n"
      "s_waitcnt vmcnt(23)\n"
      "global_load_dwordx4 v[164:167], v240, s[72:73] offset:3072 sc1\n"
      "v_mfma_f32_16x16x32_bf16 v[32:35], v[168:171], a[64:67], v[32:35]\n"
      "v_mfma_f32_16x16x32_bf16 v[36:39], v[168:171], a[68:71], v[36:39]\n"
      "s_waitcnt lgkmcnt(0)\n"
      "s_barrier\n"
      "v_mfma_f32_16x16x32_bf16 v[40:43], v[168:171], a[72:75], v[40:43]\n"
      "ds_read2st64_b32 v[20:21], v244 offset0:0 offset1:1\n"
      "v_mfma_f32_16x16x32_bf16 v[44:47], v[168:171], a[76:79], v[44:47]\n"
      "ds_read2st64_b32 v[22:23], v245 offset0:4 offset1:5\n"
      "v_mfma_f32_16x16x32_bf16 v[48:51], v[168:171], a[80:83], v[48:51]\n"
      "ds_read2st64_b32 v[24:25], v244 offset0:2 offset1:3\n"
      "v_mfma_f32_16x16x32_bf16 v[52:55], v[168:171], a[84:87], v[52:55]\n"
      "ds_read2st64_b32 v[26:27], v245 offset0:6 offset1:7\n"
      "v_mfma_f32_16x16x32_bf16 v[56:59], v[168:171], a[88:91], v[56:59]\n"
      "ds_read2st64_b32 v[28:29], v246 offset0:8 offset1:9\n"
      "v_mfma_f32_16x16x32_bf16 v[60:63], v[168:171], a[92:95], v[60:63]\n"
      "global_load_dwordx4 v[168:171], v241, s[10:11] offset:3072 sc1\n"
      "v_mfma_f32_16x16x32_bf16 v[32:35], v[172:175], a[96:99], v[32:35]\n"
      "ds_read2st64_b32 v[30:31], v246 offset0:10 offset1:11\n"
      "v_mfma_f32_16x16x32_bf16 v[36:39], v[172:175], a[100:103], v[36:39]\n"
      "v_mfma_f32_16x16x32_bf16 v[40:43], v[172:175], a[104:107], v[40:43]\n"
      "v_mfma_f32_16x16x32_bf16 v[44:47], v[172:175], a[108:111], v[44:47]\n"
      "v_mfma_f32_16x16x32_bf16 v[48:51], v[172:175], a[112:115], v[48:51]\n"
      "v_mfma_f32_16x16x32_bf16 v[52:55], v[172:175], a[116:119], v[52:55]\n"
      "s_waitcnt lgkmcnt(0)\n"
      "v_mfma_f32_16x16x32_bf16 v[0:3], v[250:253], v[20:23], v[0:3]\n"
      "v_mfma_f32_16x16x32_bf16 v[56:59], v[172:175], a[120:123], v[56:59]\n"
      "v_mfma_f32_16x16x32_bf16 v[4:7], v[250:253], v[24:27], v[4:7]\n"
      "v_mfma_f32_16x16x32_bf16 v[60:63], v[172:175], a[124:127], v[60:63]\n"
      "s_waitcnt vmcnt(23)\n"
      "global_load_dwordx4 v[172:175], v241, s[72:73] offset:3072 sc1\n"
      "v_mfma_f32_16x16x32_bf16 v[40:43], v[176:179], a[136:139], v[40:43]\n"
      "s_waitcnt vmcnt(10)\n"
      "v_bitop3_b32 v192, v225, v227, s37 bitop3:0x7e\n"
      "v_bitop3_b32 v192, v192, v229, s37 bitop3:0xf6\n"
      "v_bitop3_b32 v192, v192, v231, s37 bitop3:0xf6\n"
      "v_bitop3_b32 v192, v192, v233, s37 bitop3:0xf6\n"
      "v_bitop3_b32 v192, v192, v235, s37 bitop3:0xf6\n"
      "v_bitop3_b32 v192, v192, v237, s37 bitop3:0xf6\n"
      "v_bitop3_b32 v192, v192, v239, s37 bitop3:0xf6\n"
      "v_cmp_ne_u32 vcc, 0, v192\n"
      "s_nop 1\n"
      "s_cbranch_vccnz .Ls_s17_%=\n"
      ".Ls_o18_%=:\n"
      "v_mfma_f32_16x16x32_bf16 v[44:47], v[176:179], a[140:143], v[44:47]\n"
      "v_mfma_f32_16x16x32_bf16 v[0:3], v[252:255], v[28:31], v[0:3]\n"
      "v_add_f32 v192, v224, v228\n"
      "v_mfma_f32_16x16x32_bf16 v[48:51], v[176:179], a[144:147], v[48:51]\n"
      "v_mfma_f32_16x16x32_bf16 v[4:7], v[248:251], v[28:31], v[4:7]\n"
      "v_add_f32 v193, v226, v230\n"
      "v_mfma_f32_16x16x32_bf16 v[52:55], v[176:179], a[148:151], v[52:55]\n"
      "v_add_f32 v194, v232, v236\n"
      "v_add_f32 v195, v234, v238\n"
      "v_mfma_f32_16x16x32_bf16 v[56:59], v[176:179], a[152:155], v[56:59]\n"
      "ds_read_b64_tr_b16 v[224:225], v201 offset:0\n"
      "v_permlane32_swap_b32 v192, v193\n"
      "v_mfma_f32_16x16x32_bf16 v[60:63], v[176:179], a[156:159], v[60:63]\n"
      "ds_read_b64_tr_b16 v[226:227], v201 offset:512\n"
      "v_permlane32_swap_b32 v194, v195\n"
      "v_mfma_f32_16x16x32_bf16 v[40:43], v[180:183], a[168:171], v[40:43]\n"
      "ds_read_b64_tr_b16 v[228:229], v201 offset:32\n"
      "v_add_f32 v192, v192, v193\n"
      "v_mfma_f32_16x16x32_bf16 v[44:47], v[180:183], a[172:175], v[44:47]\n"
      "ds_read_b64_tr_b16 v[230:231], v201 offset:544\n"
      "v_add_f32 v194, v194, v195\n"
      "v_mfma_f32_16x16x32_bf16 v[48:51], v[180:183], a[176:179], v[48:51]\n"
      "ds_read_b64_tr_b16 v[232:233], v201 offset:64\n"
      "v_mov_b32 v193, v192\n"
      "v_mfma_f32_16x16x32_bf16 v[52:55], v[180:183], a[180:183], v[52:55]\n"
      "ds_read_b64_tr_b16 v[234:235], v201 offset:576\n"
      "v_mov_b32 v195, v194\n"
      "v_mfma_f32_16x16x32_bf16 v[56:59], v[180:183], a[184:187], v[56:59]\n"
      "ds_read_b64_tr_b16 v[236:237], v201 offset:96\n"
      "v_permlane16_swap_b32 v192, v193\n"
      "v_mfma_f32_16x16x32_bf16 v[60:63], v[180:183], a[188:191], v[60:63]\n"
      "ds_read_b64_tr_b16 v[238:239], v201 offset:608\n"
      "v_permlane16_swap_b32 v194, v195\n"
      "v_mfma_f32_16x16x32_bf16 v[40:43], v[184:187], a[200:203], v[40:43]\n"
      "v_add_f32 v192, v192, v193\n"
      "v_add_f32 v194, v194, v195\n"
      "v_mfma_f32_16x16x32_bf16 v[44:47], v[184:187], a[204:207], v[44:47]\n"
      "v_cvt_pk_bf16_f32 v192, v192, v194\n"
      "v_and_b32 v194, 0xffff0000, v192\n"
      "v_lshlrev_b32 v192, 16, v192\n"
      "v_mfma_f32_16x16x32_bf16 v[48:51], v[184:187], a[208:211], v[48:51]\n"
      "v_mul_f32 v193, 0xbfb8aa3b, v192\n"
      "v_exp_f32 v193, v193\n"
      "s_nop 0\n"
      "v_add_f32 v193, 1.0, v193\n"
      "v_mfma_f32_16x16x32_bf16 v[52:55], v[184:187], a[212:215], v[52:55]\n"
      "v_rcp_f32 v193, v193\n"
      "s_nop 0\n"
      "v_mul_f32 v192, v192, v193\n"
      "v_mfma_f32_16x16x32_bf16 v[56:59], v[184:187], a[216:219], v[56:59]\n"
      "v_cvt_pk_bf16_f32 v192, v192, v192\n"
      "v_and_b32 v192, 0xffff0000, v192\n"
      "v_mul_f32 v192, v192, v194\n"
      "v_mfma_f32_16x16x32_bf16 v[60:63], v[184:187], a[220:223], v[60:63]\n"
      "v_cvt_pk_bf16_f32 v192, v192, v192\n"
      "v_mov_b32 v11, s37\n"
      "v_mfma_f32_16x16x32_bf16 v[40:43], v[188:191], a[232:235], v[40:43]\n"
      "ds_write_b16 v202, v192\n"
      "ds_read_b128 v[192:195], v199\n"
      "v_mfma_f32_16x16x32_bf16 v[44:47], v[188:191], a[236:239], v[44:47]\n"
      "v_cvt_pk_bf16_f32 v8, v0, v1\n"
      "v_cvt_pk_bf16_f32 v9, v2, v3\n"
      "v_cvt_pk_bf16_f32 v10, v4, v5\n"
      "v_mfma_f32_16x16x32_bf16 v[48:51], v[188:191], a[240:243], v[48:51]\n"
      "v_cvt_pk_bf16_f32 v247, v6, v7\n"
      "v_mfma_f32_16x16x32_bf16 v[52:55], v[188:191], a[244:247], v[52:55]\n"
      "global_store_dwordx4 v242, v[8:11], s[52:53] offset:0 sc1\n"
      "v_mfma_f32_16x16x32_bf16 v[56:59], v[188:191], a[248:251], v[56:59]\n"
      "v_mfma_f32_16x16x32_bf16 v[60:63], v[188:191], a[252:255], v[60:63]\n"
      "v_mfma_f32_16x16x32_bf16 v[32:35], v[176:179], a[128:131], v[32:35]\n"
      "s_waitcnt lgkmcnt(0)\n"
      "v_mfma_f32_16x16x32_bf16 v[196:199], v[192:195], v[224:227], 0\n"
      "v_mfma_f32_16x16x32_bf16 v[36:39], v[176:179], a[132:135], v[36:39]\n"
      "global_load_dwordx4 v[176:179], v240, s[14:15] offset:3072 sc1\n"
      "v_mfma_f32_16x16x32_bf16 v[224:227], v[192:195], v[228:231], 0\n"
      "v_cvt_pk_bf16_f32 v40, v40, v41\n"
      "v_mfma_f32_16x16x32_bf16 v[32:35], v[180:183], a[160:163], v[32:35]\n"
      "v_mfma_f32_16x16x32_bf16 v[228:231], v[192:195], v[232:235], 0\n"
      "v_mfma_f32_16x16x32_bf16 v[232:235], v[192:195], v[236:239], 0\n"
      "v_mov_b32 v193, s37\n"
      "v_mfma_f32_16x16x32_bf16 v[36:39], v[180:183], a[164:167], v[36:39]\n"
      "global_load_dwordx4 v[180:183], v240, s[74:75] offset:3072 sc1\n"
      "v_mov_b32 v195, s37\n"
      "v_cvt_pk_bf16_f32 v41, v42, v43\n"
      "v_mfma_f32_16x16x32_bf16 v[32:35], v[184:187], a[192:195], v[32:35]\n"
      "ds_write_addtid_b32 v40 offset:3072\n"
      "v_cvt_pk_bf16_f32 v42, v44, v45\n"
      "v_mfma_f32_16x16x32_bf16 v[36:39], v[184:187], a[196:199], v[36:39]\n"
      "global_load_dwordx4 v[184:187], v241, s[14:15] offset:3072 sc1\n"
      "ds_write_addtid_b32 v41 offset:3328\n"
      "v_cndmask_b32 v192, v196, v228, s[64:65]\n"
      "v_mfma_f32_16x16x32_bf16 v[32:35], v[188:191], a[224:227], v[32:35]\n"
      "v_cndmask_b32 v194, v224, v232, s[64:65]\n"
      "v_and_b32 v192, s82, v192\n"
      "v_and_b32 v194, s82, v194\n"
      "v_mfma_f32_16x16x32_bf16 v[36:39], v[188:191], a[228:231], v[36:39]\n"
      "global_load_dwordx4 v[188:191], v241, s[74:75] offset:3072 sc1\n"
      "s_mov_b64 exec, s[66:67]\n"
      "global_store_dwordx4 v203, v[192:195], s[58:59] sc1\n"
      "s_mov_b64 exec, -1\n"
      "v_mfma_f32_16x16x32_bf16 v[72:75], v[128:131], a[8:11], 0\n"
      "s_lshl2_add_u32 s54, s36, s33\n"
      "s_lshl_b32 s55, s54, 15\n"
      "s_lshl2_add_u32 s56, s35, s34\n"
      "s_lshl_b32 s56, s56, 6\n"
      "v_mfma_f32_16x16x32_bf16 v[76:79], v[128:131], a[12:15], 0\n"
      "s_add_u32 s55, s55, s56\n"
      "v_and_b32 v8, 15, v243\n"
      "v_and_b32 v7, 48, v243\n"
      "v_lshl_or_b32 v7, v8, 10, v7\n"
      "v_mfma_f32_16x16x32_bf16 v[80:83], v[128:131], a[16:19], 0\n"
      "v_add_u32 v7, s55, v7\n"
      "v_cvt_pk_bf16_f32 v43, v46, v47\n"
      "v_mfma_f32_16x16x32_bf16 v[84:87], v[128:131], a[20:23], 0\n"
      "ds_write_addtid_b32 v42 offset:3584\n"
      "v_cvt_pk_bf16_f32 v44, v48, v49\n"
      "v_mfma_f32_16x16x32_bf16 v[88:91], v[128:131], a[24:27], 0\n"
      "ds_write_addtid_b32 v43 offset:3840\n"
      "v_cvt_pk_bf16_f32 v45, v50, v51\n"
      "v_mfma_f32_16x16x32_bf16 v[92:95], v[128:131], a[28:31], 0\n"
      "ds_write_addtid_b32 v44 offset:4096\n"
      "v_cvt_pk_bf16_f32 v46, v52, v53\n"
      "v_mfma_f32_16x16x32_bf16 v[72:75], v[132:135], a[40:43], v[72:75]\n"
      "ds_write_addtid_b32 v45 offset:4352\n"
      "v_cvt_pk_bf16_f32 v47, v54, v55\n"
      "v_mfma_f32_16x16x32_bf16 v[76:79], v[132:135], a[44:47], v[76:79]\n"
      "ds_write_addtid_b32 v46 offset:4608\n"
      "v_cvt_pk_bf16_f32 v48, v56, v57\n"
      "v_mfma_f32_16x16x32_bf16 v[80:83], v[132:135], a[48:51], v[80:83]\n"
      "ds_write_addtid_b32 v47 offset:4864\n"
      "v_cvt_pk_bf16_f32 v49, v58, v59\n"
      "v_mfma_f32_16x16x32_bf16 v[84:87], v[132:135], a[52:55], v[84:87]\n"
      "ds_write_addtid_b32 v48 offset:5120\n"
      "v_cvt_pk_bf16_f32 v50, v60, v61\n"
      "v_mfma_f32_16x16x32_bf16 v[88:91], v[132:135], a[56:59], v[88:91]\n"
      "ds_write_addtid_b32 v49 offset:5376\n"
      "v_cvt_pk_bf16_f32 v51, v62, v63\n"
      "v_mfma_f32_16x16x32_bf16 v[92:95], v[132:135], a[60:63], v[92:95]\n"
      "ds_write_addtid_b32 v50 offset:5632\n"
      "ds_write_addtid_b32 v51 offset:5888\n"
      "v_mfma_f32_16x16x32_bf16 v[72:75], v[136:139], a[72:75], v[72:75]\n"
      "v_mfma_f32_16x16x32_bf16 v[76:79], v[136:139], a[76:79], v[76:79]\n"
      "v_mfma_f32_16x16x32_bf16 v[80:83], v[136:139], a[80:83], v[80:83]\n"
      "s_waitcnt lgkmcnt(0)\n"
      "s_barrier\n"
      "v_mfma_f32_16x16x32_bf16 v[84:87], v[136:139], a[84:87], v[84:87]\n"
      "ds_read2st64_b32 v[52:53], v244 offset0:12 offset1:13\n"
      "ds_read2st64_b32 v[54:55], v245 offset0:16 offset1:17\n"
      "v_mfma_f32_16x16x32_bf16 v[88:91], v[136:139], a[88:91], v[88:91]\n"
      "ds_read2st64_b32 v[56:57], v244 offset0:14 offset1:15\n"
      "ds_read2st64_b32 v[58:59], v245 offset0:18 offset1:19\n"
      "v_add_u32 v4, 0, v242\n"
      "v_mfma_f32_16x16x32_bf16 v[92:95], v[136:139], a[92:95], v[92:95]\n"
      "global_load_dwordx4 v[12:15], v4, s[62:63] offset:0 sc1\n"
      "global_load_dwordx4 v[16:19], v4, s[62:63] offset:1024 sc1\n"
      "v_mfma_f32_16x16x32_bf16 v[72:75], v[140:143], a[104:107], v[72:75]\n"
      "global_load_dwordx4 v[20:23], v4, s[62:63] offset:2048 sc1\n"
      "ds_read2st64_b32 v[60:61], v246 offset0:20 offset1:21\n"
      "v_mfma_f32_16x16x32_bf16 v[76:79], v[140:143], a[108:111], v[76:79]\n"
      "ds_read2st64_b32 v[62:63], v246 offset0:22 offset1:23\n"
      "v_mfma_f32_16x16x32_bf16 v[80:83], v[140:143], a[112:115], v[80:83]\n"
      "v_mfma_f32_16x16x32_bf16 v[84:87], v[140:143], a[116:119], v[84:87]\n"
      "v_mfma_f32_16x16x32_bf16 v[88:91], v[140:143], a[120:123], v[88:91]\n"
      "v_mfma_f32_16x16x32_bf16 v[92:95], v[140:143], a[124:127], v[92:95]\n"
      "s_waitcnt vmcnt(18)\n"
      "v_mfma_f32_16x16x32_bf16 v[72:75], v[144:147], a[136:139], v[72:75]\n"
      "s_waitcnt lgkmcnt(0)\n"
      "v_mfma_f32_16x16x32_bf16 v[32:35], v[250:253], v[52:55], v[32:35]\n"
      "v_mfma_f32_16x16x32_bf16 v[36:39], v[250:253], v[56:59], v[36:39]\n"
      "v_mfma_f32_16x16x32_bf16 v[76:79], v[144:147], a[140:143], v[76:79]\n"
      "v_mfma_f32_16x16x32_bf16 v[32:35], v[252:255], v[60:63], v[32:35]\n"
      "v_mfma_f32_16x16x32_bf16 v[36:39], v[248:251], v[60:63], v[36:39]\n"
      "v_mov_b32 v43, s37\n"
      "v_mfma_f32_16x16x32_bf16 v[80:83], v[144:147], a[144:147], v[80:83]\n"
      "v_mov_b32 v44, v247\n"
      "v_mov_b32 v47, s37\n"
      "v_mfma_f32_16x16x32_bf16 v[84:87], v[144:147], a[148:151], v[84:87]\n"
      "v_mfma_f32_16x16x32_bf16 v[88:91], v[144:147], a[152:155], v[88:91]\n"
      "s_nop 0\n"
      "v_cvt_pk_bf16_f32 v40, v32, v33\n"
      "v_mfma_f32_16x16x32_bf16 v[92:95], v[144:147], a[156:159], v[92:95]\n"
      "v_cvt_pk_bf16_f32 v41, v34, v35\n"
      "v_cvt_pk_bf16_f32 v42, v36, v37\n"
      "v_mfma_f32_16x16x32_bf16 v[72:75], v[148:151], a[168:171], v[72:75]\n"
      "v_cvt_pk_bf16_f32 v45, v38, v39\n"
      "v_mfma_f32_16x16x32_bf16 v[76:79], v[148:151], a[172:175], v[76:79]\n"
      "global_store_dwordx4 v242, v[40:43], s[52:53] offset:1024 sc1\n"
      "v_mfma_f32_16x16x32_bf16 v[80:83], v[148:151], a[176:179], v[80:83]\n"
      "global_store_dwordx4 v242, v[44:47], s[52:53] offset:2048 sc1\n"
      "v_mfma_f32_16x16x32_bf16 v[84:87], v[148:151], a[180:183], v[84:87]\n"
      "v_mfma_f32_16x16x32_bf16 v[88:91], v[148:151], a[184:187], v[88:91]\n"
      "v_mfma_f32_16x16x32_bf16 v[92:95], v[148:151], a[188:191], v[92:95]\n"
      "s_waitcnt vmcnt(15)\n"
      "v_mfma_f32_16x16x32_bf16 v[72:75], v[152:155], a[200:203], v[72:75]\n"
      "v_mfma_f32_16x16x32_bf16 v[76:79], v[152:155], a[204:207], v[76:79]\n"
      "v_mfma_f32_16x16x32_bf16 v[80:83], v[152:155], a[208:211], v[80:83]\n"
      "v_mfma_f32_16x16x32_bf16 v[84:87], v[152:155], a[212:215], v[84:87]\n"
      "v_mfma_f32_16x16x32_bf16 v[88:91], v[152:155], a[216:219], v[88:91]\n"
      "v_mfma_f32_16x16x32_bf16 v[92:95], v[152:155], a[220:223], v[92:95]\n"
      "v_mfma_f32_16x16x32_bf16 v[72:75], v[156:159], a[232:235], v[72:75]\n"
      "v_mfma_f32_16x16x32_bf16 v[76:79], v[156:159], a[236:239], v[76:79]\n"
      "v_mfma_f32_16x16x32_bf16 v[80:83], v[156:159], a[240:243], v[80:83]\n"
      "v_mfma_f32_16x16x32_bf16 v[84:87], v[156:159], a[244:247], v[84:87]\n"
      "v_mfma_f32_16x16x32_bf16 v[88:91], v[156:159], a[248:251], v[88:91]\n"
      "v_mfma_f32_16x16x32_bf16 v[92:95], v[156:159], a[252:255], v[92:95]\n"
      "s_waitcnt vmcnt(13)\n"
      "v_mfma_f32_16x16x32_bf16 v[104:107], v[160:163], a[8:11], 0\n"
      "v_mfma_f32_16x16x32_bf16 v[108:111], v[160:163], a[12:15], 0\n"
      "v_cvt_pk_bf16_f32 v72, v72, v73\n"
      "v_cvt_pk_bf16_f32 v73, v74, v75\n"
      "v_mfma_f32_16x16x32_bf16 v[112:115], v[160:163], a[16:19], 0\n"
      "ds_write_addtid_b32 v72 offset:6144\n"
      "v_cvt_pk_bf16_f32 v74, v76, v77\n"
      "v_mfma_f32_16x16x32_bf16 v[116:119], v[160:163], a[20:23], 0\n"
      "ds_write_addtid_b32 v73 offset:6400\n"
      "v_cvt_pk_bf16_f32 v75, v78, v79\n"
      "v_mfma_f32_16x16x32_bf16 v[120:123], v[160:163], a[24:27], 0\n"
      "ds_write_addtid_b32 v74 offset:6656\n"
      "v_cvt_pk_bf16_f32 v76, v80, v81\n"
      "v_mfma_f32_16x16x32_bf16 v[124:127], v[160:163], a[28:31], 0\n"
      "ds_write_addtid_b32 v75 offset:6912\n"
      "v_cvt_pk_bf16_f32 v77, v82, v83\n"
      "v_mfma_f32_16x16x32_bf16 v[104:107], v[164:167], a[40:43], v[104:107]\n"
      "ds_write_addtid_b32 v76 offset:7168\n"
      "v_cvt_pk_bf16_f32 v78, v84, v85\n"
      "v_mfma_f32_16x16x32_bf16 v[108:111], v[164:167], a[44:47], v[108:111]\n"
      "ds_write_addtid_b32 v77 offset:7424\n"
      "v_cvt_pk_bf16_f32 v79, v86, v87\n"
      "v_mfma_f32_16x16x32_bf16 v[112:115], v[164:167], a[48:51], v[112:115]\n"
      "ds_write_addtid_b32 v78 offset:7680\n"
      "v_cvt_pk_bf16_f32 v80, v88, v89\n"
      "v_mfma_f32_16x16x32_bf16 v[116:119], v[164:167], a[52:55], v[116:119]\n"
      "ds_write_addtid_b32 v79 offset:7936\n"
      "v_cvt_pk_bf16_f32 v81, v90, v91\n"
      "v_mfma_f32_16x16x32_bf16 v[120:123], v[164:167], a[56:59], v[120:123]\n"
      "ds_write_addtid_b32 v80 offset:8192\n"
      "v_cvt_pk_bf16_f32 v82, v92, v93\n"
      "v_mfma_f32_16x16x32_bf16 v[124:127], v[164:167], a[60:63], v[124:127]\n"
      "s_waitcnt vmcnt(11)\n"
      "ds_write_addtid_b32 v81 offset:8448\n"
      "v_cvt_pk_bf16_f32 v83, v94, v95\n"
      "v_mfma_f32_16x16x32_bf16 v[104:107], v[168:171], a[72:75], v[104:107]\n"
      "ds_write_addtid_b32 v82 offset:8704\n"
      "v_mfma_f32_16x16x32_bf16 v[108:111], v[168:171], a[76:79], v[108:111]\n"
      "ds_write_addtid_b32 v83 offset:8960\n"
      "v_mfma_f32_16x16x32_bf16 v[112:115], v[168:171], a[80:83], v[112:115]\n"
      "s_waitcnt vmcnt(2)\n"
      "v_bitop3_b32 v22, v15, v19, s37 bitop3:0x7e\n"
      "v_bitop3_b32 v22, v22, v23, s37 bitop3:0xf6\n"
      "v_cmp_ne_u32 vcc, 0, v22\n"
      "s_nop 1\n"
      "s_cbranch_vccnz .Ls_s20_%=\n"
      ".Ls_o21_%=:\n"
      "v_mfma_f32_16x16x32_bf16 v[116:119], v[168:171], a[84:87], v[116:119]\n"
      "v_mov_b32 v15, v20\n"
      "v_mov_b32 v19, v21\n"
      "v_mfma_f32_16x16x32_bf16 v[120:123], v[168:171], a[88:91], v[120:123]\n"
      "s_waitcnt lgkmcnt(0)\n"
      "s_barrier\n"
      "v_mfma_f32_16x16x32_bf16 v[124:127], v[168:171], a[92:95], v[124:127]\n"
      "ds_read2st64_b32 v[84:85], v244 offset0:24 offset1:25\n"
      "v_mfma_f32_16x16x32_bf16 v[104:107], v[172:175], a[104:107], v[104:107]\n"
      "ds_read2st64_b32 v[86:87], v245 offset0:28 offset1:29\n"
      "v_add_u32 v5, 12288, v242\n"
      "v_mfma_f32_16x16x32_bf16 v[108:111], v[172:175], a[108:111], v[108:111]\n"
      "global_load_dwordx4 v[44:47], v5, s[62:63] offset:0 sc1\n"
      "v_mfma_f32_16x16x32_bf16 v[112:115], v[172:175], a[112:115], v[112:115]\n"
      "global_load_dwordx4 v[48:51], v5, s[62:63] offset:1024 sc1\n"
      "v_mfma_f32_16x16x32_bf16 v[116:119], v[172:175], a[116:119], v[116:119]\n"
      "global_load_dwordx4 v[52:55], v5, s[62:63] offset:2048 sc1\n"
      "v_mfma_f32_16x16x32_bf16 v[120:123], v[172:175], a[120:123], v[120:123]\n"
      "ds_read2st64_b32 v[88:89], v244 offset0:26 offset1:27\n"
      "v_mfma_f32_16x16x32_bf16 v[124:127], v[172:175], a[124:127], v[124:127]\n"
      "ds_read2st64_b32 v[90:91], v245 offset0:30 offset1:31\n"
      "v_mfma_f32_16x16x32_bf16 v[104:107], v[176:179], a[136:139], v[104:107]\n"
      "ds_read2st64_b32 v[92:93], v246 offset0:32 offset1:33\n"
      "v_mfma_f32_16x16x32_bf16 v[108:111], v[176:179], a[140:143], v[108:111]\n"
      "ds_read2st64_b32 v[94:95], v246 offset0:34 offset1:35\n"
      "v_mfma_f32_16x16x32_bf16 v[112:115], v[176:179], a[144:147], v[112:115]\n"
      "v_mfma_f32_16x16x32_bf16 v[64:67], v[252:255], v[12:15], 0\n"
      "v_mfma_f32_16x16x32_bf16 v[116:119], v[176:179], a[148:151], v[116:119]\n"
      "v_mfma_f32_16x16x32_bf16 v[68:71], v[248:251], v[12:15], 0\n"
      "v_mfma_f32_16x16x32_bf16 v[120:123], v[176:179], a[152:155], v[120:123]\n"
      "v_mfma_f32_16x16x32_bf16 v[96:99], v[252:255], v[16:19], 0\n"
      "v_mfma_f32_16x16x32_bf16 v[124:127], v[176:179], a[156:159], v[124:127]\n"
      "v_mfma_f32_16x16x32_bf16 v[100:103], v[248:251], v[16:19], 0\n"
      "v_mfma_f32_16x16x32_bf16 v[104:107], v[180:183], a[168:171], v[104:107]\n"
      "s_waitcnt lgkmcnt(0)\n"
      "v_mfma_f32_16x16x32_bf16 v[64:67], v[250:253], v[84:87], v[64:67]\n"
      "v_mfma_f32_16x16x32_bf16 v[108:111], v[180:183], a[172:175], v[108:111]\n"
      "v_mfma_f32_16x16x32_bf16 v[68:71], v[250:253], v[88:91], v[68:71]\n"
      "v_mfma_f32_16x16x32_bf16 v[112:115], v[180:183], a[176:179], v[112:115]\n"
      "v_mfma_f32_16x16x32_bf16 v[64:67], v[252:255], v[92:95], v[64:67]\n"
      "v_mfma_f32_16x16x32_bf16 v[116:119], v[180:183], a[180:183], v[116:119]\n"
      "v_mfma_f32_16x16x32_bf16 v[68:71], v[248:251], v[92:95], v[68:71]\n"
      "v_mfma_f32_16x16x32_bf16 v[120:123], v[180:183], a[184:187], v[120:123]\n"
      "v_mfma_f32_16x16x32_bf16 v[124:127], v[180:183], a[188:191], v[124:127]\n"
      "v_mfma_f32_16x16x32_bf16 v[104:107], v[184:187], a[200:203], v[104:107]\n"
      "v_mfma_f32_16x16x32_bf16 v[108:111], v[184:187], a[204:207], v[108:111]\n"
      "v_mfma_f32_16x16x32_bf16 v[112:115], v[184:187], a[208:211], v[112:115]\n"
      "v_mfma_f32_16x16x32_bf16 v[116:119], v[184:187], a[212:215], v[116:119]\n"
      "v_mfma_f32_16x16x32_bf16 v[120:123], v[184:187], a[216:219], v[120:123]\n"
      "v_mfma_f32_16x16x32_bf16 v[124:127], v[184:187], a[220:223], v[124:127]\n"
      "v_mfma_f32_16x16x32_bf16 v[104:107], v[188:191], a[232:235], v[104:107]\n"
      "v_mfma_f32_16x16x32_bf16 v[108:111], v[188:191], a[236:239], v[108:111]\n"
      "s_lshr_b32 s43, s42, 7\n"
      "s_cmp_eq_u32 s43, s36\n"
      "s_cbranch_scc0 .Ls_e22_%=\n"
      "s_lshl2_add_u32 s44, s35, s34\n"
      "s_lshl2_add_u32 s43, s44, s33\n"
      "s_lshl_b32 s43, s43, 12\n"
      "s_add_u32 s43, s43, 524288\n"
      "v_add_u32 v60, s43, v242\n"
      "global_load_dwordx4 v[24:27], v60, s[58:59] offset:0 sc1\n"
      "global_load_dwordx4 v[28:31], v60, s[58:59] offset:1024 sc1\n"
      "global_load_dwordx4 v[32:35], v60, s[58:59] offset:2048 sc1\n"
      "global_load_dwordx4 v[36:39], v60, s[58:59] offset:3072 sc1\n"
      "v_and_b32 v58, 15, v243\n"
      "v_lshrrev_b32 v59, 5, v243\n"
      "v_lshl_or_b32 v58, v59, 4, v58\n"
      "s_lshl4_add_u32 s44, s42, s44\n"
      "s_lshl_b32 s44, s44, 6\n"
      "v_lshl_add_u32 v59, v58, 1, s44\n"
      "global_load_ushort v57, v59, s[16:17]\n"
      "s_mov_b32 s44, 131072\n"
      "v_lshl_add_u32 v58, v58, 2, s44\n"
      "s_lshl_b32 s43, s33, 13\n"
      "v_add_u32 v59, s43, v58\n"
      ".Ls_e22_%=:\n"
      "v_mfma_f32_16x16x32_bf16 v[112:115], v[188:191], a[240:243], v[112:115]\n"
      "v_mfma_f32_16x16x32_bf16 v[116:119], v[188:191], a[244:247], v[116:119]\n"
      "v_mfma_f32_16x16x32_bf16 v[120:123], v[188:191], a[248:251], v[120:123]\n"
      "v_mfma_f32_16x16x32_bf16 v[124:127], v[188:191], a[252:255], v[124:127]\n"
      "v_mfma_f32_16x16x32_bf16 v[64:67], v[128:131], a[0:3], v[64:67]\n"
      "v_mfma_f32_16x16x32_bf16 v[68:71], v[128:131], a[4:7], v[68:71]\n"
      "v_cvt_pk_bf16_f32 v104, v104, v105\n"
      "v_cvt_pk_bf16_f32 v105, v106, v107\n"
      "v_mfma_f32_16x16x32_bf16 v[64:67], v[132:135], a[32:35], v[64:67]\n"
      "ds_write_addtid_b32 v104 offset:9216\n"
      "v_cvt_pk_bf16_f32 v106, v108, v109\n"
      "v_mfma_f32_16x16x32_bf16 v[68:71], v[132:135], a[36:39], v[68:71]\n"
      "ds_write_addtid_b32 v105 offset:9472\n"
      "v_cvt_pk_bf16_f32 v107, v110, v111\n"
      "v_mfma_f32_16x16x32_bf16 v[64:67], v[136:139], a[64:67], v[64:67]\n"
      "ds_write_addtid_b32 v106 offset:9728\n"
      "v_cvt_pk_bf16_f32 v108, v112, v113\n"
      "v_mfma_f32_16x16x32_bf16 v[68:71], v[136:139], a[68:71], v[68:71]\n"
      "ds_write_addtid_b32 v107 offset:9984\n"
      "v_cvt_pk_bf16_f32 v109, v114, v115\n"
      "v_mfma_f32_16x16x32_bf16 v[64:67], v[140:143], a[96:99], v[64:67]\n"
      "ds_write_addtid_b32 v108 offset:10240\n"
      "v_cvt_pk_bf16_f32 v110, v116, v117\n"
      "v_mfma_f32_16x16x32_bf16 v[68:71], v[140:143], a[100:103], v[68:71]\n"
      "ds_write_addtid_b32 v109 offset:10496\n"
      "v_cvt_pk_bf16_f32 v111, v118, v119\n"
      "v_mfma_f32_16x16x32_bf16 v[64:67], v[144:147], a[128:131], v[64:67]\n"
      "ds_write_addtid_b32 v110 offset:10752\n"
      "v_cvt_pk_bf16_f32 v112, v120, v121\n"
      "v_mfma_f32_16x16x32_bf16 v[68:71], v[144:147], a[132:135], v[68:71]\n"
      "ds_write_addtid_b32 v111 offset:11008\n"
      "v_cvt_pk_bf16_f32 v113, v122, v123\n"
      "v_mfma_f32_16x16x32_bf16 v[64:67], v[148:151], a[160:163], v[64:67]\n"
      "ds_write_addtid_b32 v112 offset:11264\n"
      "v_add_u32 v6, 24576, v242\n"
      "v_cvt_pk_bf16_f32 v114, v124, v125\n"
      "v_mfma_f32_16x16x32_bf16 v[68:71], v[148:151], a[164:167], v[68:71]\n"
      "global_load_dwordx4 v[224:227], v6, s[62:63] offset:0 sc1\n"
      "global_load_dwordx4 v[228:231], v6, s[62:63] offset:1024 sc1\n"
      "v_mfma_f32_16x16x32_bf16 v[64:67], v[152:155], a[192:195], v[64:67]\n"
      "global_load_dwordx4 v[232:235], v6, s[62:63] offset:2048 sc1\n"
      "ds_write_addtid_b32 v113 offset:11520\n"
      "v_cvt_pk_bf16_f32 v115, v126, v127\n"
      "v_mfma_f32_16x16x32_bf16 v[68:71], v[152:155], a[196:199], v[68:71]\n"
      "ds_write_addtid_b32 v114 offset:11776\n"
      "ds_write_addtid_b32 v115 offset:12032\n"
      "v_mfma_f32_16x16x32_bf16 v[64:67], v[156:159], a[224:227], v[64:67]\n"
      "s_waitcnt vmcnt(3)\n"
      "v_bitop3_b32 v54, v47, v51, s37 bitop3:0x7e\n"
      "v_bitop3_b32 v54, v54, v55, s37 bitop3:0xf6\n"
      "v_cmp_ne_u32 vcc, 0, v54\n"
      "s_nop 1\n"
      "s_cbranch_vccnz .Ls_s24_%=\n"
      ".Ls_o25_%=:\n"
      "v_mfma_f32_16x16x32_bf16 v[68:71], v[156:159], a[228:231], v[68:71]\n"
      "v_mov_b32 v47, v52\n"
      "v_mov_b32 v51, v53\n"
      "v_mfma_f32_16x16x32_bf16 v[96:99], v[160:163], a[0:3], v[96:99]\n"
      "s_lshr_b32 s43, s42, 7\n"
      "s_cmp_eq_u32 s43, s36\n"
      "s_cbranch_scc0 .Ls_e26_%=\n"
      "s_waitcnt vmcnt(3)\n"
      "v_bitop3_b32 v61, v25, v27, s37 bitop3:0x7e\n"
      "v_bitop3_b32 v61, v61, v29, s37 bitop3:0xf6\n"
      "v_bitop3_b32 v61, v61, v31, s37 bitop3:0xf6\n"
      "v_bitop3_b32 v61, v61, v33, s37 bitop3:0xf6\n"
      "v_bitop3_b32 v61, v61, v35, s37 bitop3:0xf6\n"
      "v_bitop3_b32 v61, v61, v37, s37 bitop3:0xf6\n"
      "v_bitop3_b32 v61, v61, v39, s37 bitop3:0xf6\n"
      "v_cmp_ne_u32 vcc, 0, v61\n"
      "s_nop 1\n"
      "s_cbranch_vccnz .Ls_s28_%=\n"
      ".Ls_o29_%=:\n"
      "v_add_f32 v24, v24, v28\n"
      "v_add_f32 v26, v26, v30\n"
      "v_add_f32 v32, v32, v36\n"
      "v_add_f32 v34, v34, v38\n"
      "v_add_f32 v24, v24, v32\n"
      "v_add_f32 v26, v26, v34\n"
      "s_nop 1\n"
      "v_permlane32_swap_b32 v24, v26\n"
      "v_add_f32 v24, v24, v26\n"
      "v_mov_b32 v26, v24\n"
      "s_nop 1\n"
      "v_permlane16_swap_b32 v24, v26\n"
      "v_add_f32 v24, v24, v26\n"
      "ds_write_b32 v59, v24\n"
      "s_waitcnt lgkmcnt(0)\n"
      ".Ls_e26_%=:\n"
      "v_mfma_f32_16x16x32_bf16 v[100:103], v[160:163], a[4:7], v[100:103]\n"
      "s_waitcnt lgkmcnt(0)\n"
      "s_barrier\n"
      "v_mfma_f32_16x16x32_bf16 v[96:99], v[164:167], a[32:35], v[96:99]\n"
      "s_lshr_b32 s43, s42, 5\n"
      "s_lshl2_add_u32 s44, s36, s33\n"
      "s_cmp_eq_u32 s43, s44\n"
      "s_cbranch_scc0 .Ls_e30_%=\n"
      "ds_read_b32 v24, v58 offset:0\n"
      "ds_read_b32 v25, v58 offset:8192\n"
      "ds_read_b32 v26, v58 offset:16384\n"
      "ds_read_b32 v27, v58 offset:24576\n"
      "v_mov_b32 v62, 0\n"
      "v_mov_b32 v63, s37\n"
      "s_or_b32 s43, s34, s35\n"
      "s_cmp_eq_u32 s43, 0\n"
      "s_cbranch_scc0 .Ls_z31_%=\n"
      "s_mov_b64 exec, 1\n"
      "global_store_dword v62, v63, s[58:59] sc1\n"
      ".Ls_z31_%=:\n"
      "s_mov_b64 exec, -1\n"
      "s_waitcnt vmcnt(3)\n"
      "v_lshlrev_b32 v57, 16, v57\n"
      "v_and_b32 v56, 48, v243\n"
      "s_mov_b32 s44, 131072\n"
      "v_lshl_add_u32 v56, v56, 1, s44\n"
      "s_waitcnt lgkmcnt(0)\n"
      "v_add_f32 v24, v24, v25\n"
      "s_waitcnt lgkmcnt(0)\n"
      "v_add_f32 v26, v26, v27\n"
      "v_add_f32 v24, v24, v26\n"
      "v_cvt_pk_bf16_f32 v24, v24, v24\n"
      "v_and_b32 v24, 0xffff0000, v24\n"
      "v_mul_f32 v24, s84, v24\n"
      "v_cvt_pk_bf16_f32 v24, v24, v24\n"
      "v_and_b32 v24, 0xffff0000, v24\n"
      "v_add_f32 v24, v24, v57\n"
      "v_cvt_pk_bf16_f32 v24, v24, v24\n"
      "v_and_b32 v24, 0xffff0000, v24\n"
      "v_sub_f32 v24, v24, v57\n"
      "ds_write_b32 v58, v24 offset:1024\n"
      "ds_read_b128 v[28:31], v56 offset:1024\n"
      "ds_read_b128 v[32:35], v56 offset:1040\n"
      ".Ls_e30_%=:\n"
      "v_mfma_f32_16x16x32_bf16 v[100:103], v[164:167], a[36:39], v[100:103]\n"
      "ds_read2st64_b32 v[116:117], v244 offset0:36 offset1:37\n"
      "ds_read2st64_b32 v[118:119], v245 offset0:40 offset1:41\n"
      "v_mfma_f32_16x16x32_bf16 v[96:99], v[168:171], a[64:67], v[96:99]\n"
      "ds_read2st64_b32 v[120:121], v244 offset0:38 offset1:39\n"
      "ds_read2st64_b32 v[122:123], v245 offset0:42 offset1:43\n"
      "v_mfma_f32_16x16x32_bf16 v[100:103], v[168:171], a[68:71], v[100:103]\n"
      "ds_read2st64_b32 v[124:125], v246 offset0:44 offset1:45\n"
      "ds_read2st64_b32 v[126:127], v246 offset0:46 offset1:47\n"
      "v_mfma_f32_16x16x32_bf16 v[96:99], v[172:175], a[96:99], v[96:99]\n"
      "v_mfma_f32_16x16x32_bf16 v[64:67], v[252:255], v[44:47], v[64:67]\n"
      "v_mfma_f32_16x16x32_bf16 v[68:71], v[248:251], v[44:47], v[68:71]\n"
      "v_mfma_f32_16x16x32_bf16 v[100:103], v[172:175], a[100:103], v[100:103]\n"
      "v_mfma_f32_16x16x32_bf16 v[96:99], v[252:255], v[48:51], v[96:99]\n"
      "v_mfma_f32_16x16x32_bf16 v[100:103], v[248:251], v[48:51], v[100:103]\n"
      "v_mfma_f32_16x16x32_bf16 v[96:99], v[176:179], a[128:131], v[96:99]\n"
      "v_mfma_f32_16x16x32_bf16 v[100:103], v[176:179], a[132:135], v[100:103]\n"
      "v_mfma_f32_16x16x32_bf16 v[96:99], v[180:183], a[160:163], v[96:99]\n"
      "s_waitcnt lgkmcnt(0)\n"
      "v_mfma_f32_16x16x32_bf16 v[96:99], v[250:253], v[116:119], v[96:99]\n"
      "v_mfma_f32_16x16x32_bf16 v[100:103], v[250:253], v[120:123], v[100:103]\n"
      "v_mfma_f32_16x16x32_bf16 v[100:103], v[180:183], a[164:167], v[100:103]\n"
      "v_mfma_f32_16x16x32_bf16 v[96:99], v[252:255], v[124:127], v[96:99]\n"
      "v_mfma_f32_16x16x32_bf16 v[100:103], v[248:251], v[124:127], v[100:103]\n"
      "v_mfma_f32_16x16x32_bf16 v[96:99], v[184:187], a[192:195], v[96:99]\n"
      "v_mfma_f32_16x16x32_bf16 v[100:103], v[184:187], a[196:199], v[100:103]\n"
      "v_mfma_f32_16x16x32_bf16 v[96:99], v[188:191], a[224:227], v[96:99]\n"
      "v_mfma_f32_16x16x32_bf16 v[100:103], v[188:191], a[228:231], v[100:103]\n"
      "s_lshr_b32 s43, s42, 5\n"
      "s_lshl2_add_u32 s44, s36, s33\n"
      "s_cmp_eq_u32 s43, s44\n"
      "s_cbranch_scc0 .Ls_e32_%=\n"
      "v_lshrrev_b32 v24, 10, v7\n"
      "v_subrev_u32 v24, s42, v24\n"
      "v_cmp_eq_u32 vcc, 0, v24\n"
      "s_nop 1\n"
      "s_mov_b64 exec, vcc\n"
      "v_fma_f32 v208, v205, v28, v208\n"
      "v_fma_f32 v209, v205, v29, v209\n"
      "v_fma_f32 v210, v205, v30, v210\n"
      "v_fma_f32 v211, v205, v31, v211\n"
      "v_fma_f32 v216, v205, v32, v216\n"
      "v_fma_f32 v217, v205, v33, v217\n"
      "v_fma_f32 v218, v205, v34, v218\n"
      "v_fma_f32 v219, v205, v35, v219\n"
      "s_mov_b64 exec, -1\n"
      "v_cmp_eq_u32 vcc, -16, v24\n"
      "s_nop 1\n"
      "s_mov_b64 exec, vcc\n"
      "v_fma_f32 v212, v207, v28, v212\n"
      "v_fma_f32 v213, v207, v29, v213\n"
      "v_fma_f32 v214, v207, v30, v214\n"
      "v_fma_f32 v215, v207, v31, v215\n"
      "v_fma_f32 v220, v207, v32, v220\n"
      "v_fma_f32 v221, v207, v33, v221\n"
      "v_fma_f32 v222, v207, v34, v222\n"
      "v_fma_f32 v223, v207, v35, v223\n"
      "s_mov_b64 exec, -1\n"
      ".Ls_e32_%=:\n"
      "s_waitcnt vmcnt(0)\n"
      "v_bitop3_b32 v234, v227, v231, s37 bitop3:0x7e\n"
      "v_bitop3_b32 v234, v234, v235, s37 bitop3:0xf6\n"
      "v_cmp_ne_u32 vcc, 0, v234\n"
      "s_nop 1\n"
      "s_cbranch_vccnz .Ls_s34_%=\n"
      ".Ls_o35_%=:\n"
      "v_mov_b32 v227, v232\n"
      "v_mov_b32 v231, v233\n"
      "s_nop 0\n"
      "v_mfma_f32_16x16x32_bf16 v[64:67], v[252:255], v[224:227], v[64:67]\n"
      "v_mfma_f32_16x16x32_bf16 v[96:99], v[252:255], v[228:231], v[96:99]\n"
      "v_mfma_f32_16x16x32_bf16 v[68:71], v[248:251], v[224:227], v[68:71]\n"
      "v_mfma_f32_16x16x32_bf16 v[100:103], v[248:251], v[228:231], v[100:103]\n"
      "s_nop 4\n"
      "v_fma_f32 v208, v204, v64, v208\n"
      "v_fma_f32 v209, v204, v65, v209\n"
      "v_fma_f32 v210, v204, v66, v210\n"
      "v_fma_f32 v211, v204, v67, v211\n"
      "v_fma_f32 v216, v204, v96, v216\n"
      "v_fma_f32 v217, v204, v97, v217\n"
      "v_fma_f32 v218, v204, v98, v218\n"
      "v_fma_f32 v219, v204, v99, v219\n"
      "v_cvt_pk_bf16_f32 v72, v208, v209\n"
      "v_cvt_pk_bf16_f32 v73, v210, v211\n"
      "v_cvt_pk_bf16_f32 v74, v216, v217\n"
      "v_cvt_pk_bf16_f32 v75, v218, v219\n"
      "global_store_dwordx4 v7, v[72:75], s[20:21]\n"
      "v_fma_f32 v212, v206, v68, v212\n"
      "v_fma_f32 v213, v206, v69, v213\n"
      "v_fma_f32 v214, v206, v70, v214\n"
      "v_fma_f32 v215, v206, v71, v215\n"
      "v_fma_f32 v220, v206, v100, v220\n"
      "v_fma_f32 v221, v206, v101, v221\n"
      "v_fma_f32 v222, v206, v102, v222\n"
      "v_fma_f32 v223, v206, v103, v223\n"
      "v_cvt_pk_bf16_f32 v76, v212, v213\n"
      "v_cvt_pk_bf16_f32 v77, v214, v215\n"
      "v_cvt_pk_bf16_f32 v78, v220, v221\n"
      "v_cvt_pk_bf16_f32 v79, v222, v223\n"
      "global_store_dwordx4 v7, v[76:79], s[68:69]\n"
      "s_endpgm\n"
      ".Lprod_%=:\n"
      "s_add_u32 s58, s18, 4718592\n"
      "s_addc_u32 s59, s19, 0\n"
      "v_mov_b32 v235, 0\n"
      "global_load_dword v235, v235, s[58:59] sc1\n"
      "v_and_b32 v238, 15, v243\n"
      "v_lshrrev_b32 v239, 4, v243\n"
      "v_xor_b32 v239, v239, v238\n"
      "v_and_b32 v239, 7, v239\n"
      "v_lshlrev_b32 v239, 4, v239\n"
      "v_lshl_or_b32 v238, v238, 7, v239\n"
      "v_add_u32 v248, s41, v238\n"
      "v_xor_b32 v249, 64, v248\n"
      "v_add_u32 v250, 65536, v238\n"
      "v_xor_b32 v251, 64, v250\n"
      "v_and_b32 v252, 8, v243\n"
      "v_lshlrev_b32 v252, 7, v252\n"
      "v_and_or_b32 v252, v243, 48, v252\n"
      "v_add_u32 v252, s41, v252\n"
      "v_add_u32 v238, s80, v242\n"
      "v_and_b32 v238, 0x3ff, v238\n"
      "v_lshl_or_b32 v244, v236, 12, v237\n"
      "v_add_u32 v245, 31744, v244\n"
      "v_add_u32 v246, 63488, v244\n"
      "v_add_u32 v247, 95232, v244\n"
      "s_bitcmp1_b32 s34, 1\n"
      "s_cselect_b64 s[8:9], s[10:11], s[8:9]\n"
      "s_and_b32 s45, s34, 1\n"
      "s_lshl2_add_u32 s45, s45, s33\n"
      "s_lshl_b32 s45, s45, 9\n"
      "s_lshl_b32 s76, s36, 19\n"
      "s_add_u32 s45, s45, s76\n"
      "s_add_u32 s8, s8, s45\n"
      "s_addc_u32 s9, s9, 0\n"
      "s_lshr_b32 s39, s32, 6\n"
      "s_and_b32 s45, s35, 2\n"
      "s_xor_b32 s39, s39, s45\n"
      "s_lshl_b32 s45, s39, 7\n"
      "s_add_u32 s76, s33, 0\n"
      "s_and_b32 s76, s76, 3\n"
      "s_lshl_b32 s76, s76, 17\n"
      "s_or_b32 s76, s76, s45\n"
      "s_add_u32 s77, s33, 1\n"
      "s_and_b32 s77, s77, 3\n"
      "s_lshl_b32 s77, s77, 17\n"
      "s_or_b32 s77, s77, s45\n"
      "s_add_u32 s78, s33, 2\n"
      "s_and_b32 s78, s78, 3\n"
      "s_lshl_b32 s78, s78, 17\n"
      "s_or_b32 s78, s78, s45\n"
      "s_add_u32 s79, s33, 3\n"
      "s_and_b32 s79, s79, 3\n"
      "s_lshl_b32 s79, s79, 17\n"
      "s_or_b32 s79, s79, s45\n"
      "s_lshl2_add_u32 s43, s34, s35\n"
      "s_and_b32 s44, s40, 31\n"
      "s_mul_i32 s45, s43, 266240\n"
      "s_lshl_b32 s42, s44, 13\n"
      "s_add_u32 s45, s45, s42\n"
      "s_add_u32 s45, s45, 4096\n"
      "s_add_u32 s62, s18, s45\n"
      "s_addc_u32 s63, s19, 0\n"
      "s_bitcmp1_b32 s34, 1\n"
      "s_cselect_b32 s85, 0x80008000, 0\n"
      "s_lshl3_add_u32 s42, s43, s83\n"
      "s_lshl2_add_u32 s42, s42, s33\n"
      "s_lshl_b32 s42, s42, 9\n"
      "s_add_u32 s42, s42, 1048576\n"
      "s_mov_b32 s64, 0xaaaaaaaa\n"
      "s_mov_b32 s65, 0xaaaaaaaa\n"
      "s_mov_b32 s66, 0xcccccccc\n"
      "s_mov_b32 s67, 0xcccccccc\n"
      "s_add_u32 s45, s80, 256\n"
      "s_and_b32 s45, s45, 0x380\n"
      "s_add_u32 s46, s68, s45\n"
      "s_addc_u32 s47, s69, 0\n"
      "s_add_u32 m0, s41, 8192\n"
      "s_nop 0\n"
      "global_load_lds_dwordx4 v240, s[46:47] offset:0\n"
      "global_load_lds_dwordx4 v241, s[46:47] offset:1024\n"
      "global_load_lds_dwordx4 v254, s[46:47] offset:2048\n"
      "global_load_lds_dwordx4 v255, s[46:47] offset:3072\n"
      "s_add_u32 s45, s80, 384\n"
      "s_and_b32 s45, s45, 0x380\n"
      "s_add_u32 s46, s68, s45\n"
      "s_addc_u32 s47, s69, 0\n"
      "s_add_u32 m0, s41, 12288\n"
      "s_nop 0\n"
      "global_load_lds_dwordx4 v240, s[46:47] offset:0\n"
      "global_load_lds_dwordx4 v241, s[46:47] offset:1024\n"
      "global_load_lds_dwordx4 v254, s[46:47] offset:2048\n"
      "global_load_lds_dwordx4 v255, s[46:47] offset:3072\n"
      "s_waitcnt vmcnt(13)\n"
      "s_barrier\n"
      "ds_read_b128 v[0:3], v248 offset:0\n"
      "ds_read_b128 v[4:7], v248 offset:2048\n"
      "ds_read_b128 v[8:11], v249 offset:0\n"
      "ds_read_b128 v[12:15], v249 offset:2048\n"
      "ds_read_b128 a[192:195], v250 offset:0\n"
      "ds_read_b128 a[196:199], v250 offset:2048\n"
      "ds_read_b128 a[200:203], v250 offset:32768\n"
      "ds_read_b128 a[204:207], v251 offset:0\n"
      "ds_read_b128 a[208:211], v251 offset:2048\n"
      "ds_read_b128 a[212:215], v251 offset:32768\n"
      "s_add_u32 s45, s34, 1\n"
      "s_and_b32 s45, s45, 3\n"
      "s_lshl_b32 s45, s45, 15\n"
      "s_or_b32 s45, s45, s81\n"
      "s_xor_b32 s45, s45, 512\n"
      "s_add_u32 s46, s12, s45\n"
      "s_addc_u32 s47, s13, 0\n"
      "s_add_u32 m0, s82, 81920\n"
      "s_nop 0\n"
      "global_load_lds_dwordx4 v240, s[46:47] offset:0\n"
      "global_load_lds_dwordx4 v241, s[46:47] offset:1024\n"
      "global_load_lds_dwordx4 v254, s[46:47] offset:2048\n"
      "global_load_lds_dwordx4 v255, s[46:47] offset:3072\n"
      "s_add_u32 s45, s34, 2\n"
      "s_and_b32 s45, s45, 3\n"
      "s_lshl_b32 s45, s45, 15\n"
      "s_or_b32 s45, s45, s81\n"
      "s_xor_b32 s45, s45, 512\n"
      "s_add_u32 s46, s12, s45\n"
      "s_addc_u32 s47, s13, 0\n"
      "s_add_u32 m0, s82, 114688\n"
      "s_nop 0\n"
      "global_load_lds_dwordx4 v240, s[46:47] offset:0\n"
      "global_load_lds_dwordx4 v241, s[46:47] offset:1024\n"
      "s_waitcnt lgkmcnt(5)\n"
      "v_mfma_f32_16x16x32_bf16 v[192:195], v[0:3], a[192:195], 0\n"
      "s_add_u32 s45, s80, 512\n"
      "s_and_b32 s45, s45, 0x380\n"
      "s_add_u32 s46, s68, s45\n"
      "s_addc_u32 s47, s69, 0\n"
      "s_add_u32 m0, s41, 0\n"
      "s_nop 0\n"
      "global_load_lds_dwordx4 v240, s[46:47] offset:0\n"
      "global_load_lds_dwordx4 v241, s[46:47] offset:1024\n"
      "global_load_lds_dwordx4 v254, s[46:47] offset:2048\n"
      "global_load_lds_dwordx4 v255, s[46:47] offset:3072\n"
      "v_mfma_f32_16x16x32_bf16 v[196:199], v[4:7], a[192:195], 0\n"
      "ds_read_b128 a[216:219], v250 offset:4096\n"
      "ds_read_b128 a[220:223], v250 offset:6144\n"
      "ds_read_b128 a[224:227], v250 offset:36864\n"
      "s_waitcnt lgkmcnt(7)\n"
      "v_mfma_f32_16x16x32_bf16 v[200:203], v[0:3], a[196:199], 0\n"
      "ds_read_b128 a[228:231], v251 offset:4096\n"
      "ds_read_b128 a[232:235], v251 offset:6144\n"
      "ds_read_b128 a[236:239], v251 offset:36864\n"
      "v_mfma_f32_16x16x32_bf16 v[204:207], v[4:7], a[196:199], 0\n"
      "s_waitcnt vmcnt(19)\n"
      "ds_read_b128 v[16:19], v248 offset:4096\n"
      "ds_read_b128 v[20:23], v248 offset:6144\n"
      "s_waitcnt lgkmcnt(11)\n"
      "v_mfma_f32_16x16x32_bf16 v[208:211], v[0:3], a[200:203], 0\n"
      "ds_read_b128 v[24:27], v249 offset:4096\n"
      "ds_read_b128 v[28:31], v249 offset:6144\n"
      "v_mfma_f32_16x16x32_bf16 v[212:215], v[4:7], a[200:203], 0\n"
      "s_waitcnt lgkmcnt(12)\n"
      "v_mfma_f32_16x16x32_bf16 v[192:195], v[8:11], a[204:207], v[192:195]\n"
      "v_mfma_f32_16x16x32_bf16 v[196:199], v[12:15], a[204:207], v[196:199]\n"
      "s_waitcnt lgkmcnt(11)\n"
      "v_mfma_f32_16x16x32_bf16 v[200:203], v[8:11], a[208:211], v[200:203]\n"
      "v_mfma_f32_16x16x32_bf16 v[204:207], v[12:15], a[208:211], v[204:207]\n"
      "s_waitcnt lgkmcnt(10)\n"
      "v_mfma_f32_16x16x32_bf16 v[208:211], v[8:11], a[212:215], v[208:211]\n"
      "v_mfma_f32_16x16x32_bf16 v[212:215], v[12:15], a[212:215], v[212:215]\n"
      "s_waitcnt lgkmcnt(3)\n"
      "v_mfma_f32_16x16x32_bf16 v[192:195], v[16:19], a[216:219], v[192:195]\n"
      "s_waitcnt lgkmcnt(0)\n"
      "s_add_u32 s45, s80, 640\n"
      "s_and_b32 s45, s45, 0x380\n"
      "s_add_u32 s46, s68, s45\n"
      "s_addc_u32 s47, s69, 0\n"
      "s_add_u32 m0, s41, 4096\n"
      "s_nop 0\n"
      "global_load_lds_dwordx4 v240, s[46:47] offset:0\n"
      "global_load_lds_dwordx4 v241, s[46:47] offset:1024\n"
      "global_load_lds_dwordx4 v254, s[46:47] offset:2048\n"
      "global_load_lds_dwordx4 v255, s[46:47] offset:3072\n"
      "v_mfma_f32_16x16x32_bf16 v[196:199], v[20:23], a[216:219], v[196:199]\n"
      "ds_read_b128 a[240:243], v250 offset:8192\n"
      "ds_read_b128 a[244:247], v250 offset:10240\n"
      "ds_read_b128 a[248:251], v250 offset:40960\n"
      "v_mfma_f32_16x16x32_bf16 v[200:203], v[16:19], a[220:223], v[200:203]\n"
      "ds_read_b128 a[252:255], v251 offset:8192\n"
      "ds_read_b128 a[192:195], v251 offset:10240\n"
      "ds_read_b128 a[196:199], v251 offset:40960\n"
      "v_mfma_f32_16x16x32_bf16 v[204:207], v[20:23], a[220:223], v[204:207]\n"
      "s_waitcnt vmcnt(18)\n"
      "ds_read_b128 v[32:35], v248 offset:8192\n"
      "ds_read_b128 v[36:39], v248 offset:10240\n"
      "v_mfma_f32_16x16x32_bf16 v[208:211], v[16:19], a[224:227], v[208:211]\n"
      "ds_read_b128 v[40:43], v249 offset:8192\n"
      "ds_read_b128 v[44:47], v249 offset:10240\n"
      "v_mfma_f32_16x16x32_bf16 v[212:215], v[20:23], a[224:227], v[212:215]\n"
      "v_mfma_f32_16x16x32_bf16 v[192:195], v[24:27], a[228:231], v[192:195]\n"
      "v_mfma_f32_16x16x32_bf16 v[196:199], v[28:31], a[228:231], v[196:199]\n"
      "v_mfma_f32_16x16x32_bf16 v[200:203], v[24:27], a[232:235], v[200:203]\n"
      "v_mfma_f32_16x16x32_bf16 v[204:207], v[28:31], a[232:235], v[204:207]\n"
      "v_mfma_f32_16x16x32_bf16 v[208:211], v[24:27], a[236:239], v[208:211]\n"
      "v_mfma_f32_16x16x32_bf16 v[212:215], v[28:31], a[236:239], v[212:215]\n"
      "s_waitcnt lgkmcnt(3)\n"
      "v_mfma_f32_16x16x32_bf16 v[192:195], v[32:35], a[240:243], v[192:195]\n"
      "s_waitcnt lgkmcnt(0)\n"
      "s_add_u32 s45, s80, 768\n"
      "s_and_b32 s45, s45, 0x380\n"
      "s_add_u32 s46, s68, s45\n"
      "s_addc_u32 s47, s69, 0\n"
      "s_add_u32 m0, s41, 8192\n"
      "s_nop 0\n"
      "global_load_lds_dwordx4 v240, s[46:47] offset:0\n"
      "global_load_lds_dwordx4 v241, s[46:47] offset:1024\n"
      "global_load_lds_dwordx4 v254, s[46:47] offset:2048\n"
      "global_load_lds_dwordx4 v255, s[46:47] offset:3072\n"
      "v_mfma_f32_16x16x32_bf16 v[196:199], v[36:39], a[240:243], v[196:199]\n"
      "v_readfirstlane_b32 s37, v235\n"
      "s_nop 1\n"
      "s_mul_i32 s37, s37, 0x9E3779B1\n"
      "v_mfma_f32_16x16x32_bf16 v[200:203], v[32:35], a[244:247], v[200:203]\n"
      "s_add_u32 s37, s37, 0x7F4A7C15\n"
      "s_mov_b32 s38, 0x8000\n"
      "ds_read_b128 a[200:203], v250 offset:12288\n"
      "ds_read_b128 a[204:207], v250 offset:14336\n"
      "v_mfma_f32_16x16x32_bf16 v[204:207], v[36:39], a[244:247], v[204:207]\n"
      "ds_read_b128 a[208:211], v250 offset:45056\n"
      "ds_read_b128 a[212:215], v251 offset:12288\n"
      "ds_read_b128 a[216:219], v251 offset:14336\n"
      "v_mfma_f32_16x16x32_bf16 v[208:211], v[32:35], a[248:251], v[208:211]\n"
      "ds_read_b128 a[220:223], v251 offset:45056\n"
      "s_waitcnt vmcnt(18)\n"
      "ds_read_b128 v[48:51], v248 offset:12288\n"
      "v_mfma_f32_16x16x32_bf16 v[212:215], v[36:39], a[248:251], v[212:215]\n"
      "ds_read_b128 v[52:55], v248 offset:14336\n"
      "ds_read_b128 v[56:59], v249 offset:12288\n"
      "ds_read_b128 v[60:63], v249 offset:14336\n"
      "v_mfma_f32_16x16x32_bf16 v[192:195], v[40:43], a[252:255], v[192:195]\n"
      "v_mfma_f32_16x16x32_bf16 v[196:199], v[44:47], a[252:255], v[196:199]\n"
      "v_mfma_f32_16x16x32_bf16 v[200:203], v[40:43], a[192:195], v[200:203]\n"
      "v_mfma_f32_16x16x32_bf16 v[204:207], v[44:47], a[192:195], v[204:207]\n"
      "v_mfma_f32_16x16x32_bf16 v[208:211], v[40:43], a[196:199], v[208:211]\n"
      "v_mfma_f32_16x16x32_bf16 v[212:215], v[44:47], a[196:199], v[212:215]\n"
      "s_waitcnt lgkmcnt(3)\n"
      "v_mfma_f32_16x16x32_bf16 v[192:195], v[48:51], a[200:203], v[192:195]\n"
      "s_waitcnt lgkmcnt(0)\n"
      "s_add_u32 s45, s80, 896\n"
      "s_and_b32 s45, s45, 0x380\n"
      "s_add_u32 s46, s68, s45\n"
      "s_addc_u32 s47, s69, 0\n"
      "s_add_u32 m0, s41, 12288\n"
      "s_nop 0\n"
      "global_load_lds_dwordx4 v240, s[46:47] offset:0\n"
      "global_load_lds_dwordx4 v241, s[46:47] offset:1024\n"
      "global_load_lds_dwordx4 v254, s[46:47] offset:2048\n"
      "global_load_lds_dwordx4 v255, s[46:47] offset:3072\n"
      "v_mfma_f32_16x16x32_bf16 v[196:199], v[52:55], a[200:203], v[196:199]\n"
      "s_waitcnt vmcnt(16)\n"
      "s_barrier\n"
      "ds_read_b128 a[224:227], v250 offset:16384\n"
      "v_mfma_f32_16x16x32_bf16 v[200:203], v[48:51], a[204:207], v[200:203]\n"
      "ds_read_b128 a[228:231], v250 offset:18432\n"
      "ds_read_b128 a[232:235], v250 offset:49152\n"
      "ds_read_b128 a[236:239], v251 offset:16384\n"
      "v_mfma_f32_16x16x32_bf16 v[204:207], v[52:55], a[204:207], v[204:207]\n"
      "ds_read_b128 a[240:243], v251 offset:18432\n"
      "ds_read_b128 a[244:247], v251 offset:49152\n"
      "s_waitcnt vmcnt(12)\n"
      "ds_read_b128 v[64:67], v248 offset:0\n"
      "v_mfma_f32_16x16x32_bf16 v[208:211], v[48:51], a[208:211], v[208:211]\n"
      "ds_read_b128 v[68:71], v248 offset:2048\n"
      "ds_read_b128 v[72:75], v249 offset:0\n"
      "ds_read_b128 v[76:79], v249 offset:2048\n"
      "v_mfma_f32_16x16x32_bf16 v[212:215], v[52:55], a[208:211], v[212:215]\n"
      "v_mfma_f32_16x16x32_bf16 v[192:195], v[56:59], a[212:215], v[192:195]\n"
      "v_mfma_f32_16x16x32_bf16 v[196:199], v[60:63], a[212:215], v[196:199]\n"
      "v_mfma_f32_16x16x32_bf16 v[200:203], v[56:59], a[216:219], v[200:203]\n"
      "v_mfma_f32_16x16x32_bf16 v[204:207], v[60:63], a[216:219], v[204:207]\n"
      "v_mfma_f32_16x16x32_bf16 v[208:211], v[56:59], a[220:223], v[208:211]\n"
      "v_mfma_f32_16x16x32_bf16 v[212:215], v[60:63], a[220:223], v[212:215]\n"
      "s_waitcnt lgkmcnt(3)\n"
      "v_mfma_f32_16x16x32_bf16 v[192:195], v[64:67], a[224:227], v[192:195]\n"
      "s_waitcnt lgkmcnt(0)\n"
      "s_add_u32 m0, s41, 0\n"
      "s_nop 0\n"
      "global_load_lds_dwordx4 v238, s[24:25]\n"
      "s_add_u32 m0, s41, 1024\n"
      "s_nop 0\n"
      "global_load_lds_dwordx4 v238, s[22:23]\n"
      "v_mfma_f32_16x16x32_bf16 v[196:199], v[68:71], a[224:227], v[196:199]\n"
      "s_add_u32 s45, s34, 2\n"
      "s_and_b32 s45, s45, 3\n"
      "s_lshl_b32 s45, s45, 15\n"
      "s_or_b32 s45, s45, s81\n"
      "s_add_u32 s46, s12, s45\n"
      "s_addc_u32 s47, s13, 0\n"
      "s_add_u32 m0, s82, 98304\n"
      "s_nop 0\n"
      "global_load_lds_dwordx4 v254, s[46:47] offset:2048\n"
      "global_load_lds_dwordx4 v255, s[46:47] offset:3072\n"
      "v_mfma_f32_16x16x32_bf16 v[200:203], v[64:67], a[228:231], v[200:203]\n"
      "ds_read_b128 a[248:251], v250 offset:20480\n"
      "ds_read_b128 a[252:255], v250 offset:22528\n"
      "ds_read_b128 a[192:195], v250 offset:53248\n"
      "v_mfma_f32_16x16x32_bf16 v[204:207], v[68:71], a[228:231], v[204:207]\n"
      "ds_read_b128 a[196:199], v251 offset:20480\n"
      "ds_read_b128 a[200:203], v251 offset:22528\n"
      "ds_read_b128 a[204:207], v251 offset:53248\n"
      "v_mfma_f32_16x16x32_bf16 v[208:211], v[64:67], a[232:235], v[208:211]\n"
      "s_waitcnt vmcnt(12)\n"
      "ds_read_b128 v[80:83], v248 offset:4096\n"
      "ds_read_b128 v[84:87], v248 offset:6144\n"
      "v_mfma_f32_16x16x32_bf16 v[212:215], v[68:71], a[232:235], v[212:215]\n"
      "ds_read_b128 v[88:91], v249 offset:4096\n"
      "ds_read_b128 v[92:95], v249 offset:6144\n"
      "v_mfma_f32_16x16x32_bf16 v[192:195], v[72:75], a[236:239], v[192:195]\n"
      "v_mfma_f32_16x16x32_bf16 v[196:199], v[76:79], a[236:239], v[196:199]\n"
      "v_mfma_f32_16x16x32_bf16 v[200:203], v[72:75], a[240:243], v[200:203]\n"
      "v_mfma_f32_16x16x32_bf16 v[204:207], v[76:79], a[240:243], v[204:207]\n"
      "v_mfma_f32_16x16x32_bf16 v[208:211], v[72:75], a[244:247], v[208:211]\n"
      "v_mfma_f32_16x16x32_bf16 v[212:215], v[76:79], a[244:247], v[212:215]\n"
      "s_waitcnt lgkmcnt(3)\n"
      "v_mfma_f32_16x16x32_bf16 v[192:195], v[80:83], a[248:251], v[192:195]\n"
      "s_add_u32 s45, s34, 2\n"
      "s_and_b32 s45, s45, 3\n"
      "s_lshl_b32 s45, s45, 15\n"
      "s_or_b32 s45, s45, s81\n"
      "s_xor_b32 s45, s45, 512\n"
      "s_add_u32 s46, s12, s45\n"
      "s_addc_u32 s47, s13, 0\n"
      "s_add_u32 m0, s82, 114688\n"
      "s_nop 0\n"
      "global_load_lds_dwordx4 v254, s[46:47] offset:2048\n"
      "global_load_lds_dwordx4 v255, s[46:47] offset:3072\n"
      "s_waitcnt lgkmcnt(2)\n"
      "v_mfma_f32_16x16x32_bf16 v[196:199], v[84:87], a[248:251], v[196:199]\n"
      "s_add_u32 s45, s34, 3\n"
      "s_and_b32 s45, s45, 3\n"
      "s_lshl_b32 s45, s45, 15\n"
      "s_or_b32 s45, s45, s81\n"
      "s_add_u32 s46, s12, s45\n"
      "s_addc_u32 s47, s13, 0\n"
      "s_add_u32 m0, s82, 131072\n"
      "s_nop 0\n"
      "global_load_lds_dwordx4 v240, s[46:47] offset:0\n"
      "global_load_lds_dwordx4 v241, s[46:47] offset:1024\n"
      "global_load_lds_dwordx4 v254, s[46:47] offset:2048\n"
      "global_load_lds_dwordx4 v255, s[46:47] offset:3072\n"
      "v_mfma_f32_16x16x32_bf16 v[200:203], v[80:83], a[252:255], v[200:203]\n"
      "ds_read_b128 a[208:211], v250 offset:24576\n"
      "ds_read_b128 a[212:215], v250 offset:26624\n"
      "ds_read_b128 a[216:219], v250 offset:57344\n"
      "v_mfma_f32_16x16x32_bf16 v[204:207], v[84:87], a[252:255], v[204:207]\n"
      "ds_read_b128 a[220:223], v251 offset:24576\n"
      "ds_read_b128 a[224:227], v251 offset:26624\n"
      "ds_read_b128 a[228:231], v251 offset:57344\n"
      "v_mfma_f32_16x16x32_bf16 v[208:211], v[80:83], a[192:195], v[208:211]\n"
      "s_waitcnt vmcnt(14)\n"
      "ds_read_b128 v[96:99], v248 offset:8192\n"
      "ds_read_b128 v[100:103], v248 offset:10240\n"
      "v_mfma_f32_16x16x32_bf16 v[212:215], v[84:87], a[192:195], v[212:215]\n"
      "ds_read_b128 v[104:107], v249 offset:8192\n"
      "ds_read_b128 v[108:111], v249 offset:10240\n"
      "s_waitcnt lgkmcnt(11)\n"
      "v_mfma_f32_16x16x32_bf16 v[192:195], v[88:91], a[196:199], v[192:195]\n"
      "s_waitcnt lgkmcnt(10)\n"
      "v_mfma_f32_16x16x32_bf16 v[196:199], v[92:95], a[196:199], v[196:199]\n"
      "v_mfma_f32_16x16x32_bf16 v[200:203], v[88:91], a[200:203], v[200:203]\n"
      "v_mfma_f32_16x16x32_bf16 v[204:207], v[92:95], a[200:203], v[204:207]\n"
      "v_mfma_f32_16x16x32_bf16 v[208:211], v[88:91], a[204:207], v[208:211]\n"
      "v_mfma_f32_16x16x32_bf16 v[212:215], v[92:95], a[204:207], v[212:215]\n"
      "s_waitcnt lgkmcnt(3)\n"
      "v_mfma_f32_16x16x32_bf16 v[192:195], v[96:99], a[208:211], v[192:195]\n"
      "s_add_u32 s45, s34, 3\n"
      "s_and_b32 s45, s45, 3\n"
      "s_lshl_b32 s45, s45, 15\n"
      "s_or_b32 s45, s45, s81\n"
      "s_xor_b32 s45, s45, 512\n"
      "s_add_u32 s46, s12, s45\n"
      "s_addc_u32 s47, s13, 0\n"
      "s_add_u32 m0, s82, 147456\n"
      "s_nop 0\n"
      "global_load_lds_dwordx4 v240, s[46:47] offset:0\n"
      "global_load_lds_dwordx4 v241, s[46:47] offset:1024\n"
      "global_load_lds_dwordx4 v254, s[46:47] offset:2048\n"
      "global_load_lds_dwordx4 v255, s[46:47] offset:3072\n"
      "s_waitcnt lgkmcnt(2)\n"
      "v_mfma_f32_16x16x32_bf16 v[196:199], v[100:103], a[208:211], v[196:199]\n"
      "ds_read_b128 a[232:235], v250 offset:28672\n"
      "ds_read_b128 a[236:239], v250 offset:30720\n"
      "ds_read_b128 a[240:243], v250 offset:61440\n"
      "v_mfma_f32_16x16x32_bf16 v[200:203], v[96:99], a[212:215], v[200:203]\n"
      "ds_read_b128 a[244:247], v251 offset:28672\n"
      "ds_read_b128 a[248:251], v251 offset:30720\n"
      "ds_read_b128 a[252:255], v251 offset:61440\n"
      "v_mfma_f32_16x16x32_bf16 v[204:207], v[100:103], a[212:215], v[204:207]\n"
      "s_waitcnt vmcnt(14)\n"
      "ds_read_b128 v[112:115], v248 offset:12288\n"
      "ds_read_b128 v[116:119], v248 offset:14336\n"
      "v_mfma_f32_16x16x32_bf16 v[208:211], v[96:99], a[216:219], v[208:211]\n"
      "ds_read_b128 v[120:123], v249 offset:12288\n"
      "ds_read_b128 v[124:127], v249 offset:14336\n"
      "v_mfma_f32_16x16x32_bf16 v[212:215], v[100:103], a[216:219], v[212:215]\n"
      "s_waitcnt lgkmcnt(11)\n"
      "v_mfma_f32_16x16x32_bf16 v[192:195], v[104:107], a[220:223], v[192:195]\n"
      "s_waitcnt lgkmcnt(10)\n"
      "v_mfma_f32_16x16x32_bf16 v[196:199], v[108:111], a[220:223], v[196:199]\n"
      "v_mfma_f32_16x16x32_bf16 v[200:203], v[104:107], a[224:227], v[200:203]\n"
      "v_mfma_f32_16x16x32_bf16 v[204:207], v[108:111], a[224:227], v[204:207]\n"
      "v_mfma_f32_16x16x32_bf16 v[208:211], v[104:107], a[228:231], v[208:211]\n"
      "v_mfma_f32_16x16x32_bf16 v[212:215], v[108:111], a[228:231], v[212:215]\n"
      "s_waitcnt lgkmcnt(3)\n"
      "v_mfma_f32_16x16x32_bf16 v[192:195], v[112:115], a[232:235], v[192:195]\n"
      "s_waitcnt vmcnt(12)\n"
      "ds_read_b128 v[136:139], v252 offset:0\n"
      "s_waitcnt lgkmcnt(3)\n"
      "v_mfma_f32_16x16x32_bf16 v[196:199], v[116:119], a[232:235], v[196:199]\n"
      "ds_read_b128 v[140:143], v252 offset:64\n"
      "s_waitcnt lgkmcnt(3)\n"
      "v_mfma_f32_16x16x32_bf16 v[192:195], v[120:123], a[244:247], v[192:195]\n"
      "ds_read_b128 v[144:147], v252 offset:128\n"
      "s_waitcnt lgkmcnt(3)\n"
      "v_mfma_f32_16x16x32_bf16 v[196:199], v[124:127], a[244:247], v[196:199]\n"
      "v_mfma_f32_16x16x32_bf16 v[200:203], v[112:115], a[236:239], v[200:203]\n"
      "ds_read_b128 v[148:151], v252 offset:192\n"
      "v_mfma_f32_16x16x32_bf16 v[204:207], v[116:119], a[236:239], v[204:207]\n"
      "v_mfma_f32_16x16x32_bf16 v[200:203], v[120:123], a[248:251], v[200:203]\n"
      "ds_read_b128 v[152:155], v252 offset:256\n"
      "v_cvt_pk_bf16_f32 v224, v192, v193\n"
      "v_cvt_pk_bf16_f32 v225, v194, v195\n"
      "v_mfma_f32_16x16x32_bf16 v[204:207], v[124:127], a[248:251], v[204:207]\n"
      "v_cvt_pk_bf16_f32 v226, v196, v197\n"
      "v_cvt_pk_bf16_f32 v227, v198, v199\n"
      "v_xor_b32 v224, s85, v224\n"
      "v_mfma_f32_16x16x32_bf16 v[208:211], v[112:115], a[240:243], v[208:211]\n"
      "ds_read_b128 v[156:159], v252 offset:320\n"
      "v_xor_b32 v225, s85, v225\n"
      "v_xor_b32 v226, s85, v226\n"
      "v_mfma_f32_16x16x32_bf16 v[212:215], v[116:119], a[240:243], v[212:215]\n"
      "v_xor_b32 v227, s85, v227\n"
      "v_mfma_f32_16x16x32_bf16 v[208:211], v[120:123], a[252:255], v[208:211]\n"
      "ds_read_b128 v[160:163], v252 offset:384\n"
      "global_store_dwordx4 v242, v[224:227], s[62:63] offset:-4096 sc1\n"
      "v_cvt_pk_bf16_f32 v228, v200, v201\n"
      "v_mfma_f32_16x16x32_bf16 v[212:215], v[124:127], a[252:255], v[212:215]\n"
      "v_cvt_pk_bf16_f32 v229, v202, v203\n"
      "v_cvt_pk_bf16_f32 v230, v204, v205\n"
      "v_cvt_pk_bf16_f32 v231, v206, v207\n"
      "s_waitcnt lgkmcnt(6)\n"
      "v_mfma_f32_16x16x32_bf16 v[128:131], v[0:3], v[136:139], 0\n"
      "ds_read_b128 v[164:167], v252 offset:448\n"
      "v_xor_b32 v228, s85, v228\n"
      "v_xor_b32 v229, s85, v229\n"
      "v_mfma_f32_16x16x32_bf16 v[132:135], v[4:7], v[136:139], 0\n"
      "v_xor_b32 v230, s85, v230\n"
      "v_xor_b32 v231, s85, v231\n"
      "s_waitcnt lgkmcnt(6)\n"
      "v_mfma_f32_16x16x32_bf16 v[128:131], v[8:11], v[140:143], v[128:131]\n"
      "ds_read_b128 v[136:139], v252 offset:512\n"
      "global_store_dwordx4 v242, v[228:231], s[62:63] offset:-3072 sc1\n"
      "v_cvt_pk_bf16_f32 v232, v208, v209\n"
      "v_mfma_f32_16x16x32_bf16 v[132:135], v[12:15], v[140:143], v[132:135]\n"
      "v_cvt_pk_bf16_f32 v233, v210, v211\n"
      "v_cvt_pk_bf16_f32 v234, v212, v213\n"
      "v_cvt_pk_bf16_f32 v235, v214, v215\n"
      "s_waitcnt lgkmcnt(6)\n"
      "v_mfma_f32_16x16x32_bf16 v[128:131], v[16:19], v[144:147], v[128:131]\n"
      "ds_read_b128 v[140:143], v252 offset:576\n"
      "v_xor_b32 v232, s85, v232\n"
      "v_xor_b32 v233, s85, v233\n"
      "v_mfma_f32_16x16x32_bf16 v[132:135], v[20:23], v[144:147], v[132:135]\n"
      "v_xor_b32 v234, s85, v234\n"
      "v_xor_b32 v235, s85, v235\n"
      "s_waitcnt lgkmcnt(6)\n"
      "v_mfma_f32_16x16x32_bf16 v[128:131], v[24:27], v[148:151], v[128:131]\n"
      "ds_read_b128 v[144:147], v252 offset:640\n"
      "global_store_dwordx4 v242, v[232:235], s[62:63] offset:-2048 sc1\n"
      "v_mfma_f32_16x16x32_bf16 v[132:135], v[28:31], v[148:151], v[132:135]\n"
      "s_xor_b32 s70, s76, 0\n"
      "s_add_u32 s70, s8, s70\n"
      "s_addc_u32 s71, s9, 0\n"
      "s_add_u32 m0, s41, 4096\n"
      "s_waitcnt lgkmcnt(6)\n"
      "v_mfma_f32_16x16x32_bf16 v[128:131], v[32:35], v[152:155], v[128:131]\n"
      "s_add_u32 s48, s82, 32768\n"
      "ds_read_b128 v[148:151], v252 offset:704\n"
      "global_load_lds_dwordx4 v244, s[70:71] offset:0\n"
      "v_subrev_u32 v236, s41, v248\n"
      "v_mfma_f32_16x16x32_bf16 v[132:135], v[36:39], v[152:155], v[132:135]\n"
      "s_add_u32 s48, s48, 65536\n"
      "s_waitcnt vmcnt(4)\n"
      "s_barrier\n"
      "s_waitcnt lgkmcnt(6)\n"
      "v_mfma_f32_16x16x32_bf16 v[128:131], v[40:43], v[156:159], v[128:131]\n"
      "ds_read_b128 v[152:155], v252 offset:768\n"
      "ds_read_b128 a[192:195], v250 offset:34816\n"
      "v_add_u32 v236, s48, v236\n"
      "v_mfma_f32_16x16x32_bf16 v[132:135], v[44:47], v[156:159], v[132:135]\n"
      "ds_read_b128 a[196:199], v251 offset:34816\n"
      "ds_read_b128 a[200:203], v250 offset:38912\n"
      "v_xor_b32 v237, 64, v236\n"
      "s_waitcnt lgkmcnt(9)\n"
      "v_mfma_f32_16x16x32_bf16 v[128:131], v[48:51], v[160:163], v[128:131]\n"
      "ds_read_b128 v[156:159], v252 offset:832\n"
      "global_load_lds_dwordx4 v245, s[70:71] offset:1024\n"
      "v_mfma_f32_16x16x32_bf16 v[132:135], v[52:55], v[160:163], v[132:135]\n"
      "ds_read_b128 a[204:207], v251 offset:38912\n"
      "ds_read_b128 a[208:211], v250 offset:43008\n"
      "s_waitcnt lgkmcnt(11)\n"
      "v_mfma_f32_16x16x32_bf16 v[128:131], v[56:59], v[164:167], v[128:131]\n"
      "ds_read_b128 v[160:163], v252 offset:896\n"
      "ds_read_b128 a[212:215], v251 offset:43008\n"
      "v_mfma_f32_16x16x32_bf16 v[132:135], v[60:63], v[164:167], v[132:135]\n"
      "ds_read_b128 a[216:219], v250 offset:47104\n"
      "s_waitcnt lgkmcnt(13)\n"
      "v_mfma_f32_16x16x32_bf16 v[128:131], v[64:67], v[136:139], v[128:131]\n"
      "ds_read_b128 v[164:167], v252 offset:960\n"
      "global_load_lds_dwordx4 v246, s[70:71] offset:2048\n"
      "v_mfma_f32_16x16x32_bf16 v[132:135], v[68:71], v[136:139], v[132:135]\n"
      "ds_read_b128 a[220:223], v251 offset:47104\n"
      "s_waitcnt lgkmcnt(14)\n"
      "v_mfma_f32_16x16x32_bf16 v[128:131], v[72:75], v[140:143], v[128:131]\n"
      "v_mfma_f32_16x16x32_bf16 v[132:135], v[76:79], v[140:143], v[132:135]\n"
      "ds_read_b128 a[224:227], v250 offset:51200\n"
      "s_waitcnt lgkmcnt(14)\n"
      "v_mfma_f32_16x16x32_bf16 v[128:131], v[80:83], v[144:147], v[128:131]\n"
      "global_load_lds_dwordx4 v247, s[70:71] offset:3072\n"
      "v_mfma_f32_16x16x32_bf16 v[132:135], v[84:87], v[144:147], v[132:135]\n"
      "ds_read_b128 a[228:231], v251 offset:51200\n"
      "s_waitcnt lgkmcnt(14)\n"
      "v_mfma_f32_16x16x32_bf16 v[128:131], v[88:91], v[148:151], v[128:131]\n"
      "v_mfma_f32_16x16x32_bf16 v[132:135], v[92:95], v[148:151], v[132:135]\n"
      "s_lshl_b32 s70, s34, 15\n"
      "s_or_b32 s70, s70, s81\n"
      "s_add_u32 s70, s12, s70\n"
      "s_addc_u32 s71, s13, 0\n"
      "s_add_u32 m0, s82, 65536\n"
      "s_waitcnt lgkmcnt(13)\n"
      "v_mfma_f32_16x16x32_bf16 v[128:131], v[96:99], v[152:155], v[128:131]\n"
      "global_load_lds_dwordx4 v240, s[70:71] offset:0\n"
      "ds_read_b128 a[232:235], v250 offset:55296\n"
      "v_mfma_f32_16x16x32_bf16 v[132:135], v[100:103], v[152:155], v[132:135]\n"
      "ds_read_b128 a[236:239], v251 offset:55296\n"
      "s_waitcnt lgkmcnt(11)\n"
      "v_mfma_f32_16x16x32_bf16 v[128:131], v[104:107], v[156:159], v[128:131]\n"
      "v_mfma_f32_16x16x32_bf16 v[132:135], v[108:111], v[156:159], v[132:135]\n"
      "ds_read_b128 a[240:243], v250 offset:59392\n"
      "s_waitcnt lgkmcnt(9)\n"
      "v_mfma_f32_16x16x32_bf16 v[128:131], v[112:115], v[160:163], v[128:131]\n"
      "global_load_lds_dwordx4 v241, s[70:71] offset:1024\n"
      "v_mfma_f32_16x16x32_bf16 v[132:135], v[116:119], v[160:163], v[132:135]\n"
      "ds_read_b128 a[244:247], v251 offset:59392\n"
      "s_waitcnt lgkmcnt(7)\n"
      "v_mfma_f32_16x16x32_bf16 v[128:131], v[120:123], v[164:167], v[128:131]\n"
      "v_mfma_f32_16x16x32_bf16 v[132:135], v[124:127], v[164:167], v[132:135]\n"
      "ds_read_b128 a[248:251], v250 offset:63488\n"
      "v_mfma_f32_16x16x32_bf16 v[216:219], v[0:3], a[192:195], 0\n"
      "global_load_lds_dwordx4 v254, s[70:71] offset:2048\n"
      "v_mfma_f32_16x16x32_bf16 v[220:223], v[4:7], a[192:195], 0\n"
      "ds_read_b128 a[252:255], v251 offset:63488\n"
      "v_xor_b32 v250, 196608, v250\n"
      "v_xor_b32 v251, 196608, v251\n"
      "v_mfma_f32_16x16x32_bf16 v[216:219], v[8:11], a[196:199], v[216:219]\n"
      "s_mov_b32 s22, 0xf0f0f0f0\n"
      "s_mov_b32 s23, 0xf0f0f0f0\n"
      "v_cndmask_b32 v128, v128, v129, s[64:65]\n"
      "v_cndmask_b32 v130, v130, v131, s[64:65]\n"
      "v_mfma_f32_16x16x32_bf16 v[220:223], v[12:15], a[196:199], v[220:223]\n"
      "ds_read_b128 a[192:195], v250 offset:0\n"
      "v_cndmask_b32 v132, v132, v133, s[64:65]\n"
      "v_cndmask_b32 v134, v134, v135, s[64:65]\n"
      "v_mfma_f32_16x16x32_bf16 v[216:219], v[16:19], a[200:203], v[216:219]\n"
      "global_load_lds_dwordx4 v255, s[70:71] offset:3072\n"
      "v_cndmask_b32 v128, v128, v130, s[66:67]\n"
      "v_cndmask_b32 v132, v132, v134, s[66:67]\n"
      "v_mfma_f32_16x16x32_bf16 v[220:223], v[20:23], a[200:203], v[220:223]\n"
      "ds_read_b128 a[196:199], v250 offset:2048\n"
      "v_lshrrev_b32 v133, 4, v243\n"
      "v_cndmask_b32 v128, v128, v132, s[22:23]\n"
      "v_mfma_f32_16x16x32_bf16 v[216:219], v[24:27], a[204:207], v[216:219]\n"
      "v_and_b32 v134, 7, v243\n"
      "v_cvt_pk_bf16_f32 v128, v128, v128\n"
      "s_lshl_b32 s48, s40, 5\n"
      "v_mfma_f32_16x16x32_bf16 v[220:223], v[28:31], a[204:207], v[220:223]\n"
      "ds_read_b128 a[200:203], v251 offset:0\n"
      "v_and_b32 v128, 0xffff0000, v128\n"
      "v_lshl_or_b32 v133, v133, 3, v134\n"
      "v_mfma_f32_16x16x32_bf16 v[216:219], v[32:35], a[208:211], v[216:219]\n"
      "s_lshl_b32 s70, s34, 15\n"
      "s_or_b32 s70, s70, s81\n"
      "s_xor_b32 s70, s70, 512\n"
      "s_add_u32 s70, s12, s70\n"
      "s_addc_u32 s71, s13, 0\n"
      "s_add_u32 m0, s82, 81920\n"
      "v_mfma_f32_16x16x32_bf16 v[220:223], v[36:39], a[208:211], v[220:223]\n"
      "global_load_lds_dwordx4 v240, s[70:71] offset:0\n"
      "ds_read_b128 a[204:207], v251 offset:2048\n"
      "v_add_u32 v133, s48, v133\n"
      "v_mfma_f32_16x16x32_bf16 v[216:219], v[40:43], a[212:215], v[216:219]\n"
      "v_ashrrev_i32 v129, 31, v128\n"
      "v_mul_f32 v130, 0xbfb8aa3b, v128\n"
      "v_or_b32 v129, 0x80000000, v129\n"
      "v_mfma_f32_16x16x32_bf16 v[220:223], v[44:47], a[212:215], v[220:223]\n"
      "ds_read_b128 a[208:211], v250 offset:4096\n"
      "v_exp_f32 v130, v130\n"
      "v_xor_b32 v129, v129, v128\n"
      "v_mfma_f32_16x16x32_bf16 v[216:219], v[48:51], a[216:219], v[216:219]\n"
      "v_sub_u32 v132, 0xfff, v133\n"
      "v_add_f32 v130, 1.0, v130\n"
      "s_mov_b32 s49, 0xffff0000\n"
      "v_mfma_f32_16x16x32_bf16 v[220:223], v[52:55], a[216:219], v[220:223]\n"
      "global_load_lds_dwordx4 v241, s[70:71] offset:1024\n"
      "ds_read_b128 a[212:215], v250 offset:6144\n"
      "v_and_or_b32 v129, v129, s49, v132\n"
      "s_waitcnt lgkmcnt(14)\n"
      "v_mfma_f32_16x16x32_bf16 v[216:219], v[56:59], a[220:223], v[216:219]\n"
      "v_rcp_f32 v130, v130\n"
      "s_mov_b32 s64, 0x00ff00ff\n"
      "s_mov_b32 s65, 0x00ff00ff\n"
      "v_mfma_f32_16x16x32_bf16 v[220:223], v[60:63], a[220:223], v[220:223]\n"
      "ds_read_b128 a[216:219], v251 offset:4096\n"
      "v_cndmask_b32 v129, v129, 0, s[64:65]\n"
      "v_cvt_pk_bf16_f32 v130, v130, v130\n"
      "s_waitcnt lgkmcnt(14)\n"
      "v_mfma_f32_16x16x32_bf16 v[216:219], v[64:67], a[224:227], v[216:219]\n"
      "s_lshr_b32 s48, s34, 1\n"
      "v_add_u32 v238, s42, v242\n"
      "v_mov_b32 v224, s37\n"
      "v_mov_b32 v225, s37\n"
      "v_mov_b32 v226, s37\n"
      "v_mov_b32 v227, s37\n"
      "s_waitcnt vmcnt(10)\n"
      "s_mov_b64 exec, 0xff\n"
      "global_store_dwordx4 v238, v[224:227], s[58:59] offset:0 sc1\n"
      "s_mov_b64 exec, -1\n"
      "v_mfma_f32_16x16x32_bf16 v[220:223], v[68:71], a[224:227], v[220:223]\n"
      "s_lshl2_add_u32 s48, s48, s35\n"
      "ds_read_b128 a[220:223], v251 offset:6144\n"
      "v_max_u32_dpp v129, v129, v129 quad_perm:[1,0,3,2] row_mask:0xf bank_mask:0xf\n"
      "v_and_b32 v130, 0xffff0000, v130\n"
      "s_waitcnt lgkmcnt(14)\n"
      "v_mfma_f32_16x16x32_bf16 v[216:219], v[72:75], a[228:231], v[216:219]\n"
      "s_lshl_b32 s49, s48, 15\n"
      "global_load_lds_dwordx4 v254, s[70:71] offset:2048\n"
      "v_max_u32_dpp v129, v129, v129 quad_perm:[2,3,0,1] row_mask:0xf bank_mask:0xf\n"
      "s_add_u32 s49, s49, 786432\n"
      "v_mfma_f32_16x16x32_bf16 v[220:223], v[76:79], a[228:231], v[220:223]\n"
      "ds_read_b128 a[224:227], v250 offset:8192\n"
      "v_lshl_add_u32 v133, v133, 4, s49\n"
      "v_max_u32_dpp v129, v129, v129 row_half_mirror row_mask:0xf bank_mask:0xf\n"
      "s_waitcnt lgkmcnt(14)\n"
      "v_mfma_f32_16x16x32_bf16 v[216:219], v[80:83], a[232:235], v[216:219]\n"
      "s_lshl_b32 s48, s48, 6\n"
      "s_add_u32 s48, s48, s40\n"
      "v_max_u32_dpp v129, v129, v129 row_mirror row_mask:0xf bank_mask:0xf\n"
      "s_lshl_b32 s48, s48, 6\n"
      "v_mfma_f32_16x16x32_bf16 v[220:223], v[84:87], a[232:235], v[220:223]\n"
      "s_add_u32 s48, s48, 4096\n"
      "ds_read_b128 a[228:231], v250 offset:10240\n"
      "v_mov_b32 v131, v129\n"
      "v_lshl_add_u32 v132, v134, 4, s48\n"
      "s_waitcnt lgkmcnt(14)\n"
      "v_mfma_f32_16x16x32_bf16 v[216:219], v[88:91], a[236:239], v[216:219]\n"
      "global_load_lds_dwordx4 v255, s[70:71] offset:3072\n"
      "v_mov_b32 v137, s37\n"
      "v_permlane16_swap_b32 v129, v131\n"
      "v_mfma_f32_16x16x32_bf16 v[220:223], v[92:95], a[236:239], v[220:223]\n"
      "ds_read_b128 a[232:235], v251 offset:8192\n"
      "v_mov_b32 v138, s37\n"
      "v_mov_b32 v139, s37\n"
      "s_waitcnt lgkmcnt(14)\n"
      "v_mfma_f32_16x16x32_bf16 v[216:219], v[96:99], a[240:243], v[216:219]\n"
      "v_max_u32 v129, v129, v131\n"
      "v_cndmask_b32 v133, v132, v133, s[64:65]\n"
      "v_mov_b32 v131, v129\n"
      "v_mfma_f32_16x16x32_bf16 v[220:223], v[100:103], a[240:243], v[220:223]\n"
      "ds_read_b128 a[236:239], v251 offset:10240\n"
      "v_permlane32_swap_b32 v129, v131\n"
      "v_max_u32 v129, v129, v131\n"
      "s_waitcnt lgkmcnt(14)\n"
      "v_mfma_f32_16x16x32_bf16 v[216:219], v[104:107], a[244:247], v[216:219]\n"
      "s_xor_b32 s70, s77, 0\n"
      "s_add_u32 s70, s8, s70\n"
      "s_addc_u32 s71, s9, 0\n"
      "s_add_u32 m0, s41, 8192\n"
      "v_mfma_f32_16x16x32_bf16 v[220:223], v[108:111], a[244:247], v[220:223]\n"
      "global_load_lds_dwordx4 v244, s[70:71] offset:0\n"
      "ds_read_b128 a[240:243], v250 offset:12288\n"
      "v_cndmask_b32 v136, v129, v130, s[64:65]\n"
      "s_waitcnt lgkmcnt(14)\n"
      "v_mfma_f32_16x16x32_bf16 v[216:219], v[112:115], a[248:251], v[216:219]\n"
      "s_mov_b32 s64, 0x00ff0fff\n"
      "s_mov_b64 exec, s[64:65]\n"
      "global_store_dwordx4 v133, v[136:139], s[58:59] sc1\n"
      "s_mov_b64 exec, -1\n"
      "v_mfma_f32_16x16x32_bf16 v[220:223], v[116:119], a[248:251], v[220:223]\n"
      "ds_read_b128 a[244:247], v250 offset:14336\n"
      "s_waitcnt vmcnt(11)\n"
      "ds_read_b128 a[0:3], v248 offset:4096\n"
      "s_waitcnt lgkmcnt(15)\n"
      "v_mfma_f32_16x16x32_bf16 v[216:219], v[120:123], a[252:255], v[216:219]\n"
      "ds_read_b128 a[4:7], v248 offset:6144\n"
      "ds_read_b128 a[32:35], v249 offset:4096\n"
      "v_mfma_f32_16x16x32_bf16 v[220:223], v[124:127], a[252:255], v[220:223]\n"
      "ds_read_b128 a[248:251], v251 offset:12288\n"
      "ds_read_b128 a[36:39], v249 offset:6144\n"
      "s_waitcnt lgkmcnt(15)\n"
      "v_mfma_f32_16x16x32_bf16 v[192:195], v[0:3], a[192:195], 0\n"
      "global_load_lds_dwordx4 v245, s[70:71] offset:1024\n"
      "v_mfma_f32_16x16x32_bf16 v[196:199], v[4:7], a[192:195], 0\n"
      "ds_read_b128 a[252:255], v251 offset:14336\n"
      "v_mfma_f32_16x16x32_bf16 v[200:203], v[0:3], a[196:199], 0\n"
      "v_cvt_pk_bf16_f32 v224, v216, v217\n"
      "v_cvt_pk_bf16_f32 v225, v218, v219\n"
      "v_cvt_pk_bf16_f32 v226, v220, v221\n"
      "v_mfma_f32_16x16x32_bf16 v[204:207], v[4:7], a[196:199], 0\n"
      "ds_read_b128 a[192:195], v250 offset:16384\n"
      "v_cvt_pk_bf16_f32 v227, v222, v223\n"
      "v_xor_b32 v224, s85, v224\n"
      "v_mfma_f32_16x16x32_bf16 v[192:195], v[8:11], a[200:203], v[192:195]\n"
      "global_load_lds_dwordx4 v246, s[70:71] offset:2048\n"
      "v_xor_b32 v225, s85, v225\n"
      "v_xor_b32 v226, s85, v226\n"
      "v_mfma_f32_16x16x32_bf16 v[196:199], v[12:15], a[200:203], v[196:199]\n"
      "ds_read_b128 a[196:199], v250 offset:18432\n"
      "v_xor_b32 v227, s85, v227\n"
      "s_lshl2_add_u32 s54, s34, s35\n"
      "v_mfma_f32_16x16x32_bf16 v[200:203], v[8:11], a[204:207], v[200:203]\n"
      "s_lshr_b32 s56, s39, 1\n"
      "s_lshl1_add_u32 s56, s33, s56\n"
      "s_lshl_b32 s56, s56, 15\n"
      "s_mul_i32 s55, s54, 266240\n"
      "v_mfma_f32_16x16x32_bf16 v[204:207], v[12:15], a[204:207], v[204:207]\n"
      "s_add_u32 s55, s55, 4096\n"
      "ds_read_b128 a[200:203], v251 offset:16384\n"
      "s_add_u32 s14, s18, s55\n"
      "s_addc_u32 s15, s19, 0\n"
      "s_waitcnt lgkmcnt(15)\n"
      "v_mfma_f32_16x16x32_bf16 v[192:195], v[16:19], a[208:211], v[192:195]\n"
      "s_add_u32 s10, s14, s56\n"
      "s_addc_u32 s11, s15, 0\n"
      "global_load_lds_dwordx4 v247, s[70:71] offset:3072\n"
      "v_mfma_f32_16x16x32_bf16 v[196:199], v[20:23], a[208:211], v[196:199]\n"
      "s_xor_b32 s56, s56, 0x8000\n"
      "ds_read_b128 a[204:207], v251 offset:18432\n"
      "s_add_u32 s14, s14, s56\n"
      "s_addc_u32 s15, s15, 0\n"
      "v_mfma_f32_16x16x32_bf16 v[200:203], v[16:19], a[212:215], v[200:203]\n"
      "s_and_b32 s55, s39, 1\n"
      "s_lshl_b32 s56, s55, 14\n"
      "v_add_u32 v240, s56, v242\n"
      "v_xor_b32 v241, 0x4000, v240\n"
      "v_mfma_f32_16x16x32_bf16 v[204:207], v[20:23], a[212:215], v[204:207]\n"
      "s_add_u32 s72, s10, 0x2000\n"
      "s_addc_u32 s73, s11, 0\n"
      "s_xor_b32 s70, s78, 0\n"
      "s_add_u32 s70, s8, s70\n"
      "s_addc_u32 s71, s9, 0\n"
      "s_add_u32 m0, s41, 12288\n"
      "v_mfma_f32_16x16x32_bf16 v[192:195], v[24:27], a[216:219], v[192:195]\n"
      "s_add_u32 s74, s14, 0x2000\n"
      "s_addc_u32 s75, s15, 0\n"
      "global_load_lds_dwordx4 v244, s[70:71] offset:0\n"
      "v_mfma_f32_16x16x32_bf16 v[196:199], v[28:31], a[216:219], v[196:199]\n"
      "s_lshl2_add_u32 s55, s54, s33\n"
      "ds_read_b128 a[208:211], v250 offset:20480\n"
      "ds_read_b128 a[212:215], v250 offset:22528\n"
      "s_lshl_b32 s55, s55, 12\n"
      "v_mfma_f32_16x16x32_bf16 v[200:203], v[24:27], a[220:223], v[200:203]\n"
      "s_add_u32 s55, s55, 1048576\n"
      "v_and_b32 v253, 7, v243\n"
      "v_lshl_add_u32 v253, v253, 9, s55\n"
      "v_mfma_f32_16x16x32_bf16 v[204:207], v[28:31], a[220:223], v[204:207]\n"
      "ds_read_b128 a[216:219], v251 offset:20480\n"
      "s_waitcnt lgkmcnt(15)\n"
      "v_mfma_f32_16x16x32_bf16 v[192:195], v[32:35], a[224:227], v[192:195]\n"
      "global_load_lds_dwordx4 v245, s[70:71] offset:1024\n"
      "v_mfma_f32_16x16x32_bf16 v[196:199], v[36:39], a[224:227], v[196:199]\n"
      "ds_read_b128 a[220:223], v251 offset:22528\n"
      "s_waitcnt vmcnt(7)\n"
      "s_barrier\n"
      "v_mfma_f32_16x16x32_bf16 v[200:203], v[32:35], a[228:231], v[200:203]\n"
      "v_mfma_f32_16x16x32_bf16 v[204:207], v[36:39], a[228:231], v[204:207]\n"
      "ds_read_b128 a[224:227], v250 offset:24576\n"
      "v_mfma_f32_16x16x32_bf16 v[192:195], v[40:43], a[232:235], v[192:195]\n"
      "global_load_lds_dwordx4 v246, s[70:71] offset:2048\n"
      "v_mfma_f32_16x16x32_bf16 v[196:199], v[44:47], a[232:235], v[196:199]\n"
      "ds_read_b128 a[228:231], v250 offset:26624\n"
      "v_mfma_f32_16x16x32_bf16 v[200:203], v[40:43], a[236:239], v[200:203]\n"
      "v_mfma_f32_16x16x32_bf16 v[204:207], v[44:47], a[236:239], v[204:207]\n"
      "ds_read_b128 a[232:235], v251 offset:24576\n"
      "s_waitcnt lgkmcnt(15)\n"
      "v_mfma_f32_16x16x32_bf16 v[192:195], v[48:51], a[240:243], v[192:195]\n"
      "global_load_lds_dwordx4 v247, s[70:71] offset:3072\n"
      "v_mfma_f32_16x16x32_bf16 v[196:199], v[52:55], a[240:243], v[196:199]\n"
      "ds_read_b128 a[236:239], v251 offset:26624\n"
      "v_mfma_f32_16x16x32_bf16 v[200:203], v[48:51], a[244:247], v[200:203]\n"
      "v_mfma_f32_16x16x32_bf16 v[204:207], v[52:55], a[244:247], v[204:207]\n"
      "s_xor_b32 s70, s79, 0\n"
      "s_add_u32 s70, s8, s70\n"
      "s_addc_u32 s71, s9, 0\n"
      "s_add_u32 m0, s82, 98304\n"
      "s_waitcnt lgkmcnt(14)\n"
      "v_mfma_f32_16x16x32_bf16 v[192:195], v[56:59], a[248:251], v[192:195]\n"
      "global_load_lds_dwordx4 v244, s[70:71] offset:0\n"
      "ds_read_b128 a[240:243], v250 offset:28672\n"
      "v_mfma_f32_16x16x32_bf16 v[196:199], v[60:63], a[248:251], v[196:199]\n"
      "ds_read_b128 a[244:247], v250 offset:30720\n"
      "s_waitcnt lgkmcnt(14)\n"
      "v_mfma_f32_16x16x32_bf16 v[200:203], v[56:59], a[252:255], v[200:203]\n"
      "v_mfma_f32_16x16x32_bf16 v[204:207], v[60:63], a[252:255], v[204:207]\n"
      "ds_read_b128 a[248:251], v251 offset:28672\n"
      "s_waitcnt lgkmcnt(14)\n"
      "v_mfma_f32_16x16x32_bf16 v[192:195], v[64:67], a[192:195], v[192:195]\n"
      "global_load_lds_dwordx4 v245, s[70:71] offset:1024\n"
      "v_mfma_f32_16x16x32_bf16 v[196:199], v[68:71], a[192:195], v[196:199]\n"
      "ds_read_b128 a[252:255], v251 offset:30720\n"
      "v_xor_b32 v250, 196608, v250\n"
      "v_xor_b32 v251, 196608, v251\n"
      "s_waitcnt lgkmcnt(14)\n"
      "v_mfma_f32_16x16x32_bf16 v[200:203], v[64:67], a[196:199], v[200:203]\n"
      "v_mfma_f32_16x16x32_bf16 v[204:207], v[68:71], a[196:199], v[204:207]\n"
      "ds_read_b128 a[192:195], v250 offset:0\n"
      "s_waitcnt lgkmcnt(14)\n"
      "v_mfma_f32_16x16x32_bf16 v[192:195], v[72:75], a[200:203], v[192:195]\n"
      "global_load_lds_dwordx4 v246, s[70:71] offset:2048\n"
      "v_mfma_f32_16x16x32_bf16 v[196:199], v[76:79], a[200:203], v[196:199]\n"
      "ds_read_b128 a[196:199], v250 offset:2048\n"
      "s_waitcnt lgkmcnt(14)\n"
      "v_mfma_f32_16x16x32_bf16 v[200:203], v[72:75], a[204:207], v[200:203]\n"
      "s_waitcnt vmcnt(7)\n"
      "ds_read_b128 a[8:11], v248 offset:8192\n"
      "v_mfma_f32_16x16x32_bf16 v[204:207], v[76:79], a[204:207], v[204:207]\n"
      "ds_read_b128 a[200:203], v251 offset:0\n"
      "ds_read_b128 a[12:15], v248 offset:10240\n"
      "s_waitcnt lgkmcnt(15)\n"
      "v_mfma_f32_16x16x32_bf16 v[192:195], v[80:83], a[208:211], v[192:195]\n"
      "global_load_lds_dwordx4 v247, s[70:71] offset:3072\n"
      "ds_read_b128 a[40:43], v249 offset:8192\n"
      "v_mfma_f32_16x16x32_bf16 v[196:199], v[84:87], a[208:211], v[196:199]\n"
      "ds_read_b128 a[204:207], v251 offset:2048\n"
      "ds_read_b128 a[44:47], v249 offset:10240\n"
      "v_mfma_f32_16x16x32_bf16 v[200:203], v[80:83], a[212:215], v[200:203]\n"
      "v_mfma_f32_16x16x32_bf16 v[204:207], v[84:87], a[212:215], v[204:207]\n"
      "s_xor_b32 s70, s76, 256\n"
      "s_add_u32 s70, s8, s70\n"
      "s_addc_u32 s71, s9, 0\n"
      "s_add_u32 m0, s82, 114688\n"
      "s_waitcnt lgkmcnt(15)\n"
      "v_mfma_f32_16x16x32_bf16 v[192:195], v[88:91], a[216:219], v[192:195]\n"
      "global_load_lds_dwordx4 v244, s[70:71] offset:0\n"
      "ds_read_b128 a[208:211], v250 offset:4096\n"
      "v_mfma_f32_16x16x32_bf16 v[196:199], v[92:95], a[216:219], v[196:199]\n"
      "ds_read_b128 a[212:215], v250 offset:6144\n"
      "v_mfma_f32_16x16x32_bf16 v[200:203], v[88:91], a[220:223], v[200:203]\n"
      "v_mfma_f32_16x16x32_bf16 v[204:207], v[92:95], a[220:223], v[204:207]\n"
      "ds_read_b128 a[216:219], v251 offset:4096\n"
      "v_mfma_f32_16x16x32_bf16 v[192:195], v[96:99], a[224:227], v[192:195]\n"
      "global_load_lds_dwordx4 v245, s[70:71] offset:1024\n"
      "v_mfma_f32_16x16x32_bf16 v[196:199], v[100:103], a[224:227], v[196:199]\n"
      "ds_read_b128 a[220:223], v251 offset:6144\n"
      "s_waitcnt lgkmcnt(15)\n"
      "v_mfma_f32_16x16x32_bf16 v[200:203], v[96:99], a[228:231], v[200:203]\n"
      "v_mfma_f32_16x16x32_bf16 v[204:207], v[100:103], a[228:231], v[204:207]\n"
      "ds_read_b128 a[224:227], v250 offset:8192\n"
      "v_mfma_f32_16x16x32_bf16 v[192:195], v[104:107], a[232:235], v[192:195]\n"
      "global_load_lds_dwordx4 v246, s[70:71] offset:2048\n"
      "v_mfma_f32_16x16x32_bf16 v[196:199], v[108:111], a[232:235], v[196:199]\n"
      "ds_read_b128 a[228:231], v250 offset:10240\n"
      "v_mfma_f32_16x16x32_bf16 v[200:203], v[104:107], a[236:239], v[200:203]\n"
      "s_waitcnt vmcnt(7)\n"
      "ds_read_b128 a[16:19], v248 offset:12288\n"
      "v_mfma_f32_16x16x32_bf16 v[204:207], v[108:111], a[236:239], v[204:207]\n"
      "ds_read_b128 a[232:235], v251 offset:8192\n"
      "ds_read_b128 a[20:23], v248 offset:14336\n"
      "v_mfma_f32_16x16x32_bf16 v[192:195], v[112:115], a[240:243], v[192:195]\n"
      "global_load_lds_dwordx4 v247, s[70:71] offset:3072\n"
      "ds_read_b128 a[48:51], v249 offset:12288\n"
      "v_mfma_f32_16x16x32_bf16 v[196:199], v[116:119], a[240:243], v[196:199]\n"
      "ds_read_b128 a[236:239], v251 offset:10240\n"
      "s_waitcnt lgkmcnt(15)\n"
      "s_barrier\n"
      "v_mfma_f32_16x16x32_bf16 v[200:203], v[112:115], a[244:247], v[200:203]\n"
      "ds_read_b128 a[52:55], v249 offset:14336\n"
      "v_mfma_f32_16x16x32_bf16 v[204:207], v[116:119], a[244:247], v[204:207]\n"
      "s_xor_b32 s70, s77, 256\n"
      "s_add_u32 s70, s8, s70\n"
      "s_addc_u32 s71, s9, 0\n"
      "s_add_u32 m0, s82, 131072\n"
      "v_mfma_f32_16x16x32_bf16 v[192:195], v[120:123], a[248:251], v[192:195]\n"
      "global_load_lds_dwordx4 v244, s[70:71] offset:0\n"
      "ds_read_b128 a[240:243], v250 offset:12288\n"
      "v_mfma_f32_16x16x32_bf16 v[196:199], v[124:127], a[248:251], v[196:199]\n"
      "ds_read_b128 a[244:247], v250 offset:14336\n"
      "v_mfma_f32_16x16x32_bf16 v[200:203], v[120:123], a[252:255], v[200:203]\n"
      "v_mfma_f32_16x16x32_bf16 v[204:207], v[124:127], a[252:255], v[204:207]\n"
      "ds_read_b128 a[248:251], v251 offset:12288\n"
      "v_mfma_f32_16x16x32_bf16 v[208:211], v[0:3], a[192:195], 0\n"
      "global_load_lds_dwordx4 v245, s[70:71] offset:1024\n"
      "v_mfma_f32_16x16x32_bf16 v[212:215], v[4:7], a[192:195], 0\n"
      "ds_read_b128 a[252:255], v251 offset:14336\n"
      "v_mfma_f32_16x16x32_bf16 v[216:219], v[0:3], a[196:199], 0\n"
      "v_cvt_pk_bf16_f32 v228, v192, v193\n"
      "v_cvt_pk_bf16_f32 v229, v194, v195\n"
      "v_cvt_pk_bf16_f32 v230, v196, v197\n"
      "v_mfma_f32_16x16x32_bf16 v[220:223], v[4:7], a[196:199], 0\n"
      "ds_read_b128 a[192:195], v250 offset:16384\n"
      "v_cvt_pk_bf16_f32 v231, v198, v199\n"
      "v_xor_b32 v228, s85, v228\n"
      "v_mfma_f32_16x16x32_bf16 v[208:211], v[8:11], a[200:203], v[208:211]\n"
      "global_load_lds_dwordx4 v246, s[70:71] offset:2048\n"
      "v_xor_b32 v229, s85, v229\n"
      "v_xor_b32 v230, s85, v230\n"
      "v_mfma_f32_16x16x32_bf16 v[212:215], v[12:15], a[200:203], v[212:215]\n"
      "ds_read_b128 a[196:199], v250 offset:18432\n"
      "v_xor_b32 v231, s85, v231\n"
      "v_cvt_pk_bf16_f32 v232, v200, v201\n"
      "s_waitcnt lgkmcnt(15)\n"
      "v_mfma_f32_16x16x32_bf16 v[216:219], v[8:11], a[204:207], v[216:219]\n"
      "s_waitcnt vmcnt(7)\n"
      "ds_read_b128 a[24:27], v236 offset:0\n"
      "v_mfma_f32_16x16x32_bf16 v[220:223], v[12:15], a[204:207], v[220:223]\n"
      "ds_read_b128 a[200:203], v251 offset:16384\n"
      "ds_read_b128 a[28:31], v236 offset:2048\n"
      "v_cvt_pk_bf16_f32 v233, v202, v203\n"
      "v_mfma_f32_16x16x32_bf16 v[208:211], v[16:19], a[208:211], v[208:211]\n"
      "global_load_lds_dwordx4 v247, s[70:71] offset:3072\n"
      "ds_read_b128 a[56:59], v237 offset:0\n"
      "v_cvt_pk_bf16_f32 v234, v204, v205\n"
      "v_mfma_f32_16x16x32_bf16 v[212:215], v[20:23], a[208:211], v[212:215]\n"
      "ds_read_b128 a[204:207], v251 offset:18432\n"
      "ds_read_b128 a[60:63], v237 offset:2048\n"
      "v_cvt_pk_bf16_f32 v235, v206, v207\n"
      "v_mfma_f32_16x16x32_bf16 v[216:219], v[16:19], a[212:215], v[216:219]\n"
      "v_xor_b32 v232, s85, v232\n"
      "v_xor_b32 v233, s85, v233\n"
      "v_xor_b32 v234, s85, v234\n"
      "v_mfma_f32_16x16x32_bf16 v[220:223], v[20:23], a[212:215], v[220:223]\n"
      "s_xor_b32 s70, s78, 256\n"
      "s_add_u32 s70, s8, s70\n"
      "s_addc_u32 s71, s9, 0\n"
      "s_add_u32 m0, s82, 147456\n"
      "v_mfma_f32_16x16x32_bf16 v[208:211], v[24:27], a[216:219], v[208:211]\n"
      "global_load_lds_dwordx4 v244, s[70:71] offset:0\n"
      "ds_read_b128 a[208:211], v250 offset:20480\n"
      "v_xor_b32 v235, s85, v235\n"
      "v_mfma_f32_16x16x32_bf16 v[212:215], v[28:31], a[216:219], v[212:215]\n"
      "ds_read_b128 a[212:215], v250 offset:22528\n"
      "s_waitcnt lgkmcnt(15)\n"
      "v_mfma_f32_16x16x32_bf16 v[216:219], v[24:27], a[220:223], v[216:219]\n"
      "v_mfma_f32_16x16x32_bf16 v[220:223], v[28:31], a[220:223], v[220:223]\n"
      "ds_read_b128 a[216:219], v251 offset:20480\n"
      "v_mfma_f32_16x16x32_bf16 v[208:211], v[32:35], a[224:227], v[208:211]\n"
      "global_load_lds_dwordx4 v245, s[70:71] offset:1024\n"
      "v_mfma_f32_16x16x32_bf16 v[212:215], v[36:39], a[224:227], v[212:215]\n"
      "ds_read_b128 a[220:223], v251 offset:22528\n"
      "v_mfma_f32_16x16x32_bf16 v[216:219], v[32:35], a[228:231], v[216:219]\n"
      "v_mfma_f32_16x16x32_bf16 v[220:223], v[36:39], a[228:231], v[220:223]\n"
      "ds_read_b128 a[224:227], v250 offset:24576\n"
      "v_mfma_f32_16x16x32_bf16 v[208:211], v[40:43], a[232:235], v[208:211]\n"
      "global_load_lds_dwordx4 v246, s[70:71] offset:2048\n"
      "v_mfma_f32_16x16x32_bf16 v[212:215], v[44:47], a[232:235], v[212:215]\n"
      "ds_read_b128 a[228:231], v250 offset:26624\n"
      "v_mfma_f32_16x16x32_bf16 v[216:219], v[40:43], a[236:239], v[216:219]\n"
      "s_waitcnt vmcnt(7)\n"
      "ds_read_b128 a[64:67], v236 offset:16384\n"
      "v_mfma_f32_16x16x32_bf16 v[220:223], v[44:47], a[236:239], v[220:223]\n"
      "ds_read_b128 a[232:235], v251 offset:24576\n"
      "ds_read_b128 a[68:71], v236 offset:18432\n"
      "s_waitcnt lgkmcnt(15)\n"
      "v_mfma_f32_16x16x32_bf16 v[208:211], v[48:51], a[240:243], v[208:211]\n"
      "global_load_lds_dwordx4 v247, s[70:71] offset:3072\n"
      "ds_read_b128 a[96:99], v237 offset:16384\n"
      "v_mfma_f32_16x16x32_bf16 v[212:215], v[52:55], a[240:243], v[212:215]\n"
      "ds_read_b128 a[236:239], v251 offset:26624\n"
      "ds_read_b128 a[100:103], v237 offset:18432\n"
      "v_mfma_f32_16x16x32_bf16 v[216:219], v[48:51], a[244:247], v[216:219]\n"
      "v_mfma_f32_16x16x32_bf16 v[220:223], v[52:55], a[244:247], v[220:223]\n"
      "global_load_dword v252, v253, s[58:59] offset:0 sc1\n"
      "s_xor_b32 s70, s79, 256\n"
      "s_add_u32 s70, s8, s70\n"
      "s_addc_u32 s71, s9, 0\n"
      "s_add_u32 m0, s41, 0\n"
      "v_mfma_f32_16x16x32_bf16 v[208:211], v[56:59], a[248:251], v[208:211]\n"
      "global_load_lds_dwordx4 v244, s[70:71] offset:0\n"
      "ds_read_b128 a[240:243], v250 offset:28672\n"
      "v_mfma_f32_16x16x32_bf16 v[212:215], v[60:63], a[248:251], v[212:215]\n"
      "ds_read_b128 a[244:247], v250 offset:30720\n"
      "v_mfma_f32_16x16x32_bf16 v[216:219], v[56:59], a[252:255], v[216:219]\n"
      "v_mfma_f32_16x16x32_bf16 v[220:223], v[60:63], a[252:255], v[220:223]\n"
      "ds_read_b128 a[248:251], v251 offset:28672\n"
      "v_mfma_f32_16x16x32_bf16 v[208:211], v[64:67], a[192:195], v[208:211]\n"
      "global_load_lds_dwordx4 v245, s[70:71] offset:1024\n"
      "v_mfma_f32_16x16x32_bf16 v[212:215], v[68:71], a[192:195], v[212:215]\n"
      "ds_read_b128 a[252:255], v251 offset:30720\n"
      "v_mfma_f32_16x16x32_bf16 v[216:219], v[64:67], a[196:199], v[216:219]\n"
      "v_mfma_f32_16x16x32_bf16 v[220:223], v[68:71], a[196:199], v[220:223]\n"
      "s_waitcnt lgkmcnt(15)\n"
      "v_mfma_f32_16x16x32_bf16 v[208:211], v[72:75], a[200:203], v[208:211]\n"
      "global_load_lds_dwordx4 v246, s[70:71] offset:2048\n"
      "v_mfma_f32_16x16x32_bf16 v[212:215], v[76:79], a[200:203], v[212:215]\n"
      "v_mfma_f32_16x16x32_bf16 v[216:219], v[72:75], a[204:207], v[216:219]\n"
      "s_waitcnt vmcnt(8)\n"
      "ds_read_b128 a[72:75], v236 offset:32768\n"
      "v_mfma_f32_16x16x32_bf16 v[220:223], v[76:79], a[204:207], v[220:223]\n"
      "ds_read_b128 a[76:79], v236 offset:34816\n"
      "ds_read_b128 a[104:107], v237 offset:32768\n"
      "v_mfma_f32_16x16x32_bf16 v[208:211], v[80:83], a[208:211], v[208:211]\n"
      "global_load_lds_dwordx4 v247, s[70:71] offset:3072\n"
      "ds_read_b128 a[108:111], v237 offset:34816\n"
      "v_mfma_f32_16x16x32_bf16 v[212:215], v[84:87], a[208:211], v[212:215]\n"
      "s_waitcnt lgkmcnt(15)\n"
      "v_mfma_f32_16x16x32_bf16 v[216:219], v[80:83], a[212:215], v[216:219]\n"
      "v_mfma_f32_16x16x32_bf16 v[220:223], v[84:87], a[212:215], v[220:223]\n"
      "s_waitcnt vmcnt(4)\n"
      "v_cmp_ne_u32 vcc, s37, v252\n"
      "s_nop 1\n"
      "s_cbranch_vccnz .Lp_s2_%=\n"
      ".Lp_o3_%=:\n"
      "v_mfma_f32_16x16x32_bf16 v[208:211], v[88:91], a[216:219], v[208:211]\n"
      "global_load_dwordx4 v[128:131], v240, s[10:11] offset:-4096 sc1\n"
      "v_mfma_f32_16x16x32_bf16 v[212:215], v[92:95], a[216:219], v[212:215]\n"
      "v_mfma_f32_16x16x32_bf16 v[216:219], v[88:91], a[220:223], v[216:219]\n"
      "v_mfma_f32_16x16x32_bf16 v[220:223], v[92:95], a[220:223], v[220:223]\n"
      "v_mfma_f32_16x16x32_bf16 v[208:211], v[96:99], a[224:227], v[208:211]\n"
      "global_load_dwordx4 v[132:135], v240, s[10:11] offset:-3072 sc1\n"
      "v_mfma_f32_16x16x32_bf16 v[212:215], v[100:103], a[224:227], v[212:215]\n"
      "s_waitcnt lgkmcnt(14)\n"
      "v_mfma_f32_16x16x32_bf16 v[216:219], v[96:99], a[228:231], v[216:219]\n"
      "v_mfma_f32_16x16x32_bf16 v[220:223], v[100:103], a[228:231], v[220:223]\n"
      "s_waitcnt lgkmcnt(12)\n"
      "v_mfma_f32_16x16x32_bf16 v[208:211], v[104:107], a[232:235], v[208:211]\n"
      "global_load_dwordx4 v[136:139], v240, s[10:11] offset:-2048 sc1\n"
      "v_mfma_f32_16x16x32_bf16 v[212:215], v[108:111], a[232:235], v[212:215]\n"
      "s_waitcnt lgkmcnt(9)\n"
      "v_mfma_f32_16x16x32_bf16 v[216:219], v[104:107], a[236:239], v[216:219]\n"
      "ds_read_b128 a[80:83], v236 offset:49152\n"
      "ds_read_b128 a[84:87], v236 offset:51200\n"
      "v_mfma_f32_16x16x32_bf16 v[220:223], v[108:111], a[236:239], v[220:223]\n"
      "ds_read_b128 a[112:115], v237 offset:49152\n"
      "ds_read_b128 a[116:119], v237 offset:51200\n"
      "s_waitcnt lgkmcnt(11)\n"
      "v_mfma_f32_16x16x32_bf16 v[208:211], v[112:115], a[240:243], v[208:211]\n"
      "global_store_dwordx4 v242, v[224:227], s[62:63] offset:-1024 sc1\n"
      "global_store_dwordx4 v242, v[228:231], s[62:63] offset:0 sc1\n"
      "v_mfma_f32_16x16x32_bf16 v[212:215], v[116:119], a[240:243], v[212:215]\n"
      "global_store_dwordx4 v242, v[232:235], s[62:63] offset:1024 sc1\n"
      "global_load_dwordx4 v[140:143], v240, s[72:73] offset:-4096 sc1\n"
      "s_waitcnt lgkmcnt(10)\n"
      "v_mfma_f32_16x16x32_bf16 v[216:219], v[112:115], a[244:247], v[216:219]\n"
      "v_mfma_f32_16x16x32_bf16 v[220:223], v[116:119], a[244:247], v[220:223]\n"
      "s_waitcnt lgkmcnt(9)\n"
      "v_mfma_f32_16x16x32_bf16 v[208:211], v[120:123], a[248:251], v[208:211]\n"
      "v_mfma_f32_16x16x32_bf16 v[212:215], v[124:127], a[248:251], v[212:215]\n"
      "global_load_dwordx4 v[144:147], v240, s[72:73] offset:-3072 sc1\n"
      "s_waitcnt lgkmcnt(8)\n"
      "v_mfma_f32_16x16x32_bf16 v[216:219], v[120:123], a[252:255], v[216:219]\n"
      "v_mfma_f32_16x16x32_bf16 v[220:223], v[124:127], a[252:255], v[220:223]\n"
      "s_waitcnt vmcnt(7)\n"
      "v_mfma_f32_16x16x32_bf16 v[0:3], v[128:131], a[0:3], 0\n"
      "v_mfma_f32_16x16x32_bf16 v[4:7], v[128:131], a[4:7], 0\n"
      "global_load_dwordx4 v[148:151], v240, s[72:73] offset:-2048 sc1\n"
      "v_mfma_f32_16x16x32_bf16 v[8:11], v[128:131], a[8:11], 0\n"
      "v_cvt_pk_bf16_f32 v192, v208, v209\n"
      "v_cvt_pk_bf16_f32 v193, v210, v211\n"
      "v_mfma_f32_16x16x32_bf16 v[12:15], v[128:131], a[12:15], 0\n"
      "v_cvt_pk_bf16_f32 v194, v212, v213\n"
      "v_cvt_pk_bf16_f32 v195, v214, v215\n"
      "v_mfma_f32_16x16x32_bf16 v[16:19], v[128:131], a[16:19], 0\n"
      "v_xor_b32 v192, s85, v192\n"
      "v_xor_b32 v193, s85, v193\n"
      "v_mfma_f32_16x16x32_bf16 v[20:23], v[128:131], a[20:23], 0\n"
      "v_xor_b32 v194, s85, v194\n"
      "v_xor_b32 v195, s85, v195\n"
      "v_mfma_f32_16x16x32_bf16 v[24:27], v[128:131], a[24:27], 0\n"
      "ds_read_b128 a[88:91], v248 offset:0\n"
      "v_cvt_pk_bf16_f32 v196, v216, v217\n"
      "v_mfma_f32_16x16x32_bf16 v[28:31], v[128:131], a[28:31], 0\n"
      "ds_read_b128 a[92:95], v248 offset:2048\n"
      "v_cvt_pk_bf16_f32 v197, v218, v219\n"
      "s_waitcnt vmcnt(7)\n"
      "v_mfma_f32_16x16x32_bf16 v[32:35], v[132:135], a[0:3], 0\n"
      "s_xor_b32 s70, s76, 128\n"
      "s_add_u32 s70, s8, s70\n"
      "s_addc_u32 s71, s9, 0\n"
      "s_add_u32 m0, s41, 4096\n"
      "v_mfma_f32_16x16x32_bf16 v[36:39], v[132:135], a[4:7], 0\n"
      "global_load_lds_dwordx4 v244, s[70:71] offset:0\n"
      "v_cvt_pk_bf16_f32 v198, v220, v221\n"
      "v_mfma_f32_16x16x32_bf16 v[40:43], v[132:135], a[8:11], 0\n"
      "ds_read_b128 a[120:123], v249 offset:0\n"
      "v_cvt_pk_bf16_f32 v199, v222, v223\n"
      "v_mfma_f32_16x16x32_bf16 v[44:47], v[132:135], a[12:15], 0\n"
      "ds_read_b128 a[124:127], v249 offset:2048\n"
      "v_xor_b32 v196, s85, v196\n"
      "v_mfma_f32_16x16x32_bf16 v[48:51], v[132:135], a[16:19], 0\n"
      "global_load_lds_dwordx4 v245, s[70:71] offset:1024\n"
      "v_xor_b32 v197, s85, v197\n"
      "v_mfma_f32_16x16x32_bf16 v[52:55], v[132:135], a[20:23], 0\n"
      "v_xor_b32 v198, s85, v198\n"
      "v_xor_b32 v199, s85, v199\n"
      "v_mfma_f32_16x16x32_bf16 v[56:59], v[132:135], a[24:27], 0\n"
      "global_store_dwordx4 v242, v[192:195], s[62:63] offset:2048 sc1\n"
      "global_store_dwordx4 v242, v[196:199], s[62:63] offset:3072 sc1\n"
      "v_mfma_f32_16x16x32_bf16 v[60:63], v[132:135], a[28:31], 0\n"
      "s_waitcnt vmcnt(6)\n"
      "global_load_lds_dwordx4 v246, s[70:71] offset:2048\n"
      "v_mfma_f32_16x16x32_bf16 v[64:67], v[136:139], a[0:3], 0\n"
      "v_mfma_f32_16x16x32_bf16 v[68:71], v[136:139], a[4:7], 0\n"
      "v_mfma_f32_16x16x32_bf16 v[72:75], v[136:139], a[8:11], 0\n"
      "global_load_lds_dwordx4 v247, s[70:71] offset:3072\n"
      "v_mfma_f32_16x16x32_bf16 v[76:79], v[136:139], a[12:15], 0\n"
      "v_mfma_f32_16x16x32_bf16 v[80:83], v[136:139], a[16:19], 0\n"
      "global_load_dwordx4 v[152:155], v240, s[14:15] offset:-4096 sc1\n"
      "v_mfma_f32_16x16x32_bf16 v[84:87], v[136:139], a[20:23], 0\n"
      "v_mfma_f32_16x16x32_bf16 v[88:91], v[136:139], a[24:27], 0\n"
      "v_mfma_f32_16x16x32_bf16 v[92:95], v[136:139], a[28:31], 0\n"
      "global_load_dwordx4 v[156:159], v240, s[14:15] offset:-3072 sc1\n"
      "v_mfma_f32_16x16x32_bf16 v[0:3], v[140:143], a[32:35], v[0:3]\n"
      "v_mfma_f32_16x16x32_bf16 v[4:7], v[140:143], a[36:39], v[4:7]\n"
      "v_mfma_f32_16x16x32_bf16 v[8:11], v[140:143], a[40:43], v[8:11]\n"
      "global_load_dwordx4 v[160:163], v240, s[14:15] offset:-2048 sc1\n"
      "v_mfma_f32_16x16x32_bf16 v[12:15], v[140:143], a[44:47], v[12:15]\n"
      "v_mfma_f32_16x16x32_bf16 v[16:19], v[140:143], a[48:51], v[16:19]\n"
      "v_mfma_f32_16x16x32_bf16 v[20:23], v[140:143], a[52:55], v[20:23]\n"
      "s_xor_b32 s70, s77, 128\n"
      "s_add_u32 s70, s8, s70\n"
      "s_addc_u32 s71, s9, 0\n"
      "s_add_u32 m0, s41, 8192\n"
      "v_mfma_f32_16x16x32_bf16 v[24:27], v[140:143], a[56:59], v[24:27]\n"
      "global_load_lds_dwordx4 v244, s[70:71] offset:0\n"
      "v_mfma_f32_16x16x32_bf16 v[28:31], v[140:143], a[60:63], v[28:31]\n"
      "s_waitcnt vmcnt(10)\n"
      "v_mfma_f32_16x16x32_bf16 v[32:35], v[144:147], a[32:35], v[32:35]\n"
      "v_mfma_f32_16x16x32_bf16 v[36:39], v[144:147], a[36:39], v[36:39]\n"
      "global_load_lds_dwordx4 v245, s[70:71] offset:1024\n"
      "v_mfma_f32_16x16x32_bf16 v[40:43], v[144:147], a[40:43], v[40:43]\n"
      "v_mfma_f32_16x16x32_bf16 v[44:47], v[144:147], a[44:47], v[44:47]\n"
      "v_mfma_f32_16x16x32_bf16 v[48:51], v[144:147], a[48:51], v[48:51]\n"
      "global_load_lds_dwordx4 v246, s[70:71] offset:2048\n"
      "v_mfma_f32_16x16x32_bf16 v[52:55], v[144:147], a[52:55], v[52:55]\n"
      "v_mfma_f32_16x16x32_bf16 v[56:59], v[144:147], a[56:59], v[56:59]\n"
      "v_mfma_f32_16x16x32_bf16 v[60:63], v[144:147], a[60:63], v[60:63]\n"
      "global_load_lds_dwordx4 v247, s[70:71] offset:3072\n"
      "v_mfma_f32_16x16x32_bf16 v[64:67], v[148:151], a[32:35], v[64:67]\n"
      "v_mfma_f32_16x16x32_bf16 v[68:71], v[148:151], a[36:39], v[68:71]\n"
      "s_xor_b32 s70, s78, 128\n"
      "s_add_u32 s70, s8, s70\n"
      "s_addc_u32 s71, s9, 0\n"
      "s_add_u32 m0, s41, 12288\n"
      "v_mfma_f32_16x16x32_bf16 v[72:75], v[148:151], a[40:43], v[72:75]\n"
      "global_load_lds_dwordx4 v244, s[70:71] offset:0\n"
      "v_add_u32 v238, s42, v242\n"
      "v_mov_b32 v200, s37\n"
      "v_mov_b32 v201, s37\n"
      "v_mov_b32 v202, s37\n"
      "v_mov_b32 v203, s37\n"
      "s_waitcnt vmcnt(10)\n"
      "s_mov_b64 exec, 0xff\n"
      "global_store_dwordx4 v238, v[200:203], s[58:59] offset:128 sc1\n"
      "s_mov_b64 exec, -1\n"
      "v_mfma_f32_16x16x32_bf16 v[76:79], v[148:151], a[44:47], v[76:79]\n"
      "s_lshl2_add_u32 s48, s36, s35\n"
      "s_mul_i32 s48, s48, 147712\n"
      "s_mul_i32 s49, s33, 3072\n"
      "v_mfma_f32_16x16x32_bf16 v[80:83], v[148:151], a[48:51], v[80:83]\n"
      "s_add_u32 s48, s48, s49\n"
      "s_add_u32 s48, s48, 6291456\n"
      "s_add_u32 s60, s18, s48\n"
      "s_addc_u32 s61, s19, 0\n"
      "v_mfma_f32_16x16x32_bf16 v[84:87], v[148:151], a[52:55], v[84:87]\n"
      "s_mul_i32 s49, s34, 36864\n"
      "global_load_lds_dwordx4 v245, s[70:71] offset:1024\n"
      "s_add_u32 s62, s60, s49\n"
      "s_addc_u32 s63, s61, 0\n"
      "v_mfma_f32_16x16x32_bf16 v[88:91], v[148:151], a[56:59], v[88:91]\n"
      "v_mfma_f32_16x16x32_bf16 v[92:95], v[148:151], a[60:63], v[92:95]\n"
      "s_waitcnt vmcnt(8)\n"
      "v_mfma_f32_16x16x32_bf16 v[0:3], v[152:155], a[64:67], v[0:3]\n"
      "global_load_lds_dwordx4 v246, s[70:71] offset:2048\n"
      "ds_read_b128 a[128:131], v248 offset:4096\n"
      "v_mfma_f32_16x16x32_bf16 v[4:7], v[152:155], a[68:71], v[4:7]\n"
      "ds_read_b128 a[132:135], v248 offset:6144\n"
      "ds_read_b128 a[160:163], v249 offset:4096\n"
      "s_waitcnt lgkmcnt(14)\n"
      "v_mfma_f32_16x16x32_bf16 v[8:11], v[152:155], a[72:75], v[8:11]\n"
      "ds_read_b128 a[164:167], v249 offset:6144\n"
      "s_waitcnt lgkmcnt(14)\n"
      "v_mfma_f32_16x16x32_bf16 v[12:15], v[152:155], a[76:79], v[12:15]\n"
      "global_load_lds_dwordx4 v247, s[70:71] offset:3072\n"
      "s_waitcnt lgkmcnt(11)\n"
      "v_mfma_f32_16x16x32_bf16 v[16:19], v[152:155], a[80:83], v[16:19]\n"
      "s_waitcnt lgkmcnt(10)\n"
      "v_mfma_f32_16x16x32_bf16 v[20:23], v[152:155], a[84:87], v[20:23]\n"
      "global_load_dwordx4 v[164:167], v240, s[74:75] offset:-4096 sc1\n"
      "s_waitcnt lgkmcnt(7)\n"
      "v_mfma_f32_16x16x32_bf16 v[24:27], v[152:155], a[88:91], v[24:27]\n"
      "s_waitcnt lgkmcnt(6)\n"
      "v_mfma_f32_16x16x32_bf16 v[28:31], v[152:155], a[92:95], v[28:31]\n"
      "v_mfma_f32_16x16x32_bf16 v[32:35], v[156:159], a[64:67], v[32:35]\n"
      "global_load_dwordx4 v[168:171], v240, s[74:75] offset:-3072 sc1\n"
      "v_mfma_f32_16x16x32_bf16 v[36:39], v[156:159], a[68:71], v[36:39]\n"
      "v_mfma_f32_16x16x32_bf16 v[40:43], v[156:159], a[72:75], v[40:43]\n"
      "v_mfma_f32_16x16x32_bf16 v[44:47], v[156:159], a[76:79], v[44:47]\n"
      "global_load_dwordx4 v[172:175], v240, s[74:75] offset:-2048 sc1\n"
      "v_mfma_f32_16x16x32_bf16 v[48:51], v[156:159], a[80:83], v[48:51]\n"
      "v_mfma_f32_16x16x32_bf16 v[52:55], v[156:159], a[84:87], v[52:55]\n"
      "v_mfma_f32_16x16x32_bf16 v[56:59], v[156:159], a[88:91], v[56:59]\n"
      "s_xor_b32 s70, s79, 128\n"
      "s_add_u32 s70, s8, s70\n"
      "s_addc_u32 s71, s9, 0\n"
      "s_add_u32 m0, s82, 98304\n"
      "v_mfma_f32_16x16x32_bf16 v[60:63], v[156:159], a[92:95], v[60:63]\n"
      "s_waitcnt vmcnt(2)\n"
      "global_load_lds_dwordx4 v244, s[70:71] offset:0\n"
      "v_mfma_f32_16x16x32_bf16 v[64:67], v[160:163], a[64:67], v[64:67]\n"
      "v_mfma_f32_16x16x32_bf16 v[68:71], v[160:163], a[68:71], v[68:71]\n"
      "v_mfma_f32_16x16x32_bf16 v[72:75], v[160:163], a[72:75], v[72:75]\n"
      "global_load_lds_dwordx4 v245, s[70:71] offset:1024\n"
      "v_mfma_f32_16x16x32_bf16 v[76:79], v[160:163], a[76:79], v[76:79]\n"
      "v_mfma_f32_16x16x32_bf16 v[80:83], v[160:163], a[80:83], v[80:83]\n"
      "v_mfma_f32_16x16x32_bf16 v[84:87], v[160:163], a[84:87], v[84:87]\n"
      "global_load_lds_dwordx4 v246, s[70:71] offset:2048\n"
      "ds_read_b128 a[136:139], v248 offset:8192\n"
      "v_mfma_f32_16x16x32_bf16 v[88:91], v[160:163], a[88:91], v[88:91]\n"
      "ds_read_b128 a[140:143], v248 offset:10240\n"
      "ds_read_b128 a[168:171], v249 offset:8192\n"
      "v_mfma_f32_16x16x32_bf16 v[92:95], v[160:163], a[92:95], v[92:95]\n"
      "ds_read_b128 a[172:175], v249 offset:10240\n"
      "v_mfma_f32_16x16x32_bf16 v[0:3], v[164:167], a[96:99], v[0:3]\n"
      "global_load_lds_dwordx4 v247, s[70:71] offset:3072\n"
      "v_mfma_f32_16x16x32_bf16 v[4:7], v[164:167], a[100:103], v[4:7]\n"
      "v_mfma_f32_16x16x32_bf16 v[8:11], v[164:167], a[104:107], v[8:11]\n"
      "global_load_dwordx4 v[176:179], v241, s[10:11] offset:-4096 sc1\n"
      "v_mfma_f32_16x16x32_bf16 v[12:15], v[164:167], a[108:111], v[12:15]\n"
      "v_mfma_f32_16x16x32_bf16 v[16:19], v[164:167], a[112:115], v[16:19]\n"
      "v_mfma_f32_16x16x32_bf16 v[20:23], v[164:167], a[116:119], v[20:23]\n"
      "global_load_dwordx4 v[180:183], v241, s[10:11] offset:-3072 sc1\n"
      "s_waitcnt lgkmcnt(9)\n"
      "v_mfma_f32_16x16x32_bf16 v[24:27], v[164:167], a[120:123], v[24:27]\n"
      "s_waitcnt lgkmcnt(8)\n"
      "v_mfma_f32_16x16x32_bf16 v[28:31], v[164:167], a[124:127], v[28:31]\n"
      "s_waitcnt vmcnt(6)\n"
      "v_mfma_f32_16x16x32_bf16 v[32:35], v[168:171], a[96:99], v[32:35]\n"
      "global_load_dwordx4 v[184:187], v241, s[10:11] offset:-2048 sc1\n"
      "global_load_dwordx4 v[188:191], v241, s[72:73] offset:-4096 sc1\n"
      "v_mfma_f32_16x16x32_bf16 v[36:39], v[168:171], a[100:103], v[36:39]\n"
      "global_load_dwordx4 v[128:131], v241, s[72:73] offset:-3072 sc1\n"
      "global_load_dwordx4 v[132:135], v241, s[72:73] offset:-2048 sc1\n"
      "v_mfma_f32_16x16x32_bf16 v[40:43], v[168:171], a[104:107], v[40:43]\n"
      "global_load_dwordx4 v[136:139], v241, s[14:15] offset:-4096 sc1\n"
      "global_load_dwordx4 v[140:143], v241, s[74:75] offset:-4096 sc1\n"
      "v_mfma_f32_16x16x32_bf16 v[44:47], v[168:171], a[108:111], v[44:47]\n"
      "s_xor_b32 s70, s76, 384\n"
      "s_add_u32 s70, s8, s70\n"
      "s_addc_u32 s71, s9, 0\n"
      "s_add_u32 m0, s82, 114688\n"
      "v_mfma_f32_16x16x32_bf16 v[48:51], v[168:171], a[112:115], v[48:51]\n"
      "global_load_lds_dwordx4 v244, s[70:71] offset:0\n"
      "global_load_dwordx4 v[144:147], v241, s[14:15] offset:-3072 sc1\n"
      "v_mfma_f32_16x16x32_bf16 v[52:55], v[168:171], a[116:119], v[52:55]\n"
      "global_load_dwordx4 v[148:151], v241, s[74:75] offset:-3072 sc1\n"
      "global_load_dwordx4 v[152:155], v241, s[14:15] offset:-2048 sc1\n"
      "v_mfma_f32_16x16x32_bf16 v[56:59], v[168:171], a[120:123], v[56:59]\n"
      "global_load_dwordx4 v[156:159], v241, s[74:75] offset:-2048 sc1\n"
      "global_load_dword v252, v253, s[58:59] offset:128 sc1\n"
      "v_mfma_f32_16x16x32_bf16 v[60:63], v[168:171], a[124:127], v[60:63]\n"
      "global_load_lds_dwordx4 v245, s[70:71] offset:1024\n"
      "ds_read_b128 a[144:147], v248 offset:12288\n"
      "v_mfma_f32_16x16x32_bf16 v[64:67], v[172:175], a[96:99], v[64:67]\n"
      "ds_read_b128 a[148:151], v248 offset:14336\n"
      "ds_read_b128 a[176:179], v249 offset:12288\n"
      "v_mfma_f32_16x16x32_bf16 v[68:71], v[172:175], a[100:103], v[68:71]\n"
      "ds_read_b128 a[180:183], v249 offset:14336\n"
      "v_mfma_f32_16x16x32_bf16 v[72:75], v[172:175], a[104:107], v[72:75]\n"
      "global_load_lds_dwordx4 v246, s[70:71] offset:2048\n"
      "v_mfma_f32_16x16x32_bf16 v[76:79], v[172:175], a[108:111], v[76:79]\n"
      "v_mfma_f32_16x16x32_bf16 v[80:83], v[172:175], a[112:115], v[80:83]\n"
      "v_mfma_f32_16x16x32_bf16 v[84:87], v[172:175], a[116:119], v[84:87]\n"
      "global_load_lds_dwordx4 v247, s[70:71] offset:3072\n"
      "v_mfma_f32_16x16x32_bf16 v[88:91], v[172:175], a[120:123], v[88:91]\n"
      "v_mfma_f32_16x16x32_bf16 v[92:95], v[172:175], a[124:127], v[92:95]\n"
      "s_waitcnt vmcnt(15)\n"
      "s_xor_b32 s70, s77, 384\n"
      "s_add_u32 s70, s8, s70\n"
      "s_addc_u32 s71, s9, 0\n"
      "s_add_u32 m0, s82, 131072\n"
      "s_waitcnt lgkmcnt(11)\n"
      "v_mfma_f32_16x16x32_bf16 v[0:3], v[176:179], a[128:131], v[0:3]\n"
      "global_load_lds_dwordx4 v244, s[70:71] offset:0\n"
      "s_waitcnt lgkmcnt(10)\n"
      "v_mfma_f32_16x16x32_bf16 v[4:7], v[176:179], a[132:135], v[4:7]\n"
      "s_waitcnt lgkmcnt(7)\n"
      "v_mfma_f32_16x16x32_bf16 v[8:11], v[176:179], a[136:139], v[8:11]\n"
      "s_waitcnt lgkmcnt(6)\n"
      "v_mfma_f32_16x16x32_bf16 v[12:15], v[176:179], a[140:143], v[12:15]\n"
      "global_load_lds_dwordx4 v245, s[70:71] offset:1024\n"
      "s_waitcnt lgkmcnt(3)\n"
      "v_mfma_f32_16x16x32_bf16 v[16:19], v[176:179], a[144:147], v[16:19]\n"
      "s_lshr_b32 s54, s34, 1\n"
      "s_lshl2_add_u32 s54, s54, s35\n"
      "s_lshl_b32 s54, s54, 12\n"
      "s_waitcnt lgkmcnt(2)\n"
      "v_mfma_f32_16x16x32_bf16 v[20:23], v[176:179], a[148:151], v[20:23]\n"
      "s_add_u32 s54, s54, 4096\n"
      "v_lshl_add_u32 v200, v243, 6, s54\n"
      "global_load_dwordx2 v[202:203], v200, s[58:59] sc1\n"
      "ds_read_b128 a[152:155], v236 offset:0\n"
      "ds_read_b128 a[156:159], v236 offset:2048\n"
      "ds_read_b128 a[184:187], v237 offset:0\n"
      "ds_read_b128 a[188:191], v237 offset:2048\n"
      "s_waitcnt lgkmcnt(3)\n"
      "v_mfma_f32_16x16x32_bf16 v[24:27], v[176:179], a[152:155], v[24:27]\n"
      "global_load_lds_dwordx4 v246, s[70:71] offset:2048\n"
      "s_waitcnt vmcnt(4)\n"
      "ds_read_b128 a[192:195], v236 offset:16384\n"
      "s_waitcnt lgkmcnt(3)\n"
      "v_mfma_f32_16x16x32_bf16 v[28:31], v[176:179], a[156:159], v[28:31]\n"
      "ds_read_b128 a[196:199], v236 offset:18432\n"
      "ds_read_b128 a[224:227], v237 offset:16384\n"
      "v_mfma_f32_16x16x32_bf16 v[32:35], v[180:183], a[128:131], v[32:35]\n"
      "ds_read_b128 a[228:231], v237 offset:18432\n"
      "v_mfma_f32_16x16x32_bf16 v[36:39], v[180:183], a[132:135], v[36:39]\n"
      "global_load_lds_dwordx4 v247, s[70:71] offset:3072\n"
      "v_mfma_f32_16x16x32_bf16 v[40:43], v[180:183], a[136:139], v[40:43]\n"
      "v_mfma_f32_16x16x32_bf16 v[44:47], v[180:183], a[140:143], v[44:47]\n"
      "s_xor_b32 s70, s78, 384\n"
      "s_add_u32 s70, s8, s70\n"
      "s_addc_u32 s71, s9, 0\n"
      "s_add_u32 m0, s82, 147456\n"
      "v_mfma_f32_16x16x32_bf16 v[48:51], v[180:183], a[144:147], v[48:51]\n"
      "global_load_lds_dwordx4 v244, s[70:71] offset:0\n"
      "v_mfma_f32_16x16x32_bf16 v[52:55], v[180:183], a[148:151], v[52:55]\n"
      "v_mfma_f32_16x16x32_bf16 v[56:59], v[180:183], a[152:155], v[56:59]\n"
      "v_mfma_f32_16x16x32_bf16 v[60:63], v[180:183], a[156:159], v[60:63]\n"
      "global_load_lds_dwordx4 v245, s[70:71] offset:1024\n"
      "v_mfma_f32_16x16x32_bf16 v[64:67], v[184:187], a[128:131], v[64:67]\n"
      "v_mfma_f32_16x16x32_bf16 v[68:71], v[184:187], a[132:135], v[68:71]\n"
      "v_mfma_f32_16x16x32_bf16 v[72:75], v[184:187], a[136:139], v[72:75]\n"
      "global_load_lds_dwordx4 v246, s[70:71] offset:2048\n"
      "v_mfma_f32_16x16x32_bf16 v[76:79], v[184:187], a[140:143], v[76:79]\n"
      "v_mfma_f32_16x16x32_bf16 v[80:83], v[184:187], a[144:147], v[80:83]\n"
      "v_mfma_f32_16x16x32_bf16 v[84:87], v[184:187], a[148:151], v[84:87]\n"
      "global_load_lds_dwordx4 v247, s[70:71] offset:3072\n"
      "v_mfma_f32_16x16x32_bf16 v[88:91], v[184:187], a[152:155], v[88:91]\n"
      "v_mfma_f32_16x16x32_bf16 v[92:95], v[184:187], a[156:159], v[92:95]\n"
      "s_xor_b32 s70, s79, 384\n"
      "s_add_u32 s70, s8, s70\n"
      "s_addc_u32 s71, s9, 0\n"
      "s_add_u32 m0, s41, 0\n"
      "v_mfma_f32_16x16x32_bf16 v[0:3], v[188:191], a[160:163], v[0:3]\n"
      "global_load_lds_dwordx4 v244, s[70:71] offset:0\n"
      "v_mfma_f32_16x16x32_bf16 v[4:7], v[188:191], a[164:167], v[4:7]\n"
      "v_mfma_f32_16x16x32_bf16 v[8:11], v[188:191], a[168:171], v[8:11]\n"
      "v_mfma_f32_16x16x32_bf16 v[12:15], v[188:191], a[172:175], v[12:15]\n"
      "global_load_lds_dwordx4 v245, s[70:71] offset:1024\n"
      "v_mfma_f32_16x16x32_bf16 v[16:19], v[188:191], a[176:179], v[16:19]\n"
      "v_mfma_f32_16x16x32_bf16 v[20:23], v[188:191], a[180:183], v[20:23]\n"
      "s_waitcnt lgkmcnt(5)\n"
      "v_mfma_f32_16x16x32_bf16 v[24:27], v[188:191], a[184:187], v[24:27]\n"
      "global_load_lds_dwordx4 v246, s[70:71] offset:2048\n"
      "s_waitcnt lgkmcnt(4)\n"
      "v_mfma_f32_16x16x32_bf16 v[28:31], v[188:191], a[188:191], v[28:31]\n"
      "v_mfma_f32_16x16x32_bf16 v[32:35], v[128:131], a[160:163], v[32:35]\n"
      "v_mfma_f32_16x16x32_bf16 v[36:39], v[128:131], a[164:167], v[36:39]\n"
      "global_load_lds_dwordx4 v247, s[70:71] offset:3072\n"
      "v_mfma_f32_16x16x32_bf16 v[40:43], v[128:131], a[168:171], v[40:43]\n"
      "v_mfma_f32_16x16x32_bf16 v[44:47], v[128:131], a[172:175], v[44:47]\n"
      "v_mfma_f32_16x16x32_bf16 v[48:51], v[128:131], a[176:179], v[48:51]\n"
      "v_mfma_f32_16x16x32_bf16 v[52:55], v[128:131], a[180:183], v[52:55]\n"
      "v_mfma_f32_16x16x32_bf16 v[56:59], v[128:131], a[184:187], v[56:59]\n"
      "v_mfma_f32_16x16x32_bf16 v[60:63], v[128:131], a[188:191], v[60:63]\n"
      "s_waitcnt vmcnt(8)\n"
      "ds_read_b128 a[200:203], v236 offset:32768\n"
      "v_mfma_f32_16x16x32_bf16 v[64:67], v[132:135], a[160:163], v[64:67]\n"
      "ds_read_b128 a[204:207], v236 offset:34816\n"
      "ds_read_b128 a[232:235], v237 offset:32768\n"
      "v_mfma_f32_16x16x32_bf16 v[68:71], v[132:135], a[164:167], v[68:71]\n"
      "ds_read_b128 a[236:239], v237 offset:34816\n"
      "v_mfma_f32_16x16x32_bf16 v[72:75], v[132:135], a[168:171], v[72:75]\n"
      "v_cmp_ne_u32 vcc, s37, v252\n"
      "s_nop 1\n"
      "s_cbranch_vccnz .Lp_s5_%=\n"
      ".Lp_o6_%=:\n"
      "v_mfma_f32_16x16x32_bf16 v[76:79], v[132:135], a[172:175], v[76:79]\n"
      "v_mfma_f32_16x16x32_bf16 v[80:83], v[132:135], a[176:179], v[80:83]\n"
      "global_load_dwordx4 v[160:163], v240, s[10:11] offset:-1024 sc1\n"
      "global_load_dwordx4 v[164:167], v240, s[72:73] offset:-1024 sc1\n"
      "v_mfma_f32_16x16x32_bf16 v[84:87], v[132:135], a[180:183], v[84:87]\n"
      "global_load_dwordx4 v[168:171], v240, s[14:15] offset:-1024 sc1\n"
      "global_load_dwordx4 v[172:175], v240, s[74:75] offset:-1024 sc1\n"
      "v_mfma_f32_16x16x32_bf16 v[88:91], v[132:135], a[184:187], v[88:91]\n"
      "global_load_dwordx4 v[176:179], v241, s[10:11] offset:-1024 sc1\n"
      "global_load_dwordx4 v[180:183], v241, s[72:73] offset:-1024 sc1\n"
      "v_mfma_f32_16x16x32_bf16 v[92:95], v[132:135], a[188:191], v[92:95]\n"
      "global_load_dwordx4 v[184:187], v241, s[14:15] offset:-1024 sc1\n"
      "global_load_dwordx4 v[188:191], v241, s[74:75] offset:-1024 sc1\n"
      "s_waitcnt lgkmcnt(7)\n"
      "v_mfma_f32_16x16x32_bf16 v[0:3], v[136:139], a[192:195], v[0:3]\n"
      "global_load_dwordx4 v[128:131], v240, s[10:11] offset:0 sc1\n"
      "global_load_dwordx4 v[132:135], v240, s[72:73] offset:0 sc1\n"
      "s_waitcnt lgkmcnt(6)\n"
      "v_mfma_f32_16x16x32_bf16 v[4:7], v[136:139], a[196:199], v[4:7]\n"
      "s_waitcnt lgkmcnt(3)\n"
      "v_mfma_f32_16x16x32_bf16 v[8:11], v[136:139], a[200:203], v[8:11]\n"
      "s_waitcnt lgkmcnt(2)\n"
      "v_mfma_f32_16x16x32_bf16 v[12:15], v[136:139], a[204:207], v[12:15]\n"
      "s_waitcnt vmcnt(14)\n"
      "ds_read_b128 a[208:211], v236 offset:49152\n"
      "ds_read_b128 a[212:215], v236 offset:51200\n"
      "ds_read_b128 a[240:243], v237 offset:49152\n"
      "ds_read_b128 a[244:247], v237 offset:51200\n"
      "s_waitcnt lgkmcnt(3)\n"
      "v_mfma_f32_16x16x32_bf16 v[16:19], v[136:139], a[208:211], v[16:19]\n"
      "s_waitcnt vmcnt(10)\n"
      "ds_read_b128 a[216:219], v248 offset:0\n"
      "s_waitcnt lgkmcnt(3)\n"
      "v_mfma_f32_16x16x32_bf16 v[20:23], v[136:139], a[212:215], v[20:23]\n"
      "ds_read_b128 a[220:223], v248 offset:2048\n"
      "ds_read_b128 a[248:251], v249 offset:0\n"
      "ds_read_b128 a[252:255], v249 offset:2048\n"
      "s_waitcnt lgkmcnt(3)\n"
      "v_mfma_f32_16x16x32_bf16 v[24:27], v[136:139], a[216:219], v[24:27]\n"
      "s_sub_u32 s48, s33, 1\n"
      "s_and_b32 s48, s48, 3\n"
      "s_lshl_b32 s48, s48, 14\n"
      "s_waitcnt lgkmcnt(2)\n"
      "v_mfma_f32_16x16x32_bf16 v[28:31], v[136:139], a[220:223], v[28:31]\n"
      "global_load_dwordx4 v[136:139], v240, s[14:15] offset:0 sc1\n"
      "v_lshl_add_u32 v244, v243, 2, s48\n"
      "v_mfma_f32_16x16x32_bf16 v[0:3], v[140:143], a[224:227], v[0:3]\n"
      "s_sub_u32 s48, s33, 2\n"
      "s_and_b32 s48, s48, 3\n"
      "s_lshl_b32 s48, s48, 14\n"
      "v_mfma_f32_16x16x32_bf16 v[4:7], v[140:143], a[228:231], v[4:7]\n"
      "v_lshl_add_u32 v245, v243, 2, s48\n"
      "s_sub_u32 s48, s33, 3\n"
      "v_mfma_f32_16x16x32_bf16 v[8:11], v[140:143], a[232:235], v[8:11]\n"
      "s_and_b32 s48, s48, 3\n"
      "s_lshl_b32 s48, s48, 14\n"
      "v_lshl_add_u32 v246, v243, 2, s48\n"
      "v_mfma_f32_16x16x32_bf16 v[12:15], v[140:143], a[236:239], v[12:15]\n"
      "v_mfma_f32_16x16x32_bf16 v[16:19], v[140:143], a[240:243], v[16:19]\n"
      "v_mfma_f32_16x16x32_bf16 v[20:23], v[140:143], a[244:247], v[20:23]\n"
      "s_waitcnt lgkmcnt(1)\n"
      "v_mfma_f32_16x16x32_bf16 v[24:27], v[140:143], a[248:251], v[24:27]\n"
      "s_waitcnt lgkmcnt(0)\n"
      "v_mfma_f32_16x16x32_bf16 v[28:31], v[140:143], a[252:255], v[28:31]\n"
      "global_load_dwordx4 v[140:143], v240, s[74:75] offset:0 sc1\n"
      "v_mfma_f32_16x16x32_bf16 v[32:35], v[144:147], a[192:195], v[32:35]\n"
      "v_cmp_ne_u32 vcc, s37, v203\n"
      "s_nop 1\n"
      "s_cbranch_vccnz .Lp_s8_%=\n"
      ".Lp_o9_%=:\n"
      "v_mfma_f32_16x16x32_bf16 v[36:39], v[144:147], a[196:199], v[36:39]\n"
      "s_mov_b32 m0, s41\n"
      "v_cvt_pk_bf16_f32 v8, v8, v9\n"
      "v_cvt_pk_bf16_f32 v9, v10, v11\n"
      "v_mfma_f32_16x16x32_bf16 v[40:43], v[144:147], a[200:203], v[40:43]\n"
      "ds_write_addtid_b32 v8 offset:0\n"
      "v_cvt_pk_bf16_f32 v10, v12, v13\n"
      "v_mfma_f32_16x16x32_bf16 v[44:47], v[144:147], a[204:207], v[44:47]\n"
      "ds_write_addtid_b32 v9 offset:256\n"
      "v_cvt_pk_bf16_f32 v11, v14, v15\n"
      "v_mfma_f32_16x16x32_bf16 v[48:51], v[144:147], a[208:211], v[48:51]\n"
      "ds_write_addtid_b32 v10 offset:512\n"
      "v_cvt_pk_bf16_f32 v12, v16, v17\n"
      "v_mfma_f32_16x16x32_bf16 v[52:55], v[144:147], a[212:215], v[52:55]\n"
      "ds_write_addtid_b32 v11 offset:768\n"
      "v_cvt_pk_bf16_f32 v13, v18, v19\n"
      "v_mfma_f32_16x16x32_bf16 v[56:59], v[144:147], a[216:219], v[56:59]\n"
      "ds_write_addtid_b32 v12 offset:1024\n"
      "v_cvt_pk_bf16_f32 v14, v20, v21\n"
      "v_mfma_f32_16x16x32_bf16 v[60:63], v[144:147], a[220:223], v[60:63]\n"
      "global_load_dwordx4 v[144:147], v241, s[10:11] offset:0 sc1\n"
      "v_max_u32_dpp v202, v202, v202 quad_perm:[1,0,3,2] row_mask:0xf bank_mask:0xf\n"
      "v_mfma_f32_16x16x32_bf16 v[32:35], v[148:151], a[224:227], v[32:35]\n"
      "ds_write_addtid_b32 v13 offset:1280\n"
      "v_cvt_pk_bf16_f32 v15, v22, v23\n"
      "v_mfma_f32_16x16x32_bf16 v[36:39], v[148:151], a[228:231], v[36:39]\n"
      "ds_write_addtid_b32 v14 offset:1536\n"
      "v_cvt_pk_bf16_f32 v16, v24, v25\n"
      "v_mfma_f32_16x16x32_bf16 v[40:43], v[148:151], a[232:235], v[40:43]\n"
      "ds_write_addtid_b32 v15 offset:1792\n"
      "v_cvt_pk_bf16_f32 v17, v26, v27\n"
      "v_mfma_f32_16x16x32_bf16 v[44:47], v[148:151], a[236:239], v[44:47]\n"
      "ds_write_addtid_b32 v16 offset:2048\n"
      "v_cvt_pk_bf16_f32 v18, v28, v29\n"
      "v_mfma_f32_16x16x32_bf16 v[48:51], v[148:151], a[240:243], v[48:51]\n"
      "ds_write_addtid_b32 v17 offset:2304\n"
      "v_cvt_pk_bf16_f32 v19, v30, v31\n"
      "v_mfma_f32_16x16x32_bf16 v[52:55], v[148:151], a[244:247], v[52:55]\n"
      "ds_write_addtid_b32 v18 offset:2560\n"
      "v_max_u32_dpp v202, v202, v202 quad_perm:[2,3,0,1] row_mask:0xf bank_mask:0xf\n"
      "v_mfma_f32_16x16x32_bf16 v[56:59], v[148:151], a[248:251], v[56:59]\n"
      "ds_write_addtid_b32 v19 offset:2816\n"
      "v_max_u32_dpp v202, v202, v202 row_half_mirror row_mask:0xf bank_mask:0xf\n"
      "v_mfma_f32_16x16x32_bf16 v[60:63], v[148:151], a[252:255], v[60:63]\n"
      "global_load_dwordx4 v[148:151], v241, s[72:73] offset:0 sc1\n"
      "v_max_u32_dpp v202, v202, v202 row_mirror row_mask:0xf bank_mask:0xf\n"
      "v_mfma_f32_16x16x32_bf16 v[64:67], v[152:155], a[192:195], v[64:67]\n"
      "v_mov_b32 v201, v202\n"
      "s_nop 1\n"
      "v_permlane16_swap_b32 v202, v201\n"
      "v_mfma_f32_16x16x32_bf16 v[68:71], v[152:155], a[196:199], v[68:71]\n"
      "v_cvt_pk_bf16_f32 v40, v40, v41\n"
      "v_cvt_pk_bf16_f32 v41, v42, v43\n"
      "v_mfma_f32_16x16x32_bf16 v[72:75], v[152:155], a[200:203], v[72:75]\n"
      "ds_write_addtid_b32 v40 offset:3072\n"
      "v_cvt_pk_bf16_f32 v42, v44, v45\n"
      "v_mfma_f32_16x16x32_bf16 v[76:79], v[152:155], a[204:207], v[76:79]\n"
      "ds_write_addtid_b32 v41 offset:3328\n"
      "v_cvt_pk_bf16_f32 v43, v46, v47\n"
      "v_mfma_f32_16x16x32_bf16 v[80:83], v[152:155], a[208:211], v[80:83]\n"
      "ds_write_addtid_b32 v42 offset:3584\n"
      "v_cvt_pk_bf16_f32 v44, v48, v49\n"
      "v_mfma_f32_16x16x32_bf16 v[84:87], v[152:155], a[212:215], v[84:87]\n"
      "ds_write_addtid_b32 v43 offset:3840\n"
      "v_cvt_pk_bf16_f32 v45, v50, v51\n"
      "v_mfma_f32_16x16x32_bf16 v[88:91], v[152:155], a[216:219], v[88:91]\n"
      "ds_write_addtid_b32 v44 offset:4096\n"
      "v_cvt_pk_bf16_f32 v46, v52, v53\n"
      "v_mfma_f32_16x16x32_bf16 v[92:95], v[152:155], a[220:223], v[92:95]\n"
      "global_load_dwordx4 v[152:155], v241, s[14:15] offset:0 sc1\n"
      "v_max_u32 v202, v202, v201\n"
      "v_mfma_f32_16x16x32_bf16 v[64:67], v[156:159], a[224:227], v[64:67]\n"
      "ds_write_addtid_b32 v45 offset:4352\n"
      "v_cvt_pk_bf16_f32 v47, v54, v55\n"
      "v_mfma_f32_16x16x32_bf16 v[68:71], v[156:159], a[228:231], v[68:71]\n"
      "ds_write_addtid_b32 v46 offset:4608\n"
      "v_cvt_pk_bf16_f32 v48, v56, v57\n"
      "v_mfma_f32_16x16x32_bf16 v[72:75], v[156:159], a[232:235], v[72:75]\n"
      "ds_write_addtid_b32 v47 offset:4864\n"
      "v_cvt_pk_bf16_f32 v49, v58, v59\n"
      "v_mfma_f32_16x16x32_bf16 v[76:79], v[156:159], a[236:239], v[76:79]\n"
      "ds_write_addtid_b32 v48 offset:5120\n"
      "v_cvt_pk_bf16_f32 v50, v60, v61\n"
      "v_mfma_f32_16x16x32_bf16 v[80:83], v[156:159], a[240:243], v[80:83]\n"
      "ds_write_addtid_b32 v49 offset:5376\n"
      "v_cvt_pk_bf16_f32 v51, v62, v63\n"
      "v_mfma_f32_16x16x32_bf16 v[84:87], v[156:159], a[244:247], v[84:87]\n"
      "ds_write_addtid_b32 v50 offset:5632\n"
      "v_mov_b32 v201, v202\n"
      "v_mfma_f32_16x16x32_bf16 v[88:91], v[156:159], a[248:251], v[88:91]\n"
      "ds_write_addtid_b32 v51 offset:5888\n"
      "v_permlane32_swap_b32 v202, v201\n"
      "v_mfma_f32_16x16x32_bf16 v[92:95], v[156:159], a[252:255], v[92:95]\n"
      "s_waitcnt vmcnt(13)\n"
      "global_load_dwordx4 v[156:159], v241, s[74:75] offset:0 sc1\n"
      "v_max_u32 v202, v202, v201\n"
      "v_mfma_f32_16x16x32_bf16 v[96:99], v[160:163], a[0:3], 0\n"
      "v_readfirstlane_b32 s40, v202\n"
      "s_nop 1\n"
      "s_andn2_b32 s42, 0x7ff, s40\n"
      "v_mfma_f32_16x16x32_bf16 v[100:103], v[160:163], a[4:7], 0\n"
      "s_waitcnt lgkmcnt(0)\n"
      "s_barrier\n"
      "v_mfma_f32_16x16x32_bf16 v[104:107], v[160:163], a[8:11], 0\n"
      "ds_read2st64_b32 v[20:21], v244 offset0:0 offset1:1\n"
      "v_bfe_u32 v248, v243, 1, 3\n"
      "v_mfma_f32_16x16x32_bf16 v[108:111], v[160:163], a[12:15], 0\n"
      "ds_read2st64_b32 v[22:23], v245 offset0:4 offset1:5\n"
      "v_lshrrev_b32 v249, 4, v243\n"
      "v_mfma_f32_16x16x32_bf16 v[112:115], v[160:163], a[16:19], 0\n"
      "ds_read2st64_b32 v[24:25], v244 offset0:2 offset1:3\n"
      "v_lshlrev_b32 v249, 1, v249\n"
      "v_mfma_f32_16x16x32_bf16 v[116:119], v[160:163], a[20:23], 0\n"
      "ds_read2st64_b32 v[26:27], v245 offset0:6 offset1:7\n"
      "v_sub_u32 v248, v248, v249\n"
      "v_mfma_f32_16x16x32_bf16 v[120:123], v[160:163], a[24:27], 0\n"
      "ds_read2st64_b32 v[28:29], v246 offset0:8 offset1:9\n"
      "v_and_b32 v249, 16, v242\n"
      "v_mfma_f32_16x16x32_bf16 v[124:127], v[160:163], a[28:31], 0\n"
      "global_load_dwordx4 v[160:163], v240, s[10:11] offset:1024 sc1\n"
      "v_mov_b32 v254, 0x3f80\n"
      "v_mfma_f32_16x16x32_bf16 v[96:99], v[164:167], a[32:35], v[96:99]\n"
      "ds_read2st64_b32 v[30:31], v246 offset0:10 offset1:11\n"
      "v_lshlrev_b32 v249, v249, v254\n"
      "v_mfma_f32_16x16x32_bf16 v[100:103], v[164:167], a[36:39], v[100:103]\n"
      "ds_read2st64_b32 v[52:53], v244 offset0:12 offset1:13\n"
      "ds_read2st64_b32 v[54:55], v245 offset0:16 offset1:17\n"
      "v_mfma_f32_16x16x32_bf16 v[104:107], v[164:167], a[40:43], v[104:107]\n"
      "ds_read2st64_b32 v[56:57], v244 offset0:14 offset1:15\n"
      "ds_read2st64_b32 v[58:59], v245 offset0:18 offset1:19\n"
      "v_mfma_f32_16x16x32_bf16 v[108:111], v[164:167], a[44:47], v[108:111]\n"
      "ds_read2st64_b32 v[60:61], v246 offset0:20 offset1:21\n"
      "ds_read2st64_b32 v[62:63], v246 offset0:22 offset1:23\n"
      "v_mfma_f32_16x16x32_bf16 v[112:115], v[164:167], a[48:51], v[112:115]\n"
      "v_cmp_eq_u32 vcc, 0, v248\n"
      "s_nop 1\n"
      "v_cndmask_b32 v250, 0, v249, vcc\n"
      "v_mfma_f32_16x16x32_bf16 v[116:119], v[164:167], a[52:55], v[116:119]\n"
      "v_cmp_eq_u32 vcc, 1, v248\n"
      "s_nop 1\n"
      "v_cndmask_b32 v251, 0, v249, vcc\n"
      "v_mfma_f32_16x16x32_bf16 v[120:123], v[164:167], a[56:59], v[120:123]\n"
      "v_cvt_pk_bf16_f32 v72, v72, v73\n"
      "v_cvt_pk_bf16_f32 v73, v74, v75\n"
      "v_mfma_f32_16x16x32_bf16 v[124:127], v[164:167], a[60:63], v[124:127]\n"
      "s_waitcnt vmcnt(13)\n"
      "global_load_dwordx4 v[164:167], v240, s[72:73] offset:1024 sc1\n"
      "v_mov_b32 v252, v250\n"
      "v_mfma_f32_16x16x32_bf16 v[96:99], v[168:171], a[64:67], v[96:99]\n"
      "ds_write_addtid_b32 v72 offset:6144\n"
      "v_cvt_pk_bf16_f32 v74, v76, v77\n"
      "v_mfma_f32_16x16x32_bf16 v[100:103], v[168:171], a[68:71], v[100:103]\n"
      "ds_write_addtid_b32 v73 offset:6400\n"
      "v_cvt_pk_bf16_f32 v75, v78, v79\n"
      "v_mfma_f32_16x16x32_bf16 v[104:107], v[168:171], a[72:75], v[104:107]\n"
      "ds_write_addtid_b32 v74 offset:6656\n"
      "v_cvt_pk_bf16_f32 v76, v80, v81\n"
      "v_mfma_f32_16x16x32_bf16 v[108:111], v[168:171], a[76:79], v[108:111]\n"
      "ds_write_addtid_b32 v75 offset:6912\n"
      "v_cvt_pk_bf16_f32 v77, v82, v83\n"
      "v_mfma_f32_16x16x32_bf16 v[112:115], v[168:171], a[80:83], v[112:115]\n"
      "ds_write_addtid_b32 v76 offset:7168\n"
      "v_cvt_pk_bf16_f32 v78, v84, v85\n"
      "v_mfma_f32_16x16x32_bf16 v[116:119], v[168:171], a[84:87], v[116:119]\n"
      "ds_write_addtid_b32 v77 offset:7424\n"
      "v_cvt_pk_bf16_f32 v79, v86, v87\n"
      "v_mfma_f32_16x16x32_bf16 v[120:123], v[168:171], a[88:91], v[120:123]\n"
      "ds_write_addtid_b32 v78 offset:7680\n"
      "v_cvt_pk_bf16_f32 v80, v88, v89\n"
      "v_mfma_f32_16x16x32_bf16 v[124:127], v[168:171], a[92:95], v[124:127]\n"
      "global_load_dwordx4 v[168:171], v240, s[14:15] offset:1024 sc1\n"
      "v_mov_b32 v253, v251\n"
      "v_mfma_f32_16x16x32_bf16 v[96:99], v[172:175], a[96:99], v[96:99]\n"
      "ds_write_addtid_b32 v79 offset:7936\n"
      "v_cvt_pk_bf16_f32 v81, v90, v91\n"
      "v_mfma_f32_16x16x32_bf16 v[100:103], v[172:175], a[100:103], v[100:103]\n"
      "ds_write_addtid_b32 v80 offset:8192\n"
      "v_cvt_pk_bf16_f32 v82, v92, v93\n"
      "v_mfma_f32_16x16x32_bf16 v[104:107], v[172:175], a[104:107], v[104:107]\n"
      "ds_write_addtid_b32 v81 offset:8448\n"
      "v_cvt_pk_bf16_f32 v83, v94, v95\n"
      "v_mfma_f32_16x16x32_bf16 v[108:111], v[172:175], a[108:111], v[108:111]\n"
      "ds_write_addtid_b32 v82 offset:8704\n"
      "v_mov_b32 v248, 0\n"
      "v_mfma_f32_16x16x32_bf16 v[112:115], v[172:175], a[112:115], v[112:115]\n"
      "ds_write_addtid_b32 v83 offset:8960\n"
      "v_mov_b32 v249, 0\n"
      "v_mfma_f32_16x16x32_bf16 v[116:119], v[172:175], a[116:119], v[116:119]\n"
      "v_mov_b32 v254, 0\n"
      "v_mov_b32 v255, 0\n"
      "v_mfma_f32_16x16x32_bf16 v[120:123], v[172:175], a[120:123], v[120:123]\n"
      "s_add_u32 s48, s34, 1\n"
      "s_lshr_b32 s43, s42, 5\n"
      "s_lshl2_add_u32 s44, s36, s33\n"
      "s_cmp_eq_u32 s43, s44\n"
      "s_cbranch_scc0 .Lp_e10_%=\n"
      "s_lshl_b32 s43, s42, 10\n"
      "v_add_u32 v197, s43, v242\n"
      "global_load_dwordx4 v[232:235], v197, s[16:17]\n"
      "global_load_dwordx4 v[236:239], v242, s[24:25]\n"
      ".Lp_e10_%=:\n"
      "v_mfma_f32_16x16x32_bf16 v[124:127], v[172:175], a[124:127], v[124:127]\n"
      "s_waitcnt vmcnt(13)\n"
      "global_load_dwordx4 v[172:175], v240, s[74:75] offset:1024 sc1\n"
      "s_and_b32 s48, s48, 3\n"
      "v_mfma_f32_16x16x32_bf16 v[96:99], v[176:179], a[128:131], v[96:99]\n"
      "s_mul_i32 s48, s48, 36864\n"
      "s_add_u32 s48, s48, 0\n"
      "s_add_u32 s52, s60, s48\n"
      "s_addc_u32 s53, s61, 0\n"
      "v_mfma_f32_16x16x32_bf16 v[100:103], v[176:179], a[132:135], v[100:103]\n"
      "s_waitcnt lgkmcnt(15)\n"
      "v_mfma_f32_16x16x32_bf16 v[0:3], v[250:253], v[20:23], v[0:3]\n"
      "s_lshl2_add_u32 s54, s36, s33\n"
      "v_mfma_f32_16x16x32_bf16 v[104:107], v[176:179], a[136:139], v[104:107]\n"
      "s_lshl_b32 s55, s54, 15\n"
      "v_mfma_f32_16x16x32_bf16 v[4:7], v[250:253], v[24:27], v[4:7]\n"
      "s_lshl2_add_u32 s56, s35, s34\n"
      "v_mfma_f32_16x16x32_bf16 v[108:111], v[176:179], a[140:143], v[108:111]\n"
      "s_lshl_b32 s56, s56, 6\n"
      "v_mfma_f32_16x16x32_bf16 v[0:3], v[252:255], v[28:31], v[0:3]\n"
      "s_add_u32 s55, s55, s56\n"
      "v_mfma_f32_16x16x32_bf16 v[112:115], v[176:179], a[144:147], v[112:115]\n"
      "v_mfma_f32_16x16x32_bf16 v[4:7], v[248:251], v[28:31], v[4:7]\n"
      "v_mov_b32 v11, s37\n"
      "v_mfma_f32_16x16x32_bf16 v[116:119], v[176:179], a[148:151], v[116:119]\n"
      "v_and_b32 v198, 15, v243\n"
      "v_and_b32 v199, 48, v243\n"
      "v_mfma_f32_16x16x32_bf16 v[120:123], v[176:179], a[152:155], v[120:123]\n"
      "v_cvt_pk_bf16_f32 v8, v0, v1\n"
      "v_cvt_pk_bf16_f32 v9, v2, v3\n"
      "v_mfma_f32_16x16x32_bf16 v[124:127], v[176:179], a[156:159], v[124:127]\n"
      "global_load_dwordx4 v[176:179], v241, s[10:11] offset:1024 sc1\n"
      "v_cvt_pk_bf16_f32 v10, v4, v5\n"
      "v_mfma_f32_16x16x32_bf16 v[96:99], v[180:183], a[160:163], v[96:99]\n"
      "v_cvt_pk_bf16_f32 v247, v6, v7\n"
      "v_lshl_or_b32 v199, v198, 10, v199\n"
      "v_mfma_f32_16x16x32_bf16 v[100:103], v[180:183], a[164:167], v[100:103]\n"
      "global_store_dwordx4 v242, v[8:11], s[52:53] offset:0 sc1\n"
      "s_waitcnt lgkmcnt(12)\n"
      "v_mfma_f32_16x16x32_bf16 v[104:107], v[180:183], a[168:171], v[104:107]\n"
      "v_mfma_f32_16x16x32_bf16 v[32:35], v[250:253], v[52:55], v[32:35]\n"
      "v_add_u32 v199, s55, v199\n"
      "v_mfma_f32_16x16x32_bf16 v[108:111], v[180:183], a[172:175], v[108:111]\n"
      "v_mfma_f32_16x16x32_bf16 v[36:39], v[250:253], v[56:59], v[36:39]\n"
      "v_add_u32 v197, 16384, v199\n"
      "v_mfma_f32_16x16x32_bf16 v[112:115], v[180:183], a[176:179], v[112:115]\n"
      "v_mfma_f32_16x16x32_bf16 v[32:35], v[252:255], v[60:63], v[32:35]\n"
      "v_mfma_f32_16x16x32_bf16 v[116:119], v[180:183], a[180:183], v[116:119]\n"
      "v_mfma_f32_16x16x32_bf16 v[36:39], v[248:251], v[60:63], v[36:39]\n"
      "v_mov_b32 v43, s37\n"
      "v_mfma_f32_16x16x32_bf16 v[120:123], v[180:183], a[184:187], v[120:123]\n"
      "global_load_dwordx4 v[224:227], v199, s[16:17]\n"
      "v_mov_b32 v44, v247\n"
      "v_mfma_f32_16x16x32_bf16 v[124:127], v[180:183], a[188:191], v[124:127]\n"
      "s_waitcnt vmcnt(15)\n"
      "global_load_dwordx4 v[180:183], v241, s[72:73] offset:1024 sc1\n"
      "v_mov_b32 v47, s37\n"
      "v_mfma_f32_16x16x32_bf16 v[96:99], v[184:187], a[192:195], v[96:99]\n"
      "global_load_dwordx4 v[228:231], v197, s[16:17]\n"
      "s_lshr_b32 s55, s34, 1\n"
      "v_mfma_f32_16x16x32_bf16 v[100:103], v[184:187], a[196:199], v[100:103]\n"
      "s_lshl2_add_u32 s55, s55, s35\n"
      "v_cvt_pk_bf16_f32 v40, v32, v33\n"
      "v_cvt_pk_bf16_f32 v41, v34, v35\n"
      "v_mfma_f32_16x16x32_bf16 v[104:107], v[184:187], a[200:203], v[104:107]\n"
      "s_lshl_b32 s55, s55, 15\n"
      "v_cvt_pk_bf16_f32 v42, v36, v37\n"
      "v_cvt_pk_bf16_f32 v45, v38, v39\n"
      "v_mfma_f32_16x16x32_bf16 v[108:111], v[184:187], a[204:207], v[108:111]\n"
      "s_lshl_b32 s54, s54, 9\n"
      "global_store_dwordx4 v242, v[40:43], s[52:53] offset:1024 sc1\n"
      "s_add_u32 s54, s54, s55\n"
      "v_mfma_f32_16x16x32_bf16 v[112:115], v[184:187], a[208:211], v[112:115]\n"
      "s_add_u32 s54, s54, 786432\n"
      "global_store_dwordx4 v242, v[44:47], s[52:53] offset:2048 sc1\n"
      "s_add_u32 s48, s34, 2\n"
      "v_mfma_f32_16x16x32_bf16 v[116:119], v[184:187], a[212:215], v[116:119]\n"
      "s_and_b32 s48, s48, 3\n"
      "s_mul_i32 s48, s48, 36864\n"
      "s_add_u32 s48, s48, 12288\n"
      "v_mfma_f32_16x16x32_bf16 v[120:123], v[184:187], a[216:219], v[120:123]\n"
      "s_add_u32 s52, s60, s48\n"
      "s_addc_u32 s53, s61, 0\n"
      "v_lshl_add_u32 v198, v198, 4, s54\n"
      "v_mfma_f32_16x16x32_bf16 v[124:127], v[184:187], a[220:223], v[124:127]\n"
      "global_load_dwordx4 v[184:187], v241, s[14:15] offset:1024 sc1\n"
      "v_mfma_f32_16x16x32_bf16 v[96:99], v[188:191], a[224:227], v[96:99]\n"
      "global_load_dwordx2 v[204:205], v198, s[58:59] offset:0 sc1\n"
      "v_mfma_f32_16x16x32_bf16 v[100:103], v[188:191], a[228:231], v[100:103]\n"
      "global_load_dwordx2 v[206:207], v198, s[58:59] offset:256 sc1\n"
      "s_add_u32 s68, s20, 16384\n"
      "s_addc_u32 s69, s21, 0\n"
      "v_mfma_f32_16x16x32_bf16 v[104:107], v[188:191], a[232:235], v[104:107]\n"
      "s_ashr_i32 s54, s40, 31\n"
      "s_not_b32 s54, s54\n"
      "s_or_b32 s54, s54, 0x80000000\n"
      "v_mfma_f32_16x16x32_bf16 v[108:111], v[188:191], a[236:239], v[108:111]\n"
      "s_xor_b32 s54, s54, s40\n"
      "s_and_b32 s54, s54, 0xffff0000\n"
      "v_mov_b32 v192, s54\n"
      "v_mfma_f32_16x16x32_bf16 v[112:115], v[188:191], a[240:243], v[112:115]\n"
      "v_mul_f32 v192, 0xbfb8aa3b, v192\n"
      "v_exp_f32 v192, v192\n"
      "v_mfma_f32_16x16x32_bf16 v[116:119], v[188:191], a[244:247], v[116:119]\n"
      "v_add_f32 v192, 1.0, v192\n"
      "v_rcp_f32 v192, v192\n"
      "v_mfma_f32_16x16x32_bf16 v[120:123], v[188:191], a[248:251], v[120:123]\n"
      "v_cvt_pk_bf16_f32 v192, v192, v192\n"
      "v_and_b32 v192, 0xffff0000, v192\n"
      "v_mfma_f32_16x16x32_bf16 v[124:127], v[188:191], a[252:255], v[124:127]\n"
      "s_waitcnt vmcnt(20)\n"
      "global_load_dwordx4 v[188:191], v241, s[74:75] offset:1024 sc1\n"
      "v_readfirstlane_b32 s84, v192\n"
      "v_mfma_f32_16x16x32_bf16 v[0:3], v[128:131], a[0:3], 0\n"
      "v_mfma_f32_16x16x32_bf16 v[4:7], v[128:131], a[4:7], 0\n"
      "v_cvt_pk_bf16_f32 v104, v104, v105\n"
      "v_cvt_pk_bf16_f32 v105, v106, v107\n"
      "v_mfma_f32_16x16x32_bf16 v[8:11], v[128:131], a[8:11], 0\n"
      "ds_write_addtid_b32 v104 offset:9216\n"
      "v_cvt_pk_bf16_f32 v106, v108, v109\n"
      "v_mfma_f32_16x16x32_bf16 v[12:15], v[128:131], a[12:15], 0\n"
      "ds_write_addtid_b32 v105 offset:9472\n"
      "v_cvt_pk_bf16_f32 v107, v110, v111\n"
      "v_mfma_f32_16x16x32_bf16 v[16:19], v[128:131], a[16:19], 0\n"
      "ds_write_addtid_b32 v106 offset:9728\n"
      "v_cvt_pk_bf16_f32 v108, v112, v113\n"
      "v_mfma_f32_16x16x32_bf16 v[20:23], v[128:131], a[20:23], 0\n"
      "ds_write_addtid_b32 v107 offset:9984\n"
      "v_cvt_pk_bf16_f32 v109, v114, v115\n"
      "v_mfma_f32_16x16x32_bf16 v[24:27], v[128:131], a[24:27], 0\n"
      "ds_write_addtid_b32 v108 offset:10240\n"
      "v_cvt_pk_bf16_f32 v110, v116, v117\n"
      "v_mfma_f32_16x16x32_bf16 v[28:31], v[128:131], a[28:31], 0\n"
      "global_load_dwordx4 v[128:131], v240, s[10:11] offset:2048 sc1\n"
      "v_mfma_f32_16x16x32_bf16 v[0:3], v[132:135], a[32:35], v[0:3]\n"
      "ds_write_addtid_b32 v109 offset:10496\n"
      "v_cvt_pk_bf16_f32 v111, v118, v119\n"
      "v_mfma_f32_16x16x32_bf16 v[4:7], v[132:135], a[36:39], v[4:7]\n"
      "ds_write_addtid_b32 v110 offset:10752\n"
      "v_cvt_pk_bf16_f32 v112, v120, v121\n"
      "v_mfma_f32_16x16x32_bf16 v[8:11], v[132:135], a[40:43], v[8:11]\n"
      "ds_write_addtid_b32 v111 offset:11008\n"
      "v_cvt_pk_bf16_f32 v113, v122, v123\n"
      "v_mfma_f32_16x16x32_bf16 v[12:15], v[132:135], a[44:47], v[12:15]\n"
      "ds_write_addtid_b32 v112 offset:11264\n"
      "v_cvt_pk_bf16_f32 v114, v124, v125\n"
      "v_mfma_f32_16x16x32_bf16 v[16:19], v[132:135], a[48:51], v[16:19]\n"
      "ds_write_addtid_b32 v113 offset:11520\n"
      "v_cvt_pk_bf16_f32 v115, v126, v127\n"
      "v_mfma_f32_16x16x32_bf16 v[20:23], v[132:135], a[52:55], v[20:23]\n"
      "ds_write_addtid_b32 v114 offset:11776\n"
      "v_mfma_f32_16x16x32_bf16 v[24:27], v[132:135], a[56:59], v[24:27]\n"
      "ds_write_addtid_b32 v115 offset:12032\n"
      "v_mfma_f32_16x16x32_bf16 v[28:31], v[132:135], a[60:63], v[28:31]\n"
      "s_waitcnt vmcnt(20)\n"
      "global_load_dwordx4 v[132:135], v240, s[72:73] offset:2048 sc1\n"
      "v_mfma_f32_16x16x32_bf16 v[0:3], v[136:139], a[64:67], v[0:3]\n"
      "v_mfma_f32_16x16x32_bf16 v[4:7], v[136:139], a[68:71], v[4:7]\n"
      "s_waitcnt lgkmcnt(0)\n"
      "s_barrier\n"
      "v_mfma_f32_16x16x32_bf16 v[8:11], v[136:139], a[72:75], v[8:11]\n"
      "ds_read2st64_b32 v[84:85], v244 offset0:24 offset1:25\n"
      "v_mfma_f32_16x16x32_bf16 v[12:15], v[136:139], a[76:79], v[12:15]\n"
      "ds_read2st64_b32 v[86:87], v245 offset0:28 offset1:29\n"
      "v_mfma_f32_16x16x32_bf16 v[16:19], v[136:139], a[80:83], v[16:19]\n"
      "ds_read2st64_b32 v[88:89], v244 offset0:26 offset1:27\n"
      "v_mfma_f32_16x16x32_bf16 v[20:23], v[136:139], a[84:87], v[20:23]\n"
      "ds_read2st64_b32 v[90:91], v245 offset0:30 offset1:31\n"
      "v_mfma_f32_16x16x32_bf16 v[24:27], v[136:139], a[88:91], v[24:27]\n"
      "ds_read2st64_b32 v[92:93], v246 offset0:32 offset1:33\n"
      "v_mfma_f32_16x16x32_bf16 v[28:31], v[136:139], a[92:95], v[28:31]\n"
      "global_load_dwordx4 v[136:139], v240, s[14:15] offset:2048 sc1\n"
      "v_mfma_f32_16x16x32_bf16 v[0:3], v[140:143], a[96:99], v[0:3]\n"
      "ds_read2st64_b32 v[94:95], v246 offset0:34 offset1:35\n"
      "v_mfma_f32_16x16x32_bf16 v[4:7], v[140:143], a[100:103], v[4:7]\n"
      "ds_read2st64_b32 v[116:117], v244 offset0:36 offset1:37\n"
      "v_mfma_f32_16x16x32_bf16 v[8:11], v[140:143], a[104:107], v[8:11]\n"
      "ds_read2st64_b32 v[118:119], v245 offset0:40 offset1:41\n"
      "v_mfma_f32_16x16x32_bf16 v[12:15], v[140:143], a[108:111], v[12:15]\n"
      "ds_read2st64_b32 v[120:121], v244 offset0:38 offset1:39\n"
      "v_mfma_f32_16x16x32_bf16 v[16:19], v[140:143], a[112:115], v[16:19]\n"
      "ds_read2st64_b32 v[122:123], v245 offset0:42 offset1:43\n"
      "v_mfma_f32_16x16x32_bf16 v[20:23], v[140:143], a[116:119], v[20:23]\n"
      "s_waitcnt lgkmcnt(4)\n"
      "v_mfma_f32_16x16x32_bf16 v[64:67], v[250:253], v[84:87], v[64:67]\n"
      "v_mfma_f32_16x16x32_bf16 v[24:27], v[140:143], a[120:123], v[24:27]\n"
      "v_mfma_f32_16x16x32_bf16 v[68:71], v[250:253], v[88:91], v[68:71]\n"
      "v_mfma_f32_16x16x32_bf16 v[28:31], v[140:143], a[124:127], v[28:31]\n"
      "s_waitcnt vmcnt(20)\n"
      "global_load_dwordx4 v[140:143], v240, s[74:75] offset:2048 sc1\n"
      "v_mfma_f32_16x16x32_bf16 v[0:3], v[144:147], a[128:131], v[0:3]\n"
      "v_mfma_f32_16x16x32_bf16 v[64:67], v[252:255], v[92:95], v[64:67]\n"
      "v_mfma_f32_16x16x32_bf16 v[4:7], v[144:147], a[132:135], v[4:7]\n"
      "v_mfma_f32_16x16x32_bf16 v[68:71], v[248:251], v[92:95], v[68:71]\n"
      "v_mov_b32 v75, s37\n"
      "v_mfma_f32_16x16x32_bf16 v[8:11], v[144:147], a[136:139], v[8:11]\n"
      "ds_read2st64_b32 v[124:125], v246 offset0:44 offset1:45\n"
      "v_mfma_f32_16x16x32_bf16 v[12:15], v[144:147], a[140:143], v[12:15]\n"
      "ds_read2st64_b32 v[126:127], v246 offset0:46 offset1:47\n"
      "s_nop 0\n"
      "v_cvt_pk_bf16_f32 v72, v64, v65\n"
      "v_mfma_f32_16x16x32_bf16 v[16:19], v[144:147], a[144:147], v[16:19]\n"
      "v_cvt_pk_bf16_f32 v73, v66, v67\n"
      "v_cvt_pk_bf16_f32 v74, v68, v69\n"
      "v_mfma_f32_16x16x32_bf16 v[20:23], v[144:147], a[148:151], v[20:23]\n"
      "v_cvt_pk_bf16_f32 v247, v70, v71\n"
      "v_mfma_f32_16x16x32_bf16 v[24:27], v[144:147], a[152:155], v[24:27]\n"
      "global_store_dwordx4 v242, v[72:75], s[52:53] offset:0 sc1\n"
      "v_mfma_f32_16x16x32_bf16 v[28:31], v[144:147], a[156:159], v[28:31]\n"
      "global_load_dwordx4 v[144:147], v241, s[10:11] offset:2048 sc1\n"
      "v_mfma_f32_16x16x32_bf16 v[0:3], v[148:151], a[160:163], v[0:3]\n"
      "s_waitcnt lgkmcnt(0)\n"
      "v_mfma_f32_16x16x32_bf16 v[96:99], v[250:253], v[116:119], v[96:99]\n"
      "v_mfma_f32_16x16x32_bf16 v[4:7], v[148:151], a[164:167], v[4:7]\n"
      "v_mfma_f32_16x16x32_bf16 v[100:103], v[250:253], v[120:123], v[100:103]\n"
      "v_mfma_f32_16x16x32_bf16 v[8:11], v[148:151], a[168:171], v[8:11]\n"
      "v_mfma_f32_16x16x32_bf16 v[96:99], v[252:255], v[124:127], v[96:99]\n"
      "v_mfma_f32_16x16x32_bf16 v[12:15], v[148:151], a[172:175], v[12:15]\n"
      "v_mfma_f32_16x16x32_bf16 v[100:103], v[248:251], v[124:127], v[100:103]\n"
      "v_mov_b32 v107, s37\n"
      "v_mfma_f32_16x16x32_bf16 v[16:19], v[148:151], a[176:179], v[16:19]\n"
      "v_mov_b32 v108, v247\n"
      "v_mov_b32 v111, s37\n"
      "v_mfma_f32_16x16x32_bf16 v[20:23], v[148:151], a[180:183], v[20:23]\n"
      "v_mfma_f32_16x16x32_bf16 v[24:27], v[148:151], a[184:187], v[24:27]\n"
      "v_cvt_pk_bf16_f32 v104, v96, v97\n"
      "v_cvt_pk_bf16_f32 v105, v98, v99\n"
      "v_mfma_f32_16x16x32_bf16 v[28:31], v[148:151], a[188:191], v[28:31]\n"
      "s_waitcnt vmcnt(21)\n"
      "global_load_dwordx4 v[148:151], v241, s[72:73] offset:2048 sc1\n"
      "v_cvt_pk_bf16_f32 v106, v100, v101\n"
      "v_mfma_f32_16x16x32_bf16 v[0:3], v[152:155], a[192:195], v[0:3]\n"
      "v_cvt_pk_bf16_f32 v109, v102, v103\n"
      "v_mfma_f32_16x16x32_bf16 v[4:7], v[152:155], a[196:199], v[4:7]\n"
      "global_store_dwordx4 v242, v[104:107], s[52:53] offset:1024 sc1\n"
      "v_mfma_f32_16x16x32_bf16 v[8:11], v[152:155], a[200:203], v[8:11]\n"
      "global_store_dwordx4 v242, v[108:111], s[52:53] offset:2048 sc1\n"
      "s_add_u32 s48, s34, 3\n"
      "v_mfma_f32_16x16x32_bf16 v[12:15], v[152:155], a[204:207], v[12:15]\n"
      "s_and_b32 s48, s48, 3\n"
      "s_waitcnt vmcnt(10)\n"
      "v_bitop3_b32 v197, v205, v207, s37 bitop3:0x7e\n"
      "v_cmp_ne_u32 vcc, 0, v197\n"
      "s_nop 1\n"
      "s_cbranch_vccnz .Lp_s12_%=\n"
      ".Lp_o13_%=:\n"
      "v_mfma_f32_16x16x32_bf16 v[16:19], v[152:155], a[208:211], v[16:19]\n"
      "s_mul_i32 s48, s48, 36864\n"
      "s_lshr_b32 s43, s42, 5\n"
      "s_lshl2_add_u32 s44, s36, s33\n"
      "s_cmp_eq_u32 s43, s44\n"
      "s_cbranch_scc0 .Lp_e14_%=\n"
      "v_lshlrev_b32 v192, 16, v232\n"
      "v_lshlrev_b32 v193, 16, v236\n"
      "v_and_b32 v194, 0xffff0000, v232\n"
      "v_and_b32 v195, 0xffff0000, v236\n"
      "v_mul_f32 v192, v192, v193\n"
      "v_mul_f32 v194, v194, v195\n"
      "v_cvt_pk_bf16_f32 v192, v192, v194\n"
      "v_lshlrev_b32 v193, 16, v192\n"
      "v_and_b32 v192, 0xffff0000, v192\n"
      "v_add_f32 v196, v192, v193\n"
      "v_lshlrev_b32 v192, 16, v233\n"
      "v_lshlrev_b32 v193, 16, v237\n"
      "v_and_b32 v194, 0xffff0000, v233\n"
      "v_and_b32 v195, 0xffff0000, v237\n"
      "v_mul_f32 v192, v192, v193\n"
      "v_mul_f32 v194, v194, v195\n"
      "v_cvt_pk_bf16_f32 v192, v192, v194\n"
      "v_lshlrev_b32 v193, 16, v192\n"
      "v_and_b32 v192, 0xffff0000, v192\n"
      "v_add_f32 v192, v192, v193\n"
      "v_add_f32 v196, v196, v192\n"
      "v_lshlrev_b32 v192, 16, v234\n"
      "v_lshlrev_b32 v193, 16, v238\n"
      "v_and_b32 v194, 0xffff0000, v234\n"
      "v_and_b32 v195, 0xffff0000, v238\n"
      "v_mul_f32 v192, v192, v193\n"
      "v_mul_f32 v194, v194, v195\n"
      "v_cvt_pk_bf16_f32 v192, v192, v194\n"
      "v_lshlrev_b32 v193, 16, v192\n"
      "v_and_b32 v192, 0xffff0000, v192\n"
      "v_add_f32 v192, v192, v193\n"
      "v_add_f32 v196, v196, v192\n"
      "v_lshlrev_b32 v192, 16, v235\n"
      "v_lshlrev_b32 v193, 16, v239\n"
      "v_and_b32 v194, 0xffff0000, v235\n"
      "v_and_b32 v195, 0xffff0000, v239\n"
      "v_mul_f32 v192, v192, v193\n"
      "v_mul_f32 v194, v194, v195\n"
      "v_cvt_pk_bf16_f32 v192, v192, v194\n"
      "v_lshlrev_b32 v193, 16, v192\n"
      "v_and_b32 v192, 0xffff0000, v192\n"
      "v_add_f32 v192, v192, v193\n"
      "v_add_f32 v196, v196, v192\n"
      "s_nop 1\n"
      "v_add_f32_dpp v196, v196, v196 quad_perm:[1,0,3,2] row_mask:0xf bank_mask:0xf\n"
      "s_nop 1\n"
      "v_add_f32_dpp v196, v196, v196 quad_perm:[2,3,0,1] row_mask:0xf bank_mask:0xf\n"
      "s_nop 1\n"
      "v_add_f32_dpp v196, v196, v196 row_half_mirror row_mask:0xf bank_mask:0xf\n"
      "s_nop 1\n"
      "v_add_f32_dpp v196, v196, v196 row_mirror row_mask:0xf bank_mask:0xf\n"
      "v_mov_b32 v192, v196\n"
      "s_nop 1\n"
      "v_permlane16_swap_b32 v196, v192\n"
      "v_add_f32 v196, v196, v192\n"
      "v_mov_b32 v192, v196\n"
      "s_nop 1\n"
      "v_permlane32_swap_b32 v196, v192\n"
      "v_add_f32 v196, v196, v192\n"
      "v_cvt_pk_bf16_f32 v196, v196, v196\n"
      "v_and_b32 v196, 0xffff0000, v196\n"
      "v_mul_f32 v196, 0xbfb8aa3b, v196\n"
      "v_exp_f32 v196, v196\n"
      "v_lshrrev_b32 v192, 10, v199\n"
      "v_add_f32 v196, 1.0, v196\n"
      "v_rcp_f32 v196, v196\n"
      "v_subrev_u32 v192, s42, v192\n"
      "v_cvt_pk_bf16_f32 v196, v196, v196\n"
      "v_and_b32 v196, 0xffff0000, v196\n"
      "v_cmp_eq_u32 vcc, 0, v192\n"
      "s_nop 1\n"
      "v_cndmask_b32 v204, v204, v196, vcc\n"
      "v_cmp_eq_u32 vcc, -16, v192\n"
      "s_nop 1\n"
      "v_cndmask_b32 v206, v206, v196, vcc\n"
      ".Lp_e14_%=:\n"
      "v_mfma_f32_16x16x32_bf16 v[20:23], v[152:155], a[212:215], v[20:23]\n"
      "s_add_u32 s48, s48, 24576\n"
      "s_add_u32 s52, s60, s48\n"
      "s_addc_u32 s53, s61, 0\n"
      "v_mfma_f32_16x16x32_bf16 v[24:27], v[152:155], a[216:219], v[24:27]\n"
      "v_sub_f32 v205, 1.0, v204\n"
      "v_cvt_pk_bf16_f32 v205, v205, v205\n"
      "v_mfma_f32_16x16x32_bf16 v[28:31], v[152:155], a[220:223], v[28:31]\n"
      "global_load_dwordx4 v[152:155], v241, s[14:15] offset:2048 sc1\n"
      "v_and_b32 v205, 0xffff0000, v205\n"
      "v_mfma_f32_16x16x32_bf16 v[0:3], v[156:159], a[224:227], v[0:3]\n"
      "v_lshlrev_b32 v208, 16, v224\n"
      "v_and_b32 v209, 0xffff0000, v224\n"
      "v_mfma_f32_16x16x32_bf16 v[4:7], v[156:159], a[228:231], v[4:7]\n"
      "v_mul_f32 v208, v205, v208\n"
      "v_mul_f32 v209, v205, v209\n"
      "v_mfma_f32_16x16x32_bf16 v[8:11], v[156:159], a[232:235], v[8:11]\n"
      "v_lshlrev_b32 v210, 16, v225\n"
      "v_and_b32 v211, 0xffff0000, v225\n"
      "v_mfma_f32_16x16x32_bf16 v[12:15], v[156:159], a[236:239], v[12:15]\n"
      "v_mul_f32 v210, v205, v210\n"
      "v_mul_f32 v211, v205, v211\n"
      "v_mfma_f32_16x16x32_bf16 v[16:19], v[156:159], a[240:243], v[16:19]\n"
      "v_lshlrev_b32 v216, 16, v226\n"
      "v_and_b32 v217, 0xffff0000, v226\n"
      "v_mfma_f32_16x16x32_bf16 v[20:23], v[156:159], a[244:247], v[20:23]\n"
      "v_mul_f32 v216, v205, v216\n"
      "v_mul_f32 v217, v205, v217\n"
      "v_mfma_f32_16x16x32_bf16 v[24:27], v[156:159], a[248:251], v[24:27]\n"
      "v_lshlrev_b32 v218, 16, v227\n"
      "v_and_b32 v219, 0xffff0000, v227\n"
      "v_mfma_f32_16x16x32_bf16 v[28:31], v[156:159], a[252:255], v[28:31]\n"
      "global_load_dwordx4 v[156:159], v241, s[74:75] offset:2048 sc1\n"
      "v_mul_f32 v218, v205, v218\n"
      "v_mfma_f32_16x16x32_bf16 v[32:35], v[160:163], a[0:3], 0\n"
      "v_mul_f32 v219, v205, v219\n"
      "v_sub_f32 v207, 1.0, v206\n"
      "v_mfma_f32_16x16x32_bf16 v[36:39], v[160:163], a[4:7], 0\n"
      "v_cvt_pk_bf16_f32 v8, v8, v9\n"
      "v_cvt_pk_bf16_f32 v9, v10, v11\n"
      "v_mfma_f32_16x16x32_bf16 v[40:43], v[160:163], a[8:11], 0\n"
      "ds_write_addtid_b32 v8 offset:0\n"
      "v_cvt_pk_bf16_f32 v10, v12, v13\n"
      "v_mfma_f32_16x16x32_bf16 v[44:47], v[160:163], a[12:15], 0\n"
      "ds_write_addtid_b32 v9 offset:256\n"
      "v_cvt_pk_bf16_f32 v11, v14, v15\n"
      "v_mfma_f32_16x16x32_bf16 v[48:51], v[160:163], a[16:19], 0\n"
      "ds_write_addtid_b32 v10 offset:512\n"
      "v_cvt_pk_bf16_f32 v12, v16, v17\n"
      "v_mfma_f32_16x16x32_bf16 v[52:55], v[160:163], a[20:23], 0\n"
      "ds_write_addtid_b32 v11 offset:768\n"
      "v_cvt_pk_bf16_f32 v13, v18, v19\n"
      "v_mfma_f32_16x16x32_bf16 v[56:59], v[160:163], a[24:27], 0\n"
      "ds_write_addtid_b32 v12 offset:1024\n"
      "v_cvt_pk_bf16_f32 v14, v20, v21\n"
      "v_mfma_f32_16x16x32_bf16 v[60:63], v[160:163], a[28:31], 0\n"
      "global_load_dwordx4 v[160:163], v240, s[10:11] offset:3072 sc1\n"
      "v_cvt_pk_bf16_f32 v207, v207, v207\n"
      "v_mfma_f32_16x16x32_bf16 v[32:35], v[164:167], a[32:35], v[32:35]\n"
      "ds_write_addtid_b32 v13 offset:1280\n"
      "v_cvt_pk_bf16_f32 v15, v22, v23\n"
      "v_mfma_f32_16x16x32_bf16 v[36:39], v[164:167], a[36:39], v[36:39]\n"
      "ds_write_addtid_b32 v14 offset:1536\n"
      "v_cvt_pk_bf16_f32 v16, v24, v25\n"
      "v_mfma_f32_16x16x32_bf16 v[40:43], v[164:167], a[40:43], v[40:43]\n"
      "ds_write_addtid_b32 v15 offset:1792\n"
      "v_cvt_pk_bf16_f32 v17, v26, v27\n"
      "v_mfma_f32_16x16x32_bf16 v[44:47], v[164:167], a[44:47], v[44:47]\n"
      "ds_write_addtid_b32 v16 offset:2048\n"
      "v_cvt_pk_bf16_f32 v18, v28, v29\n"
      "v_mfma_f32_16x16x32_bf16 v[48:51], v[164:167], a[48:51], v[48:51]\n"
      "ds_write_addtid_b32 v17 offset:2304\n"
      "v_cvt_pk_bf16_f32 v19, v30, v31\n"
      "v_mfma_f32_16x16x32_bf16 v[52:55], v[164:167], a[52:55], v[52:55]\n"
      "ds_write_addtid_b32 v18 offset:2560\n"
      "v_and_b32 v207, 0xffff0000, v207\n"
      "v_mfma_f32_16x16x32_bf16 v[56:59], v[164:167], a[56:59], v[56:59]\n"
      "ds_write_addtid_b32 v19 offset:2816\n"
      "v_lshlrev_b32 v212, 16, v228\n"
      "v_mfma_f32_16x16x32_bf16 v[60:63], v[164:167], a[60:63], v[60:63]\n"
      "global_load_dwordx4 v[164:167], v240, s[72:73] offset:3072 sc1\n"
      "v_and_b32 v213, 0xffff0000, v228\n"
      "v_mfma_f32_16x16x32_bf16 v[32:35], v[168:171], a[64:67], v[32:35]\n"
      "v_mul_f32 v212, v207, v212\n"
      "v_mul_f32 v213, v207, v213\n"
      "v_mfma_f32_16x16x32_bf16 v[36:39], v[168:171], a[68:71], v[36:39]\n"
      "s_waitcnt lgkmcnt(0)\n"
      "s_barrier\n"
      "v_mfma_f32_16x16x32_bf16 v[40:43], v[168:171], a[72:75], v[40:43]\n"
      "ds_read2st64_b32 v[20:21], v244 offset0:0 offset1:1\n"
      "v_lshlrev_b32 v214, 16, v229\n"
      "v_mfma_f32_16x16x32_bf16 v[44:47], v[168:171], a[76:79], v[44:47]\n"
      "ds_read2st64_b32 v[22:23], v245 offset0:4 offset1:5\n"
      "v_and_b32 v215, 0xffff0000, v229\n"
      "v_mfma_f32_16x16x32_bf16 v[48:51], v[168:171], a[80:83], v[48:51]\n"
      "ds_read2st64_b32 v[24:25], v244 offset0:2 offset1:3\n"
      "v_mul_f32 v214, v207, v214\n"
      "v_mfma_f32_16x16x32_bf16 v[52:55], v[168:171], a[84:87], v[52:55]\n"
      "ds_read2st64_b32 v[26:27], v245 offset0:6 offset1:7\n"
      "v_mul_f32 v215, v207, v215\n"
      "v_mfma_f32_16x16x32_bf16 v[56:59], v[168:171], a[88:91], v[56:59]\n"
      "ds_read2st64_b32 v[28:29], v246 offset0:8 offset1:9\n"
      "v_lshlrev_b32 v220, 16, v230\n"
      "v_mfma_f32_16x16x32_bf16 v[60:63], v[168:171], a[92:95], v[60:63]\n"
      "global_load_dwordx4 v[168:171], v240, s[14:15] offset:3072 sc1\n"
      "v_and_b32 v221, 0xffff0000, v230\n"
      "v_mfma_f32_16x16x32_bf16 v[32:35], v[172:175], a[96:99], v[32:35]\n"
      "ds_read2st64_b32 v[30:31], v246 offset0:10 offset1:11\n"
      "v_mul_f32 v220, v207, v220\n"
      "v_mfma_f32_16x16x32_bf16 v[36:39], v[172:175], a[100:103], v[36:39]\n"
      "v_mul_f32 v221, v207, v221\n"
      "v_lshlrev_b32 v222, 16, v231\n"
      "v_mfma_f32_16x16x32_bf16 v[40:43], v[172:175], a[104:107], v[40:43]\n"
      "v_and_b32 v223, 0xffff0000, v231\n"
      "v_mul_f32 v222, v207, v222\n"
      "v_mfma_f32_16x16x32_bf16 v[44:47], v[172:175], a[108:111], v[44:47]\n"
      "v_mul_f32 v223, v207, v223\n"
      "v_mfma_f32_16x16x32_bf16 v[48:51], v[172:175], a[112:115], v[48:51]\n"
      "v_mfma_f32_16x16x32_bf16 v[52:55], v[172:175], a[116:119], v[52:55]\n"
      "s_waitcnt lgkmcnt(0)\n"
      "v_mfma_f32_16x16x32_bf16 v[0:3], v[250:253], v[20:23], v[0:3]\n"
      "v_mfma_f32_16x16x32_bf16 v[56:59], v[172:175], a[120:123], v[56:59]\n"
      "v_mfma_f32_16x16x32_bf16 v[4:7], v[250:253], v[24:27], v[4:7]\n"
      "v_mfma_f32_16x16x32_bf16 v[60:63], v[172:175], a[124:127], v[60:63]\n"
      "global_load_dwordx4 v[172:175], v240, s[74:75] offset:3072 sc1\n"
      "v_mfma_f32_16x16x32_bf16 v[40:43], v[176:179], a[136:139], v[40:43]\n"
      "v_mfma_f32_16x16x32_bf16 v[0:3], v[252:255], v[28:31], v[0:3]\n"
      "v_mfma_f32_16x16x32_bf16 v[44:47], v[176:179], a[140:143], v[44:47]\n"
      "v_mfma_f32_16x16x32_bf16 v[4:7], v[248:251], v[28:31], v[4:7]\n"
      "v_mov_b32 v11, s37\n"
      "v_mfma_f32_16x16x32_bf16 v[48:51], v[176:179], a[144:147], v[48:51]\n"
      "v_mfma_f32_16x16x32_bf16 v[52:55], v[176:179], a[148:151], v[52:55]\n"
      "s_nop 2\n"
      "v_cvt_pk_bf16_f32 v8, v0, v1\n"
      "v_mfma_f32_16x16x32_bf16 v[56:59], v[176:179], a[152:155], v[56:59]\n"
      "v_cvt_pk_bf16_f32 v9, v2, v3\n"
      "v_cvt_pk_bf16_f32 v10, v4, v5\n"
      "v_mfma_f32_16x16x32_bf16 v[60:63], v[176:179], a[156:159], v[60:63]\n"
      "v_cvt_pk_bf16_f32 v247, v6, v7\n"
      "v_mfma_f32_16x16x32_bf16 v[40:43], v[180:183], a[168:171], v[40:43]\n"
      "global_store_dwordx4 v242, v[8:11], s[52:53] offset:0 sc1\n"
      "s_lshl2_add_u32 s54, s36, s33\n"
      "v_mfma_f32_16x16x32_bf16 v[44:47], v[180:183], a[172:175], v[44:47]\n"
      "s_lshl_b32 s55, s54, 15\n"
      "s_lshl2_add_u32 s56, s35, s34\n"
      "s_lshl_b32 s56, s56, 6\n"
      "v_mfma_f32_16x16x32_bf16 v[48:51], v[180:183], a[176:179], v[48:51]\n"
      "s_add_u32 s55, s55, s56\n"
      "v_and_b32 v8, 15, v243\n"
      "v_and_b32 v7, 48, v243\n"
      "v_mfma_f32_16x16x32_bf16 v[52:55], v[180:183], a[180:183], v[52:55]\n"
      "v_lshl_or_b32 v7, v8, 10, v7\n"
      "v_add_u32 v7, s55, v7\n"
      "v_mfma_f32_16x16x32_bf16 v[56:59], v[180:183], a[184:187], v[56:59]\n"
      "v_mfma_f32_16x16x32_bf16 v[60:63], v[180:183], a[188:191], v[60:63]\n"
      "s_waitcnt vmcnt(16)\n"
      "v_mfma_f32_16x16x32_bf16 v[40:43], v[184:187], a[200:203], v[40:43]\n"
      "v_mfma_f32_16x16x32_bf16 v[44:47], v[184:187], a[204:207], v[44:47]\n"
      "v_mfma_f32_16x16x32_bf16 v[48:51], v[184:187], a[208:211], v[48:51]\n"
      "v_mfma_f32_16x16x32_bf16 v[52:55], v[184:187], a[212:215], v[52:55]\n"
      "v_mfma_f32_16x16x32_bf16 v[56:59], v[184:187], a[216:219], v[56:59]\n"
      "v_mfma_f32_16x16x32_bf16 v[60:63], v[184:187], a[220:223], v[60:63]\n"
      "v_mfma_f32_16x16x32_bf16 v[40:43], v[188:191], a[232:235], v[40:43]\n"
      "v_mfma_f32_16x16x32_bf16 v[44:47], v[188:191], a[236:239], v[44:47]\n"
      "v_mfma_f32_16x16x32_bf16 v[48:51], v[188:191], a[240:243], v[48:51]\n"
      "v_mfma_f32_16x16x32_bf16 v[52:55], v[188:191], a[244:247], v[52:55]\n"
      "v_mfma_f32_16x16x32_bf16 v[56:59], v[188:191], a[248:251], v[56:59]\n"
      "v_mfma_f32_16x16x32_bf16 v[60:63], v[188:191], a[252:255], v[60:63]\n"
      "v_mfma_f32_16x16x32_bf16 v[32:35], v[176:179], a[128:131], v[32:35]\n"
      "v_mfma_f32_16x16x32_bf16 v[36:39], v[176:179], a[132:135], v[36:39]\n"
      "global_load_dwordx4 v[176:179], v241, s[10:11] offset:3072 sc1\n"
      "v_cvt_pk_bf16_f32 v40, v40, v41\n"
      "v_cvt_pk_bf16_f32 v41, v42, v43\n"
      "v_mfma_f32_16x16x32_bf16 v[32:35], v[180:183], a[160:163], v[32:35]\n"
      "ds_write_addtid_b32 v40 offset:3072\n"
      "v_cvt_pk_bf16_f32 v42, v44, v45\n"
      "v_mfma_f32_16x16x32_bf16 v[36:39], v[180:183], a[164:167], v[36:39]\n"
      "global_load_dwordx4 v[180:183], v241, s[72:73] offset:3072 sc1\n"
      "ds_write_addtid_b32 v41 offset:3328\n"
      "v_cvt_pk_bf16_f32 v43, v46, v47\n"
      "v_mfma_f32_16x16x32_bf16 v[32:35], v[184:187], a[192:195], v[32:35]\n"
      "ds_write_addtid_b32 v42 offset:3584\n"
      "v_cvt_pk_bf16_f32 v44, v48, v49\n"
      "v_mfma_f32_16x16x32_bf16 v[36:39], v[184:187], a[196:199], v[36:39]\n"
      "global_load_dwordx4 v[184:187], v241, s[14:15] offset:3072 sc1\n"
      "ds_write_addtid_b32 v43 offset:3840\n"
      "v_cvt_pk_bf16_f32 v45, v50, v51\n"
      "v_mfma_f32_16x16x32_bf16 v[32:35], v[188:191], a[224:227], v[32:35]\n"
      "ds_write_addtid_b32 v44 offset:4096\n"
      "v_cvt_pk_bf16_f32 v46, v52, v53\n"
      "v_mfma_f32_16x16x32_bf16 v[36:39], v[188:191], a[228:231], v[36:39]\n"
      "s_waitcnt vmcnt(17)\n"
      "global_load_dwordx4 v[188:191], v241, s[74:75] offset:3072 sc1\n"
      "ds_write_addtid_b32 v45 offset:4352\n"
      "v_cvt_pk_bf16_f32 v47, v54, v55\n"
      "v_mfma_f32_16x16x32_bf16 v[72:75], v[128:131], a[8:11], 0\n"
      "ds_write_addtid_b32 v46 offset:4608\n"
      "v_cvt_pk_bf16_f32 v48, v56, v57\n"
      "v_mfma_f32_16x16x32_bf16 v[76:79], v[128:131], a[12:15], 0\n"
      "ds_write_addtid_b32 v47 offset:4864\n"
      "v_cvt_pk_bf16_f32 v49, v58, v59\n"
      "v_mfma_f32_16x16x32_bf16 v[80:83], v[128:131], a[16:19], 0\n"
      "ds_write_addtid_b32 v48 offset:5120\n"
      "v_cvt_pk_bf16_f32 v50, v60, v61\n"
      "v_mfma_f32_16x16x32_bf16 v[84:87], v[128:131], a[20:23], 0\n"
      "ds_write_addtid_b32 v49 offset:5376\n"
      "v_cvt_pk_bf16_f32 v51, v62, v63\n"
      "v_mfma_f32_16x16x32_bf16 v[88:91], v[128:131], a[24:27], 0\n"
      "ds_write_addtid_b32 v50 offset:5632\n"
      "ds_write_addtid_b32 v51 offset:5888\n"
      "v_mfma_f32_16x16x32_bf16 v[92:95], v[128:131], a[28:31], 0\n"
      "v_mfma_f32_16x16x32_bf16 v[72:75], v[132:135], a[40:43], v[72:75]\n"
      "v_mfma_f32_16x16x32_bf16 v[76:79], v[132:135], a[44:47], v[76:79]\n"
      "s_waitcnt lgkmcnt(0)\n"
      "s_barrier\n"
      "v_mfma_f32_16x16x32_bf16 v[80:83], v[132:135], a[48:51], v[80:83]\n"
      "ds_read2st64_b32 v[52:53], v244 offset0:12 offset1:13\n"
      "ds_read2st64_b32 v[54:55], v245 offset0:16 offset1:17\n"
      "v_mfma_f32_16x16x32_bf16 v[84:87], v[132:135], a[52:55], v[84:87]\n"
      "ds_read2st64_b32 v[56:57], v244 offset0:14 offset1:15\n"
      "ds_read2st64_b32 v[58:59], v245 offset0:18 offset1:19\n"
      "v_mfma_f32_16x16x32_bf16 v[88:91], v[132:135], a[56:59], v[88:91]\n"
      "ds_read2st64_b32 v[60:61], v246 offset0:20 offset1:21\n"
      "ds_read2st64_b32 v[62:63], v246 offset0:22 offset1:23\n"
      "v_mfma_f32_16x16x32_bf16 v[92:95], v[132:135], a[60:63], v[92:95]\n"
      "s_waitcnt vmcnt(16)\n"
      "v_mfma_f32_16x16x32_bf16 v[72:75], v[136:139], a[72:75], v[72:75]\n"
      "v_mfma_f32_16x16x32_bf16 v[76:79], v[136:139], a[76:79], v[76:79]\n"
      "v_mfma_f32_16x16x32_bf16 v[80:83], v[136:139], a[80:83], v[80:83]\n"
      "v_mfma_f32_16x16x32_bf16 v[84:87], v[136:139], a[84:87], v[84:87]\n"
      "s_waitcnt lgkmcnt(0)\n"
      "v_mfma_f32_16x16x32_bf16 v[32:35], v[250:253], v[52:55], v[32:35]\n"
      "v_mfma_f32_16x16x32_bf16 v[36:39], v[250:253], v[56:59], v[36:39]\n"
      "v_mfma_f32_16x16x32_bf16 v[88:91], v[136:139], a[88:91], v[88:91]\n"
      "v_mfma_f32_16x16x32_bf16 v[32:35], v[252:255], v[60:63], v[32:35]\n"
      "v_mfma_f32_16x16x32_bf16 v[36:39], v[248:251], v[60:63], v[36:39]\n"
      "v_add_u32 v4, 0, v242\n"
      "v_mfma_f32_16x16x32_bf16 v[92:95], v[136:139], a[92:95], v[92:95]\n"
      "global_load_dwordx4 v[12:15], v4, s[62:63] offset:0 sc1\n"
      "global_load_dwordx4 v[16:19], v4, s[62:63] offset:1024 sc1\n"
      "v_mov_b32 v43, s37\n"
      "v_mfma_f32_16x16x32_bf16 v[72:75], v[140:143], a[104:107], v[72:75]\n"
      "global_load_dwordx4 v[20:23], v4, s[62:63] offset:2048 sc1\n"
      "v_mov_b32 v44, v247\n"
      "v_mov_b32 v47, s37\n"
      "v_mfma_f32_16x16x32_bf16 v[76:79], v[140:143], a[108:111], v[76:79]\n"
      "v_mfma_f32_16x16x32_bf16 v[80:83], v[140:143], a[112:115], v[80:83]\n"
      "v_cvt_pk_bf16_f32 v40, v32, v33\n"
      "v_cvt_pk_bf16_f32 v41, v34, v35\n"
      "v_cvt_pk_bf16_f32 v42, v36, v37\n"
      "v_mfma_f32_16x16x32_bf16 v[84:87], v[140:143], a[116:119], v[84:87]\n"
      "v_cvt_pk_bf16_f32 v45, v38, v39\n"
      "v_mfma_f32_16x16x32_bf16 v[88:91], v[140:143], a[120:123], v[88:91]\n"
      "global_store_dwordx4 v242, v[40:43], s[52:53] offset:1024 sc1\n"
      "global_store_dwordx4 v242, v[44:47], s[52:53] offset:2048 sc1\n"
      "v_mfma_f32_16x16x32_bf16 v[92:95], v[140:143], a[124:127], v[92:95]\n"
      "s_waitcnt vmcnt(18)\n"
      "v_mfma_f32_16x16x32_bf16 v[72:75], v[144:147], a[136:139], v[72:75]\n"
      "v_mfma_f32_16x16x32_bf16 v[76:79], v[144:147], a[140:143], v[76:79]\n"
      "v_mfma_f32_16x16x32_bf16 v[80:83], v[144:147], a[144:147], v[80:83]\n"
      "v_mfma_f32_16x16x32_bf16 v[84:87], v[144:147], a[148:151], v[84:87]\n"
      "v_mfma_f32_16x16x32_bf16 v[88:91], v[144:147], a[152:155], v[88:91]\n"
      "v_mfma_f32_16x16x32_bf16 v[92:95], v[144:147], a[156:159], v[92:95]\n"
      "v_mfma_f32_16x16x32_bf16 v[72:75], v[148:151], a[168:171], v[72:75]\n"
      "v_mfma_f32_16x16x32_bf16 v[76:79], v[148:151], a[172:175], v[76:79]\n"
      "v_mfma_f32_16x16x32_bf16 v[80:83], v[148:151], a[176:179], v[80:83]\n"
      "v_mfma_f32_16x16x32_bf16 v[84:87], v[148:151], a[180:183], v[84:87]\n"
      "v_mfma_f32_16x16x32_bf16 v[88:91], v[148:151], a[184:187], v[88:91]\n"
      "v_mfma_f32_16x16x32_bf16 v[92:95], v[148:151], a[188:191], v[92:95]\n"
      "s_waitcnt vmcnt(14)\n"
      "v_mfma_f32_16x16x32_bf16 v[72:75], v[152:155], a[200:203], v[72:75]\n"
      "v_mfma_f32_16x16x32_bf16 v[76:79], v[152:155], a[204:207], v[76:79]\n"
      "v_mfma_f32_16x16x32_bf16 v[80:83], v[152:155], a[208:211], v[80:83]\n"
      "v_mfma_f32_16x16x32_bf16 v[84:87], v[152:155], a[212:215], v[84:87]\n"
      "v_mfma_f32_16x16x32_bf16 v[88:91], v[152:155], a[216:219], v[88:91]\n"
      "v_mfma_f32_16x16x32_bf16 v[92:95], v[152:155], a[220:223], v[92:95]\n"
      "v_mfma_f32_16x16x32_bf16 v[72:75], v[156:159], a[232:235], v[72:75]\n"
      "v_mfma_f32_16x16x32_bf16 v[76:79], v[156:159], a[236:239], v[76:79]\n"
      "v_mfma_f32_16x16x32_bf16 v[80:83], v[156:159], a[240:243], v[80:83]\n"
      "v_mfma_f32_16x16x32_bf16 v[84:87], v[156:159], a[244:247], v[84:87]\n"
      "v_mfma_f32_16x16x32_bf16 v[88:91], v[156:159], a[248:251], v[88:91]\n"
      "v_mfma_f32_16x16x32_bf16 v[92:95], v[156:159], a[252:255], v[92:95]\n"
      "s_waitcnt vmcnt(12)\n"
      "v_mfma_f32_16x16x32_bf16 v[104:107], v[160:163], a[8:11], 0\n"
      "v_mfma_f32_16x16x32_bf16 v[108:111], v[160:163], a[12:15], 0\n"
      "v_cvt_pk_bf16_f32 v72, v72, v73\n"
      "v_cvt_pk_bf16_f32 v73, v74, v75\n"
      "v_mfma_f32_16x16x32_bf16 v[112:115], v[160:163], a[16:19], 0\n"
      "ds_write_addtid_b32 v72 offset:6144\n"
      "v_cvt_pk_bf16_f32 v74, v76, v77\n"
      "v_mfma_f32_16x16x32_bf16 v[116:119], v[160:163], a[20:23], 0\n"
      "ds_write_addtid_b32 v73 offset:6400\n"
      "v_cvt_pk_bf16_f32 v75, v78, v79\n"
      "v_mfma_f32_16x16x32_bf16 v[120:123], v[160:163], a[24:27], 0\n"
      "ds_write_addtid_b32 v74 offset:6656\n"
      "v_cvt_pk_bf16_f32 v76, v80, v81\n"
      "v_mfma_f32_16x16x32_bf16 v[124:127], v[160:163], a[28:31], 0\n"
      "ds_write_addtid_b32 v75 offset:6912\n"
      "v_cvt_pk_bf16_f32 v77, v82, v83\n"
      "v_mfma_f32_16x16x32_bf16 v[104:107], v[164:167], a[40:43], v[104:107]\n"
      "ds_write_addtid_b32 v76 offset:7168\n"
      "v_cvt_pk_bf16_f32 v78, v84, v85\n"
      "v_mfma_f32_16x16x32_bf16 v[108:111], v[164:167], a[44:47], v[108:111]\n"
      "ds_write_addtid_b32 v77 offset:7424\n"
      "v_cvt_pk_bf16_f32 v79, v86, v87\n"
      "v_mfma_f32_16x16x32_bf16 v[112:115], v[164:167], a[48:51], v[112:115]\n"
      "ds_write_addtid_b32 v78 offset:7680\n"
      "v_cvt_pk_bf16_f32 v80, v88, v89\n"
      "v_mfma_f32_16x16x32_bf16 v[116:119], v[164:167], a[52:55], v[116:119]\n"
      "ds_write_addtid_b32 v79 offset:7936\n"
      "v_cvt_pk_bf16_f32 v81, v90, v91\n"
      "v_mfma_f32_16x16x32_bf16 v[120:123], v[164:167], a[56:59], v[120:123]\n"
      "ds_write_addtid_b32 v80 offset:8192\n"
      "v_cvt_pk_bf16_f32 v82, v92, v93\n"
      "v_mfma_f32_16x16x32_bf16 v[124:127], v[164:167], a[60:63], v[124:127]\n"
      "s_waitcnt vmcnt(10)\n"
      "ds_write_addtid_b32 v81 offset:8448\n"
      "v_cvt_pk_bf16_f32 v83, v94, v95\n"
      "v_mfma_f32_16x16x32_bf16 v[104:107], v[168:171], a[72:75], v[104:107]\n"
      "ds_write_addtid_b32 v82 offset:8704\n"
      "v_mfma_f32_16x16x32_bf16 v[108:111], v[168:171], a[76:79], v[108:111]\n"
      "ds_write_addtid_b32 v83 offset:8960\n"
      "v_mfma_f32_16x16x32_bf16 v[112:115], v[168:171], a[80:83], v[112:115]\n"
      "s_waitcnt vmcnt(2)\n"
      "v_bitop3_b32 v22, v15, v19, s37 bitop3:0x7e\n"
      "v_bitop3_b32 v22, v22, v23, s37 bitop3:0xf6\n"
      "v_cmp_ne_u32 vcc, 0, v22\n"
      "s_nop 1\n"
      "s_cbranch_vccnz .Lp_s16_%=\n"
      ".Lp_o17_%=:\n"
      "v_mfma_f32_16x16x32_bf16 v[116:119], v[168:171], a[84:87], v[116:119]\n"
      "v_mov_b32 v15, v20\n"
      "v_mov_b32 v19, v21\n"
      "v_mfma_f32_16x16x32_bf16 v[120:123], v[168:171], a[88:91], v[120:123]\n"
      "s_waitcnt lgkmcnt(0)\n"
      "s_barrier\n"
      "v_mfma_f32_16x16x32_bf16 v[124:127], v[168:171], a[92:95], v[124:127]\n"
      "ds_read2st64_b32 v[84:85], v244 offset0:24 offset1:25\n"
      "v_mfma_f32_16x16x32_bf16 v[104:107], v[172:175], a[104:107], v[104:107]\n"
      "ds_read2st64_b32 v[86:87], v245 offset0:28 offset1:29\n"
      "v_add_u32 v5, 12288, v242\n"
      "v_mfma_f32_16x16x32_bf16 v[108:111], v[172:175], a[108:111], v[108:111]\n"
      "global_load_dwordx4 v[44:47], v5, s[62:63] offset:0 sc1\n"
      "v_mfma_f32_16x16x32_bf16 v[112:115], v[172:175], a[112:115], v[112:115]\n"
      "global_load_dwordx4 v[48:51], v5, s[62:63] offset:1024 sc1\n"
      "v_mfma_f32_16x16x32_bf16 v[116:119], v[172:175], a[116:119], v[116:119]\n"
      "global_load_dwordx4 v[52:55], v5, s[62:63] offset:2048 sc1\n"
      "v_mfma_f32_16x16x32_bf16 v[120:123], v[172:175], a[120:123], v[120:123]\n"
      "ds_read2st64_b32 v[88:89], v244 offset0:26 offset1:27\n"
      "v_mfma_f32_16x16x32_bf16 v[124:127], v[172:175], a[124:127], v[124:127]\n"
      "ds_read2st64_b32 v[90:91], v245 offset0:30 offset1:31\n"
      "v_mfma_f32_16x16x32_bf16 v[104:107], v[176:179], a[136:139], v[104:107]\n"
      "ds_read2st64_b32 v[92:93], v246 offset0:32 offset1:33\n"
      "v_mfma_f32_16x16x32_bf16 v[108:111], v[176:179], a[140:143], v[108:111]\n"
      "ds_read2st64_b32 v[94:95], v246 offset0:34 offset1:35\n"
      "v_mfma_f32_16x16x32_bf16 v[112:115], v[176:179], a[144:147], v[112:115]\n"
      "v_mfma_f32_16x16x32_bf16 v[64:67], v[252:255], v[12:15], 0\n"
      "v_mfma_f32_16x16x32_bf16 v[116:119], v[176:179], a[148:151], v[116:119]\n"
      "v_mfma_f32_16x16x32_bf16 v[68:71], v[248:251], v[12:15], 0\n"
      "v_mfma_f32_16x16x32_bf16 v[120:123], v[176:179], a[152:155], v[120:123]\n"
      "v_mfma_f32_16x16x32_bf16 v[96:99], v[252:255], v[16:19], 0\n"
      "v_mfma_f32_16x16x32_bf16 v[124:127], v[176:179], a[156:159], v[124:127]\n"
      "v_mfma_f32_16x16x32_bf16 v[100:103], v[248:251], v[16:19], 0\n"
      "v_mfma_f32_16x16x32_bf16 v[104:107], v[180:183], a[168:171], v[104:107]\n"
      "s_waitcnt lgkmcnt(0)\n"
      "v_mfma_f32_16x16x32_bf16 v[64:67], v[250:253], v[84:87], v[64:67]\n"
      "v_mfma_f32_16x16x32_bf16 v[108:111], v[180:183], a[172:175], v[108:111]\n"
      "v_mfma_f32_16x16x32_bf16 v[68:71], v[250:253], v[88:91], v[68:71]\n"
      "v_mfma_f32_16x16x32_bf16 v[112:115], v[180:183], a[176:179], v[112:115]\n"
      "v_mfma_f32_16x16x32_bf16 v[64:67], v[252:255], v[92:95], v[64:67]\n"
      "v_mfma_f32_16x16x32_bf16 v[116:119], v[180:183], a[180:183], v[116:119]\n"
      "v_mfma_f32_16x16x32_bf16 v[68:71], v[248:251], v[92:95], v[68:71]\n"
      "v_mfma_f32_16x16x32_bf16 v[120:123], v[180:183], a[184:187], v[120:123]\n"
      "v_mfma_f32_16x16x32_bf16 v[124:127], v[180:183], a[188:191], v[124:127]\n"
      "v_mfma_f32_16x16x32_bf16 v[104:107], v[184:187], a[200:203], v[104:107]\n"
      "v_mfma_f32_16x16x32_bf16 v[108:111], v[184:187], a[204:207], v[108:111]\n"
      "v_mfma_f32_16x16x32_bf16 v[112:115], v[184:187], a[208:211], v[112:115]\n"
      "v_mfma_f32_16x16x32_bf16 v[116:119], v[184:187], a[212:215], v[116:119]\n"
      "v_mfma_f32_16x16x32_bf16 v[120:123], v[184:187], a[216:219], v[120:123]\n"
      "v_mfma_f32_16x16x32_bf16 v[124:127], v[184:187], a[220:223], v[124:127]\n"
      "v_mfma_f32_16x16x32_bf16 v[104:107], v[188:191], a[232:235], v[104:107]\n"
      "v_mfma_f32_16x16x32_bf16 v[108:111], v[188:191], a[236:239], v[108:111]\n"
      "s_lshr_b32 s43, s42, 7\n"
      "s_cmp_eq_u32 s43, s36\n"
      "s_cbranch_scc0 .Lp_e18_%=\n"
      "s_lshl2_add_u32 s44, s35, s34\n"
      "s_lshl2_add_u32 s43, s44, s33\n"
      "s_lshl_b32 s43, s43, 12\n"
      "s_add_u32 s43, s43, 524288\n"
      "v_add_u32 v60, s43, v242\n"
      "global_load_dwordx4 v[24:27], v60, s[58:59] offset:0 sc1\n"
      "global_load_dwordx4 v[28:31], v60, s[58:59] offset:1024 sc1\n"
      "global_load_dwordx4 v[32:35], v60, s[58:59] offset:2048 sc1\n"
      "global_load_dwordx4 v[36:39], v60, s[58:59] offset:3072 sc1\n"
      "v_and_b32 v58, 15, v243\n"
      "v_lshrrev_b32 v59, 5, v243\n"
      "v_lshl_or_b32 v58, v59, 4, v58\n"
      "s_lshl4_add_u32 s44, s42, s44\n"
      "s_lshl_b32 s44, s44, 6\n"
      "v_lshl_add_u32 v59, v58, 1, s44\n"
      "global_load_ushort v57, v59, s[16:17]\n"
      "s_mov_b32 s44, 131072\n"
      "v_lshl_add_u32 v58, v58, 2, s44\n"
      "s_lshl_b32 s43, s33, 13\n"
      "v_add_u32 v59, s43, v58\n"
      ".Lp_e18_%=:\n"
      "v_mfma_f32_16x16x32_bf16 v[112:115], v[188:191], a[240:243], v[112:115]\n"
      "v_mfma_f32_16x16x32_bf16 v[116:119], v[188:191], a[244:247], v[116:119]\n"
      "v_mfma_f32_16x16x32_bf16 v[120:123], v[188:191], a[248:251], v[120:123]\n"
      "v_mfma_f32_16x16x32_bf16 v[124:127], v[188:191], a[252:255], v[124:127]\n"
      "v_mfma_f32_16x16x32_bf16 v[64:67], v[128:131], a[0:3], v[64:67]\n"
      "v_mfma_f32_16x16x32_bf16 v[68:71], v[128:131], a[4:7], v[68:71]\n"
      "v_cvt_pk_bf16_f32 v104, v104, v105\n"
      "v_cvt_pk_bf16_f32 v105, v106, v107\n"
      "v_mfma_f32_16x16x32_bf16 v[64:67], v[132:135], a[32:35], v[64:67]\n"
      "ds_write_addtid_b32 v104 offset:9216\n"
      "v_cvt_pk_bf16_f32 v106, v108, v109\n"
      "v_mfma_f32_16x16x32_bf16 v[68:71], v[132:135], a[36:39], v[68:71]\n"
      "ds_write_addtid_b32 v105 offset:9472\n"
      "v_cvt_pk_bf16_f32 v107, v110, v111\n"
      "v_mfma_f32_16x16x32_bf16 v[64:67], v[136:139], a[64:67], v[64:67]\n"
      "ds_write_addtid_b32 v106 offset:9728\n"
      "v_cvt_pk_bf16_f32 v108, v112, v113\n"
      "v_mfma_f32_16x16x32_bf16 v[68:71], v[136:139], a[68:71], v[68:71]\n"
      "ds_write_addtid_b32 v107 offset:9984\n"
      "v_cvt_pk_bf16_f32 v109, v114, v115\n"
      "v_mfma_f32_16x16x32_bf16 v[64:67], v[140:143], a[96:99], v[64:67]\n"
      "ds_write_addtid_b32 v108 offset:10240\n"
      "v_cvt_pk_bf16_f32 v110, v116, v117\n"
      "v_mfma_f32_16x16x32_bf16 v[68:71], v[140:143], a[100:103], v[68:71]\n"
      "ds_write_addtid_b32 v109 offset:10496\n"
      "v_cvt_pk_bf16_f32 v111, v118, v119\n"
      "v_mfma_f32_16x16x32_bf16 v[64:67], v[144:147], a[128:131], v[64:67]\n"
      "ds_write_addtid_b32 v110 offset:10752\n"
      "v_cvt_pk_bf16_f32 v112, v120, v121\n"
      "v_mfma_f32_16x16x32_bf16 v[68:71], v[144:147], a[132:135], v[68:71]\n"
      "ds_write_addtid_b32 v111 offset:11008\n"
      "v_cvt_pk_bf16_f32 v113, v122, v123\n"
      "v_mfma_f32_16x16x32_bf16 v[64:67], v[148:151], a[160:163], v[64:67]\n"
      "ds_write_addtid_b32 v112 offset:11264\n"
      "v_add_u32 v6, 24576, v242\n"
      "v_cvt_pk_bf16_f32 v114, v124, v125\n"
      "v_mfma_f32_16x16x32_bf16 v[68:71], v[148:151], a[164:167], v[68:71]\n"
      "global_load_dwordx4 v[224:227], v6, s[62:63] offset:0 sc1\n"
      "global_load_dwordx4 v[228:231], v6, s[62:63] offset:1024 sc1\n"
      "v_mfma_f32_16x16x32_bf16 v[64:67], v[152:155], a[192:195], v[64:67]\n"
      "global_load_dwordx4 v[232:235], v6, s[62:63] offset:2048 sc1\n"
      "ds_write_addtid_b32 v113 offset:11520\n"
      "v_cvt_pk_bf16_f32 v115, v126, v127\n"
      "v_mfma_f32_16x16x32_bf16 v[68:71], v[152:155], a[196:199], v[68:71]\n"
      "ds_write_addtid_b32 v114 offset:11776\n"
      "ds_write_addtid_b32 v115 offset:12032\n"
      "v_mfma_f32_16x16x32_bf16 v[64:67], v[156:159], a[224:227], v[64:67]\n"
      "s_waitcnt vmcnt(3)\n"
      "v_bitop3_b32 v54, v47, v51, s37 bitop3:0x7e\n"
      "v_bitop3_b32 v54, v54, v55, s37 bitop3:0xf6\n"
      "v_cmp_ne_u32 vcc, 0, v54\n"
      "s_nop 1\n"
      "s_cbranch_vccnz .Lp_s20_%=\n"
      ".Lp_o21_%=:\n"
      "v_mfma_f32_16x16x32_bf16 v[68:71], v[156:159], a[228:231], v[68:71]\n"
      "v_mov_b32 v47, v52\n"
      "v_mov_b32 v51, v53\n"
      "v_mfma_f32_16x16x32_bf16 v[96:99], v[160:163], a[0:3], v[96:99]\n"
      "s_lshr_b32 s43, s42, 7\n"
      "s_cmp_eq_u32 s43, s36\n"
      "s_cbranch_scc0 .Lp_e22_%=\n"
      "s_waitcnt vmcnt(3)\n"
      "v_bitop3_b32 v61, v25, v27, s37 bitop3:0x7e\n"
      "v_bitop3_b32 v61, v61, v29, s37 bitop3:0xf6\n"
      "v_bitop3_b32 v61, v61, v31, s37 bitop3:0xf6\n"
      "v_bitop3_b32 v61, v61, v33, s37 bitop3:0xf6\n"
      "v_bitop3_b32 v61, v61, v35, s37 bitop3:0xf6\n"
      "v_bitop3_b32 v61, v61, v37, s37 bitop3:0xf6\n"
      "v_bitop3_b32 v61, v61, v39, s37 bitop3:0xf6\n"
      "v_cmp_ne_u32 vcc, 0, v61\n"
      "s_nop 1\n"
      "s_cbranch_vccnz .Lp_s24_%=\n"
      ".Lp_o25_%=:\n"
      "v_add_f32 v24, v24, v28\n"
      "v_add_f32 v26, v26, v30\n"
      "v_add_f32 v32, v32, v36\n"
      "v_add_f32 v34, v34, v38\n"
      "v_add_f32 v24, v24, v32\n"
      "v_add_f32 v26, v26, v34\n"
      "s_nop 1\n"
      "v_permlane32_swap_b32 v24, v26\n"
      "v_add_f32 v24, v24, v26\n"
      "v_mov_b32 v26, v24\n"
      "s_nop 1\n"
      "v_permlane16_swap_b32 v24, v26\n"
      "v_add_f32 v24, v24, v26\n"
      "ds_write_b32 v59, v24\n"
      "s_waitcnt lgkmcnt(0)\n"
      ".Lp_e22_%=:\n"
      "v_mfma_f32_16x16x32_bf16 v[100:103], v[160:163], a[4:7], v[100:103]\n"
      "s_waitcnt lgkmcnt(0)\n"
      "s_barrier\n"
      "v_mfma_f32_16x16x32_bf16 v[96:99], v[164:167], a[32:35], v[96:99]\n"
      "s_lshr_b32 s43, s42, 5\n"
      "s_lshl2_add_u32 s44, s36, s33\n"
      "s_cmp_eq_u32 s43, s44\n"
      "s_cbranch_scc0 .Lp_e26_%=\n"
      "ds_read_b32 v24, v58 offset:0\n"
      "ds_read_b32 v25, v58 offset:8192\n"
      "ds_read_b32 v26, v58 offset:16384\n"
      "ds_read_b32 v27, v58 offset:24576\n"
      "v_mov_b32 v62, 0\n"
      "v_mov_b32 v63, s37\n"
      "s_or_b32 s43, s34, s35\n"
      "s_cmp_eq_u32 s43, 0\n"
      "s_cbranch_scc0 .Lp_z27_%=\n"
      "s_mov_b64 exec, 1\n"
      "global_store_dword v62, v63, s[58:59] sc1\n"
      ".Lp_z27_%=:\n"
      "s_mov_b64 exec, -1\n"
      "s_waitcnt vmcnt(3)\n"
      "v_lshlrev_b32 v57, 16, v57\n"
      "v_and_b32 v56, 48, v243\n"
      "s_mov_b32 s44, 131072\n"
      "v_lshl_add_u32 v56, v56, 1, s44\n"
      "s_waitcnt lgkmcnt(0)\n"
      "v_add_f32 v24, v24, v25\n"
      "s_waitcnt lgkmcnt(0)\n"
      "v_add_f32 v26, v26, v27\n"
      "v_add_f32 v24, v24, v26\n"
      "v_cvt_pk_bf16_f32 v24, v24, v24\n"
      "v_and_b32 v24, 0xffff0000, v24\n"
      "v_mul_f32 v24, s84, v24\n"
      "v_cvt_pk_bf16_f32 v24, v24, v24\n"
      "v_and_b32 v24, 0xffff0000, v24\n"
      "v_add_f32 v24, v24, v57\n"
      "v_cvt_pk_bf16_f32 v24, v24, v24\n"
      "v_and_b32 v24, 0xffff0000, v24\n"
      "v_sub_f32 v24, v24, v57\n"
      "ds_write_b32 v58, v24 offset:1024\n"
      "ds_read_b128 v[28:31], v56 offset:1024\n"
      "ds_read_b128 v[32:35], v56 offset:1040\n"
      ".Lp_e26_%=:\n"
      "v_mfma_f32_16x16x32_bf16 v[100:103], v[164:167], a[36:39], v[100:103]\n"
      "ds_read2st64_b32 v[116:117], v244 offset0:36 offset1:37\n"
      "ds_read2st64_b32 v[118:119], v245 offset0:40 offset1:41\n"
      "v_mfma_f32_16x16x32_bf16 v[96:99], v[168:171], a[64:67], v[96:99]\n"
      "ds_read2st64_b32 v[120:121], v244 offset0:38 offset1:39\n"
      "ds_read2st64_b32 v[122:123], v245 offset0:42 offset1:43\n"
      "v_mfma_f32_16x16x32_bf16 v[100:103], v[168:171], a[68:71], v[100:103]\n"
      "ds_read2st64_b32 v[124:125], v246 offset0:44 offset1:45\n"
      "ds_read2st64_b32 v[126:127], v246 offset0:46 offset1:47\n"
      "v_mfma_f32_16x16x32_bf16 v[96:99], v[172:175], a[96:99], v[96:99]\n"
      "v_mfma_f32_16x16x32_bf16 v[64:67], v[252:255], v[44:47], v[64:67]\n"
      "v_mfma_f32_16x16x32_bf16 v[68:71], v[248:251], v[44:47], v[68:71]\n"
      "v_mfma_f32_16x16x32_bf16 v[100:103], v[172:175], a[100:103], v[100:103]\n"
      "v_mfma_f32_16x16x32_bf16 v[96:99], v[252:255], v[48:51], v[96:99]\n"
      "v_mfma_f32_16x16x32_bf16 v[100:103], v[248:251], v[48:51], v[100:103]\n"
      "v_mfma_f32_16x16x32_bf16 v[96:99], v[176:179], a[128:131], v[96:99]\n"
      "v_mfma_f32_16x16x32_bf16 v[100:103], v[176:179], a[132:135], v[100:103]\n"
      "v_mfma_f32_16x16x32_bf16 v[96:99], v[180:183], a[160:163], v[96:99]\n"
      "s_waitcnt lgkmcnt(0)\n"
      "v_mfma_f32_16x16x32_bf16 v[96:99], v[250:253], v[116:119], v[96:99]\n"
      "v_mfma_f32_16x16x32_bf16 v[100:103], v[250:253], v[120:123], v[100:103]\n"
      "v_mfma_f32_16x16x32_bf16 v[100:103], v[180:183], a[164:167], v[100:103]\n"
      "v_mfma_f32_16x16x32_bf16 v[96:99], v[252:255], v[124:127], v[96:99]\n"
      "v_mfma_f32_16x16x32_bf16 v[100:103], v[248:251], v[124:127], v[100:103]\n"
      "v_mfma_f32_16x16x32_bf16 v[96:99], v[184:187], a[192:195], v[96:99]\n"
      "v_mfma_f32_16x16x32_bf16 v[100:103], v[184:187], a[196:199], v[100:103]\n"
      "v_mfma_f32_16x16x32_bf16 v[96:99], v[188:191], a[224:227], v[96:99]\n"
      "v_mfma_f32_16x16x32_bf16 v[100:103], v[188:191], a[228:231], v[100:103]\n"
      "s_lshr_b32 s43, s42, 5\n"
      "s_lshl2_add_u32 s44, s36, s33\n"
      "s_cmp_eq_u32 s43, s44\n"
      "s_cbranch_scc0 .Lp_e28_%=\n"
      "v_lshrrev_b32 v24, 10, v7\n"
      "v_subrev_u32 v24, s42, v24\n"
      "v_cmp_eq_u32 vcc, 0, v24\n"
      "s_nop 1\n"
      "s_mov_b64 exec, vcc\n"
      "v_fma_f32 v208, v205, v28, v208\n"
      "v_fma_f32 v209, v205, v29, v209\n"
      "v_fma_f32 v210, v205, v30, v210\n"
      "v_fma_f32 v211, v205, v31, v211\n"
      "v_fma_f32 v216, v205, v32, v216\n"
      "v_fma_f32 v217, v205, v33, v217\n"
      "v_fma_f32 v218, v205, v34, v218\n"
      "v_fma_f32 v219, v205, v35, v219\n"
      "s_mov_b64 exec, -1\n"
      "v_cmp_eq_u32 vcc, -16, v24\n"
      "s_nop 1\n"
      "s_mov_b64 exec, vcc\n"
      "v_fma_f32 v212, v207, v28, v212\n"
      "v_fma_f32 v213, v207, v29, v213\n"
      "v_fma_f32 v214, v207, v30, v214\n"
      "v_fma_f32 v215, v207, v31, v215\n"
      "v_fma_f32 v220, v207, v32, v220\n"
      "v_fma_f32 v221, v207, v33, v221\n"
      "v_fma_f32 v222, v207, v34, v222\n"
      "v_fma_f32 v223, v207, v35, v223\n"
      "s_mov_b64 exec, -1\n"
      ".Lp_e28_%=:\n"
      "s_waitcnt vmcnt(0)\n"
      "v_bitop3_b32 v234, v227, v231, s37 bitop3:0x7e\n"
      "v_bitop3_b32 v234, v234, v235, s37 bitop3:0xf6\n"
      "v_cmp_ne_u32 vcc, 0, v234\n"
      "s_nop 1\n"
      "s_cbranch_vccnz .Lp_s30_%=\n"
      ".Lp_o31_%=:\n"
      "v_mov_b32 v227, v232\n"
      "v_mov_b32 v231, v233\n"
      "s_nop 0\n"
      "v_mfma_f32_16x16x32_bf16 v[64:67], v[252:255], v[224:227], v[64:67]\n"
      "v_mfma_f32_16x16x32_bf16 v[96:99], v[252:255], v[228:231], v[96:99]\n"
      "v_mfma_f32_16x16x32_bf16 v[68:71], v[248:251], v[224:227], v[68:71]\n"
      "v_mfma_f32_16x16x32_bf16 v[100:103], v[248:251], v[228:231], v[100:103]\n"
      "s_nop 4\n"
      "v_fma_f32 v208, v204, v64, v208\n"
      "v_fma_f32 v209, v204, v65, v209\n"
      "v_fma_f32 v210, v204, v66, v210\n"
      "v_fma_f32 v211, v204, v67, v211\n"
      "v_fma_f32 v216, v204, v96, v216\n"
      "v_fma_f32 v217, v204, v97, v217\n"
      "v_fma_f32 v218, v204, v98, v218\n"
      "v_fma_f32 v219, v204, v99, v219\n"
      "v_cvt_pk_bf16_f32 v72, v208, v209\n"
      "v_cvt_pk_bf16_f32 v73, v210, v211\n"
      "v_cvt_pk_bf16_f32 v74, v216, v217\n"
      "v_cvt_pk_bf16_f32 v75, v218, v219\n"
      "global_store_dwordx4 v7, v[72:75], s[20:21]\n"
      "v_fma_f32 v212, v206, v68, v212\n"
      "v_fma_f32 v213, v206, v69, v213\n"
      "v_fma_f32 v214, v206, v70, v214\n"
      "v_fma_f32 v215, v206, v71, v215\n"
      "v_fma_f32 v220, v206, v100, v220\n"
      "v_fma_f32 v221, v206, v101, v221\n"
      "v_fma_f32 v222, v206, v102, v222\n"
      "v_fma_f32 v223, v206, v103, v223\n"
      "v_cvt_pk_bf16_f32 v76, v212, v213\n"
      "v_cvt_pk_bf16_f32 v77, v214, v215\n"
      "v_cvt_pk_bf16_f32 v78, v220, v221\n"
      "v_cvt_pk_bf16_f32 v79, v222, v223\n"
      "global_store_dwordx4 v7, v[76:79], s[68:69]\n"
      "s_endpgm\n"
      ".Lp_s2_%=:\n"
      "s_cmp_eq_u32 s38, 0\n"
      "s_cbranch_scc1 .Lp_o3_%=\n"
      "s_sub_u32 s38, s38, 1\n"
      "s_sleep 4\n"
      "s_nop 7\n"
      "global_load_dword v252, v253, s[58:59] offset:0 sc1\n"
      "s_waitcnt vmcnt(0)\n"
      "v_cmp_ne_u32 vcc, s37, v252\n"
      "s_nop 1\n"
      "s_cbranch_vccnz .Lp_s2_%=\n"
      "s_nop 7\n"
      "s_branch .Lp_o3_%=\n"
      ".Lp_s5_%=:\n"
      "s_cmp_eq_u32 s38, 0\n"
      "s_cbranch_scc1 .Lp_o6_%=\n"
      "s_sub_u32 s38, s38, 1\n"
      "s_sleep 4\n"
      "s_nop 7\n"
      "global_load_dword v252, v253, s[58:59] offset:128 sc1\n"
      "s_waitcnt vmcnt(0)\n"
      "v_cmp_ne_u32 vcc, s37, v252\n"
      "s_nop 1\n"
      "s_cbranch_vccnz .Lp_s5_%=\n"
      "s_nop 7\n"
      "s_branch .Lp_o6_%=\n"
      ".Lp_s8_%=:\n"
      "s_cmp_eq_u32 s38, 0\n"
      "s_cbranch_scc1 .Lp_o9_%=\n"
      "s_sub_u32 s38, s38, 1\n"
      "s_sleep 2\n"
      "s_nop 7\n"
      "global_load_dwordx2 v[202:203], v200, s[58:59] sc1\n"
      "s_waitcnt vmcnt(0)\n"
      "v_cmp_ne_u32 vcc, s37, v203\n"
      "s_nop 1\n"
      "s_cbranch_vccnz .Lp_s8_%=\n"
      "s_nop 7\n"
      "s_branch .Lp_o9_%=\n"
      ".Lp_s12_%=:\n"
      "s_cmp_eq_u32 s38, 0\n"
      "s_cbranch_scc1 .Lp_o13_%=\n"
      "s_sub_u32 s38, s38, 1\n"
      "s_sleep 1\n"
      "s_nop 7\n"
      "global_load_dwordx2 v[204:205], v198, s[58:59] offset:0 sc1\n"
      "global_load_dwordx2 v[206:207], v198, s[58:59] offset:256 sc1\n"
      "s_waitcnt vmcnt(0)\n"
      "v_bitop3_b32 v197, v205, v207, s37 bitop3:0x7e\n"
      "v_cmp_ne_u32 vcc, 0, v197\n"
      "s_nop 1\n"
      "s_cbranch_vccnz .Lp_s12_%=\n"
      "s_nop 7\n"
      "s_branch .Lp_o13_%=\n"
      ".Lp_s16_%=:\n"
      "s_cmp_eq_u32 s38, 0\n"
      "s_cbranch_scc1 .Lp_o17_%=\n"
      "s_sub_u32 s38, s38, 1\n"
      "s_nop 7\n"
      "global_load_dwordx4 v[12:15], v4, s[62:63] offset:0 sc1\n"
      "global_load_dwordx4 v[16:19], v4, s[62:63] offset:1024 sc1\n"
      "global_load_dwordx4 v[20:23], v4, s[62:63] offset:2048 sc1\n"
      "s_waitcnt vmcnt(0)\n"
      "v_bitop3_b32 v22, v15, v19, s37 bitop3:0x7e\n"
      "v_bitop3_b32 v22, v22, v23, s37 bitop3:0xf6\n"
      "v_cmp_ne_u32 vcc, 0, v22\n"
      "s_nop 1\n"
      "s_cbranch_vccnz .Lp_s16_%=\n"
      "s_nop 7\n"
      "s_branch .Lp_o17_%=\n"
      ".Lp_s20_%=:\n"
      "s_cmp_eq_u32 s38, 0\n"
      "s_cbranch_scc1 .Lp_o21_%=\n"
      "s_sub_u32 s38, s38, 1\n"
      "s_nop 7\n"
      "global_load_dwordx4 v[44:47], v5, s[62:63] offset:0 sc1\n"
      "global_load_dwordx4 v[48:51], v5, s[62:63] offset:1024 sc1\n"
      "global_load_dwordx4 v[52:55], v5, s[62:63] offset:2048 sc1\n"
      "s_waitcnt vmcnt(0)\n"
      "v_bitop3_b32 v54, v47, v51, s37 bitop3:0x7e\n"
      "v_bitop3_b32 v54, v54, v55, s37 bitop3:0xf6\n"
      "v_cmp_ne_u32 vcc, 0, v54\n"
      "s_nop 1\n"
      "s_cbranch_vccnz .Lp_s20_%=\n"
      "s_nop 7\n"
      "s_branch .Lp_o21_%=\n"
      ".Lp_s24_%=:\n"
      "s_cmp_eq_u32 s38, 0\n"
      "s_cbranch_scc1 .Lp_o25_%=\n"
      "s_sub_u32 s38, s38, 1\n"
      "s_sleep 2\n"
      "s_nop 7\n"
      "global_load_dwordx4 v[24:27], v60, s[58:59] offset:0 sc1\n"
      "global_load_dwordx4 v[28:31], v60, s[58:59] offset:1024 sc1\n"
      "global_load_dwordx4 v[32:35], v60, s[58:59] offset:2048 sc1\n"
      "global_load_dwordx4 v[36:39], v60, s[58:59] offset:3072 sc1\n"
      "s_waitcnt vmcnt(0)\n"
      "v_bitop3_b32 v61, v25, v27, s37 bitop3:0x7e\n"
      "v_bitop3_b32 v61, v61, v29, s37 bitop3:0xf6\n"
      "v_bitop3_b32 v61, v61, v31, s37 bitop3:0xf6\n"
      "v_bitop3_b32 v61, v61, v33, s37 bitop3:0xf6\n"
      "v_bitop3_b32 v61, v61, v35, s37 bitop3:0xf6\n"
      "v_bitop3_b32 v61, v61, v37, s37 bitop3:0xf6\n"
      "v_bitop3_b32 v61, v61, v39, s37 bitop3:0xf6\n"
      "v_cmp_ne_u32 vcc, 0, v61\n"
      "s_nop 1\n"
      "s_cbranch_vccnz .Lp_s24_%=\n"
      "s_nop 7\n"
      "s_branch .Lp_o25_%=\n"
      ".Lp_s30_%=:\n"
      "s_cmp_eq_u32 s38, 0\n"
      "s_cbranch_scc1 .Lp_o31_%=\n"
      "s_sub_u32 s38, s38, 1\n"
      "s_nop 7\n"
      "global_load_dwordx4 v[224:227], v6, s[62:63] offset:0 sc1\n"
      "global_load_dwordx4 v[228:231], v6, s[62:63] offset:1024 sc1\n"
      "global_load_dwordx4 v[232:235], v6, s[62:63] offset:2048 sc1\n"
      "s_waitcnt vmcnt(0)\n"
      "v_bitop3_b32 v234, v227, v231, s37 bitop3:0x7e\n"
      "v_bitop3_b32 v234, v234, v235, s37 bitop3:0xf6\n"
      "v_cmp_ne_u32 vcc, 0, v234\n"
      "s_nop 1\n"
      "s_cbranch_vccnz .Lp_s30_%=\n"
      "s_nop 7\n"
      "s_branch .Lp_o31_%=\n"
      ".Ls_s3_%=:\n"
      "s_cmp_eq_u32 s38, 0\n"
      "s_cbranch_scc1 .Ls_o4_%=\n"
      "s_sub_u32 s38, s38, 1\n"
      "s_sleep 4\n"
      "s_nop 7\n"
      "global_load_dword v252, v253, s[58:59] offset:0 sc1\n"
      "s_waitcnt vmcnt(0)\n"
      "v_cmp_ne_u32 vcc, s37, v252\n"
      "s_nop 1\n"
      "s_cbranch_vccnz .Ls_s3_%=\n"
      "s_nop 7\n"
      "s_branch .Ls_o4_%=\n"
      ".Ls_s6_%=:\n"
      "s_cmp_eq_u32 s38, 0\n"
      "s_cbranch_scc1 .Ls_o7_%=\n"
      "s_sub_u32 s38, s38, 1\n"
      "s_sleep 2\n"
      "s_nop 7\n"
      "global_load_dwordx2 v[206:207], v204, s[58:59] sc1\n"
      "s_waitcnt vmcnt(0)\n"
      "v_cmp_ne_u32 vcc, s37, v207\n"
      "s_nop 1\n"
      "s_cbranch_vccnz .Ls_s6_%=\n"
      "s_nop 7\n"
      "s_branch .Ls_o7_%=\n"
      ".Ls_s10_%=:\n"
      "s_cmp_eq_u32 s38, 0\n"
      "s_cbranch_scc1 .Ls_o11_%=\n"
      "s_sub_u32 s38, s38, 1\n"
      "s_sleep 1\n"
      "s_nop 7\n"
      "global_load_dwordx2 v[204:205], v198, s[58:59] offset:0 sc1\n"
      "global_load_dwordx2 v[206:207], v198, s[58:59] offset:256 sc1\n"
      "s_waitcnt vmcnt(0)\n"
      "v_bitop3_b32 v197, v205, v207, s37 bitop3:0x7e\n"
      "v_cmp_ne_u32 vcc, 0, v197\n"
      "s_nop 1\n"
      "s_cbranch_vccnz .Ls_s10_%=\n"
      "s_nop 7\n"
      "s_branch .Ls_o11_%=\n"
      ".Ls_s14_%=:\n"
      "s_cmp_eq_u32 s38, 0\n"
      "s_cbranch_scc1 .Ls_o15_%=\n"
      "s_sub_u32 s38, s38, 1\n"
      "s_sleep 4\n"
      "s_nop 7\n"
      "global_load_dword v252, v253, s[58:59] offset:128 sc1\n"
      "s_waitcnt vmcnt(0)\n"
      "v_cmp_ne_u32 vcc, s37, v252\n"
      "s_nop 1\n"
      "s_cbranch_vccnz .Ls_s14_%=\n"
      "s_nop 7\n"
      "s_branch .Ls_o15_%=\n"
      ".Ls_s17_%=:\n"
      "s_cmp_eq_u32 s38, 0\n"
      "s_cbranch_scc1 .Ls_o18_%=\n"
      "s_sub_u32 s38, s38, 1\n"
      "s_sleep 8\n"
      "s_nop 7\n"
      "global_load_dwordx4 v[224:227], v200, s[58:59] offset:0 sc1\n"
      "global_load_dwordx4 v[228:231], v200, s[58:59] offset:1024 sc1\n"
      "global_load_dwordx4 v[232:235], v200, s[58:59] offset:2048 sc1\n"
      "global_load_dwordx4 v[236:239], v200, s[58:59] offset:3072 sc1\n"
      "s_waitcnt vmcnt(0)\n"
      "v_bitop3_b32 v192, v225, v227, s37 bitop3:0x7e\n"
      "v_bitop3_b32 v192, v192, v229, s37 bitop3:0xf6\n"
      "v_bitop3_b32 v192, v192, v231, s37 bitop3:0xf6\n"
      "v_bitop3_b32 v192, v192, v233, s37 bitop3:0xf6\n"
      "v_bitop3_b32 v192, v192, v235, s37 bitop3:0xf6\n"
      "v_bitop3_b32 v192, v192, v237, s37 bitop3:0xf6\n"
      "v_bitop3_b32 v192, v192, v239, s37 bitop3:0xf6\n"
      "v_cmp_ne_u32 vcc, 0, v192\n"
      "s_nop 1\n"
      "s_cbranch_vccnz .Ls_s17_%=\n"
      "s_nop 7\n"
      "s_branch .Ls_o18_%=\n"
      ".Ls_s20_%=:\n"
      "s_cmp_eq_u32 s38, 0\n"
      "s_cbranch_scc1 .Ls_o21_%=\n"
      "s_sub_u32 s38, s38, 1\n"
      "s_nop 7\n"
      "global_load_dwordx4 v[12:15], v4, s[62:63] offset:0 sc1\n"
      "global_load_dwordx4 v[16:19], v4, s[62:63] offset:1024 sc1\n"
      "global_load_dwordx4 v[20:23], v4, s[62:63] offset:2048 sc1\n"
      "s_waitcnt vmcnt(0)\n"
      "v_bitop3_b32 v22, v15, v19, s37 bitop3:0x7e\n"
      "v_bitop3_b32 v22, v22, v23, s37 bitop3:0xf6\n"
      "v_cmp_ne_u32 vcc, 0, v22\n"
      "s_nop 1\n"
      "s_cbranch_vccnz .Ls_s20_%=\n"
      "s_nop 7\n"
      "s_branch .Ls_o21_%=\n"
      ".Ls_s24_%=:\n"
      "s_cmp_eq_u32 s38, 0\n"
      "s_cbranch_scc1 .Ls_o25_%=\n"
      "s_sub_u32 s38, s38, 1\n"
      "s_nop 7\n"
      "global_load_dwordx4 v[44:47], v5, s[62:63] offset:0 sc1\n"
      "global_load_dwordx4 v[48:51], v5, s[62:63] offset:1024 sc1\n"
      "global_load_dwordx4 v[52:55], v5, s[62:63] offset:2048 sc1\n"
      "s_waitcnt vmcnt(0)\n"
      "v_bitop3_b32 v54, v47, v51, s37 bitop3:0x7e\n"
      "v_bitop3_b32 v54, v54, v55, s37 bitop3:0xf6\n"
      "v_cmp_ne_u32 vcc, 0, v54\n"
      "s_nop 1\n"
      "s_cbranch_vccnz .Ls_s24_%=\n"
      "s_nop 7\n"
      "s_branch .Ls_o25_%=\n"
      ".Ls_s28_%=:\n"
      "s_cmp_eq_u32 s38, 0\n"
      "s_cbranch_scc1 .Ls_o29_%=\n"
      "s_sub_u32 s38, s38, 1\n"
      "s_sleep 2\n"
      "s_nop 7\n"
      "global_load_dwordx4 v[24:27], v60, s[58:59] offset:0 sc1\n"
      "global_load_dwordx4 v[28:31], v60, s[58:59] offset:1024 sc1\n"
      "global_load_dwordx4 v[32:35], v60, s[58:59] offset:2048 sc1\n"
      "global_load_dwordx4 v[36:39], v60, s[58:59] offset:3072 sc1\n"
      "s_waitcnt vmcnt(0)\n"
      "v_bitop3_b32 v61, v25, v27, s37 bitop3:0x7e\n"
      "v_bitop3_b32 v61, v61, v29, s37 bitop3:0xf6\n"
      "v_bitop3_b32 v61, v61, v31, s37 bitop3:0xf6\n"
      "v_bitop3_b32 v61, v61, v33, s37 bitop3:0xf6\n"
      "v_bitop3_b32 v61, v61, v35, s37 bitop3:0xf6\n"
      "v_bitop3_b32 v61, v61, v37, s37 bitop3:0xf6\n"
      "v_bitop3_b32 v61, v61, v39, s37 bitop3:0xf6\n"
      "v_cmp_ne_u32 vcc, 0, v61\n"
      "s_nop 1\n"
      "s_cbranch_vccnz .Ls_s28_%=\n"
      "s_nop 7\n"
      "s_branch .Ls_o29_%=\n"
      ".Ls_s34_%=:\n"
      "s_cmp_eq_u32 s38, 0\n"
      "s_cbranch_scc1 .Ls_o35_%=\n"
      "s_sub_u32 s38, s38, 1\n"
      "s_nop 7\n"
      "global_load_dwordx4 v[224:227], v6, s[62:63] offset:0 sc1\n"
      "global_load_dwordx4 v[228:231], v6, s[62:63] offset:1024 sc1\n"
      "global_load_dwordx4 v[232:235], v6, s[62:63] offset:2048 sc1\n"
      "s_waitcnt vmcnt(0)\n"
      "v_bitop3_b32 v234, v227, v231, s37 bitop3:0x7e\n"
      "v_bitop3_b32 v234, v234, v235, s37 bitop3:0xf6\n"
      "v_cmp_ne_u32 vcc, 0, v234\n"
      "s_nop 1\n"
      "s_cbranch_vccnz .Ls_s34_%=\n"
      "s_nop 7\n"
      "s_branch .Ls_o35_%=\n"
      ".Lend_%=:\n"
      :
      : "s"(__builtin_amdgcn_kernarg_segment_ptr()), "s"(blockIdx.x), "s"(__builtin_amdgcn_readfirstlane(threadIdx.x >> 6)),
        "s"((unsigned)(size_t)smem)
      : "memory", "scc", "vcc",
        "v0", "v1", "v2", "v3", "v4", "v5", "v6", "v7", "v8", "v9", "v10", "v11", "v12", "v13", "v14", "v15", "v16", "v17", "v18", "v19", "v20", "v21", "v22", "v23", "v24", "v25", "v26", "v27", "v28", "v29", "v30", "v31", "v32", "v33", "v34", "v35", "v36", "v37", "v38", "v39", "v40", "v41", "v42", "v43", "v44", "v45", "v46", "v47", "v48", "v49", "v50", "v51", "v52", "v53", "v54", "v55", "v56", "v57", "v58", "v59", "v60", "v61", "v62", "v63", "v64", "v65", "v66", "v67", "v68", "v69", "v70", "v71", "v72", "v73", "v74", "v75", "v76", "v77", "v78", "v79", "v80", "v81", "v82", "v83", "v84", "v85", "v86", "v87", "v88", "v89", "v90", "v91", "v92", "v93", "v94", "v95", "v96", "v97", "v98", "v99", "v100", "v101", "v102", "v103", "v104", "v105", "v106", "v107", "v108", "v109", "v110", "v111", "v112", "v113", "v114", "v115", "v116", "v117", "v118", "v119", "v120", "v121", "v122", "v123", "v124", "v125", "v126", "v127", "v128", "v129", "v130", "v131", "v132", "v133", "v134", "v135", "v136", "v137", "v138", "v139", "v140", "v141", "v142", "v143", "v144", "v145", "v146", "v147", "v148", "v149", "v150", "v151", "v152", "v153", "v154", "v155", "v156", "v157", "v158", "v159", "v160", "v161", "v162", "v163", "v164", "v165", "v166", "v167", "v168", "v169", "v170", "v171", "v172", "v173", "v174", "v175", "v176", "v177", "v178", "v179", "v180", "v181", "v182", "v183", "v184", "v185", "v186", "v187", "v188", "v189", "v190", "v191", "v192", "v193", "v194", "v195", "v196", "v197", "v198", "v199", "v200", "v201", "v202", "v203", "v204", "v205", "v206", "v207", "v208", "v209", "v210", "v211", "v212", "v213", "v214", "v215", "v216", "v217", "v218", "v219", "v220", "v221", "v222", "v223", "v224", "v225", "v226", "v227", "v228", "v229", "v230", "v231", "v232", "v233", "v234", "v235", "v236", "v237", "v238", "v239", "v240", "v241", "v242", "v243", "v244", "v245", "v246", "v247", "v248", "v249", "v250", "v251", "v252", "v253", "v254", "v255",
        "a0", "a1", "a2", "a3", "a4", "a5", "a6", "a7", "a8", "a9", "a10", "a11", "a12", "a13", "a14", "a15", "a16", "a17", "a18", "a19", "a20", "a21", "a22", "a23", "a24", "a25", "a26", "a27", "a28", "a29", "a30", "a31", "a32", "a33", "a34", "a35", "a36", "a37", "a38", "a39", "a40", "a41", "a42", "a43", "a44", "a45", "a46", "a47", "a48", "a49", "a50", "a51", "a52", "a53", "a54", "a55", "a56", "a57", "a58", "a59", "a60", "a61", "a62", "a63", "a64", "a65", "a66", "a67", "a68", "a69", "a70", "a71", "a72", "a73", "a74", "a75", "a76", "a77", "a78", "a79", "a80", "a81", "a82", "a83", "a84", "a85", "a86", "a87", "a88", "a89", "a90", "a91", "a92", "a93", "a94", "a95", "a96", "a97", "a98", "a99", "a100", "a101", "a102", "a103", "a104", "a105", "a106", "a107", "a108", "a109", "a110", "a111", "a112", "a113", "a114", "a115", "a116", "a117", "a118", "a119", "a120", "a121", "a122", "a123", "a124", "a125", "a126", "a127", "a128", "a129", "a130", "a131", "a132", "a133", "a134", "a135", "a136", "a137", "a138", "a139", "a140", "a141", "a142", "a143", "a144", "a145", "a146", "a147", "a148", "a149", "a150", "a151", "a152", "a153", "a154", "a155", "a156", "a157", "a158", "a159", "a160", "a161", "a162", "a163", "a164", "a165", "a166", "a167", "a168", "a169", "a170", "a171", "a172", "a173", "a174", "a175", "a176", "a177", "a178", "a179", "a180", "a181", "a182", "a183", "a184", "a185", "a186", "a187", "a188", "a189", "a190", "a191", "a192", "a193", "a194", "a195", "a196", "a197", "a198", "a199", "a200", "a201", "a202", "a203", "a204", "a205", "a206", "a207", "a208", "a209", "a210", "a211", "a212", "a213", "a214", "a215", "a216", "a217", "a218", "a219", "a220", "a221", "a222", "a223", "a224", "a225", "a226", "a227", "a228", "a229", "a230", "a231", "a232", "a233", "a234", "a235", "a236", "a237", "a238", "a239", "a240", "a241", "a242", "a243", "a244", "a245", "a246", "a247", "a248", "a249", "a250", "a251", "a252", "a253", "a254", "a255",
        "s8", "s9", "s10", "s11", "s12", "s13", "s14", "s15", "s16", "s17", "s18", "s19", "s20", "s21", "s22", "s23", "s24", "s25", "s26", "s27", "s28", "s29", "s30", "s31", "s32", "s33", "s34", "s35", "s36", "s37", "s38", "s39", "s40", "s41", "s42", "s43", "s44", "s45", "s46", "s47", "s48", "s49", "s50", "s51", "s52", "s53", "s54", "s55", "s56", "s57", "s58", "s59", "s60", "s61", "s62", "s63", "s64", "s65", "s66", "s67", "s68", "s69", "s70", "s71", "s72", "s73", "s74", "s75", "s76", "s77", "s78", "s79", "s80", "s81", "s82", "s83", "s84", "s85");
}

extern "C" void kernel_launch(void* const* d_in, const int* in_sizes, int n_in, void* d_out, int out_size, void* d_ws, size_t ws_size,
                              hipStream_t stream) {
  fused_k<<<256, 256, 0, stream>>>((const u16*)d_in[1], (const u16*)d_in[2], (const u16*)d_in[3], (const u16*)d_in[4], (const u16*)d_in[0],
                                   (char*)d_ws, (u16*)d_out, (const u16*)d_in[6], (const u16*)d_in[10], (const u16*)d_in[7],
                                   (const u16*)d_in[8], (const u16*)d_in[9]);
}
```

```cpp
#include <hip/hip_runtime.h>

typedef unsigned short u16;

__global__ __launch_bounds__(256) void fused_k(const u16* wsre, const u16* wsim, const u16* wdre, const u16* wdim, const u16* x, char* ws,
                                               u16* out, const u16* w_r, const u16* gate_w, const u16* w_gate, const u16* w_up,
                                               const u16* w_down) {
  __shared__ __attribute__((aligned(16))) char smem[163840];
  asm volatile(
      ".Lbegin_%=:\n"
      "s_load_dwordx16 s[8:23], %0, 0\n"
      "s_load_dwordx8 s[24:31], %0, 64\n"
      "v_mbcnt_lo_u32_b32 v243, -1, 0\n"
      "s_mov_b32 s32, %1\n"
      "v_mbcnt_hi_u32_b32 v243, -1, v243\n"
      "s_mov_b32 s33, %2\n"
      "s_bfe_u32 s34, s32, 0x20001\n"
      "s_bfe_u32 s35, s32, 0x20003\n"
      "v_lshrrev_b32 v236, 3, v243\n"
      "s_lshr_b32 s43, s32, 5\n"
      "v_xor_b32 v237, v243, v236\n"
      "s_and_b32 s36, s32, 1\n"
      "v_and_b32 v237, 7, v237\n"
      "s_lshl3_add_u32 s36, s36, s43\n"
      "v_lshlrev_b32 v237, 4, v237\n"
      "s_add_u32 s44, s43, s35\n"
      "v_lshlrev_b32 v242, 4, v243\n"
      "s_lshl_b32 s41, s33, 14\n"
      "s_bitcmp1_b32 s44, 0\n"
      "s_cbranch_scc1 .Lstream_%=\n"
      "v_and_b32 v238, 3, v236\n"
      "s_lshr_b32 s43, s43, 1\n"
      "v_and_b32 v239, 4, v236\n"
      "s_add_u32 s80, s43, s35\n"
      "v_lshl_or_b32 v238, v239, 1, v238\n"
      "s_lshl_b32 s80, s80, 8\n"
      "v_lshl_or_b32 v240, v238, 10, v237\n"
      "s_and_b32 s80, s80, 0x300\n"
      "v_add_u32 v241, 15360, v240\n"
      "s_lshr_b32 s83, s36, 1\n"
      "v_add_u32 v254, 2048, v240\n"
      "s_and_b32 s44, s34, 1\n"
      "v_add_u32 v255, 17408, v240\n"
      "s_lshl3_add_u32 s44, s44, s83\n"
      "s_lshl2_add_u32 s40, s44, s33\n"
      "s_lshl_b32 s44, s40, 15\n"
      "s_lshl_b32 s81, s33, 7\n"
      "s_add_u32 s81, s81, s80\n"
      "s_and_b32 s81, s81, 0x380\n"
      "s_lshl_b32 s45, s35, 17\n"
      "s_lshl_b32 s82, s33, 12\n"
      "s_add_u32 s76, s34, 1\n"
      "s_and_b32 s76, s76, 3\n"
      "s_lshl_b32 s76, s76, 15\n"
      "s_or_b32 s76, s76, s81\n"
      "s_add_u32 s76, s76, s45\n"
      "s_add_u32 s77, s34, 2\n"
      "s_and_b32 s77, s77, 3\n"
      "s_lshl_b32 s77, s77, 15\n"
      "s_or_b32 s77, s77, s81\n"
      "s_add_u32 s77, s77, s45\n"
      "s_add_u32 s78, s44, s80\n"
      "s_add_u32 m0, s82, 65536\n"
      "s_bitcmp1_b32 s34, 1\n"
      "s_waitcnt lgkmcnt(0)\n"
      "s_cselect_b64 s[12:13], s[14:15], s[12:13]\n"
      "s_add_u32 s46, s12, s76\n"
      "s_addc_u32 s47, s13, 0\n"
      "global_load_lds_dwordx4 v240, s[46:47] offset:0\n"
      "global_load_lds_dwordx4 v241, s[46:47] offset:1024\n"
      "global_load_lds_dwordx4 v254, s[46:47] offset:2048\n"
      "global_load_lds_dwordx4 v255, s[46:47] offset:3072\n"
      "s_add_u32 s70, s16, s78\n"
      "s_addc_u32 s71, s17, 0\n"
      "s_mov_b32 m0, s41\n"
      "s_add_u32 s46, s12, s77\n"
      "s_addc_u32 s47, s13, 0\n"
      "global_load_lds_dwordx4 v240, s[70:71] offset:0\n"
      "global_load_lds_dwordx4 v241, s[70:71] offset:1024\n"
      "global_load_lds_dwordx4 v254, s[70:71] offset:2048\n"
      "global_load_lds_dwordx4 v255, s[70:71] offset:3072\n"
      "s_add_u32 m0, s82, 98304\n"
      "s_nop 0\n"
      "global_load_lds_dwordx4 v240, s[46:47] offset:0\n"
      "global_load_lds_dwordx4 v241, s[46:47] offset:1024\n"
      "s_add_u32 m0, s41, 3968\n"
      "s_nop 0\n"
      "global_load_lds_dwordx4 v240, s[70:71] offset:128\n"
      "global_load_lds_dwordx4 v241, s[70:71] offset:1152\n"
      "global_load_lds_dwordx4 v254, s[70:71] offset:2176\n"
      "global_load_lds_dwordx4 v255, s[70:71] offset:3200\n"
      "s_branch .Lprod_%=\n"
      ".Lstream_%=:\n"
      "v_lshl_or_b32 v244, v236, 12, v237\n"
      "v_add_u32 v245, 31744, v244\n"
      "v_add_u32 v246, 63488, v244\n"
      "v_add_u32 v247, 95232, v244\n"
      "s_getpc_b64 s[46:47]\n"
      ".Lhere_%=:\n"
      "s_lshr_b32 s44, s32, 4\n"
      "s_lshl2_add_u32 s44, s44, s33\n"
      "s_lshl_b32 s44, s44, 10\n"
      "s_sub_u32 s46, s46, .Lhere_%=-.Lbegin_%=\n"
      "s_subb_u32 s47, s47, 0\n"
      "v_add_u32 v238, s44, v242\n"
      "v_min_u32 v238, .Lend_%=-.Lbegin_%=-16, v238\n"
      "global_load_dwordx4 v[192:195], v238, s[46:47]\n"
      "s_waitcnt lgkmcnt(0)\n"
      "s_bitcmp1_b32 s34, 1\n"
      "s_cselect_b64 s[8:9], s[10:11], s[8:9]\n"
      "s_and_b32 s45, s34, 1\n"
      "s_lshl2_add_u32 s45, s45, s33\n"
      "s_lshl_b32 s45, s45, 9\n"
      "s_lshl_b32 s76, s36, 19\n"
      "s_add_u32 s45, s45, s76\n"
      "s_add_u32 s8, s8, s45\n"
      "s_addc_u32 s9, s9, 0\n"
      "s_lshr_b32 s39, s32, 6\n"
      "s_and_b32 s45, s35, 2\n"
      "s_xor_b32 s39, s39, s45\n"
      "s_xor_b32 s39, s39, s33\n"
      "s_lshl_b32 s45, s39, 7\n"
      "s_add_u32 s76, s33, 0\n"
      "s_and_b32 s76, s76, 3\n"
      "s_lshl_b32 s76, s76, 17\n"
      "s_or_b32 s76, s76, s45\n"
      "s_add_u32 s77, s33, 1\n"
      "s_and_b32 s77, s77, 3\n"
      "s_lshl_b32 s77, s77, 17\n"
      "s_or_b32 s77, s77, s45\n"
      "s_add_u32 s78, s33, 2\n"
      "s_and_b32 s78, s78, 3\n"
      "s_lshl_b32 s78, s78, 17\n"
      "s_or_b32 s78, s78, s45\n"
      "s_add_u32 s79, s33, 3\n"
      "s_and_b32 s79, s79, 3\n"
      "s_lshl_b32 s79, s79, 17\n"
      "s_or_b32 s79, s79, s45\n"
      "s_lshl2_add_u32 s43, s34, s35\n"
      "s_mul_i32 s43, s43, 266240\n"
      "s_add_u32 s43, s43, 262144\n"
      "s_lshl2_add_u32 s44, s36, s35\n"
      "s_mul_i32 s44, s44, 147712\n"
      "s_add_u32 s44, s44, 6438912\n"
      "s_bitcmp1_b32 s33, 0\n"
      "s_cselect_b32 s43, s43, s44\n"
      "s_cselect_b64 s[46:47], s[22:23], s[24:25]\n"
      "s_cselect_b64 s[70:71], s[28:29], s[26:27]\n"
      "s_lshl_b32 s44, s36, 17\n"
      "s_mul_i32 s45, s32, 7168\n"
      "s_bitcmp1_b32 s33, 1\n"
      "s_cselect_b64 s[46:47], s[46:47], s[18:19]\n"
      "s_cselect_b32 s43, 0, s43\n"
      "v_mov_b32 v238, s43\n"
      "global_load_dword v197, v238, s[46:47] sc1\n"
      "s_cmp_eq_u32 s33, 2\n"
      "s_cselect_b64 s[70:71], s[30:31], s[70:71]\n"
      "s_cmp_eq_u32 s33, 3\n"
      "s_cselect_b64 s[70:71], s[20:21], s[70:71]\n"
      "s_cselect_b32 s44, s44, s45\n"
      "v_mov_b32 v239, s44\n"
      "global_load_dword v198, v239, s[70:71] sc1\n"
      "s_sleep 23\n"
      "s_bitcmp1_b32 s34, 1\n"
      "s_cselect_b64 s[46:47], s[14:15], s[12:13]\n"
      "s_min_u32 s44, s33, 2\n"
      "s_add_u32 s44, s44, s34\n"
      "s_add_u32 s44, s44, 2\n"
      "s_and_b32 s44, s44, 3\n"
      "s_lshl2_add_u32 s44, s35, s44\n"
      "s_lshr_b32 s45, s32, 6\n"
      "s_lshl2_add_u32 s44, s44, s45\n"
      "s_lshl_b32 s44, s44, 13\n"
      "v_lshl_add_u32 v238, v243, 7, s44\n"
      "s_cmp_eq_u32 s33, 3\n"
      "s_cbranch_scc1 .Ls_e1_%=\n"
      "global_load_dword v196, v238, s[46:47]\n"
      ".Ls_e1_%=:\n"
      "s_xor_b32 s45, s76, 0\n"
      "s_add_u32 s46, s8, s45\n"
      "s_addc_u32 s47, s9, 0\n"
      "s_add_u32 m0, s41, 0\n"
      "s_nop 0\n"
      "global_load_lds_dwordx4 v244, s[46:47] offset:0\n"
      "global_load_lds_dwordx4 v245, s[46:47] offset:1024\n"
      "global_load_lds_dwordx4 v246, s[46:47] offset:2048\n"
      "global_load_lds_dwordx4 v247, s[46:47] offset:3072\n"
      "s_xor_b32 s45, s77, 0\n"
      "s_add_u32 s46, s8, s45\n"
      "s_addc_u32 s47, s9, 0\n"
      "s_add_u32 m0, s41, 4096\n"
      "s_nop 0\n"
      "global_load_lds_dwordx4 v244, s[46:47] offset:0\n"
      "global_load_lds_dwordx4 v245, s[46:47] offset:1024\n"
      "global_load_lds_dwordx4 v246, s[46:47] offset:2048\n"
      "global_load_lds_dwordx4 v247, s[46:47] offset:3072\n"
      "s_xor_b32 s45, s78, 0\n"
      "s_add_u32 s46, s8, s45\n"
      "s_addc_u32 s47, s9, 0\n"
      "s_add_u32 m0, s41, 8192\n"
      "s_nop 0\n"
      "global_load_lds_dwordx4 v244, s[46:47] offset:0\n"
      "global_load_lds_dwordx4 v245, s[46:47] offset:1024\n"
      "global_load_lds_dwordx4 v246, s[46:47] offset:2048\n"
      "global_load_lds_dwordx4 v247, s[46:47] offset:3072\n"
      "s_add_u32 s58, s18, 4718592\n"
      "s_addc_u32 s59, s19, 0\n"
      "v_mov_b32 v235, 0\n"
      "global_load_dword v235, v235, s[58:59] sc1\n"
      "v_and_b32 v238, 15, v243\n"
      "v_lshrrev_b32 v239, 4, v243\n"
      "v_xor_b32 v239, v239, v238\n"
      "v_and_b32 v239, 7, v239\n"
      "v_lshlrev_b32 v239, 4, v239\n"
      "v_lshl_or_b32 v238, v238, 7, v239\n"
      "v_add_u32 v248, s41, v238\n"
      "v_xor_b32 v249, 64, v248\n"
      "s_lshl2_add_u32 s43, s34, s35\n"
      "s_lshr_b32 s45, s39, 1\n"
      "s_lshl1_add_u32 s45, s33, s45\n"
      "s_lshl_b32 s45, s45, 15\n"
      "s_mul_i32 s44, s43, 266240\n"
      "s_add_u32 s44, s44, 4096\n"
      "s_add_u32 s14, s18, s44\n"
      "s_addc_u32 s15, s19, 0\n"
      "s_add_u32 s10, s14, s45\n"
      "s_addc_u32 s11, s15, 0\n"
      "s_xor_b32 s45, s45, 0x8000\n"
      "s_add_u32 s14, s14, s45\n"
      "s_addc_u32 s15, s15, 0\n"
      "s_and_b32 s44, s39, 1\n"
      "s_lshl_b32 s45, s44, 14\n"
      "v_add_u32 v240, s45, v242\n"
      "v_xor_b32 v241, 0x4000, v240\n"
      "s_add_u32 s72, s10, 0x2000\n"
      "s_addc_u32 s73, s11, 0\n"
      "s_add_u32 s74, s14, 0x2000\n"
      "s_addc_u32 s75, s15, 0\n"
      "s_lshl2_add_u32 s44, s43, s33\n"
      "s_lshl_b32 s44, s44, 12\n"
      "s_add_u32 s44, s44, 1048576\n"
      "v_and_b32 v253, 7, v243\n"
      "v_lshl_add_u32 v253, v253, 9, s44\n"
      "s_lshl2_add_u32 s43, s36, s35\n"
      "s_mul_i32 s43, s43, 147712\n"
      "s_mul_i32 s44, s33, 3072\n"
      "s_add_u32 s43, s43, s44\n"
      "s_add_u32 s43, s43, 6291456\n"
      "s_add_u32 s60, s18, s43\n"
      "s_addc_u32 s61, s19, 0\n"
      "s_mul_i32 s44, s34, 36864\n"
      "s_add_u32 s62, s60, s44\n"
      "s_addc_u32 s63, s61, 0\n"
      "s_lshr_b32 s43, s34, 1\n"
      "s_lshl2_add_u32 s43, s43, s35\n"
      "s_lshl_b32 s43, s43, 12\n"
      "s_add_u32 s43, s43, 4096\n"
      "v_lshl_add_u32 v204, v243, 6, s43\n"
      "s_lshr_b32 s80, s32, 4\n"
      "s_bfe_u32 s43, s32, 0x10002\n"
      "s_lshl1_add_u32 s80, s80, s43\n"
      "s_and_b32 s81, s32, 3\n"
      "s_cmp_lt_u32 s80, 28\n"
      "s_cselect_b32 s82, -1, 0\n"
      "s_lshr_b32 s83, s33, 1\n"
      "s_lshl1_add_u32 s83, s81, s83\n"
      "v_add_u32 v250, 0x10000, v248\n"
      "v_add_u32 v251, 0x10000, v249\n"
      "s_xor_b32 s45, s79, 0\n"
      "s_add_u32 s46, s8, s45\n"
      "s_addc_u32 s47, s9, 0\n"
      "s_add_u32 m0, s41, 12288\n"
      "s_nop 0\n"
      "global_load_lds_dwordx4 v244, s[46:47] offset:0\n"
      "global_load_lds_dwordx4 v245, s[46:47] offset:1024\n"
      "global_load_lds_dwordx4 v246, s[46:47] offset:2048\n"
      "global_load_lds_dwordx4 v247, s[46:47] offset:3072\n"
      "s_xor_b32 s45, s76, 128\n"
      "s_add_u32 s46, s8, s45\n"
      "s_addc_u32 s47, s9, 0\n"
      "s_add_u32 m0, s41, 65536\n"
      "s_nop 0\n"
      "global_load_lds_dwordx4 v244, s[46:47] offset:0\n"
      "global_load_lds_dwordx4 v245, s[46:47] offset:1024\n"
      "global_load_lds_dwordx4 v246, s[46:47] offset:2048\n"
      "global_load_lds_dwordx4 v247, s[46:47] offset:3072\n"
      "s_waitcnt vmcnt(17)\n"
      "ds_read_b128 a[0:3], v248 offset:0\n"
      "ds_read_b128 a[4:7], v248 offset:2048\n"
      "ds_read_b128 a[32:35], v249 offset:0\n"
      "ds_read_b128 a[36:39], v249 offset:2048\n"
      "s_xor_b32 s45, s77, 128\n"
      "s_add_u32 s46, s8, s45\n"
      "s_addc_u32 s47, s9, 0\n"
      "s_add_u32 m0, s41, 69632\n"
      "s_nop 0\n"
      "global_load_lds_dwordx4 v244, s[46:47] offset:0\n"
      "global_load_lds_dwordx4 v245, s[46:47] offset:1024\n"
      "global_load_lds_dwordx4 v246, s[46:47] offset:2048\n"
      "global_load_lds_dwordx4 v247, s[46:47] offset:3072\n"
      "s_waitcnt vmcnt(17)\n"
      "ds_read_b128 a[8:11], v248 offset:4096\n"
      "ds_read_b128 a[12:15], v248 offset:6144\n"
      "ds_read_b128 a[40:43], v249 offset:4096\n"
      "ds_read_b128 a[44:47], v249 offset:6144\n"
      "s_xor_b32 s45, s78, 128\n"
      "s_add_u32 s46, s8, s45\n"
      "s_addc_u32 s47, s9, 0\n"
      "s_add_u32 m0, s41, 73728\n"
      "s_nop 0\n"
      "global_load_lds_dwordx4 v244, s[46:47] offset:0\n"
      "global_load_lds_dwordx4 v245, s[46:47] offset:1024\n"
      "global_load_lds_dwordx4 v246, s[46:47] offset:2048\n"
      "global_load_lds_dwordx4 v247, s[46:47] offset:3072\n"
      "s_waitcnt vmcnt(17)\n"
      "ds_read_b128 a[16:19], v248 offset:8192\n"
      "ds_read_b128 a[20:23], v248 offset:10240\n"
      "ds_read_b128 a[48:51], v249 offset:8192\n"
      "ds_read_b128 a[52:55], v249 offset:10240\n"
      "s_waitcnt vmcnt(16)\n"
      "v_readfirstlane_b32 s37, v235\n"
      "s_nop 1\n"
      "s_mul_i32 s37, s37, 0x9E3779B1\n"
      "s_add_u32 s37, s37, 0x7F4A7C15\n"
      "s_mov_b32 s38, 0x8000\n"
      "s_waitcnt lgkmcnt(8)\n"
      "s_xor_b32 s45, s79, 128\n"
      "s_add_u32 s46, s8, s45\n"
      "s_addc_u32 s47, s9, 0\n"
      "s_add_u32 m0, s41, 0\n"
      "s_nop 0\n"
      "global_load_lds_dwordx4 v244, s[46:47] offset:0\n"
      "global_load_lds_dwordx4 v245, s[46:47] offset:1024\n"
      "global_load_lds_dwordx4 v246, s[46:47] offset:2048\n"
      "global_load_lds_dwordx4 v247, s[46:47] offset:3072\n"
      "s_min_u32 s43, s80, 27\n"
      "v_lshrrev_b32 v202, 3, v243\n"
      "v_and_b32 v203, 0x70, v242\n"
      "v_lshl_or_b32 v200, v202, 10, v203\n"
      "v_add_u32 v201, 7168, v200\n"
      "v_add_u32 v202, 14336, v200\n"
      "v_add_u32 v203, 21504, v200\n"
      "s_lshr_b32 s44, s33, 1\n"
      "s_lshl1_add_u32 s44, s43, s44\n"
      "s_lshl_b32 s44, s44, 15\n"
      "s_and_b32 s45, s33, 1\n"
      "s_lshl1_add_u32 s45, s81, s45\n"
      "s_lshl_b32 s45, s45, 7\n"
      "s_add_u32 s44, s44, s45\n"
      "s_add_u32 s70, s30, s44\n"
      "s_addc_u32 s71, s31, 0\n"
      "s_add_u32 m0, s41, 77824\n"
      "s_nop 0\n"
      "global_load_lds_dwordx4 v200, s[70:71] offset:0\n"
      "global_load_lds_dwordx4 v201, s[70:71] offset:1024\n"
      "global_load_lds_dwordx4 v202, s[70:71] offset:2048\n"
      "global_load_lds_dwordx4 v203, s[70:71] offset:3072\n"
      "s_waitcnt vmcnt(20)\n"
      "ds_read_b128 a[24:27], v248 offset:12288\n"
      "ds_read_b128 a[28:31], v248 offset:14336\n"
      "ds_read_b128 a[56:59], v249 offset:12288\n"
      "ds_read_b128 a[60:63], v249 offset:14336\n"
      "s_waitcnt lgkmcnt(8)\n"
      "s_xor_b32 s45, s76, 256\n"
      "s_add_u32 s46, s8, s45\n"
      "s_addc_u32 s47, s9, 0\n"
      "s_add_u32 m0, s41, 4096\n"
      "s_nop 0\n"
      "global_load_lds_dwordx4 v244, s[46:47] offset:0\n"
      "global_load_lds_dwordx4 v245, s[46:47] offset:1024\n"
      "global_load_lds_dwordx4 v246, s[46:47] offset:2048\n"
      "global_load_lds_dwordx4 v247, s[46:47] offset:3072\n"
      "s_waitcnt vmcnt(20)\n"
      "ds_read_b128 a[64:67], v250 offset:0\n"
      "ds_read_b128 a[68:71], v250 offset:2048\n"
      "ds_read_b128 a[96:99], v251 offset:0\n"
      "ds_read_b128 a[100:103], v251 offset:2048\n"
      "s_waitcnt lgkmcnt(8)\n"
      "s_xor_b32 s45, s77, 256\n"
      "s_add_u32 s46, s8, s45\n"
      "s_addc_u32 s47, s9, 0\n"
      "s_add_u32 m0, s41, 8192\n"
      "s_nop 0\n"
      "global_load_lds_dwordx4 v244, s[46:47] offset:0\n"
      "global_load_lds_dwordx4 v245, s[46:47] offset:1024\n"
      "global_load_lds_dwordx4 v246, s[46:47] offset:2048\n"
      "global_load_lds_dwordx4 v247, s[46:47] offset:3072\n"
      "s_waitcnt vmcnt(20)\n"
      "ds_read_b128 a[72:75], v250 offset:4096\n"
      "ds_read_b128 a[76:79], v250 offset:6144\n"
      "ds_read_b128 a[104:107], v251 offset:4096\n"
      "ds_read_b128 a[108:111], v251 offset:6144\n"
      "s_waitcnt lgkmcnt(8)\n"
      "s_xor_b32 s45, s78, 256\n"
      "s_add_u32 s46, s8, s45\n"
      "s_addc_u32 s47, s9, 0\n"
      "s_add_u32 m0, s41, 12288\n"
      "s_nop 0\n"
      "global_load_lds_dwordx4 v244, s[46:47] offset:0\n"
      "global_load_lds_dwordx4 v245, s[46:47] offset:1024\n"
      "global_load_lds_dwordx4 v246, s[46:47] offset:2048\n"
      "global_load_lds_dwordx4 v247, s[46:47] offset:3072\n"
      "s_waitcnt vmcnt(20)\n"
      "ds_read_b128 a[80:83], v250 offset:8192\n"
      "ds_read_b128 a[84:87], v250 offset:10240\n"
      "ds_read_b128 a[112:115], v251 offset:8192\n"
      "ds_read_b128 a[116:119], v251 offset:10240\n"
      "s_waitcnt vmcnt(16)\n"
      "ds_read_b128 a[88:91], v248 offset:0\n"
      "ds_read_b128 a[92:95], v248 offset:2048\n"
      "ds_read_b128 a[120:123], v249 offset:0\n"
      "ds_read_b128 a[124:127], v249 offset:2048\n"
      "global_load_dword v252, v253, s[58:59] offset:0 sc1\n"
      "s_waitcnt lgkmcnt(12)\n"
      "s_xor_b32 s45, s79, 256\n"
      "s_add_u32 s46, s8, s45\n"
      "s_addc_u32 s47, s9, 0\n"
      "s_add_u32 m0, s41, 65536\n"
      "s_nop 0\n"
      "global_load_lds_dwordx4 v244, s[46:47] offset:0\n"
      "global_load_lds_dwordx4 v245, s[46:47] offset:1024\n"
      "global_load_lds_dwordx4 v246, s[46:47] offset:2048\n"
      "global_load_lds_dwordx4 v247, s[46:47] offset:3072\n"
      "s_waitcnt lgkmcnt(8)\n"
      "s_xor_b32 s45, s76, 384\n"
      "s_add_u32 s46, s8, s45\n"
      "s_addc_u32 s47, s9, 0\n"
      "s_add_u32 m0, s41, 69632\n"
      "s_nop 0\n"
      "global_load_lds_dwordx4 v244, s[46:47] offset:0\n"
      "global_load_lds_dwordx4 v245, s[46:47] offset:1024\n"
      "global_load_lds_dwordx4 v246, s[46:47] offset:2048\n"
      "global_load_lds_dwordx4 v247, s[46:47] offset:3072\n"
      "s_waitcnt vmcnt(17)\n"
      "ds_read_b128 a[128:131], v248 offset:4096\n"
      "ds_read_b128 a[132:135], v248 offset:6144\n"
      "ds_read_b128 a[160:163], v249 offset:4096\n"
      "ds_read_b128 a[164:167], v249 offset:6144\n"
      "s_waitcnt vmcnt(13)\n"
      "ds_read_b128 a[136:139], v248 offset:8192\n"
      "ds_read_b128 a[140:143], v248 offset:10240\n"
      "ds_read_b128 a[168:171], v249 offset:8192\n"
      "ds_read_b128 a[172:175], v249 offset:10240\n"
      "s_waitcnt vmcnt(8)\n"
      "v_cmp_ne_u32 vcc, s37, v252\n"
      "s_nop 1\n"
      "s_cbranch_vccnz .Ls_s3_%=\n"
      ".Ls_o4_%=:\n"
      "global_load_dwordx2 v[206:207], v204, s[58:59] sc1\n"
      "global_load_dwordx4 v[128:131], v240, s[10:11] offset:-4096 sc1\n"
      "global_load_dwordx4 v[132:135], v240, s[10:11] offset:-3072 sc1\n"
      "global_load_dwordx4 v[136:139], v240, s[10:11] offset:-2048 sc1\n"
      "global_load_dwordx4 v[140:143], v240, s[72:73] offset:-4096 sc1\n"
      "global_load_dwordx4 v[144:147], v240, s[72:73] offset:-3072 sc1\n"
      "global_load_dwordx4 v[148:151], v240, s[72:73] offset:-2048 sc1\n"
      "s_waitcnt vmcnt(5)\n"
      "v_mfma_f32_16x16x32_bf16 v[0:3], v[128:131], a[0:3], 0\n"
      "global_load_dwordx4 v[152:155], v241, s[10:11] offset:-4096 sc1\n"
      "v_cmp_ne_u32 vcc, s37, v207\n"
      "s_nop 1\n"
      "s_cbranch_vccnz .Ls_s6_%=\n"
      ".Ls_o7_%=:\n"
      "v_mfma_f32_16x16x32_bf16 v[4:7], v[128:131], a[4:7], 0\n"
      "v_max_u32_dpp v206, v206, v206 quad_perm:[1,0,3,2] row_mask:0xf bank_mask:0xf\n"
      "s_nop 1\n"
      "v_max_u32_dpp v206, v206, v206 quad_perm:[2,3,0,1] row_mask:0xf bank_mask:0xf\n"
      "v_mfma_f32_16x16x32_bf16 v[8:11], v[128:131], a[8:11], 0\n"
      "global_load_dwordx4 v[156:159], v241, s[10:11] offset:-3072 sc1\n"
      "v_max_u32_dpp v206, v206, v206 row_half_mirror row_mask:0xf bank_mask:0xf\n"
      "v_mfma_f32_16x16x32_bf16 v[12:15], v[128:131], a[12:15], 0\n"
      "s_nop 0\n"
      "v_max_u32_dpp v206, v206, v206 row_mirror row_mask:0xf bank_mask:0xf\n"
      "v_mfma_f32_16x16x32_bf16 v[16:19], v[128:131], a[16:19], 0\n"
      "global_load_dwordx4 v[160:163], v241, s[10:11] offset:-2048 sc1\n"
      "v_mov_b32 v205, v206\n"
      "v_mfma_f32_16x16x32_bf16 v[20:23], v[128:131], a[20:23], 0\n"
      "s_nop 0\n"
      "v_permlane16_swap_b32 v206, v205\n"
      "v_mfma_f32_16x16x32_bf16 v[24:27], v[128:131], a[24:27], 0\n"
      "global_load_dwordx4 v[164:167], v241, s[72:73] offset:-4096 sc1\n"
      "v_max_u32 v206, v206, v205\n"
      "v_mfma_f32_16x16x32_bf16 v[28:31], v[128:131], a[28:31], 0\n"
      "v_mov_b32 v205, v206\n"
      "s_nop 1\n"
      "v_permlane32_swap_b32 v206, v205\n"
      "s_waitcnt vmcnt(8)\n"
      "v_mfma_f32_16x16x32_bf16 v[32:35], v[132:135], a[0:3], 0\n"
      "global_load_dwordx4 v[168:171], v241, s[72:73] offset:-3072 sc1\n"
      "v_max_u32 v206, v206, v205\n"
      "v_mfma_f32_16x16x32_bf16 v[36:39], v[132:135], a[4:7], 0\n"
      "v_readfirstlane_b32 s40, v206\n"
      "s_nop 1\n"
      "s_andn2_b32 s42, 0x7ff, s40\n"
      "v_mfma_f32_16x16x32_bf16 v[40:43], v[132:135], a[8:11], 0\n"
      "global_load_dwordx4 v[172:175], v241, s[72:73] offset:-2048 sc1\n"
      "v_mfma_f32_16x16x32_bf16 v[44:47], v[132:135], a[12:15], 0\n"
      "v_mfma_f32_16x16x32_bf16 v[48:51], v[132:135], a[16:19], 0\n"
      "global_load_dwordx4 v[176:179], v240, s[14:15] offset:-4096 sc1\n"
      "v_mfma_f32_16x16x32_bf16 v[52:55], v[132:135], a[20:23], 0\n"
      "v_mfma_f32_16x16x32_bf16 v[56:59], v[132:135], a[24:27], 0\n"
      "global_load_dwordx4 v[180:183], v240, s[14:15] offset:-3072 sc1\n"
      "v_mfma_f32_16x16x32_bf16 v[60:63], v[132:135], a[28:31], 0\n"
      "s_waitcnt vmcnt(10)\n"
      "v_mfma_f32_16x16x32_bf16 v[64:67], v[136:139], a[0:3], 0\n"
      "global_load_dwordx4 v[184:187], v240, s[14:15] offset:-2048 sc1\n"
      "global_load_dwordx4 v[188:191], v240, s[74:75] offset:-4096 sc1\n"
      "v_mfma_f32_16x16x32_bf16 v[68:71], v[136:139], a[4:7], 0\n"
      "global_load_dwordx4 v[128:131], v240, s[74:75] offset:-3072 sc1\n"
      "global_load_dwordx4 v[132:135], v240, s[74:75] offset:-2048 sc1\n"
      "v_mfma_f32_16x16x32_bf16 v[72:75], v[136:139], a[8:11], 0\n"
      "s_waitcnt lgkmcnt(12)\n"
      "s_xor_b32 s70, s77, 384\n"
      "s_add_u32 s70, s8, s70\n"
      "s_addc_u32 s71, s9, 0\n"
      "s_add_u32 m0, s41, 73728\n"
      "v_mfma_f32_16x16x32_bf16 v[76:79], v[136:139], a[12:15], 0\n"
      "global_load_lds_dwordx4 v244, s[70:71] offset:0\n"
      "v_mfma_f32_16x16x32_bf16 v[80:83], v[136:139], a[16:19], 0\n"
      "v_mfma_f32_16x16x32_bf16 v[84:87], v[136:139], a[20:23], 0\n"
      "global_load_lds_dwordx4 v245, s[70:71] offset:1024\n"
      "v_mfma_f32_16x16x32_bf16 v[88:91], v[136:139], a[24:27], 0\n"
      "v_mfma_f32_16x16x32_bf16 v[92:95], v[136:139], a[28:31], 0\n"
      "global_load_lds_dwordx4 v246, s[70:71] offset:2048\n"
      "global_load_dwordx4 v[136:139], v241, s[14:15] offset:-4096 sc1\n"
      "v_mfma_f32_16x16x32_bf16 v[0:3], v[140:143], a[32:35], v[0:3]\n"
      "v_mfma_f32_16x16x32_bf16 v[4:7], v[140:143], a[36:39], v[4:7]\n"
      "global_load_lds_dwordx4 v247, s[70:71] offset:3072\n"
      "v_mfma_f32_16x16x32_bf16 v[8:11], v[140:143], a[40:43], v[8:11]\n"
      "s_waitcnt lgkmcnt(8)\n"
      "s_xor_b32 s70, s78, 384\n"
      "s_add_u32 s70, s8, s70\n"
      "s_addc_u32 s71, s9, 0\n"
      "s_add_u32 m0, s41, 0\n"
      "v_mfma_f32_16x16x32_bf16 v[12:15], v[140:143], a[44:47], v[12:15]\n"
      "global_load_lds_dwordx4 v244, s[70:71] offset:0\n"
      "v_mfma_f32_16x16x32_bf16 v[16:19], v[140:143], a[48:51], v[16:19]\n"
      "v_mfma_f32_16x16x32_bf16 v[20:23], v[140:143], a[52:55], v[20:23]\n"
      "global_load_lds_dwordx4 v245, s[70:71] offset:1024\n"
      "ds_read_b128 a[144:147], v248 offset:12288\n"
      "v_mfma_f32_16x16x32_bf16 v[24:27], v[140:143], a[56:59], v[24:27]\n"
      "ds_read_b128 a[148:151], v248 offset:14336\n"
      "ds_read_b128 a[176:179], v249 offset:12288\n"
      "v_mfma_f32_16x16x32_bf16 v[28:31], v[140:143], a[60:63], v[28:31]\n"
      "s_waitcnt vmcnt(19)\n"
      "global_load_lds_dwordx4 v246, s[70:71] offset:2048\n"
      "global_load_dwordx4 v[140:143], v241, s[74:75] offset:-4096 sc1\n"
      "v_mfma_f32_16x16x32_bf16 v[32:35], v[144:147], a[32:35], v[32:35]\n"
      "ds_read_b128 a[180:183], v249 offset:14336\n"
      "ds_read_b128 a[152:155], v250 offset:0\n"
      "v_mfma_f32_16x16x32_bf16 v[36:39], v[144:147], a[36:39], v[36:39]\n"
      "global_load_lds_dwordx4 v247, s[70:71] offset:3072\n"
      "ds_read_b128 a[156:159], v250 offset:2048\n"
      "v_mfma_f32_16x16x32_bf16 v[40:43], v[144:147], a[40:43], v[40:43]\n"
      "s_waitcnt lgkmcnt(10)\n"
      "s_xor_b32 s70, s79, 384\n"
      "s_add_u32 s70, s8, s70\n"
      "s_addc_u32 s71, s9, 0\n"
      "s_add_u32 m0, s41, 4096\n"
      "v_mfma_f32_16x16x32_bf16 v[44:47], v[144:147], a[44:47], v[44:47]\n"
      "global_load_lds_dwordx4 v244, s[70:71] offset:0\n"
      "ds_read_b128 a[184:187], v251 offset:0\n"
      "v_mfma_f32_16x16x32_bf16 v[48:51], v[144:147], a[48:51], v[48:51]\n"
      "ds_read_b128 a[188:191], v251 offset:2048\n"
      "ds_read_b128 a[192:195], v250 offset:4096\n"
      "v_mfma_f32_16x16x32_bf16 v[52:55], v[144:147], a[52:55], v[52:55]\n"
      "global_load_lds_dwordx4 v245, s[70:71] offset:1024\n"
      "ds_read_b128 a[196:199], v250 offset:6144\n"
      "v_mfma_f32_16x16x32_bf16 v[56:59], v[144:147], a[56:59], v[56:59]\n"
      "ds_read_b128 a[224:227], v251 offset:4096\n"
      "ds_read_b128 a[228:231], v251 offset:6144\n"
      "v_mfma_f32_16x16x32_bf16 v[60:63], v[144:147], a[60:63], v[60:63]\n"
      "global_load_lds_dwordx4 v246, s[70:71] offset:2048\n"
      "global_load_dwordx4 v[144:147], v241, s[14:15] offset:-3072 sc1\n"
      "v_mfma_f32_16x16x32_bf16 v[64:67], v[148:151], a[32:35], v[64:67]\n"
      "v_mfma_f32_16x16x32_bf16 v[68:71], v[148:151], a[36:39], v[68:71]\n"
      "global_load_lds_dwordx4 v247, s[70:71] offset:3072\n"
      "v_mfma_f32_16x16x32_bf16 v[72:75], v[148:151], a[40:43], v[72:75]\n"
      "v_mfma_f32_16x16x32_bf16 v[76:79], v[148:151], a[44:47], v[76:79]\n"
      "v_mfma_f32_16x16x32_bf16 v[80:83], v[148:151], a[48:51], v[80:83]\n"
      "v_mfma_f32_16x16x32_bf16 v[84:87], v[148:151], a[52:55], v[84:87]\n"
      "v_mfma_f32_16x16x32_bf16 v[88:91], v[148:151], a[56:59], v[88:91]\n"
      "v_mfma_f32_16x16x32_bf16 v[92:95], v[148:151], a[60:63], v[92:95]\n"
      "s_waitcnt vmcnt(25)\n"
      "global_load_dwordx4 v[148:151], v241, s[74:75] offset:-3072 sc1\n"
      "v_mfma_f32_16x16x32_bf16 v[0:3], v[152:155], a[64:67], v[0:3]\n"
      "v_mfma_f32_16x16x32_bf16 v[4:7], v[152:155], a[68:71], v[4:7]\n"
      "v_mfma_f32_16x16x32_bf16 v[8:11], v[152:155], a[72:75], v[8:11]\n"
      "v_mfma_f32_16x16x32_bf16 v[12:15], v[152:155], a[76:79], v[12:15]\n"
      "v_mfma_f32_16x16x32_bf16 v[16:19], v[152:155], a[80:83], v[16:19]\n"
      "v_mfma_f32_16x16x32_bf16 v[20:23], v[152:155], a[84:87], v[20:23]\n"
      "v_mfma_f32_16x16x32_bf16 v[24:27], v[152:155], a[88:91], v[24:27]\n"
      "v_mfma_f32_16x16x32_bf16 v[28:31], v[152:155], a[92:95], v[28:31]\n"
      "global_load_dwordx4 v[152:155], v241, s[14:15] offset:-2048 sc1\n"
      "s_waitcnt vmcnt(12)\n"
      "ds_read_b128 a[200:203], v250 offset:8192\n"
      "v_mfma_f32_16x16x32_bf16 v[32:35], v[156:159], a[64:67], v[32:35]\n"
      "ds_read_b128 a[204:207], v250 offset:10240\n"
      "ds_read_b128 a[232:235], v251 offset:8192\n"
      "v_mfma_f32_16x16x32_bf16 v[36:39], v[156:159], a[68:71], v[36:39]\n"
      "ds_read_b128 a[236:239], v251 offset:10240\n"
      "v_mfma_f32_16x16x32_bf16 v[40:43], v[156:159], a[72:75], v[40:43]\n"
      "v_mfma_f32_16x16x32_bf16 v[44:47], v[156:159], a[76:79], v[44:47]\n"
      "v_mfma_f32_16x16x32_bf16 v[48:51], v[156:159], a[80:83], v[48:51]\n"
      "s_lshl_b32 s54, s42, 10\n"
      "s_lshl_b32 s55, s83, 7\n"
      "s_add_u32 s54, s54, s55\n"
      "v_mfma_f32_16x16x32_bf16 v[52:55], v[156:159], a[84:87], v[52:55]\n"
      "v_and_b32 v224, 48, v243\n"
      "v_add_u32 v224, s54, v224\n"
      "v_mfma_f32_16x16x32_bf16 v[56:59], v[156:159], a[88:91], v[56:59]\n"
      "global_load_dwordx4 v[192:195], v224, s[16:17]\n"
      "global_load_dwordx4 v[196:199], v224, s[16:17] offset:64\n"
      "v_mfma_f32_16x16x32_bf16 v[60:63], v[156:159], a[92:95], v[60:63]\n"
      "global_load_dwordx4 v[156:159], v241, s[74:75] offset:-2048 sc1\n"
      "s_waitcnt vmcnt(10)\n"
      "ds_read_b128 a[208:211], v248 offset:0\n"
      "v_mfma_f32_16x16x32_bf16 v[64:67], v[160:163], a[64:67], v[64:67]\n"
      "ds_read_b128 a[212:215], v248 offset:2048\n"
      "ds_read_b128 a[240:243], v249 offset:0\n"
      "v_mfma_f32_16x16x32_bf16 v[68:71], v[160:163], a[68:71], v[68:71]\n"
      "ds_read_b128 a[244:247], v249 offset:2048\n"
      "s_lshr_b32 s43, s42, 5\n"
      "s_lshl2_add_u32 s44, s36, s33\n"
      "s_cmp_eq_u32 s43, s44\n"
      "s_cbranch_scc0 .Ls_e8_%=\n"
      "s_lshl_b32 s43, s42, 10\n"
      "v_add_u32 v208, s43, v242\n"
      "global_load_dwordx4 v[216:219], v208, s[16:17]\n"
      "global_load_dwordx4 v[220:223], v242, s[24:25]\n"
      ".Ls_e8_%=:\n"
      "v_mfma_f32_16x16x32_bf16 v[72:75], v[160:163], a[72:75], v[72:75]\n"
      "v_mfma_f32_16x16x32_bf16 v[76:79], v[160:163], a[76:79], v[76:79]\n"
      "v_mfma_f32_16x16x32_bf16 v[80:83], v[160:163], a[80:83], v[80:83]\n"
      "v_mfma_f32_16x16x32_bf16 v[84:87], v[160:163], a[84:87], v[84:87]\n"
      "v_mfma_f32_16x16x32_bf16 v[88:91], v[160:163], a[88:91], v[88:91]\n"
      "s_min_u32 s43, s80, 27\n"
      "v_lshrrev_b32 v202, 3, v243\n"
      "v_and_b32 v203, 0x70, v242\n"
      "s_movk_i32 s44, 3584\n"
      "v_mad_u32_u24 v200, v202, s44, v203\n"
      "v_add_u32 v201, 27648, v200\n"
      "v_add_u32 v202, 55296, v200\n"
      "v_add_u32 v203, 82944, v200\n"
      "s_bitcmp1_b32 s33, 0\n"
      "s_cselect_b64 s[70:71], s[28:29], s[26:27]\n"
      "s_mul_i32 s44, s83, 229376\n"
      "s_lshl_b32 s45, s43, 7\n"
      "s_add_u32 s44, s44, s45\n"
      "s_add_u32 s70, s70, s44\n"
      "s_addc_u32 s71, s71, 0\n"
      "s_lshl_b32 s45, s33, 13\n"
      "s_add_u32 m0, s45, 131072\n"
      "v_mfma_f32_16x16x32_bf16 v[92:95], v[160:163], a[92:95], v[92:95]\n"
      "global_load_lds_dwordx4 v200, s[70:71] offset:0\n"
      "s_waitcnt vmcnt(6)\n"
      "ds_read_b128 a[216:219], v248 offset:4096\n"
      "v_mfma_f32_16x16x32_bf16 v[0:3], v[164:167], a[96:99], v[0:3]\n"
      "global_load_lds_dwordx4 v201, s[70:71] offset:1024\n"
      "ds_read_b128 a[220:223], v248 offset:6144\n"
      "v_mfma_f32_16x16x32_bf16 v[4:7], v[164:167], a[100:103], v[4:7]\n"
      "global_load_lds_dwordx4 v202, s[70:71] offset:2048\n"
      "ds_read_b128 a[248:251], v249 offset:4096\n"
      "v_mfma_f32_16x16x32_bf16 v[8:11], v[164:167], a[104:107], v[8:11]\n"
      "global_load_lds_dwordx4 v203, s[70:71] offset:3072\n"
      "ds_read_b128 a[252:255], v249 offset:6144\n"
      "v_mfma_f32_16x16x32_bf16 v[12:15], v[164:167], a[108:111], v[12:15]\n"
      "s_add_u32 s70, s70, 114688\n"
      "s_addc_u32 s71, s71, 0\n"
      "s_add_u32 m0, m0, 4096\n"
      "v_mfma_f32_16x16x32_bf16 v[16:19], v[164:167], a[112:115], v[16:19]\n"
      "global_load_lds_dwordx4 v200, s[70:71] offset:0\n"
      "v_mfma_f32_16x16x32_bf16 v[20:23], v[164:167], a[116:119], v[20:23]\n"
      "global_load_lds_dwordx4 v201, s[70:71] offset:1024\n"
      "v_mfma_f32_16x16x32_bf16 v[24:27], v[164:167], a[120:123], v[24:27]\n"
      "global_load_lds_dwordx4 v202, s[70:71] offset:2048\n"
      "v_mfma_f32_16x16x32_bf16 v[28:31], v[164:167], a[124:127], v[28:31]\n"
      "global_load_lds_dwordx4 v203, s[70:71] offset:3072\n"
      "s_sub_u32 s48, s33, 1\n"
      "v_mfma_f32_16x16x32_bf16 v[32:35], v[168:171], a[96:99], v[32:35]\n"
      "s_and_b32 s48, s48, 3\n"
      "global_load_dword v252, v253, s[58:59] offset:128 sc1\n"
      "s_lshl_b32 s48, s48, 14\n"
      "v_mfma_f32_16x16x32_bf16 v[36:39], v[168:171], a[100:103], v[36:39]\n"
      "v_lshl_add_u32 v244, v243, 2, s48\n"
      "s_sub_u32 s48, s33, 2\n"
      "v_mfma_f32_16x16x32_bf16 v[40:43], v[168:171], a[104:107], v[40:43]\n"
      "s_and_b32 s48, s48, 3\n"
      "s_lshl_b32 s48, s48, 14\n"
      "v_lshl_add_u32 v245, v243, 2, s48\n"
      "v_mfma_f32_16x16x32_bf16 v[44:47], v[168:171], a[108:111], v[44:47]\n"
      "s_sub_u32 s48, s33, 3\n"
      "s_and_b32 s48, s48, 3\n"
      "s_lshl_b32 s48, s48, 14\n"
      "v_mfma_f32_16x16x32_bf16 v[48:51], v[168:171], a[112:115], v[48:51]\n"
      "v_lshl_add_u32 v246, v243, 2, s48\n"
      "v_lshrrev_b32 v201, 4, v243\n"
      "v_mfma_f32_16x16x32_bf16 v[52:55], v[168:171], a[116:119], v[52:55]\n"
      "v_bfe_u32 v224, v243, 2, 2\n"
      "v_lshl_or_b32 v201, v201, 3, v224\n"
      "v_mfma_f32_16x16x32_bf16 v[56:59], v[168:171], a[120:123], v[56:59]\n"
      "v_and_b32 v224, 3, v243\n"
      "v_lshlrev_b32 v224, 3, v224\n"
      "v_mfma_f32_16x16x32_bf16 v[60:63], v[168:171], a[124:127], v[60:63]\n"
      "v_lshl_or_b32 v201, v201, 7, v224\n"
      "s_mov_b32 s64, 0xffff0000\n"
      "v_mfma_f32_16x16x32_bf16 v[64:67], v[172:175], a[96:99], v[64:67]\n"
      "s_mov_b32 s65, 0xffff0000\n"
      "s_mov_b32 s66, -1\n"
      "s_mov_b32 s67, 0\n"
      "v_mfma_f32_16x16x32_bf16 v[68:71], v[172:175], a[100:103], v[68:71]\n"
      "s_lshl_b32 s54, s33, 13\n"
      "s_add_u32 s54, s54, 131072\n"
      "v_add_u32 v202, s54, v201\n"
      "v_mfma_f32_16x16x32_bf16 v[72:75], v[172:175], a[104:107], v[72:75]\n"
      "s_and_b32 s54, s33, 1\n"
      "s_lshl3_add_u32 s54, s54, s83\n"
      "s_lshl_b32 s54, s54, 8\n"
      "v_mfma_f32_16x16x32_bf16 v[76:79], v[172:175], a[108:111], v[76:79]\n"
      "s_lshl_b32 s55, s80, 13\n"
      "s_add_u32 s55, s55, 262144\n"
      "s_add_u32 s54, s54, s55\n"
      "v_mfma_f32_16x16x32_bf16 v[80:83], v[172:175], a[112:115], v[80:83]\n"
      "v_bfe_u32 v224, v243, 4, 1\n"
      "v_and_b32 v203, 0xf0, v242\n"
      "v_mfma_f32_16x16x32_bf16 v[84:87], v[172:175], a[116:119], v[84:87]\n"
      "v_lshl_or_b32 v200, v224, 12, v203\n"
      "v_add_u32 v200, s54, v200\n"
      "v_mfma_f32_16x16x32_bf16 v[88:91], v[172:175], a[120:123], v[88:91]\n"
      "v_lshl_or_b32 v203, v224, 14, v203\n"
      "v_mfma_f32_16x16x32_bf16 v[92:95], v[172:175], a[124:127], v[92:95]\n"
      "v_mfma_f32_16x16x32_bf16 v[0:3], v[176:179], a[128:131], v[0:3]\n"
      "v_mfma_f32_16x16x32_bf16 v[4:7], v[176:179], a[132:135], v[4:7]\n"
      "s_waitcnt lgkmcnt(15)\n"
      "v_mfma_f32_16x16x32_bf16 v[8:11], v[176:179], a[136:139], v[8:11]\n"
      "v_mfma_f32_16x16x32_bf16 v[12:15], v[176:179], a[140:143], v[12:15]\n"
      "v_mfma_f32_16x16x32_bf16 v[16:19], v[176:179], a[144:147], v[16:19]\n"
      "v_mfma_f32_16x16x32_bf16 v[20:23], v[176:179], a[148:151], v[20:23]\n"
      "v_mfma_f32_16x16x32_bf16 v[24:27], v[176:179], a[152:155], v[24:27]\n"
      "v_mfma_f32_16x16x32_bf16 v[28:31], v[176:179], a[156:159], v[28:31]\n"
      "v_mfma_f32_16x16x32_bf16 v[32:35], v[180:183], a[128:131], v[32:35]\n"
      "v_mfma_f32_16x16x32_bf16 v[36:39], v[180:183], a[132:135], v[36:39]\n"
      "v_mfma_f32_16x16x32_bf16 v[40:43], v[180:183], a[136:139], v[40:43]\n"
      "v_mfma_f32_16x16x32_bf16 v[44:47], v[180:183], a[140:143], v[44:47]\n"
      "v_mfma_f32_16x16x32_bf16 v[48:51], v[180:183], a[144:147], v[48:51]\n"
      "v_mfma_f32_16x16x32_bf16 v[52:55], v[180:183], a[148:151], v[52:55]\n"
      "v_mfma_f32_16x16x32_bf16 v[56:59], v[180:183], a[152:155], v[56:59]\n"
      "v_mfma_f32_16x16x32_bf16 v[60:63], v[180:183], a[156:159], v[60:63]\n"
      "v_mfma_f32_16x16x32_bf16 v[64:67], v[184:187], a[128:131], v[64:67]\n"
      "v_mfma_f32_16x16x32_bf16 v[68:71], v[184:187], a[132:135], v[68:71]\n"
      "v_mfma_f32_16x16x32_bf16 v[72:75], v[184:187], a[136:139], v[72:75]\n"
      "v_mfma_f32_16x16x32_bf16 v[76:79], v[184:187], a[140:143], v[76:79]\n"
      "v_mfma_f32_16x16x32_bf16 v[80:83], v[184:187], a[144:147], v[80:83]\n"
      "v_mfma_f32_16x16x32_bf16 v[84:87], v[184:187], a[148:151], v[84:87]\n"
      "v_mfma_f32_16x16x32_bf16 v[88:91], v[184:187], a[152:155], v[88:91]\n"
      "v_mfma_f32_16x16x32_bf16 v[92:95], v[184:187], a[156:159], v[92:95]\n"
      "v_mfma_f32_16x16x32_bf16 v[0:3], v[188:191], a[160:163], v[0:3]\n"
      "v_mfma_f32_16x16x32_bf16 v[4:7], v[188:191], a[164:167], v[4:7]\n"
      "v_mfma_f32_16x16x32_bf16 v[8:11], v[188:191], a[168:171], v[8:11]\n"
      "v_mfma_f32_16x16x32_bf16 v[12:15], v[188:191], a[172:175], v[12:15]\n"
      "v_mfma_f32_16x16x32_bf16 v[16:19], v[188:191], a[176:179], v[16:19]\n"
      "v_mfma_f32_16x16x32_bf16 v[20:23], v[188:191], a[180:183], v[20:23]\n"
      "s_lshr_b32 s43, s42, 5\n"
      "s_lshl2_add_u32 s44, s36, s33\n"
      "s_cmp_eq_u32 s43, s44\n"
      "s_cbranch_scc0 .Ls_e9_%=\n"
      "s_ashr_i32 s43, s40, 31\n"
      "s_not_b32 s43, s43\n"
      "s_or_b32 s43, s43, 0x80000000\n"
      "s_xor_b32 s43, s43, s40\n"
      "s_and_b32 s43, s43, 0xffff0000\n"
      "s_waitcnt vmcnt(9)\n"
      "v_lshlrev_b32 v208, 16, v216\n"
      "s_waitcnt vmcnt(9)\n"
      "v_lshlrev_b32 v209, 16, v220\n"
      "v_and_b32 v210, 0xffff0000, v216\n"
      "v_and_b32 v211, 0xffff0000, v220\n"
      "v_mul_f32 v208, v208, v209\n"
      "v_mul_f32 v210, v210, v211\n"
      "v_cvt_pk_bf16_f32 v208, v208, v210\n"
      "v_lshlrev_b32 v209, 16, v208\n"
      "v_and_b32 v208, 0xffff0000, v208\n"
      "v_add_f32 v212, v208, v209\n"
      "v_lshlrev_b32 v208, 16, v217\n"
      "v_lshlrev_b32 v209, 16, v221\n"
      "v_and_b32 v210, 0xffff0000, v217\n"
      "v_and_b32 v211, 0xffff0000, v221\n"
      "v_mul_f32 v208, v208, v209\n"
      "v_mul_f32 v210, v210, v211\n"
      "v_cvt_pk_bf16_f32 v208, v208, v210\n"
      "v_lshlrev_b32 v209, 16, v208\n"
      "v_and_b32 v208, 0xffff0000, v208\n"
      "v_add_f32 v208, v208, v209\n"
      "v_add_f32 v212, v212, v208\n"
      "v_lshlrev_b32 v208, 16, v218\n"
      "v_lshlrev_b32 v209, 16, v222\n"
      "v_and_b32 v210, 0xffff0000, v218\n"
      "v_and_b32 v211, 0xffff0000, v222\n"
      "v_mul_f32 v208, v208, v209\n"
      "v_mul_f32 v210, v210, v211\n"
      "v_cvt_pk_bf16_f32 v208, v208, v210\n"
      "v_lshlrev_b32 v209, 16, v208\n"
      "v_and_b32 v208, 0xffff0000, v208\n"
      "v_add_f32 v208, v208, v209\n"
      "v_add_f32 v212, v212, v208\n"
      "v_lshlrev_b32 v208, 16, v219\n"
      "v_lshlrev_b32 v209, 16, v223\n"
      "v_and_b32 v210, 0xffff0000, v219\n"
      "v_and_b32 v211, 0xffff0000, v223\n"
      "v_mul_f32 v208, v208, v209\n"
      "v_mul_f32 v210, v210, v211\n"
      "v_cvt_pk_bf16_f32 v208, v208, v210\n"
      "v_lshlrev_b32 v209, 16, v208\n"
      "v_and_b32 v208, 0xffff0000, v208\n"
      "v_add_f32 v208, v208, v209\n"
      "v_add_f32 v212, v212, v208\n"
      "s_nop 1\n"
      "v_add_f32_dpp v212, v212, v212 quad_perm:[1,0,3,2] row_mask:0xf bank_mask:0xf\n"
      "s_nop 1\n"
      "v_add_f32_dpp v212, v212, v212 quad_perm:[2,3,0,1] row_mask:0xf bank_mask:0xf\n"
      "s_nop 1\n"
      "v_add_f32_dpp v212, v212, v212 row_half_mirror row_mask:0xf bank_mask:0xf\n"
      "s_nop 1\n"
      "v_add_f32_dpp v212, v212, v212 row_mirror row_mask:0xf bank_mask:0xf\n"
      "v_mov_b32 v208, v212\n"
      "s_nop 1\n"
      "v_permlane16_swap_b32 v212, v208\n"
      "v_add_f32 v212, v212, v208\n"
      "v_mov_b32 v208, v212\n"
      "s_nop 1\n"
      "v_permlane32_swap_b32 v212, v208\n"
      "v_add_f32 v212, v212, v208\n"
      "v_cvt_pk_bf16_f32 v212, v212, v212\n"
      "v_and_b32 v212, 0xffff0000, v212\n"
      "v_writelane_b32 v212, s43, 1\n"
      "v_mul_f32 v212, 0xbfb8aa3b, v212\n"
      "v_exp_f32 v212, v212\n"
      "s_nop 0\n"
      "v_add_f32 v212, 1.0, v212\n"
      "v_rcp_f32 v212, v212\n"
      "s_nop 0\n"
      "v_cvt_pk_bf16_f32 v212, v212, v212\n"
      "v_and_b32 v212, 0xffff0000, v212\n"
      "s_nop 0\n"
      "v_readfirstlane_b32 s39, v212\n"
      "v_readlane_b32 s84, v212, 1\n"
      ".Ls_e9_%=:\n"
      "v_mfma_f32_16x16x32_bf16 v[24:27], v[188:191], a[184:187], v[24:27]\n"
      "v_mfma_f32_16x16x32_bf16 v[28:31], v[188:191], a[188:191], v[28:31]\n"
      "v_mfma_f32_16x16x32_bf16 v[32:35], v[128:131], a[160:163], v[32:35]\n"
      "v_mfma_f32_16x16x32_bf16 v[36:39], v[128:131], a[164:167], v[36:39]\n"
      "s_waitcnt vmcnt(1)\n"
      "ds_read_b64_tr_b16 v[224:225], v202 offset:0\n"
      "v_mfma_f32_16x16x32_bf16 v[40:43], v[128:131], a[168:171], v[40:43]\n"
      "ds_read_b64_tr_b16 v[226:227], v202 offset:512\n"
      "ds_read_b64_tr_b16 v[228:229], v202 offset:4096\n"
      "v_mfma_f32_16x16x32_bf16 v[44:47], v[128:131], a[172:175], v[44:47]\n"
      "ds_read_b64_tr_b16 v[230:231], v202 offset:4608\n"
      "ds_read_b64_tr_b16 v[232:233], v202 offset:64\n"
      "v_mfma_f32_16x16x32_bf16 v[48:51], v[128:131], a[176:179], v[48:51]\n"
      "ds_read_b64_tr_b16 v[234:235], v202 offset:576\n"
      "ds_read_b64_tr_b16 v[236:237], v202 offset:4160\n"
      "v_mfma_f32_16x16x32_bf16 v[52:55], v[128:131], a[180:183], v[52:55]\n"
      "ds_read_b64_tr_b16 v[238:239], v202 offset:4672\n"
      "v_mfma_f32_16x16x32_bf16 v[56:59], v[128:131], a[184:187], v[56:59]\n"
      "v_mfma_f32_16x16x32_bf16 v[60:63], v[128:131], a[188:191], v[60:63]\n"
      "v_mfma_f32_16x16x32_bf16 v[64:67], v[132:135], a[160:163], v[64:67]\n"
      "s_waitcnt lgkmcnt(6)\n"
      "v_mfma_f32_16x16x32_bf16 v[248:251], v[192:195], v[224:227], 0\n"
      "v_mfma_f32_16x16x32_bf16 v[68:71], v[132:135], a[164:167], v[68:71]\n"
      "s_waitcnt lgkmcnt(4)\n"
      "v_mfma_f32_16x16x32_bf16 v[248:251], v[196:199], v[228:231], v[248:251]\n"
      "v_mfma_f32_16x16x32_bf16 v[72:75], v[132:135], a[168:171], v[72:75]\n"
      "s_waitcnt vmcnt(0)\n"
      "v_cmp_ne_u32 vcc, s37, v252\n"
      "s_nop 1\n"
      "s_cbranch_vccnz .Ls_s11_%=\n"
      ".Ls_o12_%=:\n"
      "v_mfma_f32_16x16x32_bf16 v[76:79], v[132:135], a[172:175], v[76:79]\n"
      "s_waitcnt lgkmcnt(2)\n"
      "v_mfma_f32_16x16x32_bf16 v[224:227], v[192:195], v[232:235], 0\n"
      "v_mfma_f32_16x16x32_bf16 v[80:83], v[132:135], a[176:179], v[80:83]\n"
      "global_load_dwordx4 v[160:163], v240, s[10:11] offset:-1024 sc1\n"
      "global_load_dwordx4 v[164:167], v240, s[72:73] offset:-1024 sc1\n"
      "v_mfma_f32_16x16x32_bf16 v[84:87], v[132:135], a[180:183], v[84:87]\n"
      "global_load_dwordx4 v[168:171], v241, s[10:11] offset:-1024 sc1\n"
      "global_load_dwordx4 v[172:175], v241, s[72:73] offset:-1024 sc1\n"
      "v_mfma_f32_16x16x32_bf16 v[88:91], v[132:135], a[184:187], v[88:91]\n"
      "global_load_dwordx4 v[176:179], v240, s[14:15] offset:-1024 sc1\n"
      "global_load_dwordx4 v[180:183], v240, s[74:75] offset:-1024 sc1\n"
      "v_mfma_f32_16x16x32_bf16 v[92:95], v[132:135], a[188:191], v[92:95]\n"
      "global_load_dwordx4 v[184:187], v241, s[14:15] offset:-1024 sc1\n"
      "global_load_dwordx4 v[188:191], v241, s[74:75] offset:-1024 sc1\n"
      "v_mfma_f32_16x16x32_bf16 v[0:3], v[136:139], a[192:195], v[0:3]\n"
      "global_load_dwordx4 v[128:131], v240, s[10:11] offset:0 sc1\n"
      "global_load_dwordx4 v[132:135], v240, s[72:73] offset:0 sc1\n"
      "v_mfma_f32_16x16x32_bf16 v[4:7], v[136:139], a[196:199], v[4:7]\n"
      "s_waitcnt lgkmcnt(0)\n"
      "v_mfma_f32_16x16x32_bf16 v[224:227], v[196:199], v[236:239], v[224:227]\n"
      "v_mfma_f32_16x16x32_bf16 v[8:11], v[136:139], a[200:203], v[8:11]\n"
      "v_mfma_f32_16x16x32_bf16 v[12:15], v[136:139], a[204:207], v[12:15]\n"
      "v_mfma_f32_16x16x32_bf16 v[16:19], v[136:139], a[208:211], v[16:19]\n"
      "s_nop 4\n"
      "v_cndmask_b32 v247, v248, v224, s[64:65]\n"
      "v_mfma_f32_16x16x32_bf16 v[20:23], v[136:139], a[212:215], v[20:23]\n"
      "ds_read_b64_tr_b16 v[224:225], v202 offset:32\n"
      "ds_read_b64_tr_b16 v[226:227], v202 offset:544\n"
      "v_mfma_f32_16x16x32_bf16 v[24:27], v[136:139], a[216:219], v[24:27]\n"
      "ds_read_b64_tr_b16 v[228:229], v202 offset:4128\n"
      "ds_read_b64_tr_b16 v[230:231], v202 offset:4640\n"
      "v_mfma_f32_16x16x32_bf16 v[28:31], v[136:139], a[220:223], v[28:31]\n"
      "global_load_dwordx4 v[136:139], v241, s[10:11] offset:0 sc1\n"
      "ds_read_b64_tr_b16 v[232:233], v202 offset:96\n"
      "v_mfma_f32_16x16x32_bf16 v[0:3], v[140:143], a[224:227], v[0:3]\n"
      "ds_read_b64_tr_b16 v[234:235], v202 offset:608\n"
      "ds_read_b64_tr_b16 v[236:237], v202 offset:4192\n"
      "v_mfma_f32_16x16x32_bf16 v[4:7], v[140:143], a[228:231], v[4:7]\n"
      "ds_read_b64_tr_b16 v[238:239], v202 offset:4704\n"
      "v_mfma_f32_16x16x32_bf16 v[8:11], v[140:143], a[232:235], v[8:11]\n"
      "v_mfma_f32_16x16x32_bf16 v[12:15], v[140:143], a[236:239], v[12:15]\n"
      "v_mfma_f32_16x16x32_bf16 v[16:19], v[140:143], a[240:243], v[16:19]\n"
      "s_waitcnt lgkmcnt(6)\n"
      "v_mfma_f32_16x16x32_bf16 v[248:251], v[192:195], v[224:227], 0\n"
      "v_mfma_f32_16x16x32_bf16 v[20:23], v[140:143], a[244:247], v[20:23]\n"
      "s_waitcnt lgkmcnt(4)\n"
      "v_mfma_f32_16x16x32_bf16 v[248:251], v[196:199], v[228:231], v[248:251]\n"
      "v_mfma_f32_16x16x32_bf16 v[24:27], v[140:143], a[248:251], v[24:27]\n"
      "s_waitcnt lgkmcnt(2)\n"
      "v_mfma_f32_16x16x32_bf16 v[224:227], v[192:195], v[232:235], 0\n"
      "v_mfma_f32_16x16x32_bf16 v[28:31], v[140:143], a[252:255], v[28:31]\n"
      "global_load_dwordx4 v[140:143], v241, s[72:73] offset:0 sc1\n"
      "s_waitcnt lgkmcnt(0)\n"
      "v_mfma_f32_16x16x32_bf16 v[224:227], v[196:199], v[236:239], v[224:227]\n"
      "v_mfma_f32_16x16x32_bf16 v[32:35], v[144:147], a[192:195], v[32:35]\n"
      "v_mfma_f32_16x16x32_bf16 v[36:39], v[144:147], a[196:199], v[36:39]\n"
      "s_mov_b32 m0, s41\n"
      "v_cvt_pk_bf16_f32 v8, v8, v9\n"
      "v_cvt_pk_bf16_f32 v9, v10, v11\n"
      "v_mfma_f32_16x16x32_bf16 v[40:43], v[144:147], a[200:203], v[40:43]\n"
      "ds_write_addtid_b32 v8 offset:0\n"
      "s_nop 0\n"
      "v_cndmask_b32 v250, v248, v224, s[64:65]\n"
      "v_mfma_f32_16x16x32_bf16 v[44:47], v[144:147], a[204:207], v[44:47]\n"
      "v_and_b32 v248, s82, v247\n"
      "v_mov_b32 v249, s37\n"
      "v_mfma_f32_16x16x32_bf16 v[48:51], v[144:147], a[208:211], v[48:51]\n"
      "v_and_b32 v250, s82, v250\n"
      "v_mov_b32 v251, s37\n"
      "v_mfma_f32_16x16x32_bf16 v[52:55], v[144:147], a[212:215], v[52:55]\n"
      "s_mov_b64 exec, s[66:67]\n"
      "global_store_dwordx4 v200, v[248:251], s[58:59] sc1\n"
      "s_mov_b64 exec, -1\n"
      "v_mfma_f32_16x16x32_bf16 v[56:59], v[144:147], a[216:219], v[56:59]\n"
      "v_bfe_u32 v248, v243, 1, 3\n"
      "v_lshrrev_b32 v249, 4, v243\n"
      "v_mfma_f32_16x16x32_bf16 v[60:63], v[144:147], a[220:223], v[60:63]\n"
      "global_load_dwordx4 v[144:147], v240, s[14:15] offset:0 sc1\n"
      "v_lshlrev_b32 v249, 1, v249\n"
      "v_mfma_f32_16x16x32_bf16 v[32:35], v[148:151], a[224:227], v[32:35]\n"
      "v_sub_u32 v248, v248, v249\n"
      "v_and_b32 v249, 16, v242\n"
      "v_mfma_f32_16x16x32_bf16 v[36:39], v[148:151], a[228:231], v[36:39]\n"
      "v_mov_b32 v254, 0x3f80\n"
      "v_lshlrev_b32 v249, v249, v254\n"
      "v_mfma_f32_16x16x32_bf16 v[40:43], v[148:151], a[232:235], v[40:43]\n"
      "v_cmp_eq_u32 vcc, 0, v248\n"
      "s_nop 1\n"
      "v_cndmask_b32 v250, 0, v249, vcc\n"
      "v_mfma_f32_16x16x32_bf16 v[44:47], v[148:151], a[236:239], v[44:47]\n"
      "v_cmp_eq_u32 vcc, 1, v248\n"
      "s_nop 1\n"
      "v_cndmask_b32 v251, 0, v249, vcc\n"
      "v_mfma_f32_16x16x32_bf16 v[48:51], v[148:151], a[240:243], v[48:51]\n"
      "v_mov_b32 v252, v250\n"
      "v_mov_b32 v253, v251\n"
      "v_mfma_f32_16x16x32_bf16 v[52:55], v[148:151], a[244:247], v[52:55]\n"
      "v_mov_b32 v248, 0\n"
      "v_mov_b32 v249, 0\n"
      "v_mfma_f32_16x16x32_bf16 v[56:59], v[148:151], a[248:251], v[56:59]\n"
      "v_mov_b32 v254, 0\n"
      "v_mov_b32 v255, 0\n"
      "v_mfma_f32_16x16x32_bf16 v[60:63], v[148:151], a[252:255], v[60:63]\n"
      "global_load_dwordx4 v[148:151], v240, s[74:75] offset:0 sc1\n"
      "s_lshr_b32 s54, s33, 1\n"
      "v_mfma_f32_16x16x32_bf16 v[64:67], v[152:155], a[192:195], v[64:67]\n"
      "s_lshl1_add_u32 s54, s80, s54\n"
      "s_lshl_b32 s54, s54, 12\n"
      "s_add_u32 s54, s54, 262144\n"
      "v_mfma_f32_16x16x32_bf16 v[68:71], v[152:155], a[196:199], v[68:71]\n"
      "s_add_u32 s48, s34, 1\n"
      "v_add_u32 v200, s54, v242\n"
      "s_add_u32 s54, s41, 77824\n"
      "v_mfma_f32_16x16x32_bf16 v[72:75], v[152:155], a[200:203], v[72:75]\n"
      "s_and_b32 s48, s48, 3\n"
      "v_add_u32 v201, s54, v201\n"
      "v_and_b32 v202, 15, v243\n"
      "v_mfma_f32_16x16x32_bf16 v[76:79], v[152:155], a[204:207], v[76:79]\n"
      "s_mul_i32 s48, s48, 36864\n"
      "v_lshrrev_b32 v199, 5, v243\n"
      "v_lshl_or_b32 v202, v199, 4, v202\n"
      "v_mfma_f32_16x16x32_bf16 v[80:83], v[152:155], a[208:211], v[80:83]\n"
      "s_add_u32 s54, s41, 12288\n"
      "v_lshl_add_u32 v202, v202, 1, s54\n"
      "v_and_b32 v199, 48, v243\n"
      "v_mfma_f32_16x16x32_bf16 v[84:87], v[152:155], a[212:215], v[84:87]\n"
      "s_add_u32 s48, s48, 0\n"
      "v_add_u32 v199, s54, v199\n"
      "s_and_b32 s54, s33, 1\n"
      "v_mfma_f32_16x16x32_bf16 v[88:91], v[152:155], a[216:219], v[88:91]\n"
      "s_lshl1_add_u32 s54, s81, s54\n"
      "s_lshl_b32 s54, s54, 15\n"
      "s_lshr_b32 s55, s33, 1\n"
      "v_mfma_f32_16x16x32_bf16 v[92:95], v[152:155], a[220:223], v[92:95]\n"
      "s_lshl1_add_u32 s55, s80, s55\n"
      "global_load_dwordx4 v[152:155], v241, s[14:15] offset:0 sc1\n"
      "s_lshl_b32 s55, s55, 8\n"
      "v_mfma_f32_16x16x32_bf16 v[64:67], v[156:159], a[224:227], v[64:67]\n"
      "s_add_u32 s54, s54, s55\n"
      "s_add_u32 s54, s54, 524288\n"
      "v_add_u32 v203, s54, v203\n"
      "v_mfma_f32_16x16x32_bf16 v[68:71], v[156:159], a[228:231], v[68:71]\n"
      "s_add_u32 s52, s60, s48\n"
      "s_addc_u32 s53, s61, 0\n"
      "v_cvt_pk_bf16_f32 v10, v12, v13\n"
      "v_mfma_f32_16x16x32_bf16 v[72:75], v[156:159], a[232:235], v[72:75]\n"
      "s_lshl2_add_u32 s54, s36, s33\n"
      "ds_write_addtid_b32 v9 offset:256\n"
      "v_cvt_pk_bf16_f32 v11, v14, v15\n"
      "v_mfma_f32_16x16x32_bf16 v[76:79], v[156:159], a[236:239], v[76:79]\n"
      "s_lshl_b32 s55, s54, 15\n"
      "ds_write_addtid_b32 v10 offset:512\n"
      "v_cvt_pk_bf16_f32 v12, v16, v17\n"
      "v_mfma_f32_16x16x32_bf16 v[80:83], v[156:159], a[240:243], v[80:83]\n"
      "s_lshl2_add_u32 s56, s35, s34\n"
      "ds_write_addtid_b32 v11 offset:768\n"
      "v_cvt_pk_bf16_f32 v13, v18, v19\n"
      "v_mfma_f32_16x16x32_bf16 v[84:87], v[156:159], a[244:247], v[84:87]\n"
      "s_lshl_b32 s56, s56, 6\n"
      "ds_write_addtid_b32 v12 offset:1024\n"
      "v_cvt_pk_bf16_f32 v14, v20, v21\n"
      "v_mfma_f32_16x16x32_bf16 v[88:91], v[156:159], a[248:251], v[88:91]\n"
      "s_add_u32 s55, s55, s56\n"
      "ds_write_addtid_b32 v13 offset:1280\n"
      "v_cvt_pk_bf16_f32 v15, v22, v23\n"
      "v_mfma_f32_16x16x32_bf16 v[92:95], v[156:159], a[252:255], v[92:95]\n"
      "s_waitcnt vmcnt(14)\n"
      "global_load_dwordx4 v[156:159], v241, s[74:75] offset:0 sc1\n"
      "v_and_b32 v209, 15, v243\n"
      "v_mfma_f32_16x16x32_bf16 v[96:99], v[160:163], a[0:3], 0\n"
      "ds_write_addtid_b32 v14 offset:1536\n"
      "v_cvt_pk_bf16_f32 v16, v24, v25\n"
      "v_mfma_f32_16x16x32_bf16 v[100:103], v[160:163], a[4:7], 0\n"
      "ds_write_addtid_b32 v15 offset:1792\n"
      "v_cvt_pk_bf16_f32 v17, v26, v27\n"
      "v_mfma_f32_16x16x32_bf16 v[104:107], v[160:163], a[8:11], 0\n"
      "ds_write_addtid_b32 v16 offset:2048\n"
      "v_cvt_pk_bf16_f32 v18, v28, v29\n"
      "v_mfma_f32_16x16x32_bf16 v[108:111], v[160:163], a[12:15], 0\n"
      "ds_write_addtid_b32 v17 offset:2304\n"
      "v_cvt_pk_bf16_f32 v19, v30, v31\n"
      "v_mfma_f32_16x16x32_bf16 v[112:115], v[160:163], a[16:19], 0\n"
      "ds_write_addtid_b32 v18 offset:2560\n"
      "v_and_b32 v208, 48, v243\n"
      "v_mfma_f32_16x16x32_bf16 v[116:119], v[160:163], a[20:23], 0\n"
      "ds_write_addtid_b32 v19 offset:2816\n"
      "v_cvt_pk_bf16_f32 v40, v40, v41\n"
      "v_mfma_f32_16x16x32_bf16 v[120:123], v[160:163], a[24:27], 0\n"
      "v_cvt_pk_bf16_f32 v41, v42, v43\n"
      "v_lshl_or_b32 v208, v209, 10, v208\n"
      "v_mfma_f32_16x16x32_bf16 v[124:127], v[160:163], a[28:31], 0\n"
      "global_load_dwordx4 v[160:163], v240, s[10:11] offset:1024 sc1\n"
      "v_add_u32 v208, s55, v208\n"
      "v_mfma_f32_16x16x32_bf16 v[96:99], v[164:167], a[32:35], v[96:99]\n"
      "ds_write_addtid_b32 v40 offset:3072\n"
      "v_cvt_pk_bf16_f32 v42, v44, v45\n"
      "v_mfma_f32_16x16x32_bf16 v[100:103], v[164:167], a[36:39], v[100:103]\n"
      "ds_write_addtid_b32 v41 offset:3328\n"
      "v_cvt_pk_bf16_f32 v43, v46, v47\n"
      "v_mfma_f32_16x16x32_bf16 v[104:107], v[164:167], a[40:43], v[104:107]\n"
      "ds_write_addtid_b32 v42 offset:3584\n"
      "v_cvt_pk_bf16_f32 v44, v48, v49\n"
      "v_mfma_f32_16x16x32_bf16 v[108:111], v[164:167], a[44:47], v[108:111]\n"
      "ds_write_addtid_b32 v43 offset:3840\n"
      "v_cvt_pk_bf16_f32 v45, v50, v51\n"
      "v_mfma_f32_16x16x32_bf16 v[112:115], v[164:167], a[48:51], v[112:115]\n"
      "ds_write_addtid_b32 v44 offset:4096\n"
      "v_cvt_pk_bf16_f32 v46, v52, v53\n"
      "v_mfma_f32_16x16x32_bf16 v[116:119], v[164:167], a[52:55], v[116:119]\n"
      "ds_write_addtid_b32 v45 offset:4352\n"
      "v_cvt_pk_bf16_f32 v47, v54, v55\n"
      "v_mfma_f32_16x16x32_bf16 v[120:123], v[164:167], a[56:59], v[120:123]\n"
      "ds_write_addtid_b32 v46 offset:4608\n"
      "v_cvt_pk_bf16_f32 v48, v56, v57\n"
      "v_mfma_f32_16x16x32_bf16 v[124:127], v[164:167], a[60:63], v[124:127]\n"
      "s_waitcnt vmcnt(14)\n"
      "global_load_dwordx4 v[164:167], v240, s[72:73] offset:1024 sc1\n"
      "v_add_u32 v210, 16384, v208\n"
      "v_mfma_f32_16x16x32_bf16 v[96:99], v[168:171], a[64:67], v[96:99]\n"
      "ds_write_addtid_b32 v47 offset:4864\n"
      "v_cvt_pk_bf16_f32 v49, v58, v59\n"
      "v_mfma_f32_16x16x32_bf16 v[100:103], v[168:171], a[68:71], v[100:103]\n"
      "ds_write_addtid_b32 v48 offset:5120\n"
      "v_cvt_pk_bf16_f32 v50, v60, v61\n"
      "v_mfma_f32_16x16x32_bf16 v[104:107], v[168:171], a[72:75], v[104:107]\n"
      "ds_write_addtid_b32 v49 offset:5376\n"
      "v_cvt_pk_bf16_f32 v51, v62, v63\n"
      "v_mfma_f32_16x16x32_bf16 v[108:111], v[168:171], a[76:79], v[108:111]\n"
      "ds_write_addtid_b32 v50 offset:5632\n"
      "ds_write_addtid_b32 v51 offset:5888\n"
      "v_mfma_f32_16x16x32_bf16 v[112:115], v[168:171], a[80:83], v[112:115]\n"
      "global_load_dwordx4 v[216:219], v208, s[16:17]\n"
      "global_load_dwordx4 v[220:223], v210, s[16:17]\n"
      "v_mfma_f32_16x16x32_bf16 v[116:119], v[168:171], a[84:87], v[116:119]\n"
      "s_lshr_b32 s55, s34, 1\n"
      "s_lshl2_add_u32 s55, s55, s35\n"
      "s_lshl_b32 s55, s55, 15\n"
      "v_mfma_f32_16x16x32_bf16 v[120:123], v[168:171], a[88:91], v[120:123]\n"
      "s_lshl_b32 s54, s54, 9\n"
      "s_waitcnt lgkmcnt(0)\n"
      "s_barrier\n"
      "v_mfma_f32_16x16x32_bf16 v[124:127], v[168:171], a[92:95], v[124:127]\n"
      "s_add_u32 s54, s54, s55\n"
      "global_load_dwordx4 v[168:171], v241, s[10:11] offset:1024 sc1\n"
      "s_add_u32 s54, s54, 786432\n"
      "v_mfma_f32_16x16x32_bf16 v[96:99], v[172:175], a[96:99], v[96:99]\n"
      "ds_read2st64_b32 v[20:21], v244 offset0:0 offset1:1\n"
      "v_lshl_add_u32 v209, v209, 4, s54\n"
      "v_mfma_f32_16x16x32_bf16 v[100:103], v[172:175], a[100:103], v[100:103]\n"
      "ds_read2st64_b32 v[22:23], v245 offset0:4 offset1:5\n"
      "ds_read2st64_b32 v[24:25], v244 offset0:2 offset1:3\n"
      "v_mfma_f32_16x16x32_bf16 v[104:107], v[172:175], a[104:107], v[104:107]\n"
      "ds_read2st64_b32 v[26:27], v245 offset0:6 offset1:7\n"
      "ds_read2st64_b32 v[28:29], v246 offset0:8 offset1:9\n"
      "v_mfma_f32_16x16x32_bf16 v[108:111], v[172:175], a[108:111], v[108:111]\n"
      "ds_read2st64_b32 v[30:31], v246 offset0:10 offset1:11\n"
      "ds_read2st64_b32 v[52:53], v244 offset0:12 offset1:13\n"
      "v_mfma_f32_16x16x32_bf16 v[112:115], v[172:175], a[112:115], v[112:115]\n"
      "ds_read2st64_b32 v[54:55], v245 offset0:16 offset1:17\n"
      "ds_read2st64_b32 v[56:57], v244 offset0:14 offset1:15\n"
      "v_mfma_f32_16x16x32_bf16 v[116:119], v[172:175], a[116:119], v[116:119]\n"
      "ds_read2st64_b32 v[58:59], v245 offset0:18 offset1:19\n"
      "ds_read2st64_b32 v[60:61], v246 offset0:20 offset1:21\n"
      "v_mfma_f32_16x16x32_bf16 v[120:123], v[172:175], a[120:123], v[120:123]\n"
      "ds_read2st64_b32 v[62:63], v246 offset0:22 offset1:23\n"
      "v_cvt_pk_bf16_f32 v72, v72, v73\n"
      "v_mfma_f32_16x16x32_bf16 v[124:127], v[172:175], a[124:127], v[124:127]\n"
      "s_waitcnt vmcnt(16)\n"
      "global_load_dwordx4 v[172:175], v241, s[72:73] offset:1024 sc1\n"
      "v_cvt_pk_bf16_f32 v73, v74, v75\n"
      "v_mfma_f32_16x16x32_bf16 v[96:99], v[176:179], a[128:131], v[96:99]\n"
      "s_waitcnt lgkmcnt(6)\n"
      "v_mfma_f32_16x16x32_bf16 v[0:3], v[250:253], v[20:23], v[0:3]\n"
      "v_mfma_f32_16x16x32_bf16 v[4:7], v[250:253], v[24:27], v[4:7]\n"
      "v_mfma_f32_16x16x32_bf16 v[100:103], v[176:179], a[132:135], v[100:103]\n"
      "v_mfma_f32_16x16x32_bf16 v[0:3], v[252:255], v[28:31], v[0:3]\n"
      "v_mfma_f32_16x16x32_bf16 v[4:7], v[248:251], v[28:31], v[4:7]\n"
      "v_mfma_f32_16x16x32_bf16 v[104:107], v[176:179], a[136:139], v[104:107]\n"
      "ds_write_addtid_b32 v72 offset:6144\n"
      "v_mov_b32 v11, s37\n"
      "v_mfma_f32_16x16x32_bf16 v[108:111], v[176:179], a[140:143], v[108:111]\n"
      "global_load_dwordx2 v[204:205], v209, s[58:59] offset:0 sc1\n"
      "v_cvt_pk_bf16_f32 v74, v76, v77\n"
      "v_mfma_f32_16x16x32_bf16 v[112:115], v[176:179], a[144:147], v[112:115]\n"
      "ds_write_addtid_b32 v73 offset:6400\n"
      "v_cvt_pk_bf16_f32 v8, v0, v1\n"
      "v_mfma_f32_16x16x32_bf16 v[116:119], v[176:179], a[148:151], v[116:119]\n"
      "global_load_dwordx2 v[206:207], v209, s[58:59] offset:256 sc1\n"
      "v_cvt_pk_bf16_f32 v9, v2, v3\n"
      "v_mfma_f32_16x16x32_bf16 v[120:123], v[176:179], a[152:155], v[120:123]\n"
      "s_add_u32 s68, s20, 16384\n"
      "s_addc_u32 s69, s21, 0\n"
      "v_cvt_pk_bf16_f32 v10, v4, v5\n"
      "v_mfma_f32_16x16x32_bf16 v[124:127], v[176:179], a[156:159], v[124:127]\n"
      "global_load_dwordx4 v[176:179], v240, s[14:15] offset:1024 sc1\n"
      "v_cvt_pk_bf16_f32 v247, v6, v7\n"
      "v_mfma_f32_16x16x32_bf16 v[96:99], v[180:183], a[160:163], v[96:99]\n"
      "global_store_dwordx4 v242, v[8:11], s[52:53] offset:0 sc1\n"
      "s_waitcnt lgkmcnt(2)\n"
      "v_mfma_f32_16x16x32_bf16 v[100:103], v[180:183], a[164:167], v[100:103]\n"
      "v_mfma_f32_16x16x32_bf16 v[32:35], v[250:253], v[52:55], v[32:35]\n"
      "v_cvt_pk_bf16_f32 v75, v78, v79\n"
      "v_mfma_f32_16x16x32_bf16 v[104:107], v[180:183], a[168:171], v[104:107]\n"
      "v_mfma_f32_16x16x32_bf16 v[36:39], v[250:253], v[56:59], v[36:39]\n"
      "v_lshrrev_b32 v208, 10, v208\n"
      "v_mfma_f32_16x16x32_bf16 v[108:111], v[180:183], a[172:175], v[108:111]\n"
      "v_mfma_f32_16x16x32_bf16 v[32:35], v[252:255], v[60:63], v[32:35]\n"
      "v_subrev_u32 v208, s42, v208\n"
      "v_mfma_f32_16x16x32_bf16 v[112:115], v[180:183], a[176:179], v[112:115]\n"
      "v_mfma_f32_16x16x32_bf16 v[36:39], v[248:251], v[60:63], v[36:39]\n"
      "v_mov_b32 v43, s37\n"
      "v_mfma_f32_16x16x32_bf16 v[116:119], v[180:183], a[180:183], v[116:119]\n"
      "ds_write_addtid_b32 v74 offset:6656\n"
      "v_mov_b32 v44, v247\n"
      "v_mfma_f32_16x16x32_bf16 v[120:123], v[180:183], a[184:187], v[120:123]\n"
      "v_mov_b32 v47, s37\n"
      "v_cvt_pk_bf16_f32 v76, v80, v81\n"
      "v_mfma_f32_16x16x32_bf16 v[124:127], v[180:183], a[188:191], v[124:127]\n"
      "s_waitcnt vmcnt(19)\n"
      "global_load_dwordx4 v[180:183], v240, s[74:75] offset:1024 sc1\n"
      "v_mfma_f32_16x16x32_bf16 v[96:99], v[184:187], a[192:195], v[96:99]\n"
      "ds_write_addtid_b32 v75 offset:6912\n"
      "v_cvt_pk_bf16_f32 v40, v32, v33\n"
      "v_mfma_f32_16x16x32_bf16 v[100:103], v[184:187], a[196:199], v[100:103]\n"
      "v_cvt_pk_bf16_f32 v41, v34, v35\n"
      "v_cvt_pk_bf16_f32 v42, v36, v37\n"
      "v_mfma_f32_16x16x32_bf16 v[104:107], v[184:187], a[200:203], v[104:107]\n"
      "v_cvt_pk_bf16_f32 v45, v38, v39\n"
      "v_cvt_pk_bf16_f32 v77, v82, v83\n"
      "v_mfma_f32_16x16x32_bf16 v[108:111], v[184:187], a[204:207], v[108:111]\n"
      "global_store_dwordx4 v242, v[40:43], s[52:53] offset:1024 sc1\n"
      "v_mfma_f32_16x16x32_bf16 v[112:115], v[184:187], a[208:211], v[112:115]\n"
      "global_store_dwordx4 v242, v[44:47], s[52:53] offset:2048 sc1\n"
      "s_add_u32 s48, s34, 2\n"
      "v_mfma_f32_16x16x32_bf16 v[116:119], v[184:187], a[212:215], v[116:119]\n"
      "s_and_b32 s48, s48, 3\n"
      "ds_write_addtid_b32 v76 offset:7168\n"
      "s_mul_i32 s48, s48, 36864\n"
      "v_mfma_f32_16x16x32_bf16 v[120:123], v[184:187], a[216:219], v[120:123]\n"
      "s_add_u32 s48, s48, 12288\n"
      "s_add_u32 s52, s60, s48\n"
      "s_addc_u32 s53, s61, 0\n"
      "v_mfma_f32_16x16x32_bf16 v[124:127], v[184:187], a[220:223], v[124:127]\n"
      "global_load_dwordx4 v[184:187], v241, s[14:15] offset:1024 sc1\n"
      "v_cvt_pk_bf16_f32 v78, v84, v85\n"
      "v_mfma_f32_16x16x32_bf16 v[96:99], v[188:191], a[224:227], v[96:99]\n"
      "ds_write_addtid_b32 v77 offset:7424\n"
      "v_cvt_pk_bf16_f32 v79, v86, v87\n"
      "v_mfma_f32_16x16x32_bf16 v[100:103], v[188:191], a[228:231], v[100:103]\n"
      "ds_write_addtid_b32 v78 offset:7680\n"
      "v_cvt_pk_bf16_f32 v80, v88, v89\n"
      "v_mfma_f32_16x16x32_bf16 v[104:107], v[188:191], a[232:235], v[104:107]\n"
      "ds_write_addtid_b32 v79 offset:7936\n"
      "v_cvt_pk_bf16_f32 v81, v90, v91\n"
      "v_mfma_f32_16x16x32_bf16 v[108:111], v[188:191], a[236:239], v[108:111]\n"
      "ds_write_addtid_b32 v80 offset:8192\n"
      "v_cvt_pk_bf16_f32 v82, v92, v93\n"
      "v_mfma_f32_16x16x32_bf16 v[112:115], v[188:191], a[240:243], v[112:115]\n"
      "ds_write_addtid_b32 v81 offset:8448\n"
      "v_cvt_pk_bf16_f32 v83, v94, v95\n"
      "v_mfma_f32_16x16x32_bf16 v[116:119], v[188:191], a[244:247], v[116:119]\n"
      "ds_write_addtid_b32 v82 offset:8704\n"
      "v_mfma_f32_16x16x32_bf16 v[120:123], v[188:191], a[248:251], v[120:123]\n"
      "ds_write_addtid_b32 v83 offset:8960\n"
      "v_mfma_f32_16x16x32_bf16 v[124:127], v[188:191], a[252:255], v[124:127]\n"
      "s_waitcnt vmcnt(21)\n"
      "global_load_dwordx4 v[188:191], v241, s[74:75] offset:1024 sc1\n"
      "v_mfma_f32_16x16x32_bf16 v[0:3], v[128:131], a[0:3], 0\n"
      "v_mfma_f32_16x16x32_bf16 v[4:7], v[128:131], a[4:7], 0\n"
      "v_cvt_pk_bf16_f32 v104, v104, v105\n"
      "v_cvt_pk_bf16_f32 v105, v106, v107\n"
      "v_mfma_f32_16x16x32_bf16 v[8:11], v[128:131], a[8:11], 0\n"
      "ds_write_addtid_b32 v104 offset:9216\n"
      "v_cvt_pk_bf16_f32 v106, v108, v109\n"
      "v_mfma_f32_16x16x32_bf16 v[12:15], v[128:131], a[12:15], 0\n"
      "ds_write_addtid_b32 v105 offset:9472\n"
      "v_cvt_pk_bf16_f32 v107, v110, v111\n"
      "v_mfma_f32_16x16x32_bf16 v[16:19], v[128:131], a[16:19], 0\n"
      "ds_write_addtid_b32 v106 offset:9728\n"
      "v_cvt_pk_bf16_f32 v108, v112, v113\n"
      "v_mfma_f32_16x16x32_bf16 v[20:23], v[128:131], a[20:23], 0\n"
      "ds_write_addtid_b32 v107 offset:9984\n"
      "v_cvt_pk_bf16_f32 v109, v114, v115\n"
      "v_mfma_f32_16x16x32_bf16 v[24:27], v[128:131], a[24:27], 0\n"
      "ds_write_addtid_b32 v108 offset:10240\n"
      "v_cvt_pk_bf16_f32 v110, v116, v117\n"
      "v_mfma_f32_16x16x32_bf16 v[28:31], v[128:131], a[28:31], 0\n"
      "global_load_dwordx4 v[128:131], v240, s[10:11] offset:2048 sc1\n"
      "v_mfma_f32_16x16x32_bf16 v[0:3], v[132:135], a[32:35], v[0:3]\n"
      "ds_write_addtid_b32 v109 offset:10496\n"
      "v_cvt_pk_bf16_f32 v111, v118, v119\n"
      "v_mfma_f32_16x16x32_bf16 v[4:7], v[132:135], a[36:39], v[4:7]\n"
      "ds_write_addtid_b32 v110 offset:10752\n"
      "v_cvt_pk_bf16_f32 v112, v120, v121\n"
      "v_mfma_f32_16x16x32_bf16 v[8:11], v[132:135], a[40:43], v[8:11]\n"
      "ds_write_addtid_b32 v111 offset:11008\n"
      "v_cvt_pk_bf16_f32 v113, v122, v123\n"
      "v_mfma_f32_16x16x32_bf16 v[12:15], v[132:135], a[44:47], v[12:15]\n"
      "ds_write_addtid_b32 v112 offset:11264\n"
      "v_cvt_pk_bf16_f32 v114, v124, v125\n"
      "v_mfma_f32_16x16x32_bf16 v[16:19], v[132:135], a[48:51], v[16:19]\n"
      "ds_write_addtid_b32 v113 offset:11520\n"
      "v_cvt_pk_bf16_f32 v115, v126, v127\n"
      "v_mfma_f32_16x16x32_bf16 v[20:23], v[132:135], a[52:55], v[20:23]\n"
      "ds_write_addtid_b32 v114 offset:11776\n"
      "v_mfma_f32_16x16x32_bf16 v[24:27], v[132:135], a[56:59], v[24:27]\n"
      "ds_write_addtid_b32 v115 offset:12032\n"
      "v_mfma_f32_16x16x32_bf16 v[28:31], v[132:135], a[60:63], v[28:31]\n"
      "s_waitcnt vmcnt(21)\n"
      "global_load_dwordx4 v[132:135], v240, s[72:73] offset:2048 sc1\n"
      "v_mfma_f32_16x16x32_bf16 v[0:3], v[136:139], a[64:67], v[0:3]\n"
      "v_mfma_f32_16x16x32_bf16 v[4:7], v[136:139], a[68:71], v[4:7]\n"
      "s_waitcnt lgkmcnt(0)\n"
      "s_barrier\n"
      "v_mfma_f32_16x16x32_bf16 v[8:11], v[136:139], a[72:75], v[8:11]\n"
      "ds_read2st64_b32 v[84:85], v244 offset0:24 offset1:25\n"
      "v_mfma_f32_16x16x32_bf16 v[12:15], v[136:139], a[76:79], v[12:15]\n"
      "ds_read2st64_b32 v[86:87], v245 offset0:28 offset1:29\n"
      "v_mfma_f32_16x16x32_bf16 v[16:19], v[136:139], a[80:83], v[16:19]\n"
      "ds_read2st64_b32 v[88:89], v244 offset0:26 offset1:27\n"
      "v_mfma_f32_16x16x32_bf16 v[20:23], v[136:139], a[84:87], v[20:23]\n"
      "ds_read2st64_b32 v[90:91], v245 offset0:30 offset1:31\n"
      "v_mfma_f32_16x16x32_bf16 v[24:27], v[136:139], a[88:91], v[24:27]\n"
      "ds_read2st64_b32 v[92:93], v246 offset0:32 offset1:33\n"
      "v_mfma_f32_16x16x32_bf16 v[28:31], v[136:139], a[92:95], v[28:31]\n"
      "global_load_dwordx4 v[136:139], v241, s[10:11] offset:2048 sc1\n"
      "v_mfma_f32_16x16x32_bf16 v[0:3], v[140:143], a[96:99], v[0:3]\n"
      "ds_read2st64_b32 v[94:95], v246 offset0:34 offset1:35\n"
      "v_mfma_f32_16x16x32_bf16 v[4:7], v[140:143], a[100:103], v[4:7]\n"
      "ds_read2st64_b32 v[116:117], v244 offset0:36 offset1:37\n"
      "v_mfma_f32_16x16x32_bf16 v[8:11], v[140:143], a[104:107], v[8:11]\n"
      "ds_read2st64_b32 v[118:119], v245 offset0:40 offset1:41\n"
      "v_mfma_f32_16x16x32_bf16 v[12:15], v[140:143], a[108:111], v[12:15]\n"
      "ds_read2st64_b32 v[120:121], v244 offset0:38 offset1:39\n"
      "v_mfma_f32_16x16x32_bf16 v[16:19], v[140:143], a[112:115], v[16:19]\n"
      "ds_read2st64_b32 v[122:123], v245 offset0:42 offset1:43\n"
      "v_mfma_f32_16x16x32_bf16 v[20:23], v[140:143], a[116:119], v[20:23]\n"
      "s_waitcnt lgkmcnt(4)\n"
      "v_mfma_f32_16x16x32_bf16 v[64:67], v[250:253], v[84:87], v[64:67]\n"
      "v_mfma_f32_16x16x32_bf16 v[24:27], v[140:143], a[120:123], v[24:27]\n"
      "v_mfma_f32_16x16x32_bf16 v[68:71], v[250:253], v[88:91], v[68:71]\n"
      "v_mfma_f32_16x16x32_bf16 v[28:31], v[140:143], a[124:127], v[28:31]\n"
      "s_waitcnt vmcnt(20)\n"
      "global_load_dwordx4 v[140:143], v241, s[72:73] offset:2048 sc1\n"
      "v_mfma_f32_16x16x32_bf16 v[0:3], v[144:147], a[128:131], v[0:3]\n"
      "v_mfma_f32_16x16x32_bf16 v[64:67], v[252:255], v[92:95], v[64:67]\n"
      "v_mfma_f32_16x16x32_bf16 v[4:7], v[144:147], a[132:135], v[4:7]\n"
      "v_mfma_f32_16x16x32_bf16 v[68:71], v[248:251], v[92:95], v[68:71]\n"
      "v_mov_b32 v75, s37\n"
      "v_mfma_f32_16x16x32_bf16 v[8:11], v[144:147], a[136:139], v[8:11]\n"
      "ds_read2st64_b32 v[124:125], v246 offset0:44 offset1:45\n"
      "v_mfma_f32_16x16x32_bf16 v[12:15], v[144:147], a[140:143], v[12:15]\n"
      "ds_read2st64_b32 v[126:127], v246 offset0:46 offset1:47\n"
      "s_nop 0\n"
      "v_cvt_pk_bf16_f32 v72, v64, v65\n"
      "v_mfma_f32_16x16x32_bf16 v[16:19], v[144:147], a[144:147], v[16:19]\n"
      "s_waitcnt vmcnt(11)\n"
      "v_bitop3_b32 v210, v205, v207, s37 bitop3:0x7e\n"
      "v_cmp_ne_u32 vcc, 0, v210\n"
      "s_nop 1\n"
      "s_cbranch_vccnz .Ls_s14_%=\n"
      ".Ls_o15_%=:\n"
      "v_mfma_f32_16x16x32_bf16 v[20:23], v[144:147], a[148:151], v[20:23]\n"
      "v_cvt_pk_bf16_f32 v73, v66, v67\n"
      "v_cvt_pk_bf16_f32 v74, v68, v69\n"
      "v_mfma_f32_16x16x32_bf16 v[24:27], v[144:147], a[152:155], v[24:27]\n"
      "v_cvt_pk_bf16_f32 v247, v70, v71\n"
      "v_mov_b32 v209, s39\n"
      "v_mfma_f32_16x16x32_bf16 v[28:31], v[144:147], a[156:159], v[28:31]\n"
      "global_load_dwordx4 v[144:147], v240, s[14:15] offset:2048 sc1\n"
      "v_mfma_f32_16x16x32_bf16 v[0:3], v[148:151], a[160:163], v[0:3]\n"
      "global_store_dwordx4 v242, v[72:75], s[52:53] offset:0 sc1\n"
      "s_waitcnt lgkmcnt(0)\n"
      "v_mfma_f32_16x16x32_bf16 v[4:7], v[148:151], a[164:167], v[4:7]\n"
      "v_mfma_f32_16x16x32_bf16 v[96:99], v[250:253], v[116:119], v[96:99]\n"
      "v_mfma_f32_16x16x32_bf16 v[8:11], v[148:151], a[168:171], v[8:11]\n"
      "v_mfma_f32_16x16x32_bf16 v[100:103], v[250:253], v[120:123], v[100:103]\n"
      "v_mfma_f32_16x16x32_bf16 v[12:15], v[148:151], a[172:175], v[12:15]\n"
      "v_mfma_f32_16x16x32_bf16 v[96:99], v[252:255], v[124:127], v[96:99]\n"
      "v_mfma_f32_16x16x32_bf16 v[16:19], v[148:151], a[176:179], v[16:19]\n"
      "v_mfma_f32_16x16x32_bf16 v[100:103], v[248:251], v[124:127], v[100:103]\n"
      "v_mov_b32 v107, s37\n"
      "v_mfma_f32_16x16x32_bf16 v[20:23], v[148:151], a[180:183], v[20:23]\n"
      "v_cmp_eq_u32 vcc, 0, v208\n"
      "s_nop 1\n"
      "v_cndmask_b32 v204, v204, v209, vcc\n"
      "v_mfma_f32_16x16x32_bf16 v[24:27], v[148:151], a[184:187], v[24:27]\n"
      "v_cmp_eq_u32 vcc, -16, v208\n"
      "s_nop 1\n"
      "v_cndmask_b32 v206, v206, v209, vcc\n"
      "v_mfma_f32_16x16x32_bf16 v[28:31], v[148:151], a[188:191], v[28:31]\n"
      "global_load_dwordx4 v[148:151], v240, s[74:75] offset:2048 sc1\n"
      "v_mov_b32 v108, v247\n"
      "v_mfma_f32_16x16x32_bf16 v[0:3], v[152:155], a[192:195], v[0:3]\n"
      "v_mov_b32 v111, s37\n"
      "v_sub_f32 v205, 1.0, v204\n"
      "v_mfma_f32_16x16x32_bf16 v[4:7], v[152:155], a[196:199], v[4:7]\n"
      "global_load_dwordx4 v[224:227], v200, s[58:59] offset:0 sc1\n"
      "v_cvt_pk_bf16_f32 v205, v205, v205\n"
      "v_mfma_f32_16x16x32_bf16 v[8:11], v[152:155], a[200:203], v[8:11]\n"
      "global_load_dwordx4 v[228:231], v200, s[58:59] offset:1024 sc1\n"
      "v_cvt_pk_bf16_f32 v104, v96, v97\n"
      "v_mfma_f32_16x16x32_bf16 v[12:15], v[152:155], a[204:207], v[12:15]\n"
      "global_load_dwordx4 v[232:235], v200, s[58:59] offset:2048 sc1\n"
      "v_cvt_pk_bf16_f32 v105, v98, v99\n"
      "v_mfma_f32_16x16x32_bf16 v[16:19], v[152:155], a[208:211], v[16:19]\n"
      "global_load_dwordx4 v[236:239], v200, s[58:59] offset:3072 sc1\n"
      "v_cvt_pk_bf16_f32 v106, v100, v101\n"
      "v_mfma_f32_16x16x32_bf16 v[20:23], v[152:155], a[212:215], v[20:23]\n"
      "v_cvt_pk_bf16_f32 v109, v102, v103\n"
      "v_and_b32 v205, 0xffff0000, v205\n"
      "v_mfma_f32_16x16x32_bf16 v[24:27], v[152:155], a[216:219], v[24:27]\n"
      "global_store_dwordx4 v242, v[104:107], s[52:53] offset:1024 sc1\n"
      "v_sub_f32 v207, 1.0, v206\n"
      "v_mfma_f32_16x16x32_bf16 v[28:31], v[152:155], a[220:223], v[28:31]\n"
      "global_load_dwordx4 v[152:155], v241, s[14:15] offset:2048 sc1\n"
      "v_cvt_pk_bf16_f32 v207, v207, v207\n"
      "v_mfma_f32_16x16x32_bf16 v[0:3], v[156:159], a[224:227], v[0:3]\n"
      "global_store_dwordx4 v242, v[108:111], s[52:53] offset:2048 sc1\n"
      "s_add_u32 s48, s34, 3\n"
      "v_mfma_f32_16x16x32_bf16 v[4:7], v[156:159], a[228:231], v[4:7]\n"
      "s_and_b32 s48, s48, 3\n"
      "s_mul_i32 s48, s48, 36864\n"
      "s_add_u32 s48, s48, 24576\n"
      "v_mfma_f32_16x16x32_bf16 v[8:11], v[156:159], a[232:235], v[8:11]\n"
      "s_add_u32 s52, s60, s48\n"
      "s_addc_u32 s53, s61, 0\n"
      "v_and_b32 v207, 0xffff0000, v207\n"
      "v_mfma_f32_16x16x32_bf16 v[12:15], v[156:159], a[236:239], v[12:15]\n"
      "v_lshlrev_b32 v208, 16, v216\n"
      "v_and_b32 v209, 0xffff0000, v216\n"
      "v_mfma_f32_16x16x32_bf16 v[16:19], v[156:159], a[240:243], v[16:19]\n"
      "v_mul_f32 v208, v205, v208\n"
      "v_mul_f32 v209, v205, v209\n"
      "v_mfma_f32_16x16x32_bf16 v[20:23], v[156:159], a[244:247], v[20:23]\n"
      "v_lshlrev_b32 v212, 16, v220\n"
      "v_and_b32 v213, 0xffff0000, v220\n"
      "v_mfma_f32_16x16x32_bf16 v[24:27], v[156:159], a[248:251], v[24:27]\n"
      "v_mul_f32 v212, v207, v212\n"
      "v_mul_f32 v213, v207, v213\n"
      "v_mfma_f32_16x16x32_bf16 v[28:31], v[156:159], a[252:255], v[28:31]\n"
      "global_load_dwordx4 v[156:159], v241, s[74:75] offset:2048 sc1\n"
      "v_lshlrev_b32 v210, 16, v217\n"
      "v_mfma_f32_16x16x32_bf16 v[32:35], v[160:163], a[0:3], 0\n"
      "v_and_b32 v211, 0xffff0000, v217\n"
      "v_mul_f32 v210, v205, v210\n"
      "v_mfma_f32_16x16x32_bf16 v[36:39], v[160:163], a[4:7], 0\n"
      "v_cvt_pk_bf16_f32 v8, v8, v9\n"
      "v_cvt_pk_bf16_f32 v9, v10, v11\n"
      "v_mfma_f32_16x16x32_bf16 v[40:43], v[160:163], a[8:11], 0\n"
      "ds_write_addtid_b32 v8 offset:0\n"
      "v_cvt_pk_bf16_f32 v10, v12, v13\n"
      "v_mfma_f32_16x16x32_bf16 v[44:47], v[160:163], a[12:15], 0\n"
      "ds_write_addtid_b32 v9 offset:256\n"
      "v_cvt_pk_bf16_f32 v11, v14, v15\n"
      "v_mfma_f32_16x16x32_bf16 v[48:51], v[160:163], a[16:19], 0\n"
      "ds_write_addtid_b32 v10 offset:512\n"
      "v_cvt_pk_bf16_f32 v12, v16, v17\n"
      "v_mfma_f32_16x16x32_bf16 v[52:55], v[160:163], a[20:23], 0\n"
      "ds_write_addtid_b32 v11 offset:768\n"
      "v_cvt_pk_bf16_f32 v13, v18, v19\n"
      "v_mfma_f32_16x16x32_bf16 v[56:59], v[160:163], a[24:27], 0\n"
      "ds_write_addtid_b32 v12 offset:1024\n"
      "v_cvt_pk_bf16_f32 v14, v20, v21\n"
      "v_mfma_f32_16x16x32_bf16 v[60:63], v[160:163], a[28:31], 0\n"
      "global_load_dwordx4 v[160:163], v240, s[10:11] offset:3072 sc1\n"
      "v_mul_f32 v211, v205, v211\n"
      "v_mfma_f32_16x16x32_bf16 v[32:35], v[164:167], a[32:35], v[32:35]\n"
      "ds_write_addtid_b32 v13 offset:1280\n"
      "v_cvt_pk_bf16_f32 v15, v22, v23\n"
      "v_mfma_f32_16x16x32_bf16 v[36:39], v[164:167], a[36:39], v[36:39]\n"
      "ds_write_addtid_b32 v14 offset:1536\n"
      "v_cvt_pk_bf16_f32 v16, v24, v25\n"
      "v_mfma_f32_16x16x32_bf16 v[40:43], v[164:167], a[40:43], v[40:43]\n"
      "ds_write_addtid_b32 v15 offset:1792\n"
      "v_cvt_pk_bf16_f32 v17, v26, v27\n"
      "v_mfma_f32_16x16x32_bf16 v[44:47], v[164:167], a[44:47], v[44:47]\n"
      "ds_write_addtid_b32 v16 offset:2048\n"
      "v_cvt_pk_bf16_f32 v18, v28, v29\n"
      "v_mfma_f32_16x16x32_bf16 v[48:51], v[164:167], a[48:51], v[48:51]\n"
      "ds_write_addtid_b32 v17 offset:2304\n"
      "v_cvt_pk_bf16_f32 v19, v30, v31\n"
      "v_mfma_f32_16x16x32_bf16 v[52:55], v[164:167], a[52:55], v[52:55]\n"
      "ds_write_addtid_b32 v18 offset:2560\n"
      "v_lshlrev_b32 v214, 16, v221\n"
      "v_mfma_f32_16x16x32_bf16 v[56:59], v[164:167], a[56:59], v[56:59]\n"
      "ds_write_addtid_b32 v19 offset:2816\n"
      "v_and_b32 v215, 0xffff0000, v221\n"
      "v_mfma_f32_16x16x32_bf16 v[60:63], v[164:167], a[60:63], v[60:63]\n"
      "global_load_dwordx4 v[164:167], v240, s[72:73] offset:3072 sc1\n"
      "v_mul_f32 v214, v207, v214\n"
      "v_mfma_f32_16x16x32_bf16 v[32:35], v[168:171], a[64:67], v[32:35]\n"
      "v_mul_f32 v215, v207, v215\n"
      "v_lshlrev_b32 v216, 16, v218\n"
      "v_mfma_f32_16x16x32_bf16 v[36:39], v[168:171], a[68:71], v[36:39]\n"
      "s_waitcnt lgkmcnt(0)\n"
      "s_barrier\n"
      "v_mfma_f32_16x16x32_bf16 v[40:43], v[168:171], a[72:75], v[40:43]\n"
      "ds_read2st64_b32 v[20:21], v244 offset0:0 offset1:1\n"
      "v_and_b32 v217, 0xffff0000, v218\n"
      "v_mfma_f32_16x16x32_bf16 v[44:47], v[168:171], a[76:79], v[44:47]\n"
      "ds_read2st64_b32 v[22:23], v245 offset0:4 offset1:5\n"
      "v_mul_f32 v216, v205, v216\n"
      "v_mfma_f32_16x16x32_bf16 v[48:51], v[168:171], a[80:83], v[48:51]\n"
      "ds_read2st64_b32 v[24:25], v244 offset0:2 offset1:3\n"
      "v_mul_f32 v217, v205, v217\n"
      "v_mfma_f32_16x16x32_bf16 v[52:55], v[168:171], a[84:87], v[52:55]\n"
      "ds_read2st64_b32 v[26:27], v245 offset0:6 offset1:7\n"
      "v_lshlrev_b32 v220, 16, v222\n"
      "v_mfma_f32_16x16x32_bf16 v[56:59], v[168:171], a[88:91], v[56:59]\n"
      "ds_read2st64_b32 v[28:29], v246 offset0:8 offset1:9\n"
      "v_and_b32 v221, 0xffff0000, v222\n"
      "v_mfma_f32_16x16x32_bf16 v[60:63], v[168:171], a[92:95], v[60:63]\n"
      "global_load_dwordx4 v[168:171], v241, s[10:11] offset:3072 sc1\n"
      "v_mul_f32 v220, v207, v220\n"
      "v_mfma_f32_16x16x32_bf16 v[32:35], v[172:175], a[96:99], v[32:35]\n"
      "ds_read2st64_b32 v[30:31], v246 offset0:10 offset1:11\n"
      "v_mul_f32 v221, v207, v221\n"
      "v_mfma_f32_16x16x32_bf16 v[36:39], v[172:175], a[100:103], v[36:39]\n"
      "v_lshlrev_b32 v218, 16, v219\n"
      "v_and_b32 v219, 0xffff0000, v219\n"
      "v_mfma_f32_16x16x32_bf16 v[40:43], v[172:175], a[104:107], v[40:43]\n"
      "v_mul_f32 v218, v205, v218\n"
      "v_mul_f32 v219, v205, v219\n"
      "v_mfma_f32_16x16x32_bf16 v[44:47], v[172:175], a[108:111], v[44:47]\n"
      "v_lshlrev_b32 v222, 16, v223\n"
      "v_and_b32 v223, 0xffff0000, v223\n"
      "v_mfma_f32_16x16x32_bf16 v[48:51], v[172:175], a[112:115], v[48:51]\n"
      "v_mul_f32 v222, v207, v222\n"
      "v_mul_f32 v223, v207, v223\n"
      "v_mfma_f32_16x16x32_bf16 v[52:55], v[172:175], a[116:119], v[52:55]\n"
      "s_waitcnt lgkmcnt(0)\n"
      "v_mfma_f32_16x16x32_bf16 v[0:3], v[250:253], v[20:23], v[0:3]\n"
      "v_mfma_f32_16x16x32_bf16 v[56:59], v[172:175], a[120:123], v[56:59]\n"
      "v_mfma_f32_16x16x32_bf16 v[4:7], v[250:253], v[24:27], v[4:7]\n"
      "v_mfma_f32_16x16x32_bf16 v[60:63], v[172:175], a[124:127], v[60:63]\n"
      "s_waitcnt vmcnt(22)\n"
      "global_load_dwordx4 v[172:175], v241, s[72:73] offset:3072 sc1\n"
      "v_mfma_f32_16x16x32_bf16 v[40:43], v[176:179], a[136:139], v[40:43]\n"
      "v_mfma_f32_16x16x32_bf16 v[0:3], v[252:255], v[28:31], v[0:3]\n"
      "v_mfma_f32_16x16x32_bf16 v[44:47], v[176:179], a[140:143], v[44:47]\n"
      "v_mfma_f32_16x16x32_bf16 v[4:7], v[248:251], v[28:31], v[4:7]\n"
      "v_mov_b32 v11, s37\n"
      "v_mfma_f32_16x16x32_bf16 v[48:51], v[176:179], a[144:147], v[48:51]\n"
      "v_mfma_f32_16x16x32_bf16 v[52:55], v[176:179], a[148:151], v[52:55]\n"
      "s_nop 2\n"
      "v_cvt_pk_bf16_f32 v8, v0, v1\n"
      "v_mfma_f32_16x16x32_bf16 v[56:59], v[176:179], a[152:155], v[56:59]\n"
      "v_cvt_pk_bf16_f32 v9, v2, v3\n"
      "v_cvt_pk_bf16_f32 v10, v4, v5\n"
      "v_mfma_f32_16x16x32_bf16 v[60:63], v[176:179], a[156:159], v[60:63]\n"
      "v_cvt_pk_bf16_f32 v247, v6, v7\n"
      "v_mfma_f32_16x16x32_bf16 v[40:43], v[180:183], a[168:171], v[40:43]\n"
      "s_waitcnt vmcnt(8)\n"
      "v_bitop3_b32 v192, v225, v227, s37 bitop3:0x7e\n"
      "v_bitop3_b32 v192, v192, v229, s37 bitop3:0xf6\n"
      "v_bitop3_b32 v192, v192, v231, s37 bitop3:0xf6\n"
      "v_bitop3_b32 v192, v192, v233, s37 bitop3:0xf6\n"
      "v_bitop3_b32 v192, v192, v235, s37 bitop3:0xf6\n"
      "v_bitop3_b32 v192, v192, v237, s37 bitop3:0xf6\n"
      "v_bitop3_b32 v192, v192, v239, s37 bitop3:0xf6\n"
      "v_cmp_ne_u32 vcc, 0, v192\n"
      "s_nop 1\n"
      "s_cbranch_vccnz .Ls_s17_%=\n"
      ".Ls_o18_%=:\n"
      "v_mfma_f32_16x16x32_bf16 v[44:47], v[180:183], a[172:175], v[44:47]\n"
      "global_store_dwordx4 v242, v[8:11], s[52:53] offset:0 sc1\n"
      "v_add_f32 v192, v224, v228\n"
      "v_mfma_f32_16x16x32_bf16 v[48:51], v[180:183], a[176:179], v[48:51]\n"
      "v_add_f32 v193, v226, v230\n"
      "v_add_f32 v194, v232, v236\n"
      "v_mfma_f32_16x16x32_bf16 v[52:55], v[180:183], a[180:183], v[52:55]\n"
      "v_add_f32 v195, v234, v238\n"
      "v_mfma_f32_16x16x32_bf16 v[56:59], v[180:183], a[184:187], v[56:59]\n"
      "ds_read_b64_tr_b16 v[224:225], v201 offset:0\n"
      "v_permlane32_swap_b32 v192, v193\n"
      "v_mfma_f32_16x16x32_bf16 v[60:63], v[180:183], a[188:191], v[60:63]\n"
      "ds_read_b64_tr_b16 v[226:227], v201 offset:512\n"
      "v_permlane32_swap_b32 v194, v195\n"
      "v_mfma_f32_16x16x32_bf16 v[40:43], v[184:187], a[200:203], v[40:43]\n"
      "ds_read_b64_tr_b16 v[228:229], v201 offset:32\n"
      "v_add_f32 v192, v192, v193\n"
      "v_mfma_f32_16x16x32_bf16 v[44:47], v[184:187], a[204:207], v[44:47]\n"
      "ds_read_b64_tr_b16 v[230:231], v201 offset:544\n"
      "v_add_f32 v194, v194, v195\n"
      "v_mfma_f32_16x16x32_bf16 v[48:51], v[184:187], a[208:211], v[48:51]\n"
      "ds_read_b64_tr_b16 v[232:233], v201 offset:64\n"
      "v_mov_b32 v193, v192\n"
      "v_mfma_f32_16x16x32_bf16 v[52:55], v[184:187], a[212:215], v[52:55]\n"
      "ds_read_b64_tr_b16 v[234:235], v201 offset:576\n"
      "v_mov_b32 v195, v194\n"
      "v_mfma_f32_16x16x32_bf16 v[56:59], v[184:187], a[216:219], v[56:59]\n"
      "ds_read_b64_tr_b16 v[236:237], v201 offset:96\n"
      "v_permlane16_swap_b32 v192, v193\n"
      "v_mfma_f32_16x16x32_bf16 v[60:63], v[184:187], a[220:223], v[60:63]\n"
      "ds_read_b64_tr_b16 v[238:239], v201 offset:608\n"
      "v_permlane16_swap_b32 v194, v195\n"
      "v_add_f32 v192, v192, v193\n"
      "v_mfma_f32_16x16x32_bf16 v[40:43], v[188:191], a[232:235], v[40:43]\n"
      "v_add_f32 v194, v194, v195\n"
      "v_cvt_pk_bf16_f32 v192, v192, v194\n"
      "v_and_b32 v194, 0xffff0000, v192\n"
      "v_mfma_f32_16x16x32_bf16 v[44:47], v[188:191], a[236:239], v[44:47]\n"
      "v_lshlrev_b32 v192, 16, v192\n"
      "v_mul_f32 v193, 0xbfb8aa3b, v192\n"
      "v_exp_f32 v193, v193\n"
      "v_mfma_f32_16x16x32_bf16 v[48:51], v[188:191], a[240:243], v[48:51]\n"
      "v_add_f32 v193, 1.0, v193\n"
      "v_rcp_f32 v193, v193\n"
      "s_nop 0\n"
      "v_mul_f32 v192, v192, v193\n"
      "v_mfma_f32_16x16x32_bf16 v[52:55], v[188:191], a[244:247], v[52:55]\n"
      "v_cvt_pk_bf16_f32 v192, v192, v192\n"
      "v_and_b32 v192, 0xffff0000, v192\n"
      "v_mul_f32 v192, v192, v194\n"
      "v_mfma_f32_16x16x32_bf16 v[56:59], v[188:191], a[248:251], v[56:59]\n"
      "v_cvt_pk_bf16_f32 v192, v192, v192\n"
      "v_mfma_f32_16x16x32_bf16 v[60:63], v[188:191], a[252:255], v[60:63]\n"
      "ds_write_b16 v202, v192\n"
      "ds_read_b128 v[192:195], v199\n"
      "v_mfma_f32_16x16x32_bf16 v[32:35], v[176:179], a[128:131], v[32:35]\n"
      "v_mfma_f32_16x16x32_bf16 v[36:39], v[176:179], a[132:135], v[36:39]\n"
      "global_load_dwordx4 v[176:179], v240, s[14:15] offset:3072 sc1\n"
      "v_cvt_pk_bf16_f32 v40, v40, v41\n"
      "v_cvt_pk_bf16_f32 v41, v42, v43\n"
      "v_mfma_f32_16x16x32_bf16 v[32:35], v[180:183], a[160:163], v[32:35]\n"
      "ds_write_addtid_b32 v40 offset:3072\n"
      "v_cvt_pk_bf16_f32 v42, v44, v45\n"
      "v_mfma_f32_16x16x32_bf16 v[36:39], v[180:183], a[164:167], v[36:39]\n"
      "global_load_dwordx4 v[180:183], v240, s[74:75] offset:3072 sc1\n"
      "ds_write_addtid_b32 v41 offset:3328\n"
      "v_cvt_pk_bf16_f32 v43, v46, v47\n"
      "v_mfma_f32_16x16x32_bf16 v[32:35], v[184:187], a[192:195], v[32:35]\n"
      "ds_write_addtid_b32 v42 offset:3584\n"
      "v_cvt_pk_bf16_f32 v44, v48, v49\n"
      "v_mfma_f32_16x16x32_bf16 v[36:39], v[184:187], a[196:199], v[36:39]\n"
      "global_load_dwordx4 v[184:187], v241, s[14:15] offset:3072 sc1\n"
      "s_waitcnt lgkmcnt(3)\n"
      "v_mfma_f32_16x16x32_bf16 v[196:199], v[192:195], v[224:227], 0\n"
      "v_mfma_f32_16x16x32_bf16 v[32:35], v[188:191], a[224:227], v[32:35]\n"
      "v_mfma_f32_16x16x32_bf16 v[224:227], v[192:195], v[228:231], 0\n"
      "v_mfma_f32_16x16x32_bf16 v[228:231], v[192:195], v[232:235], 0\n"
      "v_mfma_f32_16x16x32_bf16 v[36:39], v[188:191], a[228:231], v[36:39]\n"
      "global_load_dwordx4 v[188:191], v241, s[74:75] offset:3072 sc1\n"
      "v_mfma_f32_16x16x32_bf16 v[232:235], v[192:195], v[236:239], 0\n"
      "v_mov_b32 v193, s37\n"
      "v_mfma_f32_16x16x32_bf16 v[72:75], v[128:131], a[8:11], 0\n"
      "ds_write_addtid_b32 v43 offset:3840\n"
      "v_mov_b32 v195, s37\n"
      "v_cvt_pk_bf16_f32 v45, v50, v51\n"
      "v_mfma_f32_16x16x32_bf16 v[76:79], v[128:131], a[12:15], 0\n"
      "ds_write_addtid_b32 v44 offset:4096\n"
      "v_cvt_pk_bf16_f32 v46, v52, v53\n"
      "v_mfma_f32_16x16x32_bf16 v[80:83], v[128:131], a[16:19], 0\n"
      "ds_write_addtid_b32 v45 offset:4352\n"
      "v_cndmask_b32 v192, v196, v228, s[64:65]\n"
      "v_cndmask_b32 v194, v224, v232, s[64:65]\n"
      "v_mfma_f32_16x16x32_bf16 v[84:87], v[128:131], a[20:23], 0\n"
      "v_and_b32 v192, s82, v192\n"
      "v_and_b32 v194, s82, v194\n"
      "v_cvt_pk_bf16_f32 v47, v54, v55\n"
      "v_mfma_f32_16x16x32_bf16 v[88:91], v[128:131], a[24:27], 0\n"
      "s_mov_b64 exec, s[66:67]\n"
      "global_store_dwordx4 v203, v[192:195], s[58:59] sc1\n"
      "s_mov_b64 exec, -1\n"
      "v_mfma_f32_16x16x32_bf16 v[92:95], v[128:131], a[28:31], 0\n"
      "s_lshl2_add_u32 s54, s36, s33\n"
      "ds_write_addtid_b32 v46 offset:4608\n"
      "v_cvt_pk_bf16_f32 v48, v56, v57\n"
      "s_lshl_b32 s55, s54, 15\n"
      "v_mfma_f32_16x16x32_bf16 v[72:75], v[132:135], a[40:43], v[72:75]\n"
      "s_lshl2_add_u32 s56, s35, s34\n"
      "ds_write_addtid_b32 v47 offset:4864\n"
      "v_cvt_pk_bf16_f32 v49, v58, v59\n"
      "s_lshl_b32 s56, s56, 6\n"
      "v_mfma_f32_16x16x32_bf16 v[76:79], v[132:135], a[44:47], v[76:79]\n"
      "s_add_u32 s55, s55, s56\n"
      "ds_write_addtid_b32 v48 offset:5120\n"
      "v_cvt_pk_bf16_f32 v50, v60, v61\n"
      "v_and_b32 v8, 15, v243\n"
      "v_mfma_f32_16x16x32_bf16 v[80:83], v[132:135], a[48:51], v[80:83]\n"
      "ds_write_addtid_b32 v49 offset:5376\n"
      "v_cvt_pk_bf16_f32 v51, v62, v63\n"
      "v_and_b32 v7, 48, v243\n"
      "v_mfma_f32_16x16x32_bf16 v[84:87], v[132:135], a[52:55], v[84:87]\n"
      "ds_write_addtid_b32 v50 offset:5632\n"
      "ds_write_addtid_b32 v51 offset:5888\n"
      "v_lshl_or_b32 v7, v8, 10, v7\n"
      "v_mfma_f32_16x16x32_bf16 v[88:91], v[132:135], a[56:59], v[88:91]\n"
      "v_add_u32 v7, s55, v7\n"
      "v_mfma_f32_16x16x32_bf16 v[92:95], v[132:135], a[60:63], v[92:95]\n"
      "v_mfma_f32_16x16x32_bf16 v[72:75], v[136:139], a[72:75], v[72:75]\n"
      "s_waitcnt lgkmcnt(0)\n"
      "s_barrier\n"
      "v_mfma_f32_16x16x32_bf16 v[76:79], v[136:139], a[76:79], v[76:79]\n"
      "ds_read2st64_b32 v[52:53], v244 offset0:12 offset1:13\n"
      "ds_read2st64_b32 v[54:55], v245 offset0:16 offset1:17\n"
      "v_mfma_f32_16x16x32_bf16 v[80:83], v[136:139], a[80:83], v[80:83]\n"
      "ds_read2st64_b32 v[56:57], v244 offset0:14 offset1:15\n"
      "ds_read2st64_b32 v[58:59], v245 offset0:18 offset1:19\n"
      "v_mfma_f32_16x16x32_bf16 v[84:87], v[136:139], a[84:87], v[84:87]\n"
      "ds_read2st64_b32 v[60:61], v246 offset0:20 offset1:21\n"
      "ds_read2st64_b32 v[62:63], v246 offset0:22 offset1:23\n"
      "v_mfma_f32_16x16x32_bf16 v[88:91], v[136:139], a[88:91], v[88:91]\n"
      "v_add_u32 v4, 0, v242\n"
      "v_mfma_f32_16x16x32_bf16 v[92:95], v[136:139], a[92:95], v[92:95]\n"
      "global_load_dwordx4 v[12:15], v4, s[62:63] offset:0 sc1\n"
      "global_load_dwordx4 v[16:19], v4, s[62:63] offset:1024 sc1\n"
      "v_mfma_f32_16x16x32_bf16 v[72:75], v[140:143], a[104:107], v[72:75]\n"
      "global_load_dwordx4 v[20:23], v4, s[62:63] offset:2048 sc1\n"
      "v_mfma_f32_16x16x32_bf16 v[76:79], v[140:143], a[108:111], v[76:79]\n"
      "v_mfma_f32_16x16x32_bf16 v[80:83], v[140:143], a[112:115], v[80:83]\n"
      "s_waitcnt lgkmcnt(0)\n"
      "v_mfma_f32_16x16x32_bf16 v[32:35], v[250:253], v[52:55], v[32:35]\n"
      "v_mfma_f32_16x16x32_bf16 v[36:39], v[250:253], v[56:59], v[36:39]\n"
      "v_mfma_f32_16x16x32_bf16 v[84:87], v[140:143], a[116:119], v[84:87]\n"
      "v_mfma_f32_16x16x32_bf16 v[32:35], v[252:255], v[60:63], v[32:35]\n"
      "v_mfma_f32_16x16x32_bf16 v[36:39], v[248:251], v[60:63], v[36:39]\n"
      "v_mov_b32 v43, s37\n"
      "v_mfma_f32_16x16x32_bf16 v[88:91], v[140:143], a[120:123], v[88:91]\n"
      "v_mov_b32 v44, v247\n"
      "v_mov_b32 v47, s37\n"
      "v_mfma_f32_16x16x32_bf16 v[92:95], v[140:143], a[124:127], v[92:95]\n"
      "v_mfma_f32_16x16x32_bf16 v[72:75], v[144:147], a[136:139], v[72:75]\n"
      "s_nop 0\n"
      "v_cvt_pk_bf16_f32 v40, v32, v33\n"
      "v_cvt_pk_bf16_f32 v41, v34, v35\n"
      "v_mfma_f32_16x16x32_bf16 v[76:79], v[144:147], a[140:143], v[76:79]\n"
      "v_cvt_pk_bf16_f32 v42, v36, v37\n"
      "v_cvt_pk_bf16_f32 v45, v38, v39\n"
      "v_mfma_f32_16x16x32_bf16 v[80:83], v[144:147], a[144:147], v[80:83]\n"
      "global_store_dwordx4 v242, v[40:43], s[52:53] offset:1024 sc1\n"
      "v_mfma_f32_16x16x32_bf16 v[84:87], v[144:147], a[148:151], v[84:87]\n"
      "global_store_dwordx4 v242, v[44:47], s[52:53] offset:2048 sc1\n"
      "v_mfma_f32_16x16x32_bf16 v[88:91], v[144:147], a[152:155], v[88:91]\n"
      "v_mfma_f32_16x16x32_bf16 v[92:95], v[144:147], a[156:159], v[92:95]\n"
      "v_mfma_f32_16x16x32_bf16 v[72:75], v[148:151], a[168:171], v[72:75]\n"
      "v_mfma_f32_16x16x32_bf16 v[76:79], v[148:151], a[172:175], v[76:79]\n"
      "v_mfma_f32_16x16x32_bf16 v[80:83], v[148:151], a[176:179], v[80:83]\n"
      "v_mfma_f32_16x16x32_bf16 v[84:87], v[148:151], a[180:183], v[84:87]\n"
      "v_mfma_f32_16x16x32_bf16 v[88:91], v[148:151], a[184:187], v[88:91]\n"
      "v_mfma_f32_16x16x32_bf16 v[92:95], v[148:151], a[188:191], v[92:95]\n"
      "s_waitcnt vmcnt(15)\n"
      "v_mfma_f32_16x16x32_bf16 v[72:75], v[152:155], a[200:203], v[72:75]\n"
      "v_mfma_f32_16x16x32_bf16 v[76:79], v[152:155], a[204:207], v[76:79]\n"
      "v_mfma_f32_16x16x32_bf16 v[80:83], v[152:155], a[208:211], v[80:83]\n"
      "v_mfma_f32_16x16x32_bf16 v[84:87], v[152:155], a[212:215], v[84:87]\n"
      "v_mfma_f32_16x16x32_bf16 v[88:91], v[152:155], a[216:219], v[88:91]\n"
      "v_mfma_f32_16x16x32_bf16 v[92:95], v[152:155], a[220:223], v[92:95]\n"
      "v_mfma_f32_16x16x32_bf16 v[72:75], v[156:159], a[232:235], v[72:75]\n"
      "v_mfma_f32_16x16x32_bf16 v[76:79], v[156:159], a[236:239], v[76:79]\n"
      "v_mfma_f32_16x16x32_bf16 v[80:83], v[156:159], a[240:243], v[80:83]\n"
      "v_mfma_f32_16x16x32_bf16 v[84:87], v[156:159], a[244:247], v[84:87]\n"
      "v_mfma_f32_16x16x32_bf16 v[88:91], v[156:159], a[248:251], v[88:91]\n"
      "v_mfma_f32_16x16x32_bf16 v[92:95], v[156:159], a[252:255], v[92:95]\n"
      "s_waitcnt vmcnt(13)\n"
      "v_mfma_f32_16x16x32_bf16 v[104:107], v[160:163], a[8:11], 0\n"
      "v_mfma_f32_16x16x32_bf16 v[108:111], v[160:163], a[12:15], 0\n"
      "v_cvt_pk_bf16_f32 v72, v72, v73\n"
      "v_cvt_pk_bf16_f32 v73, v74, v75\n"
      "v_mfma_f32_16x16x32_bf16 v[112:115], v[160:163], a[16:19], 0\n"
      "ds_write_addtid_b32 v72 offset:6144\n"
      "v_cvt_pk_bf16_f32 v74, v76, v77\n"
      "v_mfma_f32_16x16x32_bf16 v[116:119], v[160:163], a[20:23], 0\n"
      "ds_write_addtid_b32 v73 offset:6400\n"
      "v_cvt_pk_bf16_f32 v75, v78, v79\n"
      "v_mfma_f32_16x16x32_bf16 v[120:123], v[160:163], a[24:27], 0\n"
      "ds_write_addtid_b32 v74 offset:6656\n"
      "v_cvt_pk_bf16_f32 v76, v80, v81\n"
      "v_mfma_f32_16x16x32_bf16 v[124:127], v[160:163], a[28:31], 0\n"
      "ds_write_addtid_b32 v75 offset:6912\n"
      "v_cvt_pk_bf16_f32 v77, v82, v83\n"
      "v_mfma_f32_16x16x32_bf16 v[104:107], v[164:167], a[40:43], v[104:107]\n"
      "ds_write_addtid_b32 v76 offset:7168\n"
      "v_cvt_pk_bf16_f32 v78, v84, v85\n"
      "v_mfma_f32_16x16x32_bf16 v[108:111], v[164:167], a[44:47], v[108:111]\n"
      "ds_write_addtid_b32 v77 offset:7424\n"
      "v_cvt_pk_bf16_f32 v79, v86, v87\n"
      "v_mfma_f32_16x16x32_bf16 v[112:115], v[164:167], a[48:51], v[112:115]\n"
      "ds_write_addtid_b32 v78 offset:7680\n"
      "v_cvt_pk_bf16_f32 v80, v88, v89\n"
      "v_mfma_f32_16x16x32_bf16 v[116:119], v[164:167], a[52:55], v[116:119]\n"
      "ds_write_addtid_b32 v79 offset:7936\n"
      "v_cvt_pk_bf16_f32 v81, v90, v91\n"
      "v_mfma_f32_16x16x32_bf16 v[120:123], v[164:167], a[56:59], v[120:123]\n"
      "ds_write_addtid_b32 v80 offset:8192\n"
      "v_cvt_pk_bf16_f32 v82, v92, v93\n"
      "v_mfma_f32_16x16x32_bf16 v[124:127], v[164:167], a[60:63], v[124:127]\n"
      "s_waitcnt vmcnt(11)\n"
      "ds_write_addtid_b32 v81 offset:8448\n"
      "v_cvt_pk_bf16_f32 v83, v94, v95\n"
      "v_mfma_f32_16x16x32_bf16 v[104:107], v[168:171], a[72:75], v[104:107]\n"
      "ds_write_addtid_b32 v82 offset:8704\n"
      "v_mfma_f32_16x16x32_bf16 v[108:111], v[168:171], a[76:79], v[108:111]\n"
      "ds_write_addtid_b32 v83 offset:8960\n"
      "v_mfma_f32_16x16x32_bf16 v[112:115], v[168:171], a[80:83], v[112:115]\n"
      "s_waitcnt vmcnt(2)\n"
      "v_bitop3_b32 v22, v15, v19, s37 bitop3:0x7e\n"
      "v_bitop3_b32 v22, v22, v23, s37 bitop3:0xf6\n"
      "v_cmp_ne_u32 vcc, 0, v22\n"
      "s_nop 1\n"
      "s_cbranch_vccnz .Ls_s20_%=\n"
      ".Ls_o21_%=:\n"
      "v_mfma_f32_16x16x32_bf16 v[116:119], v[168:171], a[84:87], v[116:119]\n"
      "v_mov_b32 v15, v20\n"
      "v_mov_b32 v19, v21\n"
      "v_mfma_f32_16x16x32_bf16 v[120:123], v[168:171], a[88:91], v[120:123]\n"
      "s_waitcnt lgkmcnt(0)\n"
      "s_barrier\n"
      "v_mfma_f32_16x16x32_bf16 v[124:127], v[168:171], a[92:95], v[124:127]\n"
      "ds_read2st64_b32 v[84:85], v244 offset0:24 offset1:25\n"
      "v_mfma_f32_16x16x32_bf16 v[104:107], v[172:175], a[104:107], v[104:107]\n"
      "ds_read2st64_b32 v[86:87], v245 offset0:28 offset1:29\n"
      "v_add_u32 v5, 12288, v242\n"
      "v_mfma_f32_16x16x32_bf16 v[108:111], v[172:175], a[108:111], v[108:111]\n"
      "global_load_dwordx4 v[44:47], v5, s[62:63] offset:0 sc1\n"
      "v_mfma_f32_16x16x32_bf16 v[112:115], v[172:175], a[112:115], v[112:115]\n"
      "global_load_dwordx4 v[48:51], v5, s[62:63] offset:1024 sc1\n"
      "v_mfma_f32_16x16x32_bf16 v[116:119], v[172:175], a[116:119], v[116:119]\n"
      "global_load_dwordx4 v[52:55], v5, s[62:63] offset:2048 sc1\n"
      "v_mfma_f32_16x16x32_bf16 v[120:123], v[172:175], a[120:123], v[120:123]\n"
      "ds_read2st64_b32 v[88:89], v244 offset0:26 offset1:27\n"
      "v_mfma_f32_16x16x32_bf16 v[124:127], v[172:175], a[124:127], v[124:127]\n"
      "ds_read2st64_b32 v[90:91], v245 offset0:30 offset1:31\n"
      "v_mfma_f32_16x16x32_bf16 v[104:107], v[176:179], a[136:139], v[104:107]\n"
      "ds_read2st64_b32 v[92:93], v246 offset0:32 offset1:33\n"
      "v_mfma_f32_16x16x32_bf16 v[108:111], v[176:179], a[140:143], v[108:111]\n"
      "ds_read2st64_b32 v[94:95], v246 offset0:34 offset1:35\n"
      "v_mfma_f32_16x16x32_bf16 v[112:115], v[176:179], a[144:147], v[112:115]\n"
      "v_mfma_f32_16x16x32_bf16 v[64:67], v[252:255], v[12:15], 0\n"
      "v_mfma_f32_16x16x32_bf16 v[116:119], v[176:179], a[148:151], v[116:119]\n"
      "v_mfma_f32_16x16x32_bf16 v[68:71], v[248:251], v[12:15], 0\n"
      "v_mfma_f32_16x16x32_bf16 v[120:123], v[176:179], a[152:155], v[120:123]\n"
      "v_mfma_f32_16x16x32_bf16 v[96:99], v[252:255], v[16:19], 0\n"
      "v_mfma_f32_16x16x32_bf16 v[124:127], v[176:179], a[156:159], v[124:127]\n"
      "v_mfma_f32_16x16x32_bf16 v[100:103], v[248:251], v[16:19], 0\n"
      "v_mfma_f32_16x16x32_bf16 v[104:107], v[180:183], a[168:171], v[104:107]\n"
      "s_waitcnt lgkmcnt(0)\n"
      "v_mfma_f32_16x16x32_bf16 v[64:67], v[250:253], v[84:87], v[64:67]\n"
      "v_mfma_f32_16x16x32_bf16 v[108:111], v[180:183], a[172:175], v[108:111]\n"
      "v_mfma_f32_16x16x32_bf16 v[68:71], v[250:253], v[88:91], v[68:71]\n"
      "v_mfma_f32_16x16x32_bf16 v[112:115], v[180:183], a[176:179], v[112:115]\n"
      "v_mfma_f32_16x16x32_bf16 v[64:67], v[252:255], v[92:95], v[64:67]\n"
      "v_mfma_f32_16x16x32_bf16 v[116:119], v[180:183], a[180:183], v[116:119]\n"
      "v_mfma_f32_16x16x32_bf16 v[68:71], v[248:251], v[92:95], v[68:71]\n"
      "v_mfma_f32_16x16x32_bf16 v[120:123], v[180:183], a[184:187], v[120:123]\n"
      "v_mfma_f32_16x16x32_bf16 v[124:127], v[180:183], a[188:191], v[124:127]\n"
      "v_mfma_f32_16x16x32_bf16 v[104:107], v[184:187], a[200:203], v[104:107]\n"
      "v_mfma_f32_16x16x32_bf16 v[108:111], v[184:187], a[204:207], v[108:111]\n"
      "v_mfma_f32_16x16x32_bf16 v[112:115], v[184:187], a[208:211], v[112:115]\n"
      "v_mfma_f32_16x16x32_bf16 v[116:119], v[184:187], a[212:215], v[116:119]\n"
      "v_mfma_f32_16x16x32_bf16 v[120:123], v[184:187], a[216:219], v[120:123]\n"
      "v_mfma_f32_16x16x32_bf16 v[124:127], v[184:187], a[220:223], v[124:127]\n"
      "v_mfma_f32_16x16x32_bf16 v[104:107], v[188:191], a[232:235], v[104:107]\n"
      "v_mfma_f32_16x16x32_bf16 v[108:111], v[188:191], a[236:239], v[108:111]\n"
      "s_lshr_b32 s43, s42, 7\n"
      "s_cmp_eq_u32 s43, s36\n"
      "s_cbranch_scc0 .Ls_e22_%=\n"
      "s_lshl2_add_u32 s44, s35, s34\n"
      "s_lshl2_add_u32 s43, s44, s33\n"
      "s_lshl_b32 s43, s43, 12\n"
      "s_add_u32 s43, s43, 524288\n"
      "v_add_u32 v60, s43, v242\n"
      "global_load_dwordx4 v[24:27], v60, s[58:59] offset:0 sc1\n"
      "global_load_dwordx4 v[28:31], v60, s[58:59] offset:1024 sc1\n"
      "global_load_dwordx4 v[32:35], v60, s[58:59] offset:2048 sc1\n"
      "global_load_dwordx4 v[36:39], v60, s[58:59] offset:3072 sc1\n"
      "v_and_b32 v58, 15, v243\n"
      "v_lshrrev_b32 v59, 5, v243\n"
      "v_lshl_or_b32 v58, v59, 4, v58\n"
      "s_lshl4_add_u32 s44, s42, s44\n"
      "s_lshl_b32 s44, s44, 6\n"
      "v_lshl_add_u32 v59, v58, 1, s44\n"
      "global_load_ushort v57, v59, s[16:17]\n"
      "s_mov_b32 s44, 131072\n"
      "v_lshl_add_u32 v58, v58, 2, s44\n"
      "s_lshl_b32 s43, s33, 13\n"
      "v_add_u32 v59, s43, v58\n"
      ".Ls_e22_%=:\n"
      "v_mfma_f32_16x16x32_bf16 v[112:115], v[188:191], a[240:243], v[112:115]\n"
      "v_mfma_f32_16x16x32_bf16 v[116:119], v[188:191], a[244:247], v[116:119]\n"
      "v_mfma_f32_16x16x32_bf16 v[120:123], v[188:191], a[248:251], v[120:123]\n"
      "v_mfma_f32_16x16x32_bf16 v[124:127], v[188:191], a[252:255], v[124:127]\n"
      "v_mfma_f32_16x16x32_bf16 v[64:67], v[128:131], a[0:3], v[64:67]\n"
      "v_mfma_f32_16x16x32_bf16 v[68:71], v[128:131], a[4:7], v[68:71]\n"
      "v_cvt_pk_bf16_f32 v104, v104, v105\n"
      "v_cvt_pk_bf16_f32 v105, v106, v107\n"
      "v_mfma_f32_16x16x32_bf16 v[64:67], v[132:135], a[32:35], v[64:67]\n"
      "ds_write_addtid_b32 v104 offset:9216\n"
      "v_cvt_pk_bf16_f32 v106, v108, v109\n"
      "v_mfma_f32_16x16x32_bf16 v[68:71], v[132:135], a[36:39], v[68:71]\n"
      "ds_write_addtid_b32 v105 offset:9472\n"
      "v_cvt_pk_bf16_f32 v107, v110, v111\n"
      "v_mfma_f32_16x16x32_bf16 v[64:67], v[136:139], a[64:67], v[64:67]\n"
      "ds_write_addtid_b32 v106 offset:9728\n"
      "v_cvt_pk_bf16_f32 v108, v112, v113\n"
      "v_mfma_f32_16x16x32_bf16 v[68:71], v[136:139], a[68:71], v[68:71]\n"
      "ds_write_addtid_b32 v107 offset:9984\n"
      "v_cvt_pk_bf16_f32 v109, v114, v115\n"
      "v_mfma_f32_16x16x32_bf16 v[64:67], v[140:143], a[96:99], v[64:67]\n"
      "ds_write_addtid_b32 v108 offset:10240\n"
      "v_cvt_pk_bf16_f32 v110, v116, v117\n"
      "v_mfma_f32_16x16x32_bf16 v[68:71], v[140:143], a[100:103], v[68:71]\n"
      "ds_write_addtid_b32 v109 offset:10496\n"
      "v_cvt_pk_bf16_f32 v111, v118, v119\n"
      "v_mfma_f32_16x16x32_bf16 v[64:67], v[144:147], a[128:131], v[64:67]\n"
      "ds_write_addtid_b32 v110 offset:10752\n"
      "v_cvt_pk_bf16_f32 v112, v120, v121\n"
      "v_mfma_f32_16x16x32_bf16 v[68:71], v[144:147], a[132:135], v[68:71]\n"
      "ds_write_addtid_b32 v111 offset:11008\n"
      "v_cvt_pk_bf16_f32 v113, v122, v123\n"
      "v_mfma_f32_16x16x32_bf16 v[64:67], v[148:151], a[160:163], v[64:67]\n"
      "ds_write_addtid_b32 v112 offset:11264\n"
      "v_add_u32 v6, 24576, v242\n"
      "v_cvt_pk_bf16_f32 v114, v124, v125\n"
      "v_mfma_f32_16x16x32_bf16 v[68:71], v[148:151], a[164:167], v[68:71]\n"
      "global_load_dwordx4 v[224:227], v6, s[62:63] offset:0 sc1\n"
      "global_load_dwordx4 v[228:231], v6, s[62:63] offset:1024 sc1\n"
      "v_mfma_f32_16x16x32_bf16 v[64:67], v[152:155], a[192:195], v[64:67]\n"
      "global_load_dwordx4 v[232:235], v6, s[62:63] offset:2048 sc1\n"
      "ds_write_addtid_b32 v113 offset:11520\n"
      "v_cvt_pk_bf16_f32 v115, v126, v127\n"
      "v_mfma_f32_16x16x32_bf16 v[68:71], v[152:155], a[196:199], v[68:71]\n"
      "ds_write_addtid_b32 v114 offset:11776\n"
      "ds_write_addtid_b32 v115 offset:12032\n"
      "v_mfma_f32_16x16x32_bf16 v[64:67], v[156:159], a[224:227], v[64:67]\n"
      "s_waitcnt vmcnt(3)\n"
      "v_bitop3_b32 v54, v47, v51, s37 bitop3:0x7e\n"
      "v_bitop3_b32 v54, v54, v55, s37 bitop3:0xf6\n"
      "v_cmp_ne_u32 vcc, 0, v54\n"
      "s_nop 1\n"
      "s_cbranch_vccnz .Ls_s24_%=\n"
      ".Ls_o25_%=:\n"
      "v_mfma_f32_16x16x32_bf16 v[68:71], v[156:159], a[228:231], v[68:71]\n"
      "v_mov_b32 v47, v52\n"
      "v_mov_b32 v51, v53\n"
      "v_mfma_f32_16x16x32_bf16 v[96:99], v[160:163], a[0:3], v[96:99]\n"
      "s_lshr_b32 s43, s42, 7\n"
      "s_cmp_eq_u32 s43, s36\n"
      "s_cbranch_scc0 .Ls_e26_%=\n"
      "s_waitcnt vmcnt(3)\n"
      "v_bitop3_b32 v61, v25, v27, s37 bitop3:0x7e\n"
      "v_bitop3_b32 v61, v61, v29, s37 bitop3:0xf6\n"
      "v_bitop3_b32 v61, v61, v31, s37 bitop3:0xf6\n"
      "v_bitop3_b32 v61, v61, v33, s37 bitop3:0xf6\n"
      "v_bitop3_b32 v61, v61, v35, s37 bitop3:0xf6\n"
      "v_bitop3_b32 v61, v61, v37, s37 bitop3:0xf6\n"
      "v_bitop3_b32 v61, v61, v39, s37 bitop3:0xf6\n"
      "v_cmp_ne_u32 vcc, 0, v61\n"
      "s_nop 1\n"
      "s_cbranch_vccnz .Ls_s28_%=\n"
      ".Ls_o29_%=:\n"
      "v_add_f32 v24, v24, v28\n"
      "v_add_f32 v26, v26, v30\n"
      "v_add_f32 v32, v32, v36\n"
      "v_add_f32 v34, v34, v38\n"
      "v_add_f32 v24, v24, v32\n"
      "v_add_f32 v26, v26, v34\n"
      "s_nop 1\n"
      "v_permlane32_swap_b32 v24, v26\n"
      "v_add_f32 v24, v24, v26\n"
      "v_mov_b32 v26, v24\n"
      "s_nop 1\n"
      "v_permlane16_swap_b32 v24, v26\n"
      "v_add_f32 v24, v24, v26\n"
      "ds_write_b32 v59, v24\n"
      "s_waitcnt lgkmcnt(0)\n"
      ".Ls_e26_%=:\n"
      "v_mfma_f32_16x16x32_bf16 v[100:103], v[160:163], a[4:7], v[100:103]\n"
      "s_waitcnt lgkmcnt(0)\n"
      "s_barrier\n"
      "v_mfma_f32_16x16x32_bf16 v[96:99], v[164:167], a[32:35], v[96:99]\n"
      "s_lshr_b32 s43, s42, 5\n"
      "s_lshl2_add_u32 s44, s36, s33\n"
      "s_cmp_eq_u32 s43, s44\n"
      "s_cbranch_scc0 .Ls_e30_%=\n"
      "ds_read_b32 v24, v58 offset:0\n"
      "ds_read_b32 v25, v58 offset:8192\n"
      "ds_read_b32 v26, v58 offset:16384\n"
      "ds_read_b32 v27, v58 offset:24576\n"
      "v_mov_b32 v62, 0\n"
      "v_mov_b32 v63, s37\n"
      "s_or_b32 s43, s34, s35\n"
      "s_cmp_eq_u32 s43, 0\n"
      "s_cbranch_scc0 .Ls_z31_%=\n"
      "s_mov_b64 exec, 1\n"
      "global_store_dword v62, v63, s[58:59] sc1\n"
      ".Ls_z31_%=:\n"
      "s_mov_b64 exec, -1\n"
      "s_waitcnt vmcnt(3)\n"
      "v_lshlrev_b32 v57, 16, v57\n"
      "v_and_b32 v56, 48, v243\n"
      "s_mov_b32 s44, 131072\n"
      "v_lshl_add_u32 v56, v56, 1, s44\n"
      "s_waitcnt lgkmcnt(0)\n"
      "v_add_f32 v24, v24, v25\n"
      "s_waitcnt lgkmcnt(0)\n"
      "v_add_f32 v26, v26, v27\n"
      "v_add_f32 v24, v24, v26\n"
      "v_cvt_pk_bf16_f32 v24, v24, v24\n"
      "v_and_b32 v24, 0xffff0000, v24\n"
      "v_mul_f32 v24, s84, v24\n"
      "v_cvt_pk_bf16_f32 v24, v24, v24\n"
      "v_and_b32 v24, 0xffff0000, v24\n"
      "v_add_f32 v24, v24, v57\n"
      "v_cvt_pk_bf16_f32 v24, v24, v24\n"
      "v_and_b32 v24, 0xffff0000, v24\n"
      "v_sub_f32 v24, v24, v57\n"
      "ds_write_b32 v58, v24 offset:1024\n"
      "ds_read_b128 v[28:31], v56 offset:1024\n"
      "ds_read_b128 v[32:35], v56 offset:1040\n"
      ".Ls_e30_%=:\n"
      "v_mfma_f32_16x16x32_bf16 v[100:103], v[164:167], a[36:39], v[100:103]\n"
      "ds_read2st64_b32 v[116:117], v244 offset0:36 offset1:37\n"
      "ds_read2st64_b32 v[118:119], v245 offset0:40 offset1:41\n"
      "v_mfma_f32_16x16x32_bf16 v[96:99], v[168:171], a[64:67], v[96:99]\n"
      "ds_read2st64_b32 v[120:121], v244 offset0:38 offset1:39\n"
      "ds_read2st64_b32 v[122:123], v245 offset0:42 offset1:43\n"
      "v_mfma_f32_16x16x32_bf16 v[100:103], v[168:171], a[68:71], v[100:103]\n"
      "ds_read2st64_b32 v[124:125], v246 offset0:44 offset1:45\n"
      "ds_read2st64_b32 v[126:127], v246 offset0:46 offset1:47\n"
      "v_mfma_f32_16x16x32_bf16 v[96:99], v[172:175], a[96:99], v[96:99]\n"
      "v_mfma_f32_16x16x32_bf16 v[64:67], v[252:255], v[44:47], v[64:67]\n"
      "v_mfma_f32_16x16x32_bf16 v[68:71], v[248:251], v[44:47], v[68:71]\n"
      "v_mfma_f32_16x16x32_bf16 v[100:103], v[172:175], a[100:103], v[100:103]\n"
      "v_mfma_f32_16x16x32_bf16 v[96:99], v[252:255], v[48:51], v[96:99]\n"
      "v_mfma_f32_16x16x32_bf16 v[100:103], v[248:251], v[48:51], v[100:103]\n"
      "v_mfma_f32_16x16x32_bf16 v[96:99], v[176:179], a[128:131], v[96:99]\n"
      "v_mfma_f32_16x16x32_bf16 v[100:103], v[176:179], a[132:135], v[100:103]\n"
      "v_mfma_f32_16x16x32_bf16 v[96:99], v[180:183], a[160:163], v[96:99]\n"
      "s_waitcnt lgkmcnt(0)\n"
      "v_mfma_f32_16x16x32_bf16 v[96:99], v[250:253], v[116:119], v[96:99]\n"
      "v_mfma_f32_16x16x32_bf16 v[100:103], v[250:253], v[120:123], v[100:103]\n"
      "v_mfma_f32_16x16x32_bf16 v[100:103], v[180:183], a[164:167], v[100:103]\n"
      "v_mfma_f32_16x16x32_bf16 v[96:99], v[252:255], v[124:127], v[96:99]\n"
      "v_mfma_f32_16x16x32_bf16 v[100:103], v[248:251], v[124:127], v[100:103]\n"
      "v_mfma_f32_16x16x32_bf16 v[96:99], v[184:187], a[192:195], v[96:99]\n"
      "v_mfma_f32_16x16x32_bf16 v[100:103], v[184:187], a[196:199], v[100:103]\n"
      "v_mfma_f32_16x16x32_bf16 v[96:99], v[188:191], a[224:227], v[96:99]\n"
      "v_mfma_f32_16x16x32_bf16 v[100:103], v[188:191], a[228:231], v[100:103]\n"
      "s_lshr_b32 s43, s42, 5\n"
      "s_lshl2_add_u32 s44, s36, s33\n"
      "s_cmp_eq_u32 s43, s44\n"
      "s_cbranch_scc0 .Ls_e32_%=\n"
      "v_lshrrev_b32 v24, 10, v7\n"
      "v_subrev_u32 v24, s42, v24\n"
      "v_cmp_eq_u32 vcc, 0, v24\n"
      "s_nop 1\n"
      "s_mov_b64 exec, vcc\n"
      "v_fma_f32 v208, v205, v28, v208\n"
      "v_fma_f32 v209, v205, v29, v209\n"
      "v_fma_f32 v210, v205, v30, v210\n"
      "v_fma_f32 v211, v205, v31, v211\n"
      "v_fma_f32 v216, v205, v32, v216\n"
      "v_fma_f32 v217, v205, v33, v217\n"
      "v_fma_f32 v218, v205, v34, v218\n"
      "v_fma_f32 v219, v205, v35, v219\n"
      "s_mov_b64 exec, -1\n"
      "v_cmp_eq_u32 vcc, -16, v24\n"
      "s_nop 1\n"
      "s_mov_b64 exec, vcc\n"
      "v_fma_f32 v212, v207, v28, v212\n"
      "v_fma_f32 v213, v207, v29, v213\n"
      "v_fma_f32 v214, v207, v30, v214\n"
      "v_fma_f32 v215, v207, v31, v215\n"
      "v_fma_f32 v220, v207, v32, v220\n"
      "v_fma_f32 v221, v207, v33, v221\n"
      "v_fma_f32 v222, v207, v34, v222\n"
      "v_fma_f32 v223, v207, v35, v223\n"
      "s_mov_b64 exec, -1\n"
      ".Ls_e32_%=:\n"
      "s_waitcnt vmcnt(0)\n"
      "v_bitop3_b32 v234, v227, v231, s37 bitop3:0x7e\n"
      "v_bitop3_b32 v234, v234, v235, s37 bitop3:0xf6\n"
      "v_cmp_ne_u32 vcc, 0, v234\n"
      "s_nop 1\n"
      "s_cbranch_vccnz .Ls_s34_%=\n"
      ".Ls_o35_%=:\n"
      "v_mov_b32 v227, v232\n"
      "v_mov_b32 v231, v233\n"
      "s_nop 0\n"
      "v_mfma_f32_16x16x32_bf16 v[64:67], v[252:255], v[224:227], v[64:67]\n"
      "v_mfma_f32_16x16x32_bf16 v[96:99], v[252:255], v[228:231], v[96:99]\n"
      "v_mfma_f32_16x16x32_bf16 v[68:71], v[248:251], v[224:227], v[68:71]\n"
      "v_mfma_f32_16x16x32_bf16 v[100:103], v[248:251], v[228:231], v[100:103]\n"
      "s_nop 4\n"
      "v_fma_f32 v208, v204, v64, v208\n"
      "v_fma_f32 v209, v204, v65, v209\n"
      "v_fma_f32 v210, v204, v66, v210\n"
      "v_fma_f32 v211, v204, v67, v211\n"
      "v_fma_f32 v216, v204, v96, v216\n"
      "v_fma_f32 v217, v204, v97, v217\n"
      "v_fma_f32 v218, v204, v98, v218\n"
      "v_fma_f32 v219, v204, v99, v219\n"
      "v_cvt_pk_bf16_f32 v72, v208, v209\n"
      "v_cvt_pk_bf16_f32 v73, v210, v211\n"
      "v_cvt_pk_bf16_f32 v74, v216, v217\n"
      "v_cvt_pk_bf16_f32 v75, v218, v219\n"
      "global_store_dwordx4 v7, v[72:75], s[20:21]\n"
      "v_fma_f32 v212, v206, v68, v212\n"
      "v_fma_f32 v213, v206, v69, v213\n"
      "v_fma_f32 v214, v206, v70, v214\n"
      "v_fma_f32 v215, v206, v71, v215\n"
      "v_fma_f32 v220, v206, v100, v220\n"
      "v_fma_f32 v221, v206, v101, v221\n"
      "v_fma_f32 v222, v206, v102, v222\n"
      "v_fma_f32 v223, v206, v103, v223\n"
      "v_cvt_pk_bf16_f32 v76, v212, v213\n"
      "v_cvt_pk_bf16_f32 v77, v214, v215\n"
      "v_cvt_pk_bf16_f32 v78, v220, v221\n"
      "v_cvt_pk_bf16_f32 v79, v222, v223\n"
      "global_store_dwordx4 v7, v[76:79], s[68:69]\n"
      "s_endpgm\n"
      ".Lprod_%=:\n"
      "s_waitcnt lgkmcnt(0)\n"
      "s_add_u32 s68, s16, s44\n"
      "s_addc_u32 s69, s17, 0\n"
      "s_add_u32 s12, s12, s45\n"
      "s_addc_u32 s13, s13, 0\n"
      "s_add_u32 s58, s18, 4718592\n"
      "s_addc_u32 s59, s19, 0\n"
      "v_mov_b32 v235, 0\n"
      "global_load_dword v235, v235, s[58:59] sc1\n"
      "v_and_b32 v238, 15, v243\n"
      "v_lshrrev_b32 v239, 4, v243\n"
      "v_xor_b32 v239, v239, v238\n"
      "v_and_b32 v239, 7, v239\n"
      "v_lshlrev_b32 v239, 4, v239\n"
      "v_lshl_or_b32 v238, v238, 7, v239\n"
      "v_add_u32 v248, s41, v238\n"
      "v_xor_b32 v249, 64, v248\n"
      "v_add_u32 v250, 65536, v238\n"
      "v_xor_b32 v251, 64, v250\n"
      "v_and_b32 v252, 8, v243\n"
      "v_lshlrev_b32 v252, 7, v252\n"
      "v_and_or_b32 v252, v243, 48, v252\n"
      "v_add_u32 v252, s41, v252\n"
      "v_add_u32 v238, s80, v242\n"
      "v_and_b32 v238, 0x3ff, v238\n"
      "v_lshl_or_b32 v244, v236, 12, v237\n"
      "v_add_u32 v245, 31744, v244\n"
      "v_add_u32 v246, 63488, v244\n"
      "v_add_u32 v247, 95232, v244\n"
      "s_bitcmp1_b32 s34, 1\n"
      "s_cselect_b64 s[8:9], s[10:11], s[8:9]\n"
      "s_and_b32 s45, s34, 1\n"
      "s_lshl2_add_u32 s45, s45, s33\n"
      "s_lshl_b32 s45, s45, 9\n"
      "s_lshl_b32 s76, s36, 19\n"
      "s_add_u32 s45, s45, s76\n"
      "s_add_u32 s8, s8, s45\n"
      "s_addc_u32 s9, s9, 0\n"
      "s_lshr_b32 s39, s32, 6\n"
      "s_and_b32 s45, s35, 2\n"
      "s_xor_b32 s39, s39, s45\n"
      "s_xor_b32 s39, s39, s33\n"
      "s_lshl_b32 s45, s39, 7\n"
      "s_add_u32 s76, s33, 0\n"
      "s_and_b32 s76, s76, 3\n"
      "s_lshl_b32 s76, s76, 17\n"
      "s_or_b32 s76, s76, s45\n"
      "s_add_u32 s77, s33, 1\n"
      "s_and_b32 s77, s77, 3\n"
      "s_lshl_b32 s77, s77, 17\n"
      "s_or_b32 s77, s77, s45\n"
      "s_add_u32 s78, s33, 2\n"
      "s_and_b32 s78, s78, 3\n"
      "s_lshl_b32 s78, s78, 17\n"
      "s_or_b32 s78, s78, s45\n"
      "s_add_u32 s79, s33, 3\n"
      "s_and_b32 s79, s79, 3\n"
      "s_lshl_b32 s79, s79, 17\n"
      "s_or_b32 s79, s79, s45\n"
      "s_lshl2_add_u32 s43, s34, s35\n"
      "s_and_b32 s44, s40, 31\n"
      "s_mul_i32 s45, s43, 266240\n"
      "s_lshl_b32 s42, s44, 13\n"
      "s_add_u32 s45, s45, s42\n"
      "s_add_u32 s45, s45, 4096\n"
      "s_add_u32 s62, s18, s45\n"
      "s_addc_u32 s63, s19, 0\n"
      "s_bitcmp1_b32 s34, 1\n"
      "s_cselect_b32 s85, 0x80008000, 0\n"
      "s_lshl3_add_u32 s42, s43, s83\n"
      "s_lshl2_add_u32 s42, s42, s33\n"
      "s_lshl_b32 s42, s42, 9\n"
      "s_add_u32 s42, s42, 1048576\n"
      "s_mov_b32 s64, 0xaaaaaaaa\n"
      "s_mov_b32 s65, 0xaaaaaaaa\n"
      "s_mov_b32 s66, 0xcccccccc\n"
      "s_mov_b32 s67, 0xcccccccc\n"
      "s_add_u32 s45, s80, 256\n"
      "s_and_b32 s45, s45, 0x380\n"
      "s_add_u32 s46, s68, s45\n"
      "s_addc_u32 s47, s69, 0\n"
      "s_add_u32 m0, s41, 8192\n"
      "s_nop 0\n"
      "global_load_lds_dwordx4 v240, s[46:47] offset:0\n"
      "global_load_lds_dwordx4 v241, s[46:47] offset:1024\n"
      "global_load_lds_dwordx4 v254, s[46:47] offset:2048\n"
      "global_load_lds_dwordx4 v255, s[46:47] offset:3072\n"
      "s_add_u32 s45, s80, 384\n"
      "s_and_b32 s45, s45, 0x380\n"
      "s_add_u32 s46, s68, s45\n"
      "s_addc_u32 s47, s69, 0\n"
      "s_add_u32 m0, s41, 12288\n"
      "s_nop 0\n"
      "global_load_lds_dwordx4 v240, s[46:47] offset:0\n"
      "global_load_lds_dwordx4 v241, s[46:47] offset:1024\n"
      "global_load_lds_dwordx4 v254, s[46:47] offset:2048\n"
      "global_load_lds_dwordx4 v255, s[46:47] offset:3072\n"
      "s_waitcnt vmcnt(13)\n"
      "s_barrier\n"
      "ds_read_b128 v[0:3], v248 offset:0\n"
      "ds_read_b128 v[4:7], v248 offset:2048\n"
      "ds_read_b128 v[8:11], v249 offset:0\n"
      "ds_read_b128 v[12:15], v249 offset:2048\n"
      "ds_read_b128 a[192:195], v250 offset:0\n"
      "ds_read_b128 a[196:199], v250 offset:2048\n"
      "ds_read_b128 a[200:203], v250 offset:32768\n"
      "ds_read_b128 a[204:207], v251 offset:0\n"
      "ds_read_b128 a[208:211], v251 offset:2048\n"
      "ds_read_b128 a[212:215], v251 offset:32768\n"
      "s_add_u32 s45, s34, 1\n"
      "s_and_b32 s45, s45, 3\n"
      "s_lshl_b32 s45, s45, 15\n"
      "s_or_b32 s45, s45, s81\n"
      "s_xor_b32 s45, s45, 512\n"
      "s_add_u32 s46, s12, s45\n"
      "s_addc_u32 s47, s13, 0\n"
      "s_add_u32 m0, s82, 81920\n"
      "s_nop 0\n"
      "global_load_lds_dwordx4 v240, s[46:47] offset:0\n"
      "global_load_lds_dwordx4 v241, s[46:47] offset:1024\n"
      "global_load_lds_dwordx4 v254, s[46:47] offset:2048\n"
      "global_load_lds_dwordx4 v255, s[46:47] offset:3072\n"
      "s_add_u32 s45, s34, 2\n"
      "s_and_b32 s45, s45, 3\n"
      "s_lshl_b32 s45, s45, 15\n"
      "s_or_b32 s45, s45, s81\n"
      "s_xor_b32 s45, s45, 512\n"
      "s_add_u32 s46, s12, s45\n"
      "s_addc_u32 s47, s13, 0\n"
      "s_add_u32 m0, s82, 114688\n"
      "s_nop 0\n"
      "global_load_lds_dwordx4 v240, s[46:47] offset:0\n"
      "global_load_lds_dwordx4 v241, s[46:47] offset:1024\n"
      "s_waitcnt lgkmcnt(5)\n"
      "v_mfma_f32_16x16x32_bf16 v[192:195], v[0:3], a[192:195], 0\n"
      "s_add_u32 s45, s80, 512\n"
      "s_and_b32 s45, s45, 0x380\n"
      "s_add_u32 s46, s68, s45\n"
      "s_addc_u32 s47, s69, 0\n"
      "s_add_u32 m0, s41, 0\n"
      "s_nop 0\n"
      "global_load_lds_dwordx4 v240, s[46:47] offset:0\n"
      "global_load_lds_dwordx4 v241, s[46:47] offset:1024\n"
      "global_load_lds_dwordx4 v254, s[46:47] offset:2048\n"
      "global_load_lds_dwordx4 v255, s[46:47] offset:3072\n"
      "v_mfma_f32_16x16x32_bf16 v[196:199], v[4:7], a[192:195], 0\n"
      "ds_read_b128 a[216:219], v250 offset:4096\n"
      "ds_read_b128 a[220:223], v250 offset:6144\n"
      "ds_read_b128 a[224:227], v250 offset:36864\n"
      "s_waitcnt lgkmcnt(7)\n"
      "v_mfma_f32_16x16x32_bf16 v[200:203], v[0:3], a[196:199], 0\n"
      "ds_read_b128 a[228:231], v251 offset:4096\n"
      "ds_read_b128 a[232:235], v251 offset:6144\n"
      "ds_read_b128 a[236:239], v251 offset:36864\n"
      "v_mfma_f32_16x16x32_bf16 v[204:207], v[4:7], a[196:199], 0\n"
      "s_waitcnt vmcnt(19)\n"
      "ds_read_b128 v[16:19], v248 offset:4096\n"
      "ds_read_b128 v[20:23], v248 offset:6144\n"
      "s_waitcnt lgkmcnt(11)\n"
      "v_mfma_f32_16x16x32_bf16 v[208:211], v[0:3], a[200:203], 0\n"
      "ds_read_b128 v[24:27], v249 offset:4096\n"
      "ds_read_b128 v[28:31], v249 offset:6144\n"
      "v_mfma_f32_16x16x32_bf16 v[212:215], v[4:7], a[200:203], 0\n"
      "s_waitcnt lgkmcnt(12)\n"
      "v_mfma_f32_16x16x32_bf16 v[192:195], v[8:11], a[204:207], v[192:195]\n"
      "v_mfma_f32_16x16x32_bf16 v[196:199], v[12:15], a[204:207], v[196:199]\n"
      "s_waitcnt lgkmcnt(11)\n"
      "v_mfma_f32_16x16x32_bf16 v[200:203], v[8:11], a[208:211], v[200:203]\n"
      "v_mfma_f32_16x16x32_bf16 v[204:207], v[12:15], a[208:211], v[204:207]\n"
      "s_waitcnt lgkmcnt(10)\n"
      "v_mfma_f32_16x16x32_bf16 v[208:211], v[8:11], a[212:215], v[208:211]\n"
      "v_mfma_f32_16x16x32_bf16 v[212:215], v[12:15], a[212:215], v[212:215]\n"
      "s_waitcnt lgkmcnt(3)\n"
      "v_mfma_f32_16x16x32_bf16 v[192:195], v[16:19], a[216:219], v[192:195]\n"
      "s_waitcnt lgkmcnt(0)\n"
      "s_add_u32 s45, s80, 640\n"
      "s_and_b32 s45, s45, 0x380\n"
      "s_add_u32 s46, s68, s45\n"
      "s_addc_u32 s47, s69, 0\n"
      "s_add_u32 m0, s41, 4096\n"
      "s_nop 0\n"
      "global_load_lds_dwordx4 v240, s[46:47] offset:0\n"
      "global_load_lds_dwordx4 v241, s[46:47] offset:1024\n"
      "global_load_lds_dwordx4 v254, s[46:47] offset:2048\n"
      "global_load_lds_dwordx4 v255, s[46:47] offset:3072\n"
      "v_mfma_f32_16x16x32_bf16 v[196:199], v[20:23], a[216:219], v[196:199]\n"
      "ds_read_b128 a[240:243], v250 offset:8192\n"
      "ds_read_b128 a[244:247], v250 offset:10240\n"
      "ds_read_b128 a[248:251], v250 offset:40960\n"
      "v_mfma_f32_16x16x32_bf16 v[200:203], v[16:19], a[220:223], v[200:203]\n"
      "ds_read_b128 a[252:255], v251 offset:8192\n"
      "ds_read_b128 a[192:195], v251 offset:10240\n"
      "ds_read_b128 a[196:199], v251 offset:40960\n"
      "v_mfma_f32_16x16x32_bf16 v[204:207], v[20:23], a[220:223], v[204:207]\n"
      "s_waitcnt vmcnt(18)\n"
      "ds_read_b128 v[32:35], v248 offset:8192\n"
      "ds_read_b128 v[36:39], v248 offset:10240\n"
      "v_mfma_f32_16x16x32_bf16 v[208:211], v[16:19], a[224:227], v[208:211]\n"
      "ds_read_b128 v[40:43], v249 offset:8192\n"
      "ds_read_b128 v[44:47], v249 offset:10240\n"
      "v_mfma_f32_16x16x32_bf16 v[212:215], v[20:23], a[224:227], v[212:215]\n"
      "v_mfma_f32_16x16x32_bf16 v[192:195], v[24:27], a[228:231], v[192:195]\n"
      "v_mfma_f32_16x16x32_bf16 v[196:199], v[28:31], a[228:231], v[196:199]\n"
      "v_mfma_f32_16x16x32_bf16 v[200:203], v[24:27], a[232:235], v[200:203]\n"
      "v_mfma_f32_16x16x32_bf16 v[204:207], v[28:31], a[232:235], v[204:207]\n"
      "v_mfma_f32_16x16x32_bf16 v[208:211], v[24:27], a[236:239], v[208:211]\n"
      "v_mfma_f32_16x16x32_bf16 v[212:215], v[28:31], a[236:239], v[212:215]\n"
      "s_waitcnt lgkmcnt(3)\n"
      "v_mfma_f32_16x16x32_bf16 v[192:195], v[32:35], a[240:243], v[192:195]\n"
      "s_waitcnt lgkmcnt(0)\n"
      "s_add_u32 s45, s80, 768\n"
      "s_and_b32 s45, s45, 0x380\n"
      "s_add_u32 s46, s68, s45\n"
      "s_addc_u32 s47, s69, 0\n"
      "s_add_u32 m0, s41, 8192\n"
      "s_nop 0\n"
      "global_load_lds_dwordx4 v240, s[46:47] offset:0\n"
      "global_load_lds_dwordx4 v241, s[46:47] offset:1024\n"
      "global_load_lds_dwordx4 v254, s[46:47] offset:2048\n"
      "global_load_lds_dwordx4 v255, s[46:47] offset:3072\n"
      "v_mfma_f32_16x16x32_bf16 v[196:199], v[36:39], a[240:243], v[196:199]\n"
      "v_readfirstlane_b32 s37, v235\n"
      "s_nop 1\n"
      "s_mul_i32 s37, s37, 0x9E3779B1\n"
      "v_mfma_f32_16x16x32_bf16 v[200:203], v[32:35], a[244:247], v[200:203]\n"
      "s_add_u32 s37, s37, 0x7F4A7C15\n"
      "s_mov_b32 s38, 0x8000\n"
      "ds_read_b128 a[200:203], v250 offset:12288\n"
      "ds_read_b128 a[204:207], v250 offset:14336\n"
      "v_mfma_f32_16x16x32_bf16 v[204:207], v[36:39], a[244:247], v[204:207]\n"
      "ds_read_b128 a[208:211], v250 offset:45056\n"
      "ds_read_b128 a[212:215], v251 offset:12288\n"
      "ds_read_b128 a[216:219], v251 offset:14336\n"
      "v_mfma_f32_16x16x32_bf16 v[208:211], v[32:35], a[248:251], v[208:211]\n"
      "ds_read_b128 a[220:223], v251 offset:45056\n"
      "s_waitcnt vmcnt(18)\n"
      "ds_read_b128 v[48:51], v248 offset:12288\n"
      "v_mfma_f32_16x16x32_bf16 v[212:215], v[36:39], a[248:251], v[212:215]\n"
      "ds_read_b128 v[52:55], v248 offset:14336\n"
      "ds_read_b128 v[56:59], v249 offset:12288\n"
      "ds_read_b128 v[60:63], v249 offset:14336\n"
      "v_mfma_f32_16x16x32_bf16 v[192:195], v[40:43], a[252:255], v[192:195]\n"
      "v_mfma_f32_16x16x32_bf16 v[196:199], v[44:47], a[252:255], v[196:199]\n"
      "v_mfma_f32_16x16x32_bf16 v[200:203], v[40:43], a[192:195], v[200:203]\n"
      "v_mfma_f32_16x16x32_bf16 v[204:207], v[44:47], a[192:195], v[204:207]\n"
      "v_mfma_f32_16x16x32_bf16 v[208:211], v[40:43], a[196:199], v[208:211]\n"
      "v_mfma_f32_16x16x32_bf16 v[212:215], v[44:47], a[196:199], v[212:215]\n"
      "s_waitcnt lgkmcnt(3)\n"
      "v_mfma_f32_16x16x32_bf16 v[192:195], v[48:51], a[200:203], v[192:195]\n"
      "s_waitcnt lgkmcnt(0)\n"
      "s_add_u32 s45, s80, 896\n"
      "s_and_b32 s45, s45, 0x380\n"
      "s_add_u32 s46, s68, s45\n"
      "s_addc_u32 s47, s69, 0\n"
      "s_add_u32 m0, s41, 12288\n"
      "s_nop 0\n"
      "global_load_lds_dwordx4 v240, s[46:47] offset:0\n"
      "global_load_lds_dwordx4 v241, s[46:47] offset:1024\n"
      "global_load_lds_dwordx4 v254, s[46:47] offset:2048\n"
      "global_load_lds_dwordx4 v255, s[46:47] offset:3072\n"
      "v_mfma_f32_16x16x32_bf16 v[196:199], v[52:55], a[200:203], v[196:199]\n"
      "s_waitcnt vmcnt(16)\n"
      "s_barrier\n"
      "ds_read_b128 a[224:227], v250 offset:16384\n"
      "v_mfma_f32_16x16x32_bf16 v[200:203], v[48:51], a[204:207], v[200:203]\n"
      "ds_read_b128 a[228:231], v250 offset:18432\n"
      "ds_read_b128 a[232:235], v250 offset:49152\n"
      "ds_read_b128 a[236:239], v251 offset:16384\n"
      "v_mfma_f32_16x16x32_bf16 v[204:207], v[52:55], a[204:207], v[204:207]\n"
      "ds_read_b128 a[240:243], v251 offset:18432\n"
      "ds_read_b128 a[244:247], v251 offset:49152\n"
      "s_waitcnt vmcnt(12)\n"
      "ds_read_b128 v[64:67], v248 offset:0\n"
      "v_mfma_f32_16x16x32_bf16 v[208:211], v[48:51], a[208:211], v[208:211]\n"
      "ds_read_b128 v[68:71], v248 offset:2048\n"
      "ds_read_b128 v[72:75], v249 offset:0\n"
      "ds_read_b128 v[76:79], v249 offset:2048\n"
      "v_mfma_f32_16x16x32_bf16 v[212:215], v[52:55], a[208:211], v[212:215]\n"
      "v_mfma_f32_16x16x32_bf16 v[192:195], v[56:59], a[212:215], v[192:195]\n"
      "v_mfma_f32_16x16x32_bf16 v[196:199], v[60:63], a[212:215], v[196:199]\n"
      "v_mfma_f32_16x16x32_bf16 v[200:203], v[56:59], a[216:219], v[200:203]\n"
      "v_mfma_f32_16x16x32_bf16 v[204:207], v[60:63], a[216:219], v[204:207]\n"
      "v_mfma_f32_16x16x32_bf16 v[208:211], v[56:59], a[220:223], v[208:211]\n"
      "v_mfma_f32_16x16x32_bf16 v[212:215], v[60:63], a[220:223], v[212:215]\n"
      "s_waitcnt lgkmcnt(3)\n"
      "v_mfma_f32_16x16x32_bf16 v[192:195], v[64:67], a[224:227], v[192:195]\n"
      "s_waitcnt lgkmcnt(0)\n"
      "s_add_u32 m0, s41, 0\n"
      "s_nop 0\n"
      "global_load_lds_dwordx4 v238, s[24:25]\n"
      "s_add_u32 m0, s41, 1024\n"
      "s_nop 0\n"
      "global_load_lds_dwordx4 v238, s[22:23]\n"
      "v_mfma_f32_16x16x32_bf16 v[196:199], v[68:71], a[224:227], v[196:199]\n"
      "s_add_u32 s45, s34, 2\n"
      "s_and_b32 s45, s45, 3\n"
      "s_lshl_b32 s45, s45, 15\n"
      "s_or_b32 s45, s45, s81\n"
      "s_add_u32 s46, s12, s45\n"
      "s_addc_u32 s47, s13, 0\n"
      "s_add_u32 m0, s82, 98304\n"
      "s_nop 0\n"
      "global_load_lds_dwordx4 v254, s[46:47] offset:2048\n"
      "global_load_lds_dwordx4 v255, s[46:47] offset:3072\n"
      "v_mfma_f32_16x16x32_bf16 v[200:203], v[64:67], a[228:231], v[200:203]\n"
      "ds_read_b128 a[248:251], v250 offset:20480\n"
      "ds_read_b128 a[252:255], v250 offset:22528\n"
      "ds_read_b128 a[192:195], v250 offset:53248\n"
      "v_mfma_f32_16x16x32_bf16 v[204:207], v[68:71], a[228:231], v[204:207]\n"
      "ds_read_b128 a[196:199], v251 offset:20480\n"
      "ds_read_b128 a[200:203], v251 offset:22528\n"
      "ds_read_b128 a[204:207], v251 offset:53248\n"
      "v_mfma_f32_16x16x32_bf16 v[208:211], v[64:67], a[232:235], v[208:211]\n"
      "s_waitcnt vmcnt(12)\n"
      "ds_read_b128 v[80:83], v248 offset:4096\n"
      "ds_read_b128 v[84:87], v248 offset:6144\n"
      "v_mfma_f32_16x16x32_bf16 v[212:215], v[68:71], a[232:235], v[212:215]\n"
      "ds_read_b128 v[88:91], v249 offset:4096\n"
      "ds_read_b128 v[92:95], v249 offset:6144\n"
      "v_mfma_f32_16x16x32_bf16 v[192:195], v[72:75], a[236:239], v[192:195]\n"
      "v_mfma_f32_16x16x32_bf16 v[196:199], v[76:79], a[236:239], v[196:199]\n"
      "v_mfma_f32_16x16x32_bf16 v[200:203], v[72:75], a[240:243], v[200:203]\n"
      "v_mfma_f32_16x16x32_bf16 v[204:207], v[76:79], a[240:243], v[204:207]\n"
      "v_mfma_f32_16x16x32_bf16 v[208:211], v[72:75], a[244:247], v[208:211]\n"
      "v_mfma_f32_16x16x32_bf16 v[212:215], v[76:79], a[244:247], v[212:215]\n"
      "s_waitcnt lgkmcnt(3)\n"
      "v_mfma_f32_16x16x32_bf16 v[192:195], v[80:83], a[248:251], v[192:195]\n"
      "s_add_u32 s45, s34, 2\n"
      "s_and_b32 s45, s45, 3\n"
      "s_lshl_b32 s45, s45, 15\n"
      "s_or_b32 s45, s45, s81\n"
      "s_xor_b32 s45, s45, 512\n"
      "s_add_u32 s46, s12, s45\n"
      "s_addc_u32 s47, s13, 0\n"
      "s_add_u32 m0, s82, 114688\n"
      "s_nop 0\n"
      "global_load_lds_dwordx4 v254, s[46:47] offset:2048\n"
      "global_load_lds_dwordx4 v255, s[46:47] offset:3072\n"
      "s_waitcnt lgkmcnt(2)\n"
      "v_mfma_f32_16x16x32_bf16 v[196:199], v[84:87], a[248:251], v[196:199]\n"
      "s_add_u32 s45, s34, 3\n"
      "s_and_b32 s45, s45, 3\n"
      "s_lshl_b32 s45, s45, 15\n"
      "s_or_b32 s45, s45, s81\n"
      "s_add_u32 s46, s12, s45\n"
      "s_addc_u32 s47, s13, 0\n"
      "s_add_u32 m0, s82, 131072\n"
      "s_nop 0\n"
      "global_load_lds_dwordx4 v240, s[46:47] offset:0\n"
      "global_load_lds_dwordx4 v241, s[46:47] offset:1024\n"
      "global_load_lds_dwordx4 v254, s[46:47] offset:2048\n"
      "global_load_lds_dwordx4 v255, s[46:47] offset:3072\n"
      "v_mfma_f32_16x16x32_bf16 v[200:203], v[80:83], a[252:255], v[200:203]\n"
      "ds_read_b128 a[208:211], v250 offset:24576\n"
      "ds_read_b128 a[212:215], v250 offset:26624\n"
      "ds_read_b128 a[216:219], v250 offset:57344\n"
      "v_mfma_f32_16x16x32_bf16 v[204:207], v[84:87], a[252:255], v[204:207]\n"
      "ds_read_b128 a[220:223], v251 offset:24576\n"
      "ds_read_b128 a[224:227], v251 offset:26624\n"
      "ds_read_b128 a[228:231], v251 offset:57344\n"
      "v_mfma_f32_16x16x32_bf16 v[208:211], v[80:83], a[192:195], v[208:211]\n"
      "s_waitcnt vmcnt(14)\n"
      "ds_read_b128 v[96:99], v248 offset:8192\n"
      "ds_read_b128 v[100:103], v248 offset:10240\n"
      "v_mfma_f32_16x16x32_bf16 v[212:215], v[84:87], a[192:195], v[212:215]\n"
      "ds_read_b128 v[104:107], v249 offset:8192\n"
      "ds_read_b128 v[108:111], v249 offset:10240\n"
      "s_waitcnt lgkmcnt(11)\n"
      "v_mfma_f32_16x16x32_bf16 v[192:195], v[88:91], a[196:199], v[192:195]\n"
      "s_waitcnt lgkmcnt(10)\n"
      "v_mfma_f32_16x16x32_bf16 v[196:199], v[92:95], a[196:199], v[196:199]\n"
      "v_mfma_f32_16x16x32_bf16 v[200:203], v[88:91], a[200:203], v[200:203]\n"
      "v_mfma_f32_16x16x32_bf16 v[204:207], v[92:95], a[200:203], v[204:207]\n"
      "v_mfma_f32_16x16x32_bf16 v[208:211], v[88:91], a[204:207], v[208:211]\n"
      "v_mfma_f32_16x16x32_bf16 v[212:215], v[92:95], a[204:207], v[212:215]\n"
      "s_waitcnt lgkmcnt(3)\n"
      "v_mfma_f32_16x16x32_bf16 v[192:195], v[96:99], a[208:211], v[192:195]\n"
      "s_add_u32 s45, s34, 3\n"
      "s_and_b32 s45, s45, 3\n"
      "s_lshl_b32 s45, s45, 15\n"
      "s_or_b32 s45, s45, s81\n"
      "s_xor_b32 s45, s45, 512\n"
      "s_add_u32 s46, s12, s45\n"
      "s_addc_u32 s47, s13, 0\n"
      "s_add_u32 m0, s82, 147456\n"
      "s_nop 0\n"
      "global_load_lds_dwordx4 v240, s[46:47] offset:0\n"
      "global_load_lds_dwordx4 v241, s[46:47] offset:1024\n"
      "global_load_lds_dwordx4 v254, s[46:47] offset:2048\n"
      "global_load_lds_dwordx4 v255, s[46:47] offset:3072\n"
      "s_waitcnt lgkmcnt(2)\n"
      "v_mfma_f32_16x16x32_bf16 v[196:199], v[100:103], a[208:211], v[196:199]\n"
      "ds_read_b128 a[232:235], v250 offset:28672\n"
      "ds_read_b128 a[236:239], v250 offset:30720\n"
      "ds_read_b128 a[240:243], v250 offset:61440\n"
      "v_mfma_f32_16x16x32_bf16 v[200:203], v[96:99], a[212:215], v[200:203]\n"
      "ds_read_b128 a[244:247], v251 offset:28672\n"
      "ds_read_b128 a[248:251], v251 offset:30720\n"
      "ds_read_b128 a[252:255], v251 offset:61440\n"
      "v_mfma_f32_16x16x32_bf16 v[204:207], v[100:103], a[212:215], v[204:207]\n"
      "s_waitcnt vmcnt(14)\n"
      "ds_read_b128 v[112:115], v248 offset:12288\n"
      "ds_read_b128 v[116:119], v248 offset:14336\n"
      "v_mfma_f32_16x16x32_bf16 v[208:211], v[96:99], a[216:219], v[208:211]\n"
      "ds_read_b128 v[120:123], v249 offset:12288\n"
      "ds_read_b128 v[124:127], v249 offset:14336\n"
      "v_mfma_f32_16x16x32_bf16 v[212:215], v[100:103], a[216:219], v[212:215]\n"
      "s_waitcnt lgkmcnt(11)\n"
      "v_mfma_f32_16x16x32_bf16 v[192:195], v[104:107], a[220:223], v[192:195]\n"
      "s_waitcnt lgkmcnt(10)\n"
      "v_mfma_f32_16x16x32_bf16 v[196:199], v[108:111], a[220:223], v[196:199]\n"
      "v_mfma_f32_16x16x32_bf16 v[200:203], v[104:107], a[224:227], v[200:203]\n"
      "v_mfma_f32_16x16x32_bf16 v[204:207], v[108:111], a[224:227], v[204:207]\n"
      "v_mfma_f32_16x16x32_bf16 v[208:211], v[104:107], a[228:231], v[208:211]\n"
      "v_mfma_f32_16x16x32_bf16 v[212:215], v[108:111], a[228:231], v[212:215]\n"
      "s_waitcnt lgkmcnt(3)\n"
      "v_mfma_f32_16x16x32_bf16 v[192:195], v[112:115], a[232:235], v[192:195]\n"
      "s_waitcnt vmcnt(12)\n"
      "ds_read_b128 v[136:139], v252 offset:0\n"
      "s_waitcnt lgkmcnt(3)\n"
      "v_mfma_f32_16x16x32_bf16 v[196:199], v[116:119], a[232:235], v[196:199]\n"
      "ds_read_b128 v[140:143], v252 offset:64\n"
      "s_waitcnt lgkmcnt(3)\n"
      "v_mfma_f32_16x16x32_bf16 v[192:195], v[120:123], a[244:247], v[192:195]\n"
      "ds_read_b128 v[144:147], v252 offset:128\n"
      "s_waitcnt lgkmcnt(3)\n"
      "v_mfma_f32_16x16x32_bf16 v[196:199], v[124:127], a[244:247], v[196:199]\n"
      "v_mfma_f32_16x16x32_bf16 v[200:203], v[112:115], a[236:239], v[200:203]\n"
      "ds_read_b128 v[148:151], v252 offset:192\n"
      "v_mfma_f32_16x16x32_bf16 v[204:207], v[116:119], a[236:239], v[204:207]\n"
      "v_mfma_f32_16x16x32_bf16 v[200:203], v[120:123], a[248:251], v[200:203]\n"
      "ds_read_b128 v[152:155], v252 offset:256\n"
      "v_cvt_pk_bf16_f32 v224, v192, v193\n"
      "v_cvt_pk_bf16_f32 v225, v194, v195\n"
      "v_mfma_f32_16x16x32_bf16 v[204:207], v[124:127], a[248:251], v[204:207]\n"
      "v_cvt_pk_bf16_f32 v226, v196, v197\n"
      "v_cvt_pk_bf16_f32 v227, v198, v199\n"
      "v_xor_b32 v224, s85, v224\n"
      "v_mfma_f32_16x16x32_bf16 v[208:211], v[112:115], a[240:243], v[208:211]\n"
      "ds_read_b128 v[156:159], v252 offset:320\n"
      "v_xor_b32 v225, s85, v225\n"
      "v_xor_b32 v226, s85, v226\n"
      "v_mfma_f32_16x16x32_bf16 v[212:215], v[116:119], a[240:243], v[212:215]\n"
      "v_xor_b32 v227, s85, v227\n"
      "v_mfma_f32_16x16x32_bf16 v[208:211], v[120:123], a[252:255], v[208:211]\n"
      "ds_read_b128 v[160:163], v252 offset:384\n"
      "global_store_dwordx4 v242, v[224:227], s[62:63] offset:-4096 sc1\n"
      "v_cvt_pk_bf16_f32 v228, v200, v201\n"
      "v_mfma_f32_16x16x32_bf16 v[212:215], v[124:127], a[252:255], v[212:215]\n"
      "v_cvt_pk_bf16_f32 v229, v202, v203\n"
      "v_cvt_pk_bf16_f32 v230, v204, v205\n"
      "v_cvt_pk_bf16_f32 v231, v206, v207\n"
      "s_waitcnt lgkmcnt(6)\n"
      "v_mfma_f32_16x16x32_bf16 v[128:131], v[0:3], v[136:139], 0\n"
      "ds_read_b128 v[164:167], v252 offset:448\n"
      "v_xor_b32 v228, s85, v228\n"
      "v_xor_b32 v229, s85, v229\n"
      "v_mfma_f32_16x16x32_bf16 v[132:135], v[4:7], v[136:139], 0\n"
      "v_xor_b32 v230, s85, v230\n"
      "v_xor_b32 v231, s85, v231\n"
      "s_waitcnt lgkmcnt(6)\n"
      "v_mfma_f32_16x16x32_bf16 v[128:131], v[8:11], v[140:143], v[128:131]\n"
      "ds_read_b128 v[136:139], v252 offset:512\n"
      "global_store_dwordx4 v242, v[228:231], s[62:63] offset:-3072 sc1\n"
      "v_cvt_pk_bf16_f32 v232, v208, v209\n"
      "v_mfma_f32_16x16x32_bf16 v[132:135], v[12:15], v[140:143], v[132:135]\n"
      "v_cvt_pk_bf16_f32 v233, v210, v211\n"
      "v_cvt_pk_bf16_f32 v234, v212, v213\n"
      "v_cvt_pk_bf16_f32 v235, v214, v215\n"
      "s_waitcnt lgkmcnt(6)\n"
      "v_mfma_f32_16x16x32_bf16 v[128:131], v[16:19], v[144:147], v[128:131]\n"
      "ds_read_b128 v[140:143], v252 offset:576\n"
      "v_xor_b32 v232, s85, v232\n"
      "v_xor_b32 v233, s85, v233\n"
      "v_mfma_f32_16x16x32_bf16 v[132:135], v[20:23], v[144:147], v[132:135]\n"
      "v_xor_b32 v234, s85, v234\n"
      "v_xor_b32 v235, s85, v235\n"
      "s_waitcnt lgkmcnt(6)\n"
      "v_mfma_f32_16x16x32_bf16 v[128:131], v[24:27], v[148:151], v[128:131]\n"
      "ds_read_b128 v[144:147], v252 offset:640\n"
      "global_store_dwordx4 v242, v[232:235], s[62:63] offset:-2048 sc1\n"
      "v_mfma_f32_16x16x32_bf16 v[132:135], v[28:31], v[148:151], v[132:135]\n"
      "s_xor_b32 s70, s76, 0\n"
      "s_add_u32 s70, s8, s70\n"
      "s_addc_u32 s71, s9, 0\n"
      "s_add_u32 m0, s41, 4096\n"
      "s_waitcnt lgkmcnt(6)\n"
      "v_mfma_f32_16x16x32_bf16 v[128:131], v[32:35], v[152:155], v[128:131]\n"
      "s_add_u32 s48, s82, 32768\n"
      "ds_read_b128 v[148:151], v252 offset:704\n"
      "global_load_lds_dwordx4 v244, s[70:71] offset:0\n"
      "v_subrev_u32 v236, s41, v248\n"
      "v_mfma_f32_16x16x32_bf16 v[132:135], v[36:39], v[152:155], v[132:135]\n"
      "s_add_u32 s48, s48, 65536\n"
      "s_waitcnt vmcnt(4)\n"
      "s_barrier\n"
      "s_waitcnt lgkmcnt(6)\n"
      "v_mfma_f32_16x16x32_bf16 v[128:131], v[40:43], v[156:159], v[128:131]\n"
      "ds_read_b128 v[152:155], v252 offset:768\n"
      "ds_read_b128 a[192:195], v250 offset:34816\n"
      "v_add_u32 v236, s48, v236\n"
      "v_mfma_f32_16x16x32_bf16 v[132:135], v[44:47], v[156:159], v[132:135]\n"
      "ds_read_b128 a[196:199], v251 offset:34816\n"
      "ds_read_b128 a[200:203], v250 offset:38912\n"
      "v_xor_b32 v237, 64, v236\n"
      "s_waitcnt lgkmcnt(9)\n"
      "v_mfma_f32_16x16x32_bf16 v[128:131], v[48:51], v[160:163], v[128:131]\n"
      "ds_read_b128 v[156:159], v252 offset:832\n"
      "global_load_lds_dwordx4 v245, s[70:71] offset:1024\n"
      "v_mfma_f32_16x16x32_bf16 v[132:135], v[52:55], v[160:163], v[132:135]\n"
      "ds_read_b128 a[204:207], v251 offset:38912\n"
      "ds_read_b128 a[208:211], v250 offset:43008\n"
      "s_waitcnt lgkmcnt(11)\n"
      "v_mfma_f32_16x16x32_bf16 v[128:131], v[56:59], v[164:167], v[128:131]\n"
      "ds_read_b128 v[160:163], v252 offset:896\n"
      "ds_read_b128 a[212:215], v251 offset:43008\n"
      "v_mfma_f32_16x16x32_bf16 v[132:135], v[60:63], v[164:167], v[132:135]\n"
      "ds_read_b128 a[216:219], v250 offset:47104\n"
      "s_waitcnt lgkmcnt(13)\n"
      "v_mfma_f32_16x16x32_bf16 v[128:131], v[64:67], v[136:139], v[128:131]\n"
      "ds_read_b128 v[164:167], v252 offset:960\n"
      "global_load_lds_dwordx4 v246, s[70:71] offset:2048\n"
      "v_mfma_f32_16x16x32_bf16 v[132:135], v[68:71], v[136:139], v[132:135]\n"
      "ds_read_b128 a[220:223], v251 offset:47104\n"
      "s_waitcnt lgkmcnt(14)\n"
      "v_mfma_f32_16x16x32_bf16 v[128:131], v[72:75], v[140:143], v[128:131]\n"
      "v_mfma_f32_16x16x32_bf16 v[132:135], v[76:79], v[140:143], v[132:135]\n"
      "ds_read_b128 a[224:227], v250 offset:51200\n"
      "s_waitcnt lgkmcnt(14)\n"
      "v_mfma_f32_16x16x32_bf16 v[128:131], v[80:83], v[144:147], v[128:131]\n"
      "global_load_lds_dwordx4 v247, s[70:71] offset:3072\n"
      "v_mfma_f32_16x16x32_bf16 v[132:135], v[84:87], v[144:147], v[132:135]\n"
      "ds_read_b128 a[228:231], v251 offset:51200\n"
      "s_waitcnt lgkmcnt(14)\n"
      "v_mfma_f32_16x16x32_bf16 v[128:131], v[88:91], v[148:151], v[128:131]\n"
      "v_mfma_f32_16x16x32_bf16 v[132:135], v[92:95], v[148:151], v[132:135]\n"
      "s_lshl_b32 s70, s34, 15\n"
      "s_or_b32 s70, s70, s81\n"
      "s_add_u32 s70, s12, s70\n"
      "s_addc_u32 s71, s13, 0\n"
      "s_add_u32 m0, s82, 65536\n"
      "s_waitcnt lgkmcnt(13)\n"
      "v_mfma_f32_16x16x32_bf16 v[128:131], v[96:99], v[152:155], v[128:131]\n"
      "global_load_lds_dwordx4 v240, s[70:71] offset:0\n"
      "ds_read_b128 a[232:235], v250 offset:55296\n"
      "v_mfma_f32_16x16x32_bf16 v[132:135], v[100:103], v[152:155], v[132:135]\n"
      "ds_read_b128 a[236:239], v251 offset:55296\n"
      "s_waitcnt lgkmcnt(11)\n"
      "v_mfma_f32_16x16x32_bf16 v[128:131], v[104:107], v[156:159], v[128:131]\n"
      "v_mfma_f32_16x16x32_bf16 v[132:135], v[108:111], v[156:159], v[132:135]\n"
      "ds_read_b128 a[240:243], v250 offset:59392\n"
      "s_waitcnt lgkmcnt(9)\n"
      "v_mfma_f32_16x16x32_bf16 v[128:131], v[112:115], v[160:163], v[128:131]\n"
      "global_load_lds_dwordx4 v241, s[70:71] offset:1024\n"
      "v_mfma_f32_16x16x32_bf16 v[132:135], v[116:119], v[160:163], v[132:135]\n"
      "ds_read_b128 a[244:247], v251 offset:59392\n"
      "s_waitcnt lgkmcnt(7)\n"
      "v_mfma_f32_16x16x32_bf16 v[128:131], v[120:123], v[164:167], v[128:131]\n"
      "v_mfma_f32_16x16x32_bf16 v[132:135], v[124:127], v[164:167], v[132:135]\n"
      "ds_read_b128 a[248:251], v250 offset:63488\n"
      "v_mfma_f32_16x16x32_bf16 v[216:219], v[0:3], a[192:195], 0\n"
      "global_load_lds_dwordx4 v254, s[70:71] offset:2048\n"
      "v_mfma_f32_16x16x32_bf16 v[220:223], v[4:7], a[192:195], 0\n"
      "ds_read_b128 a[252:255], v251 offset:63488\n"
      "v_xor_b32 v250, 196608, v250\n"
      "v_xor_b32 v251, 196608, v251\n"
      "v_mfma_f32_16x16x32_bf16 v[216:219], v[8:11], a[196:199], v[216:219]\n"
      "s_mov_b32 s22, 0xf0f0f0f0\n"
      "s_mov_b32 s23, 0xf0f0f0f0\n"
      "v_cndmask_b32 v128, v128, v129, s[64:65]\n"
      "v_cndmask_b32 v130, v130, v131, s[64:65]\n"
      "v_mfma_f32_16x16x32_bf16 v[220:223], v[12:15], a[196:199], v[220:223]\n"
      "ds_read_b128 a[192:195], v250 offset:0\n"
      "v_cndmask_b32 v132, v132, v133, s[64:65]\n"
      "v_cndmask_b32 v134, v134, v135, s[64:65]\n"
      "v_mfma_f32_16x16x32_bf16 v[216:219], v[16:19], a[200:203], v[216:219]\n"
      "global_load_lds_dwordx4 v255, s[70:71] offset:3072\n"
      "v_cndmask_b32 v128, v128, v130, s[66:67]\n"
      "v_cndmask_b32 v132, v132, v134, s[66:67]\n"
      "v_mfma_f32_16x16x32_bf16 v[220:223], v[20:23], a[200:203], v[220:223]\n"
      "ds_read_b128 a[196:199], v250 offset:2048\n"
      "v_lshrrev_b32 v133, 4, v243\n"
      "v_cndmask_b32 v128, v128, v132, s[22:23]\n"
      "v_mfma_f32_16x16x32_bf16 v[216:219], v[24:27], a[204:207], v[216:219]\n"
      "v_and_b32 v134, 7, v243\n"
      "v_cvt_pk_bf16_f32 v128, v128, v128\n"
      "s_lshl_b32 s48, s40, 5\n"
      "v_mfma_f32_16x16x32_bf16 v[220:223], v[28:31], a[204:207], v[220:223]\n"
      "ds_read_b128 a[200:203], v251 offset:0\n"
      "v_and_b32 v128, 0xffff0000, v128\n"
      "v_lshl_or_b32 v133, v133, 3, v134\n"
      "v_mfma_f32_16x16x32_bf16 v[216:219], v[32:35], a[208:211], v[216:219]\n"
      "s_lshl_b32 s70, s34, 15\n"
      "s_or_b32 s70, s70, s81\n"
      "s_xor_b32 s70, s70, 512\n"
      "s_add_u32 s70, s12, s70\n"
      "s_addc_u32 s71, s13, 0\n"
      "s_add_u32 m0, s82, 81920\n"
      "v_mfma_f32_16x16x32_bf16 v[220:223], v[36:39], a[208:211], v[220:223]\n"
      "global_load_lds_dwordx4 v240, s[70:71] offset:0\n"
      "ds_read_b128 a[204:207], v251 offset:2048\n"
      "v_add_u32 v133, s48, v133\n"
      "v_mfma_f32_16x16x32_bf16 v[216:219], v[40:43], a[212:215], v[216:219]\n"
      "v_ashrrev_i32 v129, 31, v128\n"
      "v_mul_f32 v130, 0xbfb8aa3b, v128\n"
      "v_or_b32 v129, 0x80000000, v129\n"
      "v_mfma_f32_16x16x32_bf16 v[220:223], v[44:47], a[212:215], v[220:223]\n"
      "ds_read_b128 a[208:211], v250 offset:4096\n"
      "v_exp_f32 v130, v130\n"
      "v_xor_b32 v129, v129, v128\n"
      "v_mfma_f32_16x16x32_bf16 v[216:219], v[48:51], a[216:219], v[216:219]\n"
      "v_sub_u32 v132, 0xfff, v133\n"
      "v_add_f32 v130, 1.0, v130\n"
      "s_mov_b32 s49, 0xffff0000\n"
      "v_mfma_f32_16x16x32_bf16 v[220:223], v[52:55], a[216:219], v[220:223]\n"
      "global_load_lds_dwordx4 v241, s[70:71] offset:1024\n"
      "ds_read_b128 a[212:215], v250 offset:6144\n"
      "v_and_or_b32 v129, v129, s49, v132\n"
      "s_waitcnt lgkmcnt(14)\n"
      "v_mfma_f32_16x16x32_bf16 v[216:219], v[56:59], a[220:223], v[216:219]\n"
      "v_rcp_f32 v130, v130\n"
      "s_mov_b32 s64, 0x00ff00ff\n"
      "s_mov_b32 s65, 0x00ff00ff\n"
      "v_mfma_f32_16x16x32_bf16 v[220:223], v[60:63], a[220:223], v[220:223]\n"
      "ds_read_b128 a[216:219], v251 offset:4096\n"
      "v_cndmask_b32 v129, v129, 0, s[64:65]\n"
      "v_cvt_pk_bf16_f32 v130, v130, v130\n"
      "s_waitcnt lgkmcnt(14)\n"
      "v_mfma_f32_16x16x32_bf16 v[216:219], v[64:67], a[224:227], v[216:219]\n"
      "s_lshr_b32 s48, s34, 1\n"
      "v_add_u32 v238, s42, v242\n"
      "v_mov_b32 v224, s37\n"
      "v_mov_b32 v225, s37\n"
      "v_mov_b32 v226, s37\n"
      "v_mov_b32 v227, s37\n"
      "s_waitcnt vmcnt(10)\n"
      "s_mov_b64 exec, 0xff\n"
      "global_store_dwordx4 v238, v[224:227], s[58:59] offset:0 sc1\n"
      "s_mov_b64 exec, -1\n"
      "v_mfma_f32_16x16x32_bf16 v[220:223], v[68:71], a[224:227], v[220:223]\n"
      "s_lshl2_add_u32 s48, s48, s35\n"
      "ds_read_b128 a[220:223], v251 offset:6144\n"
      "v_max_u32_dpp v129, v129, v129 quad_perm:[1,0,3,2] row_mask:0xf bank_mask:0xf\n"
      "v_and_b32 v130, 0xffff0000, v130\n"
      "s_waitcnt lgkmcnt(14)\n"
      "v_mfma_f32_16x16x32_bf16 v[216:219], v[72:75], a[228:231], v[216:219]\n"
      "s_lshl_b32 s49, s48, 15\n"
      "global_load_lds_dwordx4 v254, s[70:71] offset:2048\n"
      "v_max_u32_dpp v129, v129, v129 quad_perm:[2,3,0,1] row_mask:0xf bank_mask:0xf\n"
      "s_add_u32 s49, s49, 786432\n"
      "v_mfma_f32_16x16x32_bf16 v[220:223], v[76:79], a[228:231], v[220:223]\n"
      "ds_read_b128 a[224:227], v250 offset:8192\n"
      "v_lshl_add_u32 v133, v133, 4, s49\n"
      "v_max_u32_dpp v129, v129, v129 row_half_mirror row_mask:0xf bank_mask:0xf\n"
      "s_waitcnt lgkmcnt(14)\n"
      "v_mfma_f32_16x16x32_bf16 v[216:219], v[80:83], a[232:235], v[216:219]\n"
      "s_lshl_b32 s48, s48, 6\n"
      "s_add_u32 s48, s48, s40\n"
      "v_max_u32_dpp v129, v129, v129 row_mirror row_mask:0xf bank_mask:0xf\n"
      "s_lshl_b32 s48, s48, 6\n"
      "v_mfma_f32_16x16x32_bf16 v[220:223], v[84:87], a[232:235], v[220:223]\n"
      "s_add_u32 s48, s48, 4096\n"
      "ds_read_b128 a[228:231], v250 offset:10240\n"
      "v_mov_b32 v131, v129\n"
      "v_lshl_add_u32 v132, v134, 4, s48\n"
      "s_waitcnt lgkmcnt(14)\n"
      "v_mfma_f32_16x16x32_bf16 v[216:219], v[88:91], a[236:239], v[216:219]\n"
      "global_load_lds_dwordx4 v255, s[70:71] offset:3072\n"
      "v_mov_b32 v137, s37\n"
      "v_permlane16_swap_b32 v129, v131\n"
      "v_mfma_f32_16x16x32_bf16 v[220:223], v[92:95], a[236:239], v[220:223]\n"
      "ds_read_b128 a[232:235], v251 offset:8192\n"
      "v_mov_b32 v138, s37\n"
      "v_mov_b32 v139, s37\n"
      "s_waitcnt lgkmcnt(14)\n"
      "v_mfma_f32_16x16x32_bf16 v[216:219], v[96:99], a[240:243], v[216:219]\n"
      "v_max_u32 v129, v129, v131\n"
      "v_cndmask_b32 v133, v132, v133, s[64:65]\n"
      "v_mov_b32 v131, v129\n"
      "v_mfma_f32_16x16x32_bf16 v[220:223], v[100:103], a[240:243], v[220:223]\n"
      "ds_read_b128 a[236:239], v251 offset:10240\n"
      "v_permlane32_swap_b32 v129, v131\n"
      "v_max_u32 v129, v129, v131\n"
      "s_waitcnt lgkmcnt(14)\n"
      "v_mfma_f32_16x16x32_bf16 v[216:219], v[104:107], a[244:247], v[216:219]\n"
      "s_xor_b32 s70, s77, 0\n"
      "s_add_u32 s70, s8, s70\n"
      "s_addc_u32 s71, s9, 0\n"
      "s_add_u32 m0, s41, 8192\n"
      "v_mfma_f32_16x16x32_bf16 v[220:223], v[108:111], a[244:247], v[220:223]\n"
      "global_load_lds_dwordx4 v244, s[70:71] offset:0\n"
      "ds_read_b128 a[240:243], v250 offset:12288\n"
      "v_cndmask_b32 v136, v129, v130, s[64:65]\n"
      "s_waitcnt lgkmcnt(14)\n"
      "v_mfma_f32_16x16x32_bf16 v[216:219], v[112:115], a[248:251], v[216:219]\n"
      "s_mov_b32 s64, 0x00ff0fff\n"
      "s_mov_b64 exec, s[64:65]\n"
      "global_store_dwordx4 v133, v[136:139], s[58:59] sc1\n"
      "s_mov_b64 exec, -1\n"
      "v_mfma_f32_16x16x32_bf16 v[220:223], v[116:119], a[248:251], v[220:223]\n"
      "ds_read_b128 a[244:247], v250 offset:14336\n"
      "s_waitcnt vmcnt(11)\n"
      "ds_read_b128 a[0:3], v248 offset:4096\n"
      "s_waitcnt lgkmcnt(15)\n"
      "v_mfma_f32_16x16x32_bf16 v[216:219], v[120:123], a[252:255], v[216:219]\n"
      "ds_read_b128 a[4:7], v248 offset:6144\n"
      "ds_read_b128 a[32:35], v249 offset:4096\n"
      "v_mfma_f32_16x16x32_bf16 v[220:223], v[124:127], a[252:255], v[220:223]\n"
      "ds_read_b128 a[248:251], v251 offset:12288\n"
      "ds_read_b128 a[36:39], v249 offset:6144\n"
      "s_waitcnt lgkmcnt(15)\n"
      "v_mfma_f32_16x16x32_bf16 v[192:195], v[0:3], a[192:195], 0\n"
      "global_load_lds_dwordx4 v245, s[70:71] offset:1024\n"
      "v_mfma_f32_16x16x32_bf16 v[196:199], v[4:7], a[192:195], 0\n"
      "ds_read_b128 a[252:255], v251 offset:14336\n"
      "v_mfma_f32_16x16x32_bf16 v[200:203], v[0:3], a[196:199], 0\n"
      "v_cvt_pk_bf16_f32 v224, v216, v217\n"
      "v_cvt_pk_bf16_f32 v225, v218, v219\n"
      "v_cvt_pk_bf16_f32 v226, v220, v221\n"
      "v_mfma_f32_16x16x32_bf16 v[204:207], v[4:7], a[196:199], 0\n"
      "ds_read_b128 a[192:195], v250 offset:16384\n"
      "v_cvt_pk_bf16_f32 v227, v222, v223\n"
      "v_xor_b32 v224, s85, v224\n"
      "v_mfma_f32_16x16x32_bf16 v[192:195], v[8:11], a[200:203], v[192:195]\n"
      "global_load_lds_dwordx4 v246, s[70:71] offset:2048\n"
      "v_xor_b32 v225, s85, v225\n"
      "v_xor_b32 v226, s85, v226\n"
      "v_mfma_f32_16x16x32_bf16 v[196:199], v[12:15], a[200:203], v[196:199]\n"
      "ds_read_b128 a[196:199], v250 offset:18432\n"
      "v_xor_b32 v227, s85, v227\n"
      "s_lshl2_add_u32 s54, s34, s35\n"
      "v_mfma_f32_16x16x32_bf16 v[200:203], v[8:11], a[204:207], v[200:203]\n"
      "s_lshr_b32 s56, s39, 1\n"
      "s_lshl1_add_u32 s56, s33, s56\n"
      "s_lshl_b32 s56, s56, 15\n"
      "s_mul_i32 s55, s54, 266240\n"
      "v_mfma_f32_16x16x32_bf16 v[204:207], v[12:15], a[204:207], v[204:207]\n"
      "s_add_u32 s55, s55, 4096\n"
      "ds_read_b128 a[200:203], v251 offset:16384\n"
      "s_add_u32 s14, s18, s55\n"
      "s_addc_u32 s15, s19, 0\n"
      "s_waitcnt lgkmcnt(15)\n"
      "v_mfma_f32_16x16x32_bf16 v[192:195], v[16:19], a[208:211], v[192:195]\n"
      "s_add_u32 s10, s14, s56\n"
      "s_addc_u32 s11, s15, 0\n"
      "global_load_lds_dwordx4 v247, s[70:71] offset:3072\n"
      "v_mfma_f32_16x16x32_bf16 v[196:199], v[20:23], a[208:211], v[196:199]\n"
      "s_xor_b32 s56, s56, 0x8000\n"
      "ds_read_b128 a[204:207], v251 offset:18432\n"
      "s_add_u32 s14, s14, s56\n"
      "s_addc_u32 s15, s15, 0\n"
      "v_mfma_f32_16x16x32_bf16 v[200:203], v[16:19], a[212:215], v[200:203]\n"
      "s_and_b32 s55, s39, 1\n"
      "s_lshl_b32 s56, s55, 14\n"
      "v_add_u32 v240, s56, v242\n"
      "v_xor_b32 v241, 0x4000, v240\n"
      "v_mfma_f32_16x16x32_bf16 v[204:207], v[20:23], a[212:215], v[204:207]\n"
      "s_add_u32 s72, s10, 0x2000\n"
      "s_addc_u32 s73, s11, 0\n"
      "s_xor_b32 s70, s78, 0\n"
      "s_add_u32 s70, s8, s70\n"
      "s_addc_u32 s71, s9, 0\n"
      "s_add_u32 m0, s41, 12288\n"
      "v_mfma_f32_16x16x32_bf16 v[192:195], v[24:27], a[216:219], v[192:195]\n"
      "s_add_u32 s74, s14, 0x2000\n"
      "s_addc_u32 s75, s15, 0\n"
      "global_load_lds_dwordx4 v244, s[70:71] offset:0\n"
      "v_mfma_f32_16x16x32_bf16 v[196:199], v[28:31], a[216:219], v[196:199]\n"
      "s_lshl2_add_u32 s55, s54, s33\n"
      "ds_read_b128 a[208:211], v250 offset:20480\n"
      "ds_read_b128 a[212:215], v250 offset:22528\n"
      "s_lshl_b32 s55, s55, 12\n"
      "v_mfma_f32_16x16x32_bf16 v[200:203], v[24:27], a[220:223], v[200:203]\n"
      "s_add_u32 s55, s55, 1048576\n"
      "v_and_b32 v253, 7, v243\n"
      "v_lshl_add_u32 v253, v253, 9, s55\n"
      "v_mfma_f32_16x16x32_bf16 v[204:207], v[28:31], a[220:223], v[204:207]\n"
      "ds_read_b128 a[216:219], v251 offset:20480\n"
      "s_waitcnt lgkmcnt(15)\n"
      "v_mfma_f32_16x16x32_bf16 v[192:195], v[32:35], a[224:227], v[192:195]\n"
      "global_load_lds_dwordx4 v245, s[70:71] offset:1024\n"
      "v_mfma_f32_16x16x32_bf16 v[196:199], v[36:39], a[224:227], v[196:199]\n"
      "ds_read_b128 a[220:223], v251 offset:22528\n"
      "s_waitcnt vmcnt(7)\n"
      "s_barrier\n"
      "v_mfma_f32_16x16x32_bf16 v[200:203], v[32:35], a[228:231], v[200:203]\n"
      "v_mfma_f32_16x16x32_bf16 v[204:207], v[36:39], a[228:231], v[204:207]\n"
      "ds_read_b128 a[224:227], v250 offset:24576\n"
      "v_mfma_f32_16x16x32_bf16 v[192:195], v[40:43], a[232:235], v[192:195]\n"
      "global_load_lds_dwordx4 v246, s[70:71] offset:2048\n"
      "v_mfma_f32_16x16x32_bf16 v[196:199], v[44:47], a[232:235], v[196:199]\n"
      "ds_read_b128 a[228:231], v250 offset:26624\n"
      "v_mfma_f32_16x16x32_bf16 v[200:203], v[40:43], a[236:239], v[200:203]\n"
      "v_mfma_f32_16x16x32_bf16 v[204:207], v[44:47], a[236:239], v[204:207]\n"
      "ds_read_b128 a[232:235], v251 offset:24576\n"
      "s_waitcnt lgkmcnt(15)\n"
      "v_mfma_f32_16x16x32_bf16 v[192:195], v[48:51], a[240:243], v[192:195]\n"
      "global_load_lds_dwordx4 v247, s[70:71] offset:3072\n"
      "v_mfma_f32_16x16x32_bf16 v[196:199], v[52:55], a[240:243], v[196:199]\n"
      "ds_read_b128 a[236:239], v251 offset:26624\n"
      "v_mfma_f32_16x16x32_bf16 v[200:203], v[48:51], a[244:247], v[200:203]\n"
      "v_mfma_f32_16x16x32_bf16 v[204:207], v[52:55], a[244:247], v[204:207]\n"
      "s_xor_b32 s70, s79, 0\n"
      "s_add_u32 s70, s8, s70\n"
      "s_addc_u32 s71, s9, 0\n"
      "s_add_u32 m0, s82, 98304\n"
      "s_waitcnt lgkmcnt(14)\n"
      "v_mfma_f32_16x16x32_bf16 v[192:195], v[56:59], a[248:251], v[192:195]\n"
      "global_load_lds_dwordx4 v244, s[70:71] offset:0\n"
      "ds_read_b128 a[240:243], v250 offset:28672\n"
      "v_mfma_f32_16x16x32_bf16 v[196:199], v[60:63], a[248:251], v[196:199]\n"
      "ds_read_b128 a[244:247], v250 offset:30720\n"
      "s_waitcnt lgkmcnt(14)\n"
      "v_mfma_f32_16x16x32_bf16 v[200:203], v[56:59], a[252:255], v[200:203]\n"
      "v_mfma_f32_16x16x32_bf16 v[204:207], v[60:63], a[252:255], v[204:207]\n"
      "ds_read_b128 a[248:251], v251 offset:28672\n"
      "s_waitcnt lgkmcnt(14)\n"
      "v_mfma_f32_16x16x32_bf16 v[192:195], v[64:67], a[192:195], v[192:195]\n"
      "global_load_lds_dwordx4 v245, s[70:71] offset:1024\n"
      "v_mfma_f32_16x16x32_bf16 v[196:199], v[68:71], a[192:195], v[196:199]\n"
      "ds_read_b128 a[252:255], v251 offset:30720\n"
      "v_xor_b32 v250, 196608, v250\n"
      "v_xor_b32 v251, 196608, v251\n"
      "s_waitcnt lgkmcnt(14)\n"
      "v_mfma_f32_16x16x32_bf16 v[200:203], v[64:67], a[196:199], v[200:203]\n"
      "v_mfma_f32_16x16x32_bf16 v[204:207], v[68:71], a[196:199], v[204:207]\n"
      "ds_read_b128 a[192:195], v250 offset:0\n"
      "s_waitcnt lgkmcnt(14)\n"
      "v_mfma_f32_16x16x32_bf16 v[192:195], v[72:75], a[200:203], v[192:195]\n"
      "global_load_lds_dwordx4 v246, s[70:71] offset:2048\n"
      "v_mfma_f32_16x16x32_bf16 v[196:199], v[76:79], a[200:203], v[196:199]\n"
      "ds_read_b128 a[196:199], v250 offset:2048\n"
      "s_waitcnt lgkmcnt(14)\n"
      "v_mfma_f32_16x16x32_bf16 v[200:203], v[72:75], a[204:207], v[200:203]\n"
      "s_waitcnt vmcnt(7)\n"
      "ds_read_b128 a[8:11], v248 offset:8192\n"
      "v_mfma_f32_16x16x32_bf16 v[204:207], v[76:79], a[204:207], v[204:207]\n"
      "ds_read_b128 a[200:203], v251 offset:0\n"
      "ds_read_b128 a[12:15], v248 offset:10240\n"
      "s_waitcnt lgkmcnt(15)\n"
      "v_mfma_f32_16x16x32_bf16 v[192:195], v[80:83], a[208:211], v[192:195]\n"
      "global_load_lds_dwordx4 v247, s[70:71] offset:3072\n"
      "ds_read_b128 a[40:43], v249 offset:8192\n"
      "v_mfma_f32_16x16x32_bf16 v[196:199], v[84:87], a[208:211], v[196:199]\n"
      "ds_read_b128 a[204:207], v251 offset:2048\n"
      "ds_read_b128 a[44:47], v249 offset:10240\n"
      "v_mfma_f32_16x16x32_bf16 v[200:203], v[80:83], a[212:215], v[200:203]\n"
      "v_mfma_f32_16x16x32_bf16 v[204:207], v[84:87], a[212:215], v[204:207]\n"
      "s_xor_b32 s70, s76, 256\n"
      "s_add_u32 s70, s8, s70\n"
      "s_addc_u32 s71, s9, 0\n"
      "s_add_u32 m0, s82, 114688\n"
      "s_waitcnt lgkmcnt(15)\n"
      "v_mfma_f32_16x16x32_bf16 v[192:195], v[88:91], a[216:219], v[192:195]\n"
      "global_load_lds_dwordx4 v244, s[70:71] offset:0\n"
      "ds_read_b128 a[208:211], v250 offset:4096\n"
      "v_mfma_f32_16x16x32_bf16 v[196:199], v[92:95], a[216:219], v[196:199]\n"
      "ds_read_b128 a[212:215], v250 offset:6144\n"
      "v_mfma_f32_16x16x32_bf16 v[200:203], v[88:91], a[220:223], v[200:203]\n"
      "v_mfma_f32_16x16x32_bf16 v[204:207], v[92:95], a[220:223], v[204:207]\n"
      "ds_read_b128 a[216:219], v251 offset:4096\n"
      "v_mfma_f32_16x16x32_bf16 v[192:195], v[96:99], a[224:227], v[192:195]\n"
      "global_load_lds_dwordx4 v245, s[70:71] offset:1024\n"
      "v_mfma_f32_16x16x32_bf16 v[196:199], v[100:103], a[224:227], v[196:199]\n"
      "ds_read_b128 a[220:223], v251 offset:6144\n"
      "s_waitcnt lgkmcnt(15)\n"
      "v_mfma_f32_16x16x32_bf16 v[200:203], v[96:99], a[228:231], v[200:203]\n"
      "v_mfma_f32_16x16x32_bf16 v[204:207], v[100:103], a[228:231], v[204:207]\n"
      "ds_read_b128 a[224:227], v250 offset:8192\n"
      "v_mfma_f32_16x16x32_bf16 v[192:195], v[104:107], a[232:235], v[192:195]\n"
      "global_load_lds_dwordx4 v246, s[70:71] offset:2048\n"
      "v_mfma_f32_16x16x32_bf16 v[196:199], v[108:111], a[232:235], v[196:199]\n"
      "ds_read_b128 a[228:231], v250 offset:10240\n"
      "v_mfma_f32_16x16x32_bf16 v[200:203], v[104:107], a[236:239], v[200:203]\n"
      "s_waitcnt vmcnt(7)\n"
      "ds_read_b128 a[16:19], v248 offset:12288\n"
      "v_mfma_f32_16x16x32_bf16 v[204:207], v[108:111], a[236:239], v[204:207]\n"
      "ds_read_b128 a[232:235], v251 offset:8192\n"
      "ds_read_b128 a[20:23], v248 offset:14336\n"
      "v_mfma_f32_16x16x32_bf16 v[192:195], v[112:115], a[240:243], v[192:195]\n"
      "global_load_lds_dwordx4 v247, s[70:71] offset:3072\n"
      "ds_read_b128 a[48:51], v249 offset:12288\n"
      "v_mfma_f32_16x16x32_bf16 v[196:199], v[116:119], a[240:243], v[196:199]\n"
      "ds_read_b128 a[236:239], v251 offset:10240\n"
      "s_waitcnt lgkmcnt(15)\n"
      "s_barrier\n"
      "v_mfma_f32_16x16x32_bf16 v[200:203], v[112:115], a[244:247], v[200:203]\n"
      "ds_read_b128 a[52:55], v249 offset:14336\n"
      "v_mfma_f32_16x16x32_bf16 v[204:207], v[116:119], a[244:247], v[204:207]\n"
      "s_xor_b32 s70, s77, 256\n"
      "s_add_u32 s70, s8, s70\n"
      "s_addc_u32 s71, s9, 0\n"
      "s_add_u32 m0, s82, 131072\n"
      "v_mfma_f32_16x16x32_bf16 v[192:195], v[120:123], a[248:251], v[192:195]\n"
      "global_load_lds_dwordx4 v244, s[70:71] offset:0\n"
      "ds_read_b128 a[240:243], v250 offset:12288\n"
      "v_mfma_f32_16x16x32_bf16 v[196:199], v[124:127], a[248:251], v[196:199]\n"
      "ds_read_b128 a[244:247], v250 offset:14336\n"
      "v_mfma_f32_16x16x32_bf16 v[200:203], v[120:123], a[252:255], v[200:203]\n"
      "v_mfma_f32_16x16x32_bf16 v[204:207], v[124:127], a[252:255], v[204:207]\n"
      "ds_read_b128 a[248:251], v251 offset:12288\n"
      "v_mfma_f32_16x16x32_bf16 v[208:211], v[0:3], a[192:195], 0\n"
      "global_load_lds_dwordx4 v245, s[70:71] offset:1024\n"
      "v_mfma_f32_16x16x32_bf16 v[212:215], v[4:7], a[192:195], 0\n"
      "ds_read_b128 a[252:255], v251 offset:14336\n"
      "v_mfma_f32_16x16x32_bf16 v[216:219], v[0:3], a[196:199], 0\n"
      "v_cvt_pk_bf16_f32 v228, v192, v193\n"
      "v_cvt_pk_bf16_f32 v229, v194, v195\n"
      "v_cvt_pk_bf16_f32 v230, v196, v197\n"
      "v_mfma_f32_16x16x32_bf16 v[220:223], v[4:7], a[196:199], 0\n"
      "ds_read_b128 a[192:195], v250 offset:16384\n"
      "v_cvt_pk_bf16_f32 v231, v198, v199\n"
      "v_xor_b32 v228, s85, v228\n"
      "v_mfma_f32_16x16x32_bf16 v[208:211], v[8:11], a[200:203], v[208:211]\n"
      "global_load_lds_dwordx4 v246, s[70:71] offset:2048\n"
      "v_xor_b32 v229, s85, v229\n"
      "v_xor_b32 v230, s85, v230\n"
      "v_mfma_f32_16x16x32_bf16 v[212:215], v[12:15], a[200:203], v[212:215]\n"
      "ds_read_b128 a[196:199], v250 offset:18432\n"
      "v_xor_b32 v231, s85, v231\n"
      "v_cvt_pk_bf16_f32 v232, v200, v201\n"
      "s_waitcnt lgkmcnt(15)\n"
      "v_mfma_f32_16x16x32_bf16 v[216:219], v[8:11], a[204:207], v[216:219]\n"
      "s_waitcnt vmcnt(7)\n"
      "ds_read_b128 a[24:27], v236 offset:0\n"
      "v_mfma_f32_16x16x32_bf16 v[220:223], v[12:15], a[204:207], v[220:223]\n"
      "ds_read_b128 a[200:203], v251 offset:16384\n"
      "ds_read_b128 a[28:31], v236 offset:2048\n"
      "v_cvt_pk_bf16_f32 v233, v202, v203\n"
      "v_mfma_f32_16x16x32_bf16 v[208:211], v[16:19], a[208:211], v[208:211]\n"
      "global_load_lds_dwordx4 v247, s[70:71] offset:3072\n"
      "ds_read_b128 a[56:59], v237 offset:0\n"
      "v_cvt_pk_bf16_f32 v234, v204, v205\n"
      "v_mfma_f32_16x16x32_bf16 v[212:215], v[20:23], a[208:211], v[212:215]\n"
      "ds_read_b128 a[204:207], v251 offset:18432\n"
      "ds_read_b128 a[60:63], v237 offset:2048\n"
      "v_cvt_pk_bf16_f32 v235, v206, v207\n"
      "v_mfma_f32_16x16x32_bf16 v[216:219], v[16:19], a[212:215], v[216:219]\n"
      "v_xor_b32 v232, s85, v232\n"
      "v_xor_b32 v233, s85, v233\n"
      "v_xor_b32 v234, s85, v234\n"
      "v_mfma_f32_16x16x32_bf16 v[220:223], v[20:23], a[212:215], v[220:223]\n"
      "s_xor_b32 s70, s78, 256\n"
      "s_add_u32 s70, s8, s70\n"
      "s_addc_u32 s71, s9, 0\n"
      "s_add_u32 m0, s82, 147456\n"
      "v_mfma_f32_16x16x32_bf16 v[208:211], v[24:27], a[216:219], v[208:211]\n"
      "global_load_lds_dwordx4 v244, s[70:71] offset:0\n"
      "ds_read_b128 a[208:211], v250 offset:20480\n"
      "v_xor_b32 v235, s85, v235\n"
      "v_mfma_f32_16x16x32_bf16 v[212:215], v[28:31], a[216:219], v[212:215]\n"
      "ds_read_b128 a[212:215], v250 offset:22528\n"
      "s_waitcnt lgkmcnt(15)\n"
      "v_mfma_f32_16x16x32_bf16 v[216:219], v[24:27], a[220:223], v[216:219]\n"
      "v_mfma_f32_16x16x32_bf16 v[220:223], v[28:31], a[220:223], v[220:223]\n"
      "ds_read_b128 a[216:219], v251 offset:20480\n"
      "v_mfma_f32_16x16x32_bf16 v[208:211], v[32:35], a[224:227], v[208:211]\n"
      "global_load_lds_dwordx4 v245, s[70:71] offset:1024\n"
      "v_mfma_f32_16x16x32_bf16 v[212:215], v[36:39], a[224:227], v[212:215]\n"
      "ds_read_b128 a[220:223], v251 offset:22528\n"
      "v_mfma_f32_16x16x32_bf16 v[216:219], v[32:35], a[228:231], v[216:219]\n"
      "v_mfma_f32_16x16x32_bf16 v[220:223], v[36:39], a[228:231], v[220:223]\n"
      "ds_read_b128 a[224:227], v250 offset:24576\n"
      "v_mfma_f32_16x16x32_bf16 v[208:211], v[40:43], a[232:235], v[208:211]\n"
      "global_load_lds_dwordx4 v246, s[70:71] offset:2048\n"
      "v_mfma_f32_16x16x32_bf16 v[212:215], v[44:47], a[232:235], v[212:215]\n"
      "ds_read_b128 a[228:231], v250 offset:26624\n"
      "v_mfma_f32_16x16x32_bf16 v[216:219], v[40:43], a[236:239], v[216:219]\n"
      "s_waitcnt vmcnt(7)\n"
      "ds_read_b128 a[64:67], v236 offset:16384\n"
      "v_mfma_f32_16x16x32_bf16 v[220:223], v[44:47], a[236:239], v[220:223]\n"
      "ds_read_b128 a[232:235], v251 offset:24576\n"
      "ds_read_b128 a[68:71], v236 offset:18432\n"
      "s_waitcnt lgkmcnt(15)\n"
      "v_mfma_f32_16x16x32_bf16 v[208:211], v[48:51], a[240:243], v[208:211]\n"
      "global_load_lds_dwordx4 v247, s[70:71] offset:3072\n"
      "ds_read_b128 a[96:99], v237 offset:16384\n"
      "v_mfma_f32_16x16x32_bf16 v[212:215], v[52:55], a[240:243], v[212:215]\n"
      "ds_read_b128 a[236:239], v251 offset:26624\n"
      "ds_read_b128 a[100:103], v237 offset:18432\n"
      "v_mfma_f32_16x16x32_bf16 v[216:219], v[48:51], a[244:247], v[216:219]\n"
      "v_mfma_f32_16x16x32_bf16 v[220:223], v[52:55], a[244:247], v[220:223]\n"
      "global_load_dword v252, v253, s[58:59] offset:0 sc1\n"
      "s_xor_b32 s70, s79, 256\n"
      "s_add_u32 s70, s8, s70\n"
      "s_addc_u32 s71, s9, 0\n"
      "s_add_u32 m0, s41, 0\n"
      "v_mfma_f32_16x16x32_bf16 v[208:211], v[56:59], a[248:251], v[208:211]\n"
      "global_load_lds_dwordx4 v244, s[70:71] offset:0\n"
      "ds_read_b128 a[240:243], v250 offset:28672\n"
      "v_mfma_f32_16x16x32_bf16 v[212:215], v[60:63], a[248:251], v[212:215]\n"
      "ds_read_b128 a[244:247], v250 offset:30720\n"
      "v_mfma_f32_16x16x32_bf16 v[216:219], v[56:59], a[252:255], v[216:219]\n"
      "v_mfma_f32_16x16x32_bf16 v[220:223], v[60:63], a[252:255], v[220:223]\n"
      "ds_read_b128 a[248:251], v251 offset:28672\n"
      "v_mfma_f32_16x16x32_bf16 v[208:211], v[64:67], a[192:195], v[208:211]\n"
      "global_load_lds_dwordx4 v245, s[70:71] offset:1024\n"
      "v_mfma_f32_16x16x32_bf16 v[212:215], v[68:71], a[192:195], v[212:215]\n"
      "ds_read_b128 a[252:255], v251 offset:30720\n"
      "v_mfma_f32_16x16x32_bf16 v[216:219], v[64:67], a[196:199], v[216:219]\n"
      "v_mfma_f32_16x16x32_bf16 v[220:223], v[68:71], a[196:199], v[220:223]\n"
      "s_waitcnt lgkmcnt(15)\n"
      "v_mfma_f32_16x16x32_bf16 v[208:211], v[72:75], a[200:203], v[208:211]\n"
      "global_load_lds_dwordx4 v246, s[70:71] offset:2048\n"
      "v_mfma_f32_16x16x32_bf16 v[212:215], v[76:79], a[200:203], v[212:215]\n"
      "v_mfma_f32_16x16x32_bf16 v[216:219], v[72:75], a[204:207], v[216:219]\n"
      "s_waitcnt vmcnt(8)\n"
      "ds_read_b128 a[72:75], v236 offset:32768\n"
      "v_mfma_f32_16x16x32_bf16 v[220:223], v[76:79], a[204:207], v[220:223]\n"
      "ds_read_b128 a[76:79], v236 offset:34816\n"
      "ds_read_b128 a[104:107], v237 offset:32768\n"
      "v_mfma_f32_16x16x32_bf16 v[208:211], v[80:83], a[208:211], v[208:211]\n"
      "global_load_lds_dwordx4 v247, s[70:71] offset:3072\n"
      "ds_read_b128 a[108:111], v237 offset:34816\n"
      "v_mfma_f32_16x16x32_bf16 v[212:215], v[84:87], a[208:211], v[212:215]\n"
      "s_waitcnt lgkmcnt(15)\n"
      "v_mfma_f32_16x16x32_bf16 v[216:219], v[80:83], a[212:215], v[216:219]\n"
      "v_mfma_f32_16x16x32_bf16 v[220:223], v[84:87], a[212:215], v[220:223]\n"
      "s_waitcnt vmcnt(4)\n"
      "v_cmp_ne_u32 vcc, s37, v252\n"
      "s_nop 1\n"
      "s_cbranch_vccnz .Lp_s2_%=\n"
      ".Lp_o3_%=:\n"
      "v_mfma_f32_16x16x32_bf16 v[208:211], v[88:91], a[216:219], v[208:211]\n"
      "global_load_dwordx4 v[128:131], v240, s[10:11] offset:-4096 sc1\n"
      "v_mfma_f32_16x16x32_bf16 v[212:215], v[92:95], a[216:219], v[212:215]\n"
      "v_mfma_f32_16x16x32_bf16 v[216:219], v[88:91], a[220:223], v[216:219]\n"
      "v_mfma_f32_16x16x32_bf16 v[220:223], v[92:95], a[220:223], v[220:223]\n"
      "v_mfma_f32_16x16x32_bf16 v[208:211], v[96:99], a[224:227], v[208:211]\n"
      "global_load_dwordx4 v[132:135], v240, s[10:11] offset:-3072 sc1\n"
      "v_mfma_f32_16x16x32_bf16 v[212:215], v[100:103], a[224:227], v[212:215]\n"
      "s_waitcnt lgkmcnt(14)\n"
      "v_mfma_f32_16x16x32_bf16 v[216:219], v[96:99], a[228:231], v[216:219]\n"
      "v_mfma_f32_16x16x32_bf16 v[220:223], v[100:103], a[228:231], v[220:223]\n"
      "s_waitcnt lgkmcnt(12)\n"
      "v_mfma_f32_16x16x32_bf16 v[208:211], v[104:107], a[232:235], v[208:211]\n"
      "global_load_dwordx4 v[136:139], v240, s[10:11] offset:-2048 sc1\n"
      "v_mfma_f32_16x16x32_bf16 v[212:215], v[108:111], a[232:235], v[212:215]\n"
      "s_waitcnt lgkmcnt(9)\n"
      "v_mfma_f32_16x16x32_bf16 v[216:219], v[104:107], a[236:239], v[216:219]\n"
      "ds_read_b128 a[80:83], v236 offset:49152\n"
      "ds_read_b128 a[84:87], v236 offset:51200\n"
      "v_mfma_f32_16x16x32_bf16 v[220:223], v[108:111], a[236:239], v[220:223]\n"
      "ds_read_b128 a[112:115], v237 offset:49152\n"
      "ds_read_b128 a[116:119], v237 offset:51200\n"
      "s_waitcnt lgkmcnt(11)\n"
      "v_mfma_f32_16x16x32_bf16 v[208:211], v[112:115], a[240:243], v[208:211]\n"
      "global_store_dwordx4 v242, v[224:227], s[62:63] offset:-1024 sc1\n"
      "global_store_dwordx4 v242, v[228:231], s[62:63] offset:0 sc1\n"
      "v_mfma_f32_16x16x32_bf16 v[212:215], v[116:119], a[240:243], v[212:215]\n"
      "global_store_dwordx4 v242, v[232:235], s[62:63] offset:1024 sc1\n"
      "global_load_dwordx4 v[140:143], v240, s[72:73] offset:-4096 sc1\n"
      "s_waitcnt lgkmcnt(10)\n"
      "v_mfma_f32_16x16x32_bf16 v[216:219], v[112:115], a[244:247], v[216:219]\n"
      "v_mfma_f32_16x16x32_bf16 v[220:223], v[116:119], a[244:247], v[220:223]\n"
      "s_waitcnt lgkmcnt(9)\n"
      "v_mfma_f32_16x16x32_bf16 v[208:211], v[120:123], a[248:251], v[208:211]\n"
      "v_mfma_f32_16x16x32_bf16 v[212:215], v[124:127], a[248:251], v[212:215]\n"
      "global_load_dwordx4 v[144:147], v240, s[72:73] offset:-3072 sc1\n"
      "s_waitcnt lgkmcnt(8)\n"
      "v_mfma_f32_16x16x32_bf16 v[216:219], v[120:123], a[252:255], v[216:219]\n"
      "v_mfma_f32_16x16x32_bf16 v[220:223], v[124:127], a[252:255], v[220:223]\n"
      "s_waitcnt vmcnt(7)\n"
      "v_mfma_f32_16x16x32_bf16 v[0:3], v[128:131], a[0:3], 0\n"
      "v_mfma_f32_16x16x32_bf16 v[4:7], v[128:131], a[4:7], 0\n"
      "global_load_dwordx4 v[148:151], v240, s[72:73] offset:-2048 sc1\n"
      "v_mfma_f32_16x16x32_bf16 v[8:11], v[128:131], a[8:11], 0\n"
      "v_cvt_pk_bf16_f32 v192, v208, v209\n"
      "v_cvt_pk_bf16_f32 v193, v210, v211\n"
      "v_mfma_f32_16x16x32_bf16 v[12:15], v[128:131], a[12:15], 0\n"
      "v_cvt_pk_bf16_f32 v194, v212, v213\n"
      "v_cvt_pk_bf16_f32 v195, v214, v215\n"
      "v_mfma_f32_16x16x32_bf16 v[16:19], v[128:131], a[16:19], 0\n"
      "v_xor_b32 v192, s85, v192\n"
      "v_xor_b32 v193, s85, v193\n"
      "v_mfma_f32_16x16x32_bf16 v[20:23], v[128:131], a[20:23], 0\n"
      "v_xor_b32 v194, s85, v194\n"
      "v_xor_b32 v195, s85, v195\n"
      "v_mfma_f32_16x16x32_bf16 v[24:27], v[128:131], a[24:27], 0\n"
      "ds_read_b128 a[88:91], v248 offset:0\n"
      "v_cvt_pk_bf16_f32 v196, v216, v217\n"
      "v_mfma_f32_16x16x32_bf16 v[28:31], v[128:131], a[28:31], 0\n"
      "ds_read_b128 a[92:95], v248 offset:2048\n"
      "v_cvt_pk_bf16_f32 v197, v218, v219\n"
      "s_waitcnt vmcnt(7)\n"
      "v_mfma_f32_16x16x32_bf16 v[32:35], v[132:135], a[0:3], 0\n"
      "s_xor_b32 s70, s76, 128\n"
      "s_add_u32 s70, s8, s70\n"
      "s_addc_u32 s71, s9, 0\n"
      "s_add_u32 m0, s41, 4096\n"
      "v_mfma_f32_16x16x32_bf16 v[36:39], v[132:135], a[4:7], 0\n"
      "global_load_lds_dwordx4 v244, s[70:71] offset:0\n"
      "v_cvt_pk_bf16_f32 v198, v220, v221\n"
      "v_mfma_f32_16x16x32_bf16 v[40:43], v[132:135], a[8:11], 0\n"
      "ds_read_b128 a[120:123], v249 offset:0\n"
      "v_cvt_pk_bf16_f32 v199, v222, v223\n"
      "v_mfma_f32_16x16x32_bf16 v[44:47], v[132:135], a[12:15], 0\n"
      "ds_read_b128 a[124:127], v249 offset:2048\n"
      "v_xor_b32 v196, s85, v196\n"
      "v_mfma_f32_16x16x32_bf16 v[48:51], v[132:135], a[16:19], 0\n"
      "global_load_lds_dwordx4 v245, s[70:71] offset:1024\n"
      "v_xor_b32 v197, s85, v197\n"
      "v_mfma_f32_16x16x32_bf16 v[52:55], v[132:135], a[20:23], 0\n"
      "v_xor_b32 v198, s85, v198\n"
      "v_xor_b32 v199, s85, v199\n"
      "v_mfma_f32_16x16x32_bf16 v[56:59], v[132:135], a[24:27], 0\n"
      "global_store_dwordx4 v242, v[192:195], s[62:63] offset:2048 sc1\n"
      "global_store_dwordx4 v242, v[196:199], s[62:63] offset:3072 sc1\n"
      "v_mfma_f32_16x16x32_bf16 v[60:63], v[132:135], a[28:31], 0\n"
      "s_waitcnt vmcnt(6)\n"
      "global_load_lds_dwordx4 v246, s[70:71] offset:2048\n"
      "v_mfma_f32_16x16x32_bf16 v[64:67], v[136:139], a[0:3], 0\n"
      "v_mfma_f32_16x16x32_bf16 v[68:71], v[136:139], a[4:7], 0\n"
      "v_mfma_f32_16x16x32_bf16 v[72:75], v[136:139], a[8:11], 0\n"
      "global_load_lds_dwordx4 v247, s[70:71] offset:3072\n"
      "v_mfma_f32_16x16x32_bf16 v[76:79], v[136:139], a[12:15], 0\n"
      "v_mfma_f32_16x16x32_bf16 v[80:83], v[136:139], a[16:19], 0\n"
      "global_load_dwordx4 v[152:155], v240, s[14:15] offset:-4096 sc1\n"
      "v_mfma_f32_16x16x32_bf16 v[84:87], v[136:139], a[20:23], 0\n"
      "v_mfma_f32_16x16x32_bf16 v[88:91], v[136:139], a[24:27], 0\n"
      "v_mfma_f32_16x16x32_bf16 v[92:95], v[136:139], a[28:31], 0\n"
      "global_load_dwordx4 v[156:159], v240, s[14:15] offset:-3072 sc1\n"
      "v_mfma_f32_16x16x32_bf16 v[0:3], v[140:143], a[32:35], v[0:3]\n"
      "v_mfma_f32_16x16x32_bf16 v[4:7], v[140:143], a[36:39], v[4:7]\n"
      "v_mfma_f32_16x16x32_bf16 v[8:11], v[140:143], a[40:43], v[8:11]\n"
      "global_load_dwordx4 v[160:163], v240, s[14:15] offset:-2048 sc1\n"
      "v_mfma_f32_16x16x32_bf16 v[12:15], v[140:143], a[44:47], v[12:15]\n"
      "v_mfma_f32_16x16x32_bf16 v[16:19], v[140:143], a[48:51], v[16:19]\n"
      "v_mfma_f32_16x16x32_bf16 v[20:23], v[140:143], a[52:55], v[20:23]\n"
      "s_xor_b32 s70, s77, 128\n"
      "s_add_u32 s70, s8, s70\n"
      "s_addc_u32 s71, s9, 0\n"
      "s_add_u32 m0, s41, 8192\n"
      "v_mfma_f32_16x16x32_bf16 v[24:27], v[140:143], a[56:59], v[24:27]\n"
      "global_load_lds_dwordx4 v244, s[70:71] offset:0\n"
      "v_mfma_f32_16x16x32_bf16 v[28:31], v[140:143], a[60:63], v[28:31]\n"
      "s_waitcnt vmcnt(10)\n"
      "v_mfma_f32_16x16x32_bf16 v[32:35], v[144:147], a[32:35], v[32:35]\n"
      "v_mfma_f32_16x16x32_bf16 v[36:39], v[144:147], a[36:39], v[36:39]\n"
      "global_load_lds_dwordx4 v245, s[70:71] offset:1024\n"
      "v_mfma_f32_16x16x32_bf16 v[40:43], v[144:147], a[40:43], v[40:43]\n"
      "v_mfma_f32_16x16x32_bf16 v[44:47], v[144:147], a[44:47], v[44:47]\n"
      "v_mfma_f32_16x16x32_bf16 v[48:51], v[144:147], a[48:51], v[48:51]\n"
      "global_load_lds_dwordx4 v246, s[70:71] offset:2048\n"
      "v_mfma_f32_16x16x32_bf16 v[52:55], v[144:147], a[52:55], v[52:55]\n"
      "v_mfma_f32_16x16x32_bf16 v[56:59], v[144:147], a[56:59], v[56:59]\n"
      "v_mfma_f32_16x16x32_bf16 v[60:63], v[144:147], a[60:63], v[60:63]\n"
      "global_load_lds_dwordx4 v247, s[70:71] offset:3072\n"
      "v_mfma_f32_16x16x32_bf16 v[64:67], v[148:151], a[32:35], v[64:67]\n"
      "v_mfma_f32_16x16x32_bf16 v[68:71], v[148:151], a[36:39], v[68:71]\n"
      "s_xor_b32 s70, s78, 128\n"
      "s_add_u32 s70, s8, s70\n"
      "s_addc_u32 s71, s9, 0\n"
      "s_add_u32 m0, s41, 12288\n"
      "v_mfma_f32_16x16x32_bf16 v[72:75], v[148:151], a[40:43], v[72:75]\n"
      "global_load_lds_dwordx4 v244, s[70:71] offset:0\n"
      "v_add_u32 v238, s42, v242\n"
      "v_mov_b32 v200, s37\n"
      "v_mov_b32 v201, s37\n"
      "v_mov_b32 v202, s37\n"
      "v_mov_b32 v203, s37\n"
      "s_waitcnt vmcnt(10)\n"
      "s_mov_b64 exec, 0xff\n"
      "global_store_dwordx4 v238, v[200:203], s[58:59] offset:128 sc1\n"
      "s_mov_b64 exec, -1\n"
      "v_mfma_f32_16x16x32_bf16 v[76:79], v[148:151], a[44:47], v[76:79]\n"
      "s_lshl2_add_u32 s48, s36, s35\n"
      "s_mul_i32 s48, s48, 147712\n"
      "s_mul_i32 s49, s33, 3072\n"
      "v_mfma_f32_16x16x32_bf16 v[80:83], v[148:151], a[48:51], v[80:83]\n"
      "s_add_u32 s48, s48, s49\n"
      "s_add_u32 s48, s48, 6291456\n"
      "s_add_u32 s60, s18, s48\n"
      "s_addc_u32 s61, s19, 0\n"
      "v_mfma_f32_16x16x32_bf16 v[84:87], v[148:151], a[52:55], v[84:87]\n"
      "s_mul_i32 s49, s34, 36864\n"
      "global_load_lds_dwordx4 v245, s[70:71] offset:1024\n"
      "s_add_u32 s62, s60, s49\n"
      "s_addc_u32 s63, s61, 0\n"
      "v_mfma_f32_16x16x32_bf16 v[88:91], v[148:151], a[56:59], v[88:91]\n"
      "v_mfma_f32_16x16x32_bf16 v[92:95], v[148:151], a[60:63], v[92:95]\n"
      "s_waitcnt vmcnt(8)\n"
      "v_mfma_f32_16x16x32_bf16 v[0:3], v[152:155], a[64:67], v[0:3]\n"
      "global_load_lds_dwordx4 v246, s[70:71] offset:2048\n"
      "ds_read_b128 a[128:131], v248 offset:4096\n"
      "v_mfma_f32_16x16x32_bf16 v[4:7], v[152:155], a[68:71], v[4:7]\n"
      "ds_read_b128 a[132:135], v248 offset:6144\n"
      "ds_read_b128 a[160:163], v249 offset:4096\n"
      "s_waitcnt lgkmcnt(14)\n"
      "v_mfma_f32_16x16x32_bf16 v[8:11], v[152:155], a[72:75], v[8:11]\n"
      "ds_read_b128 a[164:167], v249 offset:6144\n"
      "s_waitcnt lgkmcnt(14)\n"
      "v_mfma_f32_16x16x32_bf16 v[12:15], v[152:155], a[76:79], v[12:15]\n"
      "global_load_lds_dwordx4 v247, s[70:71] offset:3072\n"
      "s_waitcnt lgkmcnt(11)\n"
      "v_mfma_f32_16x16x32_bf16 v[16:19], v[152:155], a[80:83], v[16:19]\n"
      "s_waitcnt lgkmcnt(10)\n"
      "v_mfma_f32_16x16x32_bf16 v[20:23], v[152:155], a[84:87], v[20:23]\n"
      "global_load_dwordx4 v[164:167], v240, s[74:75] offset:-4096 sc1\n"
      "s_waitcnt lgkmcnt(7)\n"
      "v_mfma_f32_16x16x32_bf16 v[24:27], v[152:155], a[88:91], v[24:27]\n"
      "s_waitcnt lgkmcnt(6)\n"
      "v_mfma_f32_16x16x32_bf16 v[28:31], v[152:155], a[92:95], v[28:31]\n"
      "v_mfma_f32_16x16x32_bf16 v[32:35], v[156:159], a[64:67], v[32:35]\n"
      "global_load_dwordx4 v[168:171], v240, s[74:75] offset:-3072 sc1\n"
      "v_mfma_f32_16x16x32_bf16 v[36:39], v[156:159], a[68:71], v[36:39]\n"
      "v_mfma_f32_16x16x32_bf16 v[40:43], v[156:159], a[72:75], v[40:43]\n"
      "v_mfma_f32_16x16x32_bf16 v[44:47], v[156:159], a[76:79], v[44:47]\n"
      "global_load_dwordx4 v[172:175], v240, s[74:75] offset:-2048 sc1\n"
      "v_mfma_f32_16x16x32_bf16 v[48:51], v[156:159], a[80:83], v[48:51]\n"
      "v_mfma_f32_16x16x32_bf16 v[52:55], v[156:159], a[84:87], v[52:55]\n"
      "v_mfma_f32_16x16x32_bf16 v[56:59], v[156:159], a[88:91], v[56:59]\n"
      "s_xor_b32 s70, s79, 128\n"
      "s_add_u32 s70, s8, s70\n"
      "s_addc_u32 s71, s9, 0\n"
      "s_add_u32 m0, s82, 98304\n"
      "v_mfma_f32_16x16x32_bf16 v[60:63], v[156:159], a[92:95], v[60:63]\n"
      "s_waitcnt vmcnt(2)\n"
      "global_load_lds_dwordx4 v244, s[70:71] offset:0\n"
      "v_mfma_f32_16x16x32_bf16 v[64:67], v[160:163], a[64:67], v[64:67]\n"
      "v_mfma_f32_16x16x32_bf16 v[68:71], v[160:163], a[68:71], v[68:71]\n"
      "v_mfma_f32_16x16x32_bf16 v[72:75], v[160:163], a[72:75], v[72:75]\n"
      "global_load_lds_dwordx4 v245, s[70:71] offset:1024\n"
      "v_mfma_f32_16x16x32_bf16 v[76:79], v[160:163], a[76:79], v[76:79]\n"
      "v_mfma_f32_16x16x32_bf16 v[80:83], v[160:163], a[80:83], v[80:83]\n"
      "v_mfma_f32_16x16x32_bf16 v[84:87], v[160:163], a[84:87], v[84:87]\n"
      "global_load_lds_dwordx4 v246, s[70:71] offset:2048\n"
      "ds_read_b128 a[136:139], v248 offset:8192\n"
      "v_mfma_f32_16x16x32_bf16 v[88:91], v[160:163], a[88:91], v[88:91]\n"
      "ds_read_b128 a[140:143], v248 offset:10240\n"
      "ds_read_b128 a[168:171], v249 offset:8192\n"
      "v_mfma_f32_16x16x32_bf16 v[92:95], v[160:163], a[92:95], v[92:95]\n"
      "ds_read_b128 a[172:175], v249 offset:10240\n"
      "v_mfma_f32_16x16x32_bf16 v[0:3], v[164:167], a[96:99], v[0:3]\n"
      "global_load_lds_dwordx4 v247, s[70:71] offset:3072\n"
      "v_mfma_f32_16x16x32_bf16 v[4:7], v[164:167], a[100:103], v[4:7]\n"
      "v_mfma_f32_16x16x32_bf16 v[8:11], v[164:167], a[104:107], v[8:11]\n"
      "global_load_dwordx4 v[176:179], v241, s[10:11] offset:-4096 sc1\n"
      "v_mfma_f32_16x16x32_bf16 v[12:15], v[164:167], a[108:111], v[12:15]\n"
      "v_mfma_f32_16x16x32_bf16 v[16:19], v[164:167], a[112:115], v[16:19]\n"
      "v_mfma_f32_16x16x32_bf16 v[20:23], v[164:167], a[116:119], v[20:23]\n"
      "global_load_dwordx4 v[180:183], v241, s[10:11] offset:-3072 sc1\n"
      "s_waitcnt lgkmcnt(9)\n"
      "v_mfma_f32_16x16x32_bf16 v[24:27], v[164:167], a[120:123], v[24:27]\n"
      "s_waitcnt lgkmcnt(8)\n"
      "v_mfma_f32_16x16x32_bf16 v[28:31], v[164:167], a[124:127], v[28:31]\n"
      "s_waitcnt vmcnt(6)\n"
      "v_mfma_f32_16x16x32_bf16 v[32:35], v[168:171], a[96:99], v[32:35]\n"
      "global_load_dwordx4 v[184:187], v241, s[10:11] offset:-2048 sc1\n"
      "global_load_dwordx4 v[188:191], v241, s[72:73] offset:-4096 sc1\n"
      "v_mfma_f32_16x16x32_bf16 v[36:39], v[168:171], a[100:103], v[36:39]\n"
      "global_load_dwordx4 v[128:131], v241, s[72:73] offset:-3072 sc1\n"
      "global_load_dwordx4 v[132:135], v241, s[72:73] offset:-2048 sc1\n"
      "v_mfma_f32_16x16x32_bf16 v[40:43], v[168:171], a[104:107], v[40:43]\n"
      "global_load_dwordx4 v[136:139], v241, s[14:15] offset:-4096 sc1\n"
      "global_load_dwordx4 v[140:143], v241, s[74:75] offset:-4096 sc1\n"
      "v_mfma_f32_16x16x32_bf16 v[44:47], v[168:171], a[108:111], v[44:47]\n"
      "s_xor_b32 s70, s76, 384\n"
      "s_add_u32 s70, s8, s70\n"
      "s_addc_u32 s71, s9, 0\n"
      "s_add_u32 m0, s82, 114688\n"
      "v_mfma_f32_16x16x32_bf16 v[48:51], v[168:171], a[112:115], v[48:51]\n"
      "global_load_lds_dwordx4 v244, s[70:71] offset:0\n"
      "global_load_dwordx4 v[144:147], v241, s[14:15] offset:-3072 sc1\n"
      "v_mfma_f32_16x16x32_bf16 v[52:55], v[168:171], a[116:119], v[52:55]\n"
      "global_load_dwordx4 v[148:151], v241, s[74:75] offset:-3072 sc1\n"
      "global_load_dwordx4 v[152:155], v241, s[14:15] offset:-2048 sc1\n"
      "v_mfma_f32_16x16x32_bf16 v[56:59], v[168:171], a[120:123], v[56:59]\n"
      "global_load_dwordx4 v[156:159], v241, s[74:75] offset:-2048 sc1\n"
      "global_load_dword v252, v253, s[58:59] offset:128 sc1\n"
      "v_mfma_f32_16x16x32_bf16 v[60:63], v[168:171], a[124:127], v[60:63]\n"
      "global_load_lds_dwordx4 v245, s[70:71] offset:1024\n"
      "ds_read_b128 a[144:147], v248 offset:12288\n"
      "v_mfma_f32_16x16x32_bf16 v[64:67], v[172:175], a[96:99], v[64:67]\n"
      "ds_read_b128 a[148:151], v248 offset:14336\n"
      "ds_read_b128 a[176:179], v249 offset:12288\n"
      "v_mfma_f32_16x16x32_bf16 v[68:71], v[172:175], a[100:103], v[68:71]\n"
      "ds_read_b128 a[180:183], v249 offset:14336\n"
      "v_mfma_f32_16x16x32_bf16 v[72:75], v[172:175], a[104:107], v[72:75]\n"
      "global_load_lds_dwordx4 v246, s[70:71] offset:2048\n"
      "v_mfma_f32_16x16x32_bf16 v[76:79], v[172:175], a[108:111], v[76:79]\n"
      "v_mfma_f32_16x16x32_bf16 v[80:83], v[172:175], a[112:115], v[80:83]\n"
      "v_mfma_f32_16x16x32_bf16 v[84:87], v[172:175], a[116:119], v[84:87]\n"
      "global_load_lds_dwordx4 v247, s[70:71] offset:3072\n"
      "v_mfma_f32_16x16x32_bf16 v[88:91], v[172:175], a[120:123], v[88:91]\n"
      "v_mfma_f32_16x16x32_bf16 v[92:95], v[172:175], a[124:127], v[92:95]\n"
      "s_waitcnt vmcnt(15)\n"
      "s_xor_b32 s70, s77, 384\n"
      "s_add_u32 s70, s8, s70\n"
      "s_addc_u32 s71, s9, 0\n"
      "s_add_u32 m0, s82, 131072\n"
      "s_waitcnt lgkmcnt(11)\n"
      "v_mfma_f32_16x16x32_bf16 v[0:3], v[176:179], a[128:131], v[0:3]\n"
      "global_load_lds_dwordx4 v244, s[70:71] offset:0\n"
      "s_waitcnt lgkmcnt(10)\n"
      "v_mfma_f32_16x16x32_bf16 v[4:7], v[176:179], a[132:135], v[4:7]\n"
      "s_waitcnt lgkmcnt(7)\n"
      "v_mfma_f32_16x16x32_bf16 v[8:11], v[176:179], a[136:139], v[8:11]\n"
      "s_waitcnt lgkmcnt(6)\n"
      "v_mfma_f32_16x16x32_bf16 v[12:15], v[176:179], a[140:143], v[12:15]\n"
      "global_load_lds_dwordx4 v245, s[70:71] offset:1024\n"
      "s_waitcnt lgkmcnt(3)\n"
      "v_mfma_f32_16x16x32_bf16 v[16:19], v[176:179], a[144:147], v[16:19]\n"
      "s_lshr_b32 s54, s34, 1\n"
      "s_lshl2_add_u32 s54, s54, s35\n"
      "s_lshl_b32 s54, s54, 12\n"
      "s_waitcnt lgkmcnt(2)\n"
      "v_mfma_f32_16x16x32_bf16 v[20:23], v[176:179], a[148:151], v[20:23]\n"
      "s_add_u32 s54, s54, 4096\n"
      "v_lshl_add_u32 v200, v243, 6, s54\n"
      "global_load_dwordx2 v[202:203], v200, s[58:59] sc1\n"
      "ds_read_b128 a[152:155], v236 offset:0\n"
      "ds_read_b128 a[156:159], v236 offset:2048\n"
      "ds_read_b128 a[184:187], v237 offset:0\n"
      "ds_read_b128 a[188:191], v237 offset:2048\n"
      "s_waitcnt lgkmcnt(3)\n"
      "v_mfma_f32_16x16x32_bf16 v[24:27], v[176:179], a[152:155], v[24:27]\n"
      "global_load_lds_dwordx4 v246, s[70:71] offset:2048\n"
      "s_waitcnt vmcnt(4)\n"
      "ds_read_b128 a[192:195], v236 offset:16384\n"
      "s_waitcnt lgkmcnt(3)\n"
      "v_mfma_f32_16x16x32_bf16 v[28:31], v[176:179], a[156:159], v[28:31]\n"
      "ds_read_b128 a[196:199], v236 offset:18432\n"
      "ds_read_b128 a[224:227], v237 offset:16384\n"
      "v_mfma_f32_16x16x32_bf16 v[32:35], v[180:183], a[128:131], v[32:35]\n"
      "ds_read_b128 a[228:231], v237 offset:18432\n"
      "v_mfma_f32_16x16x32_bf16 v[36:39], v[180:183], a[132:135], v[36:39]\n"
      "global_load_lds_dwordx4 v247, s[70:71] offset:3072\n"
      "v_mfma_f32_16x16x32_bf16 v[40:43], v[180:183], a[136:139], v[40:43]\n"
      "v_mfma_f32_16x16x32_bf16 v[44:47], v[180:183], a[140:143], v[44:47]\n"
      "s_xor_b32 s70, s78, 384\n"
      "s_add_u32 s70, s8, s70\n"
      "s_addc_u32 s71, s9, 0\n"
      "s_add_u32 m0, s82, 147456\n"
      "v_mfma_f32_16x16x32_bf16 v[48:51], v[180:183], a[144:147], v[48:51]\n"
      "global_load_lds_dwordx4 v244, s[70:71] offset:0\n"
      "v_mfma_f32_16x16x32_bf16 v[52:55], v[180:183], a[148:151], v[52:55]\n"
      "v_mfma_f32_16x16x32_bf16 v[56:59], v[180:183], a[152:155], v[56:59]\n"
      "v_mfma_f32_16x16x32_bf16 v[60:63], v[180:183], a[156:159], v[60:63]\n"
      "global_load_lds_dwordx4 v245, s[70:71] offset:1024\n"
      "v_mfma_f32_16x16x32_bf16 v[64:67], v[184:187], a[128:131], v[64:67]\n"
      "v_mfma_f32_16x16x32_bf16 v[68:71], v[184:187], a[132:135], v[68:71]\n"
      "v_mfma_f32_16x16x32_bf16 v[72:75], v[184:187], a[136:139], v[72:75]\n"
      "global_load_lds_dwordx4 v246, s[70:71] offset:2048\n"
      "v_mfma_f32_16x16x32_bf16 v[76:79], v[184:187], a[140:143], v[76:79]\n"
      "v_mfma_f32_16x16x32_bf16 v[80:83], v[184:187], a[144:147], v[80:83]\n"
      "v_mfma_f32_16x16x32_bf16 v[84:87], v[184:187], a[148:151], v[84:87]\n"
      "global_load_lds_dwordx4 v247, s[70:71] offset:3072\n"
      "v_mfma_f32_16x16x32_bf16 v[88:91], v[184:187], a[152:155], v[88:91]\n"
      "v_mfma_f32_16x16x32_bf16 v[92:95], v[184:187], a[156:159], v[92:95]\n"
      "s_xor_b32 s70, s79, 384\n"
      "s_add_u32 s70, s8, s70\n"
      "s_addc_u32 s71, s9, 0\n"
      "s_add_u32 m0, s41, 0\n"
      "v_mfma_f32_16x16x32_bf16 v[0:3], v[188:191], a[160:163], v[0:3]\n"
      "global_load_lds_dwordx4 v244, s[70:71] offset:0\n"
      "v_mfma_f32_16x16x32_bf16 v[4:7], v[188:191], a[164:167], v[4:7]\n"
      "v_mfma_f32_16x16x32_bf16 v[8:11], v[188:191], a[168:171], v[8:11]\n"
      "v_mfma_f32_16x16x32_bf16 v[12:15], v[188:191], a[172:175], v[12:15]\n"
      "global_load_lds_dwordx4 v245, s[70:71] offset:1024\n"
      "v_mfma_f32_16x16x32_bf16 v[16:19], v[188:191], a[176:179], v[16:19]\n"
      "v_mfma_f32_16x16x32_bf16 v[20:23], v[188:191], a[180:183], v[20:23]\n"
      "s_waitcnt lgkmcnt(5)\n"
      "v_mfma_f32_16x16x32_bf16 v[24:27], v[188:191], a[184:187], v[24:27]\n"
      "global_load_lds_dwordx4 v246, s[70:71] offset:2048\n"
      "s_waitcnt lgkmcnt(4)\n"
      "v_mfma_f32_16x16x32_bf16 v[28:31], v[188:191], a[188:191], v[28:31]\n"
      "v_mfma_f32_16x16x32_bf16 v[32:35], v[128:131], a[160:163], v[32:35]\n"
      "v_mfma_f32_16x16x32_bf16 v[36:39], v[128:131], a[164:167], v[36:39]\n"
      "global_load_lds_dwordx4 v247, s[70:71] offset:3072\n"
      "v_mfma_f32_16x16x32_bf16 v[40:43], v[128:131], a[168:171], v[40:43]\n"
      "v_mfma_f32_16x16x32_bf16 v[44:47], v[128:131], a[172:175], v[44:47]\n"
      "v_mfma_f32_16x16x32_bf16 v[48:51], v[128:131], a[176:179], v[48:51]\n"
      "v_mfma_f32_16x16x32_bf16 v[52:55], v[128:131], a[180:183], v[52:55]\n"
      "v_mfma_f32_16x16x32_bf16 v[56:59], v[128:131], a[184:187], v[56:59]\n"
      "v_mfma_f32_16x16x32_bf16 v[60:63], v[128:131], a[188:191], v[60:63]\n"
      "s_waitcnt vmcnt(8)\n"
      "ds_read_b128 a[200:203], v236 offset:32768\n"
      "v_mfma_f32_16x16x32_bf16 v[64:67], v[132:135], a[160:163], v[64:67]\n"
      "ds_read_b128 a[204:207], v236 offset:34816\n"
      "ds_read_b128 a[232:235], v237 offset:32768\n"
      "v_mfma_f32_16x16x32_bf16 v[68:71], v[132:135], a[164:167], v[68:71]\n"
      "ds_read_b128 a[236:239], v237 offset:34816\n"
      "v_mfma_f32_16x16x32_bf16 v[72:75], v[132:135], a[168:171], v[72:75]\n"
      "v_cmp_ne_u32 vcc, s37, v252\n"
      "s_nop 1\n"
      "s_cbranch_vccnz .Lp_s5_%=\n"
      ".Lp_o6_%=:\n"
      "v_mfma_f32_16x16x32_bf16 v[76:79], v[132:135], a[172:175], v[76:79]\n"
      "v_mfma_f32_16x16x32_bf16 v[80:83], v[132:135], a[176:179], v[80:83]\n"
      "global_load_dwordx4 v[160:163], v240, s[10:11] offset:-1024 sc1\n"
      "global_load_dwordx4 v[164:167], v240, s[72:73] offset:-1024 sc1\n"
      "v_mfma_f32_16x16x32_bf16 v[84:87], v[132:135], a[180:183], v[84:87]\n"
      "global_load_dwordx4 v[168:171], v240, s[14:15] offset:-1024 sc1\n"
      "global_load_dwordx4 v[172:175], v240, s[74:75] offset:-1024 sc1\n"
      "v_mfma_f32_16x16x32_bf16 v[88:91], v[132:135], a[184:187], v[88:91]\n"
      "global_load_dwordx4 v[176:179], v241, s[10:11] offset:-1024 sc1\n"
      "global_load_dwordx4 v[180:183], v241, s[72:73] offset:-1024 sc1\n"
      "v_mfma_f32_16x16x32_bf16 v[92:95], v[132:135], a[188:191], v[92:95]\n"
      "global_load_dwordx4 v[184:187], v241, s[14:15] offset:-1024 sc1\n"
      "global_load_dwordx4 v[188:191], v241, s[74:75] offset:-1024 sc1\n"
      "s_waitcnt lgkmcnt(7)\n"
      "v_mfma_f32_16x16x32_bf16 v[0:3], v[136:139], a[192:195], v[0:3]\n"
      "global_load_dwordx4 v[128:131], v240, s[10:11] offset:0 sc1\n"
      "global_load_dwordx4 v[132:135], v240, s[72:73] offset:0 sc1\n"
      "s_waitcnt lgkmcnt(6)\n"
      "v_mfma_f32_16x16x32_bf16 v[4:7], v[136:139], a[196:199], v[4:7]\n"
      "s_waitcnt lgkmcnt(3)\n"
      "v_mfma_f32_16x16x32_bf16 v[8:11], v[136:139], a[200:203], v[8:11]\n"
      "s_waitcnt lgkmcnt(2)\n"
      "v_mfma_f32_16x16x32_bf16 v[12:15], v[136:139], a[204:207], v[12:15]\n"
      "s_waitcnt vmcnt(14)\n"
      "ds_read_b128 a[208:211], v236 offset:49152\n"
      "ds_read_b128 a[212:215], v236 offset:51200\n"
      "ds_read_b128 a[240:243], v237 offset:49152\n"
      "ds_read_b128 a[244:247], v237 offset:51200\n"
      "s_waitcnt lgkmcnt(3)\n"
      "v_mfma_f32_16x16x32_bf16 v[16:19], v[136:139], a[208:211], v[16:19]\n"
      "s_waitcnt vmcnt(10)\n"
      "ds_read_b128 a[216:219], v248 offset:0\n"
      "s_waitcnt lgkmcnt(3)\n"
      "v_mfma_f32_16x16x32_bf16 v[20:23], v[136:139], a[212:215], v[20:23]\n"
      "ds_read_b128 a[220:223], v248 offset:2048\n"
      "ds_read_b128 a[248:251], v249 offset:0\n"
      "ds_read_b128 a[252:255], v249 offset:2048\n"
      "s_waitcnt lgkmcnt(3)\n"
      "v_mfma_f32_16x16x32_bf16 v[24:27], v[136:139], a[216:219], v[24:27]\n"
      "s_sub_u32 s48, s33, 1\n"
      "s_and_b32 s48, s48, 3\n"
      "s_lshl_b32 s48, s48, 14\n"
      "s_waitcnt lgkmcnt(2)\n"
      "v_mfma_f32_16x16x32_bf16 v[28:31], v[136:139], a[220:223], v[28:31]\n"
      "global_load_dwordx4 v[136:139], v240, s[14:15] offset:0 sc1\n"
      "v_lshl_add_u32 v244, v243, 2, s48\n"
      "v_mfma_f32_16x16x32_bf16 v[0:3], v[140:143], a[224:227], v[0:3]\n"
      "s_sub_u32 s48, s33, 2\n"
      "s_and_b32 s48, s48, 3\n"
      "s_lshl_b32 s48, s48, 14\n"
      "v_mfma_f32_16x16x32_bf16 v[4:7], v[140:143], a[228:231], v[4:7]\n"
      "v_lshl_add_u32 v245, v243, 2, s48\n"
      "s_sub_u32 s48, s33, 3\n"
      "v_mfma_f32_16x16x32_bf16 v[8:11], v[140:143], a[232:235], v[8:11]\n"
      "s_and_b32 s48, s48, 3\n"
      "s_lshl_b32 s48, s48, 14\n"
      "v_lshl_add_u32 v246, v243, 2, s48\n"
      "v_mfma_f32_16x16x32_bf16 v[12:15], v[140:143], a[236:239], v[12:15]\n"
      "v_bfe_u32 v248, v243, 1, 3\n"
      "v_lshrrev_b32 v249, 4, v243\n"
      "v_mfma_f32_16x16x32_bf16 v[16:19], v[140:143], a[240:243], v[16:19]\n"
      "v_lshlrev_b32 v249, 1, v249\n"
      "v_sub_u32 v248, v248, v249\n"
      "v_mfma_f32_16x16x32_bf16 v[20:23], v[140:143], a[244:247], v[20:23]\n"
      "v_and_b32 v249, 16, v242\n"
      "v_mov_b32 v254, 0x3f80\n"
      "s_waitcnt lgkmcnt(1)\n"
      "v_mfma_f32_16x16x32_bf16 v[24:27], v[140:143], a[248:251], v[24:27]\n"
      "v_lshlrev_b32 v249, v249, v254\n"
      "v_cmp_eq_u32 vcc, 0, v248\n"
      "s_nop 1\n"
      "v_cndmask_b32 v250, 0, v249, vcc\n"
      "s_waitcnt lgkmcnt(0)\n"
      "v_mfma_f32_16x16x32_bf16 v[28:31], v[140:143], a[252:255], v[28:31]\n"
      "global_load_dwordx4 v[140:143], v240, s[74:75] offset:0 sc1\n"
      "v_cmp_eq_u32 vcc, 1, v248\n"
      "s_nop 1\n"
      "v_cndmask_b32 v251, 0, v249, vcc\n"
      "v_mfma_f32_16x16x32_bf16 v[32:35], v[144:147], a[192:195], v[32:35]\n"
      "v_cmp_ne_u32 vcc, s37, v203\n"
      "s_nop 1\n"
      "s_cbranch_vccnz .Lp_s8_%=\n"
      ".Lp_o9_%=:\n"
      "v_mfma_f32_16x16x32_bf16 v[36:39], v[144:147], a[196:199], v[36:39]\n"
      "s_mov_b32 m0, s41\n"
      "v_mov_b32 v252, v250\n"
      "v_mov_b32 v253, v251\n"
      "v_mfma_f32_16x16x32_bf16 v[40:43], v[144:147], a[200:203], v[40:43]\n"
      "v_mov_b32 v248, 0\n"
      "v_mov_b32 v249, 0\n"
      "v_mfma_f32_16x16x32_bf16 v[44:47], v[144:147], a[204:207], v[44:47]\n"
      "v_mov_b32 v254, 0\n"
      "v_mov_b32 v255, 0\n"
      "v_mfma_f32_16x16x32_bf16 v[48:51], v[144:147], a[208:211], v[48:51]\n"
      "s_add_u32 s48, s34, 1\n"
      "s_and_b32 s48, s48, 3\n"
      "s_mul_i32 s48, s48, 36864\n"
      "v_mfma_f32_16x16x32_bf16 v[52:55], v[144:147], a[212:215], v[52:55]\n"
      "s_add_u32 s48, s48, 0\n"
      "s_add_u32 s52, s60, s48\n"
      "s_addc_u32 s53, s61, 0\n"
      "v_mfma_f32_16x16x32_bf16 v[56:59], v[144:147], a[216:219], v[56:59]\n"
      "v_cvt_pk_bf16_f32 v8, v8, v9\n"
      "v_cvt_pk_bf16_f32 v9, v10, v11\n"
      "v_mfma_f32_16x16x32_bf16 v[60:63], v[144:147], a[220:223], v[60:63]\n"
      "global_load_dwordx4 v[144:147], v241, s[10:11] offset:0 sc1\n"
      "v_max_u32_dpp v202, v202, v202 quad_perm:[1,0,3,2] row_mask:0xf bank_mask:0xf\n"
      "v_mfma_f32_16x16x32_bf16 v[32:35], v[148:151], a[224:227], v[32:35]\n"
      "ds_write_addtid_b32 v8 offset:0\n"
      "v_cvt_pk_bf16_f32 v10, v12, v13\n"
      "v_mfma_f32_16x16x32_bf16 v[36:39], v[148:151], a[228:231], v[36:39]\n"
      "ds_write_addtid_b32 v9 offset:256\n"
      "v_cvt_pk_bf16_f32 v11, v14, v15\n"
      "v_mfma_f32_16x16x32_bf16 v[40:43], v[148:151], a[232:235], v[40:43]\n"
      "ds_write_addtid_b32 v10 offset:512\n"
      "v_cvt_pk_bf16_f32 v12, v16, v17\n"
      "v_mfma_f32_16x16x32_bf16 v[44:47], v[148:151], a[236:239], v[44:47]\n"
      "ds_write_addtid_b32 v11 offset:768\n"
      "v_cvt_pk_bf16_f32 v13, v18, v19\n"
      "v_mfma_f32_16x16x32_bf16 v[48:51], v[148:151], a[240:243], v[48:51]\n"
      "ds_write_addtid_b32 v12 offset:1024\n"
      "v_cvt_pk_bf16_f32 v14, v20, v21\n"
      "v_mfma_f32_16x16x32_bf16 v[52:55], v[148:151], a[244:247], v[52:55]\n"
      "ds_write_addtid_b32 v13 offset:1280\n"
      "v_cvt_pk_bf16_f32 v15, v22, v23\n"
      "v_mfma_f32_16x16x32_bf16 v[56:59], v[148:151], a[248:251], v[56:59]\n"
      "ds_write_addtid_b32 v14 offset:1536\n"
      "v_cvt_pk_bf16_f32 v16, v24, v25\n"
      "v_mfma_f32_16x16x32_bf16 v[60:63], v[148:151], a[252:255], v[60:63]\n"
      "global_load_dwordx4 v[148:151], v241, s[72:73] offset:0 sc1\n"
      "v_max_u32_dpp v202, v202, v202 quad_perm:[2,3,0,1] row_mask:0xf bank_mask:0xf\n"
      "v_mfma_f32_16x16x32_bf16 v[64:67], v[152:155], a[192:195], v[64:67]\n"
      "ds_write_addtid_b32 v15 offset:1792\n"
      "v_cvt_pk_bf16_f32 v17, v26, v27\n"
      "v_mfma_f32_16x16x32_bf16 v[68:71], v[152:155], a[196:199], v[68:71]\n"
      "ds_write_addtid_b32 v16 offset:2048\n"
      "v_cvt_pk_bf16_f32 v18, v28, v29\n"
      "v_mfma_f32_16x16x32_bf16 v[72:75], v[152:155], a[200:203], v[72:75]\n"
      "ds_write_addtid_b32 v17 offset:2304\n"
      "v_cvt_pk_bf16_f32 v19, v30, v31\n"
      "v_mfma_f32_16x16x32_bf16 v[76:79], v[152:155], a[204:207], v[76:79]\n"
      "ds_write_addtid_b32 v18 offset:2560\n"
      "v_max_u32_dpp v202, v202, v202 row_half_mirror row_mask:0xf bank_mask:0xf\n"
      "v_mfma_f32_16x16x32_bf16 v[80:83], v[152:155], a[208:211], v[80:83]\n"
      "ds_write_addtid_b32 v19 offset:2816\n"
      "v_cvt_pk_bf16_f32 v40, v40, v41\n"
      "v_mfma_f32_16x16x32_bf16 v[84:87], v[152:155], a[212:215], v[84:87]\n"
      "v_cvt_pk_bf16_f32 v41, v42, v43\n"
      "v_max_u32_dpp v202, v202, v202 row_mirror row_mask:0xf bank_mask:0xf\n"
      "v_mfma_f32_16x16x32_bf16 v[88:91], v[152:155], a[216:219], v[88:91]\n"
      "ds_write_addtid_b32 v40 offset:3072\n"
      "v_cvt_pk_bf16_f32 v42, v44, v45\n"
      "v_mfma_f32_16x16x32_bf16 v[92:95], v[152:155], a[220:223], v[92:95]\n"
      "global_load_dwordx4 v[152:155], v241, s[14:15] offset:0 sc1\n"
      "v_mov_b32 v201, v202\n"
      "v_mfma_f32_16x16x32_bf16 v[64:67], v[156:159], a[224:227], v[64:67]\n"
      "ds_write_addtid_b32 v41 offset:3328\n"
      "v_cvt_pk_bf16_f32 v43, v46, v47\n"
      "v_mfma_f32_16x16x32_bf16 v[68:71], v[156:159], a[228:231], v[68:71]\n"
      "ds_write_addtid_b32 v42 offset:3584\n"
      "v_cvt_pk_bf16_f32 v44, v48, v49\n"
      "v_mfma_f32_16x16x32_bf16 v[72:75], v[156:159], a[232:235], v[72:75]\n"
      "ds_write_addtid_b32 v43 offset:3840\n"
      "v_cvt_pk_bf16_f32 v45, v50, v51\n"
      "v_mfma_f32_16x16x32_bf16 v[76:79], v[156:159], a[236:239], v[76:79]\n"
      "ds_write_addtid_b32 v44 offset:4096\n"
      "v_cvt_pk_bf16_f32 v46, v52, v53\n"
      "v_mfma_f32_16x16x32_bf16 v[80:83], v[156:159], a[240:243], v[80:83]\n"
      "ds_write_addtid_b32 v45 offset:4352\n"
      "v_cvt_pk_bf16_f32 v47, v54, v55\n"
      "v_mfma_f32_16x16x32_bf16 v[84:87], v[156:159], a[244:247], v[84:87]\n"
      "ds_write_addtid_b32 v46 offset:4608\n"
      "v_cvt_pk_bf16_f32 v48, v56, v57\n"
      "v_mfma_f32_16x16x32_bf16 v[88:91], v[156:159], a[248:251], v[88:91]\n"
      "ds_write_addtid_b32 v47 offset:4864\n"
      "v_cvt_pk_bf16_f32 v49, v58, v59\n"
      "v_mfma_f32_16x16x32_bf16 v[92:95], v[156:159], a[252:255], v[92:95]\n"
      "s_waitcnt vmcnt(13)\n"
      "global_load_dwordx4 v[156:159], v241, s[74:75] offset:0 sc1\n"
      "v_permlane16_swap_b32 v202, v201\n"
      "v_mfma_f32_16x16x32_bf16 v[96:99], v[160:163], a[0:3], 0\n"
      "ds_write_addtid_b32 v48 offset:5120\n"
      "v_cvt_pk_bf16_f32 v50, v60, v61\n"
      "v_mfma_f32_16x16x32_bf16 v[100:103], v[160:163], a[4:7], 0\n"
      "ds_write_addtid_b32 v49 offset:5376\n"
      "v_cvt_pk_bf16_f32 v51, v62, v63\n"
      "v_mfma_f32_16x16x32_bf16 v[104:107], v[160:163], a[8:11], 0\n"
      "ds_write_addtid_b32 v50 offset:5632\n"
      "v_max_u32 v202, v202, v201\n"
      "v_mfma_f32_16x16x32_bf16 v[108:111], v[160:163], a[12:15], 0\n"
      "ds_write_addtid_b32 v51 offset:5888\n"
      "v_mov_b32 v201, v202\n"
      "v_mfma_f32_16x16x32_bf16 v[112:115], v[160:163], a[16:19], 0\n"
      "s_nop 0\n"
      "v_permlane32_swap_b32 v202, v201\n"
      "v_mfma_f32_16x16x32_bf16 v[116:119], v[160:163], a[20:23], 0\n"
      "v_max_u32 v202, v202, v201\n"
      "s_nop 0\n"
      "v_readfirstlane_b32 s40, v202\n"
      "v_mfma_f32_16x16x32_bf16 v[120:123], v[160:163], a[24:27], 0\n"
      "s_nop 0\n"
      "s_andn2_b32 s42, 0x7ff, s40\n"
      "s_waitcnt lgkmcnt(0)\n"
      "s_barrier\n"
      "v_mfma_f32_16x16x32_bf16 v[124:127], v[160:163], a[28:31], 0\n"
      "global_load_dwordx4 v[160:163], v240, s[10:11] offset:1024 sc1\n"
      "ds_read2st64_b32 v[20:21], v244 offset0:0 offset1:1\n"
      "v_mfma_f32_16x16x32_bf16 v[96:99], v[164:167], a[32:35], v[96:99]\n"
      "ds_read2st64_b32 v[22:23], v245 offset0:4 offset1:5\n"
      "ds_read2st64_b32 v[24:25], v244 offset0:2 offset1:3\n"
      "v_mfma_f32_16x16x32_bf16 v[100:103], v[164:167], a[36:39], v[100:103]\n"
      "ds_read2st64_b32 v[26:27], v245 offset0:6 offset1:7\n"
      "ds_read2st64_b32 v[28:29], v246 offset0:8 offset1:9\n"
      "v_mfma_f32_16x16x32_bf16 v[104:107], v[164:167], a[40:43], v[104:107]\n"
      "ds_read2st64_b32 v[30:31], v246 offset0:10 offset1:11\n"
      "ds_read2st64_b32 v[52:53], v244 offset0:12 offset1:13\n"
      "v_mfma_f32_16x16x32_bf16 v[108:111], v[164:167], a[44:47], v[108:111]\n"
      "ds_read2st64_b32 v[54:55], v245 offset0:16 offset1:17\n"
      "ds_read2st64_b32 v[56:57], v244 offset0:14 offset1:15\n"
      "v_mfma_f32_16x16x32_bf16 v[112:115], v[164:167], a[48:51], v[112:115]\n"
      "ds_read2st64_b32 v[58:59], v245 offset0:18 offset1:19\n"
      "ds_read2st64_b32 v[60:61], v246 offset0:20 offset1:21\n"
      "v_mfma_f32_16x16x32_bf16 v[116:119], v[164:167], a[52:55], v[116:119]\n"
      "ds_read2st64_b32 v[62:63], v246 offset0:22 offset1:23\n"
      "v_cvt_pk_bf16_f32 v72, v72, v73\n"
      "v_mfma_f32_16x16x32_bf16 v[120:123], v[164:167], a[56:59], v[120:123]\n"
      "s_lshr_b32 s43, s42, 5\n"
      "s_lshl2_add_u32 s44, s36, s33\n"
      "s_cmp_eq_u32 s43, s44\n"
      "s_cbranch_scc0 .Lp_e10_%=\n"
      "s_lshl_b32 s43, s42, 10\n"
      "v_add_u32 v208, s43, v242\n"
      "global_load_dwordx4 v[216:219], v208, s[16:17]\n"
      "global_load_dwordx4 v[220:223], v242, s[24:25]\n"
      ".Lp_e10_%=:\n"
      "v_mfma_f32_16x16x32_bf16 v[124:127], v[164:167], a[60:63], v[124:127]\n"
      "s_waitcnt vmcnt(13)\n"
      "global_load_dwordx4 v[164:167], v240, s[72:73] offset:1024 sc1\n"
      "s_waitcnt lgkmcnt(6)\n"
      "v_mfma_f32_16x16x32_bf16 v[96:99], v[168:171], a[64:67], v[96:99]\n"
      "v_mfma_f32_16x16x32_bf16 v[0:3], v[250:253], v[20:23], v[0:3]\n"
      "v_cvt_pk_bf16_f32 v73, v74, v75\n"
      "v_mfma_f32_16x16x32_bf16 v[100:103], v[168:171], a[68:71], v[100:103]\n"
      "v_mfma_f32_16x16x32_bf16 v[4:7], v[250:253], v[24:27], v[4:7]\n"
      "v_mfma_f32_16x16x32_bf16 v[0:3], v[252:255], v[28:31], v[0:3]\n"
      "v_mfma_f32_16x16x32_bf16 v[104:107], v[168:171], a[72:75], v[104:107]\n"
      "v_mfma_f32_16x16x32_bf16 v[4:7], v[248:251], v[28:31], v[4:7]\n"
      "v_mov_b32 v11, s37\n"
      "v_mfma_f32_16x16x32_bf16 v[108:111], v[168:171], a[76:79], v[108:111]\n"
      "ds_write_addtid_b32 v72 offset:6144\n"
      "v_cvt_pk_bf16_f32 v74, v76, v77\n"
      "v_mfma_f32_16x16x32_bf16 v[112:115], v[168:171], a[80:83], v[112:115]\n"
      "ds_write_addtid_b32 v73 offset:6400\n"
      "v_cvt_pk_bf16_f32 v8, v0, v1\n"
      "v_mfma_f32_16x16x32_bf16 v[116:119], v[168:171], a[84:87], v[116:119]\n"
      "v_cvt_pk_bf16_f32 v9, v2, v3\n"
      "v_cvt_pk_bf16_f32 v10, v4, v5\n"
      "v_mfma_f32_16x16x32_bf16 v[120:123], v[168:171], a[88:91], v[120:123]\n"
      "v_cvt_pk_bf16_f32 v247, v6, v7\n"
      "v_cvt_pk_bf16_f32 v75, v78, v79\n"
      "v_mfma_f32_16x16x32_bf16 v[124:127], v[168:171], a[92:95], v[124:127]\n"
      "global_load_dwordx4 v[168:171], v240, s[14:15] offset:1024 sc1\n"
      "global_store_dwordx4 v242, v[8:11], s[52:53] offset:0 sc1\n"
      "v_mfma_f32_16x16x32_bf16 v[96:99], v[172:175], a[96:99], v[96:99]\n"
      "s_waitcnt lgkmcnt(2)\n"
      "v_mfma_f32_16x16x32_bf16 v[32:35], v[250:253], v[52:55], v[32:35]\n"
      "v_mfma_f32_16x16x32_bf16 v[36:39], v[250:253], v[56:59], v[36:39]\n"
      "v_mfma_f32_16x16x32_bf16 v[100:103], v[172:175], a[100:103], v[100:103]\n"
      "v_mfma_f32_16x16x32_bf16 v[32:35], v[252:255], v[60:63], v[32:35]\n"
      "v_mfma_f32_16x16x32_bf16 v[36:39], v[248:251], v[60:63], v[36:39]\n"
      "v_mfma_f32_16x16x32_bf16 v[104:107], v[172:175], a[104:107], v[104:107]\n"
      "ds_write_addtid_b32 v74 offset:6656\n"
      "v_mov_b32 v43, s37\n"
      "v_mfma_f32_16x16x32_bf16 v[108:111], v[172:175], a[108:111], v[108:111]\n"
      "v_mov_b32 v44, v247\n"
      "v_mov_b32 v47, s37\n"
      "v_mfma_f32_16x16x32_bf16 v[112:115], v[172:175], a[112:115], v[112:115]\n"
      "v_cvt_pk_bf16_f32 v76, v80, v81\n"
      "ds_write_addtid_b32 v75 offset:6912\n"
      "v_mfma_f32_16x16x32_bf16 v[116:119], v[172:175], a[116:119], v[116:119]\n"
      "v_cvt_pk_bf16_f32 v40, v32, v33\n"
      "v_cvt_pk_bf16_f32 v41, v34, v35\n"
      "v_mfma_f32_16x16x32_bf16 v[120:123], v[172:175], a[120:123], v[120:123]\n"
      "v_cvt_pk_bf16_f32 v42, v36, v37\n"
      "v_cvt_pk_bf16_f32 v45, v38, v39\n"
      "v_mfma_f32_16x16x32_bf16 v[124:127], v[172:175], a[124:127], v[124:127]\n"
      "s_waitcnt vmcnt(14)\n"
      "global_load_dwordx4 v[172:175], v240, s[74:75] offset:1024 sc1\n"
      "v_cvt_pk_bf16_f32 v77, v82, v83\n"
      "v_mfma_f32_16x16x32_bf16 v[96:99], v[176:179], a[128:131], v[96:99]\n"
      "global_store_dwordx4 v242, v[40:43], s[52:53] offset:1024 sc1\n"
      "global_store_dwordx4 v242, v[44:47], s[52:53] offset:2048 sc1\n"
      "v_mfma_f32_16x16x32_bf16 v[100:103], v[176:179], a[132:135], v[100:103]\n"
      "s_add_u32 s48, s34, 2\n"
      "ds_write_addtid_b32 v76 offset:7168\n"
      "s_and_b32 s48, s48, 3\n"
      "v_mfma_f32_16x16x32_bf16 v[104:107], v[176:179], a[136:139], v[104:107]\n"
      "s_mul_i32 s48, s48, 36864\n"
      "s_add_u32 s48, s48, 12288\n"
      "s_add_u32 s52, s60, s48\n"
      "s_addc_u32 s53, s61, 0\n"
      "v_mfma_f32_16x16x32_bf16 v[108:111], v[176:179], a[140:143], v[108:111]\n"
      "v_cvt_pk_bf16_f32 v78, v84, v85\n"
      "ds_write_addtid_b32 v77 offset:7424\n"
      "v_mfma_f32_16x16x32_bf16 v[112:115], v[176:179], a[144:147], v[112:115]\n"
      "v_cvt_pk_bf16_f32 v79, v86, v87\n"
      "ds_write_addtid_b32 v78 offset:7680\n"
      "v_mfma_f32_16x16x32_bf16 v[116:119], v[176:179], a[148:151], v[116:119]\n"
      "v_cvt_pk_bf16_f32 v80, v88, v89\n"
      "ds_write_addtid_b32 v79 offset:7936\n"
      "v_mfma_f32_16x16x32_bf16 v[120:123], v[176:179], a[152:155], v[120:123]\n"
      "v_cvt_pk_bf16_f32 v81, v90, v91\n"
      "v_mfma_f32_16x16x32_bf16 v[124:127], v[176:179], a[156:159], v[124:127]\n"
      "global_load_dwordx4 v[176:179], v241, s[10:11] offset:1024 sc1\n"
      "v_mfma_f32_16x16x32_bf16 v[96:99], v[180:183], a[160:163], v[96:99]\n"
      "ds_write_addtid_b32 v80 offset:8192\n"
      "v_cvt_pk_bf16_f32 v82, v92, v93\n"
      "v_mfma_f32_16x16x32_bf16 v[100:103], v[180:183], a[164:167], v[100:103]\n"
      "ds_write_addtid_b32 v81 offset:8448\n"
      "v_cvt_pk_bf16_f32 v83, v94, v95\n"
      "v_mfma_f32_16x16x32_bf16 v[104:107], v[180:183], a[168:171], v[104:107]\n"
      "ds_write_addtid_b32 v82 offset:8704\n"
      "v_mfma_f32_16x16x32_bf16 v[108:111], v[180:183], a[172:175], v[108:111]\n"
      "ds_write_addtid_b32 v83 offset:8960\n"
      "v_mfma_f32_16x16x32_bf16 v[112:115], v[180:183], a[176:179], v[112:115]\n"
      "v_mfma_f32_16x16x32_bf16 v[116:119], v[180:183], a[180:183], v[116:119]\n"
      "v_mfma_f32_16x16x32_bf16 v[120:123], v[180:183], a[184:187], v[120:123]\n"
      "v_mfma_f32_16x16x32_bf16 v[124:127], v[180:183], a[188:191], v[124:127]\n"
      "s_waitcnt vmcnt(16)\n"
      "global_load_dwordx4 v[180:183], v241, s[72:73] offset:1024 sc1\n"
      "v_mfma_f32_16x16x32_bf16 v[96:99], v[184:187], a[192:195], v[96:99]\n"
      "v_mfma_f32_16x16x32_bf16 v[100:103], v[184:187], a[196:199], v[100:103]\n"
      "v_mfma_f32_16x16x32_bf16 v[104:107], v[184:187], a[200:203], v[104:107]\n"
      "v_mfma_f32_16x16x32_bf16 v[108:111], v[184:187], a[204:207], v[108:111]\n"
      "v_mfma_f32_16x16x32_bf16 v[112:115], v[184:187], a[208:211], v[112:115]\n"
      "v_mfma_f32_16x16x32_bf16 v[116:119], v[184:187], a[212:215], v[116:119]\n"
      "v_mfma_f32_16x16x32_bf16 v[120:123], v[184:187], a[216:219], v[120:123]\n"
      "v_mfma_f32_16x16x32_bf16 v[124:127], v[184:187], a[220:223], v[124:127]\n"
      "global_load_dwordx4 v[184:187], v241, s[14:15] offset:1024 sc1\n"
      "v_mfma_f32_16x16x32_bf16 v[96:99], v[188:191], a[224:227], v[96:99]\n"
      "v_mfma_f32_16x16x32_bf16 v[100:103], v[188:191], a[228:231], v[100:103]\n"
      "v_mfma_f32_16x16x32_bf16 v[104:107], v[188:191], a[232:235], v[104:107]\n"
      "v_mfma_f32_16x16x32_bf16 v[108:111], v[188:191], a[236:239], v[108:111]\n"
      "v_mfma_f32_16x16x32_bf16 v[112:115], v[188:191], a[240:243], v[112:115]\n"
      "v_mfma_f32_16x16x32_bf16 v[116:119], v[188:191], a[244:247], v[116:119]\n"
      "v_mfma_f32_16x16x32_bf16 v[120:123], v[188:191], a[248:251], v[120:123]\n"
      "v_mfma_f32_16x16x32_bf16 v[124:127], v[188:191], a[252:255], v[124:127]\n"
      "s_waitcnt vmcnt(16)\n"
      "global_load_dwordx4 v[188:191], v241, s[74:75] offset:1024 sc1\n"
      "v_mfma_f32_16x16x32_bf16 v[0:3], v[128:131], a[0:3], 0\n"
      "v_mfma_f32_16x16x32_bf16 v[4:7], v[128:131], a[4:7], 0\n"
      "v_cvt_pk_bf16_f32 v104, v104, v105\n"
      "v_cvt_pk_bf16_f32 v105, v106, v107\n"
      "v_mfma_f32_16x16x32_bf16 v[8:11], v[128:131], a[8:11], 0\n"
      "ds_write_addtid_b32 v104 offset:9216\n"
      "v_cvt_pk_bf16_f32 v106, v108, v109\n"
      "v_mfma_f32_16x16x32_bf16 v[12:15], v[128:131], a[12:15], 0\n"
      "ds_write_addtid_b32 v105 offset:9472\n"
      "v_cvt_pk_bf16_f32 v107, v110, v111\n"
      "v_mfma_f32_16x16x32_bf16 v[16:19], v[128:131], a[16:19], 0\n"
      "ds_write_addtid_b32 v106 offset:9728\n"
      "v_cvt_pk_bf16_f32 v108, v112, v113\n"
      "v_mfma_f32_16x16x32_bf16 v[20:23], v[128:131], a[20:23], 0\n"
      "ds_write_addtid_b32 v107 offset:9984\n"
      "v_cvt_pk_bf16_f32 v109, v114, v115\n"
      "v_mfma_f32_16x16x32_bf16 v[24:27], v[128:131], a[24:27], 0\n"
      "ds_write_addtid_b32 v108 offset:10240\n"
      "v_cvt_pk_bf16_f32 v110, v116, v117\n"
      "v_mfma_f32_16x16x32_bf16 v[28:31], v[128:131], a[28:31], 0\n"
      "global_load_dwordx4 v[128:131], v240, s[10:11] offset:2048 sc1\n"
      "v_mfma_f32_16x16x32_bf16 v[0:3], v[132:135], a[32:35], v[0:3]\n"
      "ds_write_addtid_b32 v109 offset:10496\n"
      "v_cvt_pk_bf16_f32 v111, v118, v119\n"
      "v_mfma_f32_16x16x32_bf16 v[4:7], v[132:135], a[36:39], v[4:7]\n"
      "ds_write_addtid_b32 v110 offset:10752\n"
      "v_cvt_pk_bf16_f32 v112, v120, v121\n"
      "v_mfma_f32_16x16x32_bf16 v[8:11], v[132:135], a[40:43], v[8:11]\n"
      "ds_write_addtid_b32 v111 offset:11008\n"
      "v_cvt_pk_bf16_f32 v113, v122, v123\n"
      "v_mfma_f32_16x16x32_bf16 v[12:15], v[132:135], a[44:47], v[12:15]\n"
      "ds_write_addtid_b32 v112 offset:11264\n"
      "v_cvt_pk_bf16_f32 v114, v124, v125\n"
      "v_mfma_f32_16x16x32_bf16 v[16:19], v[132:135], a[48:51], v[16:19]\n"
      "ds_write_addtid_b32 v113 offset:11520\n"
      "v_cvt_pk_bf16_f32 v115, v126, v127\n"
      "v_mfma_f32_16x16x32_bf16 v[20:23], v[132:135], a[52:55], v[20:23]\n"
      "ds_write_addtid_b32 v114 offset:11776\n"
      "v_mfma_f32_16x16x32_bf16 v[24:27], v[132:135], a[56:59], v[24:27]\n"
      "ds_write_addtid_b32 v115 offset:12032\n"
      "v_mfma_f32_16x16x32_bf16 v[28:31], v[132:135], a[60:63], v[28:31]\n"
      "s_waitcnt vmcnt(16)\n"
      "global_load_dwordx4 v[132:135], v240, s[72:73] offset:2048 sc1\n"
      "v_mfma_f32_16x16x32_bf16 v[0:3], v[136:139], a[64:67], v[0:3]\n"
      "s_lshr_b32 s43, s42, 5\n"
      "s_lshl2_add_u32 s44, s36, s33\n"
      "s_cmp_eq_u32 s43, s44\n"
      "s_cbranch_scc0 .Lp_e11_%=\n"
      "s_ashr_i32 s43, s40, 31\n"
      "s_not_b32 s43, s43\n"
      "s_or_b32 s43, s43, 0x80000000\n"
      "s_xor_b32 s43, s43, s40\n"
      "s_and_b32 s43, s43, 0xffff0000\n"
      "s_waitcnt vmcnt(12)\n"
      "v_lshlrev_b32 v208, 16, v216\n"
      "s_waitcnt vmcnt(12)\n"
      "v_lshlrev_b32 v209, 16, v220\n"
      "v_and_b32 v210, 0xffff0000, v216\n"
      "v_and_b32 v211, 0xffff0000, v220\n"
      "v_mul_f32 v208, v208, v209\n"
      "v_mul_f32 v210, v210, v211\n"
      "v_cvt_pk_bf16_f32 v208, v208, v210\n"
      "v_lshlrev_b32 v209, 16, v208\n"
      "v_and_b32 v208, 0xffff0000, v208\n"
      "v_add_f32 v212, v208, v209\n"
      "v_lshlrev_b32 v208, 16, v217\n"
      "v_lshlrev_b32 v209, 16, v221\n"
      "v_and_b32 v210, 0xffff0000, v217\n"
      "v_and_b32 v211, 0xffff0000, v221\n"
      "v_mul_f32 v208, v208, v209\n"
      "v_mul_f32 v210, v210, v211\n"
      "v_cvt_pk_bf16_f32 v208, v208, v210\n"
      "v_lshlrev_b32 v209, 16, v208\n"
      "v_and_b32 v208, 0xffff0000, v208\n"
      "v_add_f32 v208, v208, v209\n"
      "v_add_f32 v212, v212, v208\n"
      "v_lshlrev_b32 v208, 16, v218\n"
      "v_lshlrev_b32 v209, 16, v222\n"
      "v_and_b32 v210, 0xffff0000, v218\n"
      "v_and_b32 v211, 0xffff0000, v222\n"
      "v_mul_f32 v208, v208, v209\n"
      "v_mul_f32 v210, v210, v211\n"
      "v_cvt_pk_bf16_f32 v208, v208, v210\n"
      "v_lshlrev_b32 v209, 16, v208\n"
      "v_and_b32 v208, 0xffff0000, v208\n"
      "v_add_f32 v208, v208, v209\n"
      "v_add_f32 v212, v212, v208\n"
      "v_lshlrev_b32 v208, 16, v219\n"
      "v_lshlrev_b32 v209, 16, v223\n"
      "v_and_b32 v210, 0xffff0000, v219\n"
      "v_and_b32 v211, 0xffff0000, v223\n"
      "v_mul_f32 v208, v208, v209\n"
      "v_mul_f32 v210, v210, v211\n"
      "v_cvt_pk_bf16_f32 v208, v208, v210\n"
      "v_lshlrev_b32 v209, 16, v208\n"
      "v_and_b32 v208, 0xffff0000, v208\n"
      "v_add_f32 v208, v208, v209\n"
      "v_add_f32 v212, v212, v208\n"
      "s_nop 1\n"
      "v_add_f32_dpp v212, v212, v212 quad_perm:[1,0,3,2] row_mask:0xf bank_mask:0xf\n"
      "s_nop 1\n"
      "v_add_f32_dpp v212, v212, v212 quad_perm:[2,3,0,1] row_mask:0xf bank_mask:0xf\n"
      "s_nop 1\n"
      "v_add_f32_dpp v212, v212, v212 row_half_mirror row_mask:0xf bank_mask:0xf\n"
      "s_nop 1\n"
      "v_add_f32_dpp v212, v212, v212 row_mirror row_mask:0xf bank_mask:0xf\n"
      "v_mov_b32 v208, v212\n"
      "s_nop 1\n"
      "v_permlane16_swap_b32 v212, v208\n"
      "v_add_f32 v212, v212, v208\n"
      "v_mov_b32 v208, v212\n"
      "s_nop 1\n"
      "v_permlane32_swap_b32 v212, v208\n"
      "v_add_f32 v212, v212, v208\n"
      "v_cvt_pk_bf16_f32 v212, v212, v212\n"
      "v_and_b32 v212, 0xffff0000, v212\n"
      "v_writelane_b32 v212, s43, 1\n"
      "v_mul_f32 v212, 0xbfb8aa3b, v212\n"
      "v_exp_f32 v212, v212\n"
      "s_nop 0\n"
      "v_add_f32 v212, 1.0, v212\n"
      "v_rcp_f32 v212, v212\n"
      "s_nop 0\n"
      "v_cvt_pk_bf16_f32 v212, v212, v212\n"
      "v_and_b32 v212, 0xffff0000, v212\n"
      "s_nop 0\n"
      "v_readfirstlane_b32 s39, v212\n"
      "v_readlane_b32 s84, v212, 1\n"
      ".Lp_e11_%=:\n"
      "v_mfma_f32_16x16x32_bf16 v[4:7], v[136:139], a[68:71], v[4:7]\n"
      "s_lshl2_add_u32 s54, s36, s33\n"
      "s_waitcnt lgkmcnt(0)\n"
      "s_barrier\n"
      "v_mfma_f32_16x16x32_bf16 v[8:11], v[136:139], a[72:75], v[8:11]\n"
      "s_lshl_b32 s55, s54, 15\n"
      "ds_read2st64_b32 v[84:85], v244 offset0:24 offset1:25\n"
      "s_lshl2_add_u32 s56, s35, s34\n"
      "v_mfma_f32_16x16x32_bf16 v[12:15], v[136:139], a[76:79], v[12:15]\n"
      "s_lshl_b32 s56, s56, 6\n"
      "ds_read2st64_b32 v[86:87], v245 offset0:28 offset1:29\n"
      "s_add_u32 s55, s55, s56\n"
      "v_mfma_f32_16x16x32_bf16 v[16:19], v[136:139], a[80:83], v[16:19]\n"
      "ds_read2st64_b32 v[88:89], v244 offset0:26 offset1:27\n"
      "v_and_b32 v209, 15, v243\n"
      "v_mfma_f32_16x16x32_bf16 v[20:23], v[136:139], a[84:87], v[20:23]\n"
      "ds_read2st64_b32 v[90:91], v245 offset0:30 offset1:31\n"
      "v_and_b32 v208, 48, v243\n"
      "v_mfma_f32_16x16x32_bf16 v[24:27], v[136:139], a[88:91], v[24:27]\n"
      "ds_read2st64_b32 v[92:93], v246 offset0:32 offset1:33\n"
      "v_lshl_or_b32 v208, v209, 10, v208\n"
      "v_mfma_f32_16x16x32_bf16 v[28:31], v[136:139], a[92:95], v[28:31]\n"
      "global_load_dwordx4 v[136:139], v240, s[14:15] offset:2048 sc1\n"
      "v_add_u32 v208, s55, v208\n"
      "v_mfma_f32_16x16x32_bf16 v[0:3], v[140:143], a[96:99], v[0:3]\n"
      "ds_read2st64_b32 v[94:95], v246 offset0:34 offset1:35\n"
      "v_add_u32 v210, 16384, v208\n"
      "v_mfma_f32_16x16x32_bf16 v[4:7], v[140:143], a[100:103], v[4:7]\n"
      "ds_read2st64_b32 v[116:117], v244 offset0:36 offset1:37\n"
      "v_mfma_f32_16x16x32_bf16 v[8:11], v[140:143], a[104:107], v[8:11]\n"
      "ds_read2st64_b32 v[118:119], v245 offset0:40 offset1:41\n"
      "v_mfma_f32_16x16x32_bf16 v[12:15], v[140:143], a[108:111], v[12:15]\n"
      "ds_read2st64_b32 v[120:121], v244 offset0:38 offset1:39\n"
      "v_mfma_f32_16x16x32_bf16 v[16:19], v[140:143], a[112:115], v[16:19]\n"
      "ds_read2st64_b32 v[122:123], v245 offset0:42 offset1:43\n"
      "v_mfma_f32_16x16x32_bf16 v[20:23], v[140:143], a[116:119], v[20:23]\n"
      "s_waitcnt lgkmcnt(4)\n"
      "v_mfma_f32_16x16x32_bf16 v[64:67], v[250:253], v[84:87], v[64:67]\n"
      "v_mfma_f32_16x16x32_bf16 v[24:27], v[140:143], a[120:123], v[24:27]\n"
      "v_mfma_f32_16x16x32_bf16 v[68:71], v[250:253], v[88:91], v[68:71]\n"
      "v_mfma_f32_16x16x32_bf16 v[28:31], v[140:143], a[124:127], v[28:31]\n"
      "s_waitcnt vmcnt(16)\n"
      "global_load_dwordx4 v[140:143], v240, s[74:75] offset:2048 sc1\n"
      "v_mfma_f32_16x16x32_bf16 v[0:3], v[144:147], a[128:131], v[0:3]\n"
      "v_mfma_f32_16x16x32_bf16 v[64:67], v[252:255], v[92:95], v[64:67]\n"
      "v_mfma_f32_16x16x32_bf16 v[4:7], v[144:147], a[132:135], v[4:7]\n"
      "v_mfma_f32_16x16x32_bf16 v[68:71], v[248:251], v[92:95], v[68:71]\n"
      "v_mov_b32 v75, s37\n"
      "v_mfma_f32_16x16x32_bf16 v[8:11], v[144:147], a[136:139], v[8:11]\n"
      "ds_read2st64_b32 v[124:125], v246 offset0:44 offset1:45\n"
      "v_mfma_f32_16x16x32_bf16 v[12:15], v[144:147], a[140:143], v[12:15]\n"
      "ds_read2st64_b32 v[126:127], v246 offset0:46 offset1:47\n"
      "s_nop 0\n"
      "v_cvt_pk_bf16_f32 v72, v64, v65\n"
      "v_mfma_f32_16x16x32_bf16 v[16:19], v[144:147], a[144:147], v[16:19]\n"
      "global_load_dwordx4 v[216:219], v208, s[16:17]\n"
      "v_cvt_pk_bf16_f32 v73, v66, v67\n"
      "v_mfma_f32_16x16x32_bf16 v[20:23], v[144:147], a[148:151], v[20:23]\n"
      "global_load_dwordx4 v[220:223], v210, s[16:17]\n"
      "v_cvt_pk_bf16_f32 v74, v68, v69\n"
      "v_mfma_f32_16x16x32_bf16 v[24:27], v[144:147], a[152:155], v[24:27]\n"
      "s_lshr_b32 s55, s34, 1\n"
      "v_cvt_pk_bf16_f32 v247, v70, v71\n"
      "s_lshl2_add_u32 s55, s55, s35\n"
      "v_mfma_f32_16x16x32_bf16 v[28:31], v[144:147], a[156:159], v[28:31]\n"
      "s_lshl_b32 s55, s55, 15\n"
      "global_load_dwordx4 v[144:147], v241, s[10:11] offset:2048 sc1\n"
      "s_lshl_b32 s54, s54, 9\n"
      "v_mfma_f32_16x16x32_bf16 v[0:3], v[148:151], a[160:163], v[0:3]\n"
      "s_add_u32 s54, s54, s55\n"
      "global_store_dwordx4 v242, v[72:75], s[52:53] offset:0 sc1\n"
      "s_waitcnt lgkmcnt(0)\n"
      "v_mfma_f32_16x16x32_bf16 v[4:7], v[148:151], a[164:167], v[4:7]\n"
      "s_add_u32 s54, s54, 786432\n"
      "v_mfma_f32_16x16x32_bf16 v[96:99], v[250:253], v[116:119], v[96:99]\n"
      "v_lshl_add_u32 v209, v209, 4, s54\n"
      "v_mfma_f32_16x16x32_bf16 v[8:11], v[148:151], a[168:171], v[8:11]\n"
      "v_mfma_f32_16x16x32_bf16 v[100:103], v[250:253], v[120:123], v[100:103]\n"
      "v_mfma_f32_16x16x32_bf16 v[12:15], v[148:151], a[172:175], v[12:15]\n"
      "v_mfma_f32_16x16x32_bf16 v[96:99], v[252:255], v[124:127], v[96:99]\n"
      "v_mfma_f32_16x16x32_bf16 v[16:19], v[148:151], a[176:179], v[16:19]\n"
      "v_mfma_f32_16x16x32_bf16 v[100:103], v[248:251], v[124:127], v[100:103]\n"
      "v_mov_b32 v107, s37\n"
      "v_mfma_f32_16x16x32_bf16 v[20:23], v[148:151], a[180:183], v[20:23]\n"
      "global_load_dwordx2 v[204:205], v209, s[58:59] offset:0 sc1\n"
      "v_mov_b32 v108, v247\n"
      "v_mfma_f32_16x16x32_bf16 v[24:27], v[148:151], a[184:187], v[24:27]\n"
      "global_load_dwordx2 v[206:207], v209, s[58:59] offset:256 sc1\n"
      "v_mov_b32 v111, s37\n"
      "v_mfma_f32_16x16x32_bf16 v[28:31], v[148:151], a[188:191], v[28:31]\n"
      "s_waitcnt vmcnt(21)\n"
      "global_load_dwordx4 v[148:151], v241, s[72:73] offset:2048 sc1\n"
      "s_add_u32 s68, s20, 16384\n"
      "s_addc_u32 s69, s21, 0\n"
      "v_mfma_f32_16x16x32_bf16 v[0:3], v[152:155], a[192:195], v[0:3]\n"
      "v_cvt_pk_bf16_f32 v104, v96, v97\n"
      "v_cvt_pk_bf16_f32 v105, v98, v99\n"
      "v_mfma_f32_16x16x32_bf16 v[4:7], v[152:155], a[196:199], v[4:7]\n"
      "v_cvt_pk_bf16_f32 v106, v100, v101\n"
      "v_cvt_pk_bf16_f32 v109, v102, v103\n"
      "v_mfma_f32_16x16x32_bf16 v[8:11], v[152:155], a[200:203], v[8:11]\n"
      "global_store_dwordx4 v242, v[104:107], s[52:53] offset:1024 sc1\n"
      "v_lshrrev_b32 v208, 10, v208\n"
      "v_mfma_f32_16x16x32_bf16 v[12:15], v[152:155], a[204:207], v[12:15]\n"
      "global_store_dwordx4 v242, v[108:111], s[52:53] offset:2048 sc1\n"
      "s_add_u32 s48, s34, 3\n"
      "v_mfma_f32_16x16x32_bf16 v[16:19], v[152:155], a[208:211], v[16:19]\n"
      "s_and_b32 s48, s48, 3\n"
      "s_mul_i32 s48, s48, 36864\n"
      "s_add_u32 s48, s48, 24576\n"
      "v_mfma_f32_16x16x32_bf16 v[20:23], v[152:155], a[212:215], v[20:23]\n"
      "s_add_u32 s52, s60, s48\n"
      "s_addc_u32 s53, s61, 0\n"
      "v_subrev_u32 v208, s42, v208\n"
      "v_mfma_f32_16x16x32_bf16 v[24:27], v[152:155], a[216:219], v[24:27]\n"
      "v_mfma_f32_16x16x32_bf16 v[28:31], v[152:155], a[220:223], v[28:31]\n"
      "global_load_dwordx4 v[152:155], v241, s[14:15] offset:2048 sc1\n"
      "v_mfma_f32_16x16x32_bf16 v[0:3], v[156:159], a[224:227], v[0:3]\n"
      "v_mfma_f32_16x16x32_bf16 v[4:7], v[156:159], a[228:231], v[4:7]\n"
      "v_mfma_f32_16x16x32_bf16 v[8:11], v[156:159], a[232:235], v[8:11]\n"
      "v_mfma_f32_16x16x32_bf16 v[12:15], v[156:159], a[236:239], v[12:15]\n"
      "v_mfma_f32_16x16x32_bf16 v[16:19], v[156:159], a[240:243], v[16:19]\n"
      "v_mfma_f32_16x16x32_bf16 v[20:23], v[156:159], a[244:247], v[20:23]\n"
      "v_mfma_f32_16x16x32_bf16 v[24:27], v[156:159], a[248:251], v[24:27]\n"
      "v_mfma_f32_16x16x32_bf16 v[28:31], v[156:159], a[252:255], v[28:31]\n"
      "s_waitcnt vmcnt(23)\n"
      "global_load_dwordx4 v[156:159], v241, s[74:75] offset:2048 sc1\n"
      "v_mfma_f32_16x16x32_bf16 v[32:35], v[160:163], a[0:3], 0\n"
      "v_mfma_f32_16x16x32_bf16 v[36:39], v[160:163], a[4:7], 0\n"
      "v_cvt_pk_bf16_f32 v8, v8, v9\n"
      "v_cvt_pk_bf16_f32 v9, v10, v11\n"
      "v_mfma_f32_16x16x32_bf16 v[40:43], v[160:163], a[8:11], 0\n"
      "ds_write_addtid_b32 v8 offset:0\n"
      "v_cvt_pk_bf16_f32 v10, v12, v13\n"
      "v_mfma_f32_16x16x32_bf16 v[44:47], v[160:163], a[12:15], 0\n"
      "ds_write_addtid_b32 v9 offset:256\n"
      "v_cvt_pk_bf16_f32 v11, v14, v15\n"
      "v_mfma_f32_16x16x32_bf16 v[48:51], v[160:163], a[16:19], 0\n"
      "ds_write_addtid_b32 v10 offset:512\n"
      "v_cvt_pk_bf16_f32 v12, v16, v17\n"
      "v_mfma_f32_16x16x32_bf16 v[52:55], v[160:163], a[20:23], 0\n"
      "ds_write_addtid_b32 v11 offset:768\n"
      "v_cvt_pk_bf16_f32 v13, v18, v19\n"
      "v_mfma_f32_16x16x32_bf16 v[56:59], v[160:163], a[24:27], 0\n"
      "ds_write_addtid_b32 v12 offset:1024\n"
      "v_cvt_pk_bf16_f32 v14, v20, v21\n"
      "v_mfma_f32_16x16x32_bf16 v[60:63], v[160:163], a[28:31], 0\n"
      "global_load_dwordx4 v[160:163], v240, s[10:11] offset:3072 sc1\n"
      "v_mfma_f32_16x16x32_bf16 v[32:35], v[164:167], a[32:35], v[32:35]\n"
      "ds_write_addtid_b32 v13 offset:1280\n"
      "v_cvt_pk_bf16_f32 v15, v22, v23\n"
      "v_mfma_f32_16x16x32_bf16 v[36:39], v[164:167], a[36:39], v[36:39]\n"
      "ds_write_addtid_b32 v14 offset:1536\n"
      "v_cvt_pk_bf16_f32 v16, v24, v25\n"
      "v_mfma_f32_16x16x32_bf16 v[40:43], v[164:167], a[40:43], v[40:43]\n"
      "ds_write_addtid_b32 v15 offset:1792\n"
      "v_cvt_pk_bf16_f32 v17, v26, v27\n"
      "v_mfma_f32_16x16x32_bf16 v[44:47], v[164:167], a[44:47], v[44:47]\n"
      "ds_write_addtid_b32 v16 offset:2048\n"
      "v_cvt_pk_bf16_f32 v18, v28, v29\n"
      "v_mfma_f32_16x16x32_bf16 v[48:51], v[164:167], a[48:51], v[48:51]\n"
      "ds_write_addtid_b32 v17 offset:2304\n"
      "v_cvt_pk_bf16_f32 v19, v30, v31\n"
      "v_mfma_f32_16x16x32_bf16 v[52:55], v[164:167], a[52:55], v[52:55]\n"
      "ds_write_addtid_b32 v18 offset:2560\n"
      "v_mfma_f32_16x16x32_bf16 v[56:59], v[164:167], a[56:59], v[56:59]\n"
      "ds_write_addtid_b32 v19 offset:2816\n"
      "v_mfma_f32_16x16x32_bf16 v[60:63], v[164:167], a[60:63], v[60:63]\n"
      "s_waitcnt vmcnt(22)\n"
      "global_load_dwordx4 v[164:167], v240, s[72:73] offset:3072 sc1\n"
      "v_mfma_f32_16x16x32_bf16 v[32:35], v[168:171], a[64:67], v[32:35]\n"
      "v_mfma_f32_16x16x32_bf16 v[36:39], v[168:171], a[68:71], v[36:39]\n"
      "s_waitcnt lgkmcnt(0)\n"
      "s_barrier\n"
      "v_mfma_f32_16x16x32_bf16 v[40:43], v[168:171], a[72:75], v[40:43]\n"
      "ds_read2st64_b32 v[20:21], v244 offset0:0 offset1:1\n"
      "v_mfma_f32_16x16x32_bf16 v[44:47], v[168:171], a[76:79], v[44:47]\n"
      "ds_read2st64_b32 v[22:23], v245 offset0:4 offset1:5\n"
      "v_mfma_f32_16x16x32_bf16 v[48:51], v[168:171], a[80:83], v[48:51]\n"
      "ds_read2st64_b32 v[24:25], v244 offset0:2 offset1:3\n"
      "v_mfma_f32_16x16x32_bf16 v[52:55], v[168:171], a[84:87], v[52:55]\n"
      "ds_read2st64_b32 v[26:27], v245 offset0:6 offset1:7\n"
      "v_mfma_f32_16x16x32_bf16 v[56:59], v[168:171], a[88:91], v[56:59]\n"
      "ds_read2st64_b32 v[28:29], v246 offset0:8 offset1:9\n"
      "v_mfma_f32_16x16x32_bf16 v[60:63], v[168:171], a[92:95], v[60:63]\n"
      "global_load_dwordx4 v[168:171], v240, s[14:15] offset:3072 sc1\n"
      "v_mfma_f32_16x16x32_bf16 v[32:35], v[172:175], a[96:99], v[32:35]\n"
      "ds_read2st64_b32 v[30:31], v246 offset0:10 offset1:11\n"
      "v_mfma_f32_16x16x32_bf16 v[36:39], v[172:175], a[100:103], v[36:39]\n"
      "v_mfma_f32_16x16x32_bf16 v[40:43], v[172:175], a[104:107], v[40:43]\n"
      "v_mfma_f32_16x16x32_bf16 v[44:47], v[172:175], a[108:111], v[44:47]\n"
      "v_mfma_f32_16x16x32_bf16 v[48:51], v[172:175], a[112:115], v[48:51]\n"
      "v_mfma_f32_16x16x32_bf16 v[52:55], v[172:175], a[116:119], v[52:55]\n"
      "s_waitcnt lgkmcnt(0)\n"
      "v_mfma_f32_16x16x32_bf16 v[0:3], v[250:253], v[20:23], v[0:3]\n"
      "v_mfma_f32_16x16x32_bf16 v[56:59], v[172:175], a[120:123], v[56:59]\n"
      "v_mfma_f32_16x16x32_bf16 v[4:7], v[250:253], v[24:27], v[4:7]\n"
      "v_mfma_f32_16x16x32_bf16 v[60:63], v[172:175], a[124:127], v[60:63]\n"
      "s_waitcnt vmcnt(20)\n"
      "global_load_dwordx4 v[172:175], v240, s[74:75] offset:3072 sc1\n"
      "v_mfma_f32_16x16x32_bf16 v[40:43], v[176:179], a[136:139], v[40:43]\n"
      "v_mfma_f32_16x16x32_bf16 v[0:3], v[252:255], v[28:31], v[0:3]\n"
      "v_mfma_f32_16x16x32_bf16 v[44:47], v[176:179], a[140:143], v[44:47]\n"
      "v_mfma_f32_16x16x32_bf16 v[4:7], v[248:251], v[28:31], v[4:7]\n"
      "v_mov_b32 v11, s37\n"
      "v_mfma_f32_16x16x32_bf16 v[48:51], v[176:179], a[144:147], v[48:51]\n"
      "v_mfma_f32_16x16x32_bf16 v[52:55], v[176:179], a[148:151], v[52:55]\n"
      "s_nop 2\n"
      "v_cvt_pk_bf16_f32 v8, v0, v1\n"
      "v_mfma_f32_16x16x32_bf16 v[56:59], v[176:179], a[152:155], v[56:59]\n"
      "v_cvt_pk_bf16_f32 v9, v2, v3\n"
      "v_cvt_pk_bf16_f32 v10, v4, v5\n"
      "v_mfma_f32_16x16x32_bf16 v[60:63], v[176:179], a[156:159], v[60:63]\n"
      "v_cvt_pk_bf16_f32 v247, v6, v7\n"
      "v_mfma_f32_16x16x32_bf16 v[40:43], v[180:183], a[168:171], v[40:43]\n"
      "global_store_dwordx4 v242, v[8:11], s[52:53] offset:0 sc1\n"
      "v_mfma_f32_16x16x32_bf16 v[44:47], v[180:183], a[172:175], v[44:47]\n"
      "s_waitcnt vmcnt(10)\n"
      "v_bitop3_b32 v210, v205, v207, s37 bitop3:0x7e\n"
      "v_cmp_ne_u32 vcc, 0, v210\n"
      "s_nop 1\n"
      "s_cbranch_vccnz .Lp_s13_%=\n"
      ".Lp_o14_%=:\n"
      "v_mfma_f32_16x16x32_bf16 v[48:51], v[180:183], a[176:179], v[48:51]\n"
      "v_mov_b32 v209, s39\n"
      "v_mfma_f32_16x16x32_bf16 v[52:55], v[180:183], a[180:183], v[52:55]\n"
      "v_cmp_eq_u32 vcc, 0, v208\n"
      "s_nop 1\n"
      "v_cndmask_b32 v204, v204, v209, vcc\n"
      "v_mfma_f32_16x16x32_bf16 v[56:59], v[180:183], a[184:187], v[56:59]\n"
      "v_cmp_eq_u32 vcc, -16, v208\n"
      "s_nop 1\n"
      "v_cndmask_b32 v206, v206, v209, vcc\n"
      "v_mfma_f32_16x16x32_bf16 v[60:63], v[180:183], a[188:191], v[60:63]\n"
      "v_sub_f32 v205, 1.0, v204\n"
      "v_cvt_pk_bf16_f32 v205, v205, v205\n"
      "v_mfma_f32_16x16x32_bf16 v[40:43], v[184:187], a[200:203], v[40:43]\n"
      "v_and_b32 v205, 0xffff0000, v205\n"
      "v_sub_f32 v207, 1.0, v206\n"
      "v_mfma_f32_16x16x32_bf16 v[44:47], v[184:187], a[204:207], v[44:47]\n"
      "v_cvt_pk_bf16_f32 v207, v207, v207\n"
      "v_and_b32 v207, 0xffff0000, v207\n"
      "v_lshlrev_b32 v208, 16, v216\n"
      "v_mfma_f32_16x16x32_bf16 v[48:51], v[184:187], a[208:211], v[48:51]\n"
      "v_and_b32 v209, 0xffff0000, v216\n"
      "v_mul_f32 v208, v205, v208\n"
      "v_mul_f32 v209, v205, v209\n"
      "v_mfma_f32_16x16x32_bf16 v[52:55], v[184:187], a[212:215], v[52:55]\n"
      "v_lshlrev_b32 v212, 16, v220\n"
      "v_and_b32 v213, 0xffff0000, v220\n"
      "v_mul_f32 v212, v207, v212\n"
      "v_mfma_f32_16x16x32_bf16 v[56:59], v[184:187], a[216:219], v[56:59]\n"
      "v_mul_f32 v213, v207, v213\n"
      "v_lshlrev_b32 v210, 16, v217\n"
      "v_and_b32 v211, 0xffff0000, v217\n"
      "v_mfma_f32_16x16x32_bf16 v[60:63], v[184:187], a[220:223], v[60:63]\n"
      "v_mul_f32 v210, v205, v210\n"
      "v_mul_f32 v211, v205, v211\n"
      "v_lshlrev_b32 v214, 16, v221\n"
      "v_mfma_f32_16x16x32_bf16 v[40:43], v[188:191], a[232:235], v[40:43]\n"
      "v_and_b32 v215, 0xffff0000, v221\n"
      "v_mul_f32 v214, v207, v214\n"
      "v_mul_f32 v215, v207, v215\n"
      "v_mfma_f32_16x16x32_bf16 v[44:47], v[188:191], a[236:239], v[44:47]\n"
      "v_lshlrev_b32 v216, 16, v218\n"
      "v_and_b32 v217, 0xffff0000, v218\n"
      "v_mul_f32 v216, v205, v216\n"
      "v_mfma_f32_16x16x32_bf16 v[48:51], v[188:191], a[240:243], v[48:51]\n"
      "v_mul_f32 v217, v205, v217\n"
      "v_lshlrev_b32 v220, 16, v222\n"
      "v_and_b32 v221, 0xffff0000, v222\n"
      "v_mfma_f32_16x16x32_bf16 v[52:55], v[188:191], a[244:247], v[52:55]\n"
      "v_mul_f32 v220, v207, v220\n"
      "v_mul_f32 v221, v207, v221\n"
      "v_lshlrev_b32 v218, 16, v219\n"
      "v_mfma_f32_16x16x32_bf16 v[56:59], v[188:191], a[248:251], v[56:59]\n"
      "v_and_b32 v219, 0xffff0000, v219\n"
      "v_mul_f32 v218, v205, v218\n"
      "v_mul_f32 v219, v205, v219\n"
      "v_mfma_f32_16x16x32_bf16 v[60:63], v[188:191], a[252:255], v[60:63]\n"
      "v_lshlrev_b32 v222, 16, v223\n"
      "v_and_b32 v223, 0xffff0000, v223\n"
      "v_mul_f32 v222, v207, v222\n"
      "v_mfma_f32_16x16x32_bf16 v[32:35], v[176:179], a[128:131], v[32:35]\n"
      "v_mul_f32 v223, v207, v223\n"
      "s_lshl2_add_u32 s54, s36, s33\n"
      "s_lshl_b32 s55, s54, 15\n"
      "v_mfma_f32_16x16x32_bf16 v[36:39], v[176:179], a[132:135], v[36:39]\n"
      "s_lshl2_add_u32 s56, s35, s34\n"
      "global_load_dwordx4 v[176:179], v241, s[10:11] offset:3072 sc1\n"
      "v_cvt_pk_bf16_f32 v40, v40, v41\n"
      "v_cvt_pk_bf16_f32 v41, v42, v43\n"
      "v_mfma_f32_16x16x32_bf16 v[32:35], v[180:183], a[160:163], v[32:35]\n"
      "s_lshl_b32 s56, s56, 6\n"
      "ds_write_addtid_b32 v40 offset:3072\n"
      "v_cvt_pk_bf16_f32 v42, v44, v45\n"
      "s_add_u32 s55, s55, s56\n"
      "v_mfma_f32_16x16x32_bf16 v[36:39], v[180:183], a[164:167], v[36:39]\n"
      "global_load_dwordx4 v[180:183], v241, s[72:73] offset:3072 sc1\n"
      "ds_write_addtid_b32 v41 offset:3328\n"
      "v_cvt_pk_bf16_f32 v43, v46, v47\n"
      "v_mfma_f32_16x16x32_bf16 v[32:35], v[184:187], a[192:195], v[32:35]\n"
      "ds_write_addtid_b32 v42 offset:3584\n"
      "v_cvt_pk_bf16_f32 v44, v48, v49\n"
      "v_and_b32 v8, 15, v243\n"
      "v_mfma_f32_16x16x32_bf16 v[36:39], v[184:187], a[196:199], v[36:39]\n"
      "global_load_dwordx4 v[184:187], v241, s[14:15] offset:3072 sc1\n"
      "ds_write_addtid_b32 v43 offset:3840\n"
      "v_cvt_pk_bf16_f32 v45, v50, v51\n"
      "v_mfma_f32_16x16x32_bf16 v[32:35], v[188:191], a[224:227], v[32:35]\n"
      "ds_write_addtid_b32 v44 offset:4096\n"
      "v_cvt_pk_bf16_f32 v46, v52, v53\n"
      "v_and_b32 v7, 48, v243\n"
      "v_mfma_f32_16x16x32_bf16 v[36:39], v[188:191], a[228:231], v[36:39]\n"
      "global_load_dwordx4 v[188:191], v241, s[74:75] offset:3072 sc1\n"
      "ds_write_addtid_b32 v45 offset:4352\n"
      "v_cvt_pk_bf16_f32 v47, v54, v55\n"
      "v_mfma_f32_16x16x32_bf16 v[72:75], v[128:131], a[8:11], 0\n"
      "ds_write_addtid_b32 v46 offset:4608\n"
      "v_cvt_pk_bf16_f32 v48, v56, v57\n"
      "v_lshl_or_b32 v7, v8, 10, v7\n"
      "v_mfma_f32_16x16x32_bf16 v[76:79], v[128:131], a[12:15], 0\n"
      "ds_write_addtid_b32 v47 offset:4864\n"
      "v_cvt_pk_bf16_f32 v49, v58, v59\n"
      "v_add_u32 v7, s55, v7\n"
      "v_mfma_f32_16x16x32_bf16 v[80:83], v[128:131], a[16:19], 0\n"
      "ds_write_addtid_b32 v48 offset:5120\n"
      "v_cvt_pk_bf16_f32 v50, v60, v61\n"
      "v_mfma_f32_16x16x32_bf16 v[84:87], v[128:131], a[20:23], 0\n"
      "ds_write_addtid_b32 v49 offset:5376\n"
      "v_cvt_pk_bf16_f32 v51, v62, v63\n"
      "v_mfma_f32_16x16x32_bf16 v[88:91], v[128:131], a[24:27], 0\n"
      "ds_write_addtid_b32 v50 offset:5632\n"
      "ds_write_addtid_b32 v51 offset:5888\n"
      "v_mfma_f32_16x16x32_bf16 v[92:95], v[128:131], a[28:31], 0\n"
      "v_mfma_f32_16x16x32_bf16 v[72:75], v[132:135], a[40:43], v[72:75]\n"
      "v_mfma_f32_16x16x32_bf16 v[76:79], v[132:135], a[44:47], v[76:79]\n"
      "s_waitcnt lgkmcnt(0)\n"
      "s_barrier\n"
      "v_mfma_f32_16x16x32_bf16 v[80:83], v[132:135], a[48:51], v[80:83]\n"
      "ds_read2st64_b32 v[52:53], v244 offset0:12 offset1:13\n"
      "ds_read2st64_b32 v[54:55], v245 offset0:16 offset1:17\n"
      "v_mfma_f32_16x16x32_bf16 v[84:87], v[132:135], a[52:55], v[84:87]\n"
      "ds_read2st64_b32 v[56:57], v244 offset0:14 offset1:15\n"
      "ds_read2st64_b32 v[58:59], v245 offset0:18 offset1:19\n"
      "v_mfma_f32_16x16x32_bf16 v[88:91], v[132:135], a[56:59], v[88:91]\n"
      "ds_read2st64_b32 v[60:61], v246 offset0:20 offset1:21\n"
      "ds_read2st64_b32 v[62:63], v246 offset0:22 offset1:23\n"
      "v_mfma_f32_16x16x32_bf16 v[92:95], v[132:135], a[60:63], v[92:95]\n"
      "v_mfma_f32_16x16x32_bf16 v[72:75], v[136:139], a[72:75], v[72:75]\n"
      "v_mfma_f32_16x16x32_bf16 v[76:79], v[136:139], a[76:79], v[76:79]\n"
      "v_mfma_f32_16x16x32_bf16 v[80:83], v[136:139], a[80:83], v[80:83]\n"
      "v_mfma_f32_16x16x32_bf16 v[84:87], v[136:139], a[84:87], v[84:87]\n"
      "s_waitcnt lgkmcnt(0)\n"
      "v_mfma_f32_16x16x32_bf16 v[32:35], v[250:253], v[52:55], v[32:35]\n"
      "v_mfma_f32_16x16x32_bf16 v[36:39], v[250:253], v[56:59], v[36:39]\n"
      "v_mfma_f32_16x16x32_bf16 v[88:91], v[136:139], a[88:91], v[88:91]\n"
      "v_mfma_f32_16x16x32_bf16 v[32:35], v[252:255], v[60:63], v[32:35]\n"
      "v_mfma_f32_16x16x32_bf16 v[36:39], v[248:251], v[60:63], v[36:39]\n"
      "v_add_u32 v4, 0, v242\n"
      "v_mfma_f32_16x16x32_bf16 v[92:95], v[136:139], a[92:95], v[92:95]\n"
      "global_load_dwordx4 v[12:15], v4, s[62:63] offset:0 sc1\n"
      "global_load_dwordx4 v[16:19], v4, s[62:63] offset:1024 sc1\n"
      "v_mov_b32 v43, s37\n"
      "v_mfma_f32_16x16x32_bf16 v[72:75], v[140:143], a[104:107], v[72:75]\n"
      "global_load_dwordx4 v[20:23], v4, s[62:63] offset:2048 sc1\n"
      "v_mov_b32 v44, v247\n"
      "v_mov_b32 v47, s37\n"
      "v_mfma_f32_16x16x32_bf16 v[76:79], v[140:143], a[108:111], v[76:79]\n"
      "v_mfma_f32_16x16x32_bf16 v[80:83], v[140:143], a[112:115], v[80:83]\n"
      "v_cvt_pk_bf16_f32 v40, v32, v33\n"
      "v_cvt_pk_bf16_f32 v41, v34, v35\n"
      "v_cvt_pk_bf16_f32 v42, v36, v37\n"
      "v_mfma_f32_16x16x32_bf16 v[84:87], v[140:143], a[116:119], v[84:87]\n"
      "v_cvt_pk_bf16_f32 v45, v38, v39\n"
      "v_mfma_f32_16x16x32_bf16 v[88:91], v[140:143], a[120:123], v[88:91]\n"
      "global_store_dwordx4 v242, v[40:43], s[52:53] offset:1024 sc1\n"
      "global_store_dwordx4 v242, v[44:47], s[52:53] offset:2048 sc1\n"
      "v_mfma_f32_16x16x32_bf16 v[92:95], v[140:143], a[124:127], v[92:95]\n"
      "s_waitcnt vmcnt(18)\n"
      "v_mfma_f32_16x16x32_bf16 v[72:75], v[144:147], a[136:139], v[72:75]\n"
      "v_mfma_f32_16x16x32_bf16 v[76:79], v[144:147], a[140:143], v[76:79]\n"
      "v_mfma_f32_16x16x32_bf16 v[80:83], v[144:147], a[144:147], v[80:83]\n"
      "v_mfma_f32_16x16x32_bf16 v[84:87], v[144:147], a[148:151], v[84:87]\n"
      "v_mfma_f32_16x16x32_bf16 v[88:91], v[144:147], a[152:155], v[88:91]\n"
      "v_mfma_f32_16x16x32_bf16 v[92:95], v[144:147], a[156:159], v[92:95]\n"
      "v_mfma_f32_16x16x32_bf16 v[72:75], v[148:151], a[168:171], v[72:75]\n"
      "v_mfma_f32_16x16x32_bf16 v[76:79], v[148:151], a[172:175], v[76:79]\n"
      "v_mfma_f32_16x16x32_bf16 v[80:83], v[148:151], a[176:179], v[80:83]\n"
      "v_mfma_f32_16x16x32_bf16 v[84:87], v[148:151], a[180:183], v[84:87]\n"
      "v_mfma_f32_16x16x32_bf16 v[88:91], v[148:151], a[184:187], v[88:91]\n"
      "v_mfma_f32_16x16x32_bf16 v[92:95], v[148:151], a[188:191], v[92:95]\n"
      "s_waitcnt vmcnt(14)\n"
      "v_mfma_f32_16x16x32_bf16 v[72:75], v[152:155], a[200:203], v[72:75]\n"
      "v_mfma_f32_16x16x32_bf16 v[76:79], v[152:155], a[204:207], v[76:79]\n"
      "v_mfma_f32_16x16x32_bf16 v[80:83], v[152:155], a[208:211], v[80:83]\n"
      "v_mfma_f32_16x16x32_bf16 v[84:87], v[152:155], a[212:215], v[84:87]\n"
      "v_mfma_f32_16x16x32_bf16 v[88:91], v[152:155], a[216:219], v[88:91]\n"
      "v_mfma_f32_16x16x32_bf16 v[92:95], v[152:155], a[220:223], v[92:95]\n"
      "v_mfma_f32_16x16x32_bf16 v[72:75], v[156:159], a[232:235], v[72:75]\n"
      "v_mfma_f32_16x16x32_bf16 v[76:79], v[156:159], a[236:239], v[76:79]\n"
      "v_mfma_f32_16x16x32_bf16 v[80:83], v[156:159], a[240:243], v[80:83]\n"
      "v_mfma_f32_16x16x32_bf16 v[84:87], v[156:159], a[244:247], v[84:87]\n"
      "v_mfma_f32_16x16x32_bf16 v[88:91], v[156:159], a[248:251], v[88:91]\n"
      "v_mfma_f32_16x16x32_bf16 v[92:95], v[156:159], a[252:255], v[92:95]\n"
      "s_waitcnt vmcnt(12)\n"
      "v_mfma_f32_16x16x32_bf16 v[104:107], v[160:163], a[8:11], 0\n"
      "v_mfma_f32_16x16x32_bf16 v[108:111], v[160:163], a[12:15], 0\n"
      "v_cvt_pk_bf16_f32 v72, v72, v73\n"
      "v_cvt_pk_bf16_f32 v73, v74, v75\n"
      "v_mfma_f32_16x16x32_bf16 v[112:115], v[160:163], a[16:19], 0\n"
      "ds_write_addtid_b32 v72 offset:6144\n"
      "v_cvt_pk_bf16_f32 v74, v76, v77\n"
      "v_mfma_f32_16x16x32_bf16 v[116:119], v[160:163], a[20:23], 0\n"
      "ds_write_addtid_b32 v73 offset:6400\n"
      "v_cvt_pk_bf16_f32 v75, v78, v79\n"
      "v_mfma_f32_16x16x32_bf16 v[120:123], v[160:163], a[24:27], 0\n"
      "ds_write_addtid_b32 v74 offset:6656\n"
      "v_cvt_pk_bf16_f32 v76, v80, v81\n"
      "v_mfma_f32_16x16x32_bf16 v[124:127], v[160:163], a[28:31], 0\n"
      "ds_write_addtid_b32 v75 offset:6912\n"
      "v_cvt_pk_bf16_f32 v77, v82, v83\n"
      "v_mfma_f32_16x16x32_bf16 v[104:107], v[164:167], a[40:43], v[104:107]\n"
      "ds_write_addtid_b32 v76 offset:7168\n"
      "v_cvt_pk_bf16_f32 v78, v84, v85\n"
      "v_mfma_f32_16x16x32_bf16 v[108:111], v[164:167], a[44:47], v[108:111]\n"
      "ds_write_addtid_b32 v77 offset:7424\n"
      "v_cvt_pk_bf16_f32 v79, v86, v87\n"
      "v_mfma_f32_16x16x32_bf16 v[112:115], v[164:167], a[48:51], v[112:115]\n"
      "ds_write_addtid_b32 v78 offset:7680\n"
      "v_cvt_pk_bf16_f32 v80, v88, v89\n"
      "v_mfma_f32_16x16x32_bf16 v[116:119], v[164:167], a[52:55], v[116:119]\n"
      "ds_write_addtid_b32 v79 offset:7936\n"
      "v_cvt_pk_bf16_f32 v81, v90, v91\n"
      "v_mfma_f32_16x16x32_bf16 v[120:123], v[164:167], a[56:59], v[120:123]\n"
      "ds_write_addtid_b32 v80 offset:8192\n"
      "v_cvt_pk_bf16_f32 v82, v92, v93\n"
      "v_mfma_f32_16x16x32_bf16 v[124:127], v[164:167], a[60:63], v[124:127]\n"
      "s_waitcnt vmcnt(10)\n"
      "ds_write_addtid_b32 v81 offset:8448\n"
      "v_cvt_pk_bf16_f32 v83, v94, v95\n"
      "v_mfma_f32_16x16x32_bf16 v[104:107], v[168:171], a[72:75], v[104:107]\n"
      "ds_write_addtid_b32 v82 offset:8704\n"
      "v_mfma_f32_16x16x32_bf16 v[108:111], v[168:171], a[76:79], v[108:111]\n"
      "ds_write_addtid_b32 v83 offset:8960\n"
      "v_mfma_f32_16x16x32_bf16 v[112:115], v[168:171], a[80:83], v[112:115]\n"
      "s_waitcnt vmcnt(2)\n"
      "v_bitop3_b32 v22, v15, v19, s37 bitop3:0x7e\n"
      "v_bitop3_b32 v22, v22, v23, s37 bitop3:0xf6\n"
      "v_cmp_ne_u32 vcc, 0, v22\n"
      "s_nop 1\n"
      "s_cbranch_vccnz .Lp_s16_%=\n"
      ".Lp_o17_%=:\n"
      "v_mfma_f32_16x16x32_bf16 v[116:119], v[168:171], a[84:87], v[116:119]\n"
      "v_mov_b32 v15, v20\n"
      "v_mov_b32 v19, v21\n"
      "v_mfma_f32_16x16x32_bf16 v[120:123], v[168:171], a[88:91], v[120:123]\n"
      "s_waitcnt lgkmcnt(0)\n"
      "s_barrier\n"
      "v_mfma_f32_16x16x32_bf16 v[124:127], v[168:171], a[92:95], v[124:127]\n"
      "ds_read2st64_b32 v[84:85], v244 offset0:24 offset1:25\n"
      "v_mfma_f32_16x16x32_bf16 v[104:107], v[172:175], a[104:107], v[104:107]\n"
      "ds_read2st64_b32 v[86:87], v245 offset0:28 offset1:29\n"
      "v_add_u32 v5, 12288, v242\n"
      "v_mfma_f32_16x16x32_bf16 v[108:111], v[172:175], a[108:111], v[108:111]\n"
      "global_load_dwordx4 v[44:47], v5, s[62:63] offset:0 sc1\n"
      "v_mfma_f32_16x16x32_bf16 v[112:115], v[172:175], a[112:115], v[112:115]\n"
      "global_load_dwordx4 v[48:51], v5, s[62:63] offset:1024 sc1\n"
      "v_mfma_f32_16x16x32_bf16 v[116:119], v[172:175], a[116:119], v[116:119]\n"
      "global_load_dwordx4 v[52:55], v5, s[62:63] offset:2048 sc1\n"
      "v_mfma_f32_16x16x32_bf16 v[120:123], v[172:175], a[120:123], v[120:123]\n"
      "ds_read2st64_b32 v[88:89], v244 offset0:26 offset1:27\n"
      "v_mfma_f32_16x16x32_bf16 v[124:127], v[172:175], a[124:127], v[124:127]\n"
      "ds_read2st64_b32 v[90:91], v245 offset0:30 offset1:31\n"
      "v_mfma_f32_16x16x32_bf16 v[104:107], v[176:179], a[136:139], v[104:107]\n"
      "ds_read2st64_b32 v[92:93], v246 offset0:32 offset1:33\n"
      "v_mfma_f32_16x16x32_bf16 v[108:111], v[176:179], a[140:143], v[108:111]\n"
      "ds_read2st64_b32 v[94:95], v246 offset0:34 offset1:35\n"
      "v_mfma_f32_16x16x32_bf16 v[112:115], v[176:179], a[144:147], v[112:115]\n"
      "v_mfma_f32_16x16x32_bf16 v[64:67], v[252:255], v[12:15], 0\n"
      "v_mfma_f32_16x16x32_bf16 v[116:119], v[176:179], a[148:151], v[116:119]\n"
      "v_mfma_f32_16x16x32_bf16 v[68:71], v[248:251], v[12:15], 0\n"
      "v_mfma_f32_16x16x32_bf16 v[120:123], v[176:179], a[152:155], v[120:123]\n"
      "v_mfma_f32_16x16x32_bf16 v[96:99], v[252:255], v[16:19], 0\n"
      "v_mfma_f32_16x16x32_bf16 v[124:127], v[176:179], a[156:159], v[124:127]\n"
      "v_mfma_f32_16x16x32_bf16 v[100:103], v[248:251], v[16:19], 0\n"
      "v_mfma_f32_16x16x32_bf16 v[104:107], v[180:183], a[168:171], v[104:107]\n"
      "s_waitcnt lgkmcnt(0)\n"
      "v_mfma_f32_16x16x32_bf16 v[64:67], v[250:253], v[84:87], v[64:67]\n"
      "v_mfma_f32_16x16x32_bf16 v[108:111], v[180:183], a[172:175], v[108:111]\n"
      "v_mfma_f32_16x16x32_bf16 v[68:71], v[250:253], v[88:91], v[68:71]\n"
      "v_mfma_f32_16x16x32_bf16 v[112:115], v[180:183], a[176:179], v[112:115]\n"
      "v_mfma_f32_16x16x32_bf16 v[64:67], v[252:255], v[92:95], v[64:67]\n"
      "v_mfma_f32_16x16x32_bf16 v[116:119], v[180:183], a[180:183], v[116:119]\n"
      "v_mfma_f32_16x16x32_bf16 v[68:71], v[248:251], v[92:95], v[68:71]\n"
      "v_mfma_f32_16x16x32_bf16 v[120:123], v[180:183], a[184:187], v[120:123]\n"
      "v_mfma_f32_16x16x32_bf16 v[124:127], v[180:183], a[188:191], v[124:127]\n"
      "v_mfma_f32_16x16x32_bf16 v[104:107], v[184:187], a[200:203], v[104:107]\n"
      "v_mfma_f32_16x16x32_bf16 v[108:111], v[184:187], a[204:207], v[108:111]\n"
      "v_mfma_f32_16x16x32_bf16 v[112:115], v[184:187], a[208:211], v[112:115]\n"
      "v_mfma_f32_16x16x32_bf16 v[116:119], v[184:187], a[212:215], v[116:119]\n"
      "v_mfma_f32_16x16x32_bf16 v[120:123], v[184:187], a[216:219], v[120:123]\n"
      "v_mfma_f32_16x16x32_bf16 v[124:127], v[184:187], a[220:223], v[124:127]\n"
      "v_mfma_f32_16x16x32_bf16 v[104:107], v[188:191], a[232:235], v[104:107]\n"
      "v_mfma_f32_16x16x32_bf16 v[108:111], v[188:191], a[236:239], v[108:111]\n"
      "s_lshr_b32 s43, s42, 7\n"
      "s_cmp_eq_u32 s43, s36\n"
      "s_cbranch_scc0 .Lp_e18_%=\n"
      "s_lshl2_add_u32 s44, s35, s34\n"
      "s_lshl2_add_u32 s43, s44, s33\n"
      "s_lshl_b32 s43, s43, 12\n"
      "s_add_u32 s43, s43, 524288\n"
      "v_add_u32 v60, s43, v242\n"
      "global_load_dwordx4 v[24:27], v60, s[58:59] offset:0 sc1\n"
      "global_load_dwordx4 v[28:31], v60, s[58:59] offset:1024 sc1\n"
      "global_load_dwordx4 v[32:35], v60, s[58:59] offset:2048 sc1\n"
      "global_load_dwordx4 v[36:39], v60, s[58:59] offset:3072 sc1\n"
      "v_and_b32 v58, 15, v243\n"
      "v_lshrrev_b32 v59, 5, v243\n"
      "v_lshl_or_b32 v58, v59, 4, v58\n"
      "s_lshl4_add_u32 s44, s42, s44\n"
      "s_lshl_b32 s44, s44, 6\n"
      "v_lshl_add_u32 v59, v58, 1, s44\n"
      "global_load_ushort v57, v59, s[16:17]\n"
      "s_mov_b32 s44, 131072\n"
      "v_lshl_add_u32 v58, v58, 2, s44\n"
      "s_lshl_b32 s43, s33, 13\n"
      "v_add_u32 v59, s43, v58\n"
      ".Lp_e18_%=:\n"
      "v_mfma_f32_16x16x32_bf16 v[112:115], v[188:191], a[240:243], v[112:115]\n"
      "v_mfma_f32_16x16x32_bf16 v[116:119], v[188:191], a[244:247], v[116:119]\n"
      "v_mfma_f32_16x16x32_bf16 v[120:123], v[188:191], a[248:251], v[120:123]\n"
      "v_mfma_f32_16x16x32_bf16 v[124:127], v[188:191], a[252:255], v[124:127]\n"
      "v_mfma_f32_16x16x32_bf16 v[64:67], v[128:131], a[0:3], v[64:67]\n"
      "v_mfma_f32_16x16x32_bf16 v[68:71], v[128:131], a[4:7], v[68:71]\n"
      "v_cvt_pk_bf16_f32 v104, v104, v105\n"
      "v_cvt_pk_bf16_f32 v105, v106, v107\n"
      "v_mfma_f32_16x16x32_bf16 v[64:67], v[132:135], a[32:35], v[64:67]\n"
      "ds_write_addtid_b32 v104 offset:9216\n"
      "v_cvt_pk_bf16_f32 v106, v108, v109\n"
      "v_mfma_f32_16x16x32_bf16 v[68:71], v[132:135], a[36:39], v[68:71]\n"
      "ds_write_addtid_b32 v105 offset:9472\n"
      "v_cvt_pk_bf16_f32 v107, v110, v111\n"
      "v_mfma_f32_16x16x32_bf16 v[64:67], v[136:139], a[64:67], v[64:67]\n"
      "ds_write_addtid_b32 v106 offset:9728\n"
      "v_cvt_pk_bf16_f32 v108, v112, v113\n"
      "v_mfma_f32_16x16x32_bf16 v[68:71], v[136:139], a[68:71], v[68:71]\n"
      "ds_write_addtid_b32 v107 offset:9984\n"
      "v_cvt_pk_bf16_f32 v109, v114, v115\n"
      "v_mfma_f32_16x16x32_bf16 v[64:67], v[140:143], a[96:99], v[64:67]\n"
      "ds_write_addtid_b32 v108 offset:10240\n"
      "v_cvt_pk_bf16_f32 v110, v116, v117\n"
      "v_mfma_f32_16x16x32_bf16 v[68:71], v[140:143], a[100:103], v[68:71]\n"
      "ds_write_addtid_b32 v109 offset:10496\n"
      "v_cvt_pk_bf16_f32 v111, v118, v119\n"
      "v_mfma_f32_16x16x32_bf16 v[64:67], v[144:147], a[128:131], v[64:67]\n"
      "ds_write_addtid_b32 v110 offset:10752\n"
      "v_cvt_pk_bf16_f32 v112, v120, v121\n"
      "v_mfma_f32_16x16x32_bf16 v[68:71], v[144:147], a[132:135], v[68:71]\n"
      "ds_write_addtid_b32 v111 offset:11008\n"
      "v_cvt_pk_bf16_f32 v113, v122, v123\n"
      "v_mfma_f32_16x16x32_bf16 v[64:67], v[148:151], a[160:163], v[64:67]\n"
      "ds_write_addtid_b32 v112 offset:11264\n"
      "v_add_u32 v6, 24576, v242\n"
      "v_cvt_pk_bf16_f32 v114, v124, v125\n"
      "v_mfma_f32_16x16x32_bf16 v[68:71], v[148:151], a[164:167], v[68:71]\n"
      "global_load_dwordx4 v[224:227], v6, s[62:63] offset:0 sc1\n"
      "global_load_dwordx4 v[228:231], v6, s[62:63] offset:1024 sc1\n"
      "v_mfma_f32_16x16x32_bf16 v[64:67], v[152:155], a[192:195], v[64:67]\n"
      "global_load_dwordx4 v[232:235], v6, s[62:63] offset:2048 sc1\n"
      "ds_write_addtid_b32 v113 offset:11520\n"
      "v_cvt_pk_bf16_f32 v115, v126, v127\n"
      "v_mfma_f32_16x16x32_bf16 v[68:71], v[152:155], a[196:199], v[68:71]\n"
      "ds_write_addtid_b32 v114 offset:11776\n"
      "ds_write_addtid_b32 v115 offset:12032\n"
      "v_mfma_f32_16x16x32_bf16 v[64:67], v[156:159], a[224:227], v[64:67]\n"
      "s_waitcnt vmcnt(3)\n"
      "v_bitop3_b32 v54, v47, v51, s37 bitop3:0x7e\n"
      "v_bitop3_b32 v54, v54, v55, s37 bitop3:0xf6\n"
      "v_cmp_ne_u32 vcc, 0, v54\n"
      "s_nop 1\n"
      "s_cbranch_vccnz .Lp_s20_%=\n"
      ".Lp_o21_%=:\n"
      "v_mfma_f32_16x16x32_bf16 v[68:71], v[156:159], a[228:231], v[68:71]\n"
      "v_mov_b32 v47, v52\n"
      "v_mov_b32 v51, v53\n"
      "v_mfma_f32_16x16x32_bf16 v[96:99], v[160:163], a[0:3], v[96:99]\n"
      "s_lshr_b32 s43, s42, 7\n"
      "s_cmp_eq_u32 s43, s36\n"
      "s_cbranch_scc0 .Lp_e22_%=\n"
      "s_waitcnt vmcnt(3)\n"
      "v_bitop3_b32 v61, v25, v27, s37 bitop3:0x7e\n"
      "v_bitop3_b32 v61, v61, v29, s37 bitop3:0xf6\n"
      "v_bitop3_b32 v61, v61, v31, s37 bitop3:0xf6\n"
      "v_bitop3_b32 v61, v61, v33, s37 bitop3:0xf6\n"
      "v_bitop3_b32 v61, v61, v35, s37 bitop3:0xf6\n"
      "v_bitop3_b32 v61, v61, v37, s37 bitop3:0xf6\n"
      "v_bitop3_b32 v61, v61, v39, s37 bitop3:0xf6\n"
      "v_cmp_ne_u32 vcc, 0, v61\n"
      "s_nop 1\n"
      "s_cbranch_vccnz .Lp_s24_%=\n"
      ".Lp_o25_%=:\n"
      "v_add_f32 v24, v24, v28\n"
      "v_add_f32 v26, v26, v30\n"
      "v_add_f32 v32, v32, v36\n"
      "v_add_f32 v34, v34, v38\n"
      "v_add_f32 v24, v24, v32\n"
      "v_add_f32 v26, v26, v34\n"
      "s_nop 1\n"
      "v_permlane32_swap_b32 v24, v26\n"
      "v_add_f32 v24, v24, v26\n"
      "v_mov_b32 v26, v24\n"
      "s_nop 1\n"
      "v_permlane16_swap_b32 v24, v26\n"
      "v_add_f32 v24, v24, v26\n"
      "ds_write_b32 v59, v24\n"
      "s_waitcnt lgkmcnt(0)\n"
      ".Lp_e22_%=:\n"
      "v_mfma_f32_16x16x32_bf16 v[100:103], v[160:163], a[4:7], v[100:103]\n"
      "s_waitcnt lgkmcnt(0)\n"
      "s_barrier\n"
      "v_mfma_f32_16x16x32_bf16 v[96:99], v[164:167], a[32:35], v[96:99]\n"
      "s_lshr_b32 s43, s42, 5\n"
      "s_lshl2_add_u32 s44, s36, s33\n"
      "s_cmp_eq_u32 s43, s44\n"
      "s_cbranch_scc0 .Lp_e26_%=\n"
      "ds_read_b32 v24, v58 offset:0\n"
      "ds_read_b32 v25, v58 offset:8192\n"
      "ds_read_b32 v26, v58 offset:16384\n"
      "ds_read_b32 v27, v58 offset:24576\n"
      "v_mov_b32 v62, 0\n"
      "v_mov_b32 v63, s37\n"
      "s_or_b32 s43, s34, s35\n"
      "s_cmp_eq_u32 s43, 0\n"
      "s_cbranch_scc0 .Lp_z27_%=\n"
      "s_mov_b64 exec, 1\n"
      "global_store_dword v62, v63, s[58:59] sc1\n"
      ".Lp_z27_%=:\n"
      "s_mov_b64 exec, -1\n"
      "s_waitcnt vmcnt(3)\n"
      "v_lshlrev_b32 v57, 16, v57\n"
      "v_and_b32 v56, 48, v243\n"
      "s_mov_b32 s44, 131072\n"
      "v_lshl_add_u32 v56, v56, 1, s44\n"
      "s_waitcnt lgkmcnt(0)\n"
      "v_add_f32 v24, v24, v25\n"
      "s_waitcnt lgkmcnt(0)\n"
      "v_add_f32 v26, v26, v27\n"
      "v_add_f32 v24, v24, v26\n"
      "v_cvt_pk_bf16_f32 v24, v24, v24\n"
      "v_and_b32 v24, 0xffff0000, v24\n"
      "v_mul_f32 v24, s84, v24\n"
      "v_cvt_pk_bf16_f32 v24, v24, v24\n"
      "v_and_b32 v24, 0xffff0000, v24\n"
      "v_add_f32 v24, v24, v57\n"
      "v_cvt_pk_bf16_f32 v24, v24, v24\n"
      "v_and_b32 v24, 0xffff0000, v24\n"
      "v_sub_f32 v24, v24, v57\n"
      "ds_write_b32 v58, v24 offset:1024\n"
      "ds_read_b128 v[28:31], v56 offset:1024\n"
      "ds_read_b128 v[32:35], v56 offset:1040\n"
      ".Lp_e26_%=:\n"
      "v_mfma_f32_16x16x32_bf16 v[100:103], v[164:167], a[36:39], v[100:103]\n"
      "ds_read2st64_b32 v[116:117], v244 offset0:36 offset1:37\n"
      "ds_read2st64_b32 v[118:119], v245 offset0:40 offset1:41\n"
      "v_mfma_f32_16x16x32_bf16 v[96:99], v[168:171], a[64:67], v[96:99]\n"
      "ds_read2st64_b32 v[120:121], v244 offset0:38 offset1:39\n"
      "ds_read2st64_b32 v[122:123], v245 offset0:42 offset1:43\n"
      "v_mfma_f32_16x16x32_bf16 v[100:103], v[168:171], a[68:71], v[100:103]\n"
      "ds_read2st64_b32 v[124:125], v246 offset0:44 offset1:45\n"
      "ds_read2st64_b32 v[126:127], v246 offset0:46 offset1:47\n"
      "v_mfma_f32_16x16x32_bf16 v[96:99], v[172:175], a[96:99], v[96:99]\n"
      "v_mfma_f32_16x16x32_bf16 v[64:67], v[252:255], v[44:47], v[64:67]\n"
      "v_mfma_f32_16x16x32_bf16 v[68:71], v[248:251], v[44:47], v[68:71]\n"
      "v_mfma_f32_16x16x32_bf16 v[100:103], v[172:175], a[100:103], v[100:103]\n"
      "v_mfma_f32_16x16x32_bf16 v[96:99], v[252:255], v[48:51], v[96:99]\n"
      "v_mfma_f32_16x16x32_bf16 v[100:103], v[248:251], v[48:51], v[100:103]\n"
      "v_mfma_f32_16x16x32_bf16 v[96:99], v[176:179], a[128:131], v[96:99]\n"
      "v_mfma_f32_16x16x32_bf16 v[100:103], v[176:179], a[132:135], v[100:103]\n"
      "v_mfma_f32_16x16x32_bf16 v[96:99], v[180:183], a[160:163], v[96:99]\n"
      "s_waitcnt lgkmcnt(0)\n"
      "v_mfma_f32_16x16x32_bf16 v[96:99], v[250:253], v[116:119], v[96:99]\n"
      "v_mfma_f32_16x16x32_bf16 v[100:103], v[250:253], v[120:123], v[100:103]\n"
      "v_mfma_f32_16x16x32_bf16 v[100:103], v[180:183], a[164:167], v[100:103]\n"
      "v_mfma_f32_16x16x32_bf16 v[96:99], v[252:255], v[124:127], v[96:99]\n"
      "v_mfma_f32_16x16x32_bf16 v[100:103], v[248:251], v[124:127], v[100:103]\n"
      "v_mfma_f32_16x16x32_bf16 v[96:99], v[184:187], a[192:195], v[96:99]\n"
      "v_mfma_f32_16x16x32_bf16 v[100:103], v[184:187], a[196:199], v[100:103]\n"
      "v_mfma_f32_16x16x32_bf16 v[96:99], v[188:191], a[224:227], v[96:99]\n"
      "v_mfma_f32_16x16x32_bf16 v[100:103], v[188:191], a[228:231], v[100:103]\n"
      "s_lshr_b32 s43, s42, 5\n"
      "s_lshl2_add_u32 s44, s36, s33\n"
      "s_cmp_eq_u32 s43, s44\n"
      "s_cbranch_scc0 .Lp_e28_%=\n"
      "v_lshrrev_b32 v24, 10, v7\n"
      "v_subrev_u32 v24, s42, v24\n"
      "v_cmp_eq_u32 vcc, 0, v24\n"
      "s_nop 1\n"
      "s_mov_b64 exec, vcc\n"
      "v_fma_f32 v208, v205, v28, v208\n"
      "v_fma_f32 v209, v205, v29, v209\n"
      "v_fma_f32 v210, v205, v30, v210\n"
      "v_fma_f32 v211, v205, v31, v211\n"
      "v_fma_f32 v216, v205, v32, v216\n"
      "v_fma_f32 v217, v205, v33, v217\n"
      "v_fma_f32 v218, v205, v34, v218\n"
      "v_fma_f32 v219, v205, v35, v219\n"
      "s_mov_b64 exec, -1\n"
      "v_cmp_eq_u32 vcc, -16, v24\n"
      "s_nop 1\n"
      "s_mov_b64 exec, vcc\n"
      "v_fma_f32 v212, v207, v28, v212\n"
      "v_fma_f32 v213, v207, v29, v213\n"
      "v_fma_f32 v214, v207, v30, v214\n"
      "v_fma_f32 v215, v207, v31, v215\n"
      "v_fma_f32 v220, v207, v32, v220\n"
      "v_fma_f32 v221, v207, v33, v221\n"
      "v_fma_f32 v222, v207, v34, v222\n"
      "v_fma_f32 v223, v207, v35, v223\n"
      "s_mov_b64 exec, -1\n"
      ".Lp_e28_%=:\n"
      "s_waitcnt vmcnt(0)\n"
      "v_bitop3_b32 v234, v227, v231, s37 bitop3:0x7e\n"
      "v_bitop3_b32 v234, v234, v235, s37 bitop3:0xf6\n"
      "v_cmp_ne_u32 vcc, 0, v234\n"
      "s_nop 1\n"
      "s_cbranch_vccnz .Lp_s30_%=\n"
      ".Lp_o31_%=:\n"
      "v_mov_b32 v227, v232\n"
      "v_mov_b32 v231, v233\n"
      "s_nop 0\n"
      "v_mfma_f32_16x16x32_bf16 v[64:67], v[252:255], v[224:227], v[64:67]\n"
      "v_mfma_f32_16x16x32_bf16 v[96:99], v[252:255], v[228:231], v[96:99]\n"
      "v_mfma_f32_16x16x32_bf16 v[68:71], v[248:251], v[224:227], v[68:71]\n"
      "v_mfma_f32_16x16x32_bf16 v[100:103], v[248:251], v[228:231], v[100:103]\n"
      "s_nop 4\n"
      "v_fma_f32 v208, v204, v64, v208\n"
      "v_fma_f32 v209, v204, v65, v209\n"
      "v_fma_f32 v210, v204, v66, v210\n"
      "v_fma_f32 v211, v204, v67, v211\n"
      "v_fma_f32 v216, v204, v96, v216\n"
      "v_fma_f32 v217, v204, v97, v217\n"
      "v_fma_f32 v218, v204, v98, v218\n"
      "v_fma_f32 v219, v204, v99, v219\n"
      "v_cvt_pk_bf16_f32 v72, v208, v209\n"
      "v_cvt_pk_bf16_f32 v73, v210, v211\n"
      "v_cvt_pk_bf16_f32 v74, v216, v217\n"
      "v_cvt_pk_bf16_f32 v75, v218, v219\n"
      "global_store_dwordx4 v7, v[72:75], s[20:21]\n"
      "v_fma_f32 v212, v206, v68, v212\n"
      "v_fma_f32 v213, v206, v69, v213\n"
      "v_fma_f32 v214, v206, v70, v214\n"
      "v_fma_f32 v215, v206, v71, v215\n"
      "v_fma_f32 v220, v206, v100, v220\n"
      "v_fma_f32 v221, v206, v101, v221\n"
      "v_fma_f32 v222, v206, v102, v222\n"
      "v_fma_f32 v223, v206, v103, v223\n"
      "v_cvt_pk_bf16_f32 v76, v212, v213\n"
      "v_cvt_pk_bf16_f32 v77, v214, v215\n"
      "v_cvt_pk_bf16_f32 v78, v220, v221\n"
      "v_cvt_pk_bf16_f32 v79, v222, v223\n"
      "global_store_dwordx4 v7, v[76:79], s[68:69]\n"
      "s_endpgm\n"
      ".Lp_s2_%=:\n"
      "s_cmp_eq_u32 s38, 0\n"
      "s_cbranch_scc1 .Lp_o3_%=\n"
      "s_sub_u32 s38, s38, 1\n"
      "s_sleep 4\n"
      "s_nop 7\n"
      "global_load_dword v252, v253, s[58:59] offset:0 sc1\n"
      "s_waitcnt vmcnt(0)\n"
      "v_cmp_ne_u32 vcc, s37, v252\n"
      "s_nop 1\n"
      "s_cbranch_vccnz .Lp_s2_%=\n"
      "s_nop 7\n"
      "s_branch .Lp_o3_%=\n"
      ".Lp_s5_%=:\n"
      "s_cmp_eq_u32 s38, 0\n"
      "s_cbranch_scc1 .Lp_o6_%=\n"
      "s_sub_u32 s38, s38, 1\n"
      "s_sleep 4\n"
      "s_nop 7\n"
      "global_load_dword v252, v253, s[58:59] offset:128 sc1\n"
      "s_waitcnt vmcnt(0)\n"
      "v_cmp_ne_u32 vcc, s37, v252\n"
      "s_nop 1\n"
      "s_cbranch_vccnz .Lp_s5_%=\n"
      "s_nop 7\n"
      "s_branch .Lp_o6_%=\n"
      ".Lp_s8_%=:\n"
      "s_cmp_eq_u32 s38, 0\n"
      "s_cbranch_scc1 .Lp_o9_%=\n"
      "s_sub_u32 s38, s38, 1\n"
      "s_sleep 2\n"
      "s_nop 7\n"
      "global_load_dwordx2 v[202:203], v200, s[58:59] sc1\n"
      "s_waitcnt vmcnt(0)\n"
      "v_cmp_ne_u32 vcc, s37, v203\n"
      "s_nop 1\n"
      "s_cbranch_vccnz .Lp_s8_%=\n"
      "s_nop 7\n"
      "s_branch .Lp_o9_%=\n"
      ".Lp_s13_%=:\n"
      "s_cmp_eq_u32 s38, 0\n"
      "s_cbranch_scc1 .Lp_o14_%=\n"
      "s_sub_u32 s38, s38, 1\n"
      "s_sleep 1\n"
      "s_nop 7\n"
      "global_load_dwordx2 v[204:205], v209, s[58:59] offset:0 sc1\n"
      "global_load_dwordx2 v[206:207], v209, s[58:59] offset:256 sc1\n"
      "s_waitcnt vmcnt(0)\n"
      "v_bitop3_b32 v210, v205, v207, s37 bitop3:0x7e\n"
      "v_cmp_ne_u32 vcc, 0, v210\n"
      "s_nop 1\n"
      "s_cbranch_vccnz .Lp_s13_%=\n"
      "s_nop 7\n"
      "s_branch .Lp_o14_%=\n"
      ".Lp_s16_%=:\n"
      "s_cmp_eq_u32 s38, 0\n"
      "s_cbranch_scc1 .Lp_o17_%=\n"
      "s_sub_u32 s38, s38, 1\n"
      "s_nop 7\n"
      "global_load_dwordx4 v[12:15], v4, s[62:63] offset:0 sc1\n"
      "global_load_dwordx4 v[16:19], v4, s[62:63] offset:1024 sc1\n"
      "global_load_dwordx4 v[20:23], v4, s[62:63] offset:2048 sc1\n"
      "s_waitcnt vmcnt(0)\n"
      "v_bitop3_b32 v22, v15, v19, s37 bitop3:0x7e\n"
      "v_bitop3_b32 v22, v22, v23, s37 bitop3:0xf6\n"
      "v_cmp_ne_u32 vcc, 0, v22\n"
      "s_nop 1\n"
      "s_cbranch_vccnz .Lp_s16_%=\n"
      "s_nop 7\n"
      "s_branch .Lp_o17_%=\n"
      ".Lp_s20_%=:\n"
      "s_cmp_eq_u32 s38, 0\n"
      "s_cbranch_scc1 .Lp_o21_%=\n"
      "s_sub_u32 s38, s38, 1\n"
      "s_nop 7\n"
      "global_load_dwordx4 v[44:47], v5, s[62:63] offset:0 sc1\n"
      "global_load_dwordx4 v[48:51], v5, s[62:63] offset:1024 sc1\n"
      "global_load_dwordx4 v[52:55], v5, s[62:63] offset:2048 sc1\n"
      "s_waitcnt vmcnt(0)\n"
      "v_bitop3_b32 v54, v47, v51, s37 bitop3:0x7e\n"
      "v_bitop3_b32 v54, v54, v55, s37 bitop3:0xf6\n"
      "v_cmp_ne_u32 vcc, 0, v54\n"
      "s_nop 1\n"
      "s_cbranch_vccnz .Lp_s20_%=\n"
      "s_nop 7\n"
      "s_branch .Lp_o21_%=\n"
      ".Lp_s24_%=:\n"
      "s_cmp_eq_u32 s38, 0\n"
      "s_cbranch_scc1 .Lp_o25_%=\n"
      "s_sub_u32 s38, s38, 1\n"
      "s_sleep 2\n"
      "s_nop 7\n"
      "global_load_dwordx4 v[24:27], v60, s[58:59] offset:0 sc1\n"
      "global_load_dwordx4 v[28:31], v60, s[58:59] offset:1024 sc1\n"
      "global_load_dwordx4 v[32:35], v60, s[58:59] offset:2048 sc1\n"
      "global_load_dwordx4 v[36:39], v60, s[58:59] offset:3072 sc1\n"
      "s_waitcnt vmcnt(0)\n"
      "v_bitop3_b32 v61, v25, v27, s37 bitop3:0x7e\n"
      "v_bitop3_b32 v61, v61, v29, s37 bitop3:0xf6\n"
      "v_bitop3_b32 v61, v61, v31, s37 bitop3:0xf6\n"
      "v_bitop3_b32 v61, v61, v33, s37 bitop3:0xf6\n"
      "v_bitop3_b32 v61, v61, v35, s37 bitop3:0xf6\n"
      "v_bitop3_b32 v61, v61, v37, s37 bitop3:0xf6\n"
      "v_bitop3_b32 v61, v61, v39, s37 bitop3:0xf6\n"
      "v_cmp_ne_u32 vcc, 0, v61\n"
      "s_nop 1\n"
      "s_cbranch_vccnz .Lp_s24_%=\n"
      "s_nop 7\n"
      "s_branch .Lp_o25_%=\n"
      ".Lp_s30_%=:\n"
      "s_cmp_eq_u32 s38, 0\n"
      "s_cbranch_scc1 .Lp_o31_%=\n"
      "s_sub_u32 s38, s38, 1\n"
      "s_nop 7\n"
      "global_load_dwordx4 v[224:227], v6, s[62:63] offset:0 sc1\n"
      "global_load_dwordx4 v[228:231], v6, s[62:63] offset:1024 sc1\n"
      "global_load_dwordx4 v[232:235], v6, s[62:63] offset:2048 sc1\n"
      "s_waitcnt vmcnt(0)\n"
      "v_bitop3_b32 v234, v227, v231, s37 bitop3:0x7e\n"
      "v_bitop3_b32 v234, v234, v235, s37 bitop3:0xf6\n"
      "v_cmp_ne_u32 vcc, 0, v234\n"
      "s_nop 1\n"
      "s_cbranch_vccnz .Lp_s30_%=\n"
      "s_nop 7\n"
      "s_branch .Lp_o31_%=\n"
      ".Ls_s3_%=:\n"
      "s_cmp_eq_u32 s38, 0\n"
      "s_cbranch_scc1 .Ls_o4_%=\n"
      "s_sub_u32 s38, s38, 1\n"
      "s_sleep 4\n"
      "s_nop 7\n"
      "global_load_dword v252, v253, s[58:59] offset:0 sc1\n"
      "s_waitcnt vmcnt(0)\n"
      "v_cmp_ne_u32 vcc, s37, v252\n"
      "s_nop 1\n"
      "s_cbranch_vccnz .Ls_s3_%=\n"
      "s_nop 7\n"
      "s_branch .Ls_o4_%=\n"
      ".Ls_s6_%=:\n"
      "s_cmp_eq_u32 s38, 0\n"
      "s_cbranch_scc1 .Ls_o7_%=\n"
      "s_sub_u32 s38, s38, 1\n"
      "s_sleep 2\n"
      "s_nop 7\n"
      "global_load_dwordx2 v[206:207], v204, s[58:59] sc1\n"
      "s_waitcnt vmcnt(0)\n"
      "v_cmp_ne_u32 vcc, s37, v207\n"
      "s_nop 1\n"
      "s_cbranch_vccnz .Ls_s6_%=\n"
      "s_nop 7\n"
      "s_branch .Ls_o7_%=\n"
      ".Ls_s11_%=:\n"
      "s_cmp_eq_u32 s38, 0\n"
      "s_cbranch_scc1 .Ls_o12_%=\n"
      "s_sub_u32 s38, s38, 1\n"
      "s_sleep 4\n"
      "s_nop 7\n"
      "global_load_dword v252, v253, s[58:59] offset:128 sc1\n"
      "s_waitcnt vmcnt(0)\n"
      "v_cmp_ne_u32 vcc, s37, v252\n"
      "s_nop 1\n"
      "s_cbranch_vccnz .Ls_s11_%=\n"
      "s_nop 7\n"
      "s_branch .Ls_o12_%=\n"
      ".Ls_s14_%=:\n"
      "s_cmp_eq_u32 s38, 0\n"
      "s_cbranch_scc1 .Ls_o15_%=\n"
      "s_sub_u32 s38, s38, 1\n"
      "s_sleep 1\n"
      "s_nop 7\n"
      "global_load_dwordx2 v[204:205], v209, s[58:59] offset:0 sc1\n"
      "global_load_dwordx2 v[206:207], v209, s[58:59] offset:256 sc1\n"
      "s_waitcnt vmcnt(0)\n"
      "v_bitop3_b32 v210, v205, v207, s37 bitop3:0x7e\n"
      "v_cmp_ne_u32 vcc, 0, v210\n"
      "s_nop 1\n"
      "s_cbranch_vccnz .Ls_s14_%=\n"
      "s_nop 7\n"
      "s_branch .Ls_o15_%=\n"
      ".Ls_s17_%=:\n"
      "s_cmp_eq_u32 s38, 0\n"
      "s_cbranch_scc1 .Ls_o18_%=\n"
      "s_sub_u32 s38, s38, 1\n"
      "s_sleep 8\n"
      "s_nop 7\n"
      "global_load_dwordx4 v[224:227], v200, s[58:59] offset:0 sc1\n"
      "global_load_dwordx4 v[228:231], v200, s[58:59] offset:1024 sc1\n"
      "global_load_dwordx4 v[232:235], v200, s[58:59] offset:2048 sc1\n"
      "global_load_dwordx4 v[236:239], v200, s[58:59] offset:3072 sc1\n"
      "s_waitcnt vmcnt(0)\n"
      "v_bitop3_b32 v192, v225, v227, s37 bitop3:0x7e\n"
      "v_bitop3_b32 v192, v192, v229, s37 bitop3:0xf6\n"
      "v_bitop3_b32 v192, v192, v231, s37 bitop3:0xf6\n"
      "v_bitop3_b32 v192, v192, v233, s37 bitop3:0xf6\n"
      "v_bitop3_b32 v192, v192, v235, s37 bitop3:0xf6\n"
      "v_bitop3_b32 v192, v192, v237, s37 bitop3:0xf6\n"
      "v_bitop3_b32 v192, v192, v239, s37 bitop3:0xf6\n"
      "v_cmp_ne_u32 vcc, 0, v192\n"
      "s_nop 1\n"
      "s_cbranch_vccnz .Ls_s17_%=\n"
      "s_nop 7\n"
      "s_branch .Ls_o18_%=\n"
      ".Ls_s20_%=:\n"
      "s_cmp_eq_u32 s38, 0\n"
      "s_cbranch_scc1 .Ls_o21_%=\n"
      "s_sub_u32 s38, s38, 1\n"
      "s_nop 7\n"
      "global_load_dwordx4 v[12:15], v4, s[62:63] offset:0 sc1\n"
      "global_load_dwordx4 v[16:19], v4, s[62:63] offset:1024 sc1\n"
      "global_load_dwordx4 v[20:23], v4, s[62:63] offset:2048 sc1\n"
      "s_waitcnt vmcnt(0)\n"
      "v_bitop3_b32 v22, v15, v19, s37 bitop3:0x7e\n"
      "v_bitop3_b32 v22, v22, v23, s37 bitop3:0xf6\n"
      "v_cmp_ne_u32 vcc, 0, v22\n"
      "s_nop 1\n"
      "s_cbranch_vccnz .Ls_s20_%=\n"
      "s_nop 7\n"
      "s_branch .Ls_o21_%=\n"
      ".Ls_s24_%=:\n"
      "s_cmp_eq_u32 s38, 0\n"
      "s_cbranch_scc1 .Ls_o25_%=\n"
      "s_sub_u32 s38, s38, 1\n"
      "s_nop 7\n"
      "global_load_dwordx4 v[44:47], v5, s[62:63] offset:0 sc1\n"
      "global_load_dwordx4 v[48:51], v5, s[62:63] offset:1024 sc1\n"
      "global_load_dwordx4 v[52:55], v5, s[62:63] offset:2048 sc1\n"
      "s_waitcnt vmcnt(0)\n"
      "v_bitop3_b32 v54, v47, v51, s37 bitop3:0x7e\n"
      "v_bitop3_b32 v54, v54, v55, s37 bitop3:0xf6\n"
      "v_cmp_ne_u32 vcc, 0, v54\n"
      "s_nop 1\n"
      "s_cbranch_vccnz .Ls_s24_%=\n"
      "s_nop 7\n"
      "s_branch .Ls_o25_%=\n"
      ".Ls_s28_%=:\n"
      "s_cmp_eq_u32 s38, 0\n"
      "s_cbranch_scc1 .Ls_o29_%=\n"
      "s_sub_u32 s38, s38, 1\n"
      "s_sleep 2\n"
      "s_nop 7\n"
      "global_load_dwordx4 v[24:27], v60, s[58:59] offset:0 sc1\n"
      "global_load_dwordx4 v[28:31], v60, s[58:59] offset:1024 sc1\n"
      "global_load_dwordx4 v[32:35], v60, s[58:59] offset:2048 sc1\n"
      "global_load_dwordx4 v[36:39], v60, s[58:59] offset:3072 sc1\n"
      "s_waitcnt vmcnt(0)\n"
      "v_bitop3_b32 v61, v25, v27, s37 bitop3:0x7e\n"
      "v_bitop3_b32 v61, v61, v29, s37 bitop3:0xf6\n"
      "v_bitop3_b32 v61, v61, v31, s37 bitop3:0xf6\n"
      "v_bitop3_b32 v61, v61, v33, s37 bitop3:0xf6\n"
      "v_bitop3_b32 v61, v61, v35, s37 bitop3:0xf6\n"
      "v_bitop3_b32 v61, v61, v37, s37 bitop3:0xf6\n"
      "v_bitop3_b32 v61, v61, v39, s37 bitop3:0xf6\n"
      "v_cmp_ne_u32 vcc, 0, v61\n"
      "s_nop 1\n"
      "s_cbranch_vccnz .Ls_s28_%=\n"
      "s_nop 7\n"
      "s_branch .Ls_o29_%=\n"
      ".Ls_s34_%=:\n"
      "s_cmp_eq_u32 s38, 0\n"
      "s_cbranch_scc1 .Ls_o35_%=\n"
      "s_sub_u32 s38, s38, 1\n"
      "s_nop 7\n"
      "global_load_dwordx4 v[224:227], v6, s[62:63] offset:0 sc1\n"
      "global_load_dwordx4 v[228:231], v6, s[62:63] offset:1024 sc1\n"
      "global_load_dwordx4 v[232:235], v6, s[62:63] offset:2048 sc1\n"
      "s_waitcnt vmcnt(0)\n"
      "v_bitop3_b32 v234, v227, v231, s37 bitop3:0x7e\n"
      "v_bitop3_b32 v234, v234, v235, s37 bitop3:0xf6\n"
      "v_cmp_ne_u32 vcc, 0, v234\n"
      "s_nop 1\n"
      "s_cbranch_vccnz .Ls_s34_%=\n"
      "s_nop 7\n"
      "s_branch .Ls_o35_%=\n"
      ".Lend_%=:\n"
      :
      : "s"(__builtin_amdgcn_kernarg_segment_ptr()), "s"(blockIdx.x), "s"(__builtin_amdgcn_readfirstlane(threadIdx.x >> 6)),
        "s"((unsigned)(size_t)smem)
      : "memory", "scc", "vcc",
        "v0", "v1", "v2", "v3", "v4", "v5", "v6", "v7", "v8", "v9", "v10", "v11", "v12", "v13", "v14", "v15", "v16", "v17", "v18", "v19", "v20", "v21", "v22", "v23", "v24", "v25", "v26", "v27", "v28", "v29", "v30", "v31", "v32", "v33", "v34", "v35", "v36", "v37", "v38", "v39", "v40", "v41", "v42", "v43", "v44", "v45", "v46", "v47", "v48", "v49", "v50", "v51", "v52", "v53", "v54", "v55", "v56", "v57", "v58", "v59", "v60", "v61", "v62", "v63", "v64", "v65", "v66", "v67", "v68", "v69", "v70", "v71", "v72", "v73", "v74", "v75", "v76", "v77", "v78", "v79", "v80", "v81", "v82", "v83", "v84", "v85", "v86", "v87", "v88", "v89", "v90", "v91", "v92", "v93", "v94", "v95", "v96", "v97", "v98", "v99", "v100", "v101", "v102", "v103", "v104", "v105", "v106", "v107", "v108", "v109", "v110", "v111", "v112", "v113", "v114", "v115", "v116", "v117", "v118", "v119", "v120", "v121", "v122", "v123", "v124", "v125", "v126", "v127", "v128", "v129", "v130", "v131", "v132", "v133", "v134", "v135", "v136", "v137", "v138", "v139", "v140", "v141", "v142", "v143", "v144", "v145", "v146", "v147", "v148", "v149", "v150", "v151", "v152", "v153", "v154", "v155", "v156", "v157", "v158", "v159", "v160", "v161", "v162", "v163", "v164", "v165", "v166", "v167", "v168", "v169", "v170", "v171", "v172", "v173", "v174", "v175", "v176", "v177", "v178", "v179", "v180", "v181", "v182", "v183", "v184", "v185", "v186", "v187", "v188", "v189", "v190", "v191", "v192", "v193", "v194", "v195", "v196", "v197", "v198", "v199", "v200", "v201", "v202", "v203", "v204", "v205", "v206", "v207", "v208", "v209", "v210", "v211", "v212", "v213", "v214", "v215", "v216", "v217", "v218", "v219", "v220", "v221", "v222", "v223", "v224", "v225", "v226", "v227", "v228", "v229", "v230", "v231", "v232", "v233", "v234", "v235", "v236", "v237", "v238", "v239", "v240", "v241", "v242", "v243", "v244", "v245", "v246", "v247", "v248", "v249", "v250", "v251", "v252", "v253", "v254", "v255",
        "a0", "a1", "a2", "a3", "a4", "a5", "a6", "a7", "a8", "a9", "a10", "a11", "a12", "a13", "a14", "a15", "a16", "a17", "a18", "a19", "a20", "a21", "a22", "a23", "a24", "a25", "a26", "a27", "a28", "a29", "a30", "a31", "a32", "a33", "a34", "a35", "a36", "a37", "a38", "a39", "a40", "a41", "a42", "a43", "a44", "a45", "a46", "a47", "a48", "a49", "a50", "a51", "a52", "a53", "a54", "a55", "a56", "a57", "a58", "a59", "a60", "a61", "a62", "a63", "a64", "a65", "a66", "a67", "a68", "a69", "a70", "a71", "a72", "a73", "a74", "a75", "a76", "a77", "a78", "a79", "a80", "a81", "a82", "a83", "a84", "a85", "a86", "a87", "a88", "a89", "a90", "a91", "a92", "a93", "a94", "a95", "a96", "a97", "a98", "a99", "a100", "a101", "a102", "a103", "a104", "a105", "a106", "a107", "a108", "a109", "a110", "a111", "a112", "a113", "a114", "a115", "a116", "a117", "a118", "a119", "a120", "a121", "a122", "a123", "a124", "a125", "a126", "a127", "a128", "a129", "a130", "a131", "a132", "a133", "a134", "a135", "a136", "a137", "a138", "a139", "a140", "a141", "a142", "a143", "a144", "a145", "a146", "a147", "a148", "a149", "a150", "a151", "a152", "a153", "a154", "a155", "a156", "a157", "a158", "a159", "a160", "a161", "a162", "a163", "a164", "a165", "a166", "a167", "a168", "a169", "a170", "a171", "a172", "a173", "a174", "a175", "a176", "a177", "a178", "a179", "a180", "a181", "a182", "a183", "a184", "a185", "a186", "a187", "a188", "a189", "a190", "a191", "a192", "a193", "a194", "a195", "a196", "a197", "a198", "a199", "a200", "a201", "a202", "a203", "a204", "a205", "a206", "a207", "a208", "a209", "a210", "a211", "a212", "a213", "a214", "a215", "a216", "a217", "a218", "a219", "a220", "a221", "a222", "a223", "a224", "a225", "a226", "a227", "a228", "a229", "a230", "a231", "a232", "a233", "a234", "a235", "a236", "a237", "a238", "a239", "a240", "a241", "a242", "a243", "a244", "a245", "a246", "a247", "a248", "a249", "a250", "a251", "a252", "a253", "a254", "a255",
        "s8", "s9", "s10", "s11", "s12", "s13", "s14", "s15", "s16", "s17", "s18", "s19", "s20", "s21", "s22", "s23", "s24", "s25", "s26", "s27", "s28", "s29", "s30", "s31", "s32", "s33", "s34", "s35", "s36", "s37", "s38", "s39", "s40", "s41", "s42", "s43", "s44", "s45", "s46", "s47", "s48", "s49", "s50", "s51", "s52", "s53", "s54", "s55", "s56", "s57", "s58", "s59", "s60", "s61", "s62", "s63", "s64", "s65", "s66", "s67", "s68", "s69", "s70", "s71", "s72", "s73", "s74", "s75", "s76", "s77", "s78", "s79", "s80", "s81", "s82", "s83", "s84", "s85");
}

extern "C" void kernel_launch(void* const* d_in, const int* in_sizes, int n_in, void* d_out, int out_size, void* d_ws, size_t ws_size,
                              hipStream_t stream) {
  fused_k<<<256, 256, 0, stream>>>((const u16*)d_in[1], (const u16*)d_in[2], (const u16*)d_in[3], (const u16*)d_in[4], (const u16*)d_in[0],
                                   (char*)d_ws, (u16*)d_out, (const u16*)d_in[6], (const u16*)d_in[10], (const u16*)d_in[7],
                                   (const u16*)d_in[8], (const u16*)d_in[9]);
}
```

```cpp
#include <hip/hip_runtime.h>

typedef unsigned short u16;

__global__ __launch_bounds__(256) void fused_k(const u16* wsre, const u16* wsim, const u16* wdre, const u16* wdim, const u16* x, char* ws,
                                               u16* out, const u16* w_r, const u16* gate_w, const u16* w_gate, const u16* w_up,
                                               const u16* w_down) {
  __shared__ __attribute__((aligned(16))) char smem[163840];
  asm volatile(
      ".Lbegin_%=:\n"
      "s_load_dwordx16 s[8:23], %0, 0\n"
      "s_load_dwordx8 s[24:31], %0, 64\n"
      "v_mbcnt_lo_u32_b32 v243, -1, 0\n"
      "s_mov_b32 s32, %1\n"
      "v_mbcnt_hi_u32_b32 v243, -1, v243\n"
      "s_mov_b32 s33, %2\n"
      "s_bfe_u32 s34, s32, 0x20001\n"
      "s_bfe_u32 s35, s32, 0x20003\n"
      "v_lshrrev_b32 v236, 3, v243\n"
      "s_lshr_b32 s43, s32, 5\n"
      "v_xor_b32 v237, v243, v236\n"
      "s_and_b32 s36, s32, 1\n"
      "v_and_b32 v237, 7, v237\n"
      "s_lshl3_add_u32 s36, s36, s43\n"
      "v_lshlrev_b32 v237, 4, v237\n"
      "s_add_u32 s44, s43, s35\n"
      "v_lshlrev_b32 v242, 4, v243\n"
      "s_lshl_b32 s41, s33, 14\n"
      "s_bitcmp1_b32 s44, 0\n"
      "s_cbranch_scc1 .Lstream_%=\n"
      "v_and_b32 v238, 3, v236\n"
      "s_lshr_b32 s43, s43, 1\n"
      "v_and_b32 v239, 4, v236\n"
      "s_add_u32 s80, s43, s35\n"
      "v_lshl_or_b32 v238, v239, 1, v238\n"
      "s_lshl_b32 s80, s80, 8\n"
      "v_lshl_or_b32 v240, v238, 10, v237\n"
      "s_and_b32 s80, s80, 0x300\n"
      "v_add_u32 v241, 15360, v240\n"
      "s_lshr_b32 s83, s36, 1\n"
      "v_add_u32 v254, 2048, v240\n"
      "s_and_b32 s44, s34, 1\n"
      "v_add_u32 v255, 17408, v240\n"
      "s_lshl3_add_u32 s44, s44, s83\n"
      "s_lshl2_add_u32 s40, s44, s33\n"
      "s_lshl_b32 s44, s40, 15\n"
      "s_lshl_b32 s81, s33, 7\n"
      "s_add_u32 s81, s81, s80\n"
      "s_and_b32 s81, s81, 0x380\n"
      "s_lshl_b32 s45, s35, 17\n"
      "s_lshl_b32 s82, s33, 12\n"
      "s_add_u32 s76, s34, 1\n"
      "s_and_b32 s76, s76, 3\n"
      "s_lshl_b32 s76, s76, 15\n"
      "s_or_b32 s76, s76, s81\n"
      "s_add_u32 s76, s76, s45\n"
      "s_add_u32 s77, s34, 2\n"
      "s_and_b32 s77, s77, 3\n"
      "s_lshl_b32 s77, s77, 15\n"
      "s_or_b32 s77, s77, s81\n"
      "s_add_u32 s77, s77, s45\n"
      "s_add_u32 s78, s44, s80\n"
      "s_add_u32 m0, s82, 65536\n"
      "s_bitcmp1_b32 s34, 1\n"
      "s_waitcnt lgkmcnt(0)\n"
      "s_cselect_b64 s[12:13], s[14:15], s[12:13]\n"
      "s_add_u32 s46, s12, s76\n"
      "s_addc_u32 s47, s13, 0\n"
      "global_load_lds_dwordx4 v240, s[46:47] offset:0\n"
      "global_load_lds_dwordx4 v241, s[46:47] offset:1024\n"
      "global_load_lds_dwordx4 v254, s[46:47] offset:2048\n"
      "global_load_lds_dwordx4 v255, s[46:47] offset:3072\n"
      "s_add_u32 s70, s16, s78\n"
      "s_addc_u32 s71, s17, 0\n"
      "s_mov_b32 m0, s41\n"
      "s_add_u32 s46, s12, s77\n"
      "s_addc_u32 s47, s13, 0\n"
      "global_load_lds_dwordx4 v240, s[70:71] offset:0\n"
      "global_load_lds_dwordx4 v241, s[70:71] offset:1024\n"
      "global_load_lds_dwordx4 v254, s[70:71] offset:2048\n"
      "global_load_lds_dwordx4 v255, s[70:71] offset:3072\n"
      "s_add_u32 m0, s82, 98304\n"
      "s_nop 0\n"
      "global_load_lds_dwordx4 v240, s[46:47] offset:0\n"
      "global_load_lds_dwordx4 v241, s[46:47] offset:1024\n"
      "s_add_u32 m0, s41, 3968\n"
      "s_nop 0\n"
      "global_load_lds_dwordx4 v240, s[70:71] offset:128\n"
      "global_load_lds_dwordx4 v241, s[70:71] offset:1152\n"
      "global_load_lds_dwordx4 v254, s[70:71] offset:2176\n"
      "global_load_lds_dwordx4 v255, s[70:71] offset:3200\n"
      "s_branch .Lprod_%=\n"
      ".Lstream_%=:\n"
      "v_lshl_or_b32 v244, v236, 12, v237\n"
      "v_add_u32 v245, 31744, v244\n"
      "v_add_u32 v246, 63488, v244\n"
      "v_add_u32 v247, 95232, v244\n"
      "s_getpc_b64 s[46:47]\n"
      ".Lhere_%=:\n"
      "s_lshr_b32 s44, s32, 4\n"
      "s_lshl2_add_u32 s44, s44, s33\n"
      "s_lshl_b32 s44, s44, 10\n"
      "s_sub_u32 s46, s46, .Lhere_%=-.Lbegin_%=\n"
      "s_subb_u32 s47, s47, 0\n"
      "v_add_u32 v238, s44, v242\n"
      "v_min_u32 v238, .Lend_%=-.Lbegin_%=-16, v238\n"
      "global_load_dwordx4 v[192:195], v238, s[46:47]\n"
      "s_waitcnt lgkmcnt(0)\n"
      "s_bitcmp1_b32 s34, 1\n"
      "s_cselect_b64 s[8:9], s[10:11], s[8:9]\n"
      "s_and_b32 s45, s34, 1\n"
      "s_lshl2_add_u32 s45, s45, s33\n"
      "s_lshl_b32 s45, s45, 9\n"
      "s_lshl_b32 s76, s36, 19\n"
      "s_add_u32 s45, s45, s76\n"
      "s_add_u32 s8, s8, s45\n"
      "s_addc_u32 s9, s9, 0\n"
      "s_lshr_b32 s39, s32, 6\n"
      "s_and_b32 s45, s35, 2\n"
      "s_xor_b32 s39, s39, s45\n"
      "s_xor_b32 s39, s39, s33\n"
      "s_lshl_b32 s45, s39, 7\n"
      "s_add_u32 s76, s33, 0\n"
      "s_and_b32 s76, s76, 3\n"
      "s_lshl_b32 s76, s76, 17\n"
      "s_or_b32 s76, s76, s45\n"
      "s_add_u32 s77, s33, 1\n"
      "s_and_b32 s77, s77, 3\n"
      "s_lshl_b32 s77, s77, 17\n"
      "s_or_b32 s77, s77, s45\n"
      "s_add_u32 s78, s33, 2\n"
      "s_and_b32 s78, s78, 3\n"
      "s_lshl_b32 s78, s78, 17\n"
      "s_or_b32 s78, s78, s45\n"
      "s_add_u32 s79, s33, 3\n"
      "s_and_b32 s79, s79, 3\n"
      "s_lshl_b32 s79, s79, 17\n"
      "s_or_b32 s79, s79, s45\n"
      "v_mov_b32 v239, 0\n"
      "global_load_dword v199, v239, s[8:9]\n"
      "s_add_u32 s58, s18, 4718592\n"
      "s_addc_u32 s59, s19, 0\n"
      "global_load_dword v235, v239, s[58:59] sc1\n"
      "s_lshl2_add_u32 s43, s34, s35\n"
      "s_mul_i32 s43, s43, 266240\n"
      "s_add_u32 s43, s43, 262144\n"
      "s_lshl2_add_u32 s44, s36, s35\n"
      "s_mul_i32 s44, s44, 147712\n"
      "s_add_u32 s44, s44, 6438912\n"
      "s_bitcmp1_b32 s33, 0\n"
      "s_cselect_b32 s43, s43, s44\n"
      "s_cselect_b64 s[46:47], s[22:23], s[24:25]\n"
      "s_cselect_b64 s[70:71], s[28:29], s[26:27]\n"
      "s_lshl_b32 s44, s36, 17\n"
      "s_mul_i32 s45, s32, 7168\n"
      "s_bitcmp1_b32 s33, 1\n"
      "s_cselect_b64 s[46:47], s[46:47], s[18:19]\n"
      "s_cselect_b32 s43, 0, s43\n"
      "v_mov_b32 v238, s43\n"
      "global_load_dword v197, v238, s[46:47] sc1\n"
      "s_cmp_eq_u32 s33, 2\n"
      "s_cselect_b64 s[70:71], s[30:31], s[70:71]\n"
      "s_cmp_eq_u32 s33, 3\n"
      "s_cselect_b64 s[70:71], s[20:21], s[70:71]\n"
      "s_cselect_b32 s44, s44, s45\n"
      "v_mov_b32 v239, s44\n"
      "global_load_dword v198, v239, s[70:71] sc1\n"
      "s_sleep 25\n"
      "s_bitcmp1_b32 s34, 1\n"
      "s_cselect_b64 s[46:47], s[14:15], s[12:13]\n"
      "s_min_u32 s44, s33, 2\n"
      "s_add_u32 s44, s44, s34\n"
      "s_add_u32 s44, s44, 2\n"
      "s_and_b32 s44, s44, 3\n"
      "s_lshl2_add_u32 s44, s35, s44\n"
      "s_lshr_b32 s45, s32, 6\n"
      "s_lshl2_add_u32 s44, s44, s45\n"
      "s_lshl_b32 s44, s44, 13\n"
      "v_lshl_add_u32 v238, v243, 7, s44\n"
      "s_cmp_eq_u32 s33, 3\n"
      "s_cbranch_scc1 .Ls_e1_%=\n"
      "global_load_dword v196, v238, s[46:47]\n"
      ".Ls_e1_%=:\n"
      "s_xor_b32 s45, s76, 0\n"
      "s_add_u32 s46, s8, s45\n"
      "s_addc_u32 s47, s9, 0\n"
      "s_add_u32 m0, s41, 0\n"
      "s_nop 0\n"
      "global_load_lds_dwordx4 v244, s[46:47] offset:0\n"
      "global_load_lds_dwordx4 v245, s[46:47] offset:1024\n"
      "global_load_lds_dwordx4 v246, s[46:47] offset:2048\n"
      "global_load_lds_dwordx4 v247, s[46:47] offset:3072\n"
      "s_xor_b32 s45, s77, 0\n"
      "s_add_u32 s46, s8, s45\n"
      "s_addc_u32 s47, s9, 0\n"
      "s_add_u32 m0, s41, 4096\n"
      "s_nop 0\n"
      "global_load_lds_dwordx4 v244, s[46:47] offset:0\n"
      "global_load_lds_dwordx4 v245, s[46:47] offset:1024\n"
      "global_load_lds_dwordx4 v246, s[46:47] offset:2048\n"
      "global_load_lds_dwordx4 v247, s[46:47] offset:3072\n"
      "s_xor_b32 s45, s78, 0\n"
      "s_add_u32 s46, s8, s45\n"
      "s_addc_u32 s47, s9, 0\n"
      "s_add_u32 m0, s41, 8192\n"
      "s_nop 0\n"
      "global_load_lds_dwordx4 v244, s[46:47] offset:0\n"
      "global_load_lds_dwordx4 v245, s[46:47] offset:1024\n"
      "global_load_lds_dwordx4 v246, s[46:47] offset:2048\n"
      "global_load_lds_dwordx4 v247, s[46:47] offset:3072\n"
      "v_and_b32 v238, 15, v243\n"
      "v_lshrrev_b32 v239, 4, v243\n"
      "v_xor_b32 v239, v239, v238\n"
      "v_and_b32 v239, 7, v239\n"
      "v_lshlrev_b32 v239, 4, v239\n"
      "v_lshl_or_b32 v238, v238, 7, v239\n"
      "v_add_u32 v248, s41, v238\n"
      "v_xor_b32 v249, 64, v248\n"
      "s_lshl2_add_u32 s43, s34, s35\n"
      "s_lshr_b32 s45, s39, 1\n"
      "s_lshl1_add_u32 s45, s33, s45\n"
      "s_lshl_b32 s45, s45, 15\n"
      "s_mul_i32 s44, s43, 266240\n"
      "s_add_u32 s44, s44, 4096\n"
      "s_add_u32 s14, s18, s44\n"
      "s_addc_u32 s15, s19, 0\n"
      "s_add_u32 s10, s14, s45\n"
      "s_addc_u32 s11, s15, 0\n"
      "s_xor_b32 s45, s45, 0x8000\n"
      "s_add_u32 s14, s14, s45\n"
      "s_addc_u32 s15, s15, 0\n"
      "s_and_b32 s44, s39, 1\n"
      "s_lshl_b32 s45, s44, 14\n"
      "v_add_u32 v240, s45, v242\n"
      "v_xor_b32 v241, 0x4000, v240\n"
      "s_add_u32 s72, s10, 0x2000\n"
      "s_addc_u32 s73, s11, 0\n"
      "s_add_u32 s74, s14, 0x2000\n"
      "s_addc_u32 s75, s15, 0\n"
      "s_lshl2_add_u32 s44, s43, s33\n"
      "s_lshl_b32 s44, s44, 12\n"
      "s_add_u32 s44, s44, 1048576\n"
      "v_and_b32 v253, 7, v243\n"
      "v_lshl_add_u32 v253, v253, 9, s44\n"
      "s_lshl2_add_u32 s43, s36, s35\n"
      "s_mul_i32 s43, s43, 147712\n"
      "s_mul_i32 s44, s33, 3072\n"
      "s_add_u32 s43, s43, s44\n"
      "s_add_u32 s43, s43, 6291456\n"
      "s_add_u32 s60, s18, s43\n"
      "s_addc_u32 s61, s19, 0\n"
      "s_mul_i32 s44, s34, 36864\n"
      "s_add_u32 s62, s60, s44\n"
      "s_addc_u32 s63, s61, 0\n"
      "s_lshr_b32 s43, s34, 1\n"
      "s_lshl2_add_u32 s43, s43, s35\n"
      "s_lshl_b32 s43, s43, 12\n"
      "s_add_u32 s43, s43, 4096\n"
      "v_lshl_add_u32 v204, v243, 6, s43\n"
      "s_lshr_b32 s80, s32, 4\n"
      "s_bfe_u32 s43, s32, 0x10002\n"
      "s_lshl1_add_u32 s80, s80, s43\n"
      "s_and_b32 s81, s32, 3\n"
      "s_cmp_lt_u32 s80, 28\n"
      "s_cselect_b32 s82, -1, 0\n"
      "s_lshr_b32 s83, s33, 1\n"
      "s_lshl1_add_u32 s83, s81, s83\n"
      "v_add_u32 v250, 0x10000, v248\n"
      "v_add_u32 v251, 0x10000, v249\n"
      "s_xor_b32 s45, s79, 0\n"
      "s_add_u32 s46, s8, s45\n"
      "s_addc_u32 s47, s9, 0\n"
      "s_add_u32 m0, s41, 12288\n"
      "s_nop 0\n"
      "global_load_lds_dwordx4 v244, s[46:47] offset:0\n"
      "global_load_lds_dwordx4 v245, s[46:47] offset:1024\n"
      "global_load_lds_dwordx4 v246, s[46:47] offset:2048\n"
      "global_load_lds_dwordx4 v247, s[46:47] offset:3072\n"
      "s_xor_b32 s45, s76, 128\n"
      "s_add_u32 s46, s8, s45\n"
      "s_addc_u32 s47, s9, 0\n"
      "s_add_u32 m0, s41, 65536\n"
      "s_nop 0\n"
      "global_load_lds_dwordx4 v244, s[46:47] offset:0\n"
      "global_load_lds_dwordx4 v245, s[46:47] offset:1024\n"
      "global_load_lds_dwordx4 v246, s[46:47] offset:2048\n"
      "global_load_lds_dwordx4 v247, s[46:47] offset:3072\n"
      "s_waitcnt vmcnt(16)\n"
      "ds_read_b128 a[0:3], v248 offset:0\n"
      "ds_read_b128 a[4:7], v248 offset:2048\n"
      "ds_read_b128 a[32:35], v249 offset:0\n"
      "ds_read_b128 a[36:39], v249 offset:2048\n"
      "s_xor_b32 s45, s77, 128\n"
      "s_add_u32 s46, s8, s45\n"
      "s_addc_u32 s47, s9, 0\n"
      "s_add_u32 m0, s41, 69632\n"
      "s_nop 0\n"
      "global_load_lds_dwordx4 v244, s[46:47] offset:0\n"
      "global_load_lds_dwordx4 v245, s[46:47] offset:1024\n"
      "global_load_lds_dwordx4 v246, s[46:47] offset:2048\n"
      "global_load_lds_dwordx4 v247, s[46:47] offset:3072\n"
      "s_waitcnt vmcnt(16)\n"
      "ds_read_b128 a[8:11], v248 offset:4096\n"
      "ds_read_b128 a[12:15], v248 offset:6144\n"
      "ds_read_b128 a[40:43], v249 offset:4096\n"
      "ds_read_b128 a[44:47], v249 offset:6144\n"
      "s_xor_b32 s45, s78, 128\n"
      "s_add_u32 s46, s8, s45\n"
      "s_addc_u32 s47, s9, 0\n"
      "s_add_u32 m0, s41, 73728\n"
      "s_nop 0\n"
      "global_load_lds_dwordx4 v244, s[46:47] offset:0\n"
      "global_load_lds_dwordx4 v245, s[46:47] offset:1024\n"
      "global_load_lds_dwordx4 v246, s[46:47] offset:2048\n"
      "global_load_lds_dwordx4 v247, s[46:47] offset:3072\n"
      "s_waitcnt vmcnt(16)\n"
      "ds_read_b128 a[16:19], v248 offset:8192\n"
      "ds_read_b128 a[20:23], v248 offset:10240\n"
      "ds_read_b128 a[48:51], v249 offset:8192\n"
      "ds_read_b128 a[52:55], v249 offset:10240\n"
      "v_readfirstlane_b32 s37, v235\n"
      "s_nop 1\n"
      "s_mul_i32 s37, s37, 0x9E3779B1\n"
      "s_add_u32 s37, s37, 0x7F4A7C15\n"
      "s_mov_b32 s38, 0x8000\n"
      "s_waitcnt lgkmcnt(8)\n"
      "s_xor_b32 s45, s79, 128\n"
      "s_add_u32 s46, s8, s45\n"
      "s_addc_u32 s47, s9, 0\n"
      "s_add_u32 m0, s41, 0\n"
      "s_nop 0\n"
      "global_load_lds_dwordx4 v244, s[46:47] offset:0\n"
      "global_load_lds_dwordx4 v245, s[46:47] offset:1024\n"
      "global_load_lds_dwordx4 v246, s[46:47] offset:2048\n"
      "global_load_lds_dwordx4 v247, s[46:47] offset:3072\n"
      "s_barrier\n"
      "s_min_u32 s43, s80, 27\n"
      "v_lshrrev_b32 v202, 3, v243\n"
      "v_and_b32 v203, 0x70, v242\n"
      "v_lshl_or_b32 v200, v202, 10, v203\n"
      "v_add_u32 v201, 7168, v200\n"
      "v_add_u32 v202, 14336, v200\n"
      "v_add_u32 v203, 21504, v200\n"
      "s_lshr_b32 s44, s33, 1\n"
      "s_lshl1_add_u32 s44, s43, s44\n"
      "s_lshl_b32 s44, s44, 15\n"
      "s_and_b32 s45, s33, 1\n"
      "s_lshl1_add_u32 s45, s81, s45\n"
      "s_lshl_b32 s45, s45, 7\n"
      "s_add_u32 s44, s44, s45\n"
      "s_add_u32 s70, s30, s44\n"
      "s_addc_u32 s71, s31, 0\n"
      "s_add_u32 m0, s41, 77824\n"
      "s_nop 0\n"
      "global_load_lds_dwordx4 v200, s[70:71] offset:0\n"
      "global_load_lds_dwordx4 v201, s[70:71] offset:1024\n"
      "global_load_lds_dwordx4 v202, s[70:71] offset:2048\n"
      "global_load_lds_dwordx4 v203, s[70:71] offset:3072\n"
      "s_waitcnt vmcnt(20)\n"
      "ds_read_b128 a[24:27], v248 offset:12288\n"
      "ds_read_b128 a[28:31], v248 offset:14336\n"
      "ds_read_b128 a[56:59], v249 offset:12288\n"
      "ds_read_b128 a[60:63], v249 offset:14336\n"
      "s_waitcnt lgkmcnt(8)\n"
      "s_xor_b32 s45, s76, 256\n"
      "s_add_u32 s46, s8, s45\n"
      "s_addc_u32 s47, s9, 0\n"
      "s_add_u32 m0, s41, 4096\n"
      "s_nop 0\n"
      "global_load_lds_dwordx4 v244, s[46:47] offset:0\n"
      "global_load_lds_dwordx4 v245, s[46:47] offset:1024\n"
      "global_load_lds_dwordx4 v246, s[46:47] offset:2048\n"
      "global_load_lds_dwordx4 v247, s[46:47] offset:3072\n"
      "s_waitcnt vmcnt(20)\n"
      "ds_read_b128 a[64:67], v250 offset:0\n"
      "ds_read_b128 a[68:71], v250 offset:2048\n"
      "ds_read_b128 a[96:99], v251 offset:0\n"
      "ds_read_b128 a[100:103], v251 offset:2048\n"
      "s_waitcnt lgkmcnt(8)\n"
      "s_xor_b32 s45, s77, 256\n"
      "s_add_u32 s46, s8, s45\n"
      "s_addc_u32 s47, s9, 0\n"
      "s_add_u32 m0, s41, 8192\n"
      "s_nop 0\n"
      "global_load_lds_dwordx4 v244, s[46:47] offset:0\n"
      "global_load_lds_dwordx4 v245, s[46:47] offset:1024\n"
      "global_load_lds_dwordx4 v246, s[46:47] offset:2048\n"
      "global_load_lds_dwordx4 v247, s[46:47] offset:3072\n"
      "s_waitcnt vmcnt(20)\n"
      "ds_read_b128 a[72:75], v250 offset:4096\n"
      "ds_read_b128 a[76:79], v250 offset:6144\n"
      "ds_read_b128 a[104:107], v251 offset:4096\n"
      "ds_read_b128 a[108:111], v251 offset:6144\n"
      "s_waitcnt lgkmcnt(8)\n"
      "s_xor_b32 s45, s78, 256\n"
      "s_add_u32 s46, s8, s45\n"
      "s_addc_u32 s47, s9, 0\n"
      "s_add_u32 m0, s41, 12288\n"
      "s_nop 0\n"
      "global_load_lds_dwordx4 v244, s[46:47] offset:0\n"
      "global_load_lds_dwordx4 v245, s[46:47] offset:1024\n"
      "global_load_lds_dwordx4 v246, s[46:47] offset:2048\n"
      "global_load_lds_dwordx4 v247, s[46:47] offset:3072\n"
      "s_waitcnt vmcnt(20)\n"
      "ds_read_b128 a[80:83], v250 offset:8192\n"
      "ds_read_b128 a[84:87], v250 offset:10240\n"
      "ds_read_b128 a[112:115], v251 offset:8192\n"
      "ds_read_b128 a[116:119], v251 offset:10240\n"
      "s_waitcnt vmcnt(16)\n"
      "ds_read_b128 a[88:91], v248 offset:0\n"
      "ds_read_b128 a[92:95], v248 offset:2048\n"
      "ds_read_b128 a[120:123], v249 offset:0\n"
      "ds_read_b128 a[124:127], v249 offset:2048\n"
      "global_load_dword v252, v253, s[58:59] offset:0 sc1\n"
      "s_waitcnt lgkmcnt(12)\n"
      "s_xor_b32 s45, s79, 256\n"
      "s_add_u32 s46, s8, s45\n"
      "s_addc_u32 s47, s9, 0\n"
      "s_add_u32 m0, s41, 65536\n"
      "s_nop 0\n"
      "global_load_lds_dwordx4 v244, s[46:47] offset:0\n"
      "global_load_lds_dwordx4 v245, s[46:47] offset:1024\n"
      "global_load_lds_dwordx4 v246, s[46:47] offset:2048\n"
      "global_load_lds_dwordx4 v247, s[46:47] offset:3072\n"
      "s_waitcnt lgkmcnt(8)\n"
      "s_xor_b32 s45, s76, 384\n"
      "s_add_u32 s46, s8, s45\n"
      "s_addc_u32 s47, s9, 0\n"
      "s_add_u32 m0, s41, 69632\n"
      "s_nop 0\n"
      "global_load_lds_dwordx4 v244, s[46:47] offset:0\n"
      "global_load_lds_dwordx4 v245, s[46:47] offset:1024\n"
      "global_load_lds_dwordx4 v246, s[46:47] offset:2048\n"
      "global_load_lds_dwordx4 v247, s[46:47] offset:3072\n"
      "s_waitcnt vmcnt(17)\n"
      "ds_read_b128 a[128:131], v248 offset:4096\n"
      "ds_read_b128 a[132:135], v248 offset:6144\n"
      "ds_read_b128 a[160:163], v249 offset:4096\n"
      "ds_read_b128 a[164:167], v249 offset:6144\n"
      "s_waitcnt vmcnt(13)\n"
      "ds_read_b128 a[136:139], v248 offset:8192\n"
      "ds_read_b128 a[140:143], v248 offset:10240\n"
      "ds_read_b128 a[168:171], v249 offset:8192\n"
      "ds_read_b128 a[172:175], v249 offset:10240\n"
      "s_barrier\n"
      "s_waitcnt vmcnt(8)\n"
      "v_cmp_ne_u32 vcc, s37, v252\n"
      "s_nop 1\n"
      "s_cbranch_vccnz .Ls_s3_%=\n"
      ".Ls_o4_%=:\n"
      "global_load_dwordx2 v[206:207], v204, s[58:59] sc1\n"
      "global_load_dwordx4 v[128:131], v240, s[10:11] offset:-4096 sc1\n"
      "global_load_dwordx4 v[132:135], v240, s[10:11] offset:-3072 sc1\n"
      "global_load_dwordx4 v[136:139], v240, s[10:11] offset:-2048 sc1\n"
      "global_load_dwordx4 v[140:143], v240, s[72:73] offset:-4096 sc1\n"
      "global_load_dwordx4 v[144:147], v240, s[72:73] offset:-3072 sc1\n"
      "global_load_dwordx4 v[148:151], v240, s[72:73] offset:-2048 sc1\n"
      "s_waitcnt vmcnt(5)\n"
      "v_mfma_f32_16x16x32_bf16 v[0:3], v[128:131], a[0:3], 0\n"
      "global_load_dwordx4 v[152:155], v241, s[10:11] offset:-4096 sc1\n"
      "v_cmp_ne_u32 vcc, s37, v207\n"
      "s_nop 1\n"
      "s_cbranch_vccnz .Ls_s6_%=\n"
      ".Ls_o7_%=:\n"
      "v_mfma_f32_16x16x32_bf16 v[4:7], v[128:131], a[4:7], 0\n"
      "v_max_u32_dpp v206, v206, v206 quad_perm:[1,0,3,2] row_mask:0xf bank_mask:0xf\n"
      "s_nop 1\n"
      "v_max_u32_dpp v206, v206, v206 quad_perm:[2,3,0,1] row_mask:0xf bank_mask:0xf\n"
      "v_mfma_f32_16x16x32_bf16 v[8:11], v[128:131], a[8:11], 0\n"
      "global_load_dwordx4 v[156:159], v241, s[10:11] offset:-3072 sc1\n"
      "v_max_u32_dpp v206, v206, v206 row_half_mirror row_mask:0xf bank_mask:0xf\n"
      "v_mfma_f32_16x16x32_bf16 v[12:15], v[128:131], a[12:15], 0\n"
      "s_nop 0\n"
      "v_max_u32_dpp v206, v206, v206 row_mirror row_mask:0xf bank_mask:0xf\n"
      "v_mfma_f32_16x16x32_bf16 v[16:19], v[128:131], a[16:19], 0\n"
      "global_load_dwordx4 v[160:163], v241, s[10:11] offset:-2048 sc1\n"
      "v_mov_b32 v205, v206\n"
      "v_mfma_f32_16x16x32_bf16 v[20:23], v[128:131], a[20:23], 0\n"
      "s_nop 0\n"
      "v_permlane16_swap_b32 v206, v205\n"
      "v_mfma_f32_16x16x32_bf16 v[24:27], v[128:131], a[24:27], 0\n"
      "global_load_dwordx4 v[164:167], v241, s[72:73] offset:-4096 sc1\n"
      "v_max_u32 v206, v206, v205\n"
      "v_mfma_f32_16x16x32_bf16 v[28:31], v[128:131], a[28:31], 0\n"
      "v_mov_b32 v205, v206\n"
      "s_nop 1\n"
      "v_permlane32_swap_b32 v206, v205\n"
      "s_waitcnt vmcnt(8)\n"
      "v_mfma_f32_16x16x32_bf16 v[32:35], v[132:135], a[0:3], 0\n"
      "global_load_dwordx4 v[168:171], v241, s[72:73] offset:-3072 sc1\n"
      "v_max_u32 v206, v206, v205\n"
      "v_mfma_f32_16x16x32_bf16 v[36:39], v[132:135], a[4:7], 0\n"
      "v_readfirstlane_b32 s40, v206\n"
      "s_nop 1\n"
      "s_andn2_b32 s42, 0x7ff, s40\n"
      "v_mfma_f32_16x16x32_bf16 v[40:43], v[132:135], a[8:11], 0\n"
      "global_load_dwordx4 v[172:175], v241, s[72:73] offset:-2048 sc1\n"
      "s_lshr_b32 s43, s42, 5\n"
      "s_lshl2_add_u32 s44, s36, s33\n"
      "s_cmp_eq_u32 s43, s44\n"
      "s_cbranch_scc0 .Ls_e8_%=\n"
      "s_lshl_b32 s43, s42, 10\n"
      "v_add_u32 v208, s43, v242\n"
      "global_load_dwordx4 v[216:219], v208, s[16:17]\n"
      "global_load_dwordx4 v[220:223], v242, s[24:25]\n"
      ".Ls_e8_%=:\n"
      "v_mfma_f32_16x16x32_bf16 v[44:47], v[132:135], a[12:15], 0\n"
      "v_mfma_f32_16x16x32_bf16 v[48:51], v[132:135], a[16:19], 0\n"
      "global_load_dwordx4 v[176:179], v240, s[14:15] offset:-4096 sc1\n"
      "v_mfma_f32_16x16x32_bf16 v[52:55], v[132:135], a[20:23], 0\n"
      "v_mfma_f32_16x16x32_bf16 v[56:59], v[132:135], a[24:27], 0\n"
      "global_load_dwordx4 v[180:183], v240, s[14:15] offset:-3072 sc1\n"
      "v_mfma_f32_16x16x32_bf16 v[60:63], v[132:135], a[28:31], 0\n"
      "s_waitcnt vmcnt(10)\n"
      "v_mfma_f32_16x16x32_bf16 v[64:67], v[136:139], a[0:3], 0\n"
      "global_load_dwordx4 v[184:187], v240, s[14:15] offset:-2048 sc1\n"
      "global_load_dwordx4 v[188:191], v240, s[74:75] offset:-4096 sc1\n"
      "v_mfma_f32_16x16x32_bf16 v[68:71], v[136:139], a[4:7], 0\n"
      "global_load_dwordx4 v[128:131], v240, s[74:75] offset:-3072 sc1\n"
      "global_load_dwordx4 v[132:135], v240, s[74:75] offset:-2048 sc1\n"
      "v_mfma_f32_16x16x32_bf16 v[72:75], v[136:139], a[8:11], 0\n"
      "s_waitcnt lgkmcnt(12)\n"
      "s_xor_b32 s70, s77, 384\n"
      "s_add_u32 s70, s8, s70\n"
      "s_addc_u32 s71, s9, 0\n"
      "s_add_u32 m0, s41, 73728\n"
      "v_mfma_f32_16x16x32_bf16 v[76:79], v[136:139], a[12:15], 0\n"
      "global_load_lds_dwordx4 v244, s[70:71] offset:0\n"
      "v_mfma_f32_16x16x32_bf16 v[80:83], v[136:139], a[16:19], 0\n"
      "v_mfma_f32_16x16x32_bf16 v[84:87], v[136:139], a[20:23], 0\n"
      "global_load_lds_dwordx4 v245, s[70:71] offset:1024\n"
      "v_mfma_f32_16x16x32_bf16 v[88:91], v[136:139], a[24:27], 0\n"
      "v_mfma_f32_16x16x32_bf16 v[92:95], v[136:139], a[28:31], 0\n"
      "global_load_lds_dwordx4 v246, s[70:71] offset:2048\n"
      "global_load_dwordx4 v[136:139], v241, s[14:15] offset:-4096 sc1\n"
      "v_mfma_f32_16x16x32_bf16 v[0:3], v[140:143], a[32:35], v[0:3]\n"
      "v_mfma_f32_16x16x32_bf16 v[4:7], v[140:143], a[36:39], v[4:7]\n"
      "global_load_lds_dwordx4 v247, s[70:71] offset:3072\n"
      "v_mfma_f32_16x16x32_bf16 v[8:11], v[140:143], a[40:43], v[8:11]\n"
      "s_waitcnt lgkmcnt(8)\n"
      "s_xor_b32 s70, s78, 384\n"
      "s_add_u32 s70, s8, s70\n"
      "s_addc_u32 s71, s9, 0\n"
      "s_add_u32 m0, s41, 0\n"
      "v_mfma_f32_16x16x32_bf16 v[12:15], v[140:143], a[44:47], v[12:15]\n"
      "global_load_lds_dwordx4 v244, s[70:71] offset:0\n"
      "v_mfma_f32_16x16x32_bf16 v[16:19], v[140:143], a[48:51], v[16:19]\n"
      "v_mfma_f32_16x16x32_bf16 v[20:23], v[140:143], a[52:55], v[20:23]\n"
      "global_load_lds_dwordx4 v245, s[70:71] offset:1024\n"
      "ds_read_b128 a[144:147], v248 offset:12288\n"
      "v_mfma_f32_16x16x32_bf16 v[24:27], v[140:143], a[56:59], v[24:27]\n"
      "ds_read_b128 a[148:151], v248 offset:14336\n"
      "ds_read_b128 a[176:179], v249 offset:12288\n"
      "v_mfma_f32_16x16x32_bf16 v[28:31], v[140:143], a[60:63], v[28:31]\n"
      "s_waitcnt vmcnt(19)\n"
      "global_load_lds_dwordx4 v246, s[70:71] offset:2048\n"
      "global_load_dwordx4 v[140:143], v241, s[74:75] offset:-4096 sc1\n"
      "v_mfma_f32_16x16x32_bf16 v[32:35], v[144:147], a[32:35], v[32:35]\n"
      "ds_read_b128 a[180:183], v249 offset:14336\n"
      "ds_read_b128 a[152:155], v250 offset:0\n"
      "v_mfma_f32_16x16x32_bf16 v[36:39], v[144:147], a[36:39], v[36:39]\n"
      "global_load_lds_dwordx4 v247, s[70:71] offset:3072\n"
      "ds_read_b128 a[156:159], v250 offset:2048\n"
      "v_mfma_f32_16x16x32_bf16 v[40:43], v[144:147], a[40:43], v[40:43]\n"
      "s_waitcnt lgkmcnt(10)\n"
      "s_xor_b32 s70, s79, 384\n"
      "s_add_u32 s70, s8, s70\n"
      "s_addc_u32 s71, s9, 0\n"
      "s_add_u32 m0, s41, 4096\n"
      "v_mfma_f32_16x16x32_bf16 v[44:47], v[144:147], a[44:47], v[44:47]\n"
      "global_load_lds_dwordx4 v244, s[70:71] offset:0\n"
      "ds_read_b128 a[184:187], v251 offset:0\n"
      "v_mfma_f32_16x16x32_bf16 v[48:51], v[144:147], a[48:51], v[48:51]\n"
      "ds_read_b128 a[188:191], v251 offset:2048\n"
      "ds_read_b128 a[192:195], v250 offset:4096\n"
      "v_mfma_f32_16x16x32_bf16 v[52:55], v[144:147], a[52:55], v[52:55]\n"
      "global_load_lds_dwordx4 v245, s[70:71] offset:1024\n"
      "ds_read_b128 a[196:199], v250 offset:6144\n"
      "v_mfma_f32_16x16x32_bf16 v[56:59], v[144:147], a[56:59], v[56:59]\n"
      "ds_read_b128 a[224:227], v251 offset:4096\n"
      "ds_read_b128 a[228:231], v251 offset:6144\n"
      "v_mfma_f32_16x16x32_bf16 v[60:63], v[144:147], a[60:63], v[60:63]\n"
      "global_load_lds_dwordx4 v246, s[70:71] offset:2048\n"
      "global_load_dwordx4 v[144:147], v241, s[14:15] offset:-3072 sc1\n"
      "v_mfma_f32_16x16x32_bf16 v[64:67], v[148:151], a[32:35], v[64:67]\n"
      "v_mfma_f32_16x16x32_bf16 v[68:71], v[148:151], a[36:39], v[68:71]\n"
      "global_load_lds_dwordx4 v247, s[70:71] offset:3072\n"
      "v_mfma_f32_16x16x32_bf16 v[72:75], v[148:151], a[40:43], v[72:75]\n"
      "v_mfma_f32_16x16x32_bf16 v[76:79], v[148:151], a[44:47], v[76:79]\n"
      "v_mfma_f32_16x16x32_bf16 v[80:83], v[148:151], a[48:51], v[80:83]\n"
      "v_mfma_f32_16x16x32_bf16 v[84:87], v[148:151], a[52:55], v[84:87]\n"
      "v_mfma_f32_16x16x32_bf16 v[88:91], v[148:151], a[56:59], v[88:91]\n"
      "v_mfma_f32_16x16x32_bf16 v[92:95], v[148:151], a[60:63], v[92:95]\n"
      "s_waitcnt vmcnt(25)\n"
      "global_load_dwordx4 v[148:151], v241, s[74:75] offset:-3072 sc1\n"
      "v_mfma_f32_16x16x32_bf16 v[0:3], v[152:155], a[64:67], v[0:3]\n"
      "v_mfma_f32_16x16x32_bf16 v[4:7], v[152:155], a[68:71], v[4:7]\n"
      "v_mfma_f32_16x16x32_bf16 v[8:11], v[152:155], a[72:75], v[8:11]\n"
      "v_mfma_f32_16x16x32_bf16 v[12:15], v[152:155], a[76:79], v[12:15]\n"
      "v_mfma_f32_16x16x32_bf16 v[16:19], v[152:155], a[80:83], v[16:19]\n"
      "v_mfma_f32_16x16x32_bf16 v[20:23], v[152:155], a[84:87], v[20:23]\n"
      "v_mfma_f32_16x16x32_bf16 v[24:27], v[152:155], a[88:91], v[24:27]\n"
      "v_mfma_f32_16x16x32_bf16 v[28:31], v[152:155], a[92:95], v[28:31]\n"
      "global_load_dwordx4 v[152:155], v241, s[14:15] offset:-2048 sc1\n"
      "s_waitcnt vmcnt(12)\n"
      "ds_read_b128 a[200:203], v250 offset:8192\n"
      "v_mfma_f32_16x16x32_bf16 v[32:35], v[156:159], a[64:67], v[32:35]\n"
      "ds_read_b128 a[204:207], v250 offset:10240\n"
      "ds_read_b128 a[232:235], v251 offset:8192\n"
      "v_mfma_f32_16x16x32_bf16 v[36:39], v[156:159], a[68:71], v[36:39]\n"
      "ds_read_b128 a[236:239], v251 offset:10240\n"
      "v_mfma_f32_16x16x32_bf16 v[40:43], v[156:159], a[72:75], v[40:43]\n"
      "v_mfma_f32_16x16x32_bf16 v[44:47], v[156:159], a[76:79], v[44:47]\n"
      "v_mfma_f32_16x16x32_bf16 v[48:51], v[156:159], a[80:83], v[48:51]\n"
      "s_lshl_b32 s54, s42, 10\n"
      "s_lshl_b32 s55, s83, 7\n"
      "s_add_u32 s54, s54, s55\n"
      "v_mfma_f32_16x16x32_bf16 v[52:55], v[156:159], a[84:87], v[52:55]\n"
      "v_and_b32 v224, 48, v243\n"
      "v_add_u32 v224, s54, v224\n"
      "v_mfma_f32_16x16x32_bf16 v[56:59], v[156:159], a[88:91], v[56:59]\n"
      "global_load_dwordx4 v[192:195], v224, s[16:17]\n"
      "global_load_dwordx4 v[196:199], v224, s[16:17] offset:64\n"
      "v_mfma_f32_16x16x32_bf16 v[60:63], v[156:159], a[92:95], v[60:63]\n"
      "global_load_dwordx4 v[156:159], v241, s[74:75] offset:-2048 sc1\n"
      "s_waitcnt vmcnt(10)\n"
      "ds_read_b128 a[208:211], v248 offset:0\n"
      "v_mfma_f32_16x16x32_bf16 v[64:67], v[160:163], a[64:67], v[64:67]\n"
      "ds_read_b128 a[212:215], v248 offset:2048\n"
      "ds_read_b128 a[240:243], v249 offset:0\n"
      "v_mfma_f32_16x16x32_bf16 v[68:71], v[160:163], a[68:71], v[68:71]\n"
      "ds_read_b128 a[244:247], v249 offset:2048\n"
      "v_mfma_f32_16x16x32_bf16 v[72:75], v[160:163], a[72:75], v[72:75]\n"
      "v_mfma_f32_16x16x32_bf16 v[76:79], v[160:163], a[76:79], v[76:79]\n"
      "v_mfma_f32_16x16x32_bf16 v[80:83], v[160:163], a[80:83], v[80:83]\n"
      "v_mfma_f32_16x16x32_bf16 v[84:87], v[160:163], a[84:87], v[84:87]\n"
      "v_mfma_f32_16x16x32_bf16 v[88:91], v[160:163], a[88:91], v[88:91]\n"
      "s_ashr_i32 s84, s40, 31\n"
      "s_min_u32 s43, s80, 27\n"
      "v_lshrrev_b32 v202, 3, v243\n"
      "v_and_b32 v203, 0x70, v242\n"
      "s_movk_i32 s44, 3584\n"
      "v_mad_u32_u24 v200, v202, s44, v203\n"
      "v_add_u32 v201, 27648, v200\n"
      "v_add_u32 v202, 55296, v200\n"
      "v_add_u32 v203, 82944, v200\n"
      "s_bitcmp1_b32 s33, 0\n"
      "s_cselect_b64 s[70:71], s[28:29], s[26:27]\n"
      "s_mul_i32 s44, s83, 229376\n"
      "s_lshl_b32 s45, s43, 7\n"
      "s_add_u32 s44, s44, s45\n"
      "s_add_u32 s70, s70, s44\n"
      "s_addc_u32 s71, s71, 0\n"
      "s_lshl_b32 s45, s33, 13\n"
      "s_add_u32 m0, s45, 131072\n"
      "v_mfma_f32_16x16x32_bf16 v[92:95], v[160:163], a[92:95], v[92:95]\n"
      "s_not_b32 s84, s84\n"
      "global_load_lds_dwordx4 v200, s[70:71] offset:0\n"
      "s_or_b32 s84, s84, 0x80000000\n"
      "v_mfma_f32_16x16x32_bf16 v[0:3], v[164:167], a[96:99], v[0:3]\n"
      "s_xor_b32 s84, s84, s40\n"
      "global_load_lds_dwordx4 v201, s[70:71] offset:1024\n"
      "s_and_b32 s84, s84, 0xffff0000\n"
      "v_mfma_f32_16x16x32_bf16 v[4:7], v[164:167], a[100:103], v[4:7]\n"
      "global_load_lds_dwordx4 v202, s[70:71] offset:2048\n"
      "v_lshlrev_b32 v208, 16, v216\n"
      "v_mfma_f32_16x16x32_bf16 v[8:11], v[164:167], a[104:107], v[8:11]\n"
      "global_load_lds_dwordx4 v203, s[70:71] offset:3072\n"
      "v_lshlrev_b32 v209, 16, v220\n"
      "v_mfma_f32_16x16x32_bf16 v[12:15], v[164:167], a[108:111], v[12:15]\n"
      "s_add_u32 s70, s70, 114688\n"
      "s_addc_u32 s71, s71, 0\n"
      "s_add_u32 m0, m0, 4096\n"
      "v_mfma_f32_16x16x32_bf16 v[16:19], v[164:167], a[112:115], v[16:19]\n"
      "global_load_lds_dwordx4 v200, s[70:71] offset:0\n"
      "v_and_b32 v216, 0xffff0000, v216\n"
      "v_mfma_f32_16x16x32_bf16 v[20:23], v[164:167], a[116:119], v[20:23]\n"
      "global_load_lds_dwordx4 v201, s[70:71] offset:1024\n"
      "v_and_b32 v220, 0xffff0000, v220\n"
      "v_mfma_f32_16x16x32_bf16 v[24:27], v[164:167], a[120:123], v[24:27]\n"
      "global_load_lds_dwordx4 v202, s[70:71] offset:2048\n"
      "v_mul_f32 v208, v208, v209\n"
      "v_mfma_f32_16x16x32_bf16 v[28:31], v[164:167], a[124:127], v[28:31]\n"
      "global_load_lds_dwordx4 v203, s[70:71] offset:3072\n"
      "v_lshrrev_b32 v201, 4, v243\n"
      "v_mfma_f32_16x16x32_bf16 v[32:35], v[168:171], a[96:99], v[32:35]\n"
      "global_load_dword v252, v253, s[58:59] offset:128 sc1\n"
      "v_bfe_u32 v224, v243, 2, 2\n"
      "v_mfma_f32_16x16x32_bf16 v[36:39], v[168:171], a[100:103], v[36:39]\n"
      "s_waitcnt vmcnt(14)\n"
      "ds_read_b128 a[216:219], v248 offset:4096\n"
      "v_mfma_f32_16x16x32_bf16 v[40:43], v[168:171], a[104:107], v[40:43]\n"
      "ds_read_b128 a[220:223], v248 offset:6144\n"
      "v_lshl_or_b32 v201, v201, 3, v224\n"
      "v_mfma_f32_16x16x32_bf16 v[44:47], v[168:171], a[108:111], v[44:47]\n"
      "ds_read_b128 a[248:251], v249 offset:4096\n"
      "v_and_b32 v224, 3, v243\n"
      "v_mfma_f32_16x16x32_bf16 v[48:51], v[168:171], a[112:115], v[48:51]\n"
      "ds_read_b128 a[252:255], v249 offset:6144\n"
      "v_lshlrev_b32 v224, 3, v224\n"
      "v_mfma_f32_16x16x32_bf16 v[52:55], v[168:171], a[116:119], v[52:55]\n"
      "s_sub_u32 s48, s33, 1\n"
      "s_and_b32 s48, s48, 3\n"
      "v_mfma_f32_16x16x32_bf16 v[56:59], v[168:171], a[120:123], v[56:59]\n"
      "s_lshl_b32 s48, s48, 14\n"
      "v_lshl_add_u32 v244, v243, 2, s48\n"
      "s_sub_u32 s48, s33, 2\n"
      "v_mfma_f32_16x16x32_bf16 v[60:63], v[168:171], a[124:127], v[60:63]\n"
      "s_and_b32 s48, s48, 3\n"
      "s_lshl_b32 s48, s48, 14\n"
      "v_lshl_add_u32 v245, v243, 2, s48\n"
      "v_mfma_f32_16x16x32_bf16 v[64:67], v[172:175], a[96:99], v[64:67]\n"
      "s_sub_u32 s48, s33, 3\n"
      "s_and_b32 s48, s48, 3\n"
      "s_lshl_b32 s48, s48, 14\n"
      "v_mfma_f32_16x16x32_bf16 v[68:71], v[172:175], a[100:103], v[68:71]\n"
      "s_mov_b32 m0, s41\n"
      "v_lshl_add_u32 v246, v243, 2, s48\n"
      "v_lshl_or_b32 v201, v201, 7, v224\n"
      "v_mfma_f32_16x16x32_bf16 v[72:75], v[172:175], a[104:107], v[72:75]\n"
      "s_mov_b32 s64, 0xffff0000\n"
      "s_mov_b32 s65, 0xffff0000\n"
      "s_mov_b32 s66, -1\n"
      "v_mfma_f32_16x16x32_bf16 v[76:79], v[172:175], a[108:111], v[76:79]\n"
      "s_mov_b32 s67, 0\n"
      "s_lshl_b32 s54, s33, 13\n"
      "s_add_u32 s54, s54, 131072\n"
      "v_mfma_f32_16x16x32_bf16 v[80:83], v[172:175], a[112:115], v[80:83]\n"
      "v_add_u32 v202, s54, v201\n"
      "s_and_b32 s54, s33, 1\n"
      "v_mfma_f32_16x16x32_bf16 v[84:87], v[172:175], a[116:119], v[84:87]\n"
      "s_lshl3_add_u32 s54, s54, s83\n"
      "s_lshl_b32 s54, s54, 8\n"
      "s_lshl_b32 s55, s80, 13\n"
      "v_mfma_f32_16x16x32_bf16 v[88:91], v[172:175], a[120:123], v[88:91]\n"
      "s_add_u32 s55, s55, 262144\n"
      "s_add_u32 s54, s54, s55\n"
      "v_bfe_u32 v224, v243, 4, 1\n"
      "v_mfma_f32_16x16x32_bf16 v[92:95], v[172:175], a[124:127], v[92:95]\n"
      "v_and_b32 v203, 0xf0, v242\n"
      "v_lshl_or_b32 v200, v224, 12, v203\n"
      "v_mfma_f32_16x16x32_bf16 v[0:3], v[176:179], a[128:131], v[0:3]\n"
      "v_add_u32 v200, s54, v200\n"
      "v_lshl_or_b32 v203, v224, 14, v203\n"
      "v_mfma_f32_16x16x32_bf16 v[4:7], v[176:179], a[132:135], v[4:7]\n"
      "v_mul_f32 v210, v216, v220\n"
      "v_cvt_pk_bf16_f32 v208, v208, v210\n"
      "s_waitcnt lgkmcnt(15)\n"
      "v_mfma_f32_16x16x32_bf16 v[8:11], v[176:179], a[136:139], v[8:11]\n"
      "v_lshlrev_b32 v209, 16, v208\n"
      "v_and_b32 v208, 0xffff0000, v208\n"
      "v_mfma_f32_16x16x32_bf16 v[12:15], v[176:179], a[140:143], v[12:15]\n"
      "v_add_f32 v211, v208, v209\n"
      "v_lshlrev_b32 v208, 16, v217\n"
      "v_mfma_f32_16x16x32_bf16 v[16:19], v[176:179], a[144:147], v[16:19]\n"
      "v_lshlrev_b32 v209, 16, v221\n"
      "v_and_b32 v217, 0xffff0000, v217\n"
      "v_mfma_f32_16x16x32_bf16 v[20:23], v[176:179], a[148:151], v[20:23]\n"
      "v_and_b32 v221, 0xffff0000, v221\n"
      "v_mul_f32 v208, v208, v209\n"
      "v_mfma_f32_16x16x32_bf16 v[24:27], v[176:179], a[152:155], v[24:27]\n"
      "v_mul_f32 v210, v217, v221\n"
      "v_cvt_pk_bf16_f32 v208, v208, v210\n"
      "v_mfma_f32_16x16x32_bf16 v[28:31], v[176:179], a[156:159], v[28:31]\n"
      "v_lshlrev_b32 v209, 16, v208\n"
      "v_and_b32 v208, 0xffff0000, v208\n"
      "v_mfma_f32_16x16x32_bf16 v[32:35], v[180:183], a[128:131], v[32:35]\n"
      "v_add_f32 v208, v208, v209\n"
      "v_add_f32 v211, v211, v208\n"
      "v_mfma_f32_16x16x32_bf16 v[36:39], v[180:183], a[132:135], v[36:39]\n"
      "v_lshlrev_b32 v208, 16, v218\n"
      "v_lshlrev_b32 v209, 16, v222\n"
      "v_mfma_f32_16x16x32_bf16 v[40:43], v[180:183], a[136:139], v[40:43]\n"
      "v_and_b32 v218, 0xffff0000, v218\n"
      "v_and_b32 v222, 0xffff0000, v222\n"
      "v_mfma_f32_16x16x32_bf16 v[44:47], v[180:183], a[140:143], v[44:47]\n"
      "v_mul_f32 v208, v208, v209\n"
      "v_mul_f32 v210, v218, v222\n"
      "v_mfma_f32_16x16x32_bf16 v[48:51], v[180:183], a[144:147], v[48:51]\n"
      "v_cvt_pk_bf16_f32 v208, v208, v210\n"
      "v_lshlrev_b32 v209, 16, v208\n"
      "v_mfma_f32_16x16x32_bf16 v[52:55], v[180:183], a[148:151], v[52:55]\n"
      "v_and_b32 v208, 0xffff0000, v208\n"
      "v_add_f32 v208, v208, v209\n"
      "v_mfma_f32_16x16x32_bf16 v[56:59], v[180:183], a[152:155], v[56:59]\n"
      "v_add_f32 v211, v211, v208\n"
      "v_lshlrev_b32 v208, 16, v219\n"
      "v_mfma_f32_16x16x32_bf16 v[60:63], v[180:183], a[156:159], v[60:63]\n"
      "v_lshlrev_b32 v209, 16, v223\n"
      "v_and_b32 v219, 0xffff0000, v219\n"
      "v_mfma_f32_16x16x32_bf16 v[64:67], v[184:187], a[128:131], v[64:67]\n"
      "v_and_b32 v223, 0xffff0000, v223\n"
      "v_mul_f32 v208, v208, v209\n"
      "v_mfma_f32_16x16x32_bf16 v[68:71], v[184:187], a[132:135], v[68:71]\n"
      "v_mul_f32 v210, v219, v223\n"
      "v_cvt_pk_bf16_f32 v208, v208, v210\n"
      "v_mfma_f32_16x16x32_bf16 v[72:75], v[184:187], a[136:139], v[72:75]\n"
      "v_lshlrev_b32 v209, 16, v208\n"
      "v_and_b32 v208, 0xffff0000, v208\n"
      "v_mfma_f32_16x16x32_bf16 v[76:79], v[184:187], a[140:143], v[76:79]\n"
      "v_add_f32 v208, v208, v209\n"
      "v_add_f32 v211, v211, v208\n"
      "v_mfma_f32_16x16x32_bf16 v[80:83], v[184:187], a[144:147], v[80:83]\n"
      "s_nop 0\n"
      "v_add_f32_dpp v211, v211, v211 quad_perm:[1,0,3,2] row_mask:0xf bank_mask:0xf\n"
      "v_mfma_f32_16x16x32_bf16 v[84:87], v[184:187], a[148:151], v[84:87]\n"
      "s_nop 0\n"
      "v_add_f32_dpp v211, v211, v211 quad_perm:[2,3,0,1] row_mask:0xf bank_mask:0xf\n"
      "v_mfma_f32_16x16x32_bf16 v[88:91], v[184:187], a[152:155], v[88:91]\n"
      "s_nop 0\n"
      "v_add_f32_dpp v211, v211, v211 row_half_mirror row_mask:0xf bank_mask:0xf\n"
      "v_mfma_f32_16x16x32_bf16 v[92:95], v[184:187], a[156:159], v[92:95]\n"
      "s_nop 0\n"
      "v_add_f32_dpp v211, v211, v211 row_mirror row_mask:0xf bank_mask:0xf\n"
      "v_mfma_f32_16x16x32_bf16 v[0:3], v[188:191], a[160:163], v[0:3]\n"
      "v_mov_b32 v208, v211\n"
      "v_mfma_f32_16x16x32_bf16 v[4:7], v[188:191], a[164:167], v[4:7]\n"
      "s_nop 0\n"
      "v_permlane16_swap_b32 v211, v208\n"
      "v_mfma_f32_16x16x32_bf16 v[8:11], v[188:191], a[168:171], v[8:11]\n"
      "v_add_f32 v211, v211, v208\n"
      "v_mov_b32 v208, v211\n"
      "v_mfma_f32_16x16x32_bf16 v[12:15], v[188:191], a[172:175], v[12:15]\n"
      "s_nop 0\n"
      "v_permlane32_swap_b32 v211, v208\n"
      "v_mfma_f32_16x16x32_bf16 v[16:19], v[188:191], a[176:179], v[16:19]\n"
      "v_add_f32 v211, v211, v208\n"
      "v_cvt_pk_bf16_f32 v211, v211, v211\n"
      "v_mfma_f32_16x16x32_bf16 v[20:23], v[188:191], a[180:183], v[20:23]\n"
      "v_and_b32 v211, 0xffff0000, v211\n"
      "v_writelane_b32 v211, s84, 1\n"
      "v_mfma_f32_16x16x32_bf16 v[24:27], v[188:191], a[184:187], v[24:27]\n"
      "v_mul_f32 v211, 0xbfb8aa3b, v211\n"
      "v_exp_f32 v211, v211\n"
      "v_mfma_f32_16x16x32_bf16 v[28:31], v[188:191], a[188:191], v[28:31]\n"
      "v_add_f32 v211, 1.0, v211\n"
      "v_rcp_f32 v211, v211\n"
      "v_mfma_f32_16x16x32_bf16 v[32:35], v[128:131], a[160:163], v[32:35]\n"
      "v_cvt_pk_bf16_f32 v211, v211, v211\n"
      "v_and_b32 v211, 0xffff0000, v211\n"
      "v_mfma_f32_16x16x32_bf16 v[36:39], v[128:131], a[164:167], v[36:39]\n"
      "s_waitcnt vmcnt(1)\n"
      "v_readfirstlane_b32 s39, v211\n"
      "v_mfma_f32_16x16x32_bf16 v[40:43], v[128:131], a[168:171], v[40:43]\n"
      "ds_read_b64_tr_b16 v[224:225], v202 offset:0\n"
      "v_readlane_b32 s84, v211, 1\n"
      "v_mfma_f32_16x16x32_bf16 v[44:47], v[128:131], a[172:175], v[44:47]\n"
      "ds_read_b64_tr_b16 v[226:227], v202 offset:512\n"
      "ds_read_b64_tr_b16 v[228:229], v202 offset:4096\n"
      "v_mfma_f32_16x16x32_bf16 v[48:51], v[128:131], a[176:179], v[48:51]\n"
      "ds_read_b64_tr_b16 v[230:231], v202 offset:4608\n"
      "ds_read_b64_tr_b16 v[232:233], v202 offset:64\n"
      "v_mfma_f32_16x16x32_bf16 v[52:55], v[128:131], a[180:183], v[52:55]\n"
      "ds_read_b64_tr_b16 v[234:235], v202 offset:576\n"
      "ds_read_b64_tr_b16 v[236:237], v202 offset:4160\n"
      "v_mfma_f32_16x16x32_bf16 v[56:59], v[128:131], a[184:187], v[56:59]\n"
      "ds_read_b64_tr_b16 v[238:239], v202 offset:4672\n"
      "v_mfma_f32_16x16x32_bf16 v[60:63], v[128:131], a[188:191], v[60:63]\n"
      "v_mfma_f32_16x16x32_bf16 v[64:67], v[132:135], a[160:163], v[64:67]\n"
      "v_mfma_f32_16x16x32_bf16 v[68:71], v[132:135], a[164:167], v[68:71]\n"
      "s_waitcnt lgkmcnt(6)\n"
      "v_mfma_f32_16x16x32_bf16 v[248:251], v[192:195], v[224:227], 0\n"
      "v_mfma_f32_16x16x32_bf16 v[72:75], v[132:135], a[168:171], v[72:75]\n"
      "s_waitcnt vmcnt(0)\n"
      "v_cmp_ne_u32 vcc, s37, v252\n"
      "s_nop 1\n"
      "s_cbranch_vccnz .Ls_s10_%=\n"
      ".Ls_o11_%=:\n"
      "v_mfma_f32_16x16x32_bf16 v[76:79], v[132:135], a[172:175], v[76:79]\n"
      "s_waitcnt lgkmcnt(4)\n"
      "v_mfma_f32_16x16x32_bf16 v[248:251], v[196:199], v[228:231], v[248:251]\n"
      "v_mfma_f32_16x16x32_bf16 v[80:83], v[132:135], a[176:179], v[80:83]\n"
      "global_load_dwordx4 v[160:163], v240, s[10:11] offset:-1024 sc1\n"
      "global_load_dwordx4 v[164:167], v240, s[72:73] offset:-1024 sc1\n"
      "v_mfma_f32_16x16x32_bf16 v[84:87], v[132:135], a[180:183], v[84:87]\n"
      "global_load_dwordx4 v[168:171], v241, s[10:11] offset:-1024 sc1\n"
      "global_load_dwordx4 v[172:175], v241, s[72:73] offset:-1024 sc1\n"
      "v_mfma_f32_16x16x32_bf16 v[88:91], v[132:135], a[184:187], v[88:91]\n"
      "global_load_dwordx4 v[176:179], v240, s[14:15] offset:-1024 sc1\n"
      "global_load_dwordx4 v[180:183], v240, s[74:75] offset:-1024 sc1\n"
      "v_mfma_f32_16x16x32_bf16 v[92:95], v[132:135], a[188:191], v[92:95]\n"
      "global_load_dwordx4 v[184:187], v241, s[14:15] offset:-1024 sc1\n"
      "global_load_dwordx4 v[188:191], v241, s[74:75] offset:-1024 sc1\n"
      "v_mfma_f32_16x16x32_bf16 v[0:3], v[136:139], a[192:195], v[0:3]\n"
      "global_load_dwordx4 v[128:131], v240, s[10:11] offset:0 sc1\n"
      "global_load_dwordx4 v[132:135], v240, s[72:73] offset:0 sc1\n"
      "v_mfma_f32_16x16x32_bf16 v[4:7], v[136:139], a[196:199], v[4:7]\n"
      "s_waitcnt lgkmcnt(2)\n"
      "v_mfma_f32_16x16x32_bf16 v[224:227], v[192:195], v[232:235], 0\n"
      "v_mfma_f32_16x16x32_bf16 v[8:11], v[136:139], a[200:203], v[8:11]\n"
      "s_waitcnt lgkmcnt(0)\n"
      "v_mfma_f32_16x16x32_bf16 v[224:227], v[196:199], v[236:239], v[224:227]\n"
      "v_mfma_f32_16x16x32_bf16 v[12:15], v[136:139], a[204:207], v[12:15]\n"
      "v_mfma_f32_16x16x32_bf16 v[16:19], v[136:139], a[208:211], v[16:19]\n"
      "v_mfma_f32_16x16x32_bf16 v[20:23], v[136:139], a[212:215], v[20:23]\n"
      "s_nop 4\n"
      "v_cndmask_b32 v247, v248, v224, s[64:65]\n"
      "v_mfma_f32_16x16x32_bf16 v[24:27], v[136:139], a[216:219], v[24:27]\n"
      "ds_read_b64_tr_b16 v[224:225], v202 offset:32\n"
      "ds_read_b64_tr_b16 v[226:227], v202 offset:544\n"
      "v_mfma_f32_16x16x32_bf16 v[28:31], v[136:139], a[220:223], v[28:31]\n"
      "global_load_dwordx4 v[136:139], v241, s[10:11] offset:0 sc1\n"
      "ds_read_b64_tr_b16 v[228:229], v202 offset:4128\n"
      "v_mfma_f32_16x16x32_bf16 v[0:3], v[140:143], a[224:227], v[0:3]\n"
      "ds_read_b64_tr_b16 v[230:231], v202 offset:4640\n"
      "ds_read_b64_tr_b16 v[232:233], v202 offset:96\n"
      "v_mfma_f32_16x16x32_bf16 v[4:7], v[140:143], a[228:231], v[4:7]\n"
      "ds_read_b64_tr_b16 v[234:235], v202 offset:608\n"
      "ds_read_b64_tr_b16 v[236:237], v202 offset:4192\n"
      "v_mfma_f32_16x16x32_bf16 v[8:11], v[140:143], a[232:235], v[8:11]\n"
      "ds_read_b64_tr_b16 v[238:239], v202 offset:4704\n"
      "v_mfma_f32_16x16x32_bf16 v[12:15], v[140:143], a[236:239], v[12:15]\n"
      "v_mfma_f32_16x16x32_bf16 v[16:19], v[140:143], a[240:243], v[16:19]\n"
      "v_mfma_f32_16x16x32_bf16 v[20:23], v[140:143], a[244:247], v[20:23]\n"
      "s_waitcnt lgkmcnt(6)\n"
      "v_mfma_f32_16x16x32_bf16 v[248:251], v[192:195], v[224:227], 0\n"
      "v_mfma_f32_16x16x32_bf16 v[24:27], v[140:143], a[248:251], v[24:27]\n"
      "s_waitcnt lgkmcnt(4)\n"
      "v_mfma_f32_16x16x32_bf16 v[248:251], v[196:199], v[228:231], v[248:251]\n"
      "v_mfma_f32_16x16x32_bf16 v[28:31], v[140:143], a[252:255], v[28:31]\n"
      "global_load_dwordx4 v[140:143], v241, s[72:73] offset:0 sc1\n"
      "v_cvt_pk_bf16_f32 v8, v8, v9\n"
      "v_mfma_f32_16x16x32_bf16 v[32:35], v[144:147], a[192:195], v[32:35]\n"
      "s_waitcnt lgkmcnt(2)\n"
      "v_mfma_f32_16x16x32_bf16 v[224:227], v[192:195], v[232:235], 0\n"
      "v_mfma_f32_16x16x32_bf16 v[36:39], v[144:147], a[196:199], v[36:39]\n"
      "s_waitcnt lgkmcnt(0)\n"
      "v_mfma_f32_16x16x32_bf16 v[224:227], v[196:199], v[236:239], v[224:227]\n"
      "v_mfma_f32_16x16x32_bf16 v[40:43], v[144:147], a[200:203], v[40:43]\n"
      "v_cvt_pk_bf16_f32 v9, v10, v11\n"
      "ds_write_addtid_b32 v8 offset:0\n"
      "v_mfma_f32_16x16x32_bf16 v[44:47], v[144:147], a[204:207], v[44:47]\n"
      "v_cvt_pk_bf16_f32 v10, v12, v13\n"
      "ds_write_addtid_b32 v9 offset:256\n"
      "v_mfma_f32_16x16x32_bf16 v[48:51], v[144:147], a[208:211], v[48:51]\n"
      "s_nop 0\n"
      "v_cndmask_b32 v250, v248, v224, s[64:65]\n"
      "v_mfma_f32_16x16x32_bf16 v[52:55], v[144:147], a[212:215], v[52:55]\n"
      "v_and_b32 v248, s82, v247\n"
      "v_mov_b32 v249, s37\n"
      "v_mfma_f32_16x16x32_bf16 v[56:59], v[144:147], a[216:219], v[56:59]\n"
      "v_and_b32 v250, s82, v250\n"
      "v_mov_b32 v251, s37\n"
      "v_mfma_f32_16x16x32_bf16 v[60:63], v[144:147], a[220:223], v[60:63]\n"
      "global_load_dwordx4 v[144:147], v240, s[14:15] offset:0 sc1\n"
      "v_cvt_pk_bf16_f32 v11, v14, v15\n"
      "v_mfma_f32_16x16x32_bf16 v[32:35], v[148:151], a[224:227], v[32:35]\n"
      "s_mov_b64 exec, s[66:67]\n"
      "global_store_dwordx4 v200, v[248:251], s[58:59] sc1\n"
      "s_mov_b64 exec, -1\n"
      "v_mfma_f32_16x16x32_bf16 v[36:39], v[148:151], a[228:231], v[36:39]\n"
      "ds_write_addtid_b32 v10 offset:512\n"
      "v_bfe_u32 v248, v243, 1, 3\n"
      "v_mfma_f32_16x16x32_bf16 v[40:43], v[148:151], a[232:235], v[40:43]\n"
      "v_lshrrev_b32 v249, 4, v243\n"
      "v_lshlrev_b32 v249, 1, v249\n"
      "v_mfma_f32_16x16x32_bf16 v[44:47], v[148:151], a[236:239], v[44:47]\n"
      "v_sub_u32 v248, v248, v249\n"
      "v_and_b32 v249, 16, v242\n"
      "v_mfma_f32_16x16x32_bf16 v[48:51], v[148:151], a[240:243], v[48:51]\n"
      "v_mov_b32 v254, 0x3f80\n"
      "v_lshlrev_b32 v249, v249, v254\n"
      "v_mfma_f32_16x16x32_bf16 v[52:55], v[148:151], a[244:247], v[52:55]\n"
      "v_cmp_eq_u32 vcc, 0, v248\n"
      "s_nop 1\n"
      "v_cndmask_b32 v250, 0, v249, vcc\n"
      "v_mfma_f32_16x16x32_bf16 v[56:59], v[148:151], a[248:251], v[56:59]\n"
      "v_cmp_eq_u32 vcc, 1, v248\n"
      "s_nop 1\n"
      "v_cndmask_b32 v251, 0, v249, vcc\n"
      "v_mfma_f32_16x16x32_bf16 v[60:63], v[148:151], a[252:255], v[60:63]\n"
      "global_load_dwordx4 v[148:151], v240, s[74:75] offset:0 sc1\n"
      "v_mov_b32 v252, v250\n"
      "v_mfma_f32_16x16x32_bf16 v[64:67], v[152:155], a[192:195], v[64:67]\n"
      "v_mov_b32 v253, v251\n"
      "v_mov_b32 v248, 0\n"
      "v_mfma_f32_16x16x32_bf16 v[68:71], v[152:155], a[196:199], v[68:71]\n"
      "v_mov_b32 v249, 0\n"
      "v_mov_b32 v254, 0\n"
      "v_mfma_f32_16x16x32_bf16 v[72:75], v[152:155], a[200:203], v[72:75]\n"
      "v_mov_b32 v255, 0\n"
      "s_lshr_b32 s54, s33, 1\n"
      "v_mfma_f32_16x16x32_bf16 v[76:79], v[152:155], a[204:207], v[76:79]\n"
      "s_lshl1_add_u32 s54, s80, s54\n"
      "s_lshl_b32 s54, s54, 12\n"
      "s_add_u32 s54, s54, 262144\n"
      "v_mfma_f32_16x16x32_bf16 v[80:83], v[152:155], a[208:211], v[80:83]\n"
      "s_add_u32 s48, s34, 1\n"
      "v_add_u32 v200, s54, v242\n"
      "s_add_u32 s54, s41, 77824\n"
      "v_mfma_f32_16x16x32_bf16 v[84:87], v[152:155], a[212:215], v[84:87]\n"
      "s_and_b32 s48, s48, 3\n"
      "v_add_u32 v201, s54, v201\n"
      "v_and_b32 v202, 15, v243\n"
      "v_mfma_f32_16x16x32_bf16 v[88:91], v[152:155], a[216:219], v[88:91]\n"
      "s_mul_i32 s48, s48, 36864\n"
      "v_lshrrev_b32 v199, 5, v243\n"
      "v_lshl_or_b32 v202, v199, 4, v202\n"
      "v_mfma_f32_16x16x32_bf16 v[92:95], v[152:155], a[220:223], v[92:95]\n"
      "s_add_u32 s54, s41, 12288\n"
      "global_load_dwordx4 v[152:155], v241, s[14:15] offset:0 sc1\n"
      "v_lshl_add_u32 v202, v202, 1, s54\n"
      "v_mfma_f32_16x16x32_bf16 v[64:67], v[156:159], a[224:227], v[64:67]\n"
      "s_add_u32 s48, s48, 0\n"
      "v_and_b32 v199, 48, v243\n"
      "v_add_u32 v199, s54, v199\n"
      "v_mfma_f32_16x16x32_bf16 v[68:71], v[156:159], a[228:231], v[68:71]\n"
      "s_and_b32 s54, s33, 1\n"
      "s_lshl1_add_u32 s54, s81, s54\n"
      "s_lshl_b32 s54, s54, 15\n"
      "v_mfma_f32_16x16x32_bf16 v[72:75], v[156:159], a[232:235], v[72:75]\n"
      "s_lshr_b32 s55, s33, 1\n"
      "s_lshl1_add_u32 s55, s80, s55\n"
      "s_lshl_b32 s55, s55, 8\n"
      "v_mfma_f32_16x16x32_bf16 v[76:79], v[156:159], a[236:239], v[76:79]\n"
      "s_add_u32 s54, s54, s55\n"
      "s_add_u32 s54, s54, 524288\n"
      "v_add_u32 v203, s54, v203\n"
      "v_mfma_f32_16x16x32_bf16 v[80:83], v[156:159], a[240:243], v[80:83]\n"
      "s_add_u32 s52, s60, s48\n"
      "s_addc_u32 s53, s61, 0\n"
      "v_cvt_pk_bf16_f32 v12, v16, v17\n"
      "v_mfma_f32_16x16x32_bf16 v[84:87], v[156:159], a[244:247], v[84:87]\n"
      "s_lshl2_add_u32 s54, s36, s33\n"
      "ds_write_addtid_b32 v11 offset:768\n"
      "v_cvt_pk_bf16_f32 v13, v18, v19\n"
      "v_mfma_f32_16x16x32_bf16 v[88:91], v[156:159], a[248:251], v[88:91]\n"
      "s_lshl_b32 s55, s54, 15\n"
      "ds_write_addtid_b32 v12 offset:1024\n"
      "v_cvt_pk_bf16_f32 v14, v20, v21\n"
      "v_mfma_f32_16x16x32_bf16 v[92:95], v[156:159], a[252:255], v[92:95]\n"
      "s_waitcnt vmcnt(14)\n"
      "global_load_dwordx4 v[156:159], v241, s[74:75] offset:0 sc1\n"
      "s_lshl2_add_u32 s56, s35, s34\n"
      "v_mfma_f32_16x16x32_bf16 v[96:99], v[160:163], a[0:3], 0\n"
      "s_lshl_b32 s56, s56, 6\n"
      "ds_write_addtid_b32 v13 offset:1280\n"
      "v_cvt_pk_bf16_f32 v15, v22, v23\n"
      "v_mfma_f32_16x16x32_bf16 v[100:103], v[160:163], a[4:7], 0\n"
      "s_add_u32 s55, s55, s56\n"
      "ds_write_addtid_b32 v14 offset:1536\n"
      "v_cvt_pk_bf16_f32 v16, v24, v25\n"
      "v_mfma_f32_16x16x32_bf16 v[104:107], v[160:163], a[8:11], 0\n"
      "ds_write_addtid_b32 v15 offset:1792\n"
      "v_cvt_pk_bf16_f32 v17, v26, v27\n"
      "v_mfma_f32_16x16x32_bf16 v[108:111], v[160:163], a[12:15], 0\n"
      "ds_write_addtid_b32 v16 offset:2048\n"
      "v_cvt_pk_bf16_f32 v18, v28, v29\n"
      "v_mfma_f32_16x16x32_bf16 v[112:115], v[160:163], a[16:19], 0\n"
      "ds_write_addtid_b32 v17 offset:2304\n"
      "v_cvt_pk_bf16_f32 v19, v30, v31\n"
      "v_mfma_f32_16x16x32_bf16 v[116:119], v[160:163], a[20:23], 0\n"
      "ds_write_addtid_b32 v18 offset:2560\n"
      "v_and_b32 v209, 15, v243\n"
      "v_mfma_f32_16x16x32_bf16 v[120:123], v[160:163], a[24:27], 0\n"
      "ds_write_addtid_b32 v19 offset:2816\n"
      "v_cvt_pk_bf16_f32 v40, v40, v41\n"
      "v_mfma_f32_16x16x32_bf16 v[124:127], v[160:163], a[28:31], 0\n"
      "global_load_dwordx4 v[160:163], v240, s[10:11] offset:1024 sc1\n"
      "v_cvt_pk_bf16_f32 v41, v42, v43\n"
      "v_mfma_f32_16x16x32_bf16 v[96:99], v[164:167], a[32:35], v[96:99]\n"
      "ds_write_addtid_b32 v40 offset:3072\n"
      "v_cvt_pk_bf16_f32 v42, v44, v45\n"
      "v_mfma_f32_16x16x32_bf16 v[100:103], v[164:167], a[36:39], v[100:103]\n"
      "ds_write_addtid_b32 v41 offset:3328\n"
      "v_cvt_pk_bf16_f32 v43, v46, v47\n"
      "v_mfma_f32_16x16x32_bf16 v[104:107], v[164:167], a[40:43], v[104:107]\n"
      "ds_write_addtid_b32 v42 offset:3584\n"
      "v_cvt_pk_bf16_f32 v44, v48, v49\n"
      "v_mfma_f32_16x16x32_bf16 v[108:111], v[164:167], a[44:47], v[108:111]\n"
      "ds_write_addtid_b32 v43 offset:3840\n"
      "v_cvt_pk_bf16_f32 v45, v50, v51\n"
      "v_mfma_f32_16x16x32_bf16 v[112:115], v[164:167], a[48:51], v[112:115]\n"
      "ds_write_addtid_b32 v44 offset:4096\n"
      "v_cvt_pk_bf16_f32 v46, v52, v53\n"
      "v_mfma_f32_16x16x32_bf16 v[116:119], v[164:167], a[52:55], v[116:119]\n"
      "ds_write_addtid_b32 v45 offset:4352\n"
      "v_cvt_pk_bf16_f32 v47, v54, v55\n"
      "v_mfma_f32_16x16x32_bf16 v[120:123], v[164:167], a[56:59], v[120:123]\n"
      "ds_write_addtid_b32 v46 offset:4608\n"
      "v_cvt_pk_bf16_f32 v48, v56, v57\n"
      "v_mfma_f32_16x16x32_bf16 v[124:127], v[164:167], a[60:63], v[124:127]\n"
      "s_waitcnt vmcnt(14)\n"
      "global_load_dwordx4 v[164:167], v240, s[72:73] offset:1024 sc1\n"
      "v_and_b32 v208, 48, v243\n"
      "v_mfma_f32_16x16x32_bf16 v[96:99], v[168:171], a[64:67], v[96:99]\n"
      "ds_write_addtid_b32 v47 offset:4864\n"
      "v_cvt_pk_bf16_f32 v49, v58, v59\n"
      "v_mfma_f32_16x16x32_bf16 v[100:103], v[168:171], a[68:71], v[100:103]\n"
      "ds_write_addtid_b32 v48 offset:5120\n"
      "v_cvt_pk_bf16_f32 v50, v60, v61\n"
      "v_mfma_f32_16x16x32_bf16 v[104:107], v[168:171], a[72:75], v[104:107]\n"
      "ds_write_addtid_b32 v49 offset:5376\n"
      "v_cvt_pk_bf16_f32 v51, v62, v63\n"
      "v_mfma_f32_16x16x32_bf16 v[108:111], v[168:171], a[76:79], v[108:111]\n"
      "ds_write_addtid_b32 v50 offset:5632\n"
      "v_lshl_or_b32 v208, v209, 10, v208\n"
      "v_mfma_f32_16x16x32_bf16 v[112:115], v[168:171], a[80:83], v[112:115]\n"
      "ds_write_addtid_b32 v51 offset:5888\n"
      "v_add_u32 v208, s55, v208\n"
      "v_mfma_f32_16x16x32_bf16 v[116:119], v[168:171], a[84:87], v[116:119]\n"
      "v_add_u32 v210, 16384, v208\n"
      "global_load_dwordx4 v[216:219], v208, s[16:17]\n"
      "v_mfma_f32_16x16x32_bf16 v[120:123], v[168:171], a[88:91], v[120:123]\n"
      "global_load_dwordx4 v[220:223], v210, s[16:17]\n"
      "s_lshr_b32 s55, s34, 1\n"
      "v_mfma_f32_16x16x32_bf16 v[124:127], v[168:171], a[92:95], v[124:127]\n"
      "s_lshl2_add_u32 s55, s55, s35\n"
      "global_load_dwordx4 v[168:171], v241, s[10:11] offset:1024 sc1\n"
      "s_lshl_b32 s55, s55, 15\n"
      "v_mfma_f32_16x16x32_bf16 v[96:99], v[172:175], a[96:99], v[96:99]\n"
      "s_lshl_b32 s54, s54, 9\n"
      "s_waitcnt lgkmcnt(0)\n"
      "s_barrier\n"
      "v_mfma_f32_16x16x32_bf16 v[100:103], v[172:175], a[100:103], v[100:103]\n"
      "s_add_u32 s54, s54, s55\n"
      "ds_read2st64_b32 v[20:21], v244 offset0:0 offset1:1\n"
      "s_add_u32 s54, s54, 786432\n"
      "v_mfma_f32_16x16x32_bf16 v[104:107], v[172:175], a[104:107], v[104:107]\n"
      "ds_read2st64_b32 v[22:23], v245 offset0:4 offset1:5\n"
      "v_lshl_add_u32 v209, v209, 4, s54\n"
      "v_mfma_f32_16x16x32_bf16 v[108:111], v[172:175], a[108:111], v[108:111]\n"
      "ds_read2st64_b32 v[24:25], v244 offset0:2 offset1:3\n"
      "ds_read2st64_b32 v[26:27], v245 offset0:6 offset1:7\n"
      "v_mfma_f32_16x16x32_bf16 v[112:115], v[172:175], a[112:115], v[112:115]\n"
      "ds_read2st64_b32 v[28:29], v246 offset0:8 offset1:9\n"
      "ds_read2st64_b32 v[30:31], v246 offset0:10 offset1:11\n"
      "v_mfma_f32_16x16x32_bf16 v[116:119], v[172:175], a[116:119], v[116:119]\n"
      "ds_read2st64_b32 v[52:53], v244 offset0:12 offset1:13\n"
      "ds_read2st64_b32 v[54:55], v245 offset0:16 offset1:17\n"
      "v_mfma_f32_16x16x32_bf16 v[120:123], v[172:175], a[120:123], v[120:123]\n"
      "ds_read2st64_b32 v[56:57], v244 offset0:14 offset1:15\n"
      "ds_read2st64_b32 v[58:59], v245 offset0:18 offset1:19\n"
      "v_mfma_f32_16x16x32_bf16 v[124:127], v[172:175], a[124:127], v[124:127]\n"
      "s_waitcnt vmcnt(16)\n"
      "global_load_dwordx4 v[172:175], v241, s[72:73] offset:1024 sc1\n"
      "ds_read2st64_b32 v[60:61], v246 offset0:20 offset1:21\n"
      "v_mfma_f32_16x16x32_bf16 v[96:99], v[176:179], a[128:131], v[96:99]\n"
      "ds_read2st64_b32 v[62:63], v246 offset0:22 offset1:23\n"
      "v_cvt_pk_bf16_f32 v72, v72, v73\n"
      "v_mfma_f32_16x16x32_bf16 v[100:103], v[176:179], a[132:135], v[100:103]\n"
      "s_waitcnt lgkmcnt(6)\n"
      "v_mfma_f32_16x16x32_bf16 v[0:3], v[250:253], v[20:23], v[0:3]\n"
      "v_cvt_pk_bf16_f32 v73, v74, v75\n"
      "v_mfma_f32_16x16x32_bf16 v[104:107], v[176:179], a[136:139], v[104:107]\n"
      "v_mfma_f32_16x16x32_bf16 v[4:7], v[250:253], v[24:27], v[4:7]\n"
      "v_mfma_f32_16x16x32_bf16 v[0:3], v[252:255], v[28:31], v[0:3]\n"
      "v_mfma_f32_16x16x32_bf16 v[108:111], v[176:179], a[140:143], v[108:111]\n"
      "v_mfma_f32_16x16x32_bf16 v[4:7], v[248:251], v[28:31], v[4:7]\n"
      "v_mov_b32 v11, s37\n"
      "v_mfma_f32_16x16x32_bf16 v[112:115], v[176:179], a[144:147], v[112:115]\n"
      "ds_write_addtid_b32 v72 offset:6144\n"
      "v_cvt_pk_bf16_f32 v74, v76, v77\n"
      "v_mfma_f32_16x16x32_bf16 v[116:119], v[176:179], a[148:151], v[116:119]\n"
      "ds_write_addtid_b32 v73 offset:6400\n"
      "v_cvt_pk_bf16_f32 v8, v0, v1\n"
      "v_mfma_f32_16x16x32_bf16 v[120:123], v[176:179], a[152:155], v[120:123]\n"
      "global_load_dwordx2 v[204:205], v209, s[58:59] offset:0 sc1\n"
      "v_cvt_pk_bf16_f32 v9, v2, v3\n"
      "v_mfma_f32_16x16x32_bf16 v[124:127], v[176:179], a[156:159], v[124:127]\n"
      "global_load_dwordx4 v[176:179], v240, s[14:15] offset:1024 sc1\n"
      "v_cvt_pk_bf16_f32 v10, v4, v5\n"
      "v_mfma_f32_16x16x32_bf16 v[96:99], v[180:183], a[160:163], v[96:99]\n"
      "global_load_dwordx2 v[206:207], v209, s[58:59] offset:256 sc1\n"
      "v_cvt_pk_bf16_f32 v247, v6, v7\n"
      "v_mfma_f32_16x16x32_bf16 v[100:103], v[180:183], a[164:167], v[100:103]\n"
      "s_add_u32 s68, s20, 16384\n"
      "s_addc_u32 s69, s21, 0\n"
      "global_store_dwordx4 v242, v[8:11], s[52:53] offset:0 sc1\n"
      "v_mfma_f32_16x16x32_bf16 v[104:107], v[180:183], a[168:171], v[104:107]\n"
      "s_waitcnt lgkmcnt(2)\n"
      "v_mfma_f32_16x16x32_bf16 v[32:35], v[250:253], v[52:55], v[32:35]\n"
      "v_cvt_pk_bf16_f32 v75, v78, v79\n"
      "v_mfma_f32_16x16x32_bf16 v[108:111], v[180:183], a[172:175], v[108:111]\n"
      "v_mfma_f32_16x16x32_bf16 v[36:39], v[250:253], v[56:59], v[36:39]\n"
      "v_lshrrev_b32 v208, 10, v208\n"
      "v_mfma_f32_16x16x32_bf16 v[112:115], v[180:183], a[176:179], v[112:115]\n"
      "v_mfma_f32_16x16x32_bf16 v[32:35], v[252:255], v[60:63], v[32:35]\n"
      "v_subrev_u32 v208, s42, v208\n"
      "v_mfma_f32_16x16x32_bf16 v[116:119], v[180:183], a[180:183], v[116:119]\n"
      "v_mfma_f32_16x16x32_bf16 v[36:39], v[248:251], v[60:63], v[36:39]\n"
      "v_mov_b32 v43, s37\n"
      "v_mfma_f32_16x16x32_bf16 v[120:123], v[180:183], a[184:187], v[120:123]\n"
      "ds_write_addtid_b32 v74 offset:6656\n"
      "v_mov_b32 v44, v247\n"
      "v_mfma_f32_16x16x32_bf16 v[124:127], v[180:183], a[188:191], v[124:127]\n"
      "s_waitcnt vmcnt(19)\n"
      "global_load_dwordx4 v[180:183], v240, s[74:75] offset:1024 sc1\n"
      "v_mov_b32 v47, s37\n"
      "v_mfma_f32_16x16x32_bf16 v[96:99], v[184:187], a[192:195], v[96:99]\n"
      "v_cvt_pk_bf16_f32 v76, v80, v81\n"
      "v_mfma_f32_16x16x32_bf16 v[100:103], v[184:187], a[196:199], v[100:103]\n"
      "ds_write_addtid_b32 v75 offset:6912\n"
      "v_cvt_pk_bf16_f32 v40, v32, v33\n"
      "v_mfma_f32_16x16x32_bf16 v[104:107], v[184:187], a[200:203], v[104:107]\n"
      "v_cvt_pk_bf16_f32 v41, v34, v35\n"
      "v_cvt_pk_bf16_f32 v42, v36, v37\n"
      "v_mfma_f32_16x16x32_bf16 v[108:111], v[184:187], a[204:207], v[108:111]\n"
      "v_cvt_pk_bf16_f32 v45, v38, v39\n"
      "v_cvt_pk_bf16_f32 v77, v82, v83\n"
      "v_mfma_f32_16x16x32_bf16 v[112:115], v[184:187], a[208:211], v[112:115]\n"
      "global_store_dwordx4 v242, v[40:43], s[52:53] offset:1024 sc1\n"
      "v_mfma_f32_16x16x32_bf16 v[116:119], v[184:187], a[212:215], v[116:119]\n"
      "global_store_dwordx4 v242, v[44:47], s[52:53] offset:2048 sc1\n"
      "s_add_u32 s48, s34, 2\n"
      "v_mfma_f32_16x16x32_bf16 v[120:123], v[184:187], a[216:219], v[120:123]\n"
      "s_and_b32 s48, s48, 3\n"
      "ds_write_addtid_b32 v76 offset:7168\n"
      "s_mul_i32 s48, s48, 36864\n"
      "v_mfma_f32_16x16x32_bf16 v[124:127], v[184:187], a[220:223], v[124:127]\n"
      "s_add_u32 s48, s48, 12288\n"
      "global_load_dwordx4 v[184:187], v241, s[14:15] offset:1024 sc1\n"
      "s_add_u32 s52, s60, s48\n"
      "s_addc_u32 s53, s61, 0\n"
      "v_mfma_f32_16x16x32_bf16 v[96:99], v[188:191], a[224:227], v[96:99]\n"
      "v_cvt_pk_bf16_f32 v78, v84, v85\n"
      "v_mfma_f32_16x16x32_bf16 v[100:103], v[188:191], a[228:231], v[100:103]\n"
      "ds_write_addtid_b32 v77 offset:7424\n"
      "v_cvt_pk_bf16_f32 v79, v86, v87\n"
      "v_mfma_f32_16x16x32_bf16 v[104:107], v[188:191], a[232:235], v[104:107]\n"
      "ds_write_addtid_b32 v78 offset:7680\n"
      "v_cvt_pk_bf16_f32 v80, v88, v89\n"
      "v_mfma_f32_16x16x32_bf16 v[108:111], v[188:191], a[236:239], v[108:111]\n"
      "ds_write_addtid_b32 v79 offset:7936\n"
      "v_cvt_pk_bf16_f32 v81, v90, v91\n"
      "v_mfma_f32_16x16x32_bf16 v[112:115], v[188:191], a[240:243], v[112:115]\n"
      "ds_write_addtid_b32 v80 offset:8192\n"
      "v_cvt_pk_bf16_f32 v82, v92, v93\n"
      "v_mfma_f32_16x16x32_bf16 v[116:119], v[188:191], a[244:247], v[116:119]\n"
      "ds_write_addtid_b32 v81 offset:8448\n"
      "v_cvt_pk_bf16_f32 v83, v94, v95\n"
      "v_mfma_f32_16x16x32_bf16 v[120:123], v[188:191], a[248:251], v[120:123]\n"
      "ds_write_addtid_b32 v82 offset:8704\n"
      "v_mfma_f32_16x16x32_bf16 v[124:127], v[188:191], a[252:255], v[124:127]\n"
      "s_waitcnt vmcnt(21)\n"
      "global_load_dwordx4 v[188:191], v241, s[74:75] offset:1024 sc1\n"
      "v_mfma_f32_16x16x32_bf16 v[0:3], v[128:131], a[0:3], 0\n"
      "ds_write_addtid_b32 v83 offset:8960\n"
      "v_cvt_pk_bf16_f32 v104, v104, v105\n"
      "v_mfma_f32_16x16x32_bf16 v[4:7], v[128:131], a[4:7], 0\n"
      "v_cvt_pk_bf16_f32 v105, v106, v107\n"
      "v_mfma_f32_16x16x32_bf16 v[8:11], v[128:131], a[8:11], 0\n"
      "ds_write_addtid_b32 v104 offset:9216\n"
      "v_cvt_pk_bf16_f32 v106, v108, v109\n"
      "v_mfma_f32_16x16x32_bf16 v[12:15], v[128:131], a[12:15], 0\n"
      "ds_write_addtid_b32 v105 offset:9472\n"
      "v_cvt_pk_bf16_f32 v107, v110, v111\n"
      "v_mfma_f32_16x16x32_bf16 v[16:19], v[128:131], a[16:19], 0\n"
      "ds_write_addtid_b32 v106 offset:9728\n"
      "v_cvt_pk_bf16_f32 v108, v112, v113\n"
      "v_mfma_f32_16x16x32_bf16 v[20:23], v[128:131], a[20:23], 0\n"
      "ds_write_addtid_b32 v107 offset:9984\n"
      "v_cvt_pk_bf16_f32 v109, v114, v115\n"
      "v_mfma_f32_16x16x32_bf16 v[24:27], v[128:131], a[24:27], 0\n"
      "ds_write_addtid_b32 v108 offset:10240\n"
      "v_cvt_pk_bf16_f32 v110, v116, v117\n"
      "v_mfma_f32_16x16x32_bf16 v[28:31], v[128:131], a[28:31], 0\n"
      "global_load_dwordx4 v[128:131], v240, s[10:11] offset:2048 sc1\n"
      "v_mfma_f32_16x16x32_bf16 v[0:3], v[132:135], a[32:35], v[0:3]\n"
      "ds_write_addtid_b32 v109 offset:10496\n"
      "v_cvt_pk_bf16_f32 v111, v118, v119\n"
      "v_mfma_f32_16x16x32_bf16 v[4:7], v[132:135], a[36:39], v[4:7]\n"
      "ds_write_addtid_b32 v110 offset:10752\n"
      "v_cvt_pk_bf16_f32 v112, v120, v121\n"
      "v_mfma_f32_16x16x32_bf16 v[8:11], v[132:135], a[40:43], v[8:11]\n"
      "ds_write_addtid_b32 v111 offset:11008\n"
      "v_cvt_pk_bf16_f32 v113, v122, v123\n"
      "v_mfma_f32_16x16x32_bf16 v[12:15], v[132:135], a[44:47], v[12:15]\n"
      "ds_write_addtid_b32 v112 offset:11264\n"
      "v_cvt_pk_bf16_f32 v114, v124, v125\n"
      "v_mfma_f32_16x16x32_bf16 v[16:19], v[132:135], a[48:51], v[16:19]\n"
      "ds_write_addtid_b32 v113 offset:11520\n"
      "v_cvt_pk_bf16_f32 v115, v126, v127\n"
      "v_mfma_f32_16x16x32_bf16 v[20:23], v[132:135], a[52:55], v[20:23]\n"
      "ds_write_addtid_b32 v114 offset:11776\n"
      "v_mfma_f32_16x16x32_bf16 v[24:27], v[132:135], a[56:59], v[24:27]\n"
      "ds_write_addtid_b32 v115 offset:12032\n"
      "v_mfma_f32_16x16x32_bf16 v[28:31], v[132:135], a[60:63], v[28:31]\n"
      "s_waitcnt vmcnt(21)\n"
      "global_load_dwordx4 v[132:135], v240, s[72:73] offset:2048 sc1\n"
      "v_mfma_f32_16x16x32_bf16 v[0:3], v[136:139], a[64:67], v[0:3]\n"
      "v_mfma_f32_16x16x32_bf16 v[4:7], v[136:139], a[68:71], v[4:7]\n"
      "s_waitcnt lgkmcnt(0)\n"
      "s_barrier\n"
      "v_mfma_f32_16x16x32_bf16 v[8:11], v[136:139], a[72:75], v[8:11]\n"
      "ds_read2st64_b32 v[84:85], v244 offset0:24 offset1:25\n"
      "v_mfma_f32_16x16x32_bf16 v[12:15], v[136:139], a[76:79], v[12:15]\n"
      "ds_read2st64_b32 v[86:87], v245 offset0:28 offset1:29\n"
      "v_mfma_f32_16x16x32_bf16 v[16:19], v[136:139], a[80:83], v[16:19]\n"
      "ds_read2st64_b32 v[88:89], v244 offset0:26 offset1:27\n"
      "v_mfma_f32_16x16x32_bf16 v[20:23], v[136:139], a[84:87], v[20:23]\n"
      "ds_read2st64_b32 v[90:91], v245 offset0:30 offset1:31\n"
      "v_mfma_f32_16x16x32_bf16 v[24:27], v[136:139], a[88:91], v[24:27]\n"
      "ds_read2st64_b32 v[92:93], v246 offset0:32 offset1:33\n"
      "v_mfma_f32_16x16x32_bf16 v[28:31], v[136:139], a[92:95], v[28:31]\n"
      "global_load_dwordx4 v[136:139], v241, s[10:11] offset:2048 sc1\n"
      "v_mfma_f32_16x16x32_bf16 v[0:3], v[140:143], a[96:99], v[0:3]\n"
      "ds_read2st64_b32 v[94:95], v246 offset0:34 offset1:35\n"
      "v_mfma_f32_16x16x32_bf16 v[4:7], v[140:143], a[100:103], v[4:7]\n"
      "ds_read2st64_b32 v[116:117], v244 offset0:36 offset1:37\n"
      "v_mfma_f32_16x16x32_bf16 v[8:11], v[140:143], a[104:107], v[8:11]\n"
      "ds_read2st64_b32 v[118:119], v245 offset0:40 offset1:41\n"
      "v_mfma_f32_16x16x32_bf16 v[12:15], v[140:143], a[108:111], v[12:15]\n"
      "ds_read2st64_b32 v[120:121], v244 offset0:38 offset1:39\n"
      "v_mfma_f32_16x16x32_bf16 v[16:19], v[140:143], a[112:115], v[16:19]\n"
      "ds_read2st64_b32 v[122:123], v245 offset0:42 offset1:43\n"
      "v_mfma_f32_16x16x32_bf16 v[20:23], v[140:143], a[116:119], v[20:23]\n"
      "s_waitcnt lgkmcnt(4)\n"
      "v_mfma_f32_16x16x32_bf16 v[64:67], v[250:253], v[84:87], v[64:67]\n"
      "v_mfma_f32_16x16x32_bf16 v[24:27], v[140:143], a[120:123], v[24:27]\n"
      "v_mfma_f32_16x16x32_bf16 v[68:71], v[250:253], v[88:91], v[68:71]\n"
      "v_mfma_f32_16x16x32_bf16 v[28:31], v[140:143], a[124:127], v[28:31]\n"
      "s_waitcnt vmcnt(20)\n"
      "global_load_dwordx4 v[140:143], v241, s[72:73] offset:2048 sc1\n"
      "v_mfma_f32_16x16x32_bf16 v[0:3], v[144:147], a[128:131], v[0:3]\n"
      "v_mfma_f32_16x16x32_bf16 v[64:67], v[252:255], v[92:95], v[64:67]\n"
      "v_mfma_f32_16x16x32_bf16 v[4:7], v[144:147], a[132:135], v[4:7]\n"
      "v_mfma_f32_16x16x32_bf16 v[68:71], v[248:251], v[92:95], v[68:71]\n"
      "v_mov_b32 v75, s37\n"
      "v_mfma_f32_16x16x32_bf16 v[8:11], v[144:147], a[136:139], v[8:11]\n"
      "ds_read2st64_b32 v[124:125], v246 offset0:44 offset1:45\n"
      "v_mfma_f32_16x16x32_bf16 v[12:15], v[144:147], a[140:143], v[12:15]\n"
      "ds_read2st64_b32 v[126:127], v246 offset0:46 offset1:47\n"
      "s_nop 0\n"
      "v_cvt_pk_bf16_f32 v72, v64, v65\n"
      "v_mfma_f32_16x16x32_bf16 v[16:19], v[144:147], a[144:147], v[16:19]\n"
      "s_waitcnt vmcnt(10)\n"
      "v_bitop3_b32 v210, v205, v207, s37 bitop3:0x7e\n"
      "v_cmp_ne_u32 vcc, 0, v210\n"
      "s_nop 1\n"
      "s_cbranch_vccnz .Ls_s13_%=\n"
      ".Ls_o14_%=:\n"
      "v_mfma_f32_16x16x32_bf16 v[20:23], v[144:147], a[148:151], v[20:23]\n"
      "v_cvt_pk_bf16_f32 v73, v66, v67\n"
      "v_cvt_pk_bf16_f32 v74, v68, v69\n"
      "v_mfma_f32_16x16x32_bf16 v[24:27], v[144:147], a[152:155], v[24:27]\n"
      "v_cvt_pk_bf16_f32 v247, v70, v71\n"
      "v_mov_b32 v209, s39\n"
      "v_mfma_f32_16x16x32_bf16 v[28:31], v[144:147], a[156:159], v[28:31]\n"
      "global_load_dwordx4 v[144:147], v240, s[14:15] offset:2048 sc1\n"
      "v_mfma_f32_16x16x32_bf16 v[0:3], v[148:151], a[160:163], v[0:3]\n"
      "global_store_dwordx4 v242, v[72:75], s[52:53] offset:0 sc1\n"
      "s_waitcnt lgkmcnt(0)\n"
      "v_mfma_f32_16x16x32_bf16 v[4:7], v[148:151], a[164:167], v[4:7]\n"
      "v_mfma_f32_16x16x32_bf16 v[96:99], v[250:253], v[116:119], v[96:99]\n"
      "v_mfma_f32_16x16x32_bf16 v[8:11], v[148:151], a[168:171], v[8:11]\n"
      "v_mfma_f32_16x16x32_bf16 v[100:103], v[250:253], v[120:123], v[100:103]\n"
      "v_mfma_f32_16x16x32_bf16 v[12:15], v[148:151], a[172:175], v[12:15]\n"
      "v_mfma_f32_16x16x32_bf16 v[96:99], v[252:255], v[124:127], v[96:99]\n"
      "v_mfma_f32_16x16x32_bf16 v[16:19], v[148:151], a[176:179], v[16:19]\n"
      "v_mfma_f32_16x16x32_bf16 v[100:103], v[248:251], v[124:127], v[100:103]\n"
      "v_mov_b32 v107, s37\n"
      "v_mfma_f32_16x16x32_bf16 v[20:23], v[148:151], a[180:183], v[20:23]\n"
      "v_cmp_eq_u32 vcc, 0, v208\n"
      "s_nop 1\n"
      "v_cndmask_b32 v204, v204, v209, vcc\n"
      "v_mfma_f32_16x16x32_bf16 v[24:27], v[148:151], a[184:187], v[24:27]\n"
      "v_cmp_eq_u32 vcc, -16, v208\n"
      "s_nop 1\n"
      "v_cndmask_b32 v206, v206, v209, vcc\n"
      "v_mfma_f32_16x16x32_bf16 v[28:31], v[148:151], a[188:191], v[28:31]\n"
      "global_load_dwordx4 v[148:151], v240, s[74:75] offset:2048 sc1\n"
      "v_mov_b32 v108, v247\n"
      "v_mfma_f32_16x16x32_bf16 v[0:3], v[152:155], a[192:195], v[0:3]\n"
      "v_mov_b32 v111, s37\n"
      "v_sub_f32 v205, 1.0, v204\n"
      "v_mfma_f32_16x16x32_bf16 v[4:7], v[152:155], a[196:199], v[4:7]\n"
      "v_cvt_pk_bf16_f32 v205, v205, v205\n"
      "v_and_b32 v205, 0xffff0000, v205\n"
      "v_mfma_f32_16x16x32_bf16 v[8:11], v[152:155], a[200:203], v[8:11]\n"
      "v_cvt_pk_bf16_f32 v104, v96, v97\n"
      "v_cvt_pk_bf16_f32 v105, v98, v99\n"
      "v_mfma_f32_16x16x32_bf16 v[12:15], v[152:155], a[204:207], v[12:15]\n"
      "v_cvt_pk_bf16_f32 v106, v100, v101\n"
      "v_cvt_pk_bf16_f32 v109, v102, v103\n"
      "v_mfma_f32_16x16x32_bf16 v[16:19], v[152:155], a[208:211], v[16:19]\n"
      "global_load_dwordx4 v[224:227], v200, s[58:59] offset:0 sc1\n"
      "v_sub_f32 v207, 1.0, v206\n"
      "v_mfma_f32_16x16x32_bf16 v[20:23], v[152:155], a[212:215], v[20:23]\n"
      "global_load_dwordx4 v[228:231], v200, s[58:59] offset:1024 sc1\n"
      "v_cvt_pk_bf16_f32 v207, v207, v207\n"
      "v_mfma_f32_16x16x32_bf16 v[24:27], v[152:155], a[216:219], v[24:27]\n"
      "global_load_dwordx4 v[232:235], v200, s[58:59] offset:2048 sc1\n"
      "v_and_b32 v207, 0xffff0000, v207\n"
      "v_mfma_f32_16x16x32_bf16 v[28:31], v[152:155], a[220:223], v[28:31]\n"
      "global_load_dwordx4 v[152:155], v241, s[14:15] offset:2048 sc1\n"
      "v_lshlrev_b32 v208, 16, v216\n"
      "v_mfma_f32_16x16x32_bf16 v[0:3], v[156:159], a[224:227], v[0:3]\n"
      "global_load_dwordx4 v[236:239], v200, s[58:59] offset:3072 sc1\n"
      "v_and_b32 v209, 0xffff0000, v216\n"
      "v_mfma_f32_16x16x32_bf16 v[4:7], v[156:159], a[228:231], v[4:7]\n"
      "global_store_dwordx4 v242, v[104:107], s[52:53] offset:1024 sc1\n"
      "v_mul_f32 v208, v205, v208\n"
      "v_mfma_f32_16x16x32_bf16 v[8:11], v[156:159], a[232:235], v[8:11]\n"
      "global_store_dwordx4 v242, v[108:111], s[52:53] offset:2048 sc1\n"
      "s_add_u32 s48, s34, 3\n"
      "v_mfma_f32_16x16x32_bf16 v[12:15], v[156:159], a[236:239], v[12:15]\n"
      "s_and_b32 s48, s48, 3\n"
      "s_mul_i32 s48, s48, 36864\n"
      "s_add_u32 s48, s48, 24576\n"
      "v_mfma_f32_16x16x32_bf16 v[16:19], v[156:159], a[240:243], v[16:19]\n"
      "s_add_u32 s52, s60, s48\n"
      "s_addc_u32 s53, s61, 0\n"
      "v_mul_f32 v209, v205, v209\n"
      "v_mfma_f32_16x16x32_bf16 v[20:23], v[156:159], a[244:247], v[20:23]\n"
      "v_lshlrev_b32 v212, 16, v220\n"
      "v_and_b32 v213, 0xffff0000, v220\n"
      "v_mfma_f32_16x16x32_bf16 v[24:27], v[156:159], a[248:251], v[24:27]\n"
      "v_cvt_pk_bf16_f32 v8, v8, v9\n"
      "v_cvt_pk_bf16_f32 v9, v10, v11\n"
      "v_mfma_f32_16x16x32_bf16 v[28:31], v[156:159], a[252:255], v[28:31]\n"
      "global_load_dwordx4 v[156:159], v241, s[74:75] offset:2048 sc1\n"
      "v_mul_f32 v212, v207, v212\n"
      "v_mfma_f32_16x16x32_bf16 v[32:35], v[160:163], a[0:3], 0\n"
      "ds_write_addtid_b32 v8 offset:0\n"
      "v_cvt_pk_bf16_f32 v10, v12, v13\n"
      "v_mfma_f32_16x16x32_bf16 v[36:39], v[160:163], a[4:7], 0\n"
      "ds_write_addtid_b32 v9 offset:256\n"
      "v_cvt_pk_bf16_f32 v11, v14, v15\n"
      "v_mfma_f32_16x16x32_bf16 v[40:43], v[160:163], a[8:11], 0\n"
      "ds_write_addtid_b32 v10 offset:512\n"
      "v_cvt_pk_bf16_f32 v12, v16, v17\n"
      "v_mfma_f32_16x16x32_bf16 v[44:47], v[160:163], a[12:15], 0\n"
      "ds_write_addtid_b32 v11 offset:768\n"
      "v_cvt_pk_bf16_f32 v13, v18, v19\n"
      "v_mfma_f32_16x16x32_bf16 v[48:51], v[160:163], a[16:19], 0\n"
      "ds_write_addtid_b32 v12 offset:1024\n"
      "v_cvt_pk_bf16_f32 v14, v20, v21\n"
      "v_mfma_f32_16x16x32_bf16 v[52:55], v[160:163], a[20:23], 0\n"
      "ds_write_addtid_b32 v13 offset:1280\n"
      "v_cvt_pk_bf16_f32 v15, v22, v23\n"
      "v_mfma_f32_16x16x32_bf16 v[56:59], v[160:163], a[24:27], 0\n"
      "ds_write_addtid_b32 v14 offset:1536\n"
      "v_cvt_pk_bf16_f32 v16, v24, v25\n"
      "v_mfma_f32_16x16x32_bf16 v[60:63], v[160:163], a[28:31], 0\n"
      "global_load_dwordx4 v[160:163], v240, s[10:11] offset:3072 sc1\n"
      "v_mul_f32 v213, v207, v213\n"
      "v_mfma_f32_16x16x32_bf16 v[32:35], v[164:167], a[32:35], v[32:35]\n"
      "ds_write_addtid_b32 v15 offset:1792\n"
      "v_cvt_pk_bf16_f32 v17, v26, v27\n"
      "v_mfma_f32_16x16x32_bf16 v[36:39], v[164:167], a[36:39], v[36:39]\n"
      "ds_write_addtid_b32 v16 offset:2048\n"
      "v_cvt_pk_bf16_f32 v18, v28, v29\n"
      "v_mfma_f32_16x16x32_bf16 v[40:43], v[164:167], a[40:43], v[40:43]\n"
      "ds_write_addtid_b32 v17 offset:2304\n"
      "v_cvt_pk_bf16_f32 v19, v30, v31\n"
      "v_mfma_f32_16x16x32_bf16 v[44:47], v[164:167], a[44:47], v[44:47]\n"
      "ds_write_addtid_b32 v18 offset:2560\n"
      "v_lshlrev_b32 v210, 16, v217\n"
      "v_mfma_f32_16x16x32_bf16 v[48:51], v[164:167], a[48:51], v[48:51]\n"
      "ds_write_addtid_b32 v19 offset:2816\n"
      "v_and_b32 v211, 0xffff0000, v217\n"
      "v_mfma_f32_16x16x32_bf16 v[52:55], v[164:167], a[52:55], v[52:55]\n"
      "v_mul_f32 v210, v205, v210\n"
      "v_mul_f32 v211, v205, v211\n"
      "v_mfma_f32_16x16x32_bf16 v[56:59], v[164:167], a[56:59], v[56:59]\n"
      "v_lshlrev_b32 v214, 16, v221\n"
      "v_and_b32 v215, 0xffff0000, v221\n"
      "v_mfma_f32_16x16x32_bf16 v[60:63], v[164:167], a[60:63], v[60:63]\n"
      "global_load_dwordx4 v[164:167], v240, s[72:73] offset:3072 sc1\n"
      "v_mul_f32 v214, v207, v214\n"
      "v_mfma_f32_16x16x32_bf16 v[32:35], v[168:171], a[64:67], v[32:35]\n"
      "s_waitcnt lgkmcnt(0)\n"
      "s_barrier\n"
      "v_mfma_f32_16x16x32_bf16 v[36:39], v[168:171], a[68:71], v[36:39]\n"
      "ds_read2st64_b32 v[20:21], v244 offset0:0 offset1:1\n"
      "v_mul_f32 v215, v207, v215\n"
      "v_mfma_f32_16x16x32_bf16 v[40:43], v[168:171], a[72:75], v[40:43]\n"
      "ds_read2st64_b32 v[22:23], v245 offset0:4 offset1:5\n"
      "v_lshlrev_b32 v216, 16, v218\n"
      "v_mfma_f32_16x16x32_bf16 v[44:47], v[168:171], a[76:79], v[44:47]\n"
      "ds_read2st64_b32 v[24:25], v244 offset0:2 offset1:3\n"
      "v_and_b32 v217, 0xffff0000, v218\n"
      "v_mfma_f32_16x16x32_bf16 v[48:51], v[168:171], a[80:83], v[48:51]\n"
      "ds_read2st64_b32 v[26:27], v245 offset0:6 offset1:7\n"
      "v_mul_f32 v216, v205, v216\n"
      "v_mfma_f32_16x16x32_bf16 v[52:55], v[168:171], a[84:87], v[52:55]\n"
      "ds_read2st64_b32 v[28:29], v246 offset0:8 offset1:9\n"
      "v_mul_f32 v217, v205, v217\n"
      "v_mfma_f32_16x16x32_bf16 v[56:59], v[168:171], a[88:91], v[56:59]\n"
      "ds_read2st64_b32 v[30:31], v246 offset0:10 offset1:11\n"
      "v_lshlrev_b32 v220, 16, v222\n"
      "v_mfma_f32_16x16x32_bf16 v[60:63], v[168:171], a[92:95], v[60:63]\n"
      "global_load_dwordx4 v[168:171], v241, s[10:11] offset:3072 sc1\n"
      "v_and_b32 v221, 0xffff0000, v222\n"
      "v_mfma_f32_16x16x32_bf16 v[32:35], v[172:175], a[96:99], v[32:35]\n"
      "v_mul_f32 v220, v207, v220\n"
      "v_mul_f32 v221, v207, v221\n"
      "v_mfma_f32_16x16x32_bf16 v[36:39], v[172:175], a[100:103], v[36:39]\n"
      "v_lshlrev_b32 v218, 16, v219\n"
      "v_and_b32 v219, 0xffff0000, v219\n"
      "v_mfma_f32_16x16x32_bf16 v[40:43], v[172:175], a[104:107], v[40:43]\n"
      "v_mul_f32 v218, v205, v218\n"
      "v_mul_f32 v219, v205, v219\n"
      "v_mfma_f32_16x16x32_bf16 v[44:47], v[172:175], a[108:111], v[44:47]\n"
      "s_waitcnt lgkmcnt(0)\n"
      "v_mfma_f32_16x16x32_bf16 v[0:3], v[250:253], v[20:23], v[0:3]\n"
      "v_lshlrev_b32 v222, 16, v223\n"
      "v_mfma_f32_16x16x32_bf16 v[48:51], v[172:175], a[112:115], v[48:51]\n"
      "v_mfma_f32_16x16x32_bf16 v[4:7], v[250:253], v[24:27], v[4:7]\n"
      "v_and_b32 v223, 0xffff0000, v223\n"
      "v_mfma_f32_16x16x32_bf16 v[52:55], v[172:175], a[116:119], v[52:55]\n"
      "v_mfma_f32_16x16x32_bf16 v[0:3], v[252:255], v[28:31], v[0:3]\n"
      "v_mul_f32 v222, v207, v222\n"
      "v_mfma_f32_16x16x32_bf16 v[56:59], v[172:175], a[120:123], v[56:59]\n"
      "v_mfma_f32_16x16x32_bf16 v[4:7], v[248:251], v[28:31], v[4:7]\n"
      "v_mov_b32 v11, s37\n"
      "v_mfma_f32_16x16x32_bf16 v[60:63], v[172:175], a[124:127], v[60:63]\n"
      "s_waitcnt vmcnt(22)\n"
      "global_load_dwordx4 v[172:175], v241, s[72:73] offset:3072 sc1\n"
      "v_mul_f32 v223, v207, v223\n"
      "v_mfma_f32_16x16x32_bf16 v[40:43], v[176:179], a[136:139], v[40:43]\n"
      "v_cvt_pk_bf16_f32 v8, v0, v1\n"
      "v_cvt_pk_bf16_f32 v9, v2, v3\n"
      "v_mfma_f32_16x16x32_bf16 v[44:47], v[176:179], a[140:143], v[44:47]\n"
      "v_cvt_pk_bf16_f32 v10, v4, v5\n"
      "v_cvt_pk_bf16_f32 v247, v6, v7\n"
      "v_mfma_f32_16x16x32_bf16 v[48:51], v[176:179], a[144:147], v[48:51]\n"
      "global_store_dwordx4 v242, v[8:11], s[52:53] offset:0 sc1\n"
      "v_mfma_f32_16x16x32_bf16 v[52:55], v[176:179], a[148:151], v[52:55]\n"
      "v_mfma_f32_16x16x32_bf16 v[56:59], v[176:179], a[152:155], v[56:59]\n"
      "v_mfma_f32_16x16x32_bf16 v[60:63], v[176:179], a[156:159], v[60:63]\n"
      "v_mfma_f32_16x16x32_bf16 v[40:43], v[180:183], a[168:171], v[40:43]\n"
      "v_mfma_f32_16x16x32_bf16 v[44:47], v[180:183], a[172:175], v[44:47]\n"
      "v_mfma_f32_16x16x32_bf16 v[48:51], v[180:183], a[176:179], v[48:51]\n"
      "v_mfma_f32_16x16x32_bf16 v[52:55], v[180:183], a[180:183], v[52:55]\n"
      "v_mfma_f32_16x16x32_bf16 v[56:59], v[180:183], a[184:187], v[56:59]\n"
      "s_waitcnt vmcnt(8)\n"
      "v_bitop3_b32 v192, v225, v227, s37 bitop3:0x7e\n"
      "v_bitop3_b32 v192, v192, v229, s37 bitop3:0xf6\n"
      "v_bitop3_b32 v192, v192, v231, s37 bitop3:0xf6\n"
      "v_bitop3_b32 v192, v192, v233, s37 bitop3:0xf6\n"
      "v_bitop3_b32 v192, v192, v235, s37 bitop3:0xf6\n"
      "v_bitop3_b32 v192, v192, v237, s37 bitop3:0xf6\n"
      "v_bitop3_b32 v192, v192, v239, s37 bitop3:0xf6\n"
      "v_cmp_ne_u32 vcc, 0, v192\n"
      "s_nop 1\n"
      "s_cbranch_vccnz .Ls_s16_%=\n"
      ".Ls_o17_%=:\n"
      "v_mfma_f32_16x16x32_bf16 v[60:63], v[180:183], a[188:191], v[60:63]\n"
      "v_add_f32 v192, v224, v228\n"
      "v_add_f32 v193, v226, v230\n"
      "v_mfma_f32_16x16x32_bf16 v[40:43], v[184:187], a[200:203], v[40:43]\n"
      "v_add_f32 v194, v232, v236\n"
      "v_add_f32 v195, v234, v238\n"
      "v_mfma_f32_16x16x32_bf16 v[44:47], v[184:187], a[204:207], v[44:47]\n"
      "ds_read_b64_tr_b16 v[224:225], v201 offset:0\n"
      "v_permlane32_swap_b32 v192, v193\n"
      "v_mfma_f32_16x16x32_bf16 v[48:51], v[184:187], a[208:211], v[48:51]\n"
      "ds_read_b64_tr_b16 v[226:227], v201 offset:512\n"
      "v_permlane32_swap_b32 v194, v195\n"
      "v_mfma_f32_16x16x32_bf16 v[52:55], v[184:187], a[212:215], v[52:55]\n"
      "ds_read_b64_tr_b16 v[228:229], v201 offset:32\n"
      "v_add_f32 v192, v192, v193\n"
      "v_mfma_f32_16x16x32_bf16 v[56:59], v[184:187], a[216:219], v[56:59]\n"
      "ds_read_b64_tr_b16 v[230:231], v201 offset:544\n"
      "v_add_f32 v194, v194, v195\n"
      "v_mfma_f32_16x16x32_bf16 v[60:63], v[184:187], a[220:223], v[60:63]\n"
      "ds_read_b64_tr_b16 v[232:233], v201 offset:64\n"
      "v_mov_b32 v193, v192\n"
      "v_mfma_f32_16x16x32_bf16 v[40:43], v[188:191], a[232:235], v[40:43]\n"
      "ds_read_b64_tr_b16 v[234:235], v201 offset:576\n"
      "v_mov_b32 v195, v194\n"
      "v_mfma_f32_16x16x32_bf16 v[44:47], v[188:191], a[236:239], v[44:47]\n"
      "ds_read_b64_tr_b16 v[236:237], v201 offset:96\n"
      "v_permlane16_swap_b32 v192, v193\n"
      "v_mfma_f32_16x16x32_bf16 v[48:51], v[188:191], a[240:243], v[48:51]\n"
      "ds_read_b64_tr_b16 v[238:239], v201 offset:608\n"
      "v_permlane16_swap_b32 v194, v195\n"
      "v_add_f32 v192, v192, v193\n"
      "v_mfma_f32_16x16x32_bf16 v[52:55], v[188:191], a[244:247], v[52:55]\n"
      "v_add_f32 v194, v194, v195\n"
      "v_cvt_pk_bf16_f32 v192, v192, v194\n"
      "v_and_b32 v194, 0xffff0000, v192\n"
      "v_mfma_f32_16x16x32_bf16 v[56:59], v[188:191], a[248:251], v[56:59]\n"
      "v_lshlrev_b32 v192, 16, v192\n"
      "v_mul_f32 v193, 0xbfb8aa3b, v192\n"
      "v_exp_f32 v193, v193\n"
      "v_mfma_f32_16x16x32_bf16 v[60:63], v[188:191], a[252:255], v[60:63]\n"
      "v_add_f32 v193, 1.0, v193\n"
      "v_rcp_f32 v193, v193\n"
      "s_nop 0\n"
      "v_mul_f32 v192, v192, v193\n"
      "v_mfma_f32_16x16x32_bf16 v[32:35], v[176:179], a[128:131], v[32:35]\n"
      "v_cvt_pk_bf16_f32 v192, v192, v192\n"
      "v_and_b32 v192, 0xffff0000, v192\n"
      "v_mul_f32 v192, v192, v194\n"
      "v_mfma_f32_16x16x32_bf16 v[36:39], v[176:179], a[132:135], v[36:39]\n"
      "global_load_dwordx4 v[176:179], v240, s[14:15] offset:3072 sc1\n"
      "v_cvt_pk_bf16_f32 v192, v192, v192\n"
      "v_cvt_pk_bf16_f32 v40, v40, v41\n"
      "v_mfma_f32_16x16x32_bf16 v[32:35], v[180:183], a[160:163], v[32:35]\n"
      "ds_write_b16 v202, v192\n"
      "ds_read_b128 v[192:195], v199\n"
      "v_cvt_pk_bf16_f32 v41, v42, v43\n"
      "v_mfma_f32_16x16x32_bf16 v[36:39], v[180:183], a[164:167], v[36:39]\n"
      "global_load_dwordx4 v[180:183], v240, s[74:75] offset:3072 sc1\n"
      "ds_write_addtid_b32 v40 offset:3072\n"
      "v_cvt_pk_bf16_f32 v42, v44, v45\n"
      "v_mfma_f32_16x16x32_bf16 v[32:35], v[184:187], a[192:195], v[32:35]\n"
      "ds_write_addtid_b32 v41 offset:3328\n"
      "v_cvt_pk_bf16_f32 v43, v46, v47\n"
      "v_mfma_f32_16x16x32_bf16 v[36:39], v[184:187], a[196:199], v[36:39]\n"
      "global_load_dwordx4 v[184:187], v241, s[14:15] offset:3072 sc1\n"
      "ds_write_addtid_b32 v42 offset:3584\n"
      "v_cvt_pk_bf16_f32 v44, v48, v49\n"
      "v_mfma_f32_16x16x32_bf16 v[32:35], v[188:191], a[224:227], v[32:35]\n"
      "ds_write_addtid_b32 v43 offset:3840\n"
      "v_cvt_pk_bf16_f32 v45, v50, v51\n"
      "v_mfma_f32_16x16x32_bf16 v[36:39], v[188:191], a[228:231], v[36:39]\n"
      "global_load_dwordx4 v[188:191], v241, s[74:75] offset:3072 sc1\n"
      "ds_write_addtid_b32 v44 offset:4096\n"
      "v_cvt_pk_bf16_f32 v46, v52, v53\n"
      "v_mfma_f32_16x16x32_bf16 v[72:75], v[128:131], a[8:11], 0\n"
      "s_waitcnt lgkmcnt(5)\n"
      "v_mfma_f32_16x16x32_bf16 v[196:199], v[192:195], v[224:227], 0\n"
      "v_mfma_f32_16x16x32_bf16 v[76:79], v[128:131], a[12:15], 0\n"
      "v_mfma_f32_16x16x32_bf16 v[224:227], v[192:195], v[228:231], 0\n"
      "v_mfma_f32_16x16x32_bf16 v[228:231], v[192:195], v[232:235], 0\n"
      "v_mfma_f32_16x16x32_bf16 v[80:83], v[128:131], a[16:19], 0\n"
      "v_mfma_f32_16x16x32_bf16 v[232:235], v[192:195], v[236:239], 0\n"
      "ds_write_addtid_b32 v45 offset:4352\n"
      "v_mov_b32 v193, s37\n"
      "v_mfma_f32_16x16x32_bf16 v[84:87], v[128:131], a[20:23], 0\n"
      "v_mov_b32 v195, s37\n"
      "v_cvt_pk_bf16_f32 v47, v54, v55\n"
      "v_mfma_f32_16x16x32_bf16 v[88:91], v[128:131], a[24:27], 0\n"
      "ds_write_addtid_b32 v46 offset:4608\n"
      "v_cvt_pk_bf16_f32 v48, v56, v57\n"
      "v_mfma_f32_16x16x32_bf16 v[92:95], v[128:131], a[28:31], 0\n"
      "ds_write_addtid_b32 v47 offset:4864\n"
      "v_cndmask_b32 v192, v196, v228, s[64:65]\n"
      "v_cndmask_b32 v194, v224, v232, s[64:65]\n"
      "v_mfma_f32_16x16x32_bf16 v[72:75], v[132:135], a[40:43], v[72:75]\n"
      "v_and_b32 v192, s82, v192\n"
      "v_and_b32 v194, s82, v194\n"
      "v_cvt_pk_bf16_f32 v49, v58, v59\n"
      "v_mfma_f32_16x16x32_bf16 v[76:79], v[132:135], a[44:47], v[76:79]\n"
      "s_mov_b64 exec, s[66:67]\n"
      "global_store_dwordx4 v203, v[192:195], s[58:59] sc1\n"
      "s_mov_b64 exec, -1\n"
      "v_mfma_f32_16x16x32_bf16 v[80:83], v[132:135], a[48:51], v[80:83]\n"
      "s_lshl2_add_u32 s54, s36, s33\n"
      "ds_write_addtid_b32 v48 offset:5120\n"
      "v_cvt_pk_bf16_f32 v50, v60, v61\n"
      "s_lshl_b32 s55, s54, 15\n"
      "v_mfma_f32_16x16x32_bf16 v[84:87], v[132:135], a[52:55], v[84:87]\n"
      "s_lshl2_add_u32 s56, s35, s34\n"
      "ds_write_addtid_b32 v49 offset:5376\n"
      "v_cvt_pk_bf16_f32 v51, v62, v63\n"
      "s_lshl_b32 s56, s56, 6\n"
      "v_mfma_f32_16x16x32_bf16 v[88:91], v[132:135], a[56:59], v[88:91]\n"
      "s_add_u32 s55, s55, s56\n"
      "ds_write_addtid_b32 v50 offset:5632\n"
      "ds_write_addtid_b32 v51 offset:5888\n"
      "v_and_b32 v8, 15, v243\n"
      "v_mfma_f32_16x16x32_bf16 v[92:95], v[132:135], a[60:63], v[92:95]\n"
      "v_and_b32 v7, 48, v243\n"
      "v_lshl_or_b32 v7, v8, 10, v7\n"
      "v_add_u32 v7, s55, v7\n"
      "v_mfma_f32_16x16x32_bf16 v[72:75], v[136:139], a[72:75], v[72:75]\n"
      "v_mfma_f32_16x16x32_bf16 v[76:79], v[136:139], a[76:79], v[76:79]\n"
      "s_waitcnt lgkmcnt(0)\n"
      "s_barrier\n"
      "v_mfma_f32_16x16x32_bf16 v[80:83], v[136:139], a[80:83], v[80:83]\n"
      "ds_read2st64_b32 v[52:53], v244 offset0:12 offset1:13\n"
      "ds_read2st64_b32 v[54:55], v245 offset0:16 offset1:17\n"
      "v_mfma_f32_16x16x32_bf16 v[84:87], v[136:139], a[84:87], v[84:87]\n"
      "ds_read2st64_b32 v[56:57], v244 offset0:14 offset1:15\n"
      "ds_read2st64_b32 v[58:59], v245 offset0:18 offset1:19\n"
      "v_mfma_f32_16x16x32_bf16 v[88:91], v[136:139], a[88:91], v[88:91]\n"
      "ds_read2st64_b32 v[60:61], v246 offset0:20 offset1:21\n"
      "ds_read2st64_b32 v[62:63], v246 offset0:22 offset1:23\n"
      "v_add_u32 v4, 0, v242\n"
      "v_mfma_f32_16x16x32_bf16 v[92:95], v[136:139], a[92:95], v[92:95]\n"
      "global_load_dwordx4 v[12:15], v4, s[62:63] offset:0 sc1\n"
      "global_load_dwordx4 v[16:19], v4, s[62:63] offset:1024 sc1\n"
      "v_mfma_f32_16x16x32_bf16 v[72:75], v[140:143], a[104:107], v[72:75]\n"
      "global_load_dwordx4 v[20:23], v4, s[62:63] offset:2048 sc1\n"
      "v_mfma_f32_16x16x32_bf16 v[76:79], v[140:143], a[108:111], v[76:79]\n"
      "v_mfma_f32_16x16x32_bf16 v[80:83], v[140:143], a[112:115], v[80:83]\n"
      "v_mfma_f32_16x16x32_bf16 v[84:87], v[140:143], a[116:119], v[84:87]\n"
      "s_waitcnt lgkmcnt(0)\n"
      "v_mfma_f32_16x16x32_bf16 v[32:35], v[250:253], v[52:55], v[32:35]\n"
      "v_mfma_f32_16x16x32_bf16 v[36:39], v[250:253], v[56:59], v[36:39]\n"
      "v_mfma_f32_16x16x32_bf16 v[88:91], v[140:143], a[120:123], v[88:91]\n"
      "v_mfma_f32_16x16x32_bf16 v[32:35], v[252:255], v[60:63], v[32:35]\n"
      "v_mfma_f32_16x16x32_bf16 v[36:39], v[248:251], v[60:63], v[36:39]\n"
      "v_mov_b32 v43, s37\n"
      "v_mfma_f32_16x16x32_bf16 v[92:95], v[140:143], a[124:127], v[92:95]\n"
      "v_mov_b32 v44, v247\n"
      "v_mov_b32 v47, s37\n"
      "v_mfma_f32_16x16x32_bf16 v[72:75], v[144:147], a[136:139], v[72:75]\n"
      "v_mfma_f32_16x16x32_bf16 v[76:79], v[144:147], a[140:143], v[76:79]\n"
      "s_nop 0\n"
      "v_cvt_pk_bf16_f32 v40, v32, v33\n"
      "v_cvt_pk_bf16_f32 v41, v34, v35\n"
      "v_mfma_f32_16x16x32_bf16 v[80:83], v[144:147], a[144:147], v[80:83]\n"
      "v_cvt_pk_bf16_f32 v42, v36, v37\n"
      "v_cvt_pk_bf16_f32 v45, v38, v39\n"
      "v_mfma_f32_16x16x32_bf16 v[84:87], v[144:147], a[148:151], v[84:87]\n"
      "global_store_dwordx4 v242, v[40:43], s[52:53] offset:1024 sc1\n"
      "v_mfma_f32_16x16x32_bf16 v[88:91], v[144:147], a[152:155], v[88:91]\n"
      "global_store_dwordx4 v242, v[44:47], s[52:53] offset:2048 sc1\n"
      "v_mfma_f32_16x16x32_bf16 v[92:95], v[144:147], a[156:159], v[92:95]\n"
      "v_mfma_f32_16x16x32_bf16 v[72:75], v[148:151], a[168:171], v[72:75]\n"
      "v_mfma_f32_16x16x32_bf16 v[76:79], v[148:151], a[172:175], v[76:79]\n"
      "v_mfma_f32_16x16x32_bf16 v[80:83], v[148:151], a[176:179], v[80:83]\n"
      "v_mfma_f32_16x16x32_bf16 v[84:87], v[148:151], a[180:183], v[84:87]\n"
      "v_mfma_f32_16x16x32_bf16 v[88:91], v[148:151], a[184:187], v[88:91]\n"
      "v_mfma_f32_16x16x32_bf16 v[92:95], v[148:151], a[188:191], v[92:95]\n"
      "s_waitcnt vmcnt(15)\n"
      "v_mfma_f32_16x16x32_bf16 v[72:75], v[152:155], a[200:203], v[72:75]\n"
      "v_mfma_f32_16x16x32_bf16 v[76:79], v[152:155], a[204:207], v[76:79]\n"
      "v_mfma_f32_16x16x32_bf16 v[80:83], v[152:155], a[208:211], v[80:83]\n"
      "v_mfma_f32_16x16x32_bf16 v[84:87], v[152:155], a[212:215], v[84:87]\n"
      "v_mfma_f32_16x16x32_bf16 v[88:91], v[152:155], a[216:219], v[88:91]\n"
      "v_mfma_f32_16x16x32_bf16 v[92:95], v[152:155], a[220:223], v[92:95]\n"
      "v_mfma_f32_16x16x32_bf16 v[72:75], v[156:159], a[232:235], v[72:75]\n"
      "v_mfma_f32_16x16x32_bf16 v[76:79], v[156:159], a[236:239], v[76:79]\n"
      "v_mfma_f32_16x16x32_bf16 v[80:83], v[156:159], a[240:243], v[80:83]\n"
      "v_mfma_f32_16x16x32_bf16 v[84:87], v[156:159], a[244:247], v[84:87]\n"
      "v_mfma_f32_16x16x32_bf16 v[88:91], v[156:159], a[248:251], v[88:91]\n"
      "s_nop 3\n"
      "v_cvt_pk_bf16_f32 v72, v72, v73\n"
      "v_mfma_f32_16x16x32_bf16 v[92:95], v[156:159], a[252:255], v[92:95]\n"
      "s_waitcnt vmcnt(13)\n"
      "v_cvt_pk_bf16_f32 v73, v74, v75\n"
      "v_mfma_f32_16x16x32_bf16 v[104:107], v[160:163], a[8:11], 0\n"
      "ds_write_addtid_b32 v72 offset:6144\n"
      "v_cvt_pk_bf16_f32 v74, v76, v77\n"
      "v_mfma_f32_16x16x32_bf16 v[108:111], v[160:163], a[12:15], 0\n"
      "ds_write_addtid_b32 v73 offset:6400\n"
      "v_cvt_pk_bf16_f32 v75, v78, v79\n"
      "v_mfma_f32_16x16x32_bf16 v[112:115], v[160:163], a[16:19], 0\n"
      "ds_write_addtid_b32 v74 offset:6656\n"
      "v_cvt_pk_bf16_f32 v76, v80, v81\n"
      "v_mfma_f32_16x16x32_bf16 v[116:119], v[160:163], a[20:23], 0\n"
      "ds_write_addtid_b32 v75 offset:6912\n"
      "v_cvt_pk_bf16_f32 v77, v82, v83\n"
      "v_mfma_f32_16x16x32_bf16 v[120:123], v[160:163], a[24:27], 0\n"
      "ds_write_addtid_b32 v76 offset:7168\n"
      "v_cvt_pk_bf16_f32 v78, v84, v85\n"
      "v_mfma_f32_16x16x32_bf16 v[124:127], v[160:163], a[28:31], 0\n"
      "ds_write_addtid_b32 v77 offset:7424\n"
      "v_cvt_pk_bf16_f32 v79, v86, v87\n"
      "v_mfma_f32_16x16x32_bf16 v[104:107], v[164:167], a[40:43], v[104:107]\n"
      "ds_write_addtid_b32 v78 offset:7680\n"
      "v_cvt_pk_bf16_f32 v80, v88, v89\n"
      "v_mfma_f32_16x16x32_bf16 v[108:111], v[164:167], a[44:47], v[108:111]\n"
      "ds_write_addtid_b32 v79 offset:7936\n"
      "v_cvt_pk_bf16_f32 v81, v90, v91\n"
      "v_mfma_f32_16x16x32_bf16 v[112:115], v[164:167], a[48:51], v[112:115]\n"
      "ds_write_addtid_b32 v80 offset:8192\n"
      "v_cvt_pk_bf16_f32 v82, v92, v93\n"
      "v_mfma_f32_16x16x32_bf16 v[116:119], v[164:167], a[52:55], v[116:119]\n"
      "ds_write_addtid_b32 v81 offset:8448\n"
      "v_cvt_pk_bf16_f32 v83, v94, v95\n"
      "v_mfma_f32_16x16x32_bf16 v[120:123], v[164:167], a[56:59], v[120:123]\n"
      "ds_write_addtid_b32 v82 offset:8704\n"
      "v_mfma_f32_16x16x32_bf16 v[124:127], v[164:167], a[60:63], v[124:127]\n"
      "s_waitcnt vmcnt(11)\n"
      "ds_write_addtid_b32 v83 offset:8960\n"
      "v_mfma_f32_16x16x32_bf16 v[104:107], v[168:171], a[72:75], v[104:107]\n"
      "s_waitcnt vmcnt(2)\n"
      "v_bitop3_b32 v22, v15, v19, s37 bitop3:0x7e\n"
      "v_bitop3_b32 v22, v22, v23, s37 bitop3:0xf6\n"
      "v_cmp_ne_u32 vcc, 0, v22\n"
      "s_nop 1\n"
      "s_cbranch_vccnz .Ls_s19_%=\n"
      ".Ls_o20_%=:\n"
      "v_mfma_f32_16x16x32_bf16 v[108:111], v[168:171], a[76:79], v[108:111]\n"
      "v_mov_b32 v15, v20\n"
      "v_mov_b32 v19, v21\n"
      "v_mfma_f32_16x16x32_bf16 v[112:115], v[168:171], a[80:83], v[112:115]\n"
      "s_waitcnt lgkmcnt(0)\n"
      "s_barrier\n"
      "v_mfma_f32_16x16x32_bf16 v[116:119], v[168:171], a[84:87], v[116:119]\n"
      "ds_read2st64_b32 v[84:85], v244 offset0:24 offset1:25\n"
      "v_mfma_f32_16x16x32_bf16 v[120:123], v[168:171], a[88:91], v[120:123]\n"
      "ds_read2st64_b32 v[86:87], v245 offset0:28 offset1:29\n"
      "v_mfma_f32_16x16x32_bf16 v[124:127], v[168:171], a[92:95], v[124:127]\n"
      "ds_read2st64_b32 v[88:89], v244 offset0:26 offset1:27\n"
      "v_mfma_f32_16x16x32_bf16 v[104:107], v[172:175], a[104:107], v[104:107]\n"
      "ds_read2st64_b32 v[90:91], v245 offset0:30 offset1:31\n"
      "v_add_u32 v5, 12288, v242\n"
      "v_mfma_f32_16x16x32_bf16 v[108:111], v[172:175], a[108:111], v[108:111]\n"
      "global_load_dwordx4 v[44:47], v5, s[62:63] offset:0 sc1\n"
      "v_mfma_f32_16x16x32_bf16 v[112:115], v[172:175], a[112:115], v[112:115]\n"
      "global_load_dwordx4 v[48:51], v5, s[62:63] offset:1024 sc1\n"
      "v_mfma_f32_16x16x32_bf16 v[116:119], v[172:175], a[116:119], v[116:119]\n"
      "global_load_dwordx4 v[52:55], v5, s[62:63] offset:2048 sc1\n"
      "v_mfma_f32_16x16x32_bf16 v[120:123], v[172:175], a[120:123], v[120:123]\n"
      "ds_read2st64_b32 v[92:93], v246 offset0:32 offset1:33\n"
      "v_mfma_f32_16x16x32_bf16 v[124:127], v[172:175], a[124:127], v[124:127]\n"
      "ds_read2st64_b32 v[94:95], v246 offset0:34 offset1:35\n"
      "v_mfma_f32_16x16x32_bf16 v[104:107], v[176:179], a[136:139], v[104:107]\n"
      "v_mfma_f32_16x16x32_bf16 v[64:67], v[252:255], v[12:15], 0\n"
      "v_mfma_f32_16x16x32_bf16 v[108:111], v[176:179], a[140:143], v[108:111]\n"
      "v_mfma_f32_16x16x32_bf16 v[68:71], v[248:251], v[12:15], 0\n"
      "v_mfma_f32_16x16x32_bf16 v[112:115], v[176:179], a[144:147], v[112:115]\n"
      "v_mfma_f32_16x16x32_bf16 v[96:99], v[252:255], v[16:19], 0\n"
      "v_mfma_f32_16x16x32_bf16 v[116:119], v[176:179], a[148:151], v[116:119]\n"
      "v_mfma_f32_16x16x32_bf16 v[100:103], v[248:251], v[16:19], 0\n"
      "v_mfma_f32_16x16x32_bf16 v[120:123], v[176:179], a[152:155], v[120:123]\n"
      "s_waitcnt lgkmcnt(0)\n"
      "v_mfma_f32_16x16x32_bf16 v[64:67], v[250:253], v[84:87], v[64:67]\n"
      "v_mfma_f32_16x16x32_bf16 v[124:127], v[176:179], a[156:159], v[124:127]\n"
      "v_mfma_f32_16x16x32_bf16 v[68:71], v[250:253], v[88:91], v[68:71]\n"
      "v_mfma_f32_16x16x32_bf16 v[104:107], v[180:183], a[168:171], v[104:107]\n"
      "v_mfma_f32_16x16x32_bf16 v[64:67], v[252:255], v[92:95], v[64:67]\n"
      "v_mfma_f32_16x16x32_bf16 v[108:111], v[180:183], a[172:175], v[108:111]\n"
      "v_mfma_f32_16x16x32_bf16 v[68:71], v[248:251], v[92:95], v[68:71]\n"
      "v_mfma_f32_16x16x32_bf16 v[112:115], v[180:183], a[176:179], v[112:115]\n"
      "v_mfma_f32_16x16x32_bf16 v[116:119], v[180:183], a[180:183], v[116:119]\n"
      "v_mfma_f32_16x16x32_bf16 v[120:123], v[180:183], a[184:187], v[120:123]\n"
      "v_mfma_f32_16x16x32_bf16 v[124:127], v[180:183], a[188:191], v[124:127]\n"
      "v_mfma_f32_16x16x32_bf16 v[104:107], v[184:187], a[200:203], v[104:107]\n"
      "v_mfma_f32_16x16x32_bf16 v[108:111], v[184:187], a[204:207], v[108:111]\n"
      "v_mfma_f32_16x16x32_bf16 v[112:115], v[184:187], a[208:211], v[112:115]\n"
      "v_mfma_f32_16x16x32_bf16 v[116:119], v[184:187], a[212:215], v[116:119]\n"
      "v_mfma_f32_16x16x32_bf16 v[120:123], v[184:187], a[216:219], v[120:123]\n"
      "v_mfma_f32_16x16x32_bf16 v[124:127], v[184:187], a[220:223], v[124:127]\n"
      "v_mfma_f32_16x16x32_bf16 v[104:107], v[188:191], a[232:235], v[104:107]\n"
      "v_mfma_f32_16x16x32_bf16 v[108:111], v[188:191], a[236:239], v[108:111]\n"
      "s_lshr_b32 s43, s42, 7\n"
      "s_cmp_eq_u32 s43, s36\n"
      "s_cbranch_scc0 .Ls_e21_%=\n"
      "s_lshl2_add_u32 s44, s35, s34\n"
      "s_lshl2_add_u32 s43, s44, s33\n"
      "s_lshl_b32 s43, s43, 12\n"
      "s_add_u32 s43, s43, 524288\n"
      "v_add_u32 v60, s43, v242\n"
      "global_load_dwordx4 v[24:27], v60, s[58:59] offset:0 sc1\n"
      "global_load_dwordx4 v[28:31], v60, s[58:59] offset:1024 sc1\n"
      "global_load_dwordx4 v[32:35], v60, s[58:59] offset:2048 sc1\n"
      "global_load_dwordx4 v[36:39], v60, s[58:59] offset:3072 sc1\n"
      "v_and_b32 v58, 15, v243\n"
      "v_lshrrev_b32 v59, 5, v243\n"
      "v_lshl_or_b32 v58, v59, 4, v58\n"
      "s_lshl4_add_u32 s44, s42, s44\n"
      "s_lshl_b32 s44, s44, 6\n"
      "v_lshl_add_u32 v59, v58, 1, s44\n"
      "global_load_ushort v57, v59, s[16:17]\n"
      "s_mov_b32 s44, 131072\n"
      "v_lshl_add_u32 v58, v58, 2, s44\n"
      "s_lshl_b32 s43, s33, 13\n"
      "v_add_u32 v59, s43, v58\n"
      ".Ls_e21_%=:\n"
      "v_mfma_f32_16x16x32_bf16 v[112:115], v[188:191], a[240:243], v[112:115]\n"
      "v_mfma_f32_16x16x32_bf16 v[116:119], v[188:191], a[244:247], v[116:119]\n"
      "v_mfma_f32_16x16x32_bf16 v[120:123], v[188:191], a[248:251], v[120:123]\n"
      "s_nop 0\n"
      "v_cvt_pk_bf16_f32 v104, v104, v105\n"
      "v_mfma_f32_16x16x32_bf16 v[124:127], v[188:191], a[252:255], v[124:127]\n"
      "v_cvt_pk_bf16_f32 v105, v106, v107\n"
      "v_mfma_f32_16x16x32_bf16 v[64:67], v[128:131], a[0:3], v[64:67]\n"
      "ds_write_addtid_b32 v104 offset:9216\n"
      "v_cvt_pk_bf16_f32 v106, v108, v109\n"
      "v_mfma_f32_16x16x32_bf16 v[68:71], v[128:131], a[4:7], v[68:71]\n"
      "ds_write_addtid_b32 v105 offset:9472\n"
      "v_cvt_pk_bf16_f32 v107, v110, v111\n"
      "v_mfma_f32_16x16x32_bf16 v[64:67], v[132:135], a[32:35], v[64:67]\n"
      "ds_write_addtid_b32 v106 offset:9728\n"
      "v_cvt_pk_bf16_f32 v108, v112, v113\n"
      "v_mfma_f32_16x16x32_bf16 v[68:71], v[132:135], a[36:39], v[68:71]\n"
      "ds_write_addtid_b32 v107 offset:9984\n"
      "v_cvt_pk_bf16_f32 v109, v114, v115\n"
      "v_mfma_f32_16x16x32_bf16 v[64:67], v[136:139], a[64:67], v[64:67]\n"
      "ds_write_addtid_b32 v108 offset:10240\n"
      "v_cvt_pk_bf16_f32 v110, v116, v117\n"
      "v_mfma_f32_16x16x32_bf16 v[68:71], v[136:139], a[68:71], v[68:71]\n"
      "ds_write_addtid_b32 v109 offset:10496\n"
      "v_cvt_pk_bf16_f32 v111, v118, v119\n"
      "v_mfma_f32_16x16x32_bf16 v[64:67], v[140:143], a[96:99], v[64:67]\n"
      "ds_write_addtid_b32 v110 offset:10752\n"
      "v_cvt_pk_bf16_f32 v112, v120, v121\n"
      "v_mfma_f32_16x16x32_bf16 v[68:71], v[140:143], a[100:103], v[68:71]\n"
      "ds_write_addtid_b32 v111 offset:11008\n"
      "v_cvt_pk_bf16_f32 v113, v122, v123\n"
      "v_mfma_f32_16x16x32_bf16 v[64:67], v[144:147], a[128:131], v[64:67]\n"
      "ds_write_addtid_b32 v112 offset:11264\n"
      "v_cvt_pk_bf16_f32 v114, v124, v125\n"
      "v_mfma_f32_16x16x32_bf16 v[68:71], v[144:147], a[132:135], v[68:71]\n"
      "ds_write_addtid_b32 v113 offset:11520\n"
      "v_cvt_pk_bf16_f32 v115, v126, v127\n"
      "v_mfma_f32_16x16x32_bf16 v[64:67], v[148:151], a[160:163], v[64:67]\n"
      "ds_write_addtid_b32 v114 offset:11776\n"
      "v_add_u32 v6, 24576, v242\n"
      "v_mfma_f32_16x16x32_bf16 v[68:71], v[148:151], a[164:167], v[68:71]\n"
      "global_load_dwordx4 v[72:75], v6, s[62:63] offset:0 sc1\n"
      "v_mfma_f32_16x16x32_bf16 v[64:67], v[152:155], a[192:195], v[64:67]\n"
      "global_load_dwordx4 v[76:79], v6, s[62:63] offset:1024 sc1\n"
      "v_mfma_f32_16x16x32_bf16 v[68:71], v[152:155], a[196:199], v[68:71]\n"
      "global_load_dwordx4 v[80:83], v6, s[62:63] offset:2048 sc1\n"
      "v_mfma_f32_16x16x32_bf16 v[64:67], v[156:159], a[224:227], v[64:67]\n"
      "ds_write_addtid_b32 v115 offset:12032\n"
      "v_mfma_f32_16x16x32_bf16 v[68:71], v[156:159], a[228:231], v[68:71]\n"
      "s_waitcnt vmcnt(3)\n"
      "v_bitop3_b32 v54, v47, v51, s37 bitop3:0x7e\n"
      "v_bitop3_b32 v54, v54, v55, s37 bitop3:0xf6\n"
      "v_cmp_ne_u32 vcc, 0, v54\n"
      "s_nop 1\n"
      "s_cbranch_vccnz .Ls_s23_%=\n"
      ".Ls_o24_%=:\n"
      "v_mfma_f32_16x16x32_bf16 v[96:99], v[160:163], a[0:3], v[96:99]\n"
      "v_mov_b32 v47, v52\n"
      "v_mov_b32 v51, v53\n"
      "v_mfma_f32_16x16x32_bf16 v[100:103], v[160:163], a[4:7], v[100:103]\n"
      "s_lshr_b32 s43, s42, 7\n"
      "s_cmp_eq_u32 s43, s36\n"
      "s_cbranch_scc0 .Ls_e25_%=\n"
      "s_waitcnt vmcnt(3)\n"
      "v_bitop3_b32 v61, v25, v27, s37 bitop3:0x7e\n"
      "v_bitop3_b32 v61, v61, v29, s37 bitop3:0xf6\n"
      "v_bitop3_b32 v61, v61, v31, s37 bitop3:0xf6\n"
      "v_bitop3_b32 v61, v61, v33, s37 bitop3:0xf6\n"
      "v_bitop3_b32 v61, v61, v35, s37 bitop3:0xf6\n"
      "v_bitop3_b32 v61, v61, v37, s37 bitop3:0xf6\n"
      "v_bitop3_b32 v61, v61, v39, s37 bitop3:0xf6\n"
      "v_cmp_ne_u32 vcc, 0, v61\n"
      "s_nop 1\n"
      "s_cbranch_vccnz .Ls_s27_%=\n"
      ".Ls_o28_%=:\n"
      "v_add_f32 v24, v24, v28\n"
      "v_add_f32 v26, v26, v30\n"
      "v_add_f32 v32, v32, v36\n"
      "v_add_f32 v34, v34, v38\n"
      "v_add_f32 v24, v24, v32\n"
      "v_add_f32 v26, v26, v34\n"
      "s_nop 1\n"
      "v_permlane32_swap_b32 v24, v26\n"
      "v_add_f32 v24, v24, v26\n"
      "v_mov_b32 v26, v24\n"
      "s_nop 1\n"
      "v_permlane16_swap_b32 v24, v26\n"
      "v_add_f32 v24, v24, v26\n"
      "ds_write_b32 v59, v24\n"
      "s_waitcnt lgkmcnt(0)\n"
      ".Ls_e25_%=:\n"
      "v_mfma_f32_16x16x32_bf16 v[96:99], v[164:167], a[32:35], v[96:99]\n"
      "s_waitcnt lgkmcnt(0)\n"
      "s_barrier\n"
      "v_mfma_f32_16x16x32_bf16 v[100:103], v[164:167], a[36:39], v[100:103]\n"
      "s_lshr_b32 s43, s42, 5\n"
      "s_lshl2_add_u32 s44, s36, s33\n"
      "s_cmp_eq_u32 s43, s44\n"
      "s_cbranch_scc0 .Ls_e29_%=\n"
      "ds_read_b32 v24, v58 offset:0\n"
      "ds_read_b32 v25, v58 offset:8192\n"
      "ds_read_b32 v26, v58 offset:16384\n"
      "ds_read_b32 v27, v58 offset:24576\n"
      "v_mov_b32 v62, 0\n"
      "v_mov_b32 v63, s37\n"
      "s_or_b32 s43, s34, s35\n"
      "s_cmp_eq_u32 s43, 0\n"
      "s_cbranch_scc0 .Ls_z30_%=\n"
      "s_mov_b64 exec, 1\n"
      "global_store_dword v62, v63, s[58:59] sc1\n"
      ".Ls_z30_%=:\n"
      "s_mov_b64 exec, -1\n"
      "s_waitcnt vmcnt(3)\n"
      "v_lshlrev_b32 v57, 16, v57\n"
      "v_and_b32 v56, 48, v243\n"
      "s_mov_b32 s44, 131072\n"
      "v_lshl_add_u32 v56, v56, 1, s44\n"
      "s_waitcnt lgkmcnt(0)\n"
      "v_add_f32 v24, v24, v25\n"
      "s_waitcnt lgkmcnt(0)\n"
      "v_add_f32 v26, v26, v27\n"
      "v_add_f32 v24, v24, v26\n"
      "v_cvt_pk_bf16_f32 v24, v24, v24\n"
      "v_and_b32 v24, 0xffff0000, v24\n"
      "v_mul_f32 v24, s84, v24\n"
      "v_cvt_pk_bf16_f32 v24, v24, v24\n"
      "v_and_b32 v24, 0xffff0000, v24\n"
      "v_add_f32 v24, v24, v57\n"
      "v_cvt_pk_bf16_f32 v24, v24, v24\n"
      "v_and_b32 v24, 0xffff0000, v24\n"
      "v_sub_f32 v24, v24, v57\n"
      "ds_write_b32 v58, v24 offset:1024\n"
      "ds_read_b128 v[28:31], v56 offset:1024\n"
      "ds_read_b128 v[32:35], v56 offset:1040\n"
      ".Ls_e29_%=:\n"
      "v_mfma_f32_16x16x32_bf16 v[96:99], v[168:171], a[64:67], v[96:99]\n"
      "ds_read2st64_b32 v[116:117], v244 offset0:36 offset1:37\n"
      "v_mfma_f32_16x16x32_bf16 v[100:103], v[168:171], a[68:71], v[100:103]\n"
      "ds_read2st64_b32 v[118:119], v245 offset0:40 offset1:41\n"
      "v_mfma_f32_16x16x32_bf16 v[96:99], v[172:175], a[96:99], v[96:99]\n"
      "ds_read2st64_b32 v[120:121], v244 offset0:38 offset1:39\n"
      "v_mfma_f32_16x16x32_bf16 v[100:103], v[172:175], a[100:103], v[100:103]\n"
      "ds_read2st64_b32 v[122:123], v245 offset0:42 offset1:43\n"
      "v_mfma_f32_16x16x32_bf16 v[96:99], v[176:179], a[128:131], v[96:99]\n"
      "ds_read2st64_b32 v[124:125], v246 offset0:44 offset1:45\n"
      "v_mfma_f32_16x16x32_bf16 v[100:103], v[176:179], a[132:135], v[100:103]\n"
      "ds_read2st64_b32 v[126:127], v246 offset0:46 offset1:47\n"
      "v_mfma_f32_16x16x32_bf16 v[96:99], v[180:183], a[160:163], v[96:99]\n"
      "v_mfma_f32_16x16x32_bf16 v[64:67], v[252:255], v[44:47], v[64:67]\n"
      "v_mfma_f32_16x16x32_bf16 v[100:103], v[180:183], a[164:167], v[100:103]\n"
      "v_mfma_f32_16x16x32_bf16 v[68:71], v[248:251], v[44:47], v[68:71]\n"
      "v_mfma_f32_16x16x32_bf16 v[96:99], v[184:187], a[192:195], v[96:99]\n"
      "v_mfma_f32_16x16x32_bf16 v[96:99], v[252:255], v[48:51], v[96:99]\n"
      "v_mfma_f32_16x16x32_bf16 v[100:103], v[184:187], a[196:199], v[100:103]\n"
      "v_mfma_f32_16x16x32_bf16 v[100:103], v[248:251], v[48:51], v[100:103]\n"
      "v_mfma_f32_16x16x32_bf16 v[96:99], v[188:191], a[224:227], v[96:99]\n"
      "s_waitcnt lgkmcnt(0)\n"
      "v_mfma_f32_16x16x32_bf16 v[96:99], v[250:253], v[116:119], v[96:99]\n"
      "v_mfma_f32_16x16x32_bf16 v[100:103], v[188:191], a[228:231], v[100:103]\n"
      "v_mfma_f32_16x16x32_bf16 v[100:103], v[250:253], v[120:123], v[100:103]\n"
      "v_mfma_f32_16x16x32_bf16 v[96:99], v[252:255], v[124:127], v[96:99]\n"
      "v_mfma_f32_16x16x32_bf16 v[100:103], v[248:251], v[124:127], v[100:103]\n"
      "s_lshr_b32 s43, s42, 5\n"
      "s_lshl2_add_u32 s44, s36, s33\n"
      "s_cmp_eq_u32 s43, s44\n"
      "s_cbranch_scc0 .Ls_e31_%=\n"
      "v_lshrrev_b32 v24, 10, v7\n"
      "v_subrev_u32 v24, s42, v24\n"
      "v_cmp_eq_u32 vcc, 0, v24\n"
      "s_nop 1\n"
      "s_mov_b64 exec, vcc\n"
      "v_fma_f32 v208, v205, v28, v208\n"
      "v_fma_f32 v209, v205, v29, v209\n"
      "v_fma_f32 v210, v205, v30, v210\n"
      "v_fma_f32 v211, v205, v31, v211\n"
      "v_fma_f32 v216, v205, v32, v216\n"
      "v_fma_f32 v217, v205, v33, v217\n"
      "v_fma_f32 v218, v205, v34, v218\n"
      "v_fma_f32 v219, v205, v35, v219\n"
      "s_mov_b64 exec, -1\n"
      "v_cmp_eq_u32 vcc, -16, v24\n"
      "s_nop 1\n"
      "s_mov_b64 exec, vcc\n"
      "v_fma_f32 v212, v207, v28, v212\n"
      "v_fma_f32 v213, v207, v29, v213\n"
      "v_fma_f32 v214, v207, v30, v214\n"
      "v_fma_f32 v215, v207, v31, v215\n"
      "v_fma_f32 v220, v207, v32, v220\n"
      "v_fma_f32 v221, v207, v33, v221\n"
      "v_fma_f32 v222, v207, v34, v222\n"
      "v_fma_f32 v223, v207, v35, v223\n"
      "s_mov_b64 exec, -1\n"
      ".Ls_e31_%=:\n"
      "s_waitcnt vmcnt(0)\n"
      "v_bitop3_b32 v82, v75, v79, s37 bitop3:0x7e\n"
      "v_bitop3_b32 v82, v82, v83, s37 bitop3:0xf6\n"
      "v_cmp_ne_u32 vcc, 0, v82\n"
      "s_nop 1\n"
      "s_cbranch_vccnz .Ls_s33_%=\n"
      ".Ls_o34_%=:\n"
      "v_mov_b32 v75, v80\n"
      "v_mov_b32 v79, v81\n"
      "s_nop 0\n"
      "v_mfma_f32_16x16x32_bf16 v[64:67], v[252:255], v[72:75], v[64:67]\n"
      "v_mfma_f32_16x16x32_bf16 v[96:99], v[252:255], v[76:79], v[96:99]\n"
      "v_mfma_f32_16x16x32_bf16 v[68:71], v[248:251], v[72:75], v[68:71]\n"
      "v_mfma_f32_16x16x32_bf16 v[100:103], v[248:251], v[76:79], v[100:103]\n"
      "s_nop 4\n"
      "v_fma_f32 v208, v204, v64, v208\n"
      "v_fma_f32 v209, v204, v65, v209\n"
      "v_fma_f32 v210, v204, v66, v210\n"
      "v_fma_f32 v211, v204, v67, v211\n"
      "v_fma_f32 v216, v204, v96, v216\n"
      "v_fma_f32 v217, v204, v97, v217\n"
      "v_fma_f32 v218, v204, v98, v218\n"
      "v_fma_f32 v219, v204, v99, v219\n"
      "v_cvt_pk_bf16_f32 v104, v208, v209\n"
      "v_cvt_pk_bf16_f32 v105, v210, v211\n"
      "v_cvt_pk_bf16_f32 v106, v216, v217\n"
      "v_cvt_pk_bf16_f32 v107, v218, v219\n"
      "global_store_dwordx4 v7, v[104:107], s[20:21]\n"
      "v_fma_f32 v212, v206, v68, v212\n"
      "v_fma_f32 v213, v206, v69, v213\n"
      "v_fma_f32 v214, v206, v70, v214\n"
      "v_fma_f32 v215, v206, v71, v215\n"
      "v_fma_f32 v220, v206, v100, v220\n"
      "v_fma_f32 v221, v206, v101, v221\n"
      "v_fma_f32 v222, v206, v102, v222\n"
      "v_fma_f32 v223, v206, v103, v223\n"
      "v_cvt_pk_bf16_f32 v108, v212, v213\n"
      "v_cvt_pk_bf16_f32 v109, v214, v215\n"
      "v_cvt_pk_bf16_f32 v110, v220, v221\n"
      "v_cvt_pk_bf16_f32 v111, v222, v223\n"
      "global_store_dwordx4 v7, v[108:111], s[68:69]\n"
      "s_endpgm\n"
      ".Lprod_%=:\n"
      "s_waitcnt lgkmcnt(0)\n"
      "s_add_u32 s68, s16, s44\n"
      "s_addc_u32 s69, s17, 0\n"
      "s_add_u32 s12, s12, s45\n"
      "s_addc_u32 s13, s13, 0\n"
      "s_add_u32 s58, s18, 4718592\n"
      "s_addc_u32 s59, s19, 0\n"
      "v_mov_b32 v235, 0\n"
      "global_load_dword v235, v235, s[58:59] sc1\n"
      "v_and_b32 v238, 15, v243\n"
      "v_lshrrev_b32 v239, 4, v243\n"
      "v_xor_b32 v239, v239, v238\n"
      "v_and_b32 v239, 7, v239\n"
      "v_lshlrev_b32 v239, 4, v239\n"
      "v_lshl_or_b32 v238, v238, 7, v239\n"
      "v_add_u32 v248, s41, v238\n"
      "v_xor_b32 v249, 64, v248\n"
      "v_add_u32 v250, 65536, v238\n"
      "v_xor_b32 v251, 64, v250\n"
      "v_and_b32 v252, 8, v243\n"
      "v_lshlrev_b32 v252, 7, v252\n"
      "v_and_or_b32 v252, v243, 48, v252\n"
      "v_add_u32 v252, s41, v252\n"
      "v_add_u32 v238, s80, v242\n"
      "v_and_b32 v238, 0x3ff, v238\n"
      "v_lshl_or_b32 v244, v236, 12, v237\n"
      "v_add_u32 v245, 31744, v244\n"
      "v_add_u32 v246, 63488, v244\n"
      "v_add_u32 v247, 95232, v244\n"
      "s_bitcmp1_b32 s34, 1\n"
      "s_cselect_b64 s[8:9], s[10:11], s[8:9]\n"
      "s_and_b32 s45, s34, 1\n"
      "s_lshl2_add_u32 s45, s45, s33\n"
      "s_lshl_b32 s45, s45, 9\n"
      "s_lshl_b32 s76, s36, 19\n"
      "s_add_u32 s45, s45, s76\n"
      "s_add_u32 s8, s8, s45\n"
      "s_addc_u32 s9, s9, 0\n"
      "s_lshr_b32 s39, s32, 6\n"
      "s_and_b32 s45, s35, 2\n"
      "s_xor_b32 s39, s39, s45\n"
      "s_xor_b32 s39, s39, s33\n"
      "s_lshl_b32 s45, s39, 7\n"
      "s_add_u32 s76, s33, 0\n"
      "s_and_b32 s76, s76, 3\n"
      "s_lshl_b32 s76, s76, 17\n"
      "s_or_b32 s76, s76, s45\n"
      "s_add_u32 s77, s33, 1\n"
      "s_and_b32 s77, s77, 3\n"
      "s_lshl_b32 s77, s77, 17\n"
      "s_or_b32 s77, s77, s45\n"
      "s_add_u32 s78, s33, 2\n"
      "s_and_b32 s78, s78, 3\n"
      "s_lshl_b32 s78, s78, 17\n"
      "s_or_b32 s78, s78, s45\n"
      "s_add_u32 s79, s33, 3\n"
      "s_and_b32 s79, s79, 3\n"
      "s_lshl_b32 s79, s79, 17\n"
      "s_or_b32 s79, s79, s45\n"
      "s_lshl2_add_u32 s43, s34, s35\n"
      "s_and_b32 s44, s40, 31\n"
      "s_mul_i32 s45, s43, 266240\n"
      "s_lshl_b32 s42, s44, 13\n"
      "s_add_u32 s45, s45, s42\n"
      "s_add_u32 s45, s45, 4096\n"
      "s_add_u32 s62, s18, s45\n"
      "s_addc_u32 s63, s19, 0\n"
      "s_bitcmp1_b32 s34, 1\n"
      "s_cselect_b32 s85, 0x80008000, 0\n"
      "s_lshl3_add_u32 s42, s43, s83\n"
      "s_lshl2_add_u32 s42, s42, s33\n"
      "s_lshl_b32 s42, s42, 9\n"
      "s_add_u32 s42, s42, 1048576\n"
      "s_mov_b32 s64, 0xaaaaaaaa\n"
      "s_mov_b32 s65, 0xaaaaaaaa\n"
      "s_mov_b32 s66, 0xcccccccc\n"
      "s_mov_b32 s67, 0xcccccccc\n"
      "s_add_u32 s45, s80, 256\n"
      "s_and_b32 s45, s45, 0x380\n"
      "s_add_u32 s46, s68, s45\n"
      "s_addc_u32 s47, s69, 0\n"
      "s_add_u32 m0, s41, 8192\n"
      "s_nop 0\n"
      "global_load_lds_dwordx4 v240, s[46:47] offset:0\n"
      "global_load_lds_dwordx4 v241, s[46:47] offset:1024\n"
      "global_load_lds_dwordx4 v254, s[46:47] offset:2048\n"
      "global_load_lds_dwordx4 v255, s[46:47] offset:3072\n"
      "s_add_u32 s45, s80, 384\n"
      "s_and_b32 s45, s45, 0x380\n"
      "s_add_u32 s46, s68, s45\n"
      "s_addc_u32 s47, s69, 0\n"
      "s_add_u32 m0, s41, 12288\n"
      "s_nop 0\n"
      "global_load_lds_dwordx4 v240, s[46:47] offset:0\n"
      "global_load_lds_dwordx4 v241, s[46:47] offset:1024\n"
      "global_load_lds_dwordx4 v254, s[46:47] offset:2048\n"
      "global_load_lds_dwordx4 v255, s[46:47] offset:3072\n"
      "s_waitcnt vmcnt(13)\n"
      "s_barrier\n"
      "ds_read_b128 v[0:3], v248 offset:0\n"
      "ds_read_b128 v[4:7], v248 offset:2048\n"
      "ds_read_b128 v[8:11], v249 offset:0\n"
      "ds_read_b128 v[12:15], v249 offset:2048\n"
      "ds_read_b128 a[192:195], v250 offset:0\n"
      "ds_read_b128 a[196:199], v250 offset:2048\n"
      "ds_read_b128 a[200:203], v250 offset:32768\n"
      "ds_read_b128 a[204:207], v251 offset:0\n"
      "ds_read_b128 a[208:211], v251 offset:2048\n"
      "ds_read_b128 a[212:215], v251 offset:32768\n"
      "s_add_u32 s45, s34, 1\n"
      "s_and_b32 s45, s45, 3\n"
      "s_lshl_b32 s45, s45, 15\n"
      "s_or_b32 s45, s45, s81\n"
      "s_xor_b32 s45, s45, 512\n"
      "s_add_u32 s46, s12, s45\n"
      "s_addc_u32 s47, s13, 0\n"
      "s_add_u32 m0, s82, 81920\n"
      "s_nop 0\n"
      "global_load_lds_dwordx4 v240, s[46:47] offset:0\n"
      "global_load_lds_dwordx4 v241, s[46:47] offset:1024\n"
      "global_load_lds_dwordx4 v254, s[46:47] offset:2048\n"
      "global_load_lds_dwordx4 v255, s[46:47] offset:3072\n"
      "s_add_u32 s45, s34, 2\n"
      "s_and_b32 s45, s45, 3\n"
      "s_lshl_b32 s45, s45, 15\n"
      "s_or_b32 s45, s45, s81\n"
      "s_xor_b32 s45, s45, 512\n"
      "s_add_u32 s46, s12, s45\n"
      "s_addc_u32 s47, s13, 0\n"
      "s_add_u32 m0, s82, 114688\n"
      "s_nop 0\n"
      "global_load_lds_dwordx4 v240, s[46:47] offset:0\n"
      "global_load_lds_dwordx4 v241, s[46:47] offset:1024\n"
      "s_waitcnt lgkmcnt(5)\n"
      "v_mfma_f32_16x16x32_bf16 v[192:195], v[0:3], a[192:195], 0\n"
      "s_add_u32 s45, s80, 512\n"
      "s_and_b32 s45, s45, 0x380\n"
      "s_add_u32 s46, s68, s45\n"
      "s_addc_u32 s47, s69, 0\n"
      "s_add_u32 m0, s41, 0\n"
      "s_nop 0\n"
      "global_load_lds_dwordx4 v240, s[46:47] offset:0\n"
      "global_load_lds_dwordx4 v241, s[46:47] offset:1024\n"
      "global_load_lds_dwordx4 v254, s[46:47] offset:2048\n"
      "global_load_lds_dwordx4 v255, s[46:47] offset:3072\n"
      "v_mfma_f32_16x16x32_bf16 v[196:199], v[4:7], a[192:195], 0\n"
      "ds_read_b128 a[216:219], v250 offset:4096\n"
      "ds_read_b128 a[220:223], v250 offset:6144\n"
      "ds_read_b128 a[224:227], v250 offset:36864\n"
      "s_waitcnt lgkmcnt(7)\n"
      "v_mfma_f32_16x16x32_bf16 v[200:203], v[0:3], a[196:199], 0\n"
      "ds_read_b128 a[228:231], v251 offset:4096\n"
      "ds_read_b128 a[232:235], v251 offset:6144\n"
      "ds_read_b128 a[236:239], v251 offset:36864\n"
      "v_mfma_f32_16x16x32_bf16 v[204:207], v[4:7], a[196:199], 0\n"
      "s_waitcnt vmcnt(19)\n"
      "ds_read_b128 v[16:19], v248 offset:4096\n"
      "ds_read_b128 v[20:23], v248 offset:6144\n"
      "s_waitcnt lgkmcnt(11)\n"
      "v_mfma_f32_16x16x32_bf16 v[208:211], v[0:3], a[200:203], 0\n"
      "ds_read_b128 v[24:27], v249 offset:4096\n"
      "ds_read_b128 v[28:31], v249 offset:6144\n"
      "v_mfma_f32_16x16x32_bf16 v[212:215], v[4:7], a[200:203], 0\n"
      "s_waitcnt lgkmcnt(12)\n"
      "v_mfma_f32_16x16x32_bf16 v[192:195], v[8:11], a[204:207], v[192:195]\n"
      "v_mfma_f32_16x16x32_bf16 v[196:199], v[12:15], a[204:207], v[196:199]\n"
      "s_waitcnt lgkmcnt(11)\n"
      "v_mfma_f32_16x16x32_bf16 v[200:203], v[8:11], a[208:211], v[200:203]\n"
      "v_mfma_f32_16x16x32_bf16 v[204:207], v[12:15], a[208:211], v[204:207]\n"
      "s_waitcnt lgkmcnt(10)\n"
      "v_mfma_f32_16x16x32_bf16 v[208:211], v[8:11], a[212:215], v[208:211]\n"
      "v_mfma_f32_16x16x32_bf16 v[212:215], v[12:15], a[212:215], v[212:215]\n"
      "s_waitcnt lgkmcnt(3)\n"
      "v_mfma_f32_16x16x32_bf16 v[192:195], v[16:19], a[216:219], v[192:195]\n"
      "s_waitcnt lgkmcnt(0)\n"
      "s_add_u32 s45, s80, 640\n"
      "s_and_b32 s45, s45, 0x380\n"
      "s_add_u32 s46, s68, s45\n"
      "s_addc_u32 s47, s69, 0\n"
      "s_add_u32 m0, s41, 4096\n"
      "s_nop 0\n"
      "global_load_lds_dwordx4 v240, s[46:47] offset:0\n"
      "global_load_lds_dwordx4 v241, s[46:47] offset:1024\n"
      "global_load_lds_dwordx4 v254, s[46:47] offset:2048\n"
      "global_load_lds_dwordx4 v255, s[46:47] offset:3072\n"
      "v_mfma_f32_16x16x32_bf16 v[196:199], v[20:23], a[216:219], v[196:199]\n"
      "ds_read_b128 a[240:243], v250 offset:8192\n"
      "ds_read_b128 a[244:247], v250 offset:10240\n"
      "ds_read_b128 a[248:251], v250 offset:40960\n"
      "v_mfma_f32_16x16x32_bf16 v[200:203], v[16:19], a[220:223], v[200:203]\n"
      "ds_read_b128 a[252:255], v251 offset:8192\n"
      "ds_read_b128 a[192:195], v251 offset:10240\n"
      "ds_read_b128 a[196:199], v251 offset:40960\n"
      "v_mfma_f32_16x16x32_bf16 v[204:207], v[20:23], a[220:223], v[204:207]\n"
      "s_waitcnt vmcnt(18)\n"
      "ds_read_b128 v[32:35], v248 offset:8192\n"
      "ds_read_b128 v[36:39], v248 offset:10240\n"
      "v_mfma_f32_16x16x32_bf16 v[208:211], v[16:19], a[224:227], v[208:211]\n"
      "ds_read_b128 v[40:43], v249 offset:8192\n"
      "ds_read_b128 v[44:47], v249 offset:10240\n"
      "v_mfma_f32_16x16x32_bf16 v[212:215], v[20:23], a[224:227], v[212:215]\n"
      "v_mfma_f32_16x16x32_bf16 v[192:195], v[24:27], a[228:231], v[192:195]\n"
      "v_mfma_f32_16x16x32_bf16 v[196:199], v[28:31], a[228:231], v[196:199]\n"
      "v_mfma_f32_16x16x32_bf16 v[200:203], v[24:27], a[232:235], v[200:203]\n"
      "v_mfma_f32_16x16x32_bf16 v[204:207], v[28:31], a[232:235], v[204:207]\n"
      "v_mfma_f32_16x16x32_bf16 v[208:211], v[24:27], a[236:239], v[208:211]\n"
      "v_mfma_f32_16x16x32_bf16 v[212:215], v[28:31], a[236:239], v[212:215]\n"
      "s_waitcnt lgkmcnt(3)\n"
      "v_mfma_f32_16x16x32_bf16 v[192:195], v[32:35], a[240:243], v[192:195]\n"
      "s_waitcnt lgkmcnt(0)\n"
      "s_add_u32 s45, s80, 768\n"
      "s_and_b32 s45, s45, 0x380\n"
      "s_add_u32 s46, s68, s45\n"
      "s_addc_u32 s47, s69, 0\n"
      "s_add_u32 m0, s41, 8192\n"
      "s_nop 0\n"
      "global_load_lds_dwordx4 v240, s[46:47] offset:0\n"
      "global_load_lds_dwordx4 v241, s[46:47] offset:1024\n"
      "global_load_lds_dwordx4 v254, s[46:47] offset:2048\n"
      "global_load_lds_dwordx4 v255, s[46:47] offset:3072\n"
      "v_mfma_f32_16x16x32_bf16 v[196:199], v[36:39], a[240:243], v[196:199]\n"
      "v_readfirstlane_b32 s37, v235\n"
      "s_nop 1\n"
      "s_mul_i32 s37, s37, 0x9E3779B1\n"
      "v_mfma_f32_16x16x32_bf16 v[200:203], v[32:35], a[244:247], v[200:203]\n"
      "s_add_u32 s37, s37, 0x7F4A7C15\n"
      "s_mov_b32 s38, 0x8000\n"
      "ds_read_b128 a[200:203], v250 offset:12288\n"
      "ds_read_b128 a[204:207], v250 offset:14336\n"
      "v_mfma_f32_16x16x32_bf16 v[204:207], v[36:39], a[244:247], v[204:207]\n"
      "ds_read_b128 a[208:211], v250 offset:45056\n"
      "ds_read_b128 a[212:215], v251 offset:12288\n"
      "ds_read_b128 a[216:219], v251 offset:14336\n"
      "v_mfma_f32_16x16x32_bf16 v[208:211], v[32:35], a[248:251], v[208:211]\n"
      "ds_read_b128 a[220:223], v251 offset:45056\n"
      "s_waitcnt vmcnt(18)\n"
      "ds_read_b128 v[48:51], v248 offset:12288\n"
      "v_mfma_f32_16x16x32_bf16 v[212:215], v[36:39], a[248:251], v[212:215]\n"
      "ds_read_b128 v[52:55], v248 offset:14336\n"
      "ds_read_b128 v[56:59], v249 offset:12288\n"
      "ds_read_b128 v[60:63], v249 offset:14336\n"
      "v_mfma_f32_16x16x32_bf16 v[192:195], v[40:43], a[252:255], v[192:195]\n"
      "v_mfma_f32_16x16x32_bf16 v[196:199], v[44:47], a[252:255], v[196:199]\n"
      "v_mfma_f32_16x16x32_bf16 v[200:203], v[40:43], a[192:195], v[200:203]\n"
      "v_mfma_f32_16x16x32_bf16 v[204:207], v[44:47], a[192:195], v[204:207]\n"
      "v_mfma_f32_16x16x32_bf16 v[208:211], v[40:43], a[196:199], v[208:211]\n"
      "v_mfma_f32_16x16x32_bf16 v[212:215], v[44:47], a[196:199], v[212:215]\n"
      "s_waitcnt lgkmcnt(3)\n"
      "v_mfma_f32_16x16x32_bf16 v[192:195], v[48:51], a[200:203], v[192:195]\n"
      "s_waitcnt lgkmcnt(0)\n"
      "s_add_u32 s45, s80, 896\n"
      "s_and_b32 s45, s45, 0x380\n"
      "s_add_u32 s46, s68, s45\n"
      "s_addc_u32 s47, s69, 0\n"
      "s_add_u32 m0, s41, 12288\n"
      "s_nop 0\n"
      "global_load_lds_dwordx4 v240, s[46:47] offset:0\n"
      "global_load_lds_dwordx4 v241, s[46:47] offset:1024\n"
      "global_load_lds_dwordx4 v254, s[46:47] offset:2048\n"
      "global_load_lds_dwordx4 v255, s[46:47] offset:3072\n"
      "v_mfma_f32_16x16x32_bf16 v[196:199], v[52:55], a[200:203], v[196:199]\n"
      "s_waitcnt vmcnt(16)\n"
      "s_barrier\n"
      "ds_read_b128 a[224:227], v250 offset:16384\n"
      "v_mfma_f32_16x16x32_bf16 v[200:203], v[48:51], a[204:207], v[200:203]\n"
      "ds_read_b128 a[228:231], v250 offset:18432\n"
      "ds_read_b128 a[232:235], v250 offset:49152\n"
      "ds_read_b128 a[236:239], v251 offset:16384\n"
      "v_mfma_f32_16x16x32_bf16 v[204:207], v[52:55], a[204:207], v[204:207]\n"
      "ds_read_b128 a[240:243], v251 offset:18432\n"
      "ds_read_b128 a[244:247], v251 offset:49152\n"
      "s_waitcnt vmcnt(12)\n"
      "ds_read_b128 v[64:67], v248 offset:0\n"
      "v_mfma_f32_16x16x32_bf16 v[208:211], v[48:51], a[208:211], v[208:211]\n"
      "ds_read_b128 v[68:71], v248 offset:2048\n"
      "ds_read_b128 v[72:75], v249 offset:0\n"
      "ds_read_b128 v[76:79], v249 offset:2048\n"
      "v_mfma_f32_16x16x32_bf16 v[212:215], v[52:55], a[208:211], v[212:215]\n"
      "v_mfma_f32_16x16x32_bf16 v[192:195], v[56:59], a[212:215], v[192:195]\n"
      "v_mfma_f32_16x16x32_bf16 v[196:199], v[60:63], a[212:215], v[196:199]\n"
      "v_mfma_f32_16x16x32_bf16 v[200:203], v[56:59], a[216:219], v[200:203]\n"
      "v_mfma_f32_16x16x32_bf16 v[204:207], v[60:63], a[216:219], v[204:207]\n"
      "v_mfma_f32_16x16x32_bf16 v[208:211], v[56:59], a[220:223], v[208:211]\n"
      "v_mfma_f32_16x16x32_bf16 v[212:215], v[60:63], a[220:223], v[212:215]\n"
      "s_waitcnt lgkmcnt(3)\n"
      "v_mfma_f32_16x16x32_bf16 v[192:195], v[64:67], a[224:227], v[192:195]\n"
      "s_waitcnt lgkmcnt(0)\n"
      "s_add_u32 m0, s41, 0\n"
      "s_nop 0\n"
      "global_load_lds_dwordx4 v238, s[24:25]\n"
      "s_add_u32 m0, s41, 1024\n"
      "s_nop 0\n"
      "global_load_lds_dwordx4 v238, s[22:23]\n"
      "v_mfma_f32_16x16x32_bf16 v[196:199], v[68:71], a[224:227], v[196:199]\n"
      "s_add_u32 s45, s34, 2\n"
      "s_and_b32 s45, s45, 3\n"
      "s_lshl_b32 s45, s45, 15\n"
      "s_or_b32 s45, s45, s81\n"
      "s_add_u32 s46, s12, s45\n"
      "s_addc_u32 s47, s13, 0\n"
      "s_add_u32 m0, s82, 98304\n"
      "s_nop 0\n"
      "global_load_lds_dwordx4 v254, s[46:47] offset:2048\n"
      "global_load_lds_dwordx4 v255, s[46:47] offset:3072\n"
      "v_mfma_f32_16x16x32_bf16 v[200:203], v[64:67], a[228:231], v[200:203]\n"
      "ds_read_b128 a[248:251], v250 offset:20480\n"
      "ds_read_b128 a[252:255], v250 offset:22528\n"
      "ds_read_b128 a[192:195], v250 offset:53248\n"
      "v_mfma_f32_16x16x32_bf16 v[204:207], v[68:71], a[228:231], v[204:207]\n"
      "ds_read_b128 a[196:199], v251 offset:20480\n"
      "ds_read_b128 a[200:203], v251 offset:22528\n"
      "ds_read_b128 a[204:207], v251 offset:53248\n"
      "v_mfma_f32_16x16x32_bf16 v[208:211], v[64:67], a[232:235], v[208:211]\n"
      "s_waitcnt vmcnt(12)\n"
      "ds_read_b128 v[80:83], v248 offset:4096\n"
      "ds_read_b128 v[84:87], v248 offset:6144\n"
      "v_mfma_f32_16x16x32_bf16 v[212:215], v[68:71], a[232:235], v[212:215]\n"
      "ds_read_b128 v[88:91], v249 offset:4096\n"
      "ds_read_b128 v[92:95], v249 offset:6144\n"
      "v_mfma_f32_16x16x32_bf16 v[192:195], v[72:75], a[236:239], v[192:195]\n"
      "v_mfma_f32_16x16x32_bf16 v[196:199], v[76:79], a[236:239], v[196:199]\n"
      "v_mfma_f32_16x16x32_bf16 v[200:203], v[72:75], a[240:243], v[200:203]\n"
      "v_mfma_f32_16x16x32_bf16 v[204:207], v[76:79], a[240:243], v[204:207]\n"
      "v_mfma_f32_16x16x32_bf16 v[208:211], v[72:75], a[244:247], v[208:211]\n"
      "v_mfma_f32_16x16x32_bf16 v[212:215], v[76:79], a[244:247], v[212:215]\n"
      "s_waitcnt lgkmcnt(3)\n"
      "v_mfma_f32_16x16x32_bf16 v[192:195], v[80:83], a[248:251], v[192:195]\n"
      "s_add_u32 s45, s34, 2\n"
      "s_and_b32 s45, s45, 3\n"
      "s_lshl_b32 s45, s45, 15\n"
      "s_or_b32 s45, s45, s81\n"
      "s_xor_b32 s45, s45, 512\n"
      "s_add_u32 s46, s12, s45\n"
      "s_addc_u32 s47, s13, 0\n"
      "s_add_u32 m0, s82, 114688\n"
      "s_nop 0\n"
      "global_load_lds_dwordx4 v254, s[46:47] offset:2048\n"
      "global_load_lds_dwordx4 v255, s[46:47] offset:3072\n"
      "s_waitcnt lgkmcnt(2)\n"
      "v_mfma_f32_16x16x32_bf16 v[196:199], v[84:87], a[248:251], v[196:199]\n"
      "s_add_u32 s45, s34, 3\n"
      "s_and_b32 s45, s45, 3\n"
      "s_lshl_b32 s45, s45, 15\n"
      "s_or_b32 s45, s45, s81\n"
      "s_add_u32 s46, s12, s45\n"
      "s_addc_u32 s47, s13, 0\n"
      "s_add_u32 m0, s82, 131072\n"
      "s_nop 0\n"
      "global_load_lds_dwordx4 v240, s[46:47] offset:0\n"
      "global_load_lds_dwordx4 v241, s[46:47] offset:1024\n"
      "global_load_lds_dwordx4 v254, s[46:47] offset:2048\n"
      "global_load_lds_dwordx4 v255, s[46:47] offset:3072\n"
      "v_mfma_f32_16x16x32_bf16 v[200:203], v[80:83], a[252:255], v[200:203]\n"
      "ds_read_b128 a[208:211], v250 offset:24576\n"
      "ds_read_b128 a[212:215], v250 offset:26624\n"
      "ds_read_b128 a[216:219], v250 offset:57344\n"
      "v_mfma_f32_16x16x32_bf16 v[204:207], v[84:87], a[252:255], v[204:207]\n"
      "ds_read_b128 a[220:223], v251 offset:24576\n"
      "ds_read_b128 a[224:227], v251 offset:26624\n"
      "ds_read_b128 a[228:231], v251 offset:57344\n"
      "v_mfma_f32_16x16x32_bf16 v[208:211], v[80:83], a[192:195], v[208:211]\n"
      "s_waitcnt vmcnt(14)\n"
      "ds_read_b128 v[96:99], v248 offset:8192\n"
      "ds_read_b128 v[100:103], v248 offset:10240\n"
      "v_mfma_f32_16x16x32_bf16 v[212:215], v[84:87], a[192:195], v[212:215]\n"
      "ds_read_b128 v[104:107], v249 offset:8192\n"
      "ds_read_b128 v[108:111], v249 offset:10240\n"
      "s_waitcnt lgkmcnt(11)\n"
      "v_mfma_f32_16x16x32_bf16 v[192:195], v[88:91], a[196:199], v[192:195]\n"
      "s_waitcnt lgkmcnt(10)\n"
      "v_mfma_f32_16x16x32_bf16 v[196:199], v[92:95], a[196:199], v[196:199]\n"
      "v_mfma_f32_16x16x32_bf16 v[200:203], v[88:91], a[200:203], v[200:203]\n"
      "v_mfma_f32_16x16x32_bf16 v[204:207], v[92:95], a[200:203], v[204:207]\n"
      "v_mfma_f32_16x16x32_bf16 v[208:211], v[88:91], a[204:207], v[208:211]\n"
      "v_mfma_f32_16x16x32_bf16 v[212:215], v[92:95], a[204:207], v[212:215]\n"
      "s_waitcnt lgkmcnt(3)\n"
      "v_mfma_f32_16x16x32_bf16 v[192:195], v[96:99], a[208:211], v[192:195]\n"
      "s_add_u32 s45, s34, 3\n"
      "s_and_b32 s45, s45, 3\n"
      "s_lshl_b32 s45, s45, 15\n"
      "s_or_b32 s45, s45, s81\n"
      "s_xor_b32 s45, s45, 512\n"
      "s_add_u32 s46, s12, s45\n"
      "s_addc_u32 s47, s13, 0\n"
      "s_add_u32 m0, s82, 147456\n"
      "s_nop 0\n"
      "global_load_lds_dwordx4 v240, s[46:47] offset:0\n"
      "global_load_lds_dwordx4 v241, s[46:47] offset:1024\n"
      "global_load_lds_dwordx4 v254, s[46:47] offset:2048\n"
      "global_load_lds_dwordx4 v255, s[46:47] offset:3072\n"
      "s_waitcnt lgkmcnt(2)\n"
      "v_mfma_f32_16x16x32_bf16 v[196:199], v[100:103], a[208:211], v[196:199]\n"
      "ds_read_b128 a[232:235], v250 offset:28672\n"
      "ds_read_b128 a[236:239], v250 offset:30720\n"
      "ds_read_b128 a[240:243], v250 offset:61440\n"
      "v_mfma_f32_16x16x32_bf16 v[200:203], v[96:99], a[212:215], v[200:203]\n"
      "ds_read_b128 a[244:247], v251 offset:28672\n"
      "ds_read_b128 a[248:251], v251 offset:30720\n"
      "ds_read_b128 a[252:255], v251 offset:61440\n"
      "v_mfma_f32_16x16x32_bf16 v[204:207], v[100:103], a[212:215], v[204:207]\n"
      "s_waitcnt vmcnt(14)\n"
      "ds_read_b128 v[112:115], v248 offset:12288\n"
      "ds_read_b128 v[116:119], v248 offset:14336\n"
      "v_mfma_f32_16x16x32_bf16 v[208:211], v[96:99], a[216:219], v[208:211]\n"
      "ds_read_b128 v[120:123], v249 offset:12288\n"
      "ds_read_b128 v[124:127], v249 offset:14336\n"
      "v_mfma_f32_16x16x32_bf16 v[212:215], v[100:103], a[216:219], v[212:215]\n"
      "s_waitcnt lgkmcnt(11)\n"
      "v_mfma_f32_16x16x32_bf16 v[192:195], v[104:107], a[220:223], v[192:195]\n"
      "s_waitcnt lgkmcnt(10)\n"
      "v_mfma_f32_16x16x32_bf16 v[196:199], v[108:111], a[220:223], v[196:199]\n"
      "v_mfma_f32_16x16x32_bf16 v[200:203], v[104:107], a[224:227], v[200:203]\n"
      "v_mfma_f32_16x16x32_bf16 v[204:207], v[108:111], a[224:227], v[204:207]\n"
      "v_mfma_f32_16x16x32_bf16 v[208:211], v[104:107], a[228:231], v[208:211]\n"
      "v_mfma_f32_16x16x32_bf16 v[212:215], v[108:111], a[228:231], v[212:215]\n"
      "s_waitcnt lgkmcnt(3)\n"
      "v_mfma_f32_16x16x32_bf16 v[192:195], v[112:115], a[232:235], v[192:195]\n"
      "s_waitcnt vmcnt(12)\n"
      "ds_read_b128 v[136:139], v252 offset:0\n"
      "s_waitcnt lgkmcnt(3)\n"
      "v_mfma_f32_16x16x32_bf16 v[196:199], v[116:119], a[232:235], v[196:199]\n"
      "ds_read_b128 v[140:143], v252 offset:64\n"
      "s_waitcnt lgkmcnt(3)\n"
      "v_mfma_f32_16x16x32_bf16 v[192:195], v[120:123], a[244:247], v[192:195]\n"
      "ds_read_b128 v[144:147], v252 offset:128\n"
      "s_waitcnt lgkmcnt(3)\n"
      "v_mfma_f32_16x16x32_bf16 v[196:199], v[124:127], a[244:247], v[196:199]\n"
      "v_mfma_f32_16x16x32_bf16 v[200:203], v[112:115], a[236:239], v[200:203]\n"
      "ds_read_b128 v[148:151], v252 offset:192\n"
      "v_mfma_f32_16x16x32_bf16 v[204:207], v[116:119], a[236:239], v[204:207]\n"
      "v_mfma_f32_16x16x32_bf16 v[200:203], v[120:123], a[248:251], v[200:203]\n"
      "ds_read_b128 v[152:155], v252 offset:256\n"
      "v_cvt_pk_bf16_f32 v224, v192, v193\n"
      "v_cvt_pk_bf16_f32 v225, v194, v195\n"
      "v_mfma_f32_16x16x32_bf16 v[204:207], v[124:127], a[248:251], v[204:207]\n"
      "v_cvt_pk_bf16_f32 v226, v196, v197\n"
      "v_cvt_pk_bf16_f32 v227, v198, v199\n"
      "v_xor_b32 v224, s85, v224\n"
      "v_mfma_f32_16x16x32_bf16 v[208:211], v[112:115], a[240:243], v[208:211]\n"
      "ds_read_b128 v[156:159], v252 offset:320\n"
      "v_xor_b32 v225, s85, v225\n"
      "v_xor_b32 v226, s85, v226\n"
      "v_mfma_f32_16x16x32_bf16 v[212:215], v[116:119], a[240:243], v[212:215]\n"
      "v_xor_b32 v227, s85, v227\n"
      "v_mfma_f32_16x16x32_bf16 v[208:211], v[120:123], a[252:255], v[208:211]\n"
      "ds_read_b128 v[160:163], v252 offset:384\n"
      "global_store_dwordx4 v242, v[224:227], s[62:63] offset:-4096 sc1\n"
      "v_cvt_pk_bf16_f32 v228, v200, v201\n"
      "v_mfma_f32_16x16x32_bf16 v[212:215], v[124:127], a[252:255], v[212:215]\n"
      "v_cvt_pk_bf16_f32 v229, v202, v203\n"
      "v_cvt_pk_bf16_f32 v230, v204, v205\n"
      "v_cvt_pk_bf16_f32 v231, v206, v207\n"
      "s_waitcnt lgkmcnt(6)\n"
      "v_mfma_f32_16x16x32_bf16 v[128:131], v[0:3], v[136:139], 0\n"
      "ds_read_b128 v[164:167], v252 offset:448\n"
      "v_xor_b32 v228, s85, v228\n"
      "v_xor_b32 v229, s85, v229\n"
      "v_mfma_f32_16x16x32_bf16 v[132:135], v[4:7], v[136:139], 0\n"
      "v_xor_b32 v230, s85, v230\n"
      "v_xor_b32 v231, s85, v231\n"
      "s_waitcnt lgkmcnt(6)\n"
      "v_mfma_f32_16x16x32_bf16 v[128:131], v[8:11], v[140:143], v[128:131]\n"
      "ds_read_b128 v[136:139], v252 offset:512\n"
      "global_store_dwordx4 v242, v[228:231], s[62:63] offset:-3072 sc1\n"
      "v_cvt_pk_bf16_f32 v232, v208, v209\n"
      "v_mfma_f32_16x16x32_bf16 v[132:135], v[12:15], v[140:143], v[132:135]\n"
      "v_cvt_pk_bf16_f32 v233, v210, v211\n"
      "v_cvt_pk_bf16_f32 v234, v212, v213\n"
      "v_cvt_pk_bf16_f32 v235, v214, v215\n"
      "s_waitcnt lgkmcnt(6)\n"
      "v_mfma_f32_16x16x32_bf16 v[128:131], v[16:19], v[144:147], v[128:131]\n"
      "ds_read_b128 v[140:143], v252 offset:576\n"
      "v_xor_b32 v232, s85, v232\n"
      "v_xor_b32 v233, s85, v233\n"
      "v_mfma_f32_16x16x32_bf16 v[132:135], v[20:23], v[144:147], v[132:135]\n"
      "v_xor_b32 v234, s85, v234\n"
      "v_xor_b32 v235, s85, v235\n"
      "s_waitcnt lgkmcnt(6)\n"
      "v_mfma_f32_16x16x32_bf16 v[128:131], v[24:27], v[148:151], v[128:131]\n"
      "ds_read_b128 v[144:147], v252 offset:640\n"
      "global_store_dwordx4 v242, v[232:235], s[62:63] offset:-2048 sc1\n"
      "v_mfma_f32_16x16x32_bf16 v[132:135], v[28:31], v[148:151], v[132:135]\n"
      "s_xor_b32 s70, s76, 0\n"
      "s_add_u32 s70, s8, s70\n"
      "s_addc_u32 s71, s9, 0\n"
      "s_add_u32 m0, s41, 4096\n"
      "s_waitcnt lgkmcnt(6)\n"
      "v_mfma_f32_16x16x32_bf16 v[128:131], v[32:35], v[152:155], v[128:131]\n"
      "s_add_u32 s48, s82, 32768\n"
      "ds_read_b128 v[148:151], v252 offset:704\n"
      "global_load_lds_dwordx4 v244, s[70:71] offset:0\n"
      "v_subrev_u32 v236, s41, v248\n"
      "v_mfma_f32_16x16x32_bf16 v[132:135], v[36:39], v[152:155], v[132:135]\n"
      "s_add_u32 s48, s48, 65536\n"
      "s_waitcnt vmcnt(4)\n"
      "s_barrier\n"
      "s_waitcnt lgkmcnt(6)\n"
      "v_mfma_f32_16x16x32_bf16 v[128:131], v[40:43], v[156:159], v[128:131]\n"
      "ds_read_b128 v[152:155], v252 offset:768\n"
      "ds_read_b128 a[192:195], v250 offset:34816\n"
      "v_add_u32 v236, s48, v236\n"
      "v_mfma_f32_16x16x32_bf16 v[132:135], v[44:47], v[156:159], v[132:135]\n"
      "ds_read_b128 a[196:199], v251 offset:34816\n"
      "ds_read_b128 a[200:203], v250 offset:38912\n"
      "v_xor_b32 v237, 64, v236\n"
      "s_waitcnt lgkmcnt(9)\n"
      "v_mfma_f32_16x16x32_bf16 v[128:131], v[48:51], v[160:163], v[128:131]\n"
      "ds_read_b128 v[156:159], v252 offset:832\n"
      "global_load_lds_dwordx4 v245, s[70:71] offset:1024\n"
      "v_mfma_f32_16x16x32_bf16 v[132:135], v[52:55], v[160:163], v[132:135]\n"
      "ds_read_b128 a[204:207], v251 offset:38912\n"
      "ds_read_b128 a[208:211], v250 offset:43008\n"
      "s_waitcnt lgkmcnt(11)\n"
      "v_mfma_f32_16x16x32_bf16 v[128:131], v[56:59], v[164:167], v[128:131]\n"
      "ds_read_b128 v[160:163], v252 offset:896\n"
      "ds_read_b128 a[212:215], v251 offset:43008\n"
      "v_mfma_f32_16x16x32_bf16 v[132:135], v[60:63], v[164:167], v[132:135]\n"
      "ds_read_b128 a[216:219], v250 offset:47104\n"
      "s_waitcnt lgkmcnt(13)\n"
      "v_mfma_f32_16x16x32_bf16 v[128:131], v[64:67], v[136:139], v[128:131]\n"
      "ds_read_b128 v[164:167], v252 offset:960\n"
      "global_load_lds_dwordx4 v246, s[70:71] offset:2048\n"
      "v_mfma_f32_16x16x32_bf16 v[132:135], v[68:71], v[136:139], v[132:135]\n"
      "ds_read_b128 a[220:223], v251 offset:47104\n"
      "s_waitcnt lgkmcnt(14)\n"
      "v_mfma_f32_16x16x32_bf16 v[128:131], v[72:75], v[140:143], v[128:131]\n"
      "v_mfma_f32_16x16x32_bf16 v[132:135], v[76:79], v[140:143], v[132:135]\n"
      "ds_read_b128 a[224:227], v250 offset:51200\n"
      "s_waitcnt lgkmcnt(14)\n"
      "v_mfma_f32_16x16x32_bf16 v[128:131], v[80:83], v[144:147], v[128:131]\n"
      "global_load_lds_dwordx4 v247, s[70:71] offset:3072\n"
      "v_mfma_f32_16x16x32_bf16 v[132:135], v[84:87], v[144:147], v[132:135]\n"
      "ds_read_b128 a[228:231], v251 offset:51200\n"
      "s_waitcnt lgkmcnt(14)\n"
      "v_mfma_f32_16x16x32_bf16 v[128:131], v[88:91], v[148:151], v[128:131]\n"
      "v_mfma_f32_16x16x32_bf16 v[132:135], v[92:95], v[148:151], v[132:135]\n"
      "s_lshl_b32 s70, s34, 15\n"
      "s_or_b32 s70, s70, s81\n"
      "s_add_u32 s70, s12, s70\n"
      "s_addc_u32 s71, s13, 0\n"
      "s_add_u32 m0, s82, 65536\n"
      "s_waitcnt lgkmcnt(13)\n"
      "v_mfma_f32_16x16x32_bf16 v[128:131], v[96:99], v[152:155], v[128:131]\n"
      "global_load_lds_dwordx4 v240, s[70:71] offset:0\n"
      "ds_read_b128 a[232:235], v250 offset:55296\n"
      "v_mfma_f32_16x16x32_bf16 v[132:135], v[100:103], v[152:155], v[132:135]\n"
      "ds_read_b128 a[236:239], v251 offset:55296\n"
      "s_waitcnt lgkmcnt(11)\n"
      "v_mfma_f32_16x16x32_bf16 v[128:131], v[104:107], v[156:159], v[128:131]\n"
      "v_mfma_f32_16x16x32_bf16 v[132:135], v[108:111], v[156:159], v[132:135]\n"
      "ds_read_b128 a[240:243], v250 offset:59392\n"
      "s_waitcnt lgkmcnt(9)\n"
      "v_mfma_f32_16x16x32_bf16 v[128:131], v[112:115], v[160:163], v[128:131]\n"
      "global_load_lds_dwordx4 v241, s[70:71] offset:1024\n"
      "v_mfma_f32_16x16x32_bf16 v[132:135], v[116:119], v[160:163], v[132:135]\n"
      "ds_read_b128 a[244:247], v251 offset:59392\n"
      "s_waitcnt lgkmcnt(7)\n"
      "v_mfma_f32_16x16x32_bf16 v[128:131], v[120:123], v[164:167], v[128:131]\n"
      "v_mfma_f32_16x16x32_bf16 v[132:135], v[124:127], v[164:167], v[132:135]\n"
      "ds_read_b128 a[248:251], v250 offset:63488\n"
      "v_mfma_f32_16x16x32_bf16 v[216:219], v[0:3], a[192:195], 0\n"
      "global_load_lds_dwordx4 v254, s[70:71] offset:2048\n"
      "v_mfma_f32_16x16x32_bf16 v[220:223], v[4:7], a[192:195], 0\n"
      "ds_read_b128 a[252:255], v251 offset:63488\n"
      "v_xor_b32 v250, 196608, v250\n"
      "v_xor_b32 v251, 196608, v251\n"
      "v_mfma_f32_16x16x32_bf16 v[216:219], v[8:11], a[196:199], v[216:219]\n"
      "s_mov_b32 s22, 0xf0f0f0f0\n"
      "s_mov_b32 s23, 0xf0f0f0f0\n"
      "v_cndmask_b32 v128, v128, v129, s[64:65]\n"
      "v_cndmask_b32 v130, v130, v131, s[64:65]\n"
      "v_mfma_f32_16x16x32_bf16 v[220:223], v[12:15], a[196:199], v[220:223]\n"
      "ds_read_b128 a[192:195], v250 offset:0\n"
      "v_cndmask_b32 v132, v132, v133, s[64:65]\n"
      "v_cndmask_b32 v134, v134, v135, s[64:65]\n"
      "v_mfma_f32_16x16x32_bf16 v[216:219], v[16:19], a[200:203], v[216:219]\n"
      "global_load_lds_dwordx4 v255, s[70:71] offset:3072\n"
      "v_cndmask_b32 v128, v128, v130, s[66:67]\n"
      "v_cndmask_b32 v132, v132, v134, s[66:67]\n"
      "v_mfma_f32_16x16x32_bf16 v[220:223], v[20:23], a[200:203], v[220:223]\n"
      "ds_read_b128 a[196:199], v250 offset:2048\n"
      "v_lshrrev_b32 v133, 4, v243\n"
      "v_cndmask_b32 v128, v128, v132, s[22:23]\n"
      "v_mfma_f32_16x16x32_bf16 v[216:219], v[24:27], a[204:207], v[216:219]\n"
      "v_and_b32 v134, 7, v243\n"
      "v_cvt_pk_bf16_f32 v128, v128, v128\n"
      "s_lshl_b32 s48, s40, 5\n"
      "v_mfma_f32_16x16x32_bf16 v[220:223], v[28:31], a[204:207], v[220:223]\n"
      "ds_read_b128 a[200:203], v251 offset:0\n"
      "v_and_b32 v128, 0xffff0000, v128\n"
      "v_lshl_or_b32 v133, v133, 3, v134\n"
      "v_mfma_f32_16x16x32_bf16 v[216:219], v[32:35], a[208:211], v[216:219]\n"
      "s_lshl_b32 s70, s34, 15\n"
      "s_or_b32 s70, s70, s81\n"
      "s_xor_b32 s70, s70, 512\n"
      "s_add_u32 s70, s12, s70\n"
      "s_addc_u32 s71, s13, 0\n"
      "s_add_u32 m0, s82, 81920\n"
      "v_mfma_f32_16x16x32_bf16 v[220:223], v[36:39], a[208:211], v[220:223]\n"
      "global_load_lds_dwordx4 v240, s[70:71] offset:0\n"
      "ds_read_b128 a[204:207], v251 offset:2048\n"
      "v_add_u32 v133, s48, v133\n"
      "v_mfma_f32_16x16x32_bf16 v[216:219], v[40:43], a[212:215], v[216:219]\n"
      "v_ashrrev_i32 v129, 31, v128\n"
      "v_mul_f32 v130, 0xbfb8aa3b, v128\n"
      "v_or_b32 v129, 0x80000000, v129\n"
      "v_mfma_f32_16x16x32_bf16 v[220:223], v[44:47], a[212:215], v[220:223]\n"
      "ds_read_b128 a[208:211], v250 offset:4096\n"
      "v_exp_f32 v130, v130\n"
      "v_xor_b32 v129, v129, v128\n"
      "v_mfma_f32_16x16x32_bf16 v[216:219], v[48:51], a[216:219], v[216:219]\n"
      "v_sub_u32 v132, 0xfff, v133\n"
      "v_add_f32 v130, 1.0, v130\n"
      "s_mov_b32 s49, 0xffff0000\n"
      "v_mfma_f32_16x16x32_bf16 v[220:223], v[52:55], a[216:219], v[220:223]\n"
      "global_load_lds_dwordx4 v241, s[70:71] offset:1024\n"
      "ds_read_b128 a[212:215], v250 offset:6144\n"
      "v_and_or_b32 v129, v129, s49, v132\n"
      "s_waitcnt lgkmcnt(14)\n"
      "v_mfma_f32_16x16x32_bf16 v[216:219], v[56:59], a[220:223], v[216:219]\n"
      "v_rcp_f32 v130, v130\n"
      "s_mov_b32 s64, 0x00ff00ff\n"
      "s_mov_b32 s65, 0x00ff00ff\n"
      "v_mfma_f32_16x16x32_bf16 v[220:223], v[60:63], a[220:223], v[220:223]\n"
      "ds_read_b128 a[216:219], v251 offset:4096\n"
      "v_cndmask_b32 v129, v129, 0, s[64:65]\n"
      "v_cvt_pk_bf16_f32 v130, v130, v130\n"
      "s_waitcnt lgkmcnt(14)\n"
      "v_mfma_f32_16x16x32_bf16 v[216:219], v[64:67], a[224:227], v[216:219]\n"
      "s_lshr_b32 s48, s34, 1\n"
      "v_add_u32 v238, s42, v242\n"
      "v_mov_b32 v224, s37\n"
      "v_mov_b32 v225, s37\n"
      "v_mov_b32 v226, s37\n"
      "v_mov_b32 v227, s37\n"
      "s_waitcnt vmcnt(10)\n"
      "s_mov_b64 exec, 0xff\n"
      "global_store_dwordx4 v238, v[224:227], s[58:59] offset:0 sc1\n"
      "s_mov_b64 exec, -1\n"
      "v_mfma_f32_16x16x32_bf16 v[220:223], v[68:71], a[224:227], v[220:223]\n"
      "s_lshl2_add_u32 s48, s48, s35\n"
      "ds_read_b128 a[220:223], v251 offset:6144\n"
      "v_max_u32_dpp v129, v129, v129 quad_perm:[1,0,3,2] row_mask:0xf bank_mask:0xf\n"
      "v_and_b32 v130, 0xffff0000, v130\n"
      "s_waitcnt lgkmcnt(14)\n"
      "v_mfma_f32_16x16x32_bf16 v[216:219], v[72:75], a[228:231], v[216:219]\n"
      "s_lshl_b32 s49, s48, 15\n"
      "global_load_lds_dwordx4 v254, s[70:71] offset:2048\n"
      "v_max_u32_dpp v129, v129, v129 quad_perm:[2,3,0,1] row_mask:0xf bank_mask:0xf\n"
      "s_add_u32 s49, s49, 786432\n"
      "v_mfma_f32_16x16x32_bf16 v[220:223], v[76:79], a[228:231], v[220:223]\n"
      "ds_read_b128 a[224:227], v250 offset:8192\n"
      "v_lshl_add_u32 v133, v133, 4, s49\n"
      "v_max_u32_dpp v129, v129, v129 row_half_mirror row_mask:0xf bank_mask:0xf\n"
      "s_waitcnt lgkmcnt(14)\n"
      "v_mfma_f32_16x16x32_bf16 v[216:219], v[80:83], a[232:235], v[216:219]\n"
      "s_lshl_b32 s48, s48, 6\n"
      "s_add_u32 s48, s48, s40\n"
      "v_max_u32_dpp v129, v129, v129 row_mirror row_mask:0xf bank_mask:0xf\n"
      "s_lshl_b32 s48, s48, 6\n"
      "v_mfma_f32_16x16x32_bf16 v[220:223], v[84:87], a[232:235], v[220:223]\n"
      "s_add_u32 s48, s48, 4096\n"
      "ds_read_b128 a[228:231], v250 offset:10240\n"
      "v_mov_b32 v131, v129\n"
      "v_lshl_add_u32 v132, v134, 4, s48\n"
      "s_waitcnt lgkmcnt(14)\n"
      "v_mfma_f32_16x16x32_bf16 v[216:219], v[88:91], a[236:239], v[216:219]\n"
      "global_load_lds_dwordx4 v255, s[70:71] offset:3072\n"
      "v_mov_b32 v137, s37\n"
      "v_permlane16_swap_b32 v129, v131\n"
      "v_mfma_f32_16x16x32_bf16 v[220:223], v[92:95], a[236:239], v[220:223]\n"
      "ds_read_b128 a[232:235], v251 offset:8192\n"
      "v_mov_b32 v138, s37\n"
      "v_mov_b32 v139, s37\n"
      "s_waitcnt lgkmcnt(14)\n"
      "v_mfma_f32_16x16x32_bf16 v[216:219], v[96:99], a[240:243], v[216:219]\n"
      "v_max_u32 v129, v129, v131\n"
      "v_cndmask_b32 v133, v132, v133, s[64:65]\n"
      "v_mov_b32 v131, v129\n"
      "v_mfma_f32_16x16x32_bf16 v[220:223], v[100:103], a[240:243], v[220:223]\n"
      "ds_read_b128 a[236:239], v251 offset:10240\n"
      "v_permlane32_swap_b32 v129, v131\n"
      "v_max_u32 v129, v129, v131\n"
      "s_waitcnt lgkmcnt(14)\n"
      "v_mfma_f32_16x16x32_bf16 v[216:219], v[104:107], a[244:247], v[216:219]\n"
      "s_xor_b32 s70, s77, 0\n"
      "s_add_u32 s70, s8, s70\n"
      "s_addc_u32 s71, s9, 0\n"
      "s_add_u32 m0, s41, 8192\n"
      "v_mfma_f32_16x16x32_bf16 v[220:223], v[108:111], a[244:247], v[220:223]\n"
      "global_load_lds_dwordx4 v244, s[70:71] offset:0\n"
      "ds_read_b128 a[240:243], v250 offset:12288\n"
      "v_cndmask_b32 v136, v129, v130, s[64:65]\n"
      "s_waitcnt lgkmcnt(14)\n"
      "v_mfma_f32_16x16x32_bf16 v[216:219], v[112:115], a[248:251], v[216:219]\n"
      "s_mov_b32 s64, 0x00ff0fff\n"
      "s_mov_b64 exec, s[64:65]\n"
      "global_store_dwordx4 v133, v[136:139], s[58:59] sc1\n"
      "s_mov_b64 exec, -1\n"
      "v_mfma_f32_16x16x32_bf16 v[220:223], v[116:119], a[248:251], v[220:223]\n"
      "ds_read_b128 a[244:247], v250 offset:14336\n"
      "s_waitcnt lgkmcnt(14)\n"
      "v_mfma_f32_16x16x32_bf16 v[216:219], v[120:123], a[252:255], v[216:219]\n"
      "v_mfma_f32_16x16x32_bf16 v[220:223], v[124:127], a[252:255], v[220:223]\n"
      "ds_read_b128 a[248:251], v251 offset:12288\n"
      "s_waitcnt lgkmcnt(14)\n"
      "v_mfma_f32_16x16x32_bf16 v[192:195], v[0:3], a[192:195], 0\n"
      "global_load_lds_dwordx4 v245, s[70:71] offset:1024\n"
      "v_mfma_f32_16x16x32_bf16 v[196:199], v[4:7], a[192:195], 0\n"
      "ds_read_b128 a[252:255], v251 offset:14336\n"
      "s_waitcnt vmcnt(12)\n"
      "ds_read_b128 a[0:3], v248 offset:4096\n"
      "s_waitcnt lgkmcnt(15)\n"
      "v_mfma_f32_16x16x32_bf16 v[200:203], v[0:3], a[196:199], 0\n"
      "ds_read_b128 a[4:7], v248 offset:6144\n"
      "ds_read_b128 a[32:35], v249 offset:4096\n"
      "v_cvt_pk_bf16_f32 v224, v216, v217\n"
      "v_mfma_f32_16x16x32_bf16 v[204:207], v[4:7], a[196:199], 0\n"
      "ds_read_b128 a[192:195], v250 offset:16384\n"
      "ds_read_b128 a[36:39], v249 offset:6144\n"
      "v_cvt_pk_bf16_f32 v225, v218, v219\n"
      "s_waitcnt lgkmcnt(15)\n"
      "v_mfma_f32_16x16x32_bf16 v[192:195], v[8:11], a[200:203], v[192:195]\n"
      "global_load_lds_dwordx4 v246, s[70:71] offset:2048\n"
      "v_cvt_pk_bf16_f32 v226, v220, v221\n"
      "v_cvt_pk_bf16_f32 v227, v222, v223\n"
      "v_mfma_f32_16x16x32_bf16 v[196:199], v[12:15], a[200:203], v[196:199]\n"
      "ds_read_b128 a[196:199], v250 offset:18432\n"
      "v_xor_b32 v224, s85, v224\n"
      "v_xor_b32 v225, s85, v225\n"
      "v_mfma_f32_16x16x32_bf16 v[200:203], v[8:11], a[204:207], v[200:203]\n"
      "v_xor_b32 v226, s85, v226\n"
      "v_xor_b32 v227, s85, v227\n"
      "s_lshl2_add_u32 s54, s34, s35\n"
      "v_mfma_f32_16x16x32_bf16 v[204:207], v[12:15], a[204:207], v[204:207]\n"
      "s_lshr_b32 s56, s39, 1\n"
      "ds_read_b128 a[200:203], v251 offset:16384\n"
      "s_lshl1_add_u32 s56, s33, s56\n"
      "s_lshl_b32 s56, s56, 15\n"
      "v_mfma_f32_16x16x32_bf16 v[192:195], v[16:19], a[208:211], v[192:195]\n"
      "s_mul_i32 s55, s54, 266240\n"
      "global_load_lds_dwordx4 v247, s[70:71] offset:3072\n"
      "s_add_u32 s55, s55, 4096\n"
      "s_add_u32 s14, s18, s55\n"
      "s_addc_u32 s15, s19, 0\n"
      "v_mfma_f32_16x16x32_bf16 v[196:199], v[20:23], a[208:211], v[196:199]\n"
      "s_add_u32 s10, s14, s56\n"
      "s_addc_u32 s11, s15, 0\n"
      "ds_read_b128 a[204:207], v251 offset:18432\n"
      "v_mfma_f32_16x16x32_bf16 v[200:203], v[16:19], a[212:215], v[200:203]\n"
      "s_xor_b32 s56, s56, 0x8000\n"
      "s_add_u32 s14, s14, s56\n"
      "s_addc_u32 s15, s15, 0\n"
      "s_and_b32 s55, s39, 1\n"
      "v_mfma_f32_16x16x32_bf16 v[204:207], v[20:23], a[212:215], v[204:207]\n"
      "s_lshl_b32 s56, s55, 14\n"
      "s_xor_b32 s70, s78, 0\n"
      "s_add_u32 s70, s8, s70\n"
      "s_addc_u32 s71, s9, 0\n"
      "s_add_u32 m0, s41, 12288\n"
      "s_waitcnt lgkmcnt(15)\n"
      "v_mfma_f32_16x16x32_bf16 v[192:195], v[24:27], a[216:219], v[192:195]\n"
      "global_load_lds_dwordx4 v244, s[70:71] offset:0\n"
      "ds_read_b128 a[208:211], v250 offset:20480\n"
      "v_add_u32 v240, s56, v242\n"
      "v_mfma_f32_16x16x32_bf16 v[196:199], v[28:31], a[216:219], v[196:199]\n"
      "ds_read_b128 a[212:215], v250 offset:22528\n"
      "v_xor_b32 v241, 0x4000, v240\n"
      "s_add_u32 s72, s10, 0x2000\n"
      "s_addc_u32 s73, s11, 0\n"
      "v_mfma_f32_16x16x32_bf16 v[200:203], v[24:27], a[220:223], v[200:203]\n"
      "s_add_u32 s74, s14, 0x2000\n"
      "s_addc_u32 s75, s15, 0\n"
      "s_lshl2_add_u32 s55, s54, s33\n"
      "s_lshl_b32 s55, s55, 12\n"
      "v_mfma_f32_16x16x32_bf16 v[204:207], v[28:31], a[220:223], v[204:207]\n"
      "s_add_u32 s55, s55, 1048576\n"
      "ds_read_b128 a[216:219], v251 offset:20480\n"
      "v_and_b32 v253, 7, v243\n"
      "v_lshl_add_u32 v253, v253, 9, s55\n"
      "v_mfma_f32_16x16x32_bf16 v[192:195], v[32:35], a[224:227], v[192:195]\n"
      "global_load_lds_dwordx4 v245, s[70:71] offset:1024\n"
      "v_mfma_f32_16x16x32_bf16 v[196:199], v[36:39], a[224:227], v[196:199]\n"
      "ds_read_b128 a[220:223], v251 offset:22528\n"
      "s_waitcnt vmcnt(7)\n"
      "s_barrier\n"
      "s_waitcnt lgkmcnt(15)\n"
      "v_mfma_f32_16x16x32_bf16 v[200:203], v[32:35], a[228:231], v[200:203]\n"
      "v_mfma_f32_16x16x32_bf16 v[204:207], v[36:39], a[228:231], v[204:207]\n"
      "ds_read_b128 a[224:227], v250 offset:24576\n"
      "v_mfma_f32_16x16x32_bf16 v[192:195], v[40:43], a[232:235], v[192:195]\n"
      "global_load_lds_dwordx4 v246, s[70:71] offset:2048\n"
      "v_mfma_f32_16x16x32_bf16 v[196:199], v[44:47], a[232:235], v[196:199]\n"
      "ds_read_b128 a[228:231], v250 offset:26624\n"
      "v_mfma_f32_16x16x32_bf16 v[200:203], v[40:43], a[236:239], v[200:203]\n"
      "v_mfma_f32_16x16x32_bf16 v[204:207], v[44:47], a[236:239], v[204:207]\n"
      "ds_read_b128 a[232:235], v251 offset:24576\n"
      "v_mfma_f32_16x16x32_bf16 v[192:195], v[48:51], a[240:243], v[192:195]\n"
      "global_load_lds_dwordx4 v247, s[70:71] offset:3072\n"
      "v_mfma_f32_16x16x32_bf16 v[196:199], v[52:55], a[240:243], v[196:199]\n"
      "ds_read_b128 a[236:239], v251 offset:26624\n"
      "s_waitcnt lgkmcnt(15)\n"
      "v_mfma_f32_16x16x32_bf16 v[200:203], v[48:51], a[244:247], v[200:203]\n"
      "v_mfma_f32_16x16x32_bf16 v[204:207], v[52:55], a[244:247], v[204:207]\n"
      "s_xor_b32 s70, s79, 0\n"
      "s_add_u32 s70, s8, s70\n"
      "s_addc_u32 s71, s9, 0\n"
      "s_add_u32 m0, s82, 98304\n"
      "v_mfma_f32_16x16x32_bf16 v[192:195], v[56:59], a[248:251], v[192:195]\n"
      "global_load_lds_dwordx4 v244, s[70:71] offset:0\n"
      "ds_read_b128 a[240:243], v250 offset:28672\n"
      "v_mfma_f32_16x16x32_bf16 v[196:199], v[60:63], a[248:251], v[196:199]\n"
      "ds_read_b128 a[244:247], v250 offset:30720\n"
      "v_mfma_f32_16x16x32_bf16 v[200:203], v[56:59], a[252:255], v[200:203]\n"
      "v_mfma_f32_16x16x32_bf16 v[204:207], v[60:63], a[252:255], v[204:207]\n"
      "ds_read_b128 a[248:251], v251 offset:28672\n"
      "s_waitcnt lgkmcnt(15)\n"
      "v_mfma_f32_16x16x32_bf16 v[192:195], v[64:67], a[192:195], v[192:195]\n"
      "global_load_lds_dwordx4 v245, s[70:71] offset:1024\n"
      "v_mfma_f32_16x16x32_bf16 v[196:199], v[68:71], a[192:195], v[196:199]\n"
      "ds_read_b128 a[252:255], v251 offset:30720\n"
      "v_xor_b32 v250, 196608, v250\n"
      "v_xor_b32 v251, 196608, v251\n"
      "s_waitcnt lgkmcnt(14)\n"
      "v_mfma_f32_16x16x32_bf16 v[200:203], v[64:67], a[196:199], v[200:203]\n"
      "v_mfma_f32_16x16x32_bf16 v[204:207], v[68:71], a[196:199], v[204:207]\n"
      "ds_read_b128 a[192:195], v250 offset:0\n"
      "s_waitcnt lgkmcnt(14)\n"
      "v_mfma_f32_16x16x32_bf16 v[192:195], v[72:75], a[200:203], v[192:195]\n"
      "global_load_lds_dwordx4 v246, s[70:71] offset:2048\n"
      "v_mfma_f32_16x16x32_bf16 v[196:199], v[76:79], a[200:203], v[196:199]\n"
      "ds_read_b128 a[196:199], v250 offset:2048\n"
      "s_waitcnt lgkmcnt(14)\n"
      "v_mfma_f32_16x16x32_bf16 v[200:203], v[72:75], a[204:207], v[200:203]\n"
      "v_mfma_f32_16x16x32_bf16 v[204:207], v[76:79], a[204:207], v[204:207]\n"
      "ds_read_b128 a[200:203], v251 offset:0\n"
      "s_waitcnt lgkmcnt(14)\n"
      "v_mfma_f32_16x16x32_bf16 v[192:195], v[80:83], a[208:211], v[192:195]\n"
      "global_load_lds_dwordx4 v247, s[70:71] offset:3072\n"
      "v_mfma_f32_16x16x32_bf16 v[196:199], v[84:87], a[208:211], v[196:199]\n"
      "ds_read_b128 a[204:207], v251 offset:2048\n"
      "s_waitcnt lgkmcnt(14)\n"
      "v_mfma_f32_16x16x32_bf16 v[200:203], v[80:83], a[212:215], v[200:203]\n"
      "v_mfma_f32_16x16x32_bf16 v[204:207], v[84:87], a[212:215], v[204:207]\n"
      "s_xor_b32 s70, s76, 256\n"
      "s_add_u32 s70, s8, s70\n"
      "s_addc_u32 s71, s9, 0\n"
      "s_add_u32 m0, s82, 114688\n"
      "s_waitcnt lgkmcnt(13)\n"
      "v_mfma_f32_16x16x32_bf16 v[192:195], v[88:91], a[216:219], v[192:195]\n"
      "global_load_lds_dwordx4 v244, s[70:71] offset:0\n"
      "ds_read_b128 a[208:211], v250 offset:4096\n"
      "v_mfma_f32_16x16x32_bf16 v[196:199], v[92:95], a[216:219], v[196:199]\n"
      "ds_read_b128 a[212:215], v250 offset:6144\n"
      "s_waitcnt lgkmcnt(14)\n"
      "v_mfma_f32_16x16x32_bf16 v[200:203], v[88:91], a[220:223], v[200:203]\n"
      "v_mfma_f32_16x16x32_bf16 v[204:207], v[92:95], a[220:223], v[204:207]\n"
      "ds_read_b128 a[216:219], v251 offset:4096\n"
      "s_waitcnt lgkmcnt(14)\n"
      "v_mfma_f32_16x16x32_bf16 v[192:195], v[96:99], a[224:227], v[192:195]\n"
      "global_load_lds_dwordx4 v245, s[70:71] offset:1024\n"
      "v_mfma_f32_16x16x32_bf16 v[196:199], v[100:103], a[224:227], v[196:199]\n"
      "ds_read_b128 a[220:223], v251 offset:6144\n"
      "s_waitcnt lgkmcnt(14)\n"
      "v_mfma_f32_16x16x32_bf16 v[200:203], v[96:99], a[228:231], v[200:203]\n"
      "v_mfma_f32_16x16x32_bf16 v[204:207], v[100:103], a[228:231], v[204:207]\n"
      "ds_read_b128 a[224:227], v250 offset:8192\n"
      "s_waitcnt lgkmcnt(14)\n"
      "v_mfma_f32_16x16x32_bf16 v[192:195], v[104:107], a[232:235], v[192:195]\n"
      "global_load_lds_dwordx4 v246, s[70:71] offset:2048\n"
      "v_mfma_f32_16x16x32_bf16 v[196:199], v[108:111], a[232:235], v[196:199]\n"
      "ds_read_b128 a[228:231], v250 offset:10240\n"
      "s_waitcnt vmcnt(11)\n"
      "ds_read_b128 a[8:11], v248 offset:8192\n"
      "s_waitcnt lgkmcnt(15)\n"
      "v_mfma_f32_16x16x32_bf16 v[200:203], v[104:107], a[236:239], v[200:203]\n"
      "ds_read_b128 a[12:15], v248 offset:10240\n"
      "ds_read_b128 a[40:43], v249 offset:8192\n"
      "v_mfma_f32_16x16x32_bf16 v[204:207], v[108:111], a[236:239], v[204:207]\n"
      "ds_read_b128 a[232:235], v251 offset:8192\n"
      "ds_read_b128 a[44:47], v249 offset:10240\n"
      "s_waitcnt lgkmcnt(15)\n"
      "v_mfma_f32_16x16x32_bf16 v[192:195], v[112:115], a[240:243], v[192:195]\n"
      "global_load_lds_dwordx4 v247, s[70:71] offset:3072\n"
      "v_mfma_f32_16x16x32_bf16 v[196:199], v[116:119], a[240:243], v[196:199]\n"
      "ds_read_b128 a[236:239], v251 offset:10240\n"
      "s_barrier\n"
      "v_mfma_f32_16x16x32_bf16 v[200:203], v[112:115], a[244:247], v[200:203]\n"
      "v_mfma_f32_16x16x32_bf16 v[204:207], v[116:119], a[244:247], v[204:207]\n"
      "s_xor_b32 s70, s77, 256\n"
      "s_add_u32 s70, s8, s70\n"
      "s_addc_u32 s71, s9, 0\n"
      "s_add_u32 m0, s82, 131072\n"
      "v_mfma_f32_16x16x32_bf16 v[192:195], v[120:123], a[248:251], v[192:195]\n"
      "global_load_lds_dwordx4 v244, s[70:71] offset:0\n"
      "ds_read_b128 a[240:243], v250 offset:12288\n"
      "v_mfma_f32_16x16x32_bf16 v[196:199], v[124:127], a[248:251], v[196:199]\n"
      "ds_read_b128 a[244:247], v250 offset:14336\n"
      "v_mfma_f32_16x16x32_bf16 v[200:203], v[120:123], a[252:255], v[200:203]\n"
      "v_mfma_f32_16x16x32_bf16 v[204:207], v[124:127], a[252:255], v[204:207]\n"
      "ds_read_b128 a[248:251], v251 offset:12288\n"
      "s_waitcnt lgkmcnt(15)\n"
      "v_mfma_f32_16x16x32_bf16 v[208:211], v[0:3], a[192:195], 0\n"
      "global_load_lds_dwordx4 v245, s[70:71] offset:1024\n"
      "v_mfma_f32_16x16x32_bf16 v[212:215], v[4:7], a[192:195], 0\n"
      "ds_read_b128 a[252:255], v251 offset:14336\n"
      "v_mfma_f32_16x16x32_bf16 v[216:219], v[0:3], a[196:199], 0\n"
      "v_cvt_pk_bf16_f32 v228, v192, v193\n"
      "v_cvt_pk_bf16_f32 v229, v194, v195\n"
      "v_cvt_pk_bf16_f32 v230, v196, v197\n"
      "v_mfma_f32_16x16x32_bf16 v[220:223], v[4:7], a[196:199], 0\n"
      "ds_read_b128 a[192:195], v250 offset:16384\n"
      "v_cvt_pk_bf16_f32 v231, v198, v199\n"
      "v_xor_b32 v228, s85, v228\n"
      "v_mfma_f32_16x16x32_bf16 v[208:211], v[8:11], a[200:203], v[208:211]\n"
      "global_load_lds_dwordx4 v246, s[70:71] offset:2048\n"
      "v_xor_b32 v229, s85, v229\n"
      "v_xor_b32 v230, s85, v230\n"
      "v_mfma_f32_16x16x32_bf16 v[212:215], v[12:15], a[200:203], v[212:215]\n"
      "ds_read_b128 a[196:199], v250 offset:18432\n"
      "s_waitcnt vmcnt(11)\n"
      "ds_read_b128 a[16:19], v248 offset:12288\n"
      "v_mfma_f32_16x16x32_bf16 v[216:219], v[8:11], a[204:207], v[216:219]\n"
      "ds_read_b128 a[20:23], v248 offset:14336\n"
      "ds_read_b128 a[48:51], v249 offset:12288\n"
      "v_xor_b32 v231, s85, v231\n"
      "v_mfma_f32_16x16x32_bf16 v[220:223], v[12:15], a[204:207], v[220:223]\n"
      "ds_read_b128 a[200:203], v251 offset:16384\n"
      "ds_read_b128 a[52:55], v249 offset:14336\n"
      "v_cvt_pk_bf16_f32 v232, v200, v201\n"
      "s_waitcnt lgkmcnt(15)\n"
      "v_mfma_f32_16x16x32_bf16 v[208:211], v[16:19], a[208:211], v[208:211]\n"
      "global_load_lds_dwordx4 v247, s[70:71] offset:3072\n"
      "v_cvt_pk_bf16_f32 v233, v202, v203\n"
      "v_cvt_pk_bf16_f32 v234, v204, v205\n"
      "v_mfma_f32_16x16x32_bf16 v[212:215], v[20:23], a[208:211], v[212:215]\n"
      "ds_read_b128 a[204:207], v251 offset:18432\n"
      "v_cvt_pk_bf16_f32 v235, v206, v207\n"
      "v_xor_b32 v232, s85, v232\n"
      "v_mfma_f32_16x16x32_bf16 v[216:219], v[16:19], a[212:215], v[216:219]\n"
      "v_xor_b32 v233, s85, v233\n"
      "v_xor_b32 v234, s85, v234\n"
      "v_xor_b32 v235, s85, v235\n"
      "v_mfma_f32_16x16x32_bf16 v[220:223], v[20:23], a[212:215], v[220:223]\n"
      "s_xor_b32 s70, s78, 256\n"
      "s_add_u32 s70, s8, s70\n"
      "s_addc_u32 s71, s9, 0\n"
      "s_add_u32 m0, s82, 147456\n"
      "v_mfma_f32_16x16x32_bf16 v[208:211], v[24:27], a[216:219], v[208:211]\n"
      "global_load_lds_dwordx4 v244, s[70:71] offset:0\n"
      "ds_read_b128 a[208:211], v250 offset:20480\n"
      "v_mfma_f32_16x16x32_bf16 v[212:215], v[28:31], a[216:219], v[212:215]\n"
      "ds_read_b128 a[212:215], v250 offset:22528\n"
      "v_mfma_f32_16x16x32_bf16 v[216:219], v[24:27], a[220:223], v[216:219]\n"
      "v_mfma_f32_16x16x32_bf16 v[220:223], v[28:31], a[220:223], v[220:223]\n"
      "ds_read_b128 a[216:219], v251 offset:20480\n"
      "v_mfma_f32_16x16x32_bf16 v[208:211], v[32:35], a[224:227], v[208:211]\n"
      "global_load_lds_dwordx4 v245, s[70:71] offset:1024\n"
      "v_mfma_f32_16x16x32_bf16 v[212:215], v[36:39], a[224:227], v[212:215]\n"
      "ds_read_b128 a[220:223], v251 offset:22528\n"
      "v_mfma_f32_16x16x32_bf16 v[216:219], v[32:35], a[228:231], v[216:219]\n"
      "v_mfma_f32_16x16x32_bf16 v[220:223], v[36:39], a[228:231], v[220:223]\n"
      "ds_read_b128 a[224:227], v250 offset:24576\n"
      "s_waitcnt lgkmcnt(15)\n"
      "v_mfma_f32_16x16x32_bf16 v[208:211], v[40:43], a[232:235], v[208:211]\n"
      "global_load_lds_dwordx4 v246, s[70:71] offset:2048\n"
      "v_mfma_f32_16x16x32_bf16 v[212:215], v[44:47], a[232:235], v[212:215]\n"
      "ds_read_b128 a[228:231], v250 offset:26624\n"
      "s_waitcnt vmcnt(11)\n"
      "ds_read_b128 a[24:27], v236 offset:0\n"
      "v_mfma_f32_16x16x32_bf16 v[216:219], v[40:43], a[236:239], v[216:219]\n"
      "ds_read_b128 a[28:31], v236 offset:2048\n"
      "ds_read_b128 a[56:59], v237 offset:0\n"
      "v_mfma_f32_16x16x32_bf16 v[220:223], v[44:47], a[236:239], v[220:223]\n"
      "ds_read_b128 a[232:235], v251 offset:24576\n"
      "ds_read_b128 a[60:63], v237 offset:2048\n"
      "v_mfma_f32_16x16x32_bf16 v[208:211], v[48:51], a[240:243], v[208:211]\n"
      "global_load_lds_dwordx4 v247, s[70:71] offset:3072\n"
      "v_mfma_f32_16x16x32_bf16 v[212:215], v[52:55], a[240:243], v[212:215]\n"
      "ds_read_b128 a[236:239], v251 offset:26624\n"
      "v_mfma_f32_16x16x32_bf16 v[216:219], v[48:51], a[244:247], v[216:219]\n"
      "v_mfma_f32_16x16x32_bf16 v[220:223], v[52:55], a[244:247], v[220:223]\n"
      "global_load_dword v252, v253, s[58:59] offset:0 sc1\n"
      "s_xor_b32 s70, s79, 256\n"
      "s_add_u32 s70, s8, s70\n"
      "s_addc_u32 s71, s9, 0\n"
      "s_add_u32 m0, s41, 0\n"
      "s_waitcnt lgkmcnt(15)\n"
      "v_mfma_f32_16x16x32_bf16 v[208:211], v[56:59], a[248:251], v[208:211]\n"
      "global_load_lds_dwordx4 v244, s[70:71] offset:0\n"
      "ds_read_b128 a[240:243], v250 offset:28672\n"
      "v_mfma_f32_16x16x32_bf16 v[212:215], v[60:63], a[248:251], v[212:215]\n"
      "ds_read_b128 a[244:247], v250 offset:30720\n"
      "v_mfma_f32_16x16x32_bf16 v[216:219], v[56:59], a[252:255], v[216:219]\n"
      "v_mfma_f32_16x16x32_bf16 v[220:223], v[60:63], a[252:255], v[220:223]\n"
      "ds_read_b128 a[248:251], v251 offset:28672\n"
      "v_mfma_f32_16x16x32_bf16 v[208:211], v[64:67], a[192:195], v[208:211]\n"
      "global_load_lds_dwordx4 v245, s[70:71] offset:1024\n"
      "v_mfma_f32_16x16x32_bf16 v[212:215], v[68:71], a[192:195], v[212:215]\n"
      "ds_read_b128 a[252:255], v251 offset:30720\n"
      "v_mfma_f32_16x16x32_bf16 v[216:219], v[64:67], a[196:199], v[216:219]\n"
      "v_mfma_f32_16x16x32_bf16 v[220:223], v[68:71], a[196:199], v[220:223]\n"
      "s_waitcnt lgkmcnt(15)\n"
      "v_mfma_f32_16x16x32_bf16 v[208:211], v[72:75], a[200:203], v[208:211]\n"
      "global_load_lds_dwordx4 v246, s[70:71] offset:2048\n"
      "v_mfma_f32_16x16x32_bf16 v[212:215], v[76:79], a[200:203], v[212:215]\n"
      "s_waitcnt vmcnt(12)\n"
      "ds_read_b128 a[64:67], v236 offset:16384\n"
      "v_mfma_f32_16x16x32_bf16 v[216:219], v[72:75], a[204:207], v[216:219]\n"
      "ds_read_b128 a[68:71], v236 offset:18432\n"
      "ds_read_b128 a[96:99], v237 offset:16384\n"
      "v_mfma_f32_16x16x32_bf16 v[220:223], v[76:79], a[204:207], v[220:223]\n"
      "ds_read_b128 a[100:103], v237 offset:18432\n"
      "v_mfma_f32_16x16x32_bf16 v[208:211], v[80:83], a[208:211], v[208:211]\n"
      "global_load_lds_dwordx4 v247, s[70:71] offset:3072\n"
      "v_mfma_f32_16x16x32_bf16 v[212:215], v[84:87], a[208:211], v[212:215]\n"
      "s_waitcnt lgkmcnt(15)\n"
      "v_mfma_f32_16x16x32_bf16 v[216:219], v[80:83], a[212:215], v[216:219]\n"
      "v_mfma_f32_16x16x32_bf16 v[220:223], v[84:87], a[212:215], v[220:223]\n"
      "s_waitcnt vmcnt(4)\n"
      "v_cmp_ne_u32 vcc, s37, v252\n"
      "s_nop 1\n"
      "s_cbranch_vccnz .Lp_s2_%=\n"
      ".Lp_o3_%=:\n"
      "v_mfma_f32_16x16x32_bf16 v[208:211], v[88:91], a[216:219], v[208:211]\n"
      "global_load_dwordx4 v[128:131], v240, s[10:11] offset:-4096 sc1\n"
      "v_mfma_f32_16x16x32_bf16 v[212:215], v[92:95], a[216:219], v[212:215]\n"
      "v_mfma_f32_16x16x32_bf16 v[216:219], v[88:91], a[220:223], v[216:219]\n"
      "v_mfma_f32_16x16x32_bf16 v[220:223], v[92:95], a[220:223], v[220:223]\n"
      "v_mfma_f32_16x16x32_bf16 v[208:211], v[96:99], a[224:227], v[208:211]\n"
      "global_load_dwordx4 v[132:135], v240, s[10:11] offset:-3072 sc1\n"
      "v_mfma_f32_16x16x32_bf16 v[212:215], v[100:103], a[224:227], v[212:215]\n"
      "s_waitcnt lgkmcnt(14)\n"
      "v_mfma_f32_16x16x32_bf16 v[216:219], v[96:99], a[228:231], v[216:219]\n"
      "v_mfma_f32_16x16x32_bf16 v[220:223], v[100:103], a[228:231], v[220:223]\n"
      "s_waitcnt lgkmcnt(10)\n"
      "v_mfma_f32_16x16x32_bf16 v[208:211], v[104:107], a[232:235], v[208:211]\n"
      "global_load_dwordx4 v[136:139], v240, s[10:11] offset:-2048 sc1\n"
      "v_mfma_f32_16x16x32_bf16 v[212:215], v[108:111], a[232:235], v[212:215]\n"
      "ds_read_b128 a[72:75], v236 offset:32768\n"
      "ds_read_b128 a[76:79], v236 offset:34816\n"
      "s_waitcnt lgkmcnt(10)\n"
      "v_mfma_f32_16x16x32_bf16 v[216:219], v[104:107], a[236:239], v[216:219]\n"
      "ds_read_b128 a[104:107], v237 offset:32768\n"
      "ds_read_b128 a[108:111], v237 offset:34816\n"
      "v_mfma_f32_16x16x32_bf16 v[220:223], v[108:111], a[236:239], v[220:223]\n"
      "s_waitcnt lgkmcnt(11)\n"
      "v_mfma_f32_16x16x32_bf16 v[208:211], v[112:115], a[240:243], v[208:211]\n"
      "global_store_dwordx4 v242, v[224:227], s[62:63] offset:-1024 sc1\n"
      "global_store_dwordx4 v242, v[228:231], s[62:63] offset:0 sc1\n"
      "v_mfma_f32_16x16x32_bf16 v[212:215], v[116:119], a[240:243], v[212:215]\n"
      "global_store_dwordx4 v242, v[232:235], s[62:63] offset:1024 sc1\n"
      "global_load_dwordx4 v[140:143], v240, s[72:73] offset:-4096 sc1\n"
      "s_waitcnt lgkmcnt(10)\n"
      "v_mfma_f32_16x16x32_bf16 v[216:219], v[112:115], a[244:247], v[216:219]\n"
      "v_mfma_f32_16x16x32_bf16 v[220:223], v[116:119], a[244:247], v[220:223]\n"
      "s_waitcnt lgkmcnt(9)\n"
      "v_mfma_f32_16x16x32_bf16 v[208:211], v[120:123], a[248:251], v[208:211]\n"
      "v_mfma_f32_16x16x32_bf16 v[212:215], v[124:127], a[248:251], v[212:215]\n"
      "global_load_dwordx4 v[144:147], v240, s[72:73] offset:-3072 sc1\n"
      "s_waitcnt lgkmcnt(8)\n"
      "v_mfma_f32_16x16x32_bf16 v[216:219], v[120:123], a[252:255], v[216:219]\n"
      "v_mfma_f32_16x16x32_bf16 v[220:223], v[124:127], a[252:255], v[220:223]\n"
      "s_waitcnt vmcnt(7)\n"
      "v_mfma_f32_16x16x32_bf16 v[0:3], v[128:131], a[0:3], 0\n"
      "v_mfma_f32_16x16x32_bf16 v[4:7], v[128:131], a[4:7], 0\n"
      "global_load_dwordx4 v[148:151], v240, s[72:73] offset:-2048 sc1\n"
      "v_mfma_f32_16x16x32_bf16 v[8:11], v[128:131], a[8:11], 0\n"
      "v_cvt_pk_bf16_f32 v192, v208, v209\n"
      "v_cvt_pk_bf16_f32 v193, v210, v211\n"
      "v_mfma_f32_16x16x32_bf16 v[12:15], v[128:131], a[12:15], 0\n"
      "v_cvt_pk_bf16_f32 v194, v212, v213\n"
      "v_cvt_pk_bf16_f32 v195, v214, v215\n"
      "v_mfma_f32_16x16x32_bf16 v[16:19], v[128:131], a[16:19], 0\n"
      "v_xor_b32 v192, s85, v192\n"
      "v_xor_b32 v193, s85, v193\n"
      "v_mfma_f32_16x16x32_bf16 v[20:23], v[128:131], a[20:23], 0\n"
      "ds_read_b128 a[80:83], v236 offset:49152\n"
      "v_xor_b32 v194, s85, v194\n"
      "v_mfma_f32_16x16x32_bf16 v[24:27], v[128:131], a[24:27], 0\n"
      "ds_read_b128 a[84:87], v236 offset:51200\n"
      "v_xor_b32 v195, s85, v195\n"
      "v_mfma_f32_16x16x32_bf16 v[28:31], v[128:131], a[28:31], 0\n"
      "ds_read_b128 a[112:115], v237 offset:49152\n"
      "v_cvt_pk_bf16_f32 v196, v216, v217\n"
      "s_waitcnt vmcnt(7)\n"
      "v_mfma_f32_16x16x32_bf16 v[32:35], v[132:135], a[0:3], 0\n"
      "s_xor_b32 s70, s76, 128\n"
      "s_add_u32 s70, s8, s70\n"
      "s_addc_u32 s71, s9, 0\n"
      "s_add_u32 m0, s41, 4096\n"
      "v_mfma_f32_16x16x32_bf16 v[36:39], v[132:135], a[4:7], 0\n"
      "global_load_lds_dwordx4 v244, s[70:71] offset:0\n"
      "v_cvt_pk_bf16_f32 v197, v218, v219\n"
      "v_mfma_f32_16x16x32_bf16 v[40:43], v[132:135], a[8:11], 0\n"
      "ds_read_b128 a[116:119], v237 offset:51200\n"
      "v_cvt_pk_bf16_f32 v198, v220, v221\n"
      "v_mfma_f32_16x16x32_bf16 v[44:47], v[132:135], a[12:15], 0\n"
      "v_cvt_pk_bf16_f32 v199, v222, v223\n"
      "v_xor_b32 v196, s85, v196\n"
      "v_mfma_f32_16x16x32_bf16 v[48:51], v[132:135], a[16:19], 0\n"
      "global_load_lds_dwordx4 v245, s[70:71] offset:1024\n"
      "v_xor_b32 v197, s85, v197\n"
      "v_mfma_f32_16x16x32_bf16 v[52:55], v[132:135], a[20:23], 0\n"
      "v_xor_b32 v198, s85, v198\n"
      "v_xor_b32 v199, s85, v199\n"
      "v_mfma_f32_16x16x32_bf16 v[56:59], v[132:135], a[24:27], 0\n"
      "global_store_dwordx4 v242, v[192:195], s[62:63] offset:2048 sc1\n"
      "global_store_dwordx4 v242, v[196:199], s[62:63] offset:3072 sc1\n"
      "v_mfma_f32_16x16x32_bf16 v[60:63], v[132:135], a[28:31], 0\n"
      "s_waitcnt vmcnt(6)\n"
      "global_load_lds_dwordx4 v246, s[70:71] offset:2048\n"
      "v_mfma_f32_16x16x32_bf16 v[64:67], v[136:139], a[0:3], 0\n"
      "v_mfma_f32_16x16x32_bf16 v[68:71], v[136:139], a[4:7], 0\n"
      "v_mfma_f32_16x16x32_bf16 v[72:75], v[136:139], a[8:11], 0\n"
      "global_load_lds_dwordx4 v247, s[70:71] offset:3072\n"
      "v_mfma_f32_16x16x32_bf16 v[76:79], v[136:139], a[12:15], 0\n"
      "v_mfma_f32_16x16x32_bf16 v[80:83], v[136:139], a[16:19], 0\n"
      "global_load_dwordx4 v[152:155], v240, s[14:15] offset:-4096 sc1\n"
      "v_mfma_f32_16x16x32_bf16 v[84:87], v[136:139], a[20:23], 0\n"
      "ds_read_b128 a[88:91], v248 offset:0\n"
      "ds_read_b128 a[92:95], v248 offset:2048\n"
      "v_mfma_f32_16x16x32_bf16 v[88:91], v[136:139], a[24:27], 0\n"
      "ds_read_b128 a[120:123], v249 offset:0\n"
      "ds_read_b128 a[124:127], v249 offset:2048\n"
      "v_mfma_f32_16x16x32_bf16 v[92:95], v[136:139], a[28:31], 0\n"
      "global_load_dwordx4 v[156:159], v240, s[14:15] offset:-3072 sc1\n"
      "v_mfma_f32_16x16x32_bf16 v[0:3], v[140:143], a[32:35], v[0:3]\n"
      "v_mfma_f32_16x16x32_bf16 v[4:7], v[140:143], a[36:39], v[4:7]\n"
      "v_mfma_f32_16x16x32_bf16 v[8:11], v[140:143], a[40:43], v[8:11]\n"
      "global_load_dwordx4 v[160:163], v240, s[14:15] offset:-2048 sc1\n"
      "v_mfma_f32_16x16x32_bf16 v[12:15], v[140:143], a[44:47], v[12:15]\n"
      "v_mfma_f32_16x16x32_bf16 v[16:19], v[140:143], a[48:51], v[16:19]\n"
      "v_mfma_f32_16x16x32_bf16 v[20:23], v[140:143], a[52:55], v[20:23]\n"
      "s_xor_b32 s70, s77, 128\n"
      "s_add_u32 s70, s8, s70\n"
      "s_addc_u32 s71, s9, 0\n"
      "s_add_u32 m0, s41, 8192\n"
      "v_mfma_f32_16x16x32_bf16 v[24:27], v[140:143], a[56:59], v[24:27]\n"
      "global_load_lds_dwordx4 v244, s[70:71] offset:0\n"
      "v_mfma_f32_16x16x32_bf16 v[28:31], v[140:143], a[60:63], v[28:31]\n"
      "s_waitcnt vmcnt(10)\n"
      "v_mfma_f32_16x16x32_bf16 v[32:35], v[144:147], a[32:35], v[32:35]\n"
      "v_mfma_f32_16x16x32_bf16 v[36:39], v[144:147], a[36:39], v[36:39]\n"
      "global_load_lds_dwordx4 v245, s[70:71] offset:1024\n"
      "v_mfma_f32_16x16x32_bf16 v[40:43], v[144:147], a[40:43], v[40:43]\n"
      "v_mfma_f32_16x16x32_bf16 v[44:47], v[144:147], a[44:47], v[44:47]\n"
      "v_mfma_f32_16x16x32_bf16 v[48:51], v[144:147], a[48:51], v[48:51]\n"
      "global_load_lds_dwordx4 v246, s[70:71] offset:2048\n"
      "v_mfma_f32_16x16x32_bf16 v[52:55], v[144:147], a[52:55], v[52:55]\n"
      "v_mfma_f32_16x16x32_bf16 v[56:59], v[144:147], a[56:59], v[56:59]\n"
      "v_mfma_f32_16x16x32_bf16 v[60:63], v[144:147], a[60:63], v[60:63]\n"
      "global_load_lds_dwordx4 v247, s[70:71] offset:3072\n"
      "v_mfma_f32_16x16x32_bf16 v[64:67], v[148:151], a[32:35], v[64:67]\n"
      "v_mfma_f32_16x16x32_bf16 v[68:71], v[148:151], a[36:39], v[68:71]\n"
      "s_xor_b32 s70, s78, 128\n"
      "s_add_u32 s70, s8, s70\n"
      "s_addc_u32 s71, s9, 0\n"
      "s_add_u32 m0, s41, 12288\n"
      "v_mfma_f32_16x16x32_bf16 v[72:75], v[148:151], a[40:43], v[72:75]\n"
      "global_load_lds_dwordx4 v244, s[70:71] offset:0\n"
      "v_add_u32 v238, s42, v242\n"
      "v_mov_b32 v200, s37\n"
      "v_mov_b32 v201, s37\n"
      "v_mov_b32 v202, s37\n"
      "v_mov_b32 v203, s37\n"
      "s_waitcnt vmcnt(10)\n"
      "s_mov_b64 exec, 0xff\n"
      "global_store_dwordx4 v238, v[200:203], s[58:59] offset:128 sc1\n"
      "s_mov_b64 exec, -1\n"
      "v_mfma_f32_16x16x32_bf16 v[76:79], v[148:151], a[44:47], v[76:79]\n"
      "s_lshl2_add_u32 s48, s36, s35\n"
      "s_mul_i32 s48, s48, 147712\n"
      "s_mul_i32 s49, s33, 3072\n"
      "v_mfma_f32_16x16x32_bf16 v[80:83], v[148:151], a[48:51], v[80:83]\n"
      "s_add_u32 s48, s48, s49\n"
      "s_add_u32 s48, s48, 6291456\n"
      "s_add_u32 s60, s18, s48\n"
      "s_addc_u32 s61, s19, 0\n"
      "v_mfma_f32_16x16x32_bf16 v[84:87], v[148:151], a[52:55], v[84:87]\n"
      "s_mul_i32 s49, s34, 36864\n"
      "global_load_lds_dwordx4 v245, s[70:71] offset:1024\n"
      "s_add_u32 s62, s60, s49\n"
      "s_addc_u32 s63, s61, 0\n"
      "v_mfma_f32_16x16x32_bf16 v[88:91], v[148:151], a[56:59], v[88:91]\n"
      "v_mfma_f32_16x16x32_bf16 v[92:95], v[148:151], a[60:63], v[92:95]\n"
      "s_waitcnt vmcnt(8)\n"
      "s_waitcnt lgkmcnt(15)\n"
      "v_mfma_f32_16x16x32_bf16 v[0:3], v[152:155], a[64:67], v[0:3]\n"
      "global_load_lds_dwordx4 v246, s[70:71] offset:2048\n"
      "s_waitcnt lgkmcnt(14)\n"
      "v_mfma_f32_16x16x32_bf16 v[4:7], v[152:155], a[68:71], v[4:7]\n"
      "s_waitcnt lgkmcnt(11)\n"
      "v_mfma_f32_16x16x32_bf16 v[8:11], v[152:155], a[72:75], v[8:11]\n"
      "s_waitcnt lgkmcnt(10)\n"
      "v_mfma_f32_16x16x32_bf16 v[12:15], v[152:155], a[76:79], v[12:15]\n"
      "global_load_lds_dwordx4 v247, s[70:71] offset:3072\n"
      "s_waitcnt lgkmcnt(7)\n"
      "v_mfma_f32_16x16x32_bf16 v[16:19], v[152:155], a[80:83], v[16:19]\n"
      "s_waitcnt lgkmcnt(6)\n"
      "v_mfma_f32_16x16x32_bf16 v[20:23], v[152:155], a[84:87], v[20:23]\n"
      "global_load_dwordx4 v[164:167], v240, s[74:75] offset:-4096 sc1\n"
      "s_waitcnt lgkmcnt(3)\n"
      "v_mfma_f32_16x16x32_bf16 v[24:27], v[152:155], a[88:91], v[24:27]\n"
      "s_waitcnt lgkmcnt(2)\n"
      "v_mfma_f32_16x16x32_bf16 v[28:31], v[152:155], a[92:95], v[28:31]\n"
      "v_mfma_f32_16x16x32_bf16 v[32:35], v[156:159], a[64:67], v[32:35]\n"
      "global_load_dwordx4 v[168:171], v240, s[74:75] offset:-3072 sc1\n"
      "v_mfma_f32_16x16x32_bf16 v[36:39], v[156:159], a[68:71], v[36:39]\n"
      "v_mfma_f32_16x16x32_bf16 v[40:43], v[156:159], a[72:75], v[40:43]\n"
      "v_mfma_f32_16x16x32_bf16 v[44:47], v[156:159], a[76:79], v[44:47]\n"
      "global_load_dwordx4 v[172:175], v240, s[74:75] offset:-2048 sc1\n"
      "v_mfma_f32_16x16x32_bf16 v[48:51], v[156:159], a[80:83], v[48:51]\n"
      "v_mfma_f32_16x16x32_bf16 v[52:55], v[156:159], a[84:87], v[52:55]\n"
      "v_mfma_f32_16x16x32_bf16 v[56:59], v[156:159], a[88:91], v[56:59]\n"
      "s_xor_b32 s70, s79, 128\n"
      "s_add_u32 s70, s8, s70\n"
      "s_addc_u32 s71, s9, 0\n"
      "s_add_u32 m0, s82, 98304\n"
      "v_mfma_f32_16x16x32_bf16 v[60:63], v[156:159], a[92:95], v[60:63]\n"
      "s_waitcnt vmcnt(2)\n"
      "global_load_lds_dwordx4 v244, s[70:71] offset:0\n"
      "ds_read_b128 a[128:131], v248 offset:4096\n"
      "v_mfma_f32_16x16x32_bf16 v[64:67], v[160:163], a[64:67], v[64:67]\n"
      "ds_read_b128 a[132:135], v248 offset:6144\n"
      "ds_read_b128 a[160:163], v249 offset:4096\n"
      "v_mfma_f32_16x16x32_bf16 v[68:71], v[160:163], a[68:71], v[68:71]\n"
      "ds_read_b128 a[164:167], v249 offset:6144\n"
      "v_mfma_f32_16x16x32_bf16 v[72:75], v[160:163], a[72:75], v[72:75]\n"
      "global_load_lds_dwordx4 v245, s[70:71] offset:1024\n"
      "v_mfma_f32_16x16x32_bf16 v[76:79], v[160:163], a[76:79], v[76:79]\n"
      "v_mfma_f32_16x16x32_bf16 v[80:83], v[160:163], a[80:83], v[80:83]\n"
      "v_mfma_f32_16x16x32_bf16 v[84:87], v[160:163], a[84:87], v[84:87]\n"
      "global_load_lds_dwordx4 v246, s[70:71] offset:2048\n"
      "v_mfma_f32_16x16x32_bf16 v[88:91], v[160:163], a[88:91], v[88:91]\n"
      "s_lshr_b32 s54, s34, 1\n"
      "s_lshl2_add_u32 s54, s54, s35\n"
      "s_lshl_b32 s54, s54, 12\n"
      "v_mfma_f32_16x16x32_bf16 v[92:95], v[160:163], a[92:95], v[92:95]\n"
      "s_add_u32 s54, s54, 4096\n"
      "v_lshl_add_u32 v200, v243, 6, s54\n"
      "global_load_dwordx2 v[202:203], v200, s[58:59] sc1\n"
      "v_mfma_f32_16x16x32_bf16 v[0:3], v[164:167], a[96:99], v[0:3]\n"
      "global_load_lds_dwordx4 v247, s[70:71] offset:3072\n"
      "v_mfma_f32_16x16x32_bf16 v[4:7], v[164:167], a[100:103], v[4:7]\n"
      "v_mfma_f32_16x16x32_bf16 v[8:11], v[164:167], a[104:107], v[8:11]\n"
      "global_load_dwordx4 v[176:179], v241, s[10:11] offset:-4096 sc1\n"
      "v_mfma_f32_16x16x32_bf16 v[12:15], v[164:167], a[108:111], v[12:15]\n"
      "v_mfma_f32_16x16x32_bf16 v[16:19], v[164:167], a[112:115], v[16:19]\n"
      "v_mfma_f32_16x16x32_bf16 v[20:23], v[164:167], a[116:119], v[20:23]\n"
      "global_load_dwordx4 v[180:183], v241, s[10:11] offset:-3072 sc1\n"
      "s_waitcnt lgkmcnt(5)\n"
      "v_mfma_f32_16x16x32_bf16 v[24:27], v[164:167], a[120:123], v[24:27]\n"
      "s_waitcnt lgkmcnt(4)\n"
      "v_mfma_f32_16x16x32_bf16 v[28:31], v[164:167], a[124:127], v[28:31]\n"
      "s_waitcnt vmcnt(7)\n"
      "v_mfma_f32_16x16x32_bf16 v[32:35], v[168:171], a[96:99], v[32:35]\n"
      "global_load_dwordx4 v[184:187], v241, s[10:11] offset:-2048 sc1\n"
      "global_load_dwordx4 v[188:191], v241, s[72:73] offset:-4096 sc1\n"
      "v_mfma_f32_16x16x32_bf16 v[36:39], v[168:171], a[100:103], v[36:39]\n"
      "global_load_dwordx4 v[128:131], v241, s[72:73] offset:-3072 sc1\n"
      "global_load_dwordx4 v[132:135], v241, s[72:73] offset:-2048 sc1\n"
      "v_mfma_f32_16x16x32_bf16 v[40:43], v[168:171], a[104:107], v[40:43]\n"
      "global_load_dword v252, v253, s[58:59] offset:128 sc1\n"
      "v_mfma_f32_16x16x32_bf16 v[44:47], v[168:171], a[108:111], v[44:47]\n"
      "v_mfma_f32_16x16x32_bf16 v[48:51], v[168:171], a[112:115], v[48:51]\n"
      "ds_read_b128 a[136:139], v248 offset:8192\n"
      "ds_read_b128 a[140:143], v248 offset:10240\n"
      "v_mfma_f32_16x16x32_bf16 v[52:55], v[168:171], a[116:119], v[52:55]\n"
      "ds_read_b128 a[168:171], v249 offset:8192\n"
      "ds_read_b128 a[172:175], v249 offset:10240\n"
      "v_mfma_f32_16x16x32_bf16 v[56:59], v[168:171], a[120:123], v[56:59]\n"
      "v_mfma_f32_16x16x32_bf16 v[60:63], v[168:171], a[124:127], v[60:63]\n"
      "v_mfma_f32_16x16x32_bf16 v[64:67], v[172:175], a[96:99], v[64:67]\n"
      "v_mfma_f32_16x16x32_bf16 v[68:71], v[172:175], a[100:103], v[68:71]\n"
      "v_mfma_f32_16x16x32_bf16 v[72:75], v[172:175], a[104:107], v[72:75]\n"
      "v_mfma_f32_16x16x32_bf16 v[76:79], v[172:175], a[108:111], v[76:79]\n"
      "v_mfma_f32_16x16x32_bf16 v[80:83], v[172:175], a[112:115], v[80:83]\n"
      "v_mfma_f32_16x16x32_bf16 v[84:87], v[172:175], a[116:119], v[84:87]\n"
      "v_mfma_f32_16x16x32_bf16 v[88:91], v[172:175], a[120:123], v[88:91]\n"
      "v_mfma_f32_16x16x32_bf16 v[92:95], v[172:175], a[124:127], v[92:95]\n"
      "s_waitcnt vmcnt(5)\n"
      "s_waitcnt lgkmcnt(7)\n"
      "v_mfma_f32_16x16x32_bf16 v[0:3], v[176:179], a[128:131], v[0:3]\n"
      "ds_read_b128 a[144:147], v248 offset:12288\n"
      "ds_read_b128 a[148:151], v248 offset:14336\n"
      "s_waitcnt lgkmcnt(8)\n"
      "v_mfma_f32_16x16x32_bf16 v[4:7], v[176:179], a[132:135], v[4:7]\n"
      "ds_read_b128 a[176:179], v249 offset:12288\n"
      "ds_read_b128 a[180:183], v249 offset:14336\n"
      "s_waitcnt lgkmcnt(7)\n"
      "v_mfma_f32_16x16x32_bf16 v[8:11], v[176:179], a[136:139], v[8:11]\n"
      "s_xor_b32 s70, s76, 384\n"
      "s_add_u32 s70, s8, s70\n"
      "s_addc_u32 s71, s9, 0\n"
      "s_add_u32 m0, s82, 114688\n"
      "s_waitcnt lgkmcnt(6)\n"
      "v_mfma_f32_16x16x32_bf16 v[12:15], v[176:179], a[140:143], v[12:15]\n"
      "global_load_lds_dwordx4 v244, s[70:71] offset:0\n"
      "s_waitcnt lgkmcnt(3)\n"
      "v_mfma_f32_16x16x32_bf16 v[16:19], v[176:179], a[144:147], v[16:19]\n"
      "s_waitcnt lgkmcnt(2)\n"
      "v_mfma_f32_16x16x32_bf16 v[20:23], v[176:179], a[148:151], v[20:23]\n"
      "ds_read_b128 a[152:155], v236 offset:0\n"
      "ds_read_b128 a[156:159], v236 offset:2048\n"
      "ds_read_b128 a[184:187], v237 offset:0\n"
      "ds_read_b128 a[188:191], v237 offset:2048\n"
      "s_waitcnt lgkmcnt(3)\n"
      "v_mfma_f32_16x16x32_bf16 v[24:27], v[176:179], a[152:155], v[24:27]\n"
      "global_load_lds_dwordx4 v245, s[70:71] offset:1024\n"
      "s_waitcnt lgkmcnt(2)\n"
      "v_mfma_f32_16x16x32_bf16 v[28:31], v[176:179], a[156:159], v[28:31]\n"
      "v_mfma_f32_16x16x32_bf16 v[32:35], v[180:183], a[128:131], v[32:35]\n"
      "v_mfma_f32_16x16x32_bf16 v[36:39], v[180:183], a[132:135], v[36:39]\n"
      "global_load_lds_dwordx4 v246, s[70:71] offset:2048\n"
      "v_mfma_f32_16x16x32_bf16 v[40:43], v[180:183], a[136:139], v[40:43]\n"
      "v_mfma_f32_16x16x32_bf16 v[44:47], v[180:183], a[140:143], v[44:47]\n"
      "v_mfma_f32_16x16x32_bf16 v[48:51], v[180:183], a[144:147], v[48:51]\n"
      "global_load_lds_dwordx4 v247, s[70:71] offset:3072\n"
      "v_mfma_f32_16x16x32_bf16 v[52:55], v[180:183], a[148:151], v[52:55]\n"
      "v_mfma_f32_16x16x32_bf16 v[56:59], v[180:183], a[152:155], v[56:59]\n"
      "s_xor_b32 s70, s77, 384\n"
      "s_add_u32 s70, s8, s70\n"
      "s_addc_u32 s71, s9, 0\n"
      "s_add_u32 m0, s82, 131072\n"
      "v_mfma_f32_16x16x32_bf16 v[60:63], v[180:183], a[156:159], v[60:63]\n"
      "s_waitcnt vmcnt(7)\n"
      "global_load_lds_dwordx4 v244, s[70:71] offset:0\n"
      "s_waitcnt vmcnt(5)\n"
      "v_cmp_ne_u32 vcc, s37, v252\n"
      "s_nop 1\n"
      "s_cbranch_vccnz .Lp_s5_%=\n"
      ".Lp_o6_%=:\n"
      "v_mfma_f32_16x16x32_bf16 v[64:67], v[184:187], a[128:131], v[64:67]\n"
      "global_load_dwordx4 v[136:139], v240, s[10:11] offset:-1024 sc1\n"
      "v_mfma_f32_16x16x32_bf16 v[68:71], v[184:187], a[132:135], v[68:71]\n"
      "v_mfma_f32_16x16x32_bf16 v[72:75], v[184:187], a[136:139], v[72:75]\n"
      "global_load_lds_dwordx4 v245, s[70:71] offset:1024\n"
      "v_mfma_f32_16x16x32_bf16 v[76:79], v[184:187], a[140:143], v[76:79]\n"
      "v_mfma_f32_16x16x32_bf16 v[80:83], v[184:187], a[144:147], v[80:83]\n"
      "global_load_dwordx4 v[140:143], v240, s[72:73] offset:-1024 sc1\n"
      "v_mfma_f32_16x16x32_bf16 v[84:87], v[184:187], a[148:151], v[84:87]\n"
      "global_load_lds_dwordx4 v246, s[70:71] offset:2048\n"
      "v_mfma_f32_16x16x32_bf16 v[88:91], v[184:187], a[152:155], v[88:91]\n"
      "v_mfma_f32_16x16x32_bf16 v[92:95], v[184:187], a[156:159], v[92:95]\n"
      "v_mfma_f32_16x16x32_bf16 v[0:3], v[188:191], a[160:163], v[0:3]\n"
      "global_load_lds_dwordx4 v247, s[70:71] offset:3072\n"
      "global_load_dwordx4 v[144:147], v240, s[14:15] offset:-1024 sc1\n"
      "v_mfma_f32_16x16x32_bf16 v[4:7], v[188:191], a[164:167], v[4:7]\n"
      "v_mfma_f32_16x16x32_bf16 v[8:11], v[188:191], a[168:171], v[8:11]\n"
      "s_xor_b32 s70, s78, 384\n"
      "s_add_u32 s70, s8, s70\n"
      "s_addc_u32 s71, s9, 0\n"
      "s_add_u32 m0, s82, 147456\n"
      "v_mfma_f32_16x16x32_bf16 v[12:15], v[188:191], a[172:175], v[12:15]\n"
      "global_load_lds_dwordx4 v244, s[70:71] offset:0\n"
      "v_mfma_f32_16x16x32_bf16 v[16:19], v[188:191], a[176:179], v[16:19]\n"
      "global_load_dwordx4 v[148:151], v240, s[74:75] offset:-1024 sc1\n"
      "v_mfma_f32_16x16x32_bf16 v[20:23], v[188:191], a[180:183], v[20:23]\n"
      "s_waitcnt lgkmcnt(1)\n"
      "v_mfma_f32_16x16x32_bf16 v[24:27], v[188:191], a[184:187], v[24:27]\n"
      "global_load_lds_dwordx4 v245, s[70:71] offset:1024\n"
      "s_waitcnt lgkmcnt(0)\n"
      "v_mfma_f32_16x16x32_bf16 v[28:31], v[188:191], a[188:191], v[28:31]\n"
      "v_mfma_f32_16x16x32_bf16 v[32:35], v[128:131], a[160:163], v[32:35]\n"
      "global_load_dwordx4 v[152:155], v241, s[10:11] offset:-1024 sc1\n"
      "v_mfma_f32_16x16x32_bf16 v[36:39], v[128:131], a[164:167], v[36:39]\n"
      "global_load_lds_dwordx4 v246, s[70:71] offset:2048\n"
      "v_mfma_f32_16x16x32_bf16 v[40:43], v[128:131], a[168:171], v[40:43]\n"
      "v_cmp_ne_u32 vcc, s37, v203\n"
      "s_nop 1\n"
      "s_cbranch_vccnz .Lp_s8_%=\n"
      ".Lp_o9_%=:\n"
      "v_mfma_f32_16x16x32_bf16 v[44:47], v[128:131], a[172:175], v[44:47]\n"
      "v_max_u32_dpp v202, v202, v202 quad_perm:[1,0,3,2] row_mask:0xf bank_mask:0xf\n"
      "s_nop 1\n"
      "v_max_u32_dpp v202, v202, v202 quad_perm:[2,3,0,1] row_mask:0xf bank_mask:0xf\n"
      "v_mfma_f32_16x16x32_bf16 v[48:51], v[128:131], a[176:179], v[48:51]\n"
      "global_load_lds_dwordx4 v247, s[70:71] offset:3072\n"
      "v_max_u32_dpp v202, v202, v202 row_half_mirror row_mask:0xf bank_mask:0xf\n"
      "v_mfma_f32_16x16x32_bf16 v[52:55], v[128:131], a[180:183], v[52:55]\n"
      "global_load_dwordx4 v[156:159], v241, s[72:73] offset:-1024 sc1\n"
      "v_max_u32_dpp v202, v202, v202 row_mirror row_mask:0xf bank_mask:0xf\n"
      "v_mfma_f32_16x16x32_bf16 v[56:59], v[128:131], a[184:187], v[56:59]\n"
      "s_xor_b32 s70, s79, 384\n"
      "s_add_u32 s70, s8, s70\n"
      "s_addc_u32 s71, s9, 0\n"
      "s_add_u32 m0, s41, 0\n"
      "v_mfma_f32_16x16x32_bf16 v[60:63], v[128:131], a[188:191], v[60:63]\n"
      "global_load_lds_dwordx4 v244, s[70:71] offset:0\n"
      "v_mov_b32 v201, v202\n"
      "v_mfma_f32_16x16x32_bf16 v[64:67], v[132:135], a[160:163], v[64:67]\n"
      "global_load_dwordx4 v[160:163], v241, s[14:15] offset:-4096 sc1\n"
      "v_permlane16_swap_b32 v202, v201\n"
      "v_mfma_f32_16x16x32_bf16 v[68:71], v[132:135], a[164:167], v[68:71]\n"
      "v_max_u32 v202, v202, v201\n"
      "v_mov_b32 v201, v202\n"
      "v_mfma_f32_16x16x32_bf16 v[72:75], v[132:135], a[168:171], v[72:75]\n"
      "global_load_lds_dwordx4 v245, s[70:71] offset:1024\n"
      "v_permlane32_swap_b32 v202, v201\n"
      "v_mfma_f32_16x16x32_bf16 v[76:79], v[132:135], a[172:175], v[76:79]\n"
      "v_max_u32 v202, v202, v201\n"
      "s_nop 0\n"
      "v_readfirstlane_b32 s40, v202\n"
      "v_mfma_f32_16x16x32_bf16 v[80:83], v[132:135], a[176:179], v[80:83]\n"
      "s_nop 0\n"
      "s_andn2_b32 s42, 0x7ff, s40\n"
      "global_load_dwordx4 v[164:167], v241, s[74:75] offset:-4096 sc1\n"
      "v_mfma_f32_16x16x32_bf16 v[84:87], v[132:135], a[180:183], v[84:87]\n"
      "global_load_lds_dwordx4 v246, s[70:71] offset:2048\n"
      "s_lshr_b32 s43, s42, 5\n"
      "s_lshl2_add_u32 s44, s36, s33\n"
      "s_cmp_eq_u32 s43, s44\n"
      "s_cbranch_scc0 .Lp_e10_%=\n"
      "s_lshl_b32 s43, s42, 10\n"
      "v_add_u32 v208, s43, v242\n"
      "global_load_dwordx4 v[216:219], v208, s[16:17]\n"
      "global_load_dwordx4 v[220:223], v242, s[24:25]\n"
      ".Lp_e10_%=:\n"
      "v_mfma_f32_16x16x32_bf16 v[88:91], v[132:135], a[184:187], v[88:91]\n"
      "v_mfma_f32_16x16x32_bf16 v[92:95], v[132:135], a[188:191], v[92:95]\n"
      "s_waitcnt vmcnt(15)\n"
      "v_mfma_f32_16x16x32_bf16 v[96:99], v[136:139], a[0:3], 0\n"
      "global_load_lds_dwordx4 v247, s[70:71] offset:3072\n"
      "global_load_dwordx4 v[168:171], v241, s[14:15] offset:-3072 sc1\n"
      "v_mfma_f32_16x16x32_bf16 v[100:103], v[136:139], a[4:7], 0\n"
      "v_mfma_f32_16x16x32_bf16 v[104:107], v[136:139], a[8:11], 0\n"
      "v_mfma_f32_16x16x32_bf16 v[108:111], v[136:139], a[12:15], 0\n"
      "v_mfma_f32_16x16x32_bf16 v[112:115], v[136:139], a[16:19], 0\n"
      "global_load_dwordx4 v[172:175], v241, s[74:75] offset:-3072 sc1\n"
      "v_mfma_f32_16x16x32_bf16 v[116:119], v[136:139], a[20:23], 0\n"
      "v_mfma_f32_16x16x32_bf16 v[120:123], v[136:139], a[24:27], 0\n"
      "v_mfma_f32_16x16x32_bf16 v[124:127], v[136:139], a[28:31], 0\n"
      "v_mfma_f32_16x16x32_bf16 v[96:99], v[140:143], a[32:35], v[96:99]\n"
      "global_load_dwordx4 v[176:179], v241, s[14:15] offset:-2048 sc1\n"
      "v_mfma_f32_16x16x32_bf16 v[100:103], v[140:143], a[36:39], v[100:103]\n"
      "ds_read_b128 a[192:195], v236 offset:16384\n"
      "ds_read_b128 a[196:199], v236 offset:18432\n"
      "v_mfma_f32_16x16x32_bf16 v[104:107], v[140:143], a[40:43], v[104:107]\n"
      "ds_read_b128 a[224:227], v237 offset:16384\n"
      "ds_read_b128 a[228:231], v237 offset:18432\n"
      "v_mfma_f32_16x16x32_bf16 v[108:111], v[140:143], a[44:47], v[108:111]\n"
      "v_mfma_f32_16x16x32_bf16 v[112:115], v[140:143], a[48:51], v[112:115]\n"
      "global_load_dwordx4 v[180:183], v241, s[74:75] offset:-2048 sc1\n"
      "v_mfma_f32_16x16x32_bf16 v[116:119], v[140:143], a[52:55], v[116:119]\n"
      "v_mfma_f32_16x16x32_bf16 v[120:123], v[140:143], a[56:59], v[120:123]\n"
      "v_mfma_f32_16x16x32_bf16 v[124:127], v[140:143], a[60:63], v[124:127]\n"
      "s_waitcnt vmcnt(15)\n"
      "v_mfma_f32_16x16x32_bf16 v[96:99], v[144:147], a[64:67], v[96:99]\n"
      "global_load_dwordx4 v[184:187], v241, s[14:15] offset:-1024 sc1\n"
      "v_mfma_f32_16x16x32_bf16 v[100:103], v[144:147], a[68:71], v[100:103]\n"
      "v_mfma_f32_16x16x32_bf16 v[104:107], v[144:147], a[72:75], v[104:107]\n"
      "v_mfma_f32_16x16x32_bf16 v[108:111], v[144:147], a[76:79], v[108:111]\n"
      "v_mfma_f32_16x16x32_bf16 v[112:115], v[144:147], a[80:83], v[112:115]\n"
      "global_load_dwordx4 v[188:191], v241, s[74:75] offset:-1024 sc1\n"
      "v_mfma_f32_16x16x32_bf16 v[116:119], v[144:147], a[84:87], v[116:119]\n"
      "ds_read_b128 a[200:203], v236 offset:32768\n"
      "ds_read_b128 a[204:207], v236 offset:34816\n"
      "v_mfma_f32_16x16x32_bf16 v[120:123], v[144:147], a[88:91], v[120:123]\n"
      "ds_read_b128 a[232:235], v237 offset:32768\n"
      "ds_read_b128 a[236:239], v237 offset:34816\n"
      "v_mfma_f32_16x16x32_bf16 v[124:127], v[144:147], a[92:95], v[124:127]\n"
      "v_mfma_f32_16x16x32_bf16 v[96:99], v[148:151], a[96:99], v[96:99]\n"
      "global_load_dwordx4 v[128:131], v240, s[10:11] offset:0 sc1\n"
      "v_mfma_f32_16x16x32_bf16 v[100:103], v[148:151], a[100:103], v[100:103]\n"
      "v_mfma_f32_16x16x32_bf16 v[104:107], v[148:151], a[104:107], v[104:107]\n"
      "v_mfma_f32_16x16x32_bf16 v[108:111], v[148:151], a[108:111], v[108:111]\n"
      "v_mfma_f32_16x16x32_bf16 v[112:115], v[148:151], a[112:115], v[112:115]\n"
      "global_load_dwordx4 v[132:135], v240, s[72:73] offset:0 sc1\n"
      "v_mfma_f32_16x16x32_bf16 v[116:119], v[148:151], a[116:119], v[116:119]\n"
      "v_mfma_f32_16x16x32_bf16 v[120:123], v[148:151], a[120:123], v[120:123]\n"
      "v_mfma_f32_16x16x32_bf16 v[124:127], v[148:151], a[124:127], v[124:127]\n"
      "s_waitcnt vmcnt(14)\n"
      "v_mfma_f32_16x16x32_bf16 v[96:99], v[152:155], a[128:131], v[96:99]\n"
      "global_load_dwordx4 v[136:139], v240, s[14:15] offset:0 sc1\n"
      "v_mfma_f32_16x16x32_bf16 v[100:103], v[152:155], a[132:135], v[100:103]\n"
      "ds_read_b128 a[208:211], v236 offset:49152\n"
      "ds_read_b128 a[212:215], v236 offset:51200\n"
      "v_mfma_f32_16x16x32_bf16 v[104:107], v[152:155], a[136:139], v[104:107]\n"
      "ds_read_b128 a[240:243], v237 offset:49152\n"
      "ds_read_b128 a[244:247], v237 offset:51200\n"
      "v_mfma_f32_16x16x32_bf16 v[108:111], v[152:155], a[140:143], v[108:111]\n"
      "v_mfma_f32_16x16x32_bf16 v[112:115], v[152:155], a[144:147], v[112:115]\n"
      "global_load_dwordx4 v[140:143], v240, s[74:75] offset:0 sc1\n"
      "v_mfma_f32_16x16x32_bf16 v[116:119], v[152:155], a[148:151], v[116:119]\n"
      "v_mfma_f32_16x16x32_bf16 v[120:123], v[152:155], a[152:155], v[120:123]\n"
      "v_mfma_f32_16x16x32_bf16 v[124:127], v[152:155], a[156:159], v[124:127]\n"
      "v_mfma_f32_16x16x32_bf16 v[96:99], v[156:159], a[160:163], v[96:99]\n"
      "global_load_dwordx4 v[144:147], v241, s[10:11] offset:0 sc1\n"
      "v_mfma_f32_16x16x32_bf16 v[100:103], v[156:159], a[164:167], v[100:103]\n"
      "v_mfma_f32_16x16x32_bf16 v[104:107], v[156:159], a[168:171], v[104:107]\n"
      "v_mfma_f32_16x16x32_bf16 v[108:111], v[156:159], a[172:175], v[108:111]\n"
      "v_mfma_f32_16x16x32_bf16 v[112:115], v[156:159], a[176:179], v[112:115]\n"
      "global_load_dwordx4 v[148:151], v241, s[72:73] offset:0 sc1\n"
      "v_mfma_f32_16x16x32_bf16 v[116:119], v[156:159], a[180:183], v[116:119]\n"
      "s_waitcnt vmcnt(12)\n"
      "ds_read_b128 a[216:219], v248 offset:0\n"
      "v_mfma_f32_16x16x32_bf16 v[120:123], v[156:159], a[184:187], v[120:123]\n"
      "ds_read_b128 a[220:223], v248 offset:2048\n"
      "ds_read_b128 a[248:251], v249 offset:0\n"
      "v_mfma_f32_16x16x32_bf16 v[124:127], v[156:159], a[188:191], v[124:127]\n"
      "ds_read_b128 a[252:255], v249 offset:2048\n"
      "s_sub_u32 s48, s33, 1\n"
      "s_waitcnt lgkmcnt(15)\n"
      "v_mfma_f32_16x16x32_bf16 v[0:3], v[160:163], a[192:195], v[0:3]\n"
      "s_and_b32 s48, s48, 3\n"
      "global_load_dwordx4 v[152:155], v241, s[14:15] offset:0 sc1\n"
      "s_lshl_b32 s48, s48, 14\n"
      "s_waitcnt lgkmcnt(14)\n"
      "v_mfma_f32_16x16x32_bf16 v[4:7], v[160:163], a[196:199], v[4:7]\n"
      "s_mov_b32 m0, s41\n"
      "v_lshl_add_u32 v244, v243, 2, s48\n"
      "s_sub_u32 s48, s33, 2\n"
      "s_waitcnt lgkmcnt(11)\n"
      "v_mfma_f32_16x16x32_bf16 v[8:11], v[160:163], a[200:203], v[8:11]\n"
      "s_and_b32 s48, s48, 3\n"
      "s_lshl_b32 s48, s48, 14\n"
      "v_lshl_add_u32 v245, v243, 2, s48\n"
      "s_waitcnt lgkmcnt(10)\n"
      "v_mfma_f32_16x16x32_bf16 v[12:15], v[160:163], a[204:207], v[12:15]\n"
      "s_sub_u32 s48, s33, 3\n"
      "s_and_b32 s48, s48, 3\n"
      "s_lshl_b32 s48, s48, 14\n"
      "s_waitcnt lgkmcnt(7)\n"
      "v_mfma_f32_16x16x32_bf16 v[16:19], v[160:163], a[208:211], v[16:19]\n"
      "global_load_dwordx4 v[156:159], v241, s[74:75] offset:0 sc1\n"
      "v_lshl_add_u32 v246, v243, 2, s48\n"
      "s_waitcnt lgkmcnt(6)\n"
      "v_mfma_f32_16x16x32_bf16 v[20:23], v[160:163], a[212:215], v[20:23]\n"
      "v_bfe_u32 v248, v243, 1, 3\n"
      "v_lshrrev_b32 v249, 4, v243\n"
      "s_waitcnt lgkmcnt(3)\n"
      "v_mfma_f32_16x16x32_bf16 v[24:27], v[160:163], a[216:219], v[24:27]\n"
      "v_lshlrev_b32 v249, 1, v249\n"
      "v_sub_u32 v248, v248, v249\n"
      "s_waitcnt lgkmcnt(2)\n"
      "v_mfma_f32_16x16x32_bf16 v[28:31], v[160:163], a[220:223], v[28:31]\n"
      "v_and_b32 v249, 16, v242\n"
      "v_mov_b32 v254, 0x3f80\n"
      "v_mfma_f32_16x16x32_bf16 v[0:3], v[164:167], a[224:227], v[0:3]\n"
      "global_load_dwordx4 v[192:195], v240, s[10:11] offset:1024 sc1\n"
      "v_lshlrev_b32 v249, v249, v254\n"
      "v_mfma_f32_16x16x32_bf16 v[4:7], v[164:167], a[228:231], v[4:7]\n"
      "v_cmp_eq_u32 vcc, 0, v248\n"
      "s_nop 1\n"
      "v_cndmask_b32 v250, 0, v249, vcc\n"
      "v_mfma_f32_16x16x32_bf16 v[8:11], v[164:167], a[232:235], v[8:11]\n"
      "v_cmp_eq_u32 vcc, 1, v248\n"
      "s_nop 1\n"
      "v_cndmask_b32 v251, 0, v249, vcc\n"
      "v_mfma_f32_16x16x32_bf16 v[12:15], v[164:167], a[236:239], v[12:15]\n"
      "s_lshr_b32 s43, s42, 5\n"
      "s_lshl2_add_u32 s44, s36, s33\n"
      "s_cmp_eq_u32 s43, s44\n"
      "s_cbranch_scc0 .Lp_e11_%=\n"
      "s_ashr_i32 s43, s40, 31\n"
      "s_not_b32 s43, s43\n"
      "s_or_b32 s43, s43, 0x80000000\n"
      "s_xor_b32 s43, s43, s40\n"
      "s_and_b32 s43, s43, 0xffff0000\n"
      "v_lshlrev_b32 v208, 16, v216\n"
      "v_lshlrev_b32 v209, 16, v220\n"
      "v_and_b32 v216, 0xffff0000, v216\n"
      "v_and_b32 v220, 0xffff0000, v220\n"
      "v_mul_f32 v208, v208, v209\n"
      "v_mul_f32 v210, v216, v220\n"
      "v_cvt_pk_bf16_f32 v208, v208, v210\n"
      "v_lshlrev_b32 v209, 16, v208\n"
      "v_and_b32 v208, 0xffff0000, v208\n"
      "v_add_f32 v211, v208, v209\n"
      "v_lshlrev_b32 v208, 16, v217\n"
      "v_lshlrev_b32 v209, 16, v221\n"
      "v_and_b32 v217, 0xffff0000, v217\n"
      "v_and_b32 v221, 0xffff0000, v221\n"
      "v_mul_f32 v208, v208, v209\n"
      "v_mul_f32 v210, v217, v221\n"
      "v_cvt_pk_bf16_f32 v208, v208, v210\n"
      "v_lshlrev_b32 v209, 16, v208\n"
      "v_and_b32 v208, 0xffff0000, v208\n"
      "v_add_f32 v208, v208, v209\n"
      "v_add_f32 v211, v211, v208\n"
      "v_lshlrev_b32 v208, 16, v218\n"
      "v_lshlrev_b32 v209, 16, v222\n"
      "v_and_b32 v218, 0xffff0000, v218\n"
      "v_and_b32 v222, 0xffff0000, v222\n"
      "v_mul_f32 v208, v208, v209\n"
      "v_mul_f32 v210, v218, v222\n"
      "v_cvt_pk_bf16_f32 v208, v208, v210\n"
      "v_lshlrev_b32 v209, 16, v208\n"
      "v_and_b32 v208, 0xffff0000, v208\n"
      "v_add_f32 v208, v208, v209\n"
      "v_add_f32 v211, v211, v208\n"
      "v_lshlrev_b32 v208, 16, v219\n"
      "v_lshlrev_b32 v209, 16, v223\n"
      "v_and_b32 v219, 0xffff0000, v219\n"
      "v_and_b32 v223, 0xffff0000, v223\n"
      "v_mul_f32 v208, v208, v209\n"
      "v_mul_f32 v210, v219, v223\n"
      "v_cvt_pk_bf16_f32 v208, v208, v210\n"
      "v_lshlrev_b32 v209, 16, v208\n"
      "v_and_b32 v208, 0xffff0000, v208\n"
      "v_add_f32 v208, v208, v209\n"
      "v_add_f32 v211, v211, v208\n"
      "s_nop 1\n"
      "v_add_f32_dpp v211, v211, v211 quad_perm:[1,0,3,2] row_mask:0xf bank_mask:0xf\n"
      "s_nop 1\n"
      "v_add_f32_dpp v211, v211, v211 quad_perm:[2,3,0,1] row_mask:0xf bank_mask:0xf\n"
      "s_nop 1\n"
      "v_add_f32_dpp v211, v211, v211 row_half_mirror row_mask:0xf bank_mask:0xf\n"
      "s_nop 1\n"
      "v_add_f32_dpp v211, v211, v211 row_mirror row_mask:0xf bank_mask:0xf\n"
      "v_mov_b32 v208, v211\n"
      "s_nop 1\n"
      "v_permlane16_swap_b32 v211, v208\n"
      "v_add_f32 v211, v211, v208\n"
      "v_mov_b32 v208, v211\n"
      "s_nop 1\n"
      "v_permlane32_swap_b32 v211, v208\n"
      "v_add_f32 v211, v211, v208\n"
      "v_cvt_pk_bf16_f32 v211, v211, v211\n"
      "v_and_b32 v211, 0xffff0000, v211\n"
      "v_writelane_b32 v211, s43, 1\n"
      "v_mul_f32 v211, 0xbfb8aa3b, v211\n"
      "v_exp_f32 v211, v211\n"
      "s_nop 0\n"
      "v_add_f32 v211, 1.0, v211\n"
      "v_rcp_f32 v211, v211\n"
      "s_nop 0\n"
      "v_cvt_pk_bf16_f32 v211, v211, v211\n"
      "v_and_b32 v211, 0xffff0000, v211\n"
      "s_nop 0\n"
      "v_readfirstlane_b32 s39, v211\n"
      "v_readlane_b32 s84, v211, 1\n"
      ".Lp_e11_%=:\n"
      "v_mfma_f32_16x16x32_bf16 v[16:19], v[164:167], a[240:243], v[16:19]\n"
      "s_lshl2_add_u32 s54, s36, s33\n"
      "global_load_dwordx4 v[196:199], v240, s[72:73] offset:1024 sc1\n"
      "v_mov_b32 v252, v250\n"
      "v_mfma_f32_16x16x32_bf16 v[20:23], v[164:167], a[244:247], v[20:23]\n"
      "s_lshl_b32 s55, s54, 15\n"
      "v_mov_b32 v253, v251\n"
      "v_mov_b32 v248, 0\n"
      "s_waitcnt lgkmcnt(1)\n"
      "v_mfma_f32_16x16x32_bf16 v[24:27], v[164:167], a[248:251], v[24:27]\n"
      "s_lshl2_add_u32 s56, s35, s34\n"
      "v_mov_b32 v249, 0\n"
      "v_mov_b32 v254, 0\n"
      "s_waitcnt lgkmcnt(0)\n"
      "v_mfma_f32_16x16x32_bf16 v[28:31], v[164:167], a[252:255], v[28:31]\n"
      "s_waitcnt vmcnt(14)\n"
      "v_mov_b32 v255, 0\n"
      "s_add_u32 s48, s34, 1\n"
      "v_mfma_f32_16x16x32_bf16 v[32:35], v[168:171], a[192:195], v[32:35]\n"
      "s_and_b32 s48, s48, 3\n"
      "global_load_dwordx4 v[224:227], v240, s[14:15] offset:1024 sc1\n"
      "s_mul_i32 s48, s48, 36864\n"
      "v_mfma_f32_16x16x32_bf16 v[36:39], v[168:171], a[196:199], v[36:39]\n"
      "s_add_u32 s48, s48, 0\n"
      "s_add_u32 s52, s60, s48\n"
      "s_addc_u32 s53, s61, 0\n"
      "v_mfma_f32_16x16x32_bf16 v[40:43], v[168:171], a[200:203], v[40:43]\n"
      "s_lshl_b32 s56, s56, 6\n"
      "v_cvt_pk_bf16_f32 v8, v8, v9\n"
      "v_cvt_pk_bf16_f32 v9, v10, v11\n"
      "v_mfma_f32_16x16x32_bf16 v[44:47], v[168:171], a[204:207], v[44:47]\n"
      "s_add_u32 s55, s55, s56\n"
      "ds_write_addtid_b32 v8 offset:0\n"
      "v_cvt_pk_bf16_f32 v10, v12, v13\n"
      "v_mfma_f32_16x16x32_bf16 v[48:51], v[168:171], a[208:211], v[48:51]\n"
      "global_load_dwordx4 v[228:231], v240, s[74:75] offset:1024 sc1\n"
      "v_and_b32 v209, 15, v243\n"
      "v_mfma_f32_16x16x32_bf16 v[52:55], v[168:171], a[212:215], v[52:55]\n"
      "ds_write_addtid_b32 v9 offset:256\n"
      "v_cvt_pk_bf16_f32 v11, v14, v15\n"
      "v_mfma_f32_16x16x32_bf16 v[56:59], v[168:171], a[216:219], v[56:59]\n"
      "ds_write_addtid_b32 v10 offset:512\n"
      "v_cvt_pk_bf16_f32 v12, v16, v17\n"
      "v_mfma_f32_16x16x32_bf16 v[60:63], v[168:171], a[220:223], v[60:63]\n"
      "ds_write_addtid_b32 v11 offset:768\n"
      "v_cvt_pk_bf16_f32 v13, v18, v19\n"
      "v_mfma_f32_16x16x32_bf16 v[32:35], v[172:175], a[224:227], v[32:35]\n"
      "global_load_dwordx4 v[232:235], v241, s[10:11] offset:1024 sc1\n"
      "v_and_b32 v208, 48, v243\n"
      "v_mfma_f32_16x16x32_bf16 v[36:39], v[172:175], a[228:231], v[36:39]\n"
      "ds_write_addtid_b32 v12 offset:1024\n"
      "v_cvt_pk_bf16_f32 v14, v20, v21\n"
      "v_mfma_f32_16x16x32_bf16 v[40:43], v[172:175], a[232:235], v[40:43]\n"
      "ds_write_addtid_b32 v13 offset:1280\n"
      "v_cvt_pk_bf16_f32 v15, v22, v23\n"
      "v_mfma_f32_16x16x32_bf16 v[44:47], v[172:175], a[236:239], v[44:47]\n"
      "ds_write_addtid_b32 v14 offset:1536\n"
      "v_cvt_pk_bf16_f32 v16, v24, v25\n"
      "v_mfma_f32_16x16x32_bf16 v[48:51], v[172:175], a[240:243], v[48:51]\n"
      "global_load_dwordx4 v[236:239], v241, s[72:73] offset:1024 sc1\n"
      "v_lshl_or_b32 v208, v209, 10, v208\n"
      "v_mfma_f32_16x16x32_bf16 v[52:55], v[172:175], a[244:247], v[52:55]\n"
      "ds_write_addtid_b32 v15 offset:1792\n"
      "v_cvt_pk_bf16_f32 v17, v26, v27\n"
      "v_mfma_f32_16x16x32_bf16 v[56:59], v[172:175], a[248:251], v[56:59]\n"
      "ds_write_addtid_b32 v16 offset:2048\n"
      "v_cvt_pk_bf16_f32 v18, v28, v29\n"
      "v_mfma_f32_16x16x32_bf16 v[60:63], v[172:175], a[252:255], v[60:63]\n"
      "s_waitcnt vmcnt(16)\n"
      "ds_write_addtid_b32 v17 offset:2304\n"
      "v_cvt_pk_bf16_f32 v19, v30, v31\n"
      "v_mfma_f32_16x16x32_bf16 v[64:67], v[176:179], a[192:195], v[64:67]\n"
      "global_load_dwordx4 v[160:163], v241, s[14:15] offset:1024 sc1\n"
      "v_add_u32 v208, s55, v208\n"
      "v_mfma_f32_16x16x32_bf16 v[68:71], v[176:179], a[196:199], v[68:71]\n"
      "ds_write_addtid_b32 v18 offset:2560\n"
      "v_add_u32 v210, 16384, v208\n"
      "v_mfma_f32_16x16x32_bf16 v[72:75], v[176:179], a[200:203], v[72:75]\n"
      "ds_write_addtid_b32 v19 offset:2816\n"
      "global_load_dwordx4 v[216:219], v208, s[16:17]\n"
      "v_mfma_f32_16x16x32_bf16 v[76:79], v[176:179], a[204:207], v[76:79]\n"
      "global_load_dwordx4 v[220:223], v210, s[16:17]\n"
      "s_lshr_b32 s55, s34, 1\n"
      "v_mfma_f32_16x16x32_bf16 v[80:83], v[176:179], a[208:211], v[80:83]\n"
      "s_lshl2_add_u32 s55, s55, s35\n"
      "global_load_dwordx4 v[164:167], v241, s[74:75] offset:1024 sc1\n"
      "s_lshl_b32 s55, s55, 15\n"
      "v_mfma_f32_16x16x32_bf16 v[84:87], v[176:179], a[212:215], v[84:87]\n"
      "s_lshl_b32 s54, s54, 9\n"
      "s_waitcnt lgkmcnt(0)\n"
      "s_barrier\n"
      "v_mfma_f32_16x16x32_bf16 v[88:91], v[176:179], a[216:219], v[88:91]\n"
      "s_add_u32 s54, s54, s55\n"
      "ds_read2st64_b32 v[20:21], v244 offset0:0 offset1:1\n"
      "s_add_u32 s54, s54, 786432\n"
      "v_mfma_f32_16x16x32_bf16 v[92:95], v[176:179], a[220:223], v[92:95]\n"
      "ds_read2st64_b32 v[22:23], v245 offset0:4 offset1:5\n"
      "v_lshl_add_u32 v209, v209, 4, s54\n"
      "v_mfma_f32_16x16x32_bf16 v[64:67], v[180:183], a[224:227], v[64:67]\n"
      "global_load_dwordx4 v[168:171], v240, s[10:11] offset:2048 sc1\n"
      "v_mfma_f32_16x16x32_bf16 v[68:71], v[180:183], a[228:231], v[68:71]\n"
      "ds_read2st64_b32 v[24:25], v244 offset0:2 offset1:3\n"
      "v_mfma_f32_16x16x32_bf16 v[72:75], v[180:183], a[232:235], v[72:75]\n"
      "ds_read2st64_b32 v[26:27], v245 offset0:6 offset1:7\n"
      "v_mfma_f32_16x16x32_bf16 v[76:79], v[180:183], a[236:239], v[76:79]\n"
      "ds_read2st64_b32 v[28:29], v246 offset0:8 offset1:9\n"
      "v_mfma_f32_16x16x32_bf16 v[80:83], v[180:183], a[240:243], v[80:83]\n"
      "global_load_dwordx4 v[172:175], v240, s[72:73] offset:2048 sc1\n"
      "v_mfma_f32_16x16x32_bf16 v[84:87], v[180:183], a[244:247], v[84:87]\n"
      "ds_read2st64_b32 v[30:31], v246 offset0:10 offset1:11\n"
      "v_cvt_pk_bf16_f32 v40, v40, v41\n"
      "v_mfma_f32_16x16x32_bf16 v[88:91], v[180:183], a[248:251], v[88:91]\n"
      "global_load_dwordx2 v[204:205], v209, s[58:59] offset:0 sc1\n"
      "v_cvt_pk_bf16_f32 v41, v42, v43\n"
      "v_mfma_f32_16x16x32_bf16 v[92:95], v[180:183], a[252:255], v[92:95]\n"
      "s_waitcnt vmcnt(21)\n"
      "ds_write_addtid_b32 v40 offset:3072\n"
      "v_cvt_pk_bf16_f32 v42, v44, v45\n"
      "v_mfma_f32_16x16x32_bf16 v[96:99], v[184:187], a[192:195], v[96:99]\n"
      "global_load_dwordx4 v[176:179], v240, s[14:15] offset:2048 sc1\n"
      "v_mfma_f32_16x16x32_bf16 v[100:103], v[184:187], a[196:199], v[100:103]\n"
      "ds_write_addtid_b32 v41 offset:3328\n"
      "v_cvt_pk_bf16_f32 v43, v46, v47\n"
      "v_mfma_f32_16x16x32_bf16 v[104:107], v[184:187], a[200:203], v[104:107]\n"
      "s_waitcnt lgkmcnt(2)\n"
      "v_mfma_f32_16x16x32_bf16 v[0:3], v[250:253], v[20:23], v[0:3]\n"
      "v_mfma_f32_16x16x32_bf16 v[108:111], v[184:187], a[204:207], v[108:111]\n"
      "v_mfma_f32_16x16x32_bf16 v[4:7], v[250:253], v[24:27], v[4:7]\n"
      "v_mfma_f32_16x16x32_bf16 v[112:115], v[184:187], a[208:211], v[112:115]\n"
      "global_load_dwordx4 v[180:183], v240, s[74:75] offset:2048 sc1\n"
      "v_mfma_f32_16x16x32_bf16 v[116:119], v[184:187], a[212:215], v[116:119]\n"
      "v_mfma_f32_16x16x32_bf16 v[0:3], v[252:255], v[28:31], v[0:3]\n"
      "v_mfma_f32_16x16x32_bf16 v[120:123], v[184:187], a[216:219], v[120:123]\n"
      "v_mfma_f32_16x16x32_bf16 v[4:7], v[248:251], v[28:31], v[4:7]\n"
      "v_mov_b32 v11, s37\n"
      "v_mfma_f32_16x16x32_bf16 v[124:127], v[184:187], a[220:223], v[124:127]\n"
      "ds_write_addtid_b32 v42 offset:3584\n"
      "v_cvt_pk_bf16_f32 v44, v48, v49\n"
      "v_mfma_f32_16x16x32_bf16 v[96:99], v[188:191], a[224:227], v[96:99]\n"
      "global_load_dwordx4 v[184:187], v241, s[10:11] offset:2048 sc1\n"
      "v_cvt_pk_bf16_f32 v8, v0, v1\n"
      "v_mfma_f32_16x16x32_bf16 v[100:103], v[188:191], a[228:231], v[100:103]\n"
      "ds_write_addtid_b32 v43 offset:3840\n"
      "v_cvt_pk_bf16_f32 v9, v2, v3\n"
      "v_mfma_f32_16x16x32_bf16 v[104:107], v[188:191], a[232:235], v[104:107]\n"
      "global_load_dwordx2 v[206:207], v209, s[58:59] offset:256 sc1\n"
      "v_cvt_pk_bf16_f32 v10, v4, v5\n"
      "v_mfma_f32_16x16x32_bf16 v[108:111], v[188:191], a[236:239], v[108:111]\n"
      "s_add_u32 s68, s20, 16384\n"
      "s_addc_u32 s69, s21, 0\n"
      "v_cvt_pk_bf16_f32 v247, v6, v7\n"
      "v_mfma_f32_16x16x32_bf16 v[112:115], v[188:191], a[240:243], v[112:115]\n"
      "global_store_dwordx4 v242, v[8:11], s[52:53] offset:0 sc1\n"
      "v_cvt_pk_bf16_f32 v45, v50, v51\n"
      "v_mfma_f32_16x16x32_bf16 v[116:119], v[188:191], a[244:247], v[116:119]\n"
      "ds_write_addtid_b32 v44 offset:4096\n"
      "v_cvt_pk_bf16_f32 v46, v52, v53\n"
      "v_mfma_f32_16x16x32_bf16 v[120:123], v[188:191], a[248:251], v[120:123]\n"
      "ds_write_addtid_b32 v45 offset:4352\n"
      "v_cvt_pk_bf16_f32 v47, v54, v55\n"
      "v_mfma_f32_16x16x32_bf16 v[124:127], v[188:191], a[252:255], v[124:127]\n"
      "s_waitcnt vmcnt(24)\n"
      "global_load_dwordx4 v[188:191], v241, s[72:73] offset:2048 sc1\n"
      "v_lshrrev_b32 v208, 10, v208\n"
      "v_mfma_f32_16x16x32_bf16 v[0:3], v[128:131], a[0:3], 0\n"
      "ds_write_addtid_b32 v46 offset:4608\n"
      "v_cvt_pk_bf16_f32 v48, v56, v57\n"
      "v_mfma_f32_16x16x32_bf16 v[4:7], v[128:131], a[4:7], 0\n"
      "ds_write_addtid_b32 v47 offset:4864\n"
      "v_cvt_pk_bf16_f32 v49, v58, v59\n"
      "v_mfma_f32_16x16x32_bf16 v[8:11], v[128:131], a[8:11], 0\n"
      "ds_write_addtid_b32 v48 offset:5120\n"
      "v_cvt_pk_bf16_f32 v50, v60, v61\n"
      "v_mfma_f32_16x16x32_bf16 v[12:15], v[128:131], a[12:15], 0\n"
      "ds_write_addtid_b32 v49 offset:5376\n"
      "v_cvt_pk_bf16_f32 v51, v62, v63\n"
      "v_mfma_f32_16x16x32_bf16 v[16:19], v[128:131], a[16:19], 0\n"
      "ds_write_addtid_b32 v50 offset:5632\n"
      "v_subrev_u32 v208, s42, v208\n"
      "v_mfma_f32_16x16x32_bf16 v[20:23], v[128:131], a[20:23], 0\n"
      "ds_write_addtid_b32 v51 offset:5888\n"
      "v_cvt_pk_bf16_f32 v72, v72, v73\n"
      "v_mfma_f32_16x16x32_bf16 v[24:27], v[128:131], a[24:27], 0\n"
      "v_cvt_pk_bf16_f32 v73, v74, v75\n"
      "v_mfma_f32_16x16x32_bf16 v[28:31], v[128:131], a[28:31], 0\n"
      "global_load_dwordx4 v[128:131], v241, s[14:15] offset:2048 sc1\n"
      "v_mfma_f32_16x16x32_bf16 v[0:3], v[132:135], a[32:35], v[0:3]\n"
      "ds_write_addtid_b32 v72 offset:6144\n"
      "v_cvt_pk_bf16_f32 v74, v76, v77\n"
      "v_mfma_f32_16x16x32_bf16 v[4:7], v[132:135], a[36:39], v[4:7]\n"
      "ds_write_addtid_b32 v73 offset:6400\n"
      "v_cvt_pk_bf16_f32 v75, v78, v79\n"
      "v_mfma_f32_16x16x32_bf16 v[8:11], v[132:135], a[40:43], v[8:11]\n"
      "ds_write_addtid_b32 v74 offset:6656\n"
      "v_cvt_pk_bf16_f32 v76, v80, v81\n"
      "v_mfma_f32_16x16x32_bf16 v[12:15], v[132:135], a[44:47], v[12:15]\n"
      "ds_write_addtid_b32 v75 offset:6912\n"
      "v_cvt_pk_bf16_f32 v77, v82, v83\n"
      "v_mfma_f32_16x16x32_bf16 v[16:19], v[132:135], a[48:51], v[16:19]\n"
      "ds_write_addtid_b32 v76 offset:7168\n"
      "v_cvt_pk_bf16_f32 v78, v84, v85\n"
      "v_mfma_f32_16x16x32_bf16 v[20:23], v[132:135], a[52:55], v[20:23]\n"
      "ds_write_addtid_b32 v77 offset:7424\n"
      "v_cvt_pk_bf16_f32 v79, v86, v87\n"
      "v_mfma_f32_16x16x32_bf16 v[24:27], v[132:135], a[56:59], v[24:27]\n"
      "ds_write_addtid_b32 v78 offset:7680\n"
      "v_cvt_pk_bf16_f32 v80, v88, v89\n"
      "v_mfma_f32_16x16x32_bf16 v[28:31], v[132:135], a[60:63], v[28:31]\n"
      "s_waitcnt vmcnt(24)\n"
      "global_load_dwordx4 v[132:135], v241, s[74:75] offset:2048 sc1\n"
      "v_mfma_f32_16x16x32_bf16 v[0:3], v[136:139], a[64:67], v[0:3]\n"
      "ds_write_addtid_b32 v79 offset:7936\n"
      "v_cvt_pk_bf16_f32 v81, v90, v91\n"
      "v_mfma_f32_16x16x32_bf16 v[4:7], v[136:139], a[68:71], v[4:7]\n"
      "ds_write_addtid_b32 v80 offset:8192\n"
      "v_cvt_pk_bf16_f32 v82, v92, v93\n"
      "v_mfma_f32_16x16x32_bf16 v[8:11], v[136:139], a[72:75], v[8:11]\n"
      "ds_write_addtid_b32 v81 offset:8448\n"
      "v_cvt_pk_bf16_f32 v83, v94, v95\n"
      "v_mfma_f32_16x16x32_bf16 v[12:15], v[136:139], a[76:79], v[12:15]\n"
      "ds_write_addtid_b32 v82 offset:8704\n"
      "v_mfma_f32_16x16x32_bf16 v[16:19], v[136:139], a[80:83], v[16:19]\n"
      "ds_write_addtid_b32 v83 offset:8960\n"
      "v_cvt_pk_bf16_f32 v104, v104, v105\n"
      "v_mfma_f32_16x16x32_bf16 v[20:23], v[136:139], a[84:87], v[20:23]\n"
      "v_cvt_pk_bf16_f32 v105, v106, v107\n"
      "v_mfma_f32_16x16x32_bf16 v[24:27], v[136:139], a[88:91], v[24:27]\n"
      "ds_write_addtid_b32 v104 offset:9216\n"
      "v_cvt_pk_bf16_f32 v106, v108, v109\n"
      "v_mfma_f32_16x16x32_bf16 v[28:31], v[136:139], a[92:95], v[28:31]\n"
      "global_load_dwordx4 v[136:139], v240, s[10:11] offset:3072 sc1\n"
      "v_mfma_f32_16x16x32_bf16 v[0:3], v[140:143], a[96:99], v[0:3]\n"
      "ds_write_addtid_b32 v105 offset:9472\n"
      "v_cvt_pk_bf16_f32 v107, v110, v111\n"
      "v_mfma_f32_16x16x32_bf16 v[4:7], v[140:143], a[100:103], v[4:7]\n"
      "ds_write_addtid_b32 v106 offset:9728\n"
      "v_cvt_pk_bf16_f32 v108, v112, v113\n"
      "v_mfma_f32_16x16x32_bf16 v[8:11], v[140:143], a[104:107], v[8:11]\n"
      "ds_write_addtid_b32 v107 offset:9984\n"
      "v_cvt_pk_bf16_f32 v109, v114, v115\n"
      "v_mfma_f32_16x16x32_bf16 v[12:15], v[140:143], a[108:111], v[12:15]\n"
      "ds_write_addtid_b32 v108 offset:10240\n"
      "v_cvt_pk_bf16_f32 v110, v116, v117\n"
      "v_mfma_f32_16x16x32_bf16 v[16:19], v[140:143], a[112:115], v[16:19]\n"
      "ds_write_addtid_b32 v109 offset:10496\n"
      "v_cvt_pk_bf16_f32 v111, v118, v119\n"
      "v_mfma_f32_16x16x32_bf16 v[20:23], v[140:143], a[116:119], v[20:23]\n"
      "ds_write_addtid_b32 v110 offset:10752\n"
      "v_cvt_pk_bf16_f32 v112, v120, v121\n"
      "v_mfma_f32_16x16x32_bf16 v[24:27], v[140:143], a[120:123], v[24:27]\n"
      "ds_write_addtid_b32 v111 offset:11008\n"
      "v_cvt_pk_bf16_f32 v113, v122, v123\n"
      "v_mfma_f32_16x16x32_bf16 v[28:31], v[140:143], a[124:127], v[28:31]\n"
      "s_waitcnt vmcnt(24)\n"
      "global_load_dwordx4 v[140:143], v240, s[72:73] offset:3072 sc1\n"
      "v_mfma_f32_16x16x32_bf16 v[0:3], v[144:147], a[128:131], v[0:3]\n"
      "ds_write_addtid_b32 v112 offset:11264\n"
      "v_cvt_pk_bf16_f32 v114, v124, v125\n"
      "v_mfma_f32_16x16x32_bf16 v[4:7], v[144:147], a[132:135], v[4:7]\n"
      "ds_write_addtid_b32 v113 offset:11520\n"
      "v_cvt_pk_bf16_f32 v115, v126, v127\n"
      "v_mfma_f32_16x16x32_bf16 v[8:11], v[144:147], a[136:139], v[8:11]\n"
      "ds_write_addtid_b32 v114 offset:11776\n"
      "v_mfma_f32_16x16x32_bf16 v[12:15], v[144:147], a[140:143], v[12:15]\n"
      "ds_write_addtid_b32 v115 offset:12032\n"
      "v_mfma_f32_16x16x32_bf16 v[16:19], v[144:147], a[144:147], v[16:19]\n"
      "v_mfma_f32_16x16x32_bf16 v[20:23], v[144:147], a[148:151], v[20:23]\n"
      "v_mfma_f32_16x16x32_bf16 v[24:27], v[144:147], a[152:155], v[24:27]\n"
      "s_waitcnt lgkmcnt(0)\n"
      "s_barrier\n"
      "v_mfma_f32_16x16x32_bf16 v[28:31], v[144:147], a[156:159], v[28:31]\n"
      "global_load_dwordx4 v[144:147], v240, s[14:15] offset:3072 sc1\n"
      "v_mfma_f32_16x16x32_bf16 v[0:3], v[148:151], a[160:163], v[0:3]\n"
      "ds_read2st64_b32 v[52:53], v244 offset0:12 offset1:13\n"
      "v_mfma_f32_16x16x32_bf16 v[4:7], v[148:151], a[164:167], v[4:7]\n"
      "ds_read2st64_b32 v[54:55], v245 offset0:16 offset1:17\n"
      "v_mfma_f32_16x16x32_bf16 v[8:11], v[148:151], a[168:171], v[8:11]\n"
      "ds_read2st64_b32 v[56:57], v244 offset0:14 offset1:15\n"
      "v_mfma_f32_16x16x32_bf16 v[12:15], v[148:151], a[172:175], v[12:15]\n"
      "ds_read2st64_b32 v[58:59], v245 offset0:18 offset1:19\n"
      "v_mfma_f32_16x16x32_bf16 v[16:19], v[148:151], a[176:179], v[16:19]\n"
      "ds_read2st64_b32 v[60:61], v246 offset0:20 offset1:21\n"
      "v_mfma_f32_16x16x32_bf16 v[20:23], v[148:151], a[180:183], v[20:23]\n"
      "ds_read2st64_b32 v[62:63], v246 offset0:22 offset1:23\n"
      "v_mfma_f32_16x16x32_bf16 v[24:27], v[148:151], a[184:187], v[24:27]\n"
      "ds_read2st64_b32 v[84:85], v244 offset0:24 offset1:25\n"
      "v_mfma_f32_16x16x32_bf16 v[28:31], v[148:151], a[188:191], v[28:31]\n"
      "s_waitcnt vmcnt(24)\n"
      "global_load_dwordx4 v[148:151], v240, s[74:75] offset:3072 sc1\n"
      "v_mfma_f32_16x16x32_bf16 v[0:3], v[152:155], a[192:195], v[0:3]\n"
      "ds_read2st64_b32 v[86:87], v245 offset0:28 offset1:29\n"
      "v_mfma_f32_16x16x32_bf16 v[4:7], v[152:155], a[196:199], v[4:7]\n"
      "ds_read2st64_b32 v[88:89], v244 offset0:26 offset1:27\n"
      "v_mfma_f32_16x16x32_bf16 v[8:11], v[152:155], a[200:203], v[8:11]\n"
      "s_waitcnt lgkmcnt(3)\n"
      "v_mfma_f32_16x16x32_bf16 v[32:35], v[250:253], v[52:55], v[32:35]\n"
      "v_mfma_f32_16x16x32_bf16 v[12:15], v[152:155], a[204:207], v[12:15]\n"
      "v_mfma_f32_16x16x32_bf16 v[36:39], v[250:253], v[56:59], v[36:39]\n"
      "v_mfma_f32_16x16x32_bf16 v[16:19], v[152:155], a[208:211], v[16:19]\n"
      "v_mfma_f32_16x16x32_bf16 v[32:35], v[252:255], v[60:63], v[32:35]\n"
      "v_mfma_f32_16x16x32_bf16 v[20:23], v[152:155], a[212:215], v[20:23]\n"
      "v_mfma_f32_16x16x32_bf16 v[36:39], v[248:251], v[60:63], v[36:39]\n"
      "v_mov_b32 v43, s37\n"
      "v_mfma_f32_16x16x32_bf16 v[24:27], v[152:155], a[216:219], v[24:27]\n"
      "ds_read2st64_b32 v[90:91], v245 offset0:30 offset1:31\n"
      "v_mov_b32 v44, v247\n"
      "v_mfma_f32_16x16x32_bf16 v[28:31], v[152:155], a[220:223], v[28:31]\n"
      "global_load_dwordx4 v[152:155], v241, s[10:11] offset:3072 sc1\n"
      "v_mov_b32 v47, s37\n"
      "v_mfma_f32_16x16x32_bf16 v[0:3], v[156:159], a[224:227], v[0:3]\n"
      "ds_read2st64_b32 v[92:93], v246 offset0:32 offset1:33\n"
      "v_mfma_f32_16x16x32_bf16 v[4:7], v[156:159], a[228:231], v[4:7]\n"
      "ds_read2st64_b32 v[94:95], v246 offset0:34 offset1:35\n"
      "v_cvt_pk_bf16_f32 v40, v32, v33\n"
      "v_mfma_f32_16x16x32_bf16 v[8:11], v[156:159], a[232:235], v[8:11]\n"
      "ds_read2st64_b32 v[116:117], v244 offset0:36 offset1:37\n"
      "v_cvt_pk_bf16_f32 v41, v34, v35\n"
      "v_mfma_f32_16x16x32_bf16 v[12:15], v[156:159], a[236:239], v[12:15]\n"
      "ds_read2st64_b32 v[118:119], v245 offset0:40 offset1:41\n"
      "v_cvt_pk_bf16_f32 v42, v36, v37\n"
      "v_mfma_f32_16x16x32_bf16 v[16:19], v[156:159], a[240:243], v[16:19]\n"
      "ds_read2st64_b32 v[120:121], v244 offset0:38 offset1:39\n"
      "v_cvt_pk_bf16_f32 v45, v38, v39\n"
      "v_mfma_f32_16x16x32_bf16 v[20:23], v[156:159], a[244:247], v[20:23]\n"
      "global_store_dwordx4 v242, v[40:43], s[52:53] offset:1024 sc1\n"
      "v_mfma_f32_16x16x32_bf16 v[24:27], v[156:159], a[248:251], v[24:27]\n"
      "global_store_dwordx4 v242, v[44:47], s[52:53] offset:2048 sc1\n"
      "s_add_u32 s48, s34, 2\n"
      "v_mfma_f32_16x16x32_bf16 v[28:31], v[156:159], a[252:255], v[28:31]\n"
      "s_waitcnt vmcnt(26)\n"
      "global_load_dwordx4 v[156:159], v241, s[72:73] offset:3072 sc1\n"
      "s_and_b32 s48, s48, 3\n"
      "v_mfma_f32_16x16x32_bf16 v[32:35], v[192:195], a[0:3], 0\n"
      "s_mul_i32 s48, s48, 36864\n"
      "ds_read2st64_b32 v[122:123], v245 offset0:42 offset1:43\n"
      "s_add_u32 s48, s48, 12288\n"
      "v_mfma_f32_16x16x32_bf16 v[36:39], v[192:195], a[4:7], 0\n"
      "s_add_u32 s52, s60, s48\n"
      "s_addc_u32 s53, s61, 0\n"
      "ds_read2st64_b32 v[124:125], v246 offset0:44 offset1:45\n"
      "v_mfma_f32_16x16x32_bf16 v[40:43], v[192:195], a[8:11], 0\n"
      "s_waitcnt lgkmcnt(5)\n"
      "v_mfma_f32_16x16x32_bf16 v[64:67], v[250:253], v[84:87], v[64:67]\n"
      "v_mfma_f32_16x16x32_bf16 v[44:47], v[192:195], a[12:15], 0\n"
      "v_mfma_f32_16x16x32_bf16 v[68:71], v[250:253], v[88:91], v[68:71]\n"
      "v_mfma_f32_16x16x32_bf16 v[48:51], v[192:195], a[16:19], 0\n"
      "v_mfma_f32_16x16x32_bf16 v[64:67], v[252:255], v[92:95], v[64:67]\n"
      "v_mfma_f32_16x16x32_bf16 v[52:55], v[192:195], a[20:23], 0\n"
      "v_mfma_f32_16x16x32_bf16 v[68:71], v[248:251], v[92:95], v[68:71]\n"
      "v_mov_b32 v75, s37\n"
      "v_mfma_f32_16x16x32_bf16 v[56:59], v[192:195], a[24:27], 0\n"
      "ds_read2st64_b32 v[126:127], v246 offset0:46 offset1:47\n"
      "v_cvt_pk_bf16_f32 v8, v8, v9\n"
      "v_mfma_f32_16x16x32_bf16 v[60:63], v[192:195], a[28:31], 0\n"
      "global_load_dwordx4 v[192:195], v241, s[14:15] offset:3072 sc1\n"
      "v_cvt_pk_bf16_f32 v72, v64, v65\n"
      "v_mfma_f32_16x16x32_bf16 v[32:35], v[196:199], a[32:35], v[32:35]\n"
      "s_waitcnt vmcnt(13)\n"
      "v_bitop3_b32 v210, v205, v207, s37 bitop3:0x7e\n"
      "v_cmp_ne_u32 vcc, 0, v210\n"
      "s_nop 1\n"
      "s_cbranch_vccnz .Lp_s13_%=\n"
      ".Lp_o14_%=:\n"
      "v_mfma_f32_16x16x32_bf16 v[36:39], v[196:199], a[36:39], v[36:39]\n"
      "v_cvt_pk_bf16_f32 v73, v66, v67\n"
      "v_cvt_pk_bf16_f32 v74, v68, v69\n"
      "v_mfma_f32_16x16x32_bf16 v[40:43], v[196:199], a[40:43], v[40:43]\n"
      "v_cvt_pk_bf16_f32 v247, v70, v71\n"
      "v_cvt_pk_bf16_f32 v9, v10, v11\n"
      "v_mfma_f32_16x16x32_bf16 v[44:47], v[196:199], a[44:47], v[44:47]\n"
      "global_store_dwordx4 v242, v[72:75], s[52:53] offset:0 sc1\n"
      "s_waitcnt lgkmcnt(0)\n"
      "v_mfma_f32_16x16x32_bf16 v[48:51], v[196:199], a[48:51], v[48:51]\n"
      "v_mfma_f32_16x16x32_bf16 v[96:99], v[250:253], v[116:119], v[96:99]\n"
      "v_mov_b32 v209, s39\n"
      "v_mfma_f32_16x16x32_bf16 v[52:55], v[196:199], a[52:55], v[52:55]\n"
      "v_mfma_f32_16x16x32_bf16 v[100:103], v[250:253], v[120:123], v[100:103]\n"
      "v_mfma_f32_16x16x32_bf16 v[56:59], v[196:199], a[56:59], v[56:59]\n"
      "v_mfma_f32_16x16x32_bf16 v[96:99], v[252:255], v[124:127], v[96:99]\n"
      "v_mfma_f32_16x16x32_bf16 v[60:63], v[196:199], a[60:63], v[60:63]\n"
      "global_load_dwordx4 v[196:199], v241, s[74:75] offset:3072 sc1\n"
      "v_mfma_f32_16x16x32_bf16 v[32:35], v[224:227], a[64:67], v[32:35]\n"
      "v_mfma_f32_16x16x32_bf16 v[100:103], v[248:251], v[124:127], v[100:103]\n"
      "v_mov_b32 v107, s37\n"
      "v_mfma_f32_16x16x32_bf16 v[36:39], v[224:227], a[68:71], v[36:39]\n"
      "ds_write_addtid_b32 v8 offset:0\n"
      "v_mov_b32 v108, v247\n"
      "v_mfma_f32_16x16x32_bf16 v[40:43], v[224:227], a[72:75], v[40:43]\n"
      "v_cmp_eq_u32 vcc, 0, v208\n"
      "s_nop 1\n"
      "v_cndmask_b32 v204, v204, v209, vcc\n"
      "v_mfma_f32_16x16x32_bf16 v[44:47], v[224:227], a[76:79], v[44:47]\n"
      "v_cmp_eq_u32 vcc, -16, v208\n"
      "s_nop 1\n"
      "v_cndmask_b32 v206, v206, v209, vcc\n"
      "v_mfma_f32_16x16x32_bf16 v[48:51], v[224:227], a[80:83], v[48:51]\n"
      "v_mov_b32 v111, s37\n"
      "v_cvt_pk_bf16_f32 v10, v12, v13\n"
      "v_mfma_f32_16x16x32_bf16 v[52:55], v[224:227], a[84:87], v[52:55]\n"
      "ds_write_addtid_b32 v9 offset:256\n"
      "v_cvt_pk_bf16_f32 v11, v14, v15\n"
      "v_mfma_f32_16x16x32_bf16 v[56:59], v[224:227], a[88:91], v[56:59]\n"
      "ds_write_addtid_b32 v10 offset:512\n"
      "v_cvt_pk_bf16_f32 v104, v96, v97\n"
      "v_mfma_f32_16x16x32_bf16 v[60:63], v[224:227], a[92:95], v[60:63]\n"
      "v_cvt_pk_bf16_f32 v105, v98, v99\n"
      "v_cvt_pk_bf16_f32 v106, v100, v101\n"
      "v_mfma_f32_16x16x32_bf16 v[32:35], v[228:231], a[96:99], v[32:35]\n"
      "v_cvt_pk_bf16_f32 v109, v102, v103\n"
      "v_cvt_pk_bf16_f32 v12, v16, v17\n"
      "v_mfma_f32_16x16x32_bf16 v[36:39], v[228:231], a[100:103], v[36:39]\n"
      "global_store_dwordx4 v242, v[104:107], s[52:53] offset:1024 sc1\n"
      "v_sub_f32 v205, 1.0, v204\n"
      "v_mfma_f32_16x16x32_bf16 v[40:43], v[228:231], a[104:107], v[40:43]\n"
      "global_store_dwordx4 v242, v[108:111], s[52:53] offset:2048 sc1\n"
      "s_add_u32 s48, s34, 3\n"
      "v_mfma_f32_16x16x32_bf16 v[44:47], v[228:231], a[108:111], v[44:47]\n"
      "s_and_b32 s48, s48, 3\n"
      "ds_write_addtid_b32 v11 offset:768\n"
      "s_mul_i32 s48, s48, 36864\n"
      "v_mfma_f32_16x16x32_bf16 v[48:51], v[228:231], a[112:115], v[48:51]\n"
      "s_add_u32 s48, s48, 24576\n"
      "s_add_u32 s52, s60, s48\n"
      "s_addc_u32 s53, s61, 0\n"
      "v_mfma_f32_16x16x32_bf16 v[52:55], v[228:231], a[116:119], v[52:55]\n"
      "v_cvt_pk_bf16_f32 v13, v18, v19\n"
      "v_cvt_pk_bf16_f32 v205, v205, v205\n"
      "v_mfma_f32_16x16x32_bf16 v[56:59], v[228:231], a[120:123], v[56:59]\n"
      "ds_write_addtid_b32 v12 offset:1024\n"
      "v_cvt_pk_bf16_f32 v14, v20, v21\n"
      "v_mfma_f32_16x16x32_bf16 v[60:63], v[228:231], a[124:127], v[60:63]\n"
      "ds_write_addtid_b32 v13 offset:1280\n"
      "v_cvt_pk_bf16_f32 v15, v22, v23\n"
      "v_mfma_f32_16x16x32_bf16 v[40:43], v[232:235], a[136:139], v[40:43]\n"
      "ds_write_addtid_b32 v14 offset:1536\n"
      "v_cvt_pk_bf16_f32 v16, v24, v25\n"
      "v_mfma_f32_16x16x32_bf16 v[44:47], v[232:235], a[140:143], v[44:47]\n"
      "ds_write_addtid_b32 v15 offset:1792\n"
      "v_cvt_pk_bf16_f32 v17, v26, v27\n"
      "v_mfma_f32_16x16x32_bf16 v[48:51], v[232:235], a[144:147], v[48:51]\n"
      "ds_write_addtid_b32 v16 offset:2048\n"
      "v_cvt_pk_bf16_f32 v18, v28, v29\n"
      "v_mfma_f32_16x16x32_bf16 v[52:55], v[232:235], a[148:151], v[52:55]\n"
      "ds_write_addtid_b32 v17 offset:2304\n"
      "v_cvt_pk_bf16_f32 v19, v30, v31\n"
      "v_mfma_f32_16x16x32_bf16 v[56:59], v[232:235], a[152:155], v[56:59]\n"
      "ds_write_addtid_b32 v18 offset:2560\n"
      "v_and_b32 v205, 0xffff0000, v205\n"
      "v_mfma_f32_16x16x32_bf16 v[60:63], v[232:235], a[156:159], v[60:63]\n"
      "ds_write_addtid_b32 v19 offset:2816\n"
      "v_sub_f32 v207, 1.0, v206\n"
      "v_mfma_f32_16x16x32_bf16 v[40:43], v[236:239], a[168:171], v[40:43]\n"
      "v_cvt_pk_bf16_f32 v207, v207, v207\n"
      "v_and_b32 v207, 0xffff0000, v207\n"
      "v_mfma_f32_16x16x32_bf16 v[44:47], v[236:239], a[172:175], v[44:47]\n"
      "v_lshlrev_b32 v208, 16, v216\n"
      "v_and_b32 v209, 0xffff0000, v216\n"
      "v_mfma_f32_16x16x32_bf16 v[48:51], v[236:239], a[176:179], v[48:51]\n"
      "s_waitcnt lgkmcnt(0)\n"
      "s_barrier\n"
      "v_mfma_f32_16x16x32_bf16 v[52:55], v[236:239], a[180:183], v[52:55]\n"
      "ds_read2st64_b32 v[20:21], v244 offset0:0 offset1:1\n"
      "v_mul_f32 v208, v205, v208\n"
      "v_mfma_f32_16x16x32_bf16 v[56:59], v[236:239], a[184:187], v[56:59]\n"
      "ds_read2st64_b32 v[22:23], v245 offset0:4 offset1:5\n"
      "v_mul_f32 v209, v205, v209\n"
      "v_mfma_f32_16x16x32_bf16 v[60:63], v[236:239], a[188:191], v[60:63]\n"
      "ds_read2st64_b32 v[24:25], v244 offset0:2 offset1:3\n"
      "v_lshlrev_b32 v212, 16, v220\n"
      "v_mfma_f32_16x16x32_bf16 v[40:43], v[160:163], a[200:203], v[40:43]\n"
      "ds_read2st64_b32 v[26:27], v245 offset0:6 offset1:7\n"
      "v_and_b32 v213, 0xffff0000, v220\n"
      "v_mfma_f32_16x16x32_bf16 v[44:47], v[160:163], a[204:207], v[44:47]\n"
      "ds_read2st64_b32 v[28:29], v246 offset0:8 offset1:9\n"
      "ds_read2st64_b32 v[30:31], v246 offset0:10 offset1:11\n"
      "v_mul_f32 v212, v207, v212\n"
      "v_mfma_f32_16x16x32_bf16 v[48:51], v[160:163], a[208:211], v[48:51]\n"
      "v_mul_f32 v213, v207, v213\n"
      "v_lshlrev_b32 v210, 16, v217\n"
      "v_and_b32 v211, 0xffff0000, v217\n"
      "v_mfma_f32_16x16x32_bf16 v[52:55], v[160:163], a[212:215], v[52:55]\n"
      "v_mul_f32 v210, v205, v210\n"
      "v_mul_f32 v211, v205, v211\n"
      "v_lshlrev_b32 v214, 16, v221\n"
      "v_mfma_f32_16x16x32_bf16 v[56:59], v[160:163], a[216:219], v[56:59]\n"
      "v_and_b32 v215, 0xffff0000, v221\n"
      "v_mul_f32 v214, v207, v214\n"
      "v_mul_f32 v215, v207, v215\n"
      "v_mfma_f32_16x16x32_bf16 v[60:63], v[160:163], a[220:223], v[60:63]\n"
      "v_lshlrev_b32 v216, 16, v218\n"
      "v_and_b32 v217, 0xffff0000, v218\n"
      "v_mul_f32 v216, v205, v216\n"
      "v_mfma_f32_16x16x32_bf16 v[40:43], v[164:167], a[232:235], v[40:43]\n"
      "s_waitcnt lgkmcnt(0)\n"
      "v_mfma_f32_16x16x32_bf16 v[0:3], v[250:253], v[20:23], v[0:3]\n"
      "v_mfma_f32_16x16x32_bf16 v[4:7], v[250:253], v[24:27], v[4:7]\n"
      "v_mul_f32 v217, v205, v217\n"
      "v_mfma_f32_16x16x32_bf16 v[44:47], v[164:167], a[236:239], v[44:47]\n"
      "v_mfma_f32_16x16x32_bf16 v[0:3], v[252:255], v[28:31], v[0:3]\n"
      "v_mfma_f32_16x16x32_bf16 v[4:7], v[248:251], v[28:31], v[4:7]\n"
      "v_mov_b32 v11, s37\n"
      "v_mfma_f32_16x16x32_bf16 v[48:51], v[164:167], a[240:243], v[48:51]\n"
      "v_lshlrev_b32 v220, 16, v222\n"
      "v_and_b32 v221, 0xffff0000, v222\n"
      "v_mul_f32 v220, v207, v220\n"
      "v_mfma_f32_16x16x32_bf16 v[52:55], v[164:167], a[244:247], v[52:55]\n"
      "s_nop 0\n"
      "v_cvt_pk_bf16_f32 v8, v0, v1\n"
      "v_cvt_pk_bf16_f32 v9, v2, v3\n"
      "v_mfma_f32_16x16x32_bf16 v[56:59], v[164:167], a[248:251], v[56:59]\n"
      "v_cvt_pk_bf16_f32 v10, v4, v5\n"
      "v_cvt_pk_bf16_f32 v247, v6, v7\n"
      "v_cvt_pk_bf16_f32 v40, v40, v41\n"
      "v_mfma_f32_16x16x32_bf16 v[60:63], v[164:167], a[252:255], v[60:63]\n"
      "global_store_dwordx4 v242, v[8:11], s[52:53] offset:0 sc1\n"
      "v_cvt_pk_bf16_f32 v41, v42, v43\n"
      "v_mul_f32 v221, v207, v221\n"
      "v_mfma_f32_16x16x32_bf16 v[32:35], v[232:235], a[128:131], v[32:35]\n"
      "ds_write_addtid_b32 v40 offset:3072\n"
      "v_cvt_pk_bf16_f32 v42, v44, v45\n"
      "v_lshlrev_b32 v218, 16, v219\n"
      "v_mfma_f32_16x16x32_bf16 v[36:39], v[232:235], a[132:135], v[36:39]\n"
      "ds_write_addtid_b32 v41 offset:3328\n"
      "v_cvt_pk_bf16_f32 v43, v46, v47\n"
      "v_and_b32 v219, 0xffff0000, v219\n"
      "v_mfma_f32_16x16x32_bf16 v[32:35], v[236:239], a[160:163], v[32:35]\n"
      "ds_write_addtid_b32 v42 offset:3584\n"
      "v_cvt_pk_bf16_f32 v44, v48, v49\n"
      "v_mul_f32 v218, v205, v218\n"
      "v_mfma_f32_16x16x32_bf16 v[36:39], v[236:239], a[164:167], v[36:39]\n"
      "ds_write_addtid_b32 v43 offset:3840\n"
      "v_cvt_pk_bf16_f32 v45, v50, v51\n"
      "v_mul_f32 v219, v205, v219\n"
      "v_mfma_f32_16x16x32_bf16 v[32:35], v[160:163], a[192:195], v[32:35]\n"
      "ds_write_addtid_b32 v44 offset:4096\n"
      "v_cvt_pk_bf16_f32 v46, v52, v53\n"
      "v_lshlrev_b32 v222, 16, v223\n"
      "v_mfma_f32_16x16x32_bf16 v[36:39], v[160:163], a[196:199], v[36:39]\n"
      "ds_write_addtid_b32 v45 offset:4352\n"
      "v_cvt_pk_bf16_f32 v47, v54, v55\n"
      "v_and_b32 v223, 0xffff0000, v223\n"
      "v_mfma_f32_16x16x32_bf16 v[32:35], v[164:167], a[224:227], v[32:35]\n"
      "ds_write_addtid_b32 v46 offset:4608\n"
      "v_cvt_pk_bf16_f32 v48, v56, v57\n"
      "v_mul_f32 v222, v207, v222\n"
      "v_mfma_f32_16x16x32_bf16 v[36:39], v[164:167], a[228:231], v[36:39]\n"
      "ds_write_addtid_b32 v47 offset:4864\n"
      "v_cvt_pk_bf16_f32 v49, v58, v59\n"
      "v_mul_f32 v223, v207, v223\n"
      "v_mfma_f32_16x16x32_bf16 v[72:75], v[168:171], a[8:11], 0\n"
      "s_lshl2_add_u32 s54, s36, s33\n"
      "ds_write_addtid_b32 v48 offset:5120\n"
      "v_cvt_pk_bf16_f32 v50, v60, v61\n"
      "s_lshl_b32 s55, s54, 15\n"
      "v_mfma_f32_16x16x32_bf16 v[76:79], v[168:171], a[12:15], 0\n"
      "s_lshl2_add_u32 s56, s35, s34\n"
      "ds_write_addtid_b32 v49 offset:5376\n"
      "v_cvt_pk_bf16_f32 v51, v62, v63\n"
      "s_lshl_b32 s56, s56, 6\n"
      "v_mfma_f32_16x16x32_bf16 v[80:83], v[168:171], a[16:19], 0\n"
      "s_add_u32 s55, s55, s56\n"
      "ds_write_addtid_b32 v50 offset:5632\n"
      "ds_write_addtid_b32 v51 offset:5888\n"
      "v_and_b32 v8, 15, v243\n"
      "v_mfma_f32_16x16x32_bf16 v[84:87], v[168:171], a[20:23], 0\n"
      "v_and_b32 v7, 48, v243\n"
      "v_lshl_or_b32 v7, v8, 10, v7\n"
      "v_add_u32 v7, s55, v7\n"
      "v_mfma_f32_16x16x32_bf16 v[88:91], v[168:171], a[24:27], 0\n"
      "v_mfma_f32_16x16x32_bf16 v[92:95], v[168:171], a[28:31], 0\n"
      "s_waitcnt lgkmcnt(0)\n"
      "s_barrier\n"
      "v_mfma_f32_16x16x32_bf16 v[72:75], v[172:175], a[40:43], v[72:75]\n"
      "ds_read2st64_b32 v[52:53], v244 offset0:12 offset1:13\n"
      "ds_read2st64_b32 v[54:55], v245 offset0:16 offset1:17\n"
      "v_mfma_f32_16x16x32_bf16 v[76:79], v[172:175], a[44:47], v[76:79]\n"
      "ds_read2st64_b32 v[56:57], v244 offset0:14 offset1:15\n"
      "ds_read2st64_b32 v[58:59], v245 offset0:18 offset1:19\n"
      "v_mfma_f32_16x16x32_bf16 v[80:83], v[172:175], a[48:51], v[80:83]\n"
      "ds_read2st64_b32 v[60:61], v246 offset0:20 offset1:21\n"
      "ds_read2st64_b32 v[62:63], v246 offset0:22 offset1:23\n"
      "v_mfma_f32_16x16x32_bf16 v[84:87], v[172:175], a[52:55], v[84:87]\n"
      "v_mfma_f32_16x16x32_bf16 v[88:91], v[172:175], a[56:59], v[88:91]\n"
      "v_mfma_f32_16x16x32_bf16 v[92:95], v[172:175], a[60:63], v[92:95]\n"
      "v_mfma_f32_16x16x32_bf16 v[72:75], v[176:179], a[72:75], v[72:75]\n"
      "v_mfma_f32_16x16x32_bf16 v[76:79], v[176:179], a[76:79], v[76:79]\n"
      "s_waitcnt lgkmcnt(0)\n"
      "v_mfma_f32_16x16x32_bf16 v[32:35], v[250:253], v[52:55], v[32:35]\n"
      "v_mfma_f32_16x16x32_bf16 v[36:39], v[250:253], v[56:59], v[36:39]\n"
      "v_mfma_f32_16x16x32_bf16 v[80:83], v[176:179], a[80:83], v[80:83]\n"
      "v_mfma_f32_16x16x32_bf16 v[32:35], v[252:255], v[60:63], v[32:35]\n"
      "v_mfma_f32_16x16x32_bf16 v[36:39], v[248:251], v[60:63], v[36:39]\n"
      "v_mov_b32 v43, s37\n"
      "v_mfma_f32_16x16x32_bf16 v[84:87], v[176:179], a[84:87], v[84:87]\n"
      "v_mov_b32 v44, v247\n"
      "v_mov_b32 v47, s37\n"
      "v_mfma_f32_16x16x32_bf16 v[88:91], v[176:179], a[88:91], v[88:91]\n"
      "v_add_u32 v4, 0, v242\n"
      "v_mfma_f32_16x16x32_bf16 v[92:95], v[176:179], a[92:95], v[92:95]\n"
      "global_load_dwordx4 v[12:15], v4, s[62:63] offset:0 sc1\n"
      "global_load_dwordx4 v[16:19], v4, s[62:63] offset:1024 sc1\n"
      "v_cvt_pk_bf16_f32 v40, v32, v33\n"
      "v_mfma_f32_16x16x32_bf16 v[72:75], v[180:183], a[104:107], v[72:75]\n"
      "global_load_dwordx4 v[20:23], v4, s[62:63] offset:2048 sc1\n"
      "v_cvt_pk_bf16_f32 v41, v34, v35\n"
      "v_cvt_pk_bf16_f32 v42, v36, v37\n"
      "v_mfma_f32_16x16x32_bf16 v[76:79], v[180:183], a[108:111], v[76:79]\n"
      "v_cvt_pk_bf16_f32 v45, v38, v39\n"
      "v_mfma_f32_16x16x32_bf16 v[80:83], v[180:183], a[112:115], v[80:83]\n"
      "global_store_dwordx4 v242, v[40:43], s[52:53] offset:1024 sc1\n"
      "global_store_dwordx4 v242, v[44:47], s[52:53] offset:2048 sc1\n"
      "v_mfma_f32_16x16x32_bf16 v[84:87], v[180:183], a[116:119], v[84:87]\n"
      "v_mfma_f32_16x16x32_bf16 v[88:91], v[180:183], a[120:123], v[88:91]\n"
      "v_mfma_f32_16x16x32_bf16 v[92:95], v[180:183], a[124:127], v[92:95]\n"
      "s_waitcnt vmcnt(21)\n"
      "v_mfma_f32_16x16x32_bf16 v[72:75], v[184:187], a[136:139], v[72:75]\n"
      "v_mfma_f32_16x16x32_bf16 v[76:79], v[184:187], a[140:143], v[76:79]\n"
      "v_mfma_f32_16x16x32_bf16 v[80:83], v[184:187], a[144:147], v[80:83]\n"
      "v_mfma_f32_16x16x32_bf16 v[84:87], v[184:187], a[148:151], v[84:87]\n"
      "v_mfma_f32_16x16x32_bf16 v[88:91], v[184:187], a[152:155], v[88:91]\n"
      "v_mfma_f32_16x16x32_bf16 v[92:95], v[184:187], a[156:159], v[92:95]\n"
      "v_mfma_f32_16x16x32_bf16 v[72:75], v[188:191], a[168:171], v[72:75]\n"
      "v_mfma_f32_16x16x32_bf16 v[76:79], v[188:191], a[172:175], v[76:79]\n"
      "v_mfma_f32_16x16x32_bf16 v[80:83], v[188:191], a[176:179], v[80:83]\n"
      "v_mfma_f32_16x16x32_bf16 v[84:87], v[188:191], a[180:183], v[84:87]\n"
      "v_mfma_f32_16x16x32_bf16 v[88:91], v[188:191], a[184:187], v[88:91]\n"
      "v_mfma_f32_16x16x32_bf16 v[92:95], v[188:191], a[188:191], v[92:95]\n"
      "s_waitcnt vmcnt(19)\n"
      "v_mfma_f32_16x16x32_bf16 v[72:75], v[128:131], a[200:203], v[72:75]\n"
      "v_mfma_f32_16x16x32_bf16 v[76:79], v[128:131], a[204:207], v[76:79]\n"
      "v_mfma_f32_16x16x32_bf16 v[80:83], v[128:131], a[208:211], v[80:83]\n"
      "v_mfma_f32_16x16x32_bf16 v[84:87], v[128:131], a[212:215], v[84:87]\n"
      "v_mfma_f32_16x16x32_bf16 v[88:91], v[128:131], a[216:219], v[88:91]\n"
      "v_mfma_f32_16x16x32_bf16 v[92:95], v[128:131], a[220:223], v[92:95]\n"
      "v_mfma_f32_16x16x32_bf16 v[72:75], v[132:135], a[232:235], v[72:75]\n"
      "v_mfma_f32_16x16x32_bf16 v[76:79], v[132:135], a[236:239], v[76:79]\n"
      "v_mfma_f32_16x16x32_bf16 v[80:83], v[132:135], a[240:243], v[80:83]\n"
      "v_mfma_f32_16x16x32_bf16 v[84:87], v[132:135], a[244:247], v[84:87]\n"
      "v_mfma_f32_16x16x32_bf16 v[88:91], v[132:135], a[248:251], v[88:91]\n"
      "s_nop 3\n"
      "v_cvt_pk_bf16_f32 v72, v72, v73\n"
      "v_mfma_f32_16x16x32_bf16 v[92:95], v[132:135], a[252:255], v[92:95]\n"
      "s_waitcnt vmcnt(17)\n"
      "v_cvt_pk_bf16_f32 v73, v74, v75\n"
      "v_mfma_f32_16x16x32_bf16 v[104:107], v[136:139], a[8:11], 0\n"
      "ds_write_addtid_b32 v72 offset:6144\n"
      "v_cvt_pk_bf16_f32 v74, v76, v77\n"
      "v_mfma_f32_16x16x32_bf16 v[108:111], v[136:139], a[12:15], 0\n"
      "ds_write_addtid_b32 v73 offset:6400\n"
      "v_cvt_pk_bf16_f32 v75, v78, v79\n"
      "v_mfma_f32_16x16x32_bf16 v[112:115], v[136:139], a[16:19], 0\n"
      "ds_write_addtid_b32 v74 offset:6656\n"
      "v_cvt_pk_bf16_f32 v76, v80, v81\n"
      "v_mfma_f32_16x16x32_bf16 v[116:119], v[136:139], a[20:23], 0\n"
      "ds_write_addtid_b32 v75 offset:6912\n"
      "v_cvt_pk_bf16_f32 v77, v82, v83\n"
      "v_mfma_f32_16x16x32_bf16 v[120:123], v[136:139], a[24:27], 0\n"
      "ds_write_addtid_b32 v76 offset:7168\n"
      "v_cvt_pk_bf16_f32 v78, v84, v85\n"
      "v_mfma_f32_16x16x32_bf16 v[124:127], v[136:139], a[28:31], 0\n"
      "ds_write_addtid_b32 v77 offset:7424\n"
      "v_cvt_pk_bf16_f32 v79, v86, v87\n"
      "v_mfma_f32_16x16x32_bf16 v[104:107], v[140:143], a[40:43], v[104:107]\n"
      "ds_write_addtid_b32 v78 offset:7680\n"
      "v_cvt_pk_bf16_f32 v80, v88, v89\n"
      "v_mfma_f32_16x16x32_bf16 v[108:111], v[140:143], a[44:47], v[108:111]\n"
      "ds_write_addtid_b32 v79 offset:7936\n"
      "v_cvt_pk_bf16_f32 v81, v90, v91\n"
      "v_mfma_f32_16x16x32_bf16 v[112:115], v[140:143], a[48:51], v[112:115]\n"
      "ds_write_addtid_b32 v80 offset:8192\n"
      "v_cvt_pk_bf16_f32 v82, v92, v93\n"
      "v_mfma_f32_16x16x32_bf16 v[116:119], v[140:143], a[52:55], v[116:119]\n"
      "ds_write_addtid_b32 v81 offset:8448\n"
      "v_cvt_pk_bf16_f32 v83, v94, v95\n"
      "v_mfma_f32_16x16x32_bf16 v[120:123], v[140:143], a[56:59], v[120:123]\n"
      "ds_write_addtid_b32 v82 offset:8704\n"
      "v_mfma_f32_16x16x32_bf16 v[124:127], v[140:143], a[60:63], v[124:127]\n"
      "s_waitcnt vmcnt(15)\n"
      "ds_write_addtid_b32 v83 offset:8960\n"
      "v_mfma_f32_16x16x32_bf16 v[104:107], v[144:147], a[72:75], v[104:107]\n"
      "s_waitcnt vmcnt(2)\n"
      "v_bitop3_b32 v22, v15, v19, s37 bitop3:0x7e\n"
      "v_bitop3_b32 v22, v22, v23, s37 bitop3:0xf6\n"
      "v_cmp_ne_u32 vcc, 0, v22\n"
      "s_nop 1\n"
      "s_cbranch_vccnz .Lp_s16_%=\n"
      ".Lp_o17_%=:\n"
      "v_mfma_f32_16x16x32_bf16 v[108:111], v[144:147], a[76:79], v[108:111]\n"
      "v_mov_b32 v15, v20\n"
      "v_mov_b32 v19, v21\n"
      "v_mfma_f32_16x16x32_bf16 v[112:115], v[144:147], a[80:83], v[112:115]\n"
      "s_waitcnt lgkmcnt(0)\n"
      "s_barrier\n"
      "v_mfma_f32_16x16x32_bf16 v[116:119], v[144:147], a[84:87], v[116:119]\n"
      "ds_read2st64_b32 v[84:85], v244 offset0:24 offset1:25\n"
      "v_mfma_f32_16x16x32_bf16 v[120:123], v[144:147], a[88:91], v[120:123]\n"
      "ds_read2st64_b32 v[86:87], v245 offset0:28 offset1:29\n"
      "v_mfma_f32_16x16x32_bf16 v[124:127], v[144:147], a[92:95], v[124:127]\n"
      "ds_read2st64_b32 v[88:89], v244 offset0:26 offset1:27\n"
      "v_mfma_f32_16x16x32_bf16 v[104:107], v[148:151], a[104:107], v[104:107]\n"
      "ds_read2st64_b32 v[90:91], v245 offset0:30 offset1:31\n"
      "v_add_u32 v5, 12288, v242\n"
      "v_mfma_f32_16x16x32_bf16 v[108:111], v[148:151], a[108:111], v[108:111]\n"
      "global_load_dwordx4 v[44:47], v5, s[62:63] offset:0 sc1\n"
      "v_mfma_f32_16x16x32_bf16 v[112:115], v[148:151], a[112:115], v[112:115]\n"
      "global_load_dwordx4 v[48:51], v5, s[62:63] offset:1024 sc1\n"
      "v_mfma_f32_16x16x32_bf16 v[116:119], v[148:151], a[116:119], v[116:119]\n"
      "global_load_dwordx4 v[52:55], v5, s[62:63] offset:2048 sc1\n"
      "v_mfma_f32_16x16x32_bf16 v[120:123], v[148:151], a[120:123], v[120:123]\n"
      "ds_read2st64_b32 v[92:93], v246 offset0:32 offset1:33\n"
      "v_mfma_f32_16x16x32_bf16 v[124:127], v[148:151], a[124:127], v[124:127]\n"
      "ds_read2st64_b32 v[94:95], v246 offset0:34 offset1:35\n"
      "v_mfma_f32_16x16x32_bf16 v[104:107], v[152:155], a[136:139], v[104:107]\n"
      "v_mfma_f32_16x16x32_bf16 v[64:67], v[252:255], v[12:15], 0\n"
      "v_mfma_f32_16x16x32_bf16 v[108:111], v[152:155], a[140:143], v[108:111]\n"
      "v_mfma_f32_16x16x32_bf16 v[68:71], v[248:251], v[12:15], 0\n"
      "v_mfma_f32_16x16x32_bf16 v[112:115], v[152:155], a[144:147], v[112:115]\n"
      "v_mfma_f32_16x16x32_bf16 v[96:99], v[252:255], v[16:19], 0\n"
      "v_mfma_f32_16x16x32_bf16 v[116:119], v[152:155], a[148:151], v[116:119]\n"
      "v_mfma_f32_16x16x32_bf16 v[100:103], v[248:251], v[16:19], 0\n"
      "v_mfma_f32_16x16x32_bf16 v[120:123], v[152:155], a[152:155], v[120:123]\n"
      "s_waitcnt lgkmcnt(0)\n"
      "v_mfma_f32_16x16x32_bf16 v[64:67], v[250:253], v[84:87], v[64:67]\n"
      "v_mfma_f32_16x16x32_bf16 v[124:127], v[152:155], a[156:159], v[124:127]\n"
      "v_mfma_f32_16x16x32_bf16 v[68:71], v[250:253], v[88:91], v[68:71]\n"
      "v_mfma_f32_16x16x32_bf16 v[104:107], v[156:159], a[168:171], v[104:107]\n"
      "v_mfma_f32_16x16x32_bf16 v[64:67], v[252:255], v[92:95], v[64:67]\n"
      "v_mfma_f32_16x16x32_bf16 v[108:111], v[156:159], a[172:175], v[108:111]\n"
      "v_mfma_f32_16x16x32_bf16 v[68:71], v[248:251], v[92:95], v[68:71]\n"
      "v_mfma_f32_16x16x32_bf16 v[112:115], v[156:159], a[176:179], v[112:115]\n"
      "v_mfma_f32_16x16x32_bf16 v[116:119], v[156:159], a[180:183], v[116:119]\n"
      "v_mfma_f32_16x16x32_bf16 v[120:123], v[156:159], a[184:187], v[120:123]\n"
      "v_mfma_f32_16x16x32_bf16 v[124:127], v[156:159], a[188:191], v[124:127]\n"
      "v_mfma_f32_16x16x32_bf16 v[104:107], v[192:195], a[200:203], v[104:107]\n"
      "v_mfma_f32_16x16x32_bf16 v[108:111], v[192:195], a[204:207], v[108:111]\n"
      "v_mfma_f32_16x16x32_bf16 v[112:115], v[192:195], a[208:211], v[112:115]\n"
      "v_mfma_f32_16x16x32_bf16 v[116:119], v[192:195], a[212:215], v[116:119]\n"
      "v_mfma_f32_16x16x32_bf16 v[120:123], v[192:195], a[216:219], v[120:123]\n"
      "v_mfma_f32_16x16x32_bf16 v[124:127], v[192:195], a[220:223], v[124:127]\n"
      "v_mfma_f32_16x16x32_bf16 v[104:107], v[196:199], a[232:235], v[104:107]\n"
      "v_mfma_f32_16x16x32_bf16 v[108:111], v[196:199], a[236:239], v[108:111]\n"
      "s_lshr_b32 s43, s42, 7\n"
      "s_cmp_eq_u32 s43, s36\n"
      "s_cbranch_scc0 .Lp_e18_%=\n"
      "s_lshl2_add_u32 s44, s35, s34\n"
      "s_lshl2_add_u32 s43, s44, s33\n"
      "s_lshl_b32 s43, s43, 12\n"
      "s_add_u32 s43, s43, 524288\n"
      "v_add_u32 v60, s43, v242\n"
      "global_load_dwordx4 v[24:27], v60, s[58:59] offset:0 sc1\n"
      "global_load_dwordx4 v[28:31], v60, s[58:59] offset:1024 sc1\n"
      "global_load_dwordx4 v[32:35], v60, s[58:59] offset:2048 sc1\n"
      "global_load_dwordx4 v[36:39], v60, s[58:59] offset:3072 sc1\n"
      "v_and_b32 v58, 15, v243\n"
      "v_lshrrev_b32 v59, 5, v243\n"
      "v_lshl_or_b32 v58, v59, 4, v58\n"
      "s_lshl4_add_u32 s44, s42, s44\n"
      "s_lshl_b32 s44, s44, 6\n"
      "v_lshl_add_u32 v59, v58, 1, s44\n"
      "global_load_ushort v57, v59, s[16:17]\n"
      "s_mov_b32 s44, 131072\n"
      "v_lshl_add_u32 v58, v58, 2, s44\n"
      "s_lshl_b32 s43, s33, 13\n"
      "v_add_u32 v59, s43, v58\n"
      ".Lp_e18_%=:\n"
      "v_mfma_f32_16x16x32_bf16 v[112:115], v[196:199], a[240:243], v[112:115]\n"
      "v_mfma_f32_16x16x32_bf16 v[116:119], v[196:199], a[244:247], v[116:119]\n"
      "v_mfma_f32_16x16x32_bf16 v[120:123], v[196:199], a[248:251], v[120:123]\n"
      "s_nop 0\n"
      "v_cvt_pk_bf16_f32 v104, v104, v105\n"
      "v_mfma_f32_16x16x32_bf16 v[124:127], v[196:199], a[252:255], v[124:127]\n"
      "v_cvt_pk_bf16_f32 v105, v106, v107\n"
      "v_mfma_f32_16x16x32_bf16 v[64:67], v[168:171], a[0:3], v[64:67]\n"
      "ds_write_addtid_b32 v104 offset:9216\n"
      "v_cvt_pk_bf16_f32 v106, v108, v109\n"
      "v_mfma_f32_16x16x32_bf16 v[68:71], v[168:171], a[4:7], v[68:71]\n"
      "ds_write_addtid_b32 v105 offset:9472\n"
      "v_cvt_pk_bf16_f32 v107, v110, v111\n"
      "v_mfma_f32_16x16x32_bf16 v[64:67], v[172:175], a[32:35], v[64:67]\n"
      "ds_write_addtid_b32 v106 offset:9728\n"
      "v_cvt_pk_bf16_f32 v108, v112, v113\n"
      "v_mfma_f32_16x16x32_bf16 v[68:71], v[172:175], a[36:39], v[68:71]\n"
      "ds_write_addtid_b32 v107 offset:9984\n"
      "v_cvt_pk_bf16_f32 v109, v114, v115\n"
      "v_mfma_f32_16x16x32_bf16 v[64:67], v[176:179], a[64:67], v[64:67]\n"
      "ds_write_addtid_b32 v108 offset:10240\n"
      "v_cvt_pk_bf16_f32 v110, v116, v117\n"
      "v_mfma_f32_16x16x32_bf16 v[68:71], v[176:179], a[68:71], v[68:71]\n"
      "ds_write_addtid_b32 v109 offset:10496\n"
      "v_cvt_pk_bf16_f32 v111, v118, v119\n"
      "v_mfma_f32_16x16x32_bf16 v[64:67], v[180:183], a[96:99], v[64:67]\n"
      "ds_write_addtid_b32 v110 offset:10752\n"
      "v_cvt_pk_bf16_f32 v112, v120, v121\n"
      "v_mfma_f32_16x16x32_bf16 v[68:71], v[180:183], a[100:103], v[68:71]\n"
      "ds_write_addtid_b32 v111 offset:11008\n"
      "v_cvt_pk_bf16_f32 v113, v122, v123\n"
      "v_mfma_f32_16x16x32_bf16 v[64:67], v[184:187], a[128:131], v[64:67]\n"
      "ds_write_addtid_b32 v112 offset:11264\n"
      "v_cvt_pk_bf16_f32 v114, v124, v125\n"
      "v_mfma_f32_16x16x32_bf16 v[68:71], v[184:187], a[132:135], v[68:71]\n"
      "ds_write_addtid_b32 v113 offset:11520\n"
      "v_cvt_pk_bf16_f32 v115, v126, v127\n"
      "v_mfma_f32_16x16x32_bf16 v[64:67], v[188:191], a[160:163], v[64:67]\n"
      "ds_write_addtid_b32 v114 offset:11776\n"
      "v_add_u32 v6, 24576, v242\n"
      "v_mfma_f32_16x16x32_bf16 v[68:71], v[188:191], a[164:167], v[68:71]\n"
      "global_load_dwordx4 v[72:75], v6, s[62:63] offset:0 sc1\n"
      "v_mfma_f32_16x16x32_bf16 v[64:67], v[128:131], a[192:195], v[64:67]\n"
      "global_load_dwordx4 v[76:79], v6, s[62:63] offset:1024 sc1\n"
      "v_mfma_f32_16x16x32_bf16 v[68:71], v[128:131], a[196:199], v[68:71]\n"
      "global_load_dwordx4 v[80:83], v6, s[62:63] offset:2048 sc1\n"
      "v_mfma_f32_16x16x32_bf16 v[64:67], v[132:135], a[224:227], v[64:67]\n"
      "ds_write_addtid_b32 v115 offset:12032\n"
      "v_mfma_f32_16x16x32_bf16 v[68:71], v[132:135], a[228:231], v[68:71]\n"
      "s_waitcnt vmcnt(3)\n"
      "v_bitop3_b32 v54, v47, v51, s37 bitop3:0x7e\n"
      "v_bitop3_b32 v54, v54, v55, s37 bitop3:0xf6\n"
      "v_cmp_ne_u32 vcc, 0, v54\n"
      "s_nop 1\n"
      "s_cbranch_vccnz .Lp_s20_%=\n"
      ".Lp_o21_%=:\n"
      "v_mfma_f32_16x16x32_bf16 v[96:99], v[136:139], a[0:3], v[96:99]\n"
      "v_mov_b32 v47, v52\n"
      "v_mov_b32 v51, v53\n"
      "v_mfma_f32_16x16x32_bf16 v[100:103], v[136:139], a[4:7], v[100:103]\n"
      "s_lshr_b32 s43, s42, 7\n"
      "s_cmp_eq_u32 s43, s36\n"
      "s_cbranch_scc0 .Lp_e22_%=\n"
      "s_waitcnt vmcnt(3)\n"
      "v_bitop3_b32 v61, v25, v27, s37 bitop3:0x7e\n"
      "v_bitop3_b32 v61, v61, v29, s37 bitop3:0xf6\n"
      "v_bitop3_b32 v61, v61, v31, s37 bitop3:0xf6\n"
      "v_bitop3_b32 v61, v61, v33, s37 bitop3:0xf6\n"
      "v_bitop3_b32 v61, v61, v35, s37 bitop3:0xf6\n"
      "v_bitop3_b32 v61, v61, v37, s37 bitop3:0xf6\n"
      "v_bitop3_b32 v61, v61, v39, s37 bitop3:0xf6\n"
      "v_cmp_ne_u32 vcc, 0, v61\n"
      "s_nop 1\n"
      "s_cbranch_vccnz .Lp_s24_%=\n"
      ".Lp_o25_%=:\n"
      "v_add_f32 v24, v24, v28\n"
      "v_add_f32 v26, v26, v30\n"
      "v_add_f32 v32, v32, v36\n"
      "v_add_f32 v34, v34, v38\n"
      "v_add_f32 v24, v24, v32\n"
      "v_add_f32 v26, v26, v34\n"
      "s_nop 1\n"
      "v_permlane32_swap_b32 v24, v26\n"
      "v_add_f32 v24, v24, v26\n"
      "v_mov_b32 v26, v24\n"
      "s_nop 1\n"
      "v_permlane16_swap_b32 v24, v26\n"
      "v_add_f32 v24, v24, v26\n"
      "ds_write_b32 v59, v24\n"
      "s_waitcnt lgkmcnt(0)\n"
      ".Lp_e22_%=:\n"
      "v_mfma_f32_16x16x32_bf16 v[96:99], v[140:143], a[32:35], v[96:99]\n"
      "s_waitcnt lgkmcnt(0)\n"
      "s_barrier\n"
      "v_mfma_f32_16x16x32_bf16 v[100:103], v[140:143], a[36:39], v[100:103]\n"
      "s_lshr_b32 s43, s42, 5\n"
      "s_lshl2_add_u32 s44, s36, s33\n"
      "s_cmp_eq_u32 s43, s44\n"
      "s_cbranch_scc0 .Lp_e26_%=\n"
      "ds_read_b32 v24, v58 offset:0\n"
      "ds_read_b32 v25, v58 offset:8192\n"
      "ds_read_b32 v26, v58 offset:16384\n"
      "ds_read_b32 v27, v58 offset:24576\n"
      "v_mov_b32 v62, 0\n"
      "v_mov_b32 v63, s37\n"
      "s_or_b32 s43, s34, s35\n"
      "s_cmp_eq_u32 s43, 0\n"
      "s_cbranch_scc0 .Lp_z27_%=\n"
      "s_mov_b64 exec, 1\n"
      "global_store_dword v62, v63, s[58:59] sc1\n"
      ".Lp_z27_%=:\n"
      "s_mov_b64 exec, -1\n"
      "s_waitcnt vmcnt(3)\n"
      "v_lshlrev_b32 v57, 16, v57\n"
      "v_and_b32 v56, 48, v243\n"
      "s_mov_b32 s44, 131072\n"
      "v_lshl_add_u32 v56, v56, 1, s44\n"
      "s_waitcnt lgkmcnt(0)\n"
      "v_add_f32 v24, v24, v25\n"
      "s_waitcnt lgkmcnt(0)\n"
      "v_add_f32 v26, v26, v27\n"
      "v_add_f32 v24, v24, v26\n"
      "v_cvt_pk_bf16_f32 v24, v24, v24\n"
      "v_and_b32 v24, 0xffff0000, v24\n"
      "v_mul_f32 v24, s84, v24\n"
      "v_cvt_pk_bf16_f32 v24, v24, v24\n"
      "v_and_b32 v24, 0xffff0000, v24\n"
      "v_add_f32 v24, v24, v57\n"
      "v_cvt_pk_bf16_f32 v24, v24, v24\n"
      "v_and_b32 v24, 0xffff0000, v24\n"
      "v_sub_f32 v24, v24, v57\n"
      "ds_write_b32 v58, v24 offset:1024\n"
      "ds_read_b128 v[28:31], v56 offset:1024\n"
      "ds_read_b128 v[32:35], v56 offset:1040\n"
      ".Lp_e26_%=:\n"
      "v_mfma_f32_16x16x32_bf16 v[96:99], v[144:147], a[64:67], v[96:99]\n"
      "ds_read2st64_b32 v[116:117], v244 offset0:36 offset1:37\n"
      "v_mfma_f32_16x16x32_bf16 v[100:103], v[144:147], a[68:71], v[100:103]\n"
      "ds_read2st64_b32 v[118:119], v245 offset0:40 offset1:41\n"
      "v_mfma_f32_16x16x32_bf16 v[96:99], v[148:151], a[96:99], v[96:99]\n"
      "ds_read2st64_b32 v[120:121], v244 offset0:38 offset1:39\n"
      "v_mfma_f32_16x16x32_bf16 v[100:103], v[148:151], a[100:103], v[100:103]\n"
      "ds_read2st64_b32 v[122:123], v245 offset0:42 offset1:43\n"
      "v_mfma_f32_16x16x32_bf16 v[96:99], v[152:155], a[128:131], v[96:99]\n"
      "ds_read2st64_b32 v[124:125], v246 offset0:44 offset1:45\n"
      "v_mfma_f32_16x16x32_bf16 v[100:103], v[152:155], a[132:135], v[100:103]\n"
      "ds_read2st64_b32 v[126:127], v246 offset0:46 offset1:47\n"
      "v_mfma_f32_16x16x32_bf16 v[96:99], v[156:159], a[160:163], v[96:99]\n"
      "v_mfma_f32_16x16x32_bf16 v[64:67], v[252:255], v[44:47], v[64:67]\n"
      "v_mfma_f32_16x16x32_bf16 v[100:103], v[156:159], a[164:167], v[100:103]\n"
      "v_mfma_f32_16x16x32_bf16 v[68:71], v[248:251], v[44:47], v[68:71]\n"
      "v_mfma_f32_16x16x32_bf16 v[96:99], v[192:195], a[192:195], v[96:99]\n"
      "v_mfma_f32_16x16x32_bf16 v[96:99], v[252:255], v[48:51], v[96:99]\n"
      "v_mfma_f32_16x16x32_bf16 v[100:103], v[192:195], a[196:199], v[100:103]\n"
      "v_mfma_f32_16x16x32_bf16 v[100:103], v[248:251], v[48:51], v[100:103]\n"
      "v_mfma_f32_16x16x32_bf16 v[96:99], v[196:199], a[224:227], v[96:99]\n"
      "s_waitcnt lgkmcnt(0)\n"
      "v_mfma_f32_16x16x32_bf16 v[96:99], v[250:253], v[116:119], v[96:99]\n"
      "v_mfma_f32_16x16x32_bf16 v[100:103], v[196:199], a[228:231], v[100:103]\n"
      "v_mfma_f32_16x16x32_bf16 v[100:103], v[250:253], v[120:123], v[100:103]\n"
      "v_mfma_f32_16x16x32_bf16 v[96:99], v[252:255], v[124:127], v[96:99]\n"
      "v_mfma_f32_16x16x32_bf16 v[100:103], v[248:251], v[124:127], v[100:103]\n"
      "s_lshr_b32 s43, s42, 5\n"
      "s_lshl2_add_u32 s44, s36, s33\n"
      "s_cmp_eq_u32 s43, s44\n"
      "s_cbranch_scc0 .Lp_e28_%=\n"
      "v_lshrrev_b32 v24, 10, v7\n"
      "v_subrev_u32 v24, s42, v24\n"
      "v_cmp_eq_u32 vcc, 0, v24\n"
      "s_nop 1\n"
      "s_mov_b64 exec, vcc\n"
      "v_fma_f32 v208, v205, v28, v208\n"
      "v_fma_f32 v209, v205, v29, v209\n"
      "v_fma_f32 v210, v205, v30, v210\n"
      "v_fma_f32 v211, v205, v31, v211\n"
      "v_fma_f32 v216, v205, v32, v216\n"
      "v_fma_f32 v217, v205, v33, v217\n"
      "v_fma_f32 v218, v205, v34, v218\n"
      "v_fma_f32 v219, v205, v35, v219\n"
      "s_mov_b64 exec, -1\n"
      "v_cmp_eq_u32 vcc, -16, v24\n"
      "s_nop 1\n"
      "s_mov_b64 exec, vcc\n"
      "v_fma_f32 v212, v207, v28, v212\n"
      "v_fma_f32 v213, v207, v29, v213\n"
      "v_fma_f32 v214, v207, v30, v214\n"
      "v_fma_f32 v215, v207, v31, v215\n"
      "v_fma_f32 v220, v207, v32, v220\n"
      "v_fma_f32 v221, v207, v33, v221\n"
      "v_fma_f32 v222, v207, v34, v222\n"
      "v_fma_f32 v223, v207, v35, v223\n"
      "s_mov_b64 exec, -1\n"
      ".Lp_e28_%=:\n"
      "s_waitcnt vmcnt(0)\n"
      "v_bitop3_b32 v82, v75, v79, s37 bitop3:0x7e\n"
      "v_bitop3_b32 v82, v82, v83, s37 bitop3:0xf6\n"
      "v_cmp_ne_u32 vcc, 0, v82\n"
      "s_nop 1\n"
      "s_cbranch_vccnz .Lp_s30_%=\n"
      ".Lp_o31_%=:\n"
      "v_mov_b32 v75, v80\n"
      "v_mov_b32 v79, v81\n"
      "s_nop 0\n"
      "v_mfma_f32_16x16x32_bf16 v[64:67], v[252:255], v[72:75], v[64:67]\n"
      "v_mfma_f32_16x16x32_bf16 v[96:99], v[252:255], v[76:79], v[96:99]\n"
      "v_mfma_f32_16x16x32_bf16 v[68:71], v[248:251], v[72:75], v[68:71]\n"
      "v_mfma_f32_16x16x32_bf16 v[100:103], v[248:251], v[76:79], v[100:103]\n"
      "s_nop 4\n"
      "v_fma_f32 v208, v204, v64, v208\n"
      "v_fma_f32 v209, v204, v65, v209\n"
      "v_fma_f32 v210, v204, v66, v210\n"
      "v_fma_f32 v211, v204, v67, v211\n"
      "v_fma_f32 v216, v204, v96, v216\n"
      "v_fma_f32 v217, v204, v97, v217\n"
      "v_fma_f32 v218, v204, v98, v218\n"
      "v_fma_f32 v219, v204, v99, v219\n"
      "v_cvt_pk_bf16_f32 v104, v208, v209\n"
      "v_cvt_pk_bf16_f32 v105, v210, v211\n"
      "v_cvt_pk_bf16_f32 v106, v216, v217\n"
      "v_cvt_pk_bf16_f32 v107, v218, v219\n"
      "global_store_dwordx4 v7, v[104:107], s[20:21]\n"
      "v_fma_f32 v212, v206, v68, v212\n"
      "v_fma_f32 v213, v206, v69, v213\n"
      "v_fma_f32 v214, v206, v70, v214\n"
      "v_fma_f32 v215, v206, v71, v215\n"
      "v_fma_f32 v220, v206, v100, v220\n"
      "v_fma_f32 v221, v206, v101, v221\n"
      "v_fma_f32 v222, v206, v102, v222\n"
      "v_fma_f32 v223, v206, v103, v223\n"
      "v_cvt_pk_bf16_f32 v108, v212, v213\n"
      "v_cvt_pk_bf16_f32 v109, v214, v215\n"
      "v_cvt_pk_bf16_f32 v110, v220, v221\n"
      "v_cvt_pk_bf16_f32 v111, v222, v223\n"
      "global_store_dwordx4 v7, v[108:111], s[68:69]\n"
      "s_endpgm\n"
      ".Lp_s2_%=:\n"
      "s_cmp_eq_u32 s38, 0\n"
      "s_cbranch_scc1 .Lp_o3_%=\n"
      "s_sub_u32 s38, s38, 1\n"
      "s_sleep 4\n"
      "s_nop 7\n"
      "global_load_dword v252, v253, s[58:59] offset:0 sc1\n"
      "s_waitcnt vmcnt(0)\n"
      "v_cmp_ne_u32 vcc, s37, v252\n"
      "s_nop 1\n"
      "s_cbranch_vccnz .Lp_s2_%=\n"
      "s_nop 7\n"
      "s_branch .Lp_o3_%=\n"
      ".Lp_s5_%=:\n"
      "s_cmp_eq_u32 s38, 0\n"
      "s_cbranch_scc1 .Lp_o6_%=\n"
      "s_sub_u32 s38, s38, 1\n"
      "s_sleep 4\n"
      "s_nop 7\n"
      "global_load_dword v252, v253, s[58:59] offset:128 sc1\n"
      "s_waitcnt vmcnt(0)\n"
      "v_cmp_ne_u32 vcc, s37, v252\n"
      "s_nop 1\n"
      "s_cbranch_vccnz .Lp_s5_%=\n"
      "s_nop 7\n"
      "s_branch .Lp_o6_%=\n"
      ".Lp_s8_%=:\n"
      "s_cmp_eq_u32 s38, 0\n"
      "s_cbranch_scc1 .Lp_o9_%=\n"
      "s_sub_u32 s38, s38, 1\n"
      "s_sleep 2\n"
      "s_nop 7\n"
      "global_load_dwordx2 v[202:203], v200, s[58:59] sc1\n"
      "s_waitcnt vmcnt(0)\n"
      "v_cmp_ne_u32 vcc, s37, v203\n"
      "s_nop 1\n"
      "s_cbranch_vccnz .Lp_s8_%=\n"
      "s_nop 7\n"
      "s_branch .Lp_o9_%=\n"
      ".Lp_s13_%=:\n"
      "s_cmp_eq_u32 s38, 0\n"
      "s_cbranch_scc1 .Lp_o14_%=\n"
      "s_sub_u32 s38, s38, 1\n"
      "s_sleep 1\n"
      "s_nop 7\n"
      "global_load_dwordx2 v[204:205], v209, s[58:59] offset:0 sc1\n"
      "global_load_dwordx2 v[206:207], v209, s[58:59] offset:256 sc1\n"
      "s_waitcnt vmcnt(0)\n"
      "v_bitop3_b32 v210, v205, v207, s37 bitop3:0x7e\n"
      "v_cmp_ne_u32 vcc, 0, v210\n"
      "s_nop 1\n"
      "s_cbranch_vccnz .Lp_s13_%=\n"
      "s_nop 7\n"
      "s_branch .Lp_o14_%=\n"
      ".Lp_s16_%=:\n"
      "s_cmp_eq_u32 s38, 0\n"
      "s_cbranch_scc1 .Lp_o17_%=\n"
      "s_sub_u32 s38, s38, 1\n"
      "s_nop 7\n"
      "global_load_dwordx4 v[12:15], v4, s[62:63] offset:0 sc1\n"
      "global_load_dwordx4 v[16:19], v4, s[62:63] offset:1024 sc1\n"
      "global_load_dwordx4 v[20:23], v4, s[62:63] offset:2048 sc1\n"
      "s_waitcnt vmcnt(0)\n"
      "v_bitop3_b32 v22, v15, v19, s37 bitop3:0x7e\n"
      "v_bitop3_b32 v22, v22, v23, s37 bitop3:0xf6\n"
      "v_cmp_ne_u32 vcc, 0, v22\n"
      "s_nop 1\n"
      "s_cbranch_vccnz .Lp_s16_%=\n"
      "s_nop 7\n"
      "s_branch .Lp_o17_%=\n"
      ".Lp_s20_%=:\n"
      "s_cmp_eq_u32 s38, 0\n"
      "s_cbranch_scc1 .Lp_o21_%=\n"
      "s_sub_u32 s38, s38, 1\n"
      "s_nop 7\n"
      "global_load_dwordx4 v[44:47], v5, s[62:63] offset:0 sc1\n"
      "global_load_dwordx4 v[48:51], v5, s[62:63] offset:1024 sc1\n"
      "global_load_dwordx4 v[52:55], v5, s[62:63] offset:2048 sc1\n"
      "s_waitcnt vmcnt(0)\n"
      "v_bitop3_b32 v54, v47, v51, s37 bitop3:0x7e\n"
      "v_bitop3_b32 v54, v54, v55, s37 bitop3:0xf6\n"
      "v_cmp_ne_u32 vcc, 0, v54\n"
      "s_nop 1\n"
      "s_cbranch_vccnz .Lp_s20_%=\n"
      "s_nop 7\n"
      "s_branch .Lp_o21_%=\n"
      ".Lp_s24_%=:\n"
      "s_cmp_eq_u32 s38, 0\n"
      "s_cbranch_scc1 .Lp_o25_%=\n"
      "s_sub_u32 s38, s38, 1\n"
      "s_sleep 2\n"
      "s_nop 7\n"
      "global_load_dwordx4 v[24:27], v60, s[58:59] offset:0 sc1\n"
      "global_load_dwordx4 v[28:31], v60, s[58:59] offset:1024 sc1\n"
      "global_load_dwordx4 v[32:35], v60, s[58:59] offset:2048 sc1\n"
      "global_load_dwordx4 v[36:39], v60, s[58:59] offset:3072 sc1\n"
      "s_waitcnt vmcnt(0)\n"
      "v_bitop3_b32 v61, v25, v27, s37 bitop3:0x7e\n"
      "v_bitop3_b32 v61, v61, v29, s37 bitop3:0xf6\n"
      "v_bitop3_b32 v61, v61, v31, s37 bitop3:0xf6\n"
      "v_bitop3_b32 v61, v61, v33, s37 bitop3:0xf6\n"
      "v_bitop3_b32 v61, v61, v35, s37 bitop3:0xf6\n"
      "v_bitop3_b32 v61, v61, v37, s37 bitop3:0xf6\n"
      "v_bitop3_b32 v61, v61, v39, s37 bitop3:0xf6\n"
      "v_cmp_ne_u32 vcc, 0, v61\n"
      "s_nop 1\n"
      "s_cbranch_vccnz .Lp_s24_%=\n"
      "s_nop 7\n"
      "s_branch .Lp_o25_%=\n"
      ".Lp_s30_%=:\n"
      "s_cmp_eq_u32 s38, 0\n"
      "s_cbranch_scc1 .Lp_o31_%=\n"
      "s_sub_u32 s38, s38, 1\n"
      "s_nop 7\n"
      "global_load_dwordx4 v[72:75], v6, s[62:63] offset:0 sc1\n"
      "global_load_dwordx4 v[76:79], v6, s[62:63] offset:1024 sc1\n"
      "global_load_dwordx4 v[80:83], v6, s[62:63] offset:2048 sc1\n"
      "s_waitcnt vmcnt(0)\n"
      "v_bitop3_b32 v82, v75, v79, s37 bitop3:0x7e\n"
      "v_bitop3_b32 v82, v82, v83, s37 bitop3:0xf6\n"
      "v_cmp_ne_u32 vcc, 0, v82\n"
      "s_nop 1\n"
      "s_cbranch_vccnz .Lp_s30_%=\n"
      "s_nop 7\n"
      "s_branch .Lp_o31_%=\n"
      ".Ls_s3_%=:\n"
      "s_cmp_eq_u32 s38, 0\n"
      "s_cbranch_scc1 .Ls_o4_%=\n"
      "s_sub_u32 s38, s38, 1\n"
      "s_sleep 4\n"
      "s_nop 7\n"
      "global_load_dword v252, v253, s[58:59] offset:0 sc1\n"
      "s_waitcnt vmcnt(0)\n"
      "v_cmp_ne_u32 vcc, s37, v252\n"
      "s_nop 1\n"
      "s_cbranch_vccnz .Ls_s3_%=\n"
      "s_nop 7\n"
      "s_branch .Ls_o4_%=\n"
      ".Ls_s6_%=:\n"
      "s_cmp_eq_u32 s38, 0\n"
      "s_cbranch_scc1 .Ls_o7_%=\n"
      "s_sub_u32 s38, s38, 1\n"
      "s_sleep 2\n"
      "s_nop 7\n"
      "global_load_dwordx2 v[206:207], v204, s[58:59] sc1\n"
      "s_waitcnt vmcnt(0)\n"
      "v_cmp_ne_u32 vcc, s37, v207\n"
      "s_nop 1\n"
      "s_cbranch_vccnz .Ls_s6_%=\n"
      "s_nop 7\n"
      "s_branch .Ls_o7_%=\n"
      ".Ls_s10_%=:\n"
      "s_cmp_eq_u32 s38, 0\n"
      "s_cbranch_scc1 .Ls_o11_%=\n"
      "s_sub_u32 s38, s38, 1\n"
      "s_sleep 4\n"
      "s_nop 7\n"
      "global_load_dword v252, v253, s[58:59] offset:128 sc1\n"
      "s_waitcnt vmcnt(0)\n"
      "v_cmp_ne_u32 vcc, s37, v252\n"
      "s_nop 1\n"
      "s_cbranch_vccnz .Ls_s10_%=\n"
      "s_nop 7\n"
      "s_branch .Ls_o11_%=\n"
      ".Ls_s13_%=:\n"
      "s_cmp_eq_u32 s38, 0\n"
      "s_cbranch_scc1 .Ls_o14_%=\n"
      "s_sub_u32 s38, s38, 1\n"
      "s_sleep 1\n"
      "s_nop 7\n"
      "global_load_dwordx2 v[204:205], v209, s[58:59] offset:0 sc1\n"
      "global_load_dwordx2 v[206:207], v209, s[58:59] offset:256 sc1\n"
      "s_waitcnt vmcnt(0)\n"
      "v_bitop3_b32 v210, v205, v207, s37 bitop3:0x7e\n"
      "v_cmp_ne_u32 vcc, 0, v210\n"
      "s_nop 1\n"
      "s_cbranch_vccnz .Ls_s13_%=\n"
      "s_nop 7\n"
      "s_branch .Ls_o14_%=\n"
      ".Ls_s16_%=:\n"
      "s_cmp_eq_u32 s38, 0\n"
      "s_cbranch_scc1 .Ls_o17_%=\n"
      "s_sub_u32 s38, s38, 1\n"
      "s_sleep 8\n"
      "s_nop 7\n"
      "global_load_dwordx4 v[224:227], v200, s[58:59] offset:0 sc1\n"
      "global_load_dwordx4 v[228:231], v200, s[58:59] offset:1024 sc1\n"
      "global_load_dwordx4 v[232:235], v200, s[58:59] offset:2048 sc1\n"
      "global_load_dwordx4 v[236:239], v200, s[58:59] offset:3072 sc1\n"
      "s_waitcnt vmcnt(0)\n"
      "v_bitop3_b32 v192, v225, v227, s37 bitop3:0x7e\n"
      "v_bitop3_b32 v192, v192, v229, s37 bitop3:0xf6\n"
      "v_bitop3_b32 v192, v192, v231, s37 bitop3:0xf6\n"
      "v_bitop3_b32 v192, v192, v233, s37 bitop3:0xf6\n"
      "v_bitop3_b32 v192, v192, v235, s37 bitop3:0xf6\n"
      "v_bitop3_b32 v192, v192, v237, s37 bitop3:0xf6\n"
      "v_bitop3_b32 v192, v192, v239, s37 bitop3:0xf6\n"
      "v_cmp_ne_u32 vcc, 0, v192\n"
      "s_nop 1\n"
      "s_cbranch_vccnz .Ls_s16_%=\n"
      "s_nop 7\n"
      "s_branch .Ls_o17_%=\n"
      ".Ls_s19_%=:\n"
      "s_cmp_eq_u32 s38, 0\n"
      "s_cbranch_scc1 .Ls_o20_%=\n"
      "s_sub_u32 s38, s38, 1\n"
      "s_nop 7\n"
      "global_load_dwordx4 v[12:15], v4, s[62:63] offset:0 sc1\n"
      "global_load_dwordx4 v[16:19], v4, s[62:63] offset:1024 sc1\n"
      "global_load_dwordx4 v[20:23], v4, s[62:63] offset:2048 sc1\n"
      "s_waitcnt vmcnt(0)\n"
      "v_bitop3_b32 v22, v15, v19, s37 bitop3:0x7e\n"
      "v_bitop3_b32 v22, v22, v23, s37 bitop3:0xf6\n"
      "v_cmp_ne_u32 vcc, 0, v22\n"
      "s_nop 1\n"
      "s_cbranch_vccnz .Ls_s19_%=\n"
      "s_nop 7\n"
      "s_branch .Ls_o20_%=\n"
      ".Ls_s23_%=:\n"
      "s_cmp_eq_u32 s38, 0\n"
      "s_cbranch_scc1 .Ls_o24_%=\n"
      "s_sub_u32 s38, s38, 1\n"
      "s_nop 7\n"
      "global_load_dwordx4 v[44:47], v5, s[62:63] offset:0 sc1\n"
      "global_load_dwordx4 v[48:51], v5, s[62:63] offset:1024 sc1\n"
      "global_load_dwordx4 v[52:55], v5, s[62:63] offset:2048 sc1\n"
      "s_waitcnt vmcnt(0)\n"
      "v_bitop3_b32 v54, v47, v51, s37 bitop3:0x7e\n"
      "v_bitop3_b32 v54, v54, v55, s37 bitop3:0xf6\n"
      "v_cmp_ne_u32 vcc, 0, v54\n"
      "s_nop 1\n"
      "s_cbranch_vccnz .Ls_s23_%=\n"
      "s_nop 7\n"
      "s_branch .Ls_o24_%=\n"
      ".Ls_s27_%=:\n"
      "s_cmp_eq_u32 s38, 0\n"
      "s_cbranch_scc1 .Ls_o28_%=\n"
      "s_sub_u32 s38, s38, 1\n"
      "s_sleep 2\n"
      "s_nop 7\n"
      "global_load_dwordx4 v[24:27], v60, s[58:59] offset:0 sc1\n"
      "global_load_dwordx4 v[28:31], v60, s[58:59] offset:1024 sc1\n"
      "global_load_dwordx4 v[32:35], v60, s[58:59] offset:2048 sc1\n"
      "global_load_dwordx4 v[36:39], v60, s[58:59] offset:3072 sc1\n"
      "s_waitcnt vmcnt(0)\n"
      "v_bitop3_b32 v61, v25, v27, s37 bitop3:0x7e\n"
      "v_bitop3_b32 v61, v61, v29, s37 bitop3:0xf6\n"
      "v_bitop3_b32 v61, v61, v31, s37 bitop3:0xf6\n"
      "v_bitop3_b32 v61, v61, v33, s37 bitop3:0xf6\n"
      "v_bitop3_b32 v61, v61, v35, s37 bitop3:0xf6\n"
      "v_bitop3_b32 v61, v61, v37, s37 bitop3:0xf6\n"
      "v_bitop3_b32 v61, v61, v39, s37 bitop3:0xf6\n"
      "v_cmp_ne_u32 vcc, 0, v61\n"
      "s_nop 1\n"
      "s_cbranch_vccnz .Ls_s27_%=\n"
      "s_nop 7\n"
      "s_branch .Ls_o28_%=\n"
      ".Ls_s33_%=:\n"
      "s_cmp_eq_u32 s38, 0\n"
      "s_cbranch_scc1 .Ls_o34_%=\n"
      "s_sub_u32 s38, s38, 1\n"
      "s_nop 7\n"
      "global_load_dwordx4 v[72:75], v6, s[62:63] offset:0 sc1\n"
      "global_load_dwordx4 v[76:79], v6, s[62:63] offset:1024 sc1\n"
      "global_load_dwordx4 v[80:83], v6, s[62:63] offset:2048 sc1\n"
      "s_waitcnt vmcnt(0)\n"
      "v_bitop3_b32 v82, v75, v79, s37 bitop3:0x7e\n"
      "v_bitop3_b32 v82, v82, v83, s37 bitop3:0xf6\n"
      "v_cmp_ne_u32 vcc, 0, v82\n"
      "s_nop 1\n"
      "s_cbranch_vccnz .Ls_s33_%=\n"
      "s_nop 7\n"
      "s_branch .Ls_o34_%=\n"
      ".Lend_%=:\n"
      :
      : "s"(__builtin_amdgcn_kernarg_segment_ptr()), "s"(blockIdx.x), "s"(__builtin_amdgcn_readfirstlane(threadIdx.x >> 6)),
        "s"((unsigned)(size_t)smem)
      : "memory", "scc", "vcc",
        "v0", "v1", "v2", "v3", "v4", "v5", "v6", "v7", "v8", "v9", "v10", "v11", "v12", "v13", "v14", "v15", "v16", "v17", "v18", "v19", "v20", "v21", "v22", "v23", "v24", "v25", "v26", "v27", "v28", "v29", "v30", "v31", "v32", "v33", "v34", "v35", "v36", "v37", "v38", "v39", "v40", "v41", "v42", "v43", "v44", "v45", "v46", "v47", "v48", "v49", "v50", "v51", "v52", "v53", "v54", "v55", "v56", "v57", "v58", "v59", "v60", "v61", "v62", "v63", "v64", "v65", "v66", "v67", "v68", "v69", "v70", "v71", "v72", "v73", "v74", "v75", "v76", "v77", "v78", "v79", "v80", "v81", "v82", "v83", "v84", "v85", "v86", "v87", "v88", "v89", "v90", "v91", "v92", "v93", "v94", "v95", "v96", "v97", "v98", "v99", "v100", "v101", "v102", "v103", "v104", "v105", "v106", "v107", "v108", "v109", "v110", "v111", "v112", "v113", "v114", "v115", "v116", "v117", "v118", "v119", "v120", "v121", "v122", "v123", "v124", "v125", "v126", "v127", "v128", "v129", "v130", "v131", "v132", "v133", "v134", "v135", "v136", "v137", "v138", "v139", "v140", "v141", "v142", "v143", "v144", "v145", "v146", "v147", "v148", "v149", "v150", "v151", "v152", "v153", "v154", "v155", "v156", "v157", "v158", "v159", "v160", "v161", "v162", "v163", "v164", "v165", "v166", "v167", "v168", "v169", "v170", "v171", "v172", "v173", "v174", "v175", "v176", "v177", "v178", "v179", "v180", "v181", "v182", "v183", "v184", "v185", "v186", "v187", "v188", "v189", "v190", "v191", "v192", "v193", "v194", "v195", "v196", "v197", "v198", "v199", "v200", "v201", "v202", "v203", "v204", "v205", "v206", "v207", "v208", "v209", "v210", "v211", "v212", "v213", "v214", "v215", "v216", "v217", "v218", "v219", "v220", "v221", "v222", "v223", "v224", "v225", "v226", "v227", "v228", "v229", "v230", "v231", "v232", "v233", "v234", "v235", "v236", "v237", "v238", "v239", "v240", "v241", "v242", "v243", "v244", "v245", "v246", "v247", "v248", "v249", "v250", "v251", "v252", "v253", "v254", "v255",
        "a0", "a1", "a2", "a3", "a4", "a5", "a6", "a7", "a8", "a9", "a10", "a11", "a12", "a13", "a14", "a15", "a16", "a17", "a18", "a19", "a20", "a21", "a22", "a23", "a24", "a25", "a26", "a27", "a28", "a29", "a30", "a31", "a32", "a33", "a34", "a35", "a36", "a37", "a38", "a39", "a40", "a41", "a42", "a43", "a44", "a45", "a46", "a47", "a48", "a49", "a50", "a51", "a52", "a53", "a54", "a55", "a56", "a57", "a58", "a59", "a60", "a61", "a62", "a63", "a64", "a65", "a66", "a67", "a68", "a69", "a70", "a71", "a72", "a73", "a74", "a75", "a76", "a77", "a78", "a79", "a80", "a81", "a82", "a83", "a84", "a85", "a86", "a87", "a88", "a89", "a90", "a91", "a92", "a93", "a94", "a95", "a96", "a97", "a98", "a99", "a100", "a101", "a102", "a103", "a104", "a105", "a106", "a107", "a108", "a109", "a110", "a111", "a112", "a113", "a114", "a115", "a116", "a117", "a118", "a119", "a120", "a121", "a122", "a123", "a124", "a125", "a126", "a127", "a128", "a129", "a130", "a131", "a132", "a133", "a134", "a135", "a136", "a137", "a138", "a139", "a140", "a141", "a142", "a143", "a144", "a145", "a146", "a147", "a148", "a149", "a150", "a151", "a152", "a153", "a154", "a155", "a156", "a157", "a158", "a159", "a160", "a161", "a162", "a163", "a164", "a165", "a166", "a167", "a168", "a169", "a170", "a171", "a172", "a173", "a174", "a175", "a176", "a177", "a178", "a179", "a180", "a181", "a182", "a183", "a184", "a185", "a186", "a187", "a188", "a189", "a190", "a191", "a192", "a193", "a194", "a195", "a196", "a197", "a198", "a199", "a200", "a201", "a202", "a203", "a204", "a205", "a206", "a207", "a208", "a209", "a210", "a211", "a212", "a213", "a214", "a215", "a216", "a217", "a218", "a219", "a220", "a221", "a222", "a223", "a224", "a225", "a226", "a227", "a228", "a229", "a230", "a231", "a232", "a233", "a234", "a235", "a236", "a237", "a238", "a239", "a240", "a241", "a242", "a243", "a244", "a245", "a246", "a247", "a248", "a249", "a250", "a251", "a252", "a253", "a254", "a255",
        "s8", "s9", "s10", "s11", "s12", "s13", "s14", "s15", "s16", "s17", "s18", "s19", "s20", "s21", "s22", "s23", "s24", "s25", "s26", "s27", "s28", "s29", "s30", "s31", "s32", "s33", "s34", "s35", "s36", "s37", "s38", "s39", "s40", "s41", "s42", "s43", "s44", "s45", "s46", "s47", "s48", "s49", "s50", "s51", "s52", "s53", "s54", "s55", "s56", "s57", "s58", "s59", "s60", "s61", "s62", "s63", "s64", "s65", "s66", "s67", "s68", "s69", "s70", "s71", "s72", "s73", "s74", "s75", "s76", "s77", "s78", "s79", "s80", "s81", "s82", "s83", "s84", "s85");
}

extern "C" void kernel_launch(void* const* d_in, const int* in_sizes, int n_in, void* d_out, int out_size, void* d_ws, size_t ws_size,
                              hipStream_t stream) {
  fused_k<<<256, 256, 0, stream>>>((const u16*)d_in[1], (const u16*)d_in[2], (const u16*)d_in[3], (const u16*)d_in[4], (const u16*)d_in[0],
                                   (char*)d_ws, (u16*)d_out, (const u16*)d_in[6], (const u16*)d_in[10], (const u16*)d_in[7],
                                   (const u16*)d_in[8], (const u16*)d_in[9]);
}
```

```cpp
#include <hip/hip_runtime.h>

typedef unsigned short u16;

__global__ __launch_bounds__(256) void fused_k(const u16* wsre, const u16* wsim, const u16* wdre, const u16* wdim, const u16* x, char* ws,
                                               u16* out, const u16* w_r, const u16* gate_w, const u16* w_gate, const u16* w_up,
                                               const u16* w_down) {
  __shared__ __attribute__((aligned(16))) char smem[163840];
  asm volatile(
      ".Lbegin_%=:\n"
      "s_load_dwordx16 s[8:23], %0, 0\n"
      "s_load_dwordx8 s[24:31], %0, 64\n"
      "v_mbcnt_lo_u32_b32 v243, -1, 0\n"
      "s_mov_b32 s32, %1\n"
      "v_mbcnt_hi_u32_b32 v243, -1, v243\n"
      "s_mov_b32 s33, %2\n"
      "s_bfe_u32 s34, s32, 0x20001\n"
      "s_bfe_u32 s35, s32, 0x20003\n"
      "v_lshrrev_b32 v236, 3, v243\n"
      "s_lshr_b32 s43, s32, 5\n"
      "v_xor_b32 v237, v243, v236\n"
      "s_and_b32 s36, s32, 1\n"
      "v_and_b32 v237, 7, v237\n"
      "s_lshl3_add_u32 s36, s36, s43\n"
      "v_lshlrev_b32 v237, 4, v237\n"
      "s_add_u32 s44, s43, s35\n"
      "v_lshlrev_b32 v242, 4, v243\n"
      "s_lshl_b32 s41, s33, 14\n"
      "s_bitcmp1_b32 s44, 0\n"
      "s_cbranch_scc1 .Lstream_%=\n"
      "v_and_b32 v238, 3, v236\n"
      "s_lshr_b32 s43, s43, 1\n"
      "v_and_b32 v239, 4, v236\n"
      "s_add_u32 s80, s43, s35\n"
      "v_lshl_or_b32 v238, v239, 1, v238\n"
      "s_lshl_b32 s80, s80, 8\n"
      "v_lshl_or_b32 v240, v238, 10, v237\n"
      "s_and_b32 s80, s80, 0x300\n"
      "v_add_u32 v241, 15360, v240\n"
      "s_lshr_b32 s83, s36, 1\n"
      "v_add_u32 v254, 2048, v240\n"
      "s_and_b32 s44, s34, 1\n"
      "v_add_u32 v255, 17408, v240\n"
      "s_lshl3_add_u32 s44, s44, s83\n"
      "s_lshl2_add_u32 s40, s44, s33\n"
      "s_lshl_b32 s44, s40, 15\n"
      "s_lshl_b32 s81, s33, 8\n"
      "s_add_u32 s81, s81, s80\n"
      "s_and_b32 s81, s81, 0x380\n"
      "s_lshl_b32 s45, s35, 17\n"
      "s_lshl_b32 s82, s33, 12\n"
      "s_add_u32 s76, s34, 1\n"
      "s_and_b32 s76, s76, 3\n"
      "s_lshl_b32 s76, s76, 15\n"
      "s_or_b32 s76, s76, s81\n"
      "s_add_u32 s76, s76, s45\n"
      "s_add_u32 s77, s34, 2\n"
      "s_and_b32 s77, s77, 3\n"
      "s_lshl_b32 s77, s77, 15\n"
      "s_or_b32 s77, s77, s81\n"
      "s_add_u32 s77, s77, s45\n"
      "s_add_u32 s78, s44, s80\n"
      "s_add_u32 s79, s80, 256\n"
      "s_and_b32 s79, s79, 0x300\n"
      "s_add_u32 s79, s79, s44\n"
      "s_add_u32 m0, s82, 65536\n"
      "s_bitcmp1_b32 s34, 1\n"
      "s_waitcnt lgkmcnt(0)\n"
      "s_cselect_b64 s[12:13], s[14:15], s[12:13]\n"
      "s_add_u32 s46, s12, s76\n"
      "s_addc_u32 s47, s13, 0\n"
      "global_load_lds_dwordx4 v240, s[46:47] offset:0\n"
      "global_load_lds_dwordx4 v241, s[46:47] offset:1024\n"
      "global_load_lds_dwordx4 v254, s[46:47] offset:2048\n"
      "global_load_lds_dwordx4 v255, s[46:47] offset:3072\n"
      "s_add_u32 s70, s16, s78\n"
      "s_addc_u32 s71, s17, 0\n"
      "s_mov_b32 m0, s41\n"
      "s_add_u32 s46, s12, s77\n"
      "s_addc_u32 s47, s13, 0\n"
      "global_load_lds_dwordx4 v240, s[70:71] offset:0\n"
      "global_load_lds_dwordx4 v241, s[70:71] offset:1024\n"
      "global_load_lds_dwordx4 v254, s[70:71] offset:2048\n"
      "global_load_lds_dwordx4 v255, s[70:71] offset:3072\n"
      "s_add_u32 m0, s82, 98304\n"
      "s_nop 0\n"
      "global_load_lds_dwordx4 v240, s[46:47] offset:0\n"
      "global_load_lds_dwordx4 v241, s[46:47] offset:1024\n"
      "s_add_u32 s70, s16, s79\n"
      "s_addc_u32 s71, s17, 0\n"
      "s_add_u32 m0, s41, 4096\n"
      "s_nop 0\n"
      "global_load_lds_dwordx4 v240, s[70:71] offset:0\n"
      "global_load_lds_dwordx4 v241, s[70:71] offset:1024\n"
      "global_load_lds_dwordx4 v254, s[70:71] offset:2048\n"
      "global_load_lds_dwordx4 v255, s[70:71] offset:3072\n"
      "s_branch .Lprod_%=\n"
      ".Lstream_%=:\n"
      "v_lshl_or_b32 v244, v236, 12, v237\n"
      "v_add_u32 v245, 31744, v244\n"
      "v_add_u32 v246, 63488, v244\n"
      "v_add_u32 v247, 95232, v244\n"
      "s_getpc_b64 s[46:47]\n"
      ".Lhere_%=:\n"
      "s_lshr_b32 s44, s32, 4\n"
      "s_lshl2_add_u32 s44, s44, s33\n"
      "s_lshl_b32 s44, s44, 10\n"
      "s_sub_u32 s46, s46, .Lhere_%=-.Lbegin_%=\n"
      "s_subb_u32 s47, s47, 0\n"
      "v_add_u32 v238, s44, v242\n"
      "v_min_u32 v238, .Lend_%=-.Lbegin_%=-16, v238\n"
      "global_load_dwordx4 v[192:195], v238, s[46:47]\n"
      "s_and_b32 s44, s34, 1\n"
      "s_lshr_b32 s45, s36, 1\n"
      "s_lshl3_add_u32 s44, s44, s45\n"
      "s_lshl2_add_u32 s44, s44, s35\n"
      "s_lshl2_add_u32 s44, s44, s33\n"
      "s_lshl_b32 s44, s44, 13\n"
      "s_add_u32 s44, s44, 128\n"
      "v_and_b32 v233, 31, v243\n"
      "v_lshl_add_u32 v233, v233, 8, s44\n"
      "s_lshr_b32 s44, s33, 1\n"
      "s_add_u32 s44, s44, s34\n"
      "s_add_u32 s44, s44, 1\n"
      "s_and_b32 s44, s44, 3\n"
      "s_lshl2_add_u32 s44, s35, s44\n"
      "s_lshr_b32 s45, s32, 6\n"
      "s_lshl2_add_u32 s44, s44, s45\n"
      "s_and_b32 s45, s33, 1\n"
      "s_lshl1_add_u32 s44, s44, s45\n"
      "s_lshl_b32 s44, s44, 12\n"
      "s_add_u32 s44, s44, 128\n"
      "v_and_b32 v234, 15, v243\n"
      "v_lshl_add_u32 v234, v234, 8, s44\n"
      "s_bitcmp1_b32 s34, 1\n"
      "s_waitcnt lgkmcnt(0)\n"
      "s_cselect_b64 s[68:69], s[14:15], s[12:13]\n"
      "s_bitcmp1_b32 s34, 1\n"
      "s_cselect_b64 s[8:9], s[10:11], s[8:9]\n"
      "s_and_b32 s45, s34, 1\n"
      "s_lshl2_add_u32 s45, s45, s33\n"
      "s_lshl_b32 s45, s45, 9\n"
      "s_lshl_b32 s76, s36, 19\n"
      "s_add_u32 s45, s45, s76\n"
      "s_add_u32 s8, s8, s45\n"
      "s_addc_u32 s9, s9, 0\n"
      "s_lshr_b32 s39, s32, 6\n"
      "s_and_b32 s45, s35, 2\n"
      "s_xor_b32 s39, s39, s45\n"
      "s_xor_b32 s39, s39, s33\n"
      "s_lshl_b32 s45, s39, 7\n"
      "s_add_u32 s76, s33, 0\n"
      "s_and_b32 s76, s76, 3\n"
      "s_lshl_b32 s76, s76, 17\n"
      "s_or_b32 s76, s76, s45\n"
      "s_add_u32 s77, s33, 1\n"
      "s_and_b32 s77, s77, 3\n"
      "s_lshl_b32 s77, s77, 17\n"
      "s_or_b32 s77, s77, s45\n"
      "s_add_u32 s78, s33, 2\n"
      "s_and_b32 s78, s78, 3\n"
      "s_lshl_b32 s78, s78, 17\n"
      "s_or_b32 s78, s78, s45\n"
      "s_add_u32 s79, s33, 3\n"
      "s_and_b32 s79, s79, 3\n"
      "s_lshl_b32 s79, s79, 17\n"
      "s_or_b32 s79, s79, s45\n"
      "v_mov_b32 v239, 0\n"
      "global_load_dword v199, v239, s[8:9]\n"
      "s_add_u32 s58, s18, 4718592\n"
      "s_addc_u32 s59, s19, 0\n"
      "global_load_dword v235, v239, s[58:59] sc1\n"
      "s_lshl2_add_u32 s43, s34, s35\n"
      "s_mul_i32 s43, s43, 266240\n"
      "s_add_u32 s43, s43, 262144\n"
      "s_lshl2_add_u32 s44, s36, s35\n"
      "s_mul_i32 s44, s44, 147712\n"
      "s_add_u32 s44, s44, 6438912\n"
      "s_bitcmp1_b32 s33, 0\n"
      "s_cselect_b32 s43, s43, s44\n"
      "s_cselect_b64 s[46:47], s[22:23], s[24:25]\n"
      "s_cselect_b64 s[70:71], s[28:29], s[26:27]\n"
      "s_lshl_b32 s44, s36, 17\n"
      "s_mul_i32 s45, s32, 7168\n"
      "s_bitcmp1_b32 s33, 1\n"
      "s_cselect_b64 s[46:47], s[46:47], s[18:19]\n"
      "s_cselect_b32 s43, 0, s43\n"
      "s_cselect_b32 s84, 0x380, 0\n"
      "v_lshlrev_b32 v238, 7, v243\n"
      "v_and_b32 v238, s84, v238\n"
      "v_add_u32 v238, s43, v238\n"
      "global_load_dword v197, v238, s[46:47] sc1\n"
      "s_cmp_eq_u32 s33, 2\n"
      "s_cselect_b64 s[70:71], s[30:31], s[70:71]\n"
      "s_cmp_eq_u32 s33, 3\n"
      "s_cselect_b64 s[70:71], s[20:21], s[70:71]\n"
      "s_cselect_b32 s44, s44, s45\n"
      "v_mov_b32 v239, s44\n"
      "global_load_dword v198, v239, s[70:71] sc1\n"
      "s_sleep 8\n"
      "global_load_dword v200, v234, s[68:69]\n"
      "global_load_dword v201, v233, s[16:17]\n"
      "s_sleep 4\n"
      "s_bitcmp1_b32 s34, 1\n"
      "s_cselect_b64 s[46:47], s[14:15], s[12:13]\n"
      "s_min_u32 s44, s33, 2\n"
      "s_add_u32 s44, s44, s34\n"
      "s_add_u32 s44, s44, 2\n"
      "s_and_b32 s44, s44, 3\n"
      "s_lshl2_add_u32 s44, s35, s44\n"
      "s_lshr_b32 s45, s32, 6\n"
      "s_lshl2_add_u32 s44, s44, s45\n"
      "s_lshl_b32 s44, s44, 13\n"
      "v_lshl_add_u32 v238, v243, 7, s44\n"
      "s_cmp_eq_u32 s33, 0\n"
      "s_cselect_b32 s44, 4096, 0\n"
      "v_or_b32 v238, s44, v238\n"
      "s_cmp_eq_u32 s33, 3\n"
      "s_cbranch_scc1 .Ls_e1_%=\n"
      "global_load_dword v196, v238, s[46:47]\n"
      ".Ls_e1_%=:\n"
      "s_xor_b32 s45, s76, 0\n"
      "s_add_u32 s46, s8, s45\n"
      "s_addc_u32 s47, s9, 0\n"
      "s_add_u32 m0, s41, 0\n"
      "s_nop 0\n"
      "global_load_lds_dwordx4 v244, s[46:47] offset:0\n"
      "global_load_lds_dwordx4 v245, s[46:47] offset:1024\n"
      "global_load_lds_dwordx4 v246, s[46:47] offset:2048\n"
      "global_load_lds_dwordx4 v247, s[46:47] offset:3072\n"
      "s_xor_b32 s45, s77, 0\n"
      "s_add_u32 s46, s8, s45\n"
      "s_addc_u32 s47, s9, 0\n"
      "s_add_u32 m0, s41, 4096\n"
      "s_nop 0\n"
      "global_load_lds_dwordx4 v244, s[46:47] offset:0\n"
      "global_load_lds_dwordx4 v245, s[46:47] offset:1024\n"
      "global_load_lds_dwordx4 v246, s[46:47] offset:2048\n"
      "global_load_lds_dwordx4 v247, s[46:47] offset:3072\n"
      "s_xor_b32 s45, s78, 0\n"
      "s_add_u32 s46, s8, s45\n"
      "s_addc_u32 s47, s9, 0\n"
      "s_add_u32 m0, s41, 8192\n"
      "s_nop 0\n"
      "global_load_lds_dwordx4 v244, s[46:47] offset:0\n"
      "global_load_lds_dwordx4 v245, s[46:47] offset:1024\n"
      "global_load_lds_dwordx4 v246, s[46:47] offset:2048\n"
      "global_load_lds_dwordx4 v247, s[46:47] offset:3072\n"
      "v_and_b32 v238, 15, v243\n"
      "v_lshrrev_b32 v239, 4, v243\n"
      "v_xor_b32 v239, v239, v238\n"
      "v_and_b32 v239, 7, v239\n"
      "v_lshlrev_b32 v239, 4, v239\n"
      "v_lshl_or_b32 v238, v238, 7, v239\n"
      "v_add_u32 v248, s41, v238\n"
      "v_xor_b32 v249, 64, v248\n"
      "s_lshl2_add_u32 s43, s34, s35\n"
      "s_lshr_b32 s45, s39, 1\n"
      "s_lshl1_add_u32 s45, s33, s45\n"
      "s_lshl_b32 s45, s45, 15\n"
      "s_mul_i32 s44, s43, 266240\n"
      "s_add_u32 s44, s44, 4096\n"
      "s_add_u32 s14, s18, s44\n"
      "s_addc_u32 s15, s19, 0\n"
      "s_add_u32 s10, s14, s45\n"
      "s_addc_u32 s11, s15, 0\n"
      "s_xor_b32 s45, s45, 0x8000\n"
      "s_add_u32 s14, s14, s45\n"
      "s_addc_u32 s15, s15, 0\n"
      "s_and_b32 s44, s39, 1\n"
      "s_lshl_b32 s45, s44, 14\n"
      "v_add_u32 v240, s45, v242\n"
      "v_xor_b32 v241, 0x4000, v240\n"
      "s_add_u32 s72, s10, 0x2000\n"
      "s_addc_u32 s73, s11, 0\n"
      "s_add_u32 s74, s14, 0x2000\n"
      "s_addc_u32 s75, s15, 0\n"
      "s_lshl2_add_u32 s44, s43, s33\n"
      "s_lshl_b32 s44, s44, 12\n"
      "s_add_u32 s44, s44, 1048576\n"
      "v_and_b32 v253, 7, v243\n"
      "v_lshl_add_u32 v253, v253, 9, s44\n"
      "s_lshl2_add_u32 s43, s36, s35\n"
      "s_mul_i32 s43, s43, 147712\n"
      "s_mul_i32 s44, s33, 3072\n"
      "s_add_u32 s43, s43, s44\n"
      "s_add_u32 s43, s43, 6291456\n"
      "s_add_u32 s60, s18, s43\n"
      "s_addc_u32 s61, s19, 0\n"
      "s_mul_i32 s44, s34, 36864\n"
      "s_add_u32 s62, s60, s44\n"
      "s_addc_u32 s63, s61, 0\n"
      "s_lshr_b32 s43, s34, 1\n"
      "s_lshl2_add_u32 s43, s43, s35\n"
      "s_lshl_b32 s43, s43, 12\n"
      "s_add_u32 s43, s43, 4096\n"
      "v_lshl_add_u32 v204, v243, 6, s43\n"
      "s_lshr_b32 s80, s32, 4\n"
      "s_bfe_u32 s43, s32, 0x10002\n"
      "s_lshl1_add_u32 s80, s80, s43\n"
      "s_and_b32 s81, s32, 3\n"
      "s_cmp_lt_u32 s80, 28\n"
      "s_cselect_b32 s86, -1, 0\n"
      "s_lshr_b32 s83, s33, 1\n"
      "s_lshl1_add_u32 s83, s81, s83\n"
      "v_add_u32 v250, 0x10000, v248\n"
      "v_add_u32 v251, 0x10000, v249\n"
      "s_xor_b32 s45, s79, 0\n"
      "s_add_u32 s46, s8, s45\n"
      "s_addc_u32 s47, s9, 0\n"
      "s_add_u32 m0, s41, 12288\n"
      "s_nop 0\n"
      "global_load_lds_dwordx4 v244, s[46:47] offset:0\n"
      "global_load_lds_dwordx4 v245, s[46:47] offset:1024\n"
      "global_load_lds_dwordx4 v246, s[46:47] offset:2048\n"
      "global_load_lds_dwordx4 v247, s[46:47] offset:3072\n"
      "s_xor_b32 s45, s76, 128\n"
      "s_add_u32 s46, s8, s45\n"
      "s_addc_u32 s47, s9, 0\n"
      "s_add_u32 m0, s41, 65536\n"
      "s_nop 0\n"
      "global_load_lds_dwordx4 v244, s[46:47] offset:0\n"
      "global_load_lds_dwordx4 v245, s[46:47] offset:1024\n"
      "global_load_lds_dwordx4 v246, s[46:47] offset:2048\n"
      "global_load_lds_dwordx4 v247, s[46:47] offset:3072\n"
      "s_waitcnt vmcnt(16)\n"
      "ds_read_b128 a[0:3], v248 offset:0\n"
      "ds_read_b128 a[4:7], v248 offset:2048\n"
      "ds_read_b128 a[32:35], v249 offset:0\n"
      "ds_read_b128 a[36:39], v249 offset:2048\n"
      "s_xor_b32 s45, s77, 128\n"
      "s_add_u32 s46, s8, s45\n"
      "s_addc_u32 s47, s9, 0\n"
      "s_add_u32 m0, s41, 69632\n"
      "s_nop 0\n"
      "global_load_lds_dwordx4 v244, s[46:47] offset:0\n"
      "global_load_lds_dwordx4 v245, s[46:47] offset:1024\n"
      "global_load_lds_dwordx4 v246, s[46:47] offset:2048\n"
      "global_load_lds_dwordx4 v247, s[46:47] offset:3072\n"
      "s_waitcnt vmcnt(16)\n"
      "ds_read_b128 a[8:11], v248 offset:4096\n"
      "ds_read_b128 a[12:15], v248 offset:6144\n"
      "ds_read_b128 a[40:43], v249 offset:4096\n"
      "ds_read_b128 a[44:47], v249 offset:6144\n"
      "s_xor_b32 s45, s78, 128\n"
      "s_add_u32 s46, s8, s45\n"
      "s_addc_u32 s47, s9, 0\n"
      "s_add_u32 m0, s41, 73728\n"
      "s_nop 0\n"
      "global_load_lds_dwordx4 v244, s[46:47] offset:0\n"
      "global_load_lds_dwordx4 v245, s[46:47] offset:1024\n"
      "global_load_lds_dwordx4 v246, s[46:47] offset:2048\n"
      "global_load_lds_dwordx4 v247, s[46:47] offset:3072\n"
      "s_waitcnt vmcnt(16)\n"
      "ds_read_b128 a[16:19], v248 offset:8192\n"
      "ds_read_b128 a[20:23], v248 offset:10240\n"
      "ds_read_b128 a[48:51], v249 offset:8192\n"
      "ds_read_b128 a[52:55], v249 offset:10240\n"
      "v_readfirstlane_b32 s37, v235\n"
      "s_nop 1\n"
      "s_mul_i32 s37, s37, 0x9E3779B1\n"
      "s_add_u32 s37, s37, 0x7F4A7C15\n"
      "s_mov_b32 s38, 0x8000\n"
      "s_waitcnt lgkmcnt(8)\n"
      "s_xor_b32 s45, s79, 128\n"
      "s_add_u32 s46, s8, s45\n"
      "s_addc_u32 s47, s9, 0\n"
      "s_add_u32 m0, s41, 0\n"
      "s_nop 0\n"
      "global_load_lds_dwordx4 v244, s[46:47] offset:0\n"
      "global_load_lds_dwordx4 v245, s[46:47] offset:1024\n"
      "global_load_lds_dwordx4 v246, s[46:47] offset:2048\n"
      "global_load_lds_dwordx4 v247, s[46:47] offset:3072\n"
      "s_barrier\n"
      "s_waitcnt vmcnt(16)\n"
      "ds_read_b128 a[24:27], v248 offset:12288\n"
      "ds_read_b128 a[28:31], v248 offset:14336\n"
      "ds_read_b128 a[56:59], v249 offset:12288\n"
      "ds_read_b128 a[60:63], v249 offset:14336\n"
      "s_waitcnt lgkmcnt(8)\n"
      "s_xor_b32 s45, s76, 256\n"
      "s_add_u32 s46, s8, s45\n"
      "s_addc_u32 s47, s9, 0\n"
      "s_add_u32 m0, s41, 4096\n"
      "s_nop 0\n"
      "global_load_lds_dwordx4 v244, s[46:47] offset:0\n"
      "global_load_lds_dwordx4 v245, s[46:47] offset:1024\n"
      "global_load_lds_dwordx4 v246, s[46:47] offset:2048\n"
      "global_load_lds_dwordx4 v247, s[46:47] offset:3072\n"
      "s_waitcnt vmcnt(16)\n"
      "ds_read_b128 a[64:67], v250 offset:0\n"
      "ds_read_b128 a[68:71], v250 offset:2048\n"
      "ds_read_b128 a[96:99], v251 offset:0\n"
      "ds_read_b128 a[100:103], v251 offset:2048\n"
      "s_waitcnt lgkmcnt(8)\n"
      "s_xor_b32 s45, s77, 256\n"
      "s_add_u32 s46, s8, s45\n"
      "s_addc_u32 s47, s9, 0\n"
      "s_add_u32 m0, s41, 8192\n"
      "s_nop 0\n"
      "global_load_lds_dwordx4 v244, s[46:47] offset:0\n"
      "global_load_lds_dwordx4 v245, s[46:47] offset:1024\n"
      "global_load_lds_dwordx4 v246, s[46:47] offset:2048\n"
      "global_load_lds_dwordx4 v247, s[46:47] offset:3072\n"
      "s_waitcnt vmcnt(16)\n"
      "ds_read_b128 a[72:75], v250 offset:4096\n"
      "ds_read_b128 a[76:79], v250 offset:6144\n"
      "ds_read_b128 a[104:107], v251 offset:4096\n"
      "ds_read_b128 a[108:111], v251 offset:6144\n"
      "s_waitcnt lgkmcnt(8)\n"
      "s_xor_b32 s45, s78, 256\n"
      "s_add_u32 s46, s8, s45\n"
      "s_addc_u32 s47, s9, 0\n"
      "s_add_u32 m0, s41, 12288\n"
      "s_nop 0\n"
      "global_load_lds_dwordx4 v244, s[46:47] offset:0\n"
      "global_load_lds_dwordx4 v245, s[46:47] offset:1024\n"
      "global_load_lds_dwordx4 v246, s[46:47] offset:2048\n"
      "global_load_lds_dwordx4 v247, s[46:47] offset:3072\n"
      "s_waitcnt vmcnt(16)\n"
      "ds_read_b128 a[80:83], v250 offset:8192\n"
      "ds_read_b128 a[84:87], v250 offset:10240\n"
      "ds_read_b128 a[112:115], v251 offset:8192\n"
      "ds_read_b128 a[116:119], v251 offset:10240\n"
      "s_waitcnt vmcnt(12)\n"
      "ds_read_b128 a[88:91], v248 offset:0\n"
      "ds_read_b128 a[92:95], v248 offset:2048\n"
      "ds_read_b128 a[120:123], v249 offset:0\n"
      "ds_read_b128 a[124:127], v249 offset:2048\n"
      "global_load_dword v252, v253, s[58:59] offset:0 sc1\n"
      "s_waitcnt lgkmcnt(12)\n"
      "s_xor_b32 s45, s79, 256\n"
      "s_add_u32 s46, s8, s45\n"
      "s_addc_u32 s47, s9, 0\n"
      "s_add_u32 m0, s41, 65536\n"
      "s_nop 0\n"
      "global_load_lds_dwordx4 v244, s[46:47] offset:0\n"
      "global_load_lds_dwordx4 v245, s[46:47] offset:1024\n"
      "global_load_lds_dwordx4 v246, s[46:47] offset:2048\n"
      "global_load_lds_dwordx4 v247, s[46:47] offset:3072\n"
      "s_waitcnt lgkmcnt(8)\n"
      "s_xor_b32 s45, s76, 384\n"
      "s_add_u32 s46, s8, s45\n"
      "s_addc_u32 s47, s9, 0\n"
      "s_add_u32 m0, s41, 69632\n"
      "s_nop 0\n"
      "global_load_lds_dwordx4 v244, s[46:47] offset:0\n"
      "global_load_lds_dwordx4 v245, s[46:47] offset:1024\n"
      "global_load_lds_dwordx4 v246, s[46:47] offset:2048\n"
      "global_load_lds_dwordx4 v247, s[46:47] offset:3072\n"
      "s_waitcnt vmcnt(17)\n"
      "ds_read_b128 a[128:131], v248 offset:4096\n"
      "ds_read_b128 a[132:135], v248 offset:6144\n"
      "ds_read_b128 a[160:163], v249 offset:4096\n"
      "ds_read_b128 a[164:167], v249 offset:6144\n"
      "s_waitcnt vmcnt(13)\n"
      "ds_read_b128 a[136:139], v248 offset:8192\n"
      "ds_read_b128 a[140:143], v248 offset:10240\n"
      "ds_read_b128 a[168:171], v249 offset:8192\n"
      "ds_read_b128 a[172:175], v249 offset:10240\n"
      "s_barrier\n"
      "s_waitcnt vmcnt(8)\n"
      "v_cmp_ne_u32 vcc, s37, v252\n"
      "s_nop 1\n"
      "s_cbranch_vccnz .Ls_s3_%=\n"
      ".Ls_o4_%=:\n"
      "global_load_dwordx2 v[206:207], v204, s[58:59] sc1\n"
      "global_load_dwordx4 v[128:131], v240, s[10:11] offset:-4096 sc1\n"
      "global_load_dwordx4 v[132:135], v240, s[10:11] offset:-3072 sc1\n"
      "global_load_dwordx4 v[136:139], v240, s[10:11] offset:-2048 sc1\n"
      "global_load_dwordx4 v[140:143], v240, s[72:73] offset:-4096 sc1\n"
      "global_load_dwordx4 v[144:147], v240, s[72:73] offset:-3072 sc1\n"
      "global_load_dwordx4 v[148:151], v240, s[72:73] offset:-2048 sc1\n"
      "s_waitcnt vmcnt(5)\n"
      "v_mfma_f32_16x16x32_bf16 v[0:3], v[128:131], a[0:3], 0\n"
      "global_load_dwordx4 v[152:155], v241, s[10:11] offset:-4096 sc1\n"
      "v_cmp_ne_u32 vcc, s37, v207\n"
      "s_nop 1\n"
      "s_cbranch_vccnz .Ls_s6_%=\n"
      ".Ls_o7_%=:\n"
      "v_mfma_f32_16x16x32_bf16 v[4:7], v[128:131], a[4:7], 0\n"
      "v_max_u32_dpp v206, v206, v206 quad_perm:[1,0,3,2] row_mask:0xf bank_mask:0xf\n"
      "s_nop 1\n"
      "v_max_u32_dpp v206, v206, v206 quad_perm:[2,3,0,1] row_mask:0xf bank_mask:0xf\n"
      "v_mfma_f32_16x16x32_bf16 v[8:11], v[128:131], a[8:11], 0\n"
      "global_load_dwordx4 v[156:159], v241, s[10:11] offset:-3072 sc1\n"
      "v_max_u32_dpp v206, v206, v206 row_half_mirror row_mask:0xf bank_mask:0xf\n"
      "v_mfma_f32_16x16x32_bf16 v[12:15], v[128:131], a[12:15], 0\n"
      "s_nop 0\n"
      "v_max_u32_dpp v206, v206, v206 row_mirror row_mask:0xf bank_mask:0xf\n"
      "v_mfma_f32_16x16x32_bf16 v[16:19], v[128:131], a[16:19], 0\n"
      "global_load_dwordx4 v[160:163], v241, s[10:11] offset:-2048 sc1\n"
      "v_mov_b32 v205, v206\n"
      "v_mfma_f32_16x16x32_bf16 v[20:23], v[128:131], a[20:23], 0\n"
      "s_nop 0\n"
      "v_permlane16_swap_b32 v206, v205\n"
      "v_mfma_f32_16x16x32_bf16 v[24:27], v[128:131], a[24:27], 0\n"
      "global_load_dwordx4 v[164:167], v241, s[72:73] offset:-4096 sc1\n"
      "v_max_u32 v206, v206, v205\n"
      "v_mfma_f32_16x16x32_bf16 v[28:31], v[128:131], a[28:31], 0\n"
      "v_mov_b32 v205, v206\n"
      "s_nop 1\n"
      "v_permlane32_swap_b32 v206, v205\n"
      "s_waitcnt vmcnt(8)\n"
      "v_mfma_f32_16x16x32_bf16 v[32:35], v[132:135], a[0:3], 0\n"
      "global_load_dwordx4 v[168:171], v241, s[72:73] offset:-3072 sc1\n"
      "v_max_u32 v206, v206, v205\n"
      "v_mfma_f32_16x16x32_bf16 v[36:39], v[132:135], a[4:7], 0\n"
      "v_readfirstlane_b32 s40, v206\n"
      "s_nop 1\n"
      "s_andn2_b32 s42, 0x7ff, s40\n"
      "v_mfma_f32_16x16x32_bf16 v[40:43], v[132:135], a[8:11], 0\n"
      "global_load_dwordx4 v[172:175], v241, s[72:73] offset:-2048 sc1\n"
      "s_lshr_b32 s43, s42, 5\n"
      "s_lshl2_add_u32 s44, s36, s33\n"
      "s_cmp_eq_u32 s43, s44\n"
      "s_cbranch_scc0 .Ls_e8_%=\n"
      "s_lshl_b32 s43, s42, 10\n"
      "v_add_u32 v208, s43, v242\n"
      "global_load_dwordx4 v[216:219], v208, s[16:17]\n"
      "global_load_dwordx4 v[220:223], v242, s[24:25]\n"
      ".Ls_e8_%=:\n"
      "v_mfma_f32_16x16x32_bf16 v[44:47], v[132:135], a[12:15], 0\n"
      "v_mfma_f32_16x16x32_bf16 v[48:51], v[132:135], a[16:19], 0\n"
      "global_load_dwordx4 v[176:179], v240, s[14:15] offset:-4096 sc1\n"
      "v_mfma_f32_16x16x32_bf16 v[52:55], v[132:135], a[20:23], 0\n"
      "v_mfma_f32_16x16x32_bf16 v[56:59], v[132:135], a[24:27], 0\n"
      "global_load_dwordx4 v[180:183], v240, s[14:15] offset:-3072 sc1\n"
      "v_mfma_f32_16x16x32_bf16 v[60:63], v[132:135], a[28:31], 0\n"
      "s_waitcnt vmcnt(10)\n"
      "v_mfma_f32_16x16x32_bf16 v[64:67], v[136:139], a[0:3], 0\n"
      "global_load_dwordx4 v[184:187], v240, s[14:15] offset:-2048 sc1\n"
      "global_load_dwordx4 v[188:191], v240, s[74:75] offset:-4096 sc1\n"
      "v_mfma_f32_16x16x32_bf16 v[68:71], v[136:139], a[4:7], 0\n"
      "global_load_dwordx4 v[128:131], v240, s[74:75] offset:-3072 sc1\n"
      "global_load_dwordx4 v[132:135], v240, s[74:75] offset:-2048 sc1\n"
      "v_mfma_f32_16x16x32_bf16 v[72:75], v[136:139], a[8:11], 0\n"
      "s_waitcnt lgkmcnt(12)\n"
      "s_xor_b32 s70, s77, 384\n"
      "s_add_u32 s70, s8, s70\n"
      "s_addc_u32 s71, s9, 0\n"
      "s_add_u32 m0, s41, 73728\n"
      "v_mfma_f32_16x16x32_bf16 v[76:79], v[136:139], a[12:15], 0\n"
      "global_load_lds_dwordx4 v244, s[70:71] offset:0\n"
      "v_mfma_f32_16x16x32_bf16 v[80:83], v[136:139], a[16:19], 0\n"
      "v_mfma_f32_16x16x32_bf16 v[84:87], v[136:139], a[20:23], 0\n"
      "global_load_lds_dwordx4 v245, s[70:71] offset:1024\n"
      "v_mfma_f32_16x16x32_bf16 v[88:91], v[136:139], a[24:27], 0\n"
      "v_mfma_f32_16x16x32_bf16 v[92:95], v[136:139], a[28:31], 0\n"
      "global_load_lds_dwordx4 v246, s[70:71] offset:2048\n"
      "global_load_dwordx4 v[136:139], v241, s[14:15] offset:-4096 sc1\n"
      "v_mfma_f32_16x16x32_bf16 v[0:3], v[140:143], a[32:35], v[0:3]\n"
      "v_mfma_f32_16x16x32_bf16 v[4:7], v[140:143], a[36:39], v[4:7]\n"
      "global_load_lds_dwordx4 v247, s[70:71] offset:3072\n"
      "v_mfma_f32_16x16x32_bf16 v[8:11], v[140:143], a[40:43], v[8:11]\n"
      "s_waitcnt lgkmcnt(8)\n"
      "s_xor_b32 s70, s78, 384\n"
      "s_add_u32 s70, s8, s70\n"
      "s_addc_u32 s71, s9, 0\n"
      "s_add_u32 m0, s41, 0\n"
      "v_mfma_f32_16x16x32_bf16 v[12:15], v[140:143], a[44:47], v[12:15]\n"
      "global_load_lds_dwordx4 v244, s[70:71] offset:0\n"
      "v_mfma_f32_16x16x32_bf16 v[16:19], v[140:143], a[48:51], v[16:19]\n"
      "v_mfma_f32_16x16x32_bf16 v[20:23], v[140:143], a[52:55], v[20:23]\n"
      "global_load_lds_dwordx4 v245, s[70:71] offset:1024\n"
      "ds_read_b128 a[144:147], v248 offset:12288\n"
      "v_mfma_f32_16x16x32_bf16 v[24:27], v[140:143], a[56:59], v[24:27]\n"
      "ds_read_b128 a[148:151], v248 offset:14336\n"
      "ds_read_b128 a[176:179], v249 offset:12288\n"
      "v_mfma_f32_16x16x32_bf16 v[28:31], v[140:143], a[60:63], v[28:31]\n"
      "s_waitcnt vmcnt(19)\n"
      "global_load_lds_dwordx4 v246, s[70:71] offset:2048\n"
      "global_load_dwordx4 v[140:143], v241, s[74:75] offset:-4096 sc1\n"
      "v_mfma_f32_16x16x32_bf16 v[32:35], v[144:147], a[32:35], v[32:35]\n"
      "ds_read_b128 a[180:183], v249 offset:14336\n"
      "ds_read_b128 a[152:155], v250 offset:0\n"
      "v_mfma_f32_16x16x32_bf16 v[36:39], v[144:147], a[36:39], v[36:39]\n"
      "global_load_lds_dwordx4 v247, s[70:71] offset:3072\n"
      "ds_read_b128 a[156:159], v250 offset:2048\n"
      "v_mfma_f32_16x16x32_bf16 v[40:43], v[144:147], a[40:43], v[40:43]\n"
      "s_waitcnt lgkmcnt(10)\n"
      "s_xor_b32 s70, s79, 384\n"
      "s_add_u32 s70, s8, s70\n"
      "s_addc_u32 s71, s9, 0\n"
      "s_add_u32 m0, s41, 4096\n"
      "v_mfma_f32_16x16x32_bf16 v[44:47], v[144:147], a[44:47], v[44:47]\n"
      "global_load_lds_dwordx4 v244, s[70:71] offset:0\n"
      "ds_read_b128 a[184:187], v251 offset:0\n"
      "v_mfma_f32_16x16x32_bf16 v[48:51], v[144:147], a[48:51], v[48:51]\n"
      "ds_read_b128 a[188:191], v251 offset:2048\n"
      "ds_read_b128 a[192:195], v250 offset:4096\n"
      "v_mfma_f32_16x16x32_bf16 v[52:55], v[144:147], a[52:55], v[52:55]\n"
      "global_load_lds_dwordx4 v245, s[70:71] offset:1024\n"
      "ds_read_b128 a[196:199], v250 offset:6144\n"
      "v_mfma_f32_16x16x32_bf16 v[56:59], v[144:147], a[56:59], v[56:59]\n"
      "ds_read_b128 a[224:227], v251 offset:4096\n"
      "ds_read_b128 a[228:231], v251 offset:6144\n"
      "v_mfma_f32_16x16x32_bf16 v[60:63], v[144:147], a[60:63], v[60:63]\n"
      "global_load_lds_dwordx4 v246, s[70:71] offset:2048\n"
      "global_load_dwordx4 v[144:147], v241, s[14:15] offset:-3072 sc1\n"
      "v_mfma_f32_16x16x32_bf16 v[64:67], v[148:151], a[32:35], v[64:67]\n"
      "v_mfma_f32_16x16x32_bf16 v[68:71], v[148:151], a[36:39], v[68:71]\n"
      "global_load_lds_dwordx4 v247, s[70:71] offset:3072\n"
      "v_mfma_f32_16x16x32_bf16 v[72:75], v[148:151], a[40:43], v[72:75]\n"
      "v_mfma_f32_16x16x32_bf16 v[76:79], v[148:151], a[44:47], v[76:79]\n"
      "v_mfma_f32_16x16x32_bf16 v[80:83], v[148:151], a[48:51], v[80:83]\n"
      "v_mfma_f32_16x16x32_bf16 v[84:87], v[148:151], a[52:55], v[84:87]\n"
      "v_mfma_f32_16x16x32_bf16 v[88:91], v[148:151], a[56:59], v[88:91]\n"
      "v_mfma_f32_16x16x32_bf16 v[92:95], v[148:151], a[60:63], v[92:95]\n"
      "s_waitcnt vmcnt(25)\n"
      "global_load_dwordx4 v[148:151], v241, s[74:75] offset:-3072 sc1\n"
      "v_mfma_f32_16x16x32_bf16 v[0:3], v[152:155], a[64:67], v[0:3]\n"
      "v_mfma_f32_16x16x32_bf16 v[4:7], v[152:155], a[68:71], v[4:7]\n"
      "v_mfma_f32_16x16x32_bf16 v[8:11], v[152:155], a[72:75], v[8:11]\n"
      "v_mfma_f32_16x16x32_bf16 v[12:15], v[152:155], a[76:79], v[12:15]\n"
      "v_mfma_f32_16x16x32_bf16 v[16:19], v[152:155], a[80:83], v[16:19]\n"
      "v_mfma_f32_16x16x32_bf16 v[20:23], v[152:155], a[84:87], v[20:23]\n"
      "v_mfma_f32_16x16x32_bf16 v[24:27], v[152:155], a[88:91], v[24:27]\n"
      "v_mfma_f32_16x16x32_bf16 v[28:31], v[152:155], a[92:95], v[28:31]\n"
      "global_load_dwordx4 v[152:155], v241, s[14:15] offset:-2048 sc1\n"
      "s_waitcnt vmcnt(12)\n"
      "ds_read_b128 a[200:203], v250 offset:8192\n"
      "v_mfma_f32_16x16x32_bf16 v[32:35], v[156:159], a[64:67], v[32:35]\n"
      "ds_read_b128 a[204:207], v250 offset:10240\n"
      "ds_read_b128 a[232:235], v251 offset:8192\n"
      "v_mfma_f32_16x16x32_bf16 v[36:39], v[156:159], a[68:71], v[36:39]\n"
      "ds_read_b128 a[236:239], v251 offset:10240\n"
      "v_mfma_f32_16x16x32_bf16 v[40:43], v[156:159], a[72:75], v[40:43]\n"
      "v_mfma_f32_16x16x32_bf16 v[44:47], v[156:159], a[76:79], v[44:47]\n"
      "v_mfma_f32_16x16x32_bf16 v[48:51], v[156:159], a[80:83], v[48:51]\n"
      "s_lshl_b32 s54, s42, 10\n"
      "s_lshl_b32 s55, s83, 7\n"
      "s_add_u32 s54, s54, s55\n"
      "v_mfma_f32_16x16x32_bf16 v[52:55], v[156:159], a[84:87], v[52:55]\n"
      "v_and_b32 v224, 48, v243\n"
      "v_add_u32 v224, s54, v224\n"
      "v_mfma_f32_16x16x32_bf16 v[56:59], v[156:159], a[88:91], v[56:59]\n"
      "global_load_dwordx4 v[192:195], v224, s[16:17]\n"
      "global_load_dwordx4 v[196:199], v224, s[16:17] offset:64\n"
      "v_mfma_f32_16x16x32_bf16 v[60:63], v[156:159], a[92:95], v[60:63]\n"
      "global_load_dwordx4 v[156:159], v241, s[74:75] offset:-2048 sc1\n"
      "s_waitcnt vmcnt(10)\n"
      "ds_read_b128 a[208:211], v248 offset:0\n"
      "v_mfma_f32_16x16x32_bf16 v[64:67], v[160:163], a[64:67], v[64:67]\n"
      "ds_read_b128 a[212:215], v248 offset:2048\n"
      "ds_read_b128 a[240:243], v249 offset:0\n"
      "v_mfma_f32_16x16x32_bf16 v[68:71], v[160:163], a[68:71], v[68:71]\n"
      "ds_read_b128 a[244:247], v249 offset:2048\n"
      "v_mfma_f32_16x16x32_bf16 v[72:75], v[160:163], a[72:75], v[72:75]\n"
      "v_mfma_f32_16x16x32_bf16 v[76:79], v[160:163], a[76:79], v[76:79]\n"
      "v_mfma_f32_16x16x32_bf16 v[80:83], v[160:163], a[80:83], v[80:83]\n"
      "v_mfma_f32_16x16x32_bf16 v[84:87], v[160:163], a[84:87], v[84:87]\n"
      "v_mfma_f32_16x16x32_bf16 v[88:91], v[160:163], a[88:91], v[88:91]\n"
      "s_ashr_i32 s84, s40, 31\n"
      "s_min_u32 s43, s80, 27\n"
      "v_lshrrev_b32 v202, 3, v243\n"
      "v_and_b32 v203, 0x70, v242\n"
      "s_movk_i32 s44, 3584\n"
      "v_mad_u32_u24 v200, v202, s44, v203\n"
      "v_add_u32 v201, 27648, v200\n"
      "v_add_u32 v202, 55296, v200\n"
      "v_add_u32 v203, 82944, v200\n"
      "s_bitcmp1_b32 s33, 0\n"
      "s_cselect_b64 s[70:71], s[28:29], s[26:27]\n"
      "s_mul_i32 s44, s83, 229376\n"
      "s_lshl_b32 s45, s43, 7\n"
      "s_add_u32 s44, s44, s45\n"
      "s_add_u32 s70, s70, s44\n"
      "s_addc_u32 s71, s71, 0\n"
      "s_lshl_b32 s45, s33, 13\n"
      "s_add_u32 m0, s45, 131072\n"
      "v_mfma_f32_16x16x32_bf16 v[92:95], v[160:163], a[92:95], v[92:95]\n"
      "s_not_b32 s84, s84\n"
      "global_load_lds_dwordx4 v200, s[70:71] offset:0\n"
      "s_or_b32 s84, s84, 0x80000000\n"
      "v_mfma_f32_16x16x32_bf16 v[0:3], v[164:167], a[96:99], v[0:3]\n"
      "s_xor_b32 s84, s84, s40\n"
      "global_load_lds_dwordx4 v201, s[70:71] offset:1024\n"
      "s_and_b32 s84, s84, 0xffff0000\n"
      "v_mfma_f32_16x16x32_bf16 v[4:7], v[164:167], a[100:103], v[4:7]\n"
      "global_load_lds_dwordx4 v202, s[70:71] offset:2048\n"
      "v_lshlrev_b32 v208, 16, v216\n"
      "v_mfma_f32_16x16x32_bf16 v[8:11], v[164:167], a[104:107], v[8:11]\n"
      "global_load_lds_dwordx4 v203, s[70:71] offset:3072\n"
      "v_lshlrev_b32 v209, 16, v220\n"
      "v_mfma_f32_16x16x32_bf16 v[12:15], v[164:167], a[108:111], v[12:15]\n"
      "s_add_u32 s70, s70, 114688\n"
      "s_addc_u32 s71, s71, 0\n"
      "s_add_u32 m0, m0, 4096\n"
      "v_mfma_f32_16x16x32_bf16 v[16:19], v[164:167], a[112:115], v[16:19]\n"
      "global_load_lds_dwordx4 v200, s[70:71] offset:0\n"
      "v_and_b32 v216, 0xffff0000, v216\n"
      "v_mfma_f32_16x16x32_bf16 v[20:23], v[164:167], a[116:119], v[20:23]\n"
      "global_load_lds_dwordx4 v201, s[70:71] offset:1024\n"
      "v_and_b32 v220, 0xffff0000, v220\n"
      "v_mfma_f32_16x16x32_bf16 v[24:27], v[164:167], a[120:123], v[24:27]\n"
      "global_load_lds_dwordx4 v202, s[70:71] offset:2048\n"
      "v_mul_f32 v208, v208, v209\n"
      "v_mfma_f32_16x16x32_bf16 v[28:31], v[164:167], a[124:127], v[28:31]\n"
      "global_load_lds_dwordx4 v203, s[70:71] offset:3072\n"
      "v_lshrrev_b32 v201, 4, v243\n"
      "v_mfma_f32_16x16x32_bf16 v[32:35], v[168:171], a[96:99], v[32:35]\n"
      "global_load_dword v252, v253, s[58:59] offset:128 sc1\n"
      "v_bfe_u32 v224, v243, 2, 2\n"
      "v_mfma_f32_16x16x32_bf16 v[36:39], v[168:171], a[100:103], v[36:39]\n"
      "s_waitcnt vmcnt(14)\n"
      "ds_read_b128 a[216:219], v248 offset:4096\n"
      "v_mfma_f32_16x16x32_bf16 v[40:43], v[168:171], a[104:107], v[40:43]\n"
      "ds_read_b128 a[220:223], v248 offset:6144\n"
      "v_lshl_or_b32 v201, v201, 3, v224\n"
      "v_mfma_f32_16x16x32_bf16 v[44:47], v[168:171], a[108:111], v[44:47]\n"
      "ds_read_b128 a[248:251], v249 offset:4096\n"
      "v_and_b32 v224, 3, v243\n"
      "v_mfma_f32_16x16x32_bf16 v[48:51], v[168:171], a[112:115], v[48:51]\n"
      "ds_read_b128 a[252:255], v249 offset:6144\n"
      "v_lshlrev_b32 v224, 3, v224\n"
      "v_mfma_f32_16x16x32_bf16 v[52:55], v[168:171], a[116:119], v[52:55]\n"
      "s_sub_u32 s48, s33, 1\n"
      "s_and_b32 s48, s48, 3\n"
      "v_mfma_f32_16x16x32_bf16 v[56:59], v[168:171], a[120:123], v[56:59]\n"
      "s_lshl_b32 s48, s48, 14\n"
      "v_lshl_add_u32 v244, v243, 2, s48\n"
      "s_sub_u32 s48, s33, 2\n"
      "v_mfma_f32_16x16x32_bf16 v[60:63], v[168:171], a[124:127], v[60:63]\n"
      "s_and_b32 s48, s48, 3\n"
      "s_lshl_b32 s48, s48, 14\n"
      "v_lshl_add_u32 v245, v243, 2, s48\n"
      "v_mfma_f32_16x16x32_bf16 v[64:67], v[172:175], a[96:99], v[64:67]\n"
      "s_sub_u32 s48, s33, 3\n"
      "s_and_b32 s48, s48, 3\n"
      "s_lshl_b32 s48, s48, 14\n"
      "v_mfma_f32_16x16x32_bf16 v[68:71], v[172:175], a[100:103], v[68:71]\n"
      "s_mov_b32 m0, s41\n"
      "v_lshl_add_u32 v246, v243, 2, s48\n"
      "v_lshl_or_b32 v201, v201, 7, v224\n"
      "v_mfma_f32_16x16x32_bf16 v[72:75], v[172:175], a[104:107], v[72:75]\n"
      "s_mov_b32 s64, 0xffff0000\n"
      "s_mov_b32 s65, 0xffff0000\n"
      "s_mov_b32 s66, -1\n"
      "v_mfma_f32_16x16x32_bf16 v[76:79], v[172:175], a[108:111], v[76:79]\n"
      "s_mov_b32 s67, 0\n"
      "s_lshl_b32 s54, s33, 13\n"
      "s_add_u32 s54, s54, 131072\n"
      "v_mfma_f32_16x16x32_bf16 v[80:83], v[172:175], a[112:115], v[80:83]\n"
      "v_add_u32 v202, s54, v201\n"
      "s_and_b32 s54, s33, 1\n"
      "v_mfma_f32_16x16x32_bf16 v[84:87], v[172:175], a[116:119], v[84:87]\n"
      "s_lshl3_add_u32 s54, s54, s83\n"
      "s_lshl_b32 s54, s54, 8\n"
      "s_lshl_b32 s55, s80, 13\n"
      "v_mfma_f32_16x16x32_bf16 v[88:91], v[172:175], a[120:123], v[88:91]\n"
      "s_add_u32 s55, s55, 262144\n"
      "s_add_u32 s54, s54, s55\n"
      "v_bfe_u32 v224, v243, 4, 1\n"
      "v_mfma_f32_16x16x32_bf16 v[92:95], v[172:175], a[124:127], v[92:95]\n"
      "v_and_b32 v203, 0xf0, v242\n"
      "v_lshl_or_b32 v200, v224, 12, v203\n"
      "v_mfma_f32_16x16x32_bf16 v[0:3], v[176:179], a[128:131], v[0:3]\n"
      "v_add_u32 v200, s54, v200\n"
      "v_lshl_or_b32 v203, v224, 14, v203\n"
      "v_mfma_f32_16x16x32_bf16 v[4:7], v[176:179], a[132:135], v[4:7]\n"
      "v_mul_f32 v210, v216, v220\n"
      "v_cvt_pk_bf16_f32 v208, v208, v210\n"
      "s_waitcnt lgkmcnt(15)\n"
      "v_mfma_f32_16x16x32_bf16 v[8:11], v[176:179], a[136:139], v[8:11]\n"
      "v_lshlrev_b32 v209, 16, v208\n"
      "v_and_b32 v208, 0xffff0000, v208\n"
      "v_mfma_f32_16x16x32_bf16 v[12:15], v[176:179], a[140:143], v[12:15]\n"
      "v_add_f32 v211, v208, v209\n"
      "v_lshlrev_b32 v208, 16, v217\n"
      "v_mfma_f32_16x16x32_bf16 v[16:19], v[176:179], a[144:147], v[16:19]\n"
      "v_lshlrev_b32 v209, 16, v221\n"
      "v_and_b32 v217, 0xffff0000, v217\n"
      "v_mfma_f32_16x16x32_bf16 v[20:23], v[176:179], a[148:151], v[20:23]\n"
      "v_and_b32 v221, 0xffff0000, v221\n"
      "v_mul_f32 v208, v208, v209\n"
      "v_mfma_f32_16x16x32_bf16 v[24:27], v[176:179], a[152:155], v[24:27]\n"
      "v_mul_f32 v210, v217, v221\n"
      "v_cvt_pk_bf16_f32 v208, v208, v210\n"
      "v_mfma_f32_16x16x32_bf16 v[28:31], v[176:179], a[156:159], v[28:31]\n"
      "v_lshlrev_b32 v209, 16, v208\n"
      "v_and_b32 v208, 0xffff0000, v208\n"
      "v_mfma_f32_16x16x32_bf16 v[32:35], v[180:183], a[128:131], v[32:35]\n"
      "v_add_f32 v208, v208, v209\n"
      "v_add_f32 v211, v211, v208\n"
      "v_mfma_f32_16x16x32_bf16 v[36:39], v[180:183], a[132:135], v[36:39]\n"
      "v_lshlrev_b32 v208, 16, v218\n"
      "v_lshlrev_b32 v209, 16, v222\n"
      "v_mfma_f32_16x16x32_bf16 v[40:43], v[180:183], a[136:139], v[40:43]\n"
      "v_and_b32 v218, 0xffff0000, v218\n"
      "v_and_b32 v222, 0xffff0000, v222\n"
      "v_mfma_f32_16x16x32_bf16 v[44:47], v[180:183], a[140:143], v[44:47]\n"
      "v_mul_f32 v208, v208, v209\n"
      "v_mul_f32 v210, v218, v222\n"
      "v_mfma_f32_16x16x32_bf16 v[48:51], v[180:183], a[144:147], v[48:51]\n"
      "v_cvt_pk_bf16_f32 v208, v208, v210\n"
      "v_lshlrev_b32 v209, 16, v208\n"
      "v_mfma_f32_16x16x32_bf16 v[52:55], v[180:183], a[148:151], v[52:55]\n"
      "v_and_b32 v208, 0xffff0000, v208\n"
      "v_add_f32 v208, v208, v209\n"
      "v_mfma_f32_16x16x32_bf16 v[56:59], v[180:183], a[152:155], v[56:59]\n"
      "v_add_f32 v211, v211, v208\n"
      "v_lshlrev_b32 v208, 16, v219\n"
      "v_mfma_f32_16x16x32_bf16 v[60:63], v[180:183], a[156:159], v[60:63]\n"
      "v_lshlrev_b32 v209, 16, v223\n"
      "v_and_b32 v219, 0xffff0000, v219\n"
      "v_mfma_f32_16x16x32_bf16 v[64:67], v[184:187], a[128:131], v[64:67]\n"
      "v_and_b32 v223, 0xffff0000, v223\n"
      "v_mul_f32 v208, v208, v209\n"
      "v_mfma_f32_16x16x32_bf16 v[68:71], v[184:187], a[132:135], v[68:71]\n"
      "v_mul_f32 v210, v219, v223\n"
      "v_cvt_pk_bf16_f32 v208, v208, v210\n"
      "v_mfma_f32_16x16x32_bf16 v[72:75], v[184:187], a[136:139], v[72:75]\n"
      "v_lshlrev_b32 v209, 16, v208\n"
      "v_and_b32 v208, 0xffff0000, v208\n"
      "v_mfma_f32_16x16x32_bf16 v[76:79], v[184:187], a[140:143], v[76:79]\n"
      "v_add_f32 v208, v208, v209\n"
      "v_add_f32 v211, v211, v208\n"
      "v_mfma_f32_16x16x32_bf16 v[80:83], v[184:187], a[144:147], v[80:83]\n"
      "s_nop 0\n"
      "v_add_f32_dpp v211, v211, v211 quad_perm:[1,0,3,2] row_mask:0xf bank_mask:0xf\n"
      "v_mfma_f32_16x16x32_bf16 v[84:87], v[184:187], a[148:151], v[84:87]\n"
      "s_nop 0\n"
      "v_add_f32_dpp v211, v211, v211 quad_perm:[2,3,0,1] row_mask:0xf bank_mask:0xf\n"
      "v_mfma_f32_16x16x32_bf16 v[88:91], v[184:187], a[152:155], v[88:91]\n"
      "s_nop 0\n"
      "v_add_f32_dpp v211, v211, v211 row_half_mirror row_mask:0xf bank_mask:0xf\n"
      "v_mfma_f32_16x16x32_bf16 v[92:95], v[184:187], a[156:159], v[92:95]\n"
      "s_nop 0\n"
      "v_add_f32_dpp v211, v211, v211 row_mirror row_mask:0xf bank_mask:0xf\n"
      "v_mfma_f32_16x16x32_bf16 v[0:3], v[188:191], a[160:163], v[0:3]\n"
      "v_mov_b32 v208, v211\n"
      "v_mfma_f32_16x16x32_bf16 v[4:7], v[188:191], a[164:167], v[4:7]\n"
      "s_nop 0\n"
      "v_permlane16_swap_b32 v211, v208\n"
      "v_mfma_f32_16x16x32_bf16 v[8:11], v[188:191], a[168:171], v[8:11]\n"
      "v_add_f32 v211, v211, v208\n"
      "v_mov_b32 v208, v211\n"
      "v_mfma_f32_16x16x32_bf16 v[12:15], v[188:191], a[172:175], v[12:15]\n"
      "s_nop 0\n"
      "v_permlane32_swap_b32 v211, v208\n"
      "v_mfma_f32_16x16x32_bf16 v[16:19], v[188:191], a[176:179], v[16:19]\n"
      "v_add_f32 v211, v211, v208\n"
      "v_cvt_pk_bf16_f32 v211, v211, v211\n"
      "v_mfma_f32_16x16x32_bf16 v[20:23], v[188:191], a[180:183], v[20:23]\n"
      "v_and_b32 v211, 0xffff0000, v211\n"
      "v_writelane_b32 v211, s84, 1\n"
      "v_mfma_f32_16x16x32_bf16 v[24:27], v[188:191], a[184:187], v[24:27]\n"
      "v_mul_f32 v211, 0xbfb8aa3b, v211\n"
      "v_exp_f32 v211, v211\n"
      "v_mfma_f32_16x16x32_bf16 v[28:31], v[188:191], a[188:191], v[28:31]\n"
      "v_add_f32 v211, 1.0, v211\n"
      "v_rcp_f32 v211, v211\n"
      "v_mfma_f32_16x16x32_bf16 v[32:35], v[128:131], a[160:163], v[32:35]\n"
      "v_cvt_pk_bf16_f32 v211, v211, v211\n"
      "v_and_b32 v211, 0xffff0000, v211\n"
      "v_mfma_f32_16x16x32_bf16 v[36:39], v[128:131], a[164:167], v[36:39]\n"
      "s_waitcnt vmcnt(1)\n"
      "v_readfirstlane_b32 s39, v211\n"
      "v_mfma_f32_16x16x32_bf16 v[40:43], v[128:131], a[168:171], v[40:43]\n"
      "ds_read_b64_tr_b16 v[224:225], v202 offset:0\n"
      "v_readlane_b32 s84, v211, 1\n"
      "v_mfma_f32_16x16x32_bf16 v[44:47], v[128:131], a[172:175], v[44:47]\n"
      "ds_read_b64_tr_b16 v[226:227], v202 offset:512\n"
      "ds_read_b64_tr_b16 v[228:229], v202 offset:4096\n"
      "v_mfma_f32_16x16x32_bf16 v[48:51], v[128:131], a[176:179], v[48:51]\n"
      "ds_read_b64_tr_b16 v[230:231], v202 offset:4608\n"
      "ds_read_b64_tr_b16 v[232:233], v202 offset:64\n"
      "v_mfma_f32_16x16x32_bf16 v[52:55], v[128:131], a[180:183], v[52:55]\n"
      "ds_read_b64_tr_b16 v[234:235], v202 offset:576\n"
      "ds_read_b64_tr_b16 v[236:237], v202 offset:4160\n"
      "v_mfma_f32_16x16x32_bf16 v[56:59], v[128:131], a[184:187], v[56:59]\n"
      "ds_read_b64_tr_b16 v[238:239], v202 offset:4672\n"
      "v_mfma_f32_16x16x32_bf16 v[60:63], v[128:131], a[188:191], v[60:63]\n"
      "v_mfma_f32_16x16x32_bf16 v[64:67], v[132:135], a[160:163], v[64:67]\n"
      "v_mfma_f32_16x16x32_bf16 v[68:71], v[132:135], a[164:167], v[68:71]\n"
      "s_waitcnt lgkmcnt(6)\n"
      "v_mfma_f32_16x16x32_bf16 v[248:251], v[192:195], v[224:227], 0\n"
      "v_mfma_f32_16x16x32_bf16 v[72:75], v[132:135], a[168:171], v[72:75]\n"
      "s_waitcnt vmcnt(0)\n"
      "v_cmp_ne_u32 vcc, s37, v252\n"
      "s_nop 1\n"
      "s_cbranch_vccnz .Ls_s10_%=\n"
      ".Ls_o11_%=:\n"
      "v_mfma_f32_16x16x32_bf16 v[76:79], v[132:135], a[172:175], v[76:79]\n"
      "s_waitcnt lgkmcnt(4)\n"
      "v_mfma_f32_16x16x32_bf16 v[248:251], v[196:199], v[228:231], v[248:251]\n"
      "v_mfma_f32_16x16x32_bf16 v[80:83], v[132:135], a[176:179], v[80:83]\n"
      "global_load_dwordx4 v[160:163], v240, s[10:11] offset:-1024 sc1\n"
      "global_load_dwordx4 v[164:167], v240, s[72:73] offset:-1024 sc1\n"
      "v_mfma_f32_16x16x32_bf16 v[84:87], v[132:135], a[180:183], v[84:87]\n"
      "global_load_dwordx4 v[168:171], v241, s[10:11] offset:-1024 sc1\n"
      "global_load_dwordx4 v[172:175], v241, s[72:73] offset:-1024 sc1\n"
      "v_mfma_f32_16x16x32_bf16 v[88:91], v[132:135], a[184:187], v[88:91]\n"
      "global_load_dwordx4 v[176:179], v240, s[14:15] offset:-1024 sc1\n"
      "global_load_dwordx4 v[180:183], v240, s[74:75] offset:-1024 sc1\n"
      "v_mfma_f32_16x16x32_bf16 v[92:95], v[132:135], a[188:191], v[92:95]\n"
      "global_load_dwordx4 v[184:187], v241, s[14:15] offset:-1024 sc1\n"
      "global_load_dwordx4 v[188:191], v241, s[74:75] offset:-1024 sc1\n"
      "v_mfma_f32_16x16x32_bf16 v[0:3], v[136:139], a[192:195], v[0:3]\n"
      "global_load_dwordx4 v[128:131], v240, s[10:11] offset:0 sc1\n"
      "global_load_dwordx4 v[132:135], v240, s[72:73] offset:0 sc1\n"
      "v_mfma_f32_16x16x32_bf16 v[4:7], v[136:139], a[196:199], v[4:7]\n"
      "s_waitcnt lgkmcnt(2)\n"
      "v_mfma_f32_16x16x32_bf16 v[224:227], v[192:195], v[232:235], 0\n"
      "v_mfma_f32_16x16x32_bf16 v[8:11], v[136:139], a[200:203], v[8:11]\n"
      "s_waitcnt lgkmcnt(0)\n"
      "v_mfma_f32_16x16x32_bf16 v[224:227], v[196:199], v[236:239], v[224:227]\n"
      "v_mfma_f32_16x16x32_bf16 v[12:15], v[136:139], a[204:207], v[12:15]\n"
      "v_mfma_f32_16x16x32_bf16 v[16:19], v[136:139], a[208:211], v[16:19]\n"
      "v_mfma_f32_16x16x32_bf16 v[20:23], v[136:139], a[212:215], v[20:23]\n"
      "s_nop 4\n"
      "v_cndmask_b32 v247, v248, v224, s[64:65]\n"
      "v_mfma_f32_16x16x32_bf16 v[24:27], v[136:139], a[216:219], v[24:27]\n"
      "ds_read_b64_tr_b16 v[224:225], v202 offset:32\n"
      "ds_read_b64_tr_b16 v[226:227], v202 offset:544\n"
      "v_mfma_f32_16x16x32_bf16 v[28:31], v[136:139], a[220:223], v[28:31]\n"
      "global_load_dwordx4 v[136:139], v241, s[10:11] offset:0 sc1\n"
      "ds_read_b64_tr_b16 v[228:229], v202 offset:4128\n"
      "v_mfma_f32_16x16x32_bf16 v[0:3], v[140:143], a[224:227], v[0:3]\n"
      "ds_read_b64_tr_b16 v[230:231], v202 offset:4640\n"
      "ds_read_b64_tr_b16 v[232:233], v202 offset:96\n"
      "v_mfma_f32_16x16x32_bf16 v[4:7], v[140:143], a[228:231], v[4:7]\n"
      "ds_read_b64_tr_b16 v[234:235], v202 offset:608\n"
      "ds_read_b64_tr_b16 v[236:237], v202 offset:4192\n"
      "v_mfma_f32_16x16x32_bf16 v[8:11], v[140:143], a[232:235], v[8:11]\n"
      "ds_read_b64_tr_b16 v[238:239], v202 offset:4704\n"
      "v_mfma_f32_16x16x32_bf16 v[12:15], v[140:143], a[236:239], v[12:15]\n"
      "v_mfma_f32_16x16x32_bf16 v[16:19], v[140:143], a[240:243], v[16:19]\n"
      "v_mfma_f32_16x16x32_bf16 v[20:23], v[140:143], a[244:247], v[20:23]\n"
      "s_waitcnt lgkmcnt(6)\n"
      "v_mfma_f32_16x16x32_bf16 v[248:251], v[192:195], v[224:227], 0\n"
      "v_mfma_f32_16x16x32_bf16 v[24:27], v[140:143], a[248:251], v[24:27]\n"
      "s_waitcnt lgkmcnt(4)\n"
      "v_mfma_f32_16x16x32_bf16 v[248:251], v[196:199], v[228:231], v[248:251]\n"
      "v_mfma_f32_16x16x32_bf16 v[28:31], v[140:143], a[252:255], v[28:31]\n"
      "global_load_dwordx4 v[140:143], v241, s[72:73] offset:0 sc1\n"
      "v_cvt_pk_bf16_f32 v8, v8, v9\n"
      "v_mfma_f32_16x16x32_bf16 v[32:35], v[144:147], a[192:195], v[32:35]\n"
      "s_waitcnt lgkmcnt(2)\n"
      "v_mfma_f32_16x16x32_bf16 v[224:227], v[192:195], v[232:235], 0\n"
      "v_mfma_f32_16x16x32_bf16 v[36:39], v[144:147], a[196:199], v[36:39]\n"
      "s_waitcnt lgkmcnt(0)\n"
      "v_mfma_f32_16x16x32_bf16 v[224:227], v[196:199], v[236:239], v[224:227]\n"
      "v_mfma_f32_16x16x32_bf16 v[40:43], v[144:147], a[200:203], v[40:43]\n"
      "v_cvt_pk_bf16_f32 v9, v10, v11\n"
      "ds_write_addtid_b32 v8 offset:0\n"
      "v_mfma_f32_16x16x32_bf16 v[44:47], v[144:147], a[204:207], v[44:47]\n"
      "v_cvt_pk_bf16_f32 v10, v12, v13\n"
      "ds_write_addtid_b32 v9 offset:256\n"
      "v_mfma_f32_16x16x32_bf16 v[48:51], v[144:147], a[208:211], v[48:51]\n"
      "s_nop 0\n"
      "v_cndmask_b32 v250, v248, v224, s[64:65]\n"
      "v_mfma_f32_16x16x32_bf16 v[52:55], v[144:147], a[212:215], v[52:55]\n"
      "v_and_b32 v248, s86, v247\n"
      "v_mov_b32 v249, s37\n"
      "v_mfma_f32_16x16x32_bf16 v[56:59], v[144:147], a[216:219], v[56:59]\n"
      "v_and_b32 v250, s86, v250\n"
      "v_mov_b32 v251, s37\n"
      "v_mfma_f32_16x16x32_bf16 v[60:63], v[144:147], a[220:223], v[60:63]\n"
      "global_load_dwordx4 v[144:147], v240, s[14:15] offset:0 sc1\n"
      "v_cvt_pk_bf16_f32 v11, v14, v15\n"
      "v_mfma_f32_16x16x32_bf16 v[32:35], v[148:151], a[224:227], v[32:35]\n"
      "s_mov_b64 exec, s[66:67]\n"
      "global_store_dwordx4 v200, v[248:251], s[58:59] sc1\n"
      "s_mov_b64 exec, -1\n"
      "v_mfma_f32_16x16x32_bf16 v[36:39], v[148:151], a[228:231], v[36:39]\n"
      "ds_write_addtid_b32 v10 offset:512\n"
      "v_bfe_u32 v248, v243, 1, 3\n"
      "v_mfma_f32_16x16x32_bf16 v[40:43], v[148:151], a[232:235], v[40:43]\n"
      "v_lshrrev_b32 v249, 4, v243\n"
      "v_lshlrev_b32 v249, 1, v249\n"
      "v_mfma_f32_16x16x32_bf16 v[44:47], v[148:151], a[236:239], v[44:47]\n"
      "v_sub_u32 v248, v248, v249\n"
      "v_and_b32 v249, 16, v242\n"
      "v_mfma_f32_16x16x32_bf16 v[48:51], v[148:151], a[240:243], v[48:51]\n"
      "v_mov_b32 v254, 0x3f80\n"
      "v_lshlrev_b32 v249, v249, v254\n"
      "v_mfma_f32_16x16x32_bf16 v[52:55], v[148:151], a[244:247], v[52:55]\n"
      "v_cmp_eq_u32 vcc, 0, v248\n"
      "s_nop 1\n"
      "v_cndmask_b32 v250, 0, v249, vcc\n"
      "v_mfma_f32_16x16x32_bf16 v[56:59], v[148:151], a[248:251], v[56:59]\n"
      "v_cmp_eq_u32 vcc, 1, v248\n"
      "s_nop 1\n"
      "v_cndmask_b32 v251, 0, v249, vcc\n"
      "v_mfma_f32_16x16x32_bf16 v[60:63], v[148:151], a[252:255], v[60:63]\n"
      "global_load_dwordx4 v[148:151], v240, s[74:75] offset:0 sc1\n"
      "v_mov_b32 v252, v250\n"
      "v_mfma_f32_16x16x32_bf16 v[64:67], v[152:155], a[192:195], v[64:67]\n"
      "v_mov_b32 v253, v251\n"
      "v_mov_b32 v248, 0\n"
      "v_mfma_f32_16x16x32_bf16 v[68:71], v[152:155], a[196:199], v[68:71]\n"
      "v_mov_b32 v249, 0\n"
      "v_mov_b32 v254, 0\n"
      "v_mfma_f32_16x16x32_bf16 v[72:75], v[152:155], a[200:203], v[72:75]\n"
      "v_mov_b32 v255, 0\n"
      "s_add_u32 s48, s34, 1\n"
      "v_mfma_f32_16x16x32_bf16 v[76:79], v[152:155], a[204:207], v[76:79]\n"
      "s_and_b32 s48, s48, 3\n"
      "s_mul_i32 s48, s48, 36864\n"
      "s_add_u32 s48, s48, 0\n"
      "v_mfma_f32_16x16x32_bf16 v[80:83], v[152:155], a[208:211], v[80:83]\n"
      "s_add_u32 s52, s60, s48\n"
      "s_addc_u32 s53, s61, 0\n"
      "v_cvt_pk_bf16_f32 v12, v16, v17\n"
      "v_mfma_f32_16x16x32_bf16 v[84:87], v[152:155], a[212:215], v[84:87]\n"
      "s_lshl2_add_u32 s54, s36, s33\n"
      "ds_write_addtid_b32 v11 offset:768\n"
      "v_cvt_pk_bf16_f32 v13, v18, v19\n"
      "v_mfma_f32_16x16x32_bf16 v[88:91], v[152:155], a[216:219], v[88:91]\n"
      "s_lshl_b32 s55, s54, 15\n"
      "ds_write_addtid_b32 v12 offset:1024\n"
      "v_cvt_pk_bf16_f32 v14, v20, v21\n"
      "v_mfma_f32_16x16x32_bf16 v[92:95], v[152:155], a[220:223], v[92:95]\n"
      "s_lshl2_add_u32 s56, s35, s34\n"
      "global_load_dwordx4 v[152:155], v241, s[14:15] offset:0 sc1\n"
      "s_lshl_b32 s56, s56, 6\n"
      "v_mfma_f32_16x16x32_bf16 v[64:67], v[156:159], a[224:227], v[64:67]\n"
      "s_add_u32 s55, s55, s56\n"
      "ds_write_addtid_b32 v13 offset:1280\n"
      "v_cvt_pk_bf16_f32 v15, v22, v23\n"
      "v_mfma_f32_16x16x32_bf16 v[68:71], v[156:159], a[228:231], v[68:71]\n"
      "ds_write_addtid_b32 v14 offset:1536\n"
      "v_cvt_pk_bf16_f32 v16, v24, v25\n"
      "v_mfma_f32_16x16x32_bf16 v[72:75], v[156:159], a[232:235], v[72:75]\n"
      "ds_write_addtid_b32 v15 offset:1792\n"
      "v_cvt_pk_bf16_f32 v17, v26, v27\n"
      "v_mfma_f32_16x16x32_bf16 v[76:79], v[156:159], a[236:239], v[76:79]\n"
      "ds_write_addtid_b32 v16 offset:2048\n"
      "v_cvt_pk_bf16_f32 v18, v28, v29\n"
      "v_mfma_f32_16x16x32_bf16 v[80:83], v[156:159], a[240:243], v[80:83]\n"
      "ds_write_addtid_b32 v17 offset:2304\n"
      "v_cvt_pk_bf16_f32 v19, v30, v31\n"
      "v_mfma_f32_16x16x32_bf16 v[84:87], v[156:159], a[244:247], v[84:87]\n"
      "ds_write_addtid_b32 v18 offset:2560\n"
      "v_and_b32 v209, 15, v243\n"
      "v_mfma_f32_16x16x32_bf16 v[88:91], v[156:159], a[248:251], v[88:91]\n"
      "ds_write_addtid_b32 v19 offset:2816\n"
      "v_cvt_pk_bf16_f32 v40, v40, v41\n"
      "v_mfma_f32_16x16x32_bf16 v[92:95], v[156:159], a[252:255], v[92:95]\n"
      "s_waitcnt vmcnt(14)\n"
      "global_load_dwordx4 v[156:159], v241, s[74:75] offset:0 sc1\n"
      "v_cvt_pk_bf16_f32 v41, v42, v43\n"
      "v_mfma_f32_16x16x32_bf16 v[96:99], v[160:163], a[0:3], 0\n"
      "ds_write_addtid_b32 v40 offset:3072\n"
      "v_cvt_pk_bf16_f32 v42, v44, v45\n"
      "v_mfma_f32_16x16x32_bf16 v[100:103], v[160:163], a[4:7], 0\n"
      "ds_write_addtid_b32 v41 offset:3328\n"
      "v_cvt_pk_bf16_f32 v43, v46, v47\n"
      "v_mfma_f32_16x16x32_bf16 v[104:107], v[160:163], a[8:11], 0\n"
      "ds_write_addtid_b32 v42 offset:3584\n"
      "v_cvt_pk_bf16_f32 v44, v48, v49\n"
      "v_mfma_f32_16x16x32_bf16 v[108:111], v[160:163], a[12:15], 0\n"
      "ds_write_addtid_b32 v43 offset:3840\n"
      "v_cvt_pk_bf16_f32 v45, v50, v51\n"
      "v_mfma_f32_16x16x32_bf16 v[112:115], v[160:163], a[16:19], 0\n"
      "ds_write_addtid_b32 v44 offset:4096\n"
      "v_cvt_pk_bf16_f32 v46, v52, v53\n"
      "v_mfma_f32_16x16x32_bf16 v[116:119], v[160:163], a[20:23], 0\n"
      "ds_write_addtid_b32 v45 offset:4352\n"
      "v_cvt_pk_bf16_f32 v47, v54, v55\n"
      "v_mfma_f32_16x16x32_bf16 v[120:123], v[160:163], a[24:27], 0\n"
      "ds_write_addtid_b32 v46 offset:4608\n"
      "v_cvt_pk_bf16_f32 v48, v56, v57\n"
      "v_mfma_f32_16x16x32_bf16 v[124:127], v[160:163], a[28:31], 0\n"
      "global_load_dwordx4 v[160:163], v240, s[10:11] offset:1024 sc1\n"
      "v_and_b32 v208, 48, v243\n"
      "v_mfma_f32_16x16x32_bf16 v[96:99], v[164:167], a[32:35], v[96:99]\n"
      "ds_write_addtid_b32 v47 offset:4864\n"
      "v_cvt_pk_bf16_f32 v49, v58, v59\n"
      "v_mfma_f32_16x16x32_bf16 v[100:103], v[164:167], a[36:39], v[100:103]\n"
      "ds_write_addtid_b32 v48 offset:5120\n"
      "v_cvt_pk_bf16_f32 v50, v60, v61\n"
      "v_mfma_f32_16x16x32_bf16 v[104:107], v[164:167], a[40:43], v[104:107]\n"
      "ds_write_addtid_b32 v49 offset:5376\n"
      "v_cvt_pk_bf16_f32 v51, v62, v63\n"
      "v_mfma_f32_16x16x32_bf16 v[108:111], v[164:167], a[44:47], v[108:111]\n"
      "ds_write_addtid_b32 v50 offset:5632\n"
      "v_lshl_or_b32 v208, v209, 10, v208\n"
      "v_mfma_f32_16x16x32_bf16 v[112:115], v[164:167], a[48:51], v[112:115]\n"
      "ds_write_addtid_b32 v51 offset:5888\n"
      "v_add_u32 v208, s55, v208\n"
      "v_mfma_f32_16x16x32_bf16 v[116:119], v[164:167], a[52:55], v[116:119]\n"
      "v_add_u32 v210, 16384, v208\n"
      "global_load_dwordx4 v[216:219], v208, s[16:17]\n"
      "v_mfma_f32_16x16x32_bf16 v[120:123], v[164:167], a[56:59], v[120:123]\n"
      "global_load_dwordx4 v[220:223], v210, s[16:17]\n"
      "s_lshr_b32 s55, s34, 1\n"
      "v_mfma_f32_16x16x32_bf16 v[124:127], v[164:167], a[60:63], v[124:127]\n"
      "s_waitcnt vmcnt(16)\n"
      "global_load_dwordx4 v[164:167], v240, s[72:73] offset:1024 sc1\n"
      "s_lshl2_add_u32 s55, s55, s35\n"
      "v_mfma_f32_16x16x32_bf16 v[96:99], v[168:171], a[64:67], v[96:99]\n"
      "s_lshl_b32 s55, s55, 15\n"
      "s_waitcnt lgkmcnt(0)\n"
      "s_barrier\n"
      "v_mfma_f32_16x16x32_bf16 v[100:103], v[168:171], a[68:71], v[100:103]\n"
      "s_lshl_b32 s54, s54, 9\n"
      "ds_read2st64_b32 v[20:21], v244 offset0:0 offset1:1\n"
      "s_add_u32 s54, s54, s55\n"
      "v_mfma_f32_16x16x32_bf16 v[104:107], v[168:171], a[72:75], v[104:107]\n"
      "s_add_u32 s54, s54, 786432\n"
      "ds_read2st64_b32 v[22:23], v245 offset0:4 offset1:5\n"
      "v_lshl_add_u32 v209, v209, 4, s54\n"
      "v_mfma_f32_16x16x32_bf16 v[108:111], v[168:171], a[76:79], v[108:111]\n"
      "ds_read2st64_b32 v[24:25], v244 offset0:2 offset1:3\n"
      "ds_read2st64_b32 v[26:27], v245 offset0:6 offset1:7\n"
      "v_mfma_f32_16x16x32_bf16 v[112:115], v[168:171], a[80:83], v[112:115]\n"
      "ds_read2st64_b32 v[28:29], v246 offset0:8 offset1:9\n"
      "ds_read2st64_b32 v[30:31], v246 offset0:10 offset1:11\n"
      "v_mfma_f32_16x16x32_bf16 v[116:119], v[168:171], a[84:87], v[116:119]\n"
      "ds_read2st64_b32 v[52:53], v244 offset0:12 offset1:13\n"
      "ds_read2st64_b32 v[54:55], v245 offset0:16 offset1:17\n"
      "v_mfma_f32_16x16x32_bf16 v[120:123], v[168:171], a[88:91], v[120:123]\n"
      "ds_read2st64_b32 v[56:57], v244 offset0:14 offset1:15\n"
      "ds_read2st64_b32 v[58:59], v245 offset0:18 offset1:19\n"
      "v_mfma_f32_16x16x32_bf16 v[124:127], v[168:171], a[92:95], v[124:127]\n"
      "global_load_dwordx4 v[168:171], v241, s[10:11] offset:1024 sc1\n"
      "ds_read2st64_b32 v[60:61], v246 offset0:20 offset1:21\n"
      "v_mfma_f32_16x16x32_bf16 v[96:99], v[172:175], a[96:99], v[96:99]\n"
      "ds_read2st64_b32 v[62:63], v246 offset0:22 offset1:23\n"
      "v_cvt_pk_bf16_f32 v72, v72, v73\n"
      "v_mfma_f32_16x16x32_bf16 v[100:103], v[172:175], a[100:103], v[100:103]\n"
      "s_waitcnt lgkmcnt(6)\n"
      "v_mfma_f32_16x16x32_bf16 v[0:3], v[250:253], v[20:23], v[0:3]\n"
      "v_cvt_pk_bf16_f32 v73, v74, v75\n"
      "v_mfma_f32_16x16x32_bf16 v[104:107], v[172:175], a[104:107], v[104:107]\n"
      "v_mfma_f32_16x16x32_bf16 v[4:7], v[250:253], v[24:27], v[4:7]\n"
      "v_mfma_f32_16x16x32_bf16 v[0:3], v[252:255], v[28:31], v[0:3]\n"
      "v_mfma_f32_16x16x32_bf16 v[108:111], v[172:175], a[108:111], v[108:111]\n"
      "v_mfma_f32_16x16x32_bf16 v[4:7], v[248:251], v[28:31], v[4:7]\n"
      "v_mov_b32 v11, s37\n"
      "v_mfma_f32_16x16x32_bf16 v[112:115], v[172:175], a[112:115], v[112:115]\n"
      "ds_write_addtid_b32 v72 offset:6144\n"
      "v_cvt_pk_bf16_f32 v74, v76, v77\n"
      "v_mfma_f32_16x16x32_bf16 v[116:119], v[172:175], a[116:119], v[116:119]\n"
      "ds_write_addtid_b32 v73 offset:6400\n"
      "v_cvt_pk_bf16_f32 v8, v0, v1\n"
      "v_mfma_f32_16x16x32_bf16 v[120:123], v[172:175], a[120:123], v[120:123]\n"
      "global_load_dwordx2 v[204:205], v209, s[58:59] offset:0 sc1\n"
      "v_cvt_pk_bf16_f32 v9, v2, v3\n"
      "v_mfma_f32_16x16x32_bf16 v[124:127], v[172:175], a[124:127], v[124:127]\n"
      "s_waitcnt vmcnt(17)\n"
      "global_load_dwordx4 v[172:175], v241, s[72:73] offset:1024 sc1\n"
      "v_cvt_pk_bf16_f32 v10, v4, v5\n"
      "v_mfma_f32_16x16x32_bf16 v[96:99], v[176:179], a[128:131], v[96:99]\n"
      "global_load_dwordx2 v[206:207], v209, s[58:59] offset:256 sc1\n"
      "v_cvt_pk_bf16_f32 v247, v6, v7\n"
      "v_mfma_f32_16x16x32_bf16 v[100:103], v[176:179], a[132:135], v[100:103]\n"
      "s_add_u32 s68, s20, 16384\n"
      "s_addc_u32 s69, s21, 0\n"
      "global_store_dwordx4 v242, v[8:11], s[52:53] offset:0 sc1\n"
      "v_mfma_f32_16x16x32_bf16 v[104:107], v[176:179], a[136:139], v[104:107]\n"
      "s_waitcnt lgkmcnt(2)\n"
      "v_mfma_f32_16x16x32_bf16 v[32:35], v[250:253], v[52:55], v[32:35]\n"
      "v_cvt_pk_bf16_f32 v75, v78, v79\n"
      "v_mfma_f32_16x16x32_bf16 v[108:111], v[176:179], a[140:143], v[108:111]\n"
      "v_mfma_f32_16x16x32_bf16 v[36:39], v[250:253], v[56:59], v[36:39]\n"
      "v_lshrrev_b32 v208, 10, v208\n"
      "v_mfma_f32_16x16x32_bf16 v[112:115], v[176:179], a[144:147], v[112:115]\n"
      "v_mfma_f32_16x16x32_bf16 v[32:35], v[252:255], v[60:63], v[32:35]\n"
      "v_subrev_u32 v208, s42, v208\n"
      "v_mfma_f32_16x16x32_bf16 v[116:119], v[176:179], a[148:151], v[116:119]\n"
      "v_mfma_f32_16x16x32_bf16 v[36:39], v[248:251], v[60:63], v[36:39]\n"
      "v_mov_b32 v43, s37\n"
      "v_mfma_f32_16x16x32_bf16 v[120:123], v[176:179], a[152:155], v[120:123]\n"
      "ds_write_addtid_b32 v74 offset:6656\n"
      "v_mov_b32 v44, v247\n"
      "v_mfma_f32_16x16x32_bf16 v[124:127], v[176:179], a[156:159], v[124:127]\n"
      "global_load_dwordx4 v[176:179], v240, s[14:15] offset:1024 sc1\n"
      "v_mov_b32 v47, s37\n"
      "v_mfma_f32_16x16x32_bf16 v[96:99], v[180:183], a[160:163], v[96:99]\n"
      "v_cvt_pk_bf16_f32 v76, v80, v81\n"
      "v_mfma_f32_16x16x32_bf16 v[100:103], v[180:183], a[164:167], v[100:103]\n"
      "ds_write_addtid_b32 v75 offset:6912\n"
      "v_cvt_pk_bf16_f32 v40, v32, v33\n"
      "v_mfma_f32_16x16x32_bf16 v[104:107], v[180:183], a[168:171], v[104:107]\n"
      "v_cvt_pk_bf16_f32 v41, v34, v35\n"
      "v_cvt_pk_bf16_f32 v42, v36, v37\n"
      "v_mfma_f32_16x16x32_bf16 v[108:111], v[180:183], a[172:175], v[108:111]\n"
      "v_cvt_pk_bf16_f32 v45, v38, v39\n"
      "v_cvt_pk_bf16_f32 v77, v82, v83\n"
      "v_mfma_f32_16x16x32_bf16 v[112:115], v[180:183], a[176:179], v[112:115]\n"
      "global_store_dwordx4 v242, v[40:43], s[52:53] offset:1024 sc1\n"
      "v_mfma_f32_16x16x32_bf16 v[116:119], v[180:183], a[180:183], v[116:119]\n"
      "global_store_dwordx4 v242, v[44:47], s[52:53] offset:2048 sc1\n"
      "s_add_u32 s48, s34, 2\n"
      "v_mfma_f32_16x16x32_bf16 v[120:123], v[180:183], a[184:187], v[120:123]\n"
      "s_and_b32 s48, s48, 3\n"
      "ds_write_addtid_b32 v76 offset:7168\n"
      "s_mul_i32 s48, s48, 36864\n"
      "v_mfma_f32_16x16x32_bf16 v[124:127], v[180:183], a[188:191], v[124:127]\n"
      "s_waitcnt vmcnt(21)\n"
      "global_load_dwordx4 v[180:183], v240, s[74:75] offset:1024 sc1\n"
      "s_add_u32 s48, s48, 12288\n"
      "v_mfma_f32_16x16x32_bf16 v[96:99], v[184:187], a[192:195], v[96:99]\n"
      "s_add_u32 s52, s60, s48\n"
      "s_addc_u32 s53, s61, 0\n"
      "v_cvt_pk_bf16_f32 v78, v84, v85\n"
      "v_mfma_f32_16x16x32_bf16 v[100:103], v[184:187], a[196:199], v[100:103]\n"
      "ds_write_addtid_b32 v77 offset:7424\n"
      "v_cvt_pk_bf16_f32 v79, v86, v87\n"
      "v_mfma_f32_16x16x32_bf16 v[104:107], v[184:187], a[200:203], v[104:107]\n"
      "ds_write_addtid_b32 v78 offset:7680\n"
      "v_cvt_pk_bf16_f32 v80, v88, v89\n"
      "v_mfma_f32_16x16x32_bf16 v[108:111], v[184:187], a[204:207], v[108:111]\n"
      "ds_write_addtid_b32 v79 offset:7936\n"
      "v_cvt_pk_bf16_f32 v81, v90, v91\n"
      "v_mfma_f32_16x16x32_bf16 v[112:115], v[184:187], a[208:211], v[112:115]\n"
      "ds_write_addtid_b32 v80 offset:8192\n"
      "v_cvt_pk_bf16_f32 v82, v92, v93\n"
      "v_mfma_f32_16x16x32_bf16 v[116:119], v[184:187], a[212:215], v[116:119]\n"
      "ds_write_addtid_b32 v81 offset:8448\n"
      "v_cvt_pk_bf16_f32 v83, v94, v95\n"
      "v_mfma_f32_16x16x32_bf16 v[120:123], v[184:187], a[216:219], v[120:123]\n"
      "ds_write_addtid_b32 v82 offset:8704\n"
      "v_mfma_f32_16x16x32_bf16 v[124:127], v[184:187], a[220:223], v[124:127]\n"
      "global_load_dwordx4 v[184:187], v241, s[14:15] offset:1024 sc1\n"
      "v_mfma_f32_16x16x32_bf16 v[96:99], v[188:191], a[224:227], v[96:99]\n"
      "ds_write_addtid_b32 v83 offset:8960\n"
      "v_mfma_f32_16x16x32_bf16 v[100:103], v[188:191], a[228:231], v[100:103]\n"
      "v_mfma_f32_16x16x32_bf16 v[104:107], v[188:191], a[232:235], v[104:107]\n"
      "v_mfma_f32_16x16x32_bf16 v[108:111], v[188:191], a[236:239], v[108:111]\n"
      "v_mfma_f32_16x16x32_bf16 v[112:115], v[188:191], a[240:243], v[112:115]\n"
      "v_mfma_f32_16x16x32_bf16 v[116:119], v[188:191], a[244:247], v[116:119]\n"
      "v_mfma_f32_16x16x32_bf16 v[120:123], v[188:191], a[248:251], v[120:123]\n"
      "s_nop 3\n"
      "v_cvt_pk_bf16_f32 v104, v104, v105\n"
      "v_mfma_f32_16x16x32_bf16 v[124:127], v[188:191], a[252:255], v[124:127]\n"
      "s_waitcnt vmcnt(21)\n"
      "global_load_dwordx4 v[188:191], v241, s[74:75] offset:1024 sc1\n"
      "v_cvt_pk_bf16_f32 v105, v106, v107\n"
      "v_mfma_f32_16x16x32_bf16 v[0:3], v[128:131], a[0:3], 0\n"
      "ds_write_addtid_b32 v104 offset:9216\n"
      "v_cvt_pk_bf16_f32 v106, v108, v109\n"
      "v_mfma_f32_16x16x32_bf16 v[4:7], v[128:131], a[4:7], 0\n"
      "ds_write_addtid_b32 v105 offset:9472\n"
      "v_cvt_pk_bf16_f32 v107, v110, v111\n"
      "v_mfma_f32_16x16x32_bf16 v[8:11], v[128:131], a[8:11], 0\n"
      "ds_write_addtid_b32 v106 offset:9728\n"
      "v_cvt_pk_bf16_f32 v108, v112, v113\n"
      "v_mfma_f32_16x16x32_bf16 v[12:15], v[128:131], a[12:15], 0\n"
      "ds_write_addtid_b32 v107 offset:9984\n"
      "v_cvt_pk_bf16_f32 v109, v114, v115\n"
      "v_mfma_f32_16x16x32_bf16 v[16:19], v[128:131], a[16:19], 0\n"
      "ds_write_addtid_b32 v108 offset:10240\n"
      "v_cvt_pk_bf16_f32 v110, v116, v117\n"
      "v_mfma_f32_16x16x32_bf16 v[20:23], v[128:131], a[20:23], 0\n"
      "ds_write_addtid_b32 v109 offset:10496\n"
      "v_cvt_pk_bf16_f32 v111, v118, v119\n"
      "v_mfma_f32_16x16x32_bf16 v[24:27], v[128:131], a[24:27], 0\n"
      "ds_write_addtid_b32 v110 offset:10752\n"
      "v_cvt_pk_bf16_f32 v112, v120, v121\n"
      "v_mfma_f32_16x16x32_bf16 v[28:31], v[128:131], a[28:31], 0\n"
      "global_load_dwordx4 v[128:131], v240, s[10:11] offset:2048 sc1\n"
      "v_mfma_f32_16x16x32_bf16 v[0:3], v[132:135], a[32:35], v[0:3]\n"
      "ds_write_addtid_b32 v111 offset:11008\n"
      "v_cvt_pk_bf16_f32 v113, v122, v123\n"
      "v_mfma_f32_16x16x32_bf16 v[4:7], v[132:135], a[36:39], v[4:7]\n"
      "ds_write_addtid_b32 v112 offset:11264\n"
      "v_cvt_pk_bf16_f32 v114, v124, v125\n"
      "v_mfma_f32_16x16x32_bf16 v[8:11], v[132:135], a[40:43], v[8:11]\n"
      "ds_write_addtid_b32 v113 offset:11520\n"
      "v_cvt_pk_bf16_f32 v115, v126, v127\n"
      "v_mfma_f32_16x16x32_bf16 v[12:15], v[132:135], a[44:47], v[12:15]\n"
      "ds_write_addtid_b32 v114 offset:11776\n"
      "v_mfma_f32_16x16x32_bf16 v[16:19], v[132:135], a[48:51], v[16:19]\n"
      "ds_write_addtid_b32 v115 offset:12032\n"
      "v_mfma_f32_16x16x32_bf16 v[20:23], v[132:135], a[52:55], v[20:23]\n"
      "v_mfma_f32_16x16x32_bf16 v[24:27], v[132:135], a[56:59], v[24:27]\n"
      "v_mfma_f32_16x16x32_bf16 v[28:31], v[132:135], a[60:63], v[28:31]\n"
      "s_waitcnt vmcnt(21)\n"
      "global_load_dwordx4 v[132:135], v240, s[72:73] offset:2048 sc1\n"
      "v_mfma_f32_16x16x32_bf16 v[0:3], v[136:139], a[64:67], v[0:3]\n"
      "s_waitcnt lgkmcnt(0)\n"
      "s_barrier\n"
      "v_mfma_f32_16x16x32_bf16 v[4:7], v[136:139], a[68:71], v[4:7]\n"
      "ds_read2st64_b32 v[84:85], v244 offset0:24 offset1:25\n"
      "v_mfma_f32_16x16x32_bf16 v[8:11], v[136:139], a[72:75], v[8:11]\n"
      "ds_read2st64_b32 v[86:87], v245 offset0:28 offset1:29\n"
      "v_mfma_f32_16x16x32_bf16 v[12:15], v[136:139], a[76:79], v[12:15]\n"
      "ds_read2st64_b32 v[88:89], v244 offset0:26 offset1:27\n"
      "v_mfma_f32_16x16x32_bf16 v[16:19], v[136:139], a[80:83], v[16:19]\n"
      "ds_read2st64_b32 v[90:91], v245 offset0:30 offset1:31\n"
      "v_mfma_f32_16x16x32_bf16 v[20:23], v[136:139], a[84:87], v[20:23]\n"
      "ds_read2st64_b32 v[92:93], v246 offset0:32 offset1:33\n"
      "v_mfma_f32_16x16x32_bf16 v[24:27], v[136:139], a[88:91], v[24:27]\n"
      "ds_read2st64_b32 v[94:95], v246 offset0:34 offset1:35\n"
      "v_mfma_f32_16x16x32_bf16 v[28:31], v[136:139], a[92:95], v[28:31]\n"
      "global_load_dwordx4 v[136:139], v241, s[10:11] offset:2048 sc1\n"
      "v_mfma_f32_16x16x32_bf16 v[0:3], v[140:143], a[96:99], v[0:3]\n"
      "ds_read2st64_b32 v[116:117], v244 offset0:36 offset1:37\n"
      "v_mfma_f32_16x16x32_bf16 v[4:7], v[140:143], a[100:103], v[4:7]\n"
      "ds_read2st64_b32 v[118:119], v245 offset0:40 offset1:41\n"
      "v_mfma_f32_16x16x32_bf16 v[8:11], v[140:143], a[104:107], v[8:11]\n"
      "ds_read2st64_b32 v[120:121], v244 offset0:38 offset1:39\n"
      "v_mfma_f32_16x16x32_bf16 v[12:15], v[140:143], a[108:111], v[12:15]\n"
      "s_waitcnt lgkmcnt(3)\n"
      "v_mfma_f32_16x16x32_bf16 v[64:67], v[250:253], v[84:87], v[64:67]\n"
      "v_mfma_f32_16x16x32_bf16 v[16:19], v[140:143], a[112:115], v[16:19]\n"
      "v_mfma_f32_16x16x32_bf16 v[68:71], v[250:253], v[88:91], v[68:71]\n"
      "v_mfma_f32_16x16x32_bf16 v[20:23], v[140:143], a[116:119], v[20:23]\n"
      "v_mfma_f32_16x16x32_bf16 v[64:67], v[252:255], v[92:95], v[64:67]\n"
      "v_mfma_f32_16x16x32_bf16 v[24:27], v[140:143], a[120:123], v[24:27]\n"
      "v_mfma_f32_16x16x32_bf16 v[68:71], v[248:251], v[92:95], v[68:71]\n"
      "v_mov_b32 v75, s37\n"
      "v_mfma_f32_16x16x32_bf16 v[28:31], v[140:143], a[124:127], v[28:31]\n"
      "s_waitcnt vmcnt(20)\n"
      "global_load_dwordx4 v[140:143], v241, s[72:73] offset:2048 sc1\n"
      "v_mfma_f32_16x16x32_bf16 v[0:3], v[144:147], a[128:131], v[0:3]\n"
      "ds_read2st64_b32 v[122:123], v245 offset0:42 offset1:43\n"
      "v_cvt_pk_bf16_f32 v72, v64, v65\n"
      "v_mfma_f32_16x16x32_bf16 v[4:7], v[144:147], a[132:135], v[4:7]\n"
      "ds_read2st64_b32 v[124:125], v246 offset0:44 offset1:45\n"
      "v_cvt_pk_bf16_f32 v73, v66, v67\n"
      "v_mfma_f32_16x16x32_bf16 v[8:11], v[144:147], a[136:139], v[8:11]\n"
      "ds_read2st64_b32 v[126:127], v246 offset0:46 offset1:47\n"
      "v_cvt_pk_bf16_f32 v74, v68, v69\n"
      "v_mfma_f32_16x16x32_bf16 v[12:15], v[144:147], a[140:143], v[12:15]\n"
      "s_waitcnt vmcnt(11)\n"
      "v_bitop3_b32 v210, v205, v207, s37 bitop3:0x7e\n"
      "v_cmp_ne_u32 vcc, 0, v210\n"
      "s_nop 1\n"
      "s_cbranch_vccnz .Ls_s13_%=\n"
      ".Ls_o14_%=:\n"
      "v_mfma_f32_16x16x32_bf16 v[16:19], v[144:147], a[144:147], v[16:19]\n"
      "v_cvt_pk_bf16_f32 v247, v70, v71\n"
      "v_mov_b32 v209, s39\n"
      "v_mfma_f32_16x16x32_bf16 v[20:23], v[144:147], a[148:151], v[20:23]\n"
      "global_store_dwordx4 v242, v[72:75], s[52:53] offset:0 sc1\n"
      "v_mfma_f32_16x16x32_bf16 v[24:27], v[144:147], a[152:155], v[24:27]\n"
      "v_cmp_eq_u32 vcc, 0, v208\n"
      "s_nop 1\n"
      "v_cndmask_b32 v204, v204, v209, vcc\n"
      "v_mfma_f32_16x16x32_bf16 v[28:31], v[144:147], a[156:159], v[28:31]\n"
      "s_waitcnt lgkmcnt(0)\n"
      "global_load_dwordx4 v[144:147], v240, s[14:15] offset:2048 sc1\n"
      "v_mfma_f32_16x16x32_bf16 v[0:3], v[148:151], a[160:163], v[0:3]\n"
      "v_mfma_f32_16x16x32_bf16 v[96:99], v[250:253], v[116:119], v[96:99]\n"
      "v_mfma_f32_16x16x32_bf16 v[4:7], v[148:151], a[164:167], v[4:7]\n"
      "v_mfma_f32_16x16x32_bf16 v[100:103], v[250:253], v[120:123], v[100:103]\n"
      "v_mfma_f32_16x16x32_bf16 v[8:11], v[148:151], a[168:171], v[8:11]\n"
      "v_mfma_f32_16x16x32_bf16 v[96:99], v[252:255], v[124:127], v[96:99]\n"
      "v_mfma_f32_16x16x32_bf16 v[12:15], v[148:151], a[172:175], v[12:15]\n"
      "v_mfma_f32_16x16x32_bf16 v[100:103], v[248:251], v[124:127], v[100:103]\n"
      "v_mov_b32 v107, s37\n"
      "v_mfma_f32_16x16x32_bf16 v[16:19], v[148:151], a[176:179], v[16:19]\n"
      "v_cmp_eq_u32 vcc, -16, v208\n"
      "s_nop 1\n"
      "v_cndmask_b32 v206, v206, v209, vcc\n"
      "v_mfma_f32_16x16x32_bf16 v[20:23], v[148:151], a[180:183], v[20:23]\n"
      "v_mov_b32 v108, v247\n"
      "v_mov_b32 v111, s37\n"
      "v_mfma_f32_16x16x32_bf16 v[24:27], v[148:151], a[184:187], v[24:27]\n"
      "v_sub_f32 v205, 1.0, v204\n"
      "v_cvt_pk_bf16_f32 v205, v205, v205\n"
      "v_mfma_f32_16x16x32_bf16 v[28:31], v[148:151], a[188:191], v[28:31]\n"
      "global_load_dwordx4 v[148:151], v240, s[74:75] offset:2048 sc1\n"
      "v_cvt_pk_bf16_f32 v104, v96, v97\n"
      "v_mfma_f32_16x16x32_bf16 v[0:3], v[152:155], a[192:195], v[0:3]\n"
      "v_cvt_pk_bf16_f32 v105, v98, v99\n"
      "v_cvt_pk_bf16_f32 v106, v100, v101\n"
      "v_mfma_f32_16x16x32_bf16 v[4:7], v[152:155], a[196:199], v[4:7]\n"
      "v_cvt_pk_bf16_f32 v109, v102, v103\n"
      "v_and_b32 v205, 0xffff0000, v205\n"
      "v_mfma_f32_16x16x32_bf16 v[8:11], v[152:155], a[200:203], v[8:11]\n"
      "global_store_dwordx4 v242, v[104:107], s[52:53] offset:1024 sc1\n"
      "v_sub_f32 v207, 1.0, v206\n"
      "v_mfma_f32_16x16x32_bf16 v[12:15], v[152:155], a[204:207], v[12:15]\n"
      "global_store_dwordx4 v242, v[108:111], s[52:53] offset:2048 sc1\n"
      "s_add_u32 s48, s34, 3\n"
      "v_mfma_f32_16x16x32_bf16 v[16:19], v[152:155], a[208:211], v[16:19]\n"
      "s_and_b32 s48, s48, 3\n"
      "s_mul_i32 s48, s48, 36864\n"
      "s_add_u32 s48, s48, 24576\n"
      "v_mfma_f32_16x16x32_bf16 v[20:23], v[152:155], a[212:215], v[20:23]\n"
      "s_add_u32 s52, s60, s48\n"
      "s_addc_u32 s53, s61, 0\n"
      "v_cvt_pk_bf16_f32 v207, v207, v207\n"
      "v_mfma_f32_16x16x32_bf16 v[24:27], v[152:155], a[216:219], v[24:27]\n"
      "v_and_b32 v207, 0xffff0000, v207\n"
      "v_lshlrev_b32 v208, 16, v216\n"
      "v_mfma_f32_16x16x32_bf16 v[28:31], v[152:155], a[220:223], v[28:31]\n"
      "global_load_dwordx4 v[152:155], v241, s[14:15] offset:2048 sc1\n"
      "v_and_b32 v209, 0xffff0000, v216\n"
      "v_mfma_f32_16x16x32_bf16 v[0:3], v[156:159], a[224:227], v[0:3]\n"
      "v_mul_f32 v208, v205, v208\n"
      "v_mul_f32 v209, v205, v209\n"
      "v_mfma_f32_16x16x32_bf16 v[4:7], v[156:159], a[228:231], v[4:7]\n"
      "v_lshlrev_b32 v212, 16, v220\n"
      "v_and_b32 v213, 0xffff0000, v220\n"
      "v_mfma_f32_16x16x32_bf16 v[8:11], v[156:159], a[232:235], v[8:11]\n"
      "v_mul_f32 v212, v207, v212\n"
      "v_mul_f32 v213, v207, v213\n"
      "v_mfma_f32_16x16x32_bf16 v[12:15], v[156:159], a[236:239], v[12:15]\n"
      "v_lshlrev_b32 v210, 16, v217\n"
      "v_and_b32 v211, 0xffff0000, v217\n"
      "v_mfma_f32_16x16x32_bf16 v[16:19], v[156:159], a[240:243], v[16:19]\n"
      "v_mul_f32 v210, v205, v210\n"
      "v_mul_f32 v211, v205, v211\n"
      "v_mfma_f32_16x16x32_bf16 v[20:23], v[156:159], a[244:247], v[20:23]\n"
      "v_lshlrev_b32 v214, 16, v221\n"
      "v_and_b32 v215, 0xffff0000, v221\n"
      "v_mfma_f32_16x16x32_bf16 v[24:27], v[156:159], a[248:251], v[24:27]\n"
      "v_cvt_pk_bf16_f32 v8, v8, v9\n"
      "v_cvt_pk_bf16_f32 v9, v10, v11\n"
      "v_mfma_f32_16x16x32_bf16 v[28:31], v[156:159], a[252:255], v[28:31]\n"
      "global_load_dwordx4 v[156:159], v241, s[74:75] offset:2048 sc1\n"
      "v_mul_f32 v214, v207, v214\n"
      "v_mfma_f32_16x16x32_bf16 v[32:35], v[160:163], a[0:3], 0\n"
      "ds_write_addtid_b32 v8 offset:0\n"
      "v_cvt_pk_bf16_f32 v10, v12, v13\n"
      "v_mfma_f32_16x16x32_bf16 v[36:39], v[160:163], a[4:7], 0\n"
      "ds_write_addtid_b32 v9 offset:256\n"
      "v_cvt_pk_bf16_f32 v11, v14, v15\n"
      "v_mfma_f32_16x16x32_bf16 v[40:43], v[160:163], a[8:11], 0\n"
      "ds_write_addtid_b32 v10 offset:512\n"
      "v_cvt_pk_bf16_f32 v12, v16, v17\n"
      "v_mfma_f32_16x16x32_bf16 v[44:47], v[160:163], a[12:15], 0\n"
      "ds_write_addtid_b32 v11 offset:768\n"
      "v_cvt_pk_bf16_f32 v13, v18, v19\n"
      "v_mfma_f32_16x16x32_bf16 v[48:51], v[160:163], a[16:19], 0\n"
      "ds_write_addtid_b32 v12 offset:1024\n"
      "v_cvt_pk_bf16_f32 v14, v20, v21\n"
      "v_mfma_f32_16x16x32_bf16 v[52:55], v[160:163], a[20:23], 0\n"
      "ds_write_addtid_b32 v13 offset:1280\n"
      "v_cvt_pk_bf16_f32 v15, v22, v23\n"
      "v_mfma_f32_16x16x32_bf16 v[56:59], v[160:163], a[24:27], 0\n"
      "ds_write_addtid_b32 v14 offset:1536\n"
      "v_cvt_pk_bf16_f32 v16, v24, v25\n"
      "v_mfma_f32_16x16x32_bf16 v[60:63], v[160:163], a[28:31], 0\n"
      "global_load_dwordx4 v[160:163], v240, s[10:11] offset:3072 sc1\n"
      "v_mul_f32 v215, v207, v215\n"
      "v_mfma_f32_16x16x32_bf16 v[32:35], v[164:167], a[32:35], v[32:35]\n"
      "ds_write_addtid_b32 v15 offset:1792\n"
      "v_cvt_pk_bf16_f32 v17, v26, v27\n"
      "v_mfma_f32_16x16x32_bf16 v[36:39], v[164:167], a[36:39], v[36:39]\n"
      "ds_write_addtid_b32 v16 offset:2048\n"
      "v_cvt_pk_bf16_f32 v18, v28, v29\n"
      "v_mfma_f32_16x16x32_bf16 v[40:43], v[164:167], a[40:43], v[40:43]\n"
      "ds_write_addtid_b32 v17 offset:2304\n"
      "v_cvt_pk_bf16_f32 v19, v30, v31\n"
      "v_mfma_f32_16x16x32_bf16 v[44:47], v[164:167], a[44:47], v[44:47]\n"
      "ds_write_addtid_b32 v18 offset:2560\n"
      "v_lshlrev_b32 v216, 16, v218\n"
      "v_mfma_f32_16x16x32_bf16 v[48:51], v[164:167], a[48:51], v[48:51]\n"
      "ds_write_addtid_b32 v19 offset:2816\n"
      "v_and_b32 v217, 0xffff0000, v218\n"
      "v_mfma_f32_16x16x32_bf16 v[52:55], v[164:167], a[52:55], v[52:55]\n"
      "v_mul_f32 v216, v205, v216\n"
      "v_mul_f32 v217, v205, v217\n"
      "v_mfma_f32_16x16x32_bf16 v[56:59], v[164:167], a[56:59], v[56:59]\n"
      "v_lshlrev_b32 v220, 16, v222\n"
      "v_and_b32 v221, 0xffff0000, v222\n"
      "v_mfma_f32_16x16x32_bf16 v[60:63], v[164:167], a[60:63], v[60:63]\n"
      "global_load_dwordx4 v[164:167], v240, s[72:73] offset:3072 sc1\n"
      "v_mul_f32 v220, v207, v220\n"
      "v_mfma_f32_16x16x32_bf16 v[32:35], v[168:171], a[64:67], v[32:35]\n"
      "s_waitcnt lgkmcnt(0)\n"
      "s_barrier\n"
      "v_mfma_f32_16x16x32_bf16 v[36:39], v[168:171], a[68:71], v[36:39]\n"
      "ds_read2st64_b32 v[20:21], v244 offset0:0 offset1:1\n"
      "v_mul_f32 v221, v207, v221\n"
      "v_mfma_f32_16x16x32_bf16 v[40:43], v[168:171], a[72:75], v[40:43]\n"
      "ds_read2st64_b32 v[22:23], v245 offset0:4 offset1:5\n"
      "v_lshlrev_b32 v218, 16, v219\n"
      "v_mfma_f32_16x16x32_bf16 v[44:47], v[168:171], a[76:79], v[44:47]\n"
      "ds_read2st64_b32 v[24:25], v244 offset0:2 offset1:3\n"
      "v_and_b32 v219, 0xffff0000, v219\n"
      "v_mfma_f32_16x16x32_bf16 v[48:51], v[168:171], a[80:83], v[48:51]\n"
      "ds_read2st64_b32 v[26:27], v245 offset0:6 offset1:7\n"
      "v_mul_f32 v218, v205, v218\n"
      "v_mfma_f32_16x16x32_bf16 v[52:55], v[168:171], a[84:87], v[52:55]\n"
      "ds_read2st64_b32 v[28:29], v246 offset0:8 offset1:9\n"
      "v_mul_f32 v219, v205, v219\n"
      "v_mfma_f32_16x16x32_bf16 v[56:59], v[168:171], a[88:91], v[56:59]\n"
      "ds_read2st64_b32 v[30:31], v246 offset0:10 offset1:11\n"
      "v_lshlrev_b32 v222, 16, v223\n"
      "v_mfma_f32_16x16x32_bf16 v[60:63], v[168:171], a[92:95], v[60:63]\n"
      "global_load_dwordx4 v[168:171], v241, s[10:11] offset:3072 sc1\n"
      "v_and_b32 v223, 0xffff0000, v223\n"
      "v_mfma_f32_16x16x32_bf16 v[32:35], v[172:175], a[96:99], v[32:35]\n"
      "v_mul_f32 v222, v207, v222\n"
      "v_mul_f32 v223, v207, v223\n"
      "v_mfma_f32_16x16x32_bf16 v[36:39], v[172:175], a[100:103], v[36:39]\n"
      "v_mfma_f32_16x16x32_bf16 v[40:43], v[172:175], a[104:107], v[40:43]\n"
      "v_mfma_f32_16x16x32_bf16 v[44:47], v[172:175], a[108:111], v[44:47]\n"
      "s_waitcnt lgkmcnt(0)\n"
      "v_mfma_f32_16x16x32_bf16 v[0:3], v[250:253], v[20:23], v[0:3]\n"
      "v_mfma_f32_16x16x32_bf16 v[48:51], v[172:175], a[112:115], v[48:51]\n"
      "v_mfma_f32_16x16x32_bf16 v[4:7], v[250:253], v[24:27], v[4:7]\n"
      "v_mfma_f32_16x16x32_bf16 v[52:55], v[172:175], a[116:119], v[52:55]\n"
      "v_mfma_f32_16x16x32_bf16 v[0:3], v[252:255], v[28:31], v[0:3]\n"
      "v_mfma_f32_16x16x32_bf16 v[56:59], v[172:175], a[120:123], v[56:59]\n"
      "v_mfma_f32_16x16x32_bf16 v[4:7], v[248:251], v[28:31], v[4:7]\n"
      "v_mov_b32 v11, s37\n"
      "v_mfma_f32_16x16x32_bf16 v[60:63], v[172:175], a[124:127], v[60:63]\n"
      "s_waitcnt vmcnt(16)\n"
      "global_load_dwordx4 v[172:175], v241, s[72:73] offset:3072 sc1\n"
      "v_mfma_f32_16x16x32_bf16 v[40:43], v[176:179], a[136:139], v[40:43]\n"
      "s_nop 0\n"
      "v_cvt_pk_bf16_f32 v8, v0, v1\n"
      "v_mfma_f32_16x16x32_bf16 v[44:47], v[176:179], a[140:143], v[44:47]\n"
      "v_cvt_pk_bf16_f32 v9, v2, v3\n"
      "v_cvt_pk_bf16_f32 v10, v4, v5\n"
      "v_mfma_f32_16x16x32_bf16 v[48:51], v[176:179], a[144:147], v[48:51]\n"
      "v_cvt_pk_bf16_f32 v247, v6, v7\n"
      "v_mfma_f32_16x16x32_bf16 v[52:55], v[176:179], a[148:151], v[52:55]\n"
      "global_store_dwordx4 v242, v[8:11], s[52:53] offset:0 sc1\n"
      "s_lshl2_add_u32 s54, s36, s33\n"
      "v_mfma_f32_16x16x32_bf16 v[56:59], v[176:179], a[152:155], v[56:59]\n"
      "s_lshl_b32 s55, s54, 15\n"
      "s_lshl2_add_u32 s56, s35, s34\n"
      "s_lshl_b32 s56, s56, 6\n"
      "v_mfma_f32_16x16x32_bf16 v[60:63], v[176:179], a[156:159], v[60:63]\n"
      "s_add_u32 s55, s55, s56\n"
      "v_and_b32 v8, 15, v243\n"
      "v_and_b32 v7, 48, v243\n"
      "v_mfma_f32_16x16x32_bf16 v[40:43], v[180:183], a[168:171], v[40:43]\n"
      "v_lshl_or_b32 v7, v8, 10, v7\n"
      "v_add_u32 v7, s55, v7\n"
      "v_mfma_f32_16x16x32_bf16 v[44:47], v[180:183], a[172:175], v[44:47]\n"
      "v_mfma_f32_16x16x32_bf16 v[48:51], v[180:183], a[176:179], v[48:51]\n"
      "v_mfma_f32_16x16x32_bf16 v[52:55], v[180:183], a[180:183], v[52:55]\n"
      "v_mfma_f32_16x16x32_bf16 v[56:59], v[180:183], a[184:187], v[56:59]\n"
      "v_mfma_f32_16x16x32_bf16 v[60:63], v[180:183], a[188:191], v[60:63]\n"
      "s_waitcnt vmcnt(16)\n"
      "v_mfma_f32_16x16x32_bf16 v[40:43], v[184:187], a[200:203], v[40:43]\n"
      "v_mfma_f32_16x16x32_bf16 v[44:47], v[184:187], a[204:207], v[44:47]\n"
      "v_mfma_f32_16x16x32_bf16 v[48:51], v[184:187], a[208:211], v[48:51]\n"
      "v_mfma_f32_16x16x32_bf16 v[52:55], v[184:187], a[212:215], v[52:55]\n"
      "v_mfma_f32_16x16x32_bf16 v[56:59], v[184:187], a[216:219], v[56:59]\n"
      "v_mfma_f32_16x16x32_bf16 v[60:63], v[184:187], a[220:223], v[60:63]\n"
      "v_mfma_f32_16x16x32_bf16 v[40:43], v[188:191], a[232:235], v[40:43]\n"
      "v_mfma_f32_16x16x32_bf16 v[44:47], v[188:191], a[236:239], v[44:47]\n"
      "v_mfma_f32_16x16x32_bf16 v[48:51], v[188:191], a[240:243], v[48:51]\n"
      "v_mfma_f32_16x16x32_bf16 v[52:55], v[188:191], a[244:247], v[52:55]\n"
      "v_mfma_f32_16x16x32_bf16 v[56:59], v[188:191], a[248:251], v[56:59]\n"
      "s_nop 3\n"
      "v_cvt_pk_bf16_f32 v40, v40, v41\n"
      "v_cvt_pk_bf16_f32 v41, v42, v43\n"
      "v_mfma_f32_16x16x32_bf16 v[60:63], v[188:191], a[252:255], v[60:63]\n"
      "ds_write_addtid_b32 v40 offset:3072\n"
      "v_cvt_pk_bf16_f32 v42, v44, v45\n"
      "v_mfma_f32_16x16x32_bf16 v[32:35], v[176:179], a[128:131], v[32:35]\n"
      "ds_write_addtid_b32 v41 offset:3328\n"
      "v_cvt_pk_bf16_f32 v43, v46, v47\n"
      "v_mfma_f32_16x16x32_bf16 v[36:39], v[176:179], a[132:135], v[36:39]\n"
      "global_load_dwordx4 v[176:179], v240, s[14:15] offset:3072 sc1\n"
      "ds_write_addtid_b32 v42 offset:3584\n"
      "v_cvt_pk_bf16_f32 v44, v48, v49\n"
      "v_mfma_f32_16x16x32_bf16 v[32:35], v[180:183], a[160:163], v[32:35]\n"
      "ds_write_addtid_b32 v43 offset:3840\n"
      "v_cvt_pk_bf16_f32 v45, v50, v51\n"
      "v_mfma_f32_16x16x32_bf16 v[36:39], v[180:183], a[164:167], v[36:39]\n"
      "global_load_dwordx4 v[180:183], v240, s[74:75] offset:3072 sc1\n"
      "ds_write_addtid_b32 v44 offset:4096\n"
      "v_cvt_pk_bf16_f32 v46, v52, v53\n"
      "v_mfma_f32_16x16x32_bf16 v[32:35], v[184:187], a[192:195], v[32:35]\n"
      "ds_write_addtid_b32 v45 offset:4352\n"
      "v_cvt_pk_bf16_f32 v47, v54, v55\n"
      "v_mfma_f32_16x16x32_bf16 v[36:39], v[184:187], a[196:199], v[36:39]\n"
      "global_load_dwordx4 v[184:187], v241, s[14:15] offset:3072 sc1\n"
      "ds_write_addtid_b32 v46 offset:4608\n"
      "v_cvt_pk_bf16_f32 v48, v56, v57\n"
      "v_mfma_f32_16x16x32_bf16 v[32:35], v[188:191], a[224:227], v[32:35]\n"
      "ds_write_addtid_b32 v47 offset:4864\n"
      "v_cvt_pk_bf16_f32 v49, v58, v59\n"
      "v_mfma_f32_16x16x32_bf16 v[36:39], v[188:191], a[228:231], v[36:39]\n"
      "s_waitcnt vmcnt(17)\n"
      "global_load_dwordx4 v[188:191], v241, s[74:75] offset:3072 sc1\n"
      "ds_write_addtid_b32 v48 offset:5120\n"
      "v_cvt_pk_bf16_f32 v50, v60, v61\n"
      "v_mfma_f32_16x16x32_bf16 v[72:75], v[128:131], a[8:11], 0\n"
      "ds_write_addtid_b32 v49 offset:5376\n"
      "v_cvt_pk_bf16_f32 v51, v62, v63\n"
      "v_mfma_f32_16x16x32_bf16 v[76:79], v[128:131], a[12:15], 0\n"
      "ds_write_addtid_b32 v50 offset:5632\n"
      "ds_write_addtid_b32 v51 offset:5888\n"
      "v_mfma_f32_16x16x32_bf16 v[80:83], v[128:131], a[16:19], 0\n"
      "v_mfma_f32_16x16x32_bf16 v[84:87], v[128:131], a[20:23], 0\n"
      "v_mfma_f32_16x16x32_bf16 v[88:91], v[128:131], a[24:27], 0\n"
      "s_waitcnt lgkmcnt(0)\n"
      "s_barrier\n"
      "v_mfma_f32_16x16x32_bf16 v[92:95], v[128:131], a[28:31], 0\n"
      "ds_read2st64_b32 v[52:53], v244 offset0:12 offset1:13\n"
      "ds_read2st64_b32 v[54:55], v245 offset0:16 offset1:17\n"
      "v_mfma_f32_16x16x32_bf16 v[72:75], v[132:135], a[40:43], v[72:75]\n"
      "ds_read2st64_b32 v[56:57], v244 offset0:14 offset1:15\n"
      "ds_read2st64_b32 v[58:59], v245 offset0:18 offset1:19\n"
      "v_mfma_f32_16x16x32_bf16 v[76:79], v[132:135], a[44:47], v[76:79]\n"
      "ds_read2st64_b32 v[60:61], v246 offset0:20 offset1:21\n"
      "ds_read2st64_b32 v[62:63], v246 offset0:22 offset1:23\n"
      "v_mfma_f32_16x16x32_bf16 v[80:83], v[132:135], a[48:51], v[80:83]\n"
      "v_mfma_f32_16x16x32_bf16 v[84:87], v[132:135], a[52:55], v[84:87]\n"
      "v_mfma_f32_16x16x32_bf16 v[88:91], v[132:135], a[56:59], v[88:91]\n"
      "v_mfma_f32_16x16x32_bf16 v[92:95], v[132:135], a[60:63], v[92:95]\n"
      "s_waitcnt vmcnt(16)\n"
      "v_mfma_f32_16x16x32_bf16 v[72:75], v[136:139], a[72:75], v[72:75]\n"
      "s_waitcnt lgkmcnt(0)\n"
      "v_mfma_f32_16x16x32_bf16 v[32:35], v[250:253], v[52:55], v[32:35]\n"
      "v_mfma_f32_16x16x32_bf16 v[36:39], v[250:253], v[56:59], v[36:39]\n"
      "v_mfma_f32_16x16x32_bf16 v[76:79], v[136:139], a[76:79], v[76:79]\n"
      "v_mfma_f32_16x16x32_bf16 v[32:35], v[252:255], v[60:63], v[32:35]\n"
      "v_mfma_f32_16x16x32_bf16 v[36:39], v[248:251], v[60:63], v[36:39]\n"
      "v_mov_b32 v43, s37\n"
      "v_mfma_f32_16x16x32_bf16 v[80:83], v[136:139], a[80:83], v[80:83]\n"
      "v_mov_b32 v44, v247\n"
      "v_mov_b32 v47, s37\n"
      "v_mfma_f32_16x16x32_bf16 v[84:87], v[136:139], a[84:87], v[84:87]\n"
      "v_mfma_f32_16x16x32_bf16 v[88:91], v[136:139], a[88:91], v[88:91]\n"
      "v_add_u32 v4, 0, v242\n"
      "v_cvt_pk_bf16_f32 v40, v32, v33\n"
      "v_cvt_pk_bf16_f32 v41, v34, v35\n"
      "v_mfma_f32_16x16x32_bf16 v[92:95], v[136:139], a[92:95], v[92:95]\n"
      "global_load_dwordx4 v[12:15], v4, s[62:63] offset:0 sc1\n"
      "global_load_dwordx4 v[16:19], v4, s[62:63] offset:1024 sc1\n"
      "v_cvt_pk_bf16_f32 v42, v36, v37\n"
      "v_mfma_f32_16x16x32_bf16 v[72:75], v[140:143], a[104:107], v[72:75]\n"
      "global_load_dwordx4 v[20:23], v4, s[62:63] offset:2048 sc1\n"
      "v_cvt_pk_bf16_f32 v45, v38, v39\n"
      "v_mfma_f32_16x16x32_bf16 v[76:79], v[140:143], a[108:111], v[76:79]\n"
      "global_store_dwordx4 v242, v[40:43], s[52:53] offset:1024 sc1\n"
      "global_store_dwordx4 v242, v[44:47], s[52:53] offset:2048 sc1\n"
      "v_mfma_f32_16x16x32_bf16 v[80:83], v[140:143], a[112:115], v[80:83]\n"
      "v_mfma_f32_16x16x32_bf16 v[84:87], v[140:143], a[116:119], v[84:87]\n"
      "v_mfma_f32_16x16x32_bf16 v[88:91], v[140:143], a[120:123], v[88:91]\n"
      "v_mfma_f32_16x16x32_bf16 v[92:95], v[140:143], a[124:127], v[92:95]\n"
      "s_waitcnt vmcnt(18)\n"
      "v_mfma_f32_16x16x32_bf16 v[72:75], v[144:147], a[136:139], v[72:75]\n"
      "v_mfma_f32_16x16x32_bf16 v[76:79], v[144:147], a[140:143], v[76:79]\n"
      "v_mfma_f32_16x16x32_bf16 v[80:83], v[144:147], a[144:147], v[80:83]\n"
      "v_mfma_f32_16x16x32_bf16 v[84:87], v[144:147], a[148:151], v[84:87]\n"
      "v_mfma_f32_16x16x32_bf16 v[88:91], v[144:147], a[152:155], v[88:91]\n"
      "v_mfma_f32_16x16x32_bf16 v[92:95], v[144:147], a[156:159], v[92:95]\n"
      "v_mfma_f32_16x16x32_bf16 v[72:75], v[148:151], a[168:171], v[72:75]\n"
      "v_mfma_f32_16x16x32_bf16 v[76:79], v[148:151], a[172:175], v[76:79]\n"
      "v_mfma_f32_16x16x32_bf16 v[80:83], v[148:151], a[176:179], v[80:83]\n"
      "v_mfma_f32_16x16x32_bf16 v[84:87], v[148:151], a[180:183], v[84:87]\n"
      "v_mfma_f32_16x16x32_bf16 v[88:91], v[148:151], a[184:187], v[88:91]\n"
      "v_mfma_f32_16x16x32_bf16 v[92:95], v[148:151], a[188:191], v[92:95]\n"
      "s_waitcnt vmcnt(14)\n"
      "v_mfma_f32_16x16x32_bf16 v[72:75], v[152:155], a[200:203], v[72:75]\n"
      "v_mfma_f32_16x16x32_bf16 v[76:79], v[152:155], a[204:207], v[76:79]\n"
      "v_mfma_f32_16x16x32_bf16 v[80:83], v[152:155], a[208:211], v[80:83]\n"
      "v_mfma_f32_16x16x32_bf16 v[84:87], v[152:155], a[212:215], v[84:87]\n"
      "v_mfma_f32_16x16x32_bf16 v[88:91], v[152:155], a[216:219], v[88:91]\n"
      "v_mfma_f32_16x16x32_bf16 v[92:95], v[152:155], a[220:223], v[92:95]\n"
      "v_mfma_f32_16x16x32_bf16 v[72:75], v[156:159], a[232:235], v[72:75]\n"
      "v_mfma_f32_16x16x32_bf16 v[76:79], v[156:159], a[236:239], v[76:79]\n"
      "v_mfma_f32_16x16x32_bf16 v[80:83], v[156:159], a[240:243], v[80:83]\n"
      "v_mfma_f32_16x16x32_bf16 v[84:87], v[156:159], a[244:247], v[84:87]\n"
      "v_mfma_f32_16x16x32_bf16 v[88:91], v[156:159], a[248:251], v[88:91]\n"
      "s_nop 3\n"
      "v_cvt_pk_bf16_f32 v72, v72, v73\n"
      "v_mfma_f32_16x16x32_bf16 v[92:95], v[156:159], a[252:255], v[92:95]\n"
      "s_waitcnt vmcnt(12)\n"
      "v_cvt_pk_bf16_f32 v73, v74, v75\n"
      "v_mfma_f32_16x16x32_bf16 v[104:107], v[160:163], a[8:11], 0\n"
      "ds_write_addtid_b32 v72 offset:6144\n"
      "v_cvt_pk_bf16_f32 v74, v76, v77\n"
      "v_mfma_f32_16x16x32_bf16 v[108:111], v[160:163], a[12:15], 0\n"
      "ds_write_addtid_b32 v73 offset:6400\n"
      "v_cvt_pk_bf16_f32 v75, v78, v79\n"
      "v_mfma_f32_16x16x32_bf16 v[112:115], v[160:163], a[16:19], 0\n"
      "ds_write_addtid_b32 v74 offset:6656\n"
      "v_cvt_pk_bf16_f32 v76, v80, v81\n"
      "v_mfma_f32_16x16x32_bf16 v[116:119], v[160:163], a[20:23], 0\n"
      "ds_write_addtid_b32 v75 offset:6912\n"
      "v_cvt_pk_bf16_f32 v77, v82, v83\n"
      "v_mfma_f32_16x16x32_bf16 v[120:123], v[160:163], a[24:27], 0\n"
      "ds_write_addtid_b32 v76 offset:7168\n"
      "v_cvt_pk_bf16_f32 v78, v84, v85\n"
      "v_mfma_f32_16x16x32_bf16 v[124:127], v[160:163], a[28:31], 0\n"
      "ds_write_addtid_b32 v77 offset:7424\n"
      "v_cvt_pk_bf16_f32 v79, v86, v87\n"
      "v_mfma_f32_16x16x32_bf16 v[104:107], v[164:167], a[40:43], v[104:107]\n"
      "ds_write_addtid_b32 v78 offset:7680\n"
      "v_cvt_pk_bf16_f32 v80, v88, v89\n"
      "v_mfma_f32_16x16x32_bf16 v[108:111], v[164:167], a[44:47], v[108:111]\n"
      "ds_write_addtid_b32 v79 offset:7936\n"
      "v_cvt_pk_bf16_f32 v81, v90, v91\n"
      "v_mfma_f32_16x16x32_bf16 v[112:115], v[164:167], a[48:51], v[112:115]\n"
      "ds_write_addtid_b32 v80 offset:8192\n"
      "v_cvt_pk_bf16_f32 v82, v92, v93\n"
      "v_mfma_f32_16x16x32_bf16 v[116:119], v[164:167], a[52:55], v[116:119]\n"
      "ds_write_addtid_b32 v81 offset:8448\n"
      "v_cvt_pk_bf16_f32 v83, v94, v95\n"
      "v_mfma_f32_16x16x32_bf16 v[120:123], v[164:167], a[56:59], v[120:123]\n"
      "ds_write_addtid_b32 v82 offset:8704\n"
      "v_mfma_f32_16x16x32_bf16 v[124:127], v[164:167], a[60:63], v[124:127]\n"
      "s_waitcnt vmcnt(10)\n"
      "ds_write_addtid_b32 v83 offset:8960\n"
      "v_mfma_f32_16x16x32_bf16 v[104:107], v[168:171], a[72:75], v[104:107]\n"
      "s_waitcnt vmcnt(2)\n"
      "v_bitop3_b32 v22, v15, v19, s37 bitop3:0x7e\n"
      "v_bitop3_b32 v22, v22, v23, s37 bitop3:0xf6\n"
      "v_cmp_ne_u32 vcc, 0, v22\n"
      "s_nop 1\n"
      "s_cbranch_vccnz .Ls_s16_%=\n"
      ".Ls_o17_%=:\n"
      "v_mfma_f32_16x16x32_bf16 v[108:111], v[168:171], a[76:79], v[108:111]\n"
      "v_mov_b32 v15, v20\n"
      "v_mov_b32 v19, v21\n"
      "v_mfma_f32_16x16x32_bf16 v[112:115], v[168:171], a[80:83], v[112:115]\n"
      "s_waitcnt lgkmcnt(0)\n"
      "s_barrier\n"
      "v_mfma_f32_16x16x32_bf16 v[116:119], v[168:171], a[84:87], v[116:119]\n"
      "ds_read2st64_b32 v[84:85], v244 offset0:24 offset1:25\n"
      "v_mfma_f32_16x16x32_bf16 v[120:123], v[168:171], a[88:91], v[120:123]\n"
      "ds_read2st64_b32 v[86:87], v245 offset0:28 offset1:29\n"
      "v_mfma_f32_16x16x32_bf16 v[124:127], v[168:171], a[92:95], v[124:127]\n"
      "ds_read2st64_b32 v[88:89], v244 offset0:26 offset1:27\n"
      "v_mfma_f32_16x16x32_bf16 v[104:107], v[172:175], a[104:107], v[104:107]\n"
      "ds_read2st64_b32 v[90:91], v245 offset0:30 offset1:31\n"
      "v_mfma_f32_16x16x32_bf16 v[108:111], v[172:175], a[108:111], v[108:111]\n"
      "ds_read2st64_b32 v[92:93], v246 offset0:32 offset1:33\n"
      "v_mfma_f32_16x16x32_bf16 v[112:115], v[172:175], a[112:115], v[112:115]\n"
      "ds_read2st64_b32 v[94:95], v246 offset0:34 offset1:35\n"
      "v_mfma_f32_16x16x32_bf16 v[116:119], v[172:175], a[116:119], v[116:119]\n"
      "s_lshr_b32 s43, s42, 7\n"
      "s_cmp_eq_u32 s43, s36\n"
      "s_cbranch_scc0 .Ls_e18_%=\n"
      "s_lshl2_add_u32 s44, s35, s34\n"
      "s_lshl2_add_u32 s43, s44, s33\n"
      "s_lshl_b32 s43, s43, 12\n"
      "s_add_u32 s43, s43, 524288\n"
      "v_add_u32 v60, s43, v242\n"
      "global_load_dwordx4 v[24:27], v60, s[58:59] offset:0 sc1\n"
      "global_load_dwordx4 v[28:31], v60, s[58:59] offset:1024 sc1\n"
      "global_load_dwordx4 v[32:35], v60, s[58:59] offset:2048 sc1\n"
      "global_load_dwordx4 v[36:39], v60, s[58:59] offset:3072 sc1\n"
      "v_and_b32 v58, 15, v243\n"
      "v_lshrrev_b32 v59, 5, v243\n"
      "v_lshl_or_b32 v58, v59, 4, v58\n"
      "s_lshl4_add_u32 s44, s42, s44\n"
      "s_lshl_b32 s44, s44, 6\n"
      "v_lshl_add_u32 v59, v58, 1, s44\n"
      "global_load_ushort v57, v59, s[16:17]\n"
      "s_mov_b32 s44, 131072\n"
      "v_lshl_add_u32 v58, v58, 2, s44\n"
      "s_lshl_b32 s43, s33, 13\n"
      "v_add_u32 v59, s43, v58\n"
      ".Ls_e18_%=:\n"
      "v_mfma_f32_16x16x32_bf16 v[120:123], v[172:175], a[120:123], v[120:123]\n"
      "v_mfma_f32_16x16x32_bf16 v[64:67], v[252:255], v[12:15], 0\n"
      "v_mfma_f32_16x16x32_bf16 v[124:127], v[172:175], a[124:127], v[124:127]\n"
      "v_mfma_f32_16x16x32_bf16 v[68:71], v[248:251], v[12:15], 0\n"
      "v_mfma_f32_16x16x32_bf16 v[104:107], v[176:179], a[136:139], v[104:107]\n"
      "v_mfma_f32_16x16x32_bf16 v[96:99], v[252:255], v[16:19], 0\n"
      "v_mfma_f32_16x16x32_bf16 v[108:111], v[176:179], a[140:143], v[108:111]\n"
      "s_waitcnt lgkmcnt(0)\n"
      "v_mfma_f32_16x16x32_bf16 v[64:67], v[250:253], v[84:87], v[64:67]\n"
      "v_mfma_f32_16x16x32_bf16 v[112:115], v[176:179], a[144:147], v[112:115]\n"
      "v_mfma_f32_16x16x32_bf16 v[68:71], v[250:253], v[88:91], v[68:71]\n"
      "v_mfma_f32_16x16x32_bf16 v[116:119], v[176:179], a[148:151], v[116:119]\n"
      "v_mfma_f32_16x16x32_bf16 v[64:67], v[252:255], v[92:95], v[64:67]\n"
      "v_mfma_f32_16x16x32_bf16 v[120:123], v[176:179], a[152:155], v[120:123]\n"
      "v_mfma_f32_16x16x32_bf16 v[68:71], v[248:251], v[92:95], v[68:71]\n"
      "v_mfma_f32_16x16x32_bf16 v[124:127], v[176:179], a[156:159], v[124:127]\n"
      "v_mfma_f32_16x16x32_bf16 v[100:103], v[248:251], v[16:19], 0\n"
      "v_mfma_f32_16x16x32_bf16 v[104:107], v[180:183], a[168:171], v[104:107]\n"
      "v_mfma_f32_16x16x32_bf16 v[108:111], v[180:183], a[172:175], v[108:111]\n"
      "v_mfma_f32_16x16x32_bf16 v[112:115], v[180:183], a[176:179], v[112:115]\n"
      "v_mfma_f32_16x16x32_bf16 v[116:119], v[180:183], a[180:183], v[116:119]\n"
      "v_add_u32 v5, 12288, v242\n"
      "v_mfma_f32_16x16x32_bf16 v[120:123], v[180:183], a[184:187], v[120:123]\n"
      "global_load_dwordx4 v[44:47], v5, s[62:63] offset:0 sc1\n"
      "v_mfma_f32_16x16x32_bf16 v[124:127], v[180:183], a[188:191], v[124:127]\n"
      "global_load_dwordx4 v[48:51], v5, s[62:63] offset:1024 sc1\n"
      "v_mfma_f32_16x16x32_bf16 v[104:107], v[184:187], a[200:203], v[104:107]\n"
      "global_load_dwordx4 v[52:55], v5, s[62:63] offset:2048 sc1\n"
      "v_mfma_f32_16x16x32_bf16 v[108:111], v[184:187], a[204:207], v[108:111]\n"
      "v_mfma_f32_16x16x32_bf16 v[112:115], v[184:187], a[208:211], v[112:115]\n"
      "v_mfma_f32_16x16x32_bf16 v[116:119], v[184:187], a[212:215], v[116:119]\n"
      "v_mfma_f32_16x16x32_bf16 v[120:123], v[184:187], a[216:219], v[120:123]\n"
      "v_mfma_f32_16x16x32_bf16 v[124:127], v[184:187], a[220:223], v[124:127]\n"
      "v_mfma_f32_16x16x32_bf16 v[104:107], v[188:191], a[232:235], v[104:107]\n"
      "v_mfma_f32_16x16x32_bf16 v[108:111], v[188:191], a[236:239], v[108:111]\n"
      "v_mfma_f32_16x16x32_bf16 v[112:115], v[188:191], a[240:243], v[112:115]\n"
      "v_mfma_f32_16x16x32_bf16 v[116:119], v[188:191], a[244:247], v[116:119]\n"
      "v_mfma_f32_16x16x32_bf16 v[120:123], v[188:191], a[248:251], v[120:123]\n"
      "s_nop 3\n"
      "v_cvt_pk_bf16_f32 v104, v104, v105\n"
      "v_mfma_f32_16x16x32_bf16 v[124:127], v[188:191], a[252:255], v[124:127]\n"
      "v_cvt_pk_bf16_f32 v105, v106, v107\n"
      "v_mfma_f32_16x16x32_bf16 v[64:67], v[128:131], a[0:3], v[64:67]\n"
      "ds_write_addtid_b32 v104 offset:9216\n"
      "v_cvt_pk_bf16_f32 v106, v108, v109\n"
      "v_mfma_f32_16x16x32_bf16 v[68:71], v[128:131], a[4:7], v[68:71]\n"
      "ds_write_addtid_b32 v105 offset:9472\n"
      "v_cvt_pk_bf16_f32 v107, v110, v111\n"
      "v_mfma_f32_16x16x32_bf16 v[64:67], v[132:135], a[32:35], v[64:67]\n"
      "ds_write_addtid_b32 v106 offset:9728\n"
      "v_cvt_pk_bf16_f32 v108, v112, v113\n"
      "v_mfma_f32_16x16x32_bf16 v[68:71], v[132:135], a[36:39], v[68:71]\n"
      "ds_write_addtid_b32 v107 offset:9984\n"
      "v_cvt_pk_bf16_f32 v109, v114, v115\n"
      "v_mfma_f32_16x16x32_bf16 v[64:67], v[136:139], a[64:67], v[64:67]\n"
      "ds_write_addtid_b32 v108 offset:10240\n"
      "v_cvt_pk_bf16_f32 v110, v116, v117\n"
      "v_mfma_f32_16x16x32_bf16 v[68:71], v[136:139], a[68:71], v[68:71]\n"
      "ds_write_addtid_b32 v109 offset:10496\n"
      "v_cvt_pk_bf16_f32 v111, v118, v119\n"
      "v_mfma_f32_16x16x32_bf16 v[64:67], v[140:143], a[96:99], v[64:67]\n"
      "ds_write_addtid_b32 v110 offset:10752\n"
      "v_cvt_pk_bf16_f32 v112, v120, v121\n"
      "v_mfma_f32_16x16x32_bf16 v[68:71], v[140:143], a[100:103], v[68:71]\n"
      "ds_write_addtid_b32 v111 offset:11008\n"
      "v_cvt_pk_bf16_f32 v113, v122, v123\n"
      "v_mfma_f32_16x16x32_bf16 v[64:67], v[144:147], a[128:131], v[64:67]\n"
      "ds_write_addtid_b32 v112 offset:11264\n"
      "v_cvt_pk_bf16_f32 v114, v124, v125\n"
      "v_mfma_f32_16x16x32_bf16 v[68:71], v[144:147], a[132:135], v[68:71]\n"
      "ds_write_addtid_b32 v113 offset:11520\n"
      "v_cvt_pk_bf16_f32 v115, v126, v127\n"
      "v_mfma_f32_16x16x32_bf16 v[64:67], v[148:151], a[160:163], v[64:67]\n"
      "ds_write_addtid_b32 v114 offset:11776\n"
      "v_add_u32 v6, 24576, v242\n"
      "v_mfma_f32_16x16x32_bf16 v[68:71], v[148:151], a[164:167], v[68:71]\n"
      "global_load_dwordx4 v[72:75], v6, s[62:63] offset:0 sc1\n"
      "v_mfma_f32_16x16x32_bf16 v[64:67], v[152:155], a[192:195], v[64:67]\n"
      "global_load_dwordx4 v[76:79], v6, s[62:63] offset:1024 sc1\n"
      "v_mfma_f32_16x16x32_bf16 v[68:71], v[152:155], a[196:199], v[68:71]\n"
      "global_load_dwordx4 v[80:83], v6, s[62:63] offset:2048 sc1\n"
      "v_mfma_f32_16x16x32_bf16 v[64:67], v[156:159], a[224:227], v[64:67]\n"
      "ds_write_addtid_b32 v115 offset:12032\n"
      "v_mfma_f32_16x16x32_bf16 v[68:71], v[156:159], a[228:231], v[68:71]\n"
      "v_mfma_f32_16x16x32_bf16 v[96:99], v[160:163], a[0:3], v[96:99]\n"
      "v_mfma_f32_16x16x32_bf16 v[100:103], v[160:163], a[4:7], v[100:103]\n"
      "s_lshr_b32 s43, s42, 7\n"
      "s_cmp_eq_u32 s43, s36\n"
      "s_cbranch_scc0 .Ls_e19_%=\n"
      "s_waitcnt vmcnt(6)\n"
      "v_bitop3_b32 v61, v25, v27, s37 bitop3:0x7e\n"
      "v_bitop3_b32 v61, v61, v29, s37 bitop3:0xf6\n"
      "v_bitop3_b32 v61, v61, v31, s37 bitop3:0xf6\n"
      "v_bitop3_b32 v61, v61, v33, s37 bitop3:0xf6\n"
      "v_bitop3_b32 v61, v61, v35, s37 bitop3:0xf6\n"
      "v_bitop3_b32 v61, v61, v37, s37 bitop3:0xf6\n"
      "v_bitop3_b32 v61, v61, v39, s37 bitop3:0xf6\n"
      "v_cmp_ne_u32 vcc, 0, v61\n"
      "s_nop 1\n"
      "s_cbranch_vccnz .Ls_s21_%=\n"
      ".Ls_o22_%=:\n"
      "v_add_f32 v24, v24, v28\n"
      "v_add_f32 v26, v26, v30\n"
      "v_add_f32 v32, v32, v36\n"
      "v_add_f32 v34, v34, v38\n"
      "v_add_f32 v24, v24, v32\n"
      "v_add_f32 v26, v26, v34\n"
      "s_nop 1\n"
      "v_permlane32_swap_b32 v24, v26\n"
      "v_add_f32 v24, v24, v26\n"
      "v_mov_b32 v26, v24\n"
      "s_nop 1\n"
      "v_permlane16_swap_b32 v24, v26\n"
      "v_add_f32 v24, v24, v26\n"
      "ds_write_b32 v59, v24\n"
      "s_waitcnt lgkmcnt(0)\n"
      ".Ls_e19_%=:\n"
      "v_mfma_f32_16x16x32_bf16 v[96:99], v[164:167], a[32:35], v[96:99]\n"
      "s_waitcnt lgkmcnt(0)\n"
      "s_barrier\n"
      "v_mfma_f32_16x16x32_bf16 v[100:103], v[164:167], a[36:39], v[100:103]\n"
      "s_lshr_b32 s43, s42, 5\n"
      "s_lshl2_add_u32 s44, s36, s33\n"
      "s_cmp_eq_u32 s43, s44\n"
      "s_cbranch_scc0 .Ls_e23_%=\n"
      "ds_read_b32 v24, v58 offset:0\n"
      "ds_read_b32 v25, v58 offset:8192\n"
      "ds_read_b32 v26, v58 offset:16384\n"
      "ds_read_b32 v27, v58 offset:24576\n"
      "v_mov_b32 v62, 0\n"
      "v_mov_b32 v63, s37\n"
      "s_or_b32 s43, s34, s35\n"
      "s_cmp_eq_u32 s43, 0\n"
      "s_cbranch_scc0 .Ls_z24_%=\n"
      "s_mov_b64 exec, 1\n"
      "global_store_dword v62, v63, s[58:59] sc1\n"
      ".Ls_z24_%=:\n"
      "s_mov_b64 exec, -1\n"
      "s_waitcnt vmcnt(6)\n"
      "v_lshlrev_b32 v57, 16, v57\n"
      "v_and_b32 v56, 48, v243\n"
      "s_mov_b32 s44, 131072\n"
      "v_lshl_add_u32 v56, v56, 1, s44\n"
      "s_waitcnt lgkmcnt(0)\n"
      "v_add_f32 v24, v24, v25\n"
      "s_waitcnt lgkmcnt(0)\n"
      "v_add_f32 v26, v26, v27\n"
      "v_add_f32 v24, v24, v26\n"
      "v_cvt_pk_bf16_f32 v24, v24, v24\n"
      "v_and_b32 v24, 0xffff0000, v24\n"
      "v_mul_f32 v24, s84, v24\n"
      "v_cvt_pk_bf16_f32 v24, v24, v24\n"
      "v_and_b32 v24, 0xffff0000, v24\n"
      "v_add_f32 v24, v24, v57\n"
      "v_cvt_pk_bf16_f32 v24, v24, v24\n"
      "v_and_b32 v24, 0xffff0000, v24\n"
      "v_sub_f32 v24, v24, v57\n"
      "ds_write_b32 v58, v24 offset:1024\n"
      "ds_read_b128 v[28:31], v56 offset:1024\n"
      "ds_read_b128 v[32:35], v56 offset:1040\n"
      ".Ls_e23_%=:\n"
      "v_mfma_f32_16x16x32_bf16 v[96:99], v[168:171], a[64:67], v[96:99]\n"
      "ds_read2st64_b32 v[116:117], v244 offset0:36 offset1:37\n"
      "v_mfma_f32_16x16x32_bf16 v[100:103], v[168:171], a[68:71], v[100:103]\n"
      "ds_read2st64_b32 v[118:119], v245 offset0:40 offset1:41\n"
      "v_mfma_f32_16x16x32_bf16 v[96:99], v[172:175], a[96:99], v[96:99]\n"
      "ds_read2st64_b32 v[120:121], v244 offset0:38 offset1:39\n"
      "v_mfma_f32_16x16x32_bf16 v[100:103], v[172:175], a[100:103], v[100:103]\n"
      "ds_read2st64_b32 v[122:123], v245 offset0:42 offset1:43\n"
      "v_mfma_f32_16x16x32_bf16 v[96:99], v[176:179], a[128:131], v[96:99]\n"
      "ds_read2st64_b32 v[124:125], v246 offset0:44 offset1:45\n"
      "v_mfma_f32_16x16x32_bf16 v[100:103], v[176:179], a[132:135], v[100:103]\n"
      "ds_read2st64_b32 v[126:127], v246 offset0:46 offset1:47\n"
      "v_mfma_f32_16x16x32_bf16 v[96:99], v[180:183], a[160:163], v[96:99]\n"
      "v_mfma_f32_16x16x32_bf16 v[100:103], v[180:183], a[164:167], v[100:103]\n"
      "v_mfma_f32_16x16x32_bf16 v[96:99], v[184:187], a[192:195], v[96:99]\n"
      "s_waitcnt vmcnt(3)\n"
      "v_bitop3_b32 v54, v47, v51, s37 bitop3:0x7e\n"
      "v_bitop3_b32 v54, v54, v55, s37 bitop3:0xf6\n"
      "v_cmp_ne_u32 vcc, 0, v54\n"
      "s_nop 1\n"
      "s_cbranch_vccnz .Ls_s26_%=\n"
      ".Ls_o27_%=:\n"
      "v_mfma_f32_16x16x32_bf16 v[100:103], v[184:187], a[196:199], v[100:103]\n"
      "v_mov_b32 v47, v52\n"
      "v_mov_b32 v51, v53\n"
      "v_mfma_f32_16x16x32_bf16 v[96:99], v[188:191], a[224:227], v[96:99]\n"
      "s_waitcnt lgkmcnt(0)\n"
      "v_mfma_f32_16x16x32_bf16 v[96:99], v[250:253], v[116:119], v[96:99]\n"
      "v_mfma_f32_16x16x32_bf16 v[100:103], v[188:191], a[228:231], v[100:103]\n"
      "v_mfma_f32_16x16x32_bf16 v[100:103], v[250:253], v[120:123], v[100:103]\n"
      "v_mfma_f32_16x16x32_bf16 v[96:99], v[252:255], v[124:127], v[96:99]\n"
      "v_mfma_f32_16x16x32_bf16 v[100:103], v[248:251], v[124:127], v[100:103]\n"
      "v_mfma_f32_16x16x32_bf16 v[64:67], v[252:255], v[44:47], v[64:67]\n"
      "v_mfma_f32_16x16x32_bf16 v[68:71], v[248:251], v[44:47], v[68:71]\n"
      "v_mfma_f32_16x16x32_bf16 v[96:99], v[252:255], v[48:51], v[96:99]\n"
      "v_mfma_f32_16x16x32_bf16 v[100:103], v[248:251], v[48:51], v[100:103]\n"
      "s_lshr_b32 s43, s42, 5\n"
      "s_lshl2_add_u32 s44, s36, s33\n"
      "s_cmp_eq_u32 s43, s44\n"
      "s_cbranch_scc0 .Ls_e28_%=\n"
      "v_lshrrev_b32 v24, 10, v7\n"
      "v_subrev_u32 v24, s42, v24\n"
      "v_cmp_eq_u32 vcc, 0, v24\n"
      "s_nop 1\n"
      "s_mov_b64 exec, vcc\n"
      "v_fma_f32 v208, v205, v28, v208\n"
      "v_fma_f32 v209, v205, v29, v209\n"
      "v_fma_f32 v210, v205, v30, v210\n"
      "v_fma_f32 v211, v205, v31, v211\n"
      "v_fma_f32 v216, v205, v32, v216\n"
      "v_fma_f32 v217, v205, v33, v217\n"
      "v_fma_f32 v218, v205, v34, v218\n"
      "v_fma_f32 v219, v205, v35, v219\n"
      "s_mov_b64 exec, -1\n"
      "v_cmp_eq_u32 vcc, -16, v24\n"
      "s_nop 1\n"
      "s_mov_b64 exec, vcc\n"
      "v_fma_f32 v212, v207, v28, v212\n"
      "v_fma_f32 v213, v207, v29, v213\n"
      "v_fma_f32 v214, v207, v30, v214\n"
      "v_fma_f32 v215, v207, v31, v215\n"
      "v_fma_f32 v220, v207, v32, v220\n"
      "v_fma_f32 v221, v207, v33, v221\n"
      "v_fma_f32 v222, v207, v34, v222\n"
      "v_fma_f32 v223, v207, v35, v223\n"
      "s_mov_b64 exec, -1\n"
      ".Ls_e28_%=:\n"
      "s_waitcnt vmcnt(0)\n"
      "v_bitop3_b32 v82, v75, v79, s37 bitop3:0x7e\n"
      "v_bitop3_b32 v82, v82, v83, s37 bitop3:0xf6\n"
      "v_cmp_ne_u32 vcc, 0, v82\n"
      "s_nop 1\n"
      "s_cbranch_vccnz .Ls_s30_%=\n"
      ".Ls_o31_%=:\n"
      "v_mov_b32 v75, v80\n"
      "v_mov_b32 v79, v81\n"
      "s_nop 0\n"
      "v_mfma_f32_16x16x32_bf16 v[64:67], v[252:255], v[72:75], v[64:67]\n"
      "v_mfma_f32_16x16x32_bf16 v[96:99], v[252:255], v[76:79], v[96:99]\n"
      "v_mfma_f32_16x16x32_bf16 v[68:71], v[248:251], v[72:75], v[68:71]\n"
      "v_mfma_f32_16x16x32_bf16 v[100:103], v[248:251], v[76:79], v[100:103]\n"
      "s_nop 4\n"
      "v_fma_f32 v208, v204, v64, v208\n"
      "v_fma_f32 v209, v204, v65, v209\n"
      "v_fma_f32 v210, v204, v66, v210\n"
      "v_fma_f32 v211, v204, v67, v211\n"
      "v_fma_f32 v216, v204, v96, v216\n"
      "v_fma_f32 v217, v204, v97, v217\n"
      "v_fma_f32 v218, v204, v98, v218\n"
      "v_fma_f32 v219, v204, v99, v219\n"
      "v_cvt_pk_bf16_f32 v104, v208, v209\n"
      "v_cvt_pk_bf16_f32 v105, v210, v211\n"
      "v_cvt_pk_bf16_f32 v106, v216, v217\n"
      "v_cvt_pk_bf16_f32 v107, v218, v219\n"
      "global_store_dwordx4 v7, v[104:107], s[20:21]\n"
      "v_fma_f32 v212, v206, v68, v212\n"
      "v_fma_f32 v213, v206, v69, v213\n"
      "v_fma_f32 v214, v206, v70, v214\n"
      "v_fma_f32 v215, v206, v71, v215\n"
      "v_fma_f32 v220, v206, v100, v220\n"
      "v_fma_f32 v221, v206, v101, v221\n"
      "v_fma_f32 v222, v206, v102, v222\n"
      "v_fma_f32 v223, v206, v103, v223\n"
      "v_cvt_pk_bf16_f32 v108, v212, v213\n"
      "v_cvt_pk_bf16_f32 v109, v214, v215\n"
      "v_cvt_pk_bf16_f32 v110, v220, v221\n"
      "v_cvt_pk_bf16_f32 v111, v222, v223\n"
      "global_store_dwordx4 v7, v[108:111], s[68:69]\n"
      "s_endpgm\n"
      ".Lprod_%=:\n"
      "s_waitcnt lgkmcnt(0)\n"
      "s_add_u32 s68, s16, s44\n"
      "s_addc_u32 s69, s17, 0\n"
      "s_add_u32 s12, s12, s45\n"
      "s_addc_u32 s13, s13, 0\n"
      "s_add_u32 s58, s18, 4718592\n"
      "s_addc_u32 s59, s19, 0\n"
      "v_mov_b32 v235, 0\n"
      "global_load_dword v235, v235, s[58:59] sc1\n"
      "v_and_b32 v238, 15, v243\n"
      "v_lshrrev_b32 v239, 4, v243\n"
      "v_xor_b32 v239, v239, v238\n"
      "v_and_b32 v239, 7, v239\n"
      "v_lshlrev_b32 v239, 4, v239\n"
      "v_lshl_or_b32 v238, v238, 7, v239\n"
      "v_add_u32 v248, s41, v238\n"
      "v_xor_b32 v249, 64, v248\n"
      "v_add_u32 v250, 65536, v238\n"
      "v_xor_b32 v251, 64, v250\n"
      "v_and_b32 v252, 8, v243\n"
      "v_lshlrev_b32 v252, 7, v252\n"
      "v_and_or_b32 v252, v243, 48, v252\n"
      "v_add_u32 v252, s41, v252\n"
      "v_add_u32 v238, s80, v242\n"
      "v_and_b32 v238, 0x3ff, v238\n"
      "v_lshl_or_b32 v244, v236, 12, v237\n"
      "v_add_u32 v245, 31744, v244\n"
      "v_add_u32 v246, 63488, v244\n"
      "v_add_u32 v247, 95232, v244\n"
      "s_bitcmp1_b32 s34, 1\n"
      "s_cselect_b64 s[8:9], s[10:11], s[8:9]\n"
      "s_and_b32 s45, s34, 1\n"
      "s_lshl2_add_u32 s45, s45, s33\n"
      "s_lshl_b32 s45, s45, 9\n"
      "s_lshl_b32 s76, s36, 19\n"
      "s_add_u32 s45, s45, s76\n"
      "s_add_u32 s8, s8, s45\n"
      "s_addc_u32 s9, s9, 0\n"
      "s_lshr_b32 s39, s32, 6\n"
      "s_and_b32 s45, s35, 2\n"
      "s_xor_b32 s39, s39, s45\n"
      "s_xor_b32 s39, s39, s33\n"
      "s_lshl_b32 s45, s39, 7\n"
      "s_add_u32 s76, s33, 0\n"
      "s_and_b32 s76, s76, 3\n"
      "s_lshl_b32 s76, s76, 17\n"
      "s_or_b32 s76, s76, s45\n"
      "s_add_u32 s77, s33, 1\n"
      "s_and_b32 s77, s77, 3\n"
      "s_lshl_b32 s77, s77, 17\n"
      "s_or_b32 s77, s77, s45\n"
      "s_add_u32 s78, s33, 2\n"
      "s_and_b32 s78, s78, 3\n"
      "s_lshl_b32 s78, s78, 17\n"
      "s_or_b32 s78, s78, s45\n"
      "s_add_u32 s79, s33, 3\n"
      "s_and_b32 s79, s79, 3\n"
      "s_lshl_b32 s79, s79, 17\n"
      "s_or_b32 s79, s79, s45\n"
      "s_lshl2_add_u32 s43, s34, s35\n"
      "s_and_b32 s44, s40, 31\n"
      "s_mul_i32 s45, s43, 266240\n"
      "s_lshl_b32 s42, s44, 13\n"
      "s_add_u32 s45, s45, s42\n"
      "s_add_u32 s45, s45, 4096\n"
      "s_add_u32 s62, s18, s45\n"
      "s_addc_u32 s63, s19, 0\n"
      "s_bitcmp1_b32 s34, 1\n"
      "s_cselect_b32 s85, 0x80008000, 0\n"
      "s_lshl3_add_u32 s42, s43, s83\n"
      "s_lshl2_add_u32 s42, s42, s33\n"
      "s_lshl_b32 s42, s42, 9\n"
      "s_add_u32 s42, s42, 1048576\n"
      "s_mov_b32 s64, 0xaaaaaaaa\n"
      "s_mov_b32 s65, 0xaaaaaaaa\n"
      "s_mov_b32 s66, 0xcccccccc\n"
      "s_mov_b32 s67, 0xcccccccc\n"
      "s_add_u32 s45, s80, 512\n"
      "s_and_b32 s45, s45, 0x380\n"
      "s_add_u32 s46, s68, s45\n"
      "s_addc_u32 s47, s69, 0\n"
      "s_add_u32 m0, s41, 8192\n"
      "s_nop 0\n"
      "global_load_lds_dwordx4 v240, s[46:47] offset:0\n"
      "global_load_lds_dwordx4 v241, s[46:47] offset:1024\n"
      "global_load_lds_dwordx4 v254, s[46:47] offset:2048\n"
      "global_load_lds_dwordx4 v255, s[46:47] offset:3072\n"
      "s_add_u32 s45, s80, 768\n"
      "s_and_b32 s45, s45, 0x380\n"
      "s_add_u32 s46, s68, s45\n"
      "s_addc_u32 s47, s69, 0\n"
      "s_add_u32 m0, s41, 12288\n"
      "s_nop 0\n"
      "global_load_lds_dwordx4 v240, s[46:47] offset:0\n"
      "global_load_lds_dwordx4 v241, s[46:47] offset:1024\n"
      "global_load_lds_dwordx4 v254, s[46:47] offset:2048\n"
      "global_load_lds_dwordx4 v255, s[46:47] offset:3072\n"
      "s_waitcnt vmcnt(13)\n"
      "s_barrier\n"
      "ds_read_b128 v[0:3], v248 offset:0\n"
      "ds_read_b128 v[4:7], v248 offset:2048\n"
      "ds_read_b128 v[8:11], v249 offset:0\n"
      "ds_read_b128 v[12:15], v249 offset:2048\n"
      "ds_read_b128 a[192:195], v250 offset:0\n"
      "ds_read_b128 a[196:199], v250 offset:2048\n"
      "ds_read_b128 a[200:203], v250 offset:32768\n"
      "ds_read_b128 a[204:207], v251 offset:0\n"
      "ds_read_b128 a[208:211], v251 offset:2048\n"
      "ds_read_b128 a[212:215], v251 offset:32768\n"
      "s_waitcnt lgkmcnt(5)\n"
      "v_mfma_f32_16x16x32_bf16 v[192:195], v[0:3], a[192:195], 0\n"
      "s_add_u32 s70, s34, 1\n"
      "s_and_b32 s70, s70, 3\n"
      "s_lshl_b32 s70, s70, 15\n"
      "s_or_b32 s70, s70, s81\n"
      "s_xor_b32 s70, s70, 128\n"
      "s_add_u32 s70, s12, s70\n"
      "s_addc_u32 s71, s13, 0\n"
      "s_add_u32 m0, s82, 81920\n"
      "v_mfma_f32_16x16x32_bf16 v[196:199], v[4:7], a[192:195], 0\n"
      "global_load_lds_dwordx4 v240, s[70:71] offset:0\n"
      "ds_read_b128 a[216:219], v250 offset:4096\n"
      "s_waitcnt lgkmcnt(5)\n"
      "v_mfma_f32_16x16x32_bf16 v[200:203], v[0:3], a[196:199], 0\n"
      "ds_read_b128 a[220:223], v250 offset:6144\n"
      "ds_read_b128 a[224:227], v250 offset:36864\n"
      "v_mfma_f32_16x16x32_bf16 v[204:207], v[4:7], a[196:199], 0\n"
      "global_load_lds_dwordx4 v241, s[70:71] offset:1024\n"
      "s_waitcnt vmcnt(11)\n"
      "ds_read_b128 v[16:19], v248 offset:4096\n"
      "s_waitcnt lgkmcnt(7)\n"
      "v_mfma_f32_16x16x32_bf16 v[208:211], v[0:3], a[200:203], 0\n"
      "ds_read_b128 v[20:23], v248 offset:6144\n"
      "ds_read_b128 v[24:27], v249 offset:4096\n"
      "v_mfma_f32_16x16x32_bf16 v[212:215], v[4:7], a[200:203], 0\n"
      "global_load_lds_dwordx4 v254, s[70:71] offset:2048\n"
      "ds_read_b128 v[28:31], v249 offset:6144\n"
      "s_waitcnt lgkmcnt(9)\n"
      "v_mfma_f32_16x16x32_bf16 v[192:195], v[8:11], a[204:207], v[192:195]\n"
      "ds_read_b128 a[228:231], v251 offset:4096\n"
      "ds_read_b128 a[232:235], v251 offset:6144\n"
      "v_mfma_f32_16x16x32_bf16 v[196:199], v[12:15], a[204:207], v[196:199]\n"
      "global_load_lds_dwordx4 v255, s[70:71] offset:3072\n"
      "ds_read_b128 a[236:239], v251 offset:36864\n"
      "s_waitcnt lgkmcnt(11)\n"
      "v_mfma_f32_16x16x32_bf16 v[200:203], v[8:11], a[208:211], v[200:203]\n"
      "s_add_u32 s70, s34, 2\n"
      "s_and_b32 s70, s70, 3\n"
      "s_lshl_b32 s70, s70, 15\n"
      "s_or_b32 s70, s70, s81\n"
      "s_xor_b32 s70, s70, 128\n"
      "s_add_u32 s70, s12, s70\n"
      "s_addc_u32 s71, s13, 0\n"
      "s_add_u32 m0, s82, 114688\n"
      "v_mfma_f32_16x16x32_bf16 v[204:207], v[12:15], a[208:211], v[204:207]\n"
      "global_load_lds_dwordx4 v240, s[70:71] offset:0\n"
      "s_waitcnt lgkmcnt(10)\n"
      "v_mfma_f32_16x16x32_bf16 v[208:211], v[8:11], a[212:215], v[208:211]\n"
      "v_mfma_f32_16x16x32_bf16 v[212:215], v[12:15], a[212:215], v[212:215]\n"
      "global_load_lds_dwordx4 v241, s[70:71] offset:1024\n"
      "s_waitcnt lgkmcnt(6)\n"
      "v_mfma_f32_16x16x32_bf16 v[192:195], v[16:19], a[216:219], v[192:195]\n"
      "s_add_u32 s70, s80, 128\n"
      "s_and_b32 s70, s70, 0x380\n"
      "s_add_u32 s70, s68, s70\n"
      "s_addc_u32 s71, s69, 0\n"
      "s_add_u32 m0, s41, 0\n"
      "s_waitcnt lgkmcnt(5)\n"
      "v_mfma_f32_16x16x32_bf16 v[196:199], v[20:23], a[216:219], v[196:199]\n"
      "global_load_lds_dwordx4 v240, s[70:71] offset:0\n"
      "ds_read_b128 a[240:243], v250 offset:8192\n"
      "v_mfma_f32_16x16x32_bf16 v[200:203], v[16:19], a[220:223], v[200:203]\n"
      "ds_read_b128 a[244:247], v250 offset:10240\n"
      "ds_read_b128 a[248:251], v250 offset:40960\n"
      "v_mfma_f32_16x16x32_bf16 v[204:207], v[20:23], a[220:223], v[204:207]\n"
      "global_load_lds_dwordx4 v241, s[70:71] offset:1024\n"
      "s_waitcnt vmcnt(12)\n"
      "ds_read_b128 v[32:35], v248 offset:8192\n"
      "v_mfma_f32_16x16x32_bf16 v[208:211], v[16:19], a[224:227], v[208:211]\n"
      "ds_read_b128 v[36:39], v248 offset:10240\n"
      "ds_read_b128 v[40:43], v249 offset:8192\n"
      "v_mfma_f32_16x16x32_bf16 v[212:215], v[20:23], a[224:227], v[212:215]\n"
      "global_load_lds_dwordx4 v254, s[70:71] offset:2048\n"
      "ds_read_b128 v[44:47], v249 offset:10240\n"
      "s_waitcnt lgkmcnt(9)\n"
      "v_mfma_f32_16x16x32_bf16 v[192:195], v[24:27], a[228:231], v[192:195]\n"
      "ds_read_b128 a[252:255], v251 offset:8192\n"
      "ds_read_b128 a[192:195], v251 offset:10240\n"
      "v_mfma_f32_16x16x32_bf16 v[196:199], v[28:31], a[228:231], v[196:199]\n"
      "global_load_lds_dwordx4 v255, s[70:71] offset:3072\n"
      "ds_read_b128 a[196:199], v251 offset:40960\n"
      "s_waitcnt lgkmcnt(11)\n"
      "v_mfma_f32_16x16x32_bf16 v[200:203], v[24:27], a[232:235], v[200:203]\n"
      "s_add_u32 s70, s80, 384\n"
      "s_and_b32 s70, s70, 0x380\n"
      "s_add_u32 s70, s68, s70\n"
      "s_addc_u32 s71, s69, 0\n"
      "s_add_u32 m0, s41, 4096\n"
      "v_mfma_f32_16x16x32_bf16 v[204:207], v[28:31], a[232:235], v[204:207]\n"
      "global_load_lds_dwordx4 v240, s[70:71] offset:0\n"
      "s_waitcnt lgkmcnt(10)\n"
      "v_mfma_f32_16x16x32_bf16 v[208:211], v[24:27], a[236:239], v[208:211]\n"
      "v_mfma_f32_16x16x32_bf16 v[212:215], v[28:31], a[236:239], v[212:215]\n"
      "global_load_lds_dwordx4 v241, s[70:71] offset:1024\n"
      "s_waitcnt lgkmcnt(6)\n"
      "v_mfma_f32_16x16x32_bf16 v[192:195], v[32:35], a[240:243], v[192:195]\n"
      "ds_read_b128 a[200:203], v250 offset:12288\n"
      "ds_read_b128 a[204:207], v250 offset:14336\n"
      "s_waitcnt lgkmcnt(7)\n"
      "v_mfma_f32_16x16x32_bf16 v[196:199], v[36:39], a[240:243], v[196:199]\n"
      "global_load_lds_dwordx4 v254, s[70:71] offset:2048\n"
      "ds_read_b128 a[208:211], v250 offset:45056\n"
      "v_mfma_f32_16x16x32_bf16 v[200:203], v[32:35], a[244:247], v[200:203]\n"
      "s_waitcnt vmcnt(13)\n"
      "ds_read_b128 v[48:51], v248 offset:12288\n"
      "v_mfma_f32_16x16x32_bf16 v[204:207], v[36:39], a[244:247], v[204:207]\n"
      "global_load_lds_dwordx4 v255, s[70:71] offset:3072\n"
      "ds_read_b128 v[52:55], v248 offset:14336\n"
      "v_mfma_f32_16x16x32_bf16 v[208:211], v[32:35], a[248:251], v[208:211]\n"
      "s_add_u32 s70, s80, 640\n"
      "s_and_b32 s70, s70, 0x380\n"
      "s_add_u32 s70, s68, s70\n"
      "s_addc_u32 s71, s69, 0\n"
      "s_waitcnt lgkmcnt(8)\n"
      "s_add_u32 m0, s41, 8192\n"
      "v_mfma_f32_16x16x32_bf16 v[212:215], v[36:39], a[248:251], v[212:215]\n"
      "global_load_lds_dwordx4 v240, s[70:71] offset:0\n"
      "ds_read_b128 v[56:59], v249 offset:12288\n"
      "s_waitcnt lgkmcnt(8)\n"
      "v_mfma_f32_16x16x32_bf16 v[192:195], v[40:43], a[252:255], v[192:195]\n"
      "ds_read_b128 v[60:63], v249 offset:14336\n"
      "ds_read_b128 a[212:215], v251 offset:12288\n"
      "v_readfirstlane_b32 s37, v235\n"
      "v_mfma_f32_16x16x32_bf16 v[196:199], v[44:47], a[252:255], v[196:199]\n"
      "s_nop 0\n"
      "s_mul_i32 s37, s37, 0x9E3779B1\n"
      "global_load_lds_dwordx4 v241, s[70:71] offset:1024\n"
      "s_waitcnt lgkmcnt(9)\n"
      "v_mfma_f32_16x16x32_bf16 v[200:203], v[40:43], a[192:195], v[200:203]\n"
      "s_add_u32 s37, s37, 0x7F4A7C15\n"
      "ds_read_b128 a[216:219], v251 offset:14336\n"
      "ds_read_b128 a[220:223], v251 offset:45056\n"
      "s_mov_b32 s38, 0x8000\n"
      "v_mfma_f32_16x16x32_bf16 v[204:207], v[44:47], a[192:195], v[204:207]\n"
      "global_load_lds_dwordx4 v254, s[70:71] offset:2048\n"
      "s_waitcnt lgkmcnt(10)\n"
      "v_mfma_f32_16x16x32_bf16 v[208:211], v[40:43], a[196:199], v[208:211]\n"
      "v_mfma_f32_16x16x32_bf16 v[212:215], v[44:47], a[196:199], v[212:215]\n"
      "global_load_lds_dwordx4 v255, s[70:71] offset:3072\n"
      "s_waitcnt lgkmcnt(6)\n"
      "v_mfma_f32_16x16x32_bf16 v[192:195], v[48:51], a[200:203], v[192:195]\n"
      "s_add_u32 s70, s80, 896\n"
      "s_and_b32 s70, s70, 0x380\n"
      "s_add_u32 s70, s68, s70\n"
      "s_addc_u32 s71, s69, 0\n"
      "s_waitcnt lgkmcnt(3)\n"
      "s_add_u32 m0, s41, 12288\n"
      "v_mfma_f32_16x16x32_bf16 v[196:199], v[52:55], a[200:203], v[196:199]\n"
      "global_load_lds_dwordx4 v240, s[70:71] offset:0\n"
      "s_waitcnt vmcnt(13)\n"
      "s_barrier\n"
      "v_mfma_f32_16x16x32_bf16 v[200:203], v[48:51], a[204:207], v[200:203]\n"
      "ds_read_b128 a[224:227], v250 offset:16384\n"
      "ds_read_b128 a[228:231], v250 offset:18432\n"
      "v_mfma_f32_16x16x32_bf16 v[204:207], v[52:55], a[204:207], v[204:207]\n"
      "global_load_lds_dwordx4 v241, s[70:71] offset:1024\n"
      "ds_read_b128 a[232:235], v250 offset:49152\n"
      "v_mfma_f32_16x16x32_bf16 v[208:211], v[48:51], a[208:211], v[208:211]\n"
      "s_waitcnt vmcnt(10)\n"
      "ds_read_b128 v[64:67], v248 offset:0\n"
      "v_mfma_f32_16x16x32_bf16 v[212:215], v[52:55], a[208:211], v[212:215]\n"
      "global_load_lds_dwordx4 v254, s[70:71] offset:2048\n"
      "ds_read_b128 v[68:71], v248 offset:2048\n"
      "s_waitcnt lgkmcnt(7)\n"
      "v_mfma_f32_16x16x32_bf16 v[192:195], v[56:59], a[212:215], v[192:195]\n"
      "ds_read_b128 v[72:75], v249 offset:0\n"
      "ds_read_b128 v[76:79], v249 offset:2048\n"
      "v_mfma_f32_16x16x32_bf16 v[196:199], v[60:63], a[212:215], v[196:199]\n"
      "global_load_lds_dwordx4 v255, s[70:71] offset:3072\n"
      "ds_read_b128 a[236:239], v251 offset:16384\n"
      "s_waitcnt lgkmcnt(9)\n"
      "v_mfma_f32_16x16x32_bf16 v[200:203], v[56:59], a[216:219], v[200:203]\n"
      "ds_read_b128 a[240:243], v251 offset:18432\n"
      "ds_read_b128 a[244:247], v251 offset:49152\n"
      "v_mfma_f32_16x16x32_bf16 v[204:207], v[60:63], a[216:219], v[204:207]\n"
      "s_waitcnt lgkmcnt(10)\n"
      "v_mfma_f32_16x16x32_bf16 v[208:211], v[56:59], a[220:223], v[208:211]\n"
      "v_mfma_f32_16x16x32_bf16 v[212:215], v[60:63], a[220:223], v[212:215]\n"
      "s_waitcnt lgkmcnt(6)\n"
      "v_mfma_f32_16x16x32_bf16 v[192:195], v[64:67], a[224:227], v[192:195]\n"
      "s_waitcnt lgkmcnt(3)\n"
      "s_add_u32 m0, s41, 0\n"
      "s_nop 0\n"
      "global_load_lds_dwordx4 v238, s[24:25]\n"
      "s_add_u32 m0, s41, 1024\n"
      "s_nop 0\n"
      "global_load_lds_dwordx4 v238, s[22:23]\n"
      "v_mfma_f32_16x16x32_bf16 v[196:199], v[68:71], a[224:227], v[196:199]\n"
      "ds_read_b128 a[248:251], v250 offset:20480\n"
      "ds_read_b128 a[252:255], v250 offset:22528\n"
      "v_mfma_f32_16x16x32_bf16 v[200:203], v[64:67], a[228:231], v[200:203]\n"
      "s_add_u32 s70, s34, 2\n"
      "s_and_b32 s70, s70, 3\n"
      "s_lshl_b32 s70, s70, 15\n"
      "s_or_b32 s70, s70, s81\n"
      "s_add_u32 s70, s12, s70\n"
      "s_addc_u32 s71, s13, 0\n"
      "s_add_u32 m0, s82, 98304\n"
      "v_mfma_f32_16x16x32_bf16 v[204:207], v[68:71], a[228:231], v[204:207]\n"
      "global_load_lds_dwordx4 v254, s[70:71] offset:2048\n"
      "ds_read_b128 a[192:195], v250 offset:53248\n"
      "v_mfma_f32_16x16x32_bf16 v[208:211], v[64:67], a[232:235], v[208:211]\n"
      "s_waitcnt vmcnt(11)\n"
      "ds_read_b128 v[80:83], v248 offset:4096\n"
      "v_mfma_f32_16x16x32_bf16 v[212:215], v[68:71], a[232:235], v[212:215]\n"
      "ds_read_b128 v[84:87], v248 offset:6144\n"
      "ds_read_b128 v[88:91], v249 offset:4096\n"
      "s_waitcnt lgkmcnt(8)\n"
      "v_mfma_f32_16x16x32_bf16 v[192:195], v[72:75], a[236:239], v[192:195]\n"
      "global_load_lds_dwordx4 v255, s[70:71] offset:3072\n"
      "ds_read_b128 v[92:95], v249 offset:6144\n"
      "v_mfma_f32_16x16x32_bf16 v[196:199], v[76:79], a[236:239], v[196:199]\n"
      "ds_read_b128 a[196:199], v251 offset:20480\n"
      "ds_read_b128 a[200:203], v251 offset:22528\n"
      "s_waitcnt lgkmcnt(10)\n"
      "v_mfma_f32_16x16x32_bf16 v[200:203], v[72:75], a[240:243], v[200:203]\n"
      "s_add_u32 s70, s34, 2\n"
      "s_and_b32 s70, s70, 3\n"
      "s_lshl_b32 s70, s70, 15\n"
      "s_or_b32 s70, s70, s81\n"
      "s_xor_b32 s70, s70, 128\n"
      "s_add_u32 s70, s12, s70\n"
      "s_addc_u32 s71, s13, 0\n"
      "s_add_u32 m0, s82, 114688\n"
      "v_mfma_f32_16x16x32_bf16 v[204:207], v[76:79], a[240:243], v[204:207]\n"
      "global_load_lds_dwordx4 v254, s[70:71] offset:2048\n"
      "ds_read_b128 a[204:207], v251 offset:53248\n"
      "s_waitcnt lgkmcnt(10)\n"
      "v_mfma_f32_16x16x32_bf16 v[208:211], v[72:75], a[244:247], v[208:211]\n"
      "v_mfma_f32_16x16x32_bf16 v[212:215], v[76:79], a[244:247], v[212:215]\n"
      "s_waitcnt lgkmcnt(6)\n"
      "v_mfma_f32_16x16x32_bf16 v[192:195], v[80:83], a[248:251], v[192:195]\n"
      "global_load_lds_dwordx4 v255, s[70:71] offset:3072\n"
      "ds_read_b128 a[208:211], v250 offset:24576\n"
      "s_waitcnt lgkmcnt(6)\n"
      "v_mfma_f32_16x16x32_bf16 v[196:199], v[84:87], a[248:251], v[196:199]\n"
      "ds_read_b128 a[212:215], v250 offset:26624\n"
      "ds_read_b128 a[216:219], v250 offset:57344\n"
      "v_mfma_f32_16x16x32_bf16 v[200:203], v[80:83], a[252:255], v[200:203]\n"
      "s_add_u32 s70, s34, 3\n"
      "s_and_b32 s70, s70, 3\n"
      "s_lshl_b32 s70, s70, 15\n"
      "s_or_b32 s70, s70, s81\n"
      "s_add_u32 s70, s12, s70\n"
      "s_addc_u32 s71, s13, 0\n"
      "s_add_u32 m0, s82, 131072\n"
      "v_mfma_f32_16x16x32_bf16 v[204:207], v[84:87], a[252:255], v[204:207]\n"
      "global_load_lds_dwordx4 v240, s[70:71] offset:0\n"
      "s_waitcnt vmcnt(11)\n"
      "ds_read_b128 v[96:99], v248 offset:8192\n"
      "v_mfma_f32_16x16x32_bf16 v[208:211], v[80:83], a[192:195], v[208:211]\n"
      "ds_read_b128 v[100:103], v248 offset:10240\n"
      "ds_read_b128 v[104:107], v249 offset:8192\n"
      "v_mfma_f32_16x16x32_bf16 v[212:215], v[84:87], a[192:195], v[212:215]\n"
      "ds_read_b128 v[108:111], v249 offset:10240\n"
      "ds_read_b128 a[220:223], v251 offset:24576\n"
      "s_waitcnt lgkmcnt(10)\n"
      "v_mfma_f32_16x16x32_bf16 v[192:195], v[88:91], a[196:199], v[192:195]\n"
      "global_load_lds_dwordx4 v241, s[70:71] offset:1024\n"
      "ds_read_b128 a[224:227], v251 offset:26624\n"
      "v_mfma_f32_16x16x32_bf16 v[196:199], v[92:95], a[196:199], v[196:199]\n"
      "ds_read_b128 a[228:231], v251 offset:57344\n"
      "s_waitcnt lgkmcnt(11)\n"
      "v_mfma_f32_16x16x32_bf16 v[200:203], v[88:91], a[200:203], v[200:203]\n"
      "v_mfma_f32_16x16x32_bf16 v[204:207], v[92:95], a[200:203], v[204:207]\n"
      "global_load_lds_dwordx4 v254, s[70:71] offset:2048\n"
      "s_waitcnt lgkmcnt(10)\n"
      "v_mfma_f32_16x16x32_bf16 v[208:211], v[88:91], a[204:207], v[208:211]\n"
      "v_mfma_f32_16x16x32_bf16 v[212:215], v[92:95], a[204:207], v[212:215]\n"
      "s_waitcnt lgkmcnt(6)\n"
      "v_mfma_f32_16x16x32_bf16 v[192:195], v[96:99], a[208:211], v[192:195]\n"
      "global_load_lds_dwordx4 v255, s[70:71] offset:3072\n"
      "ds_read_b128 a[232:235], v250 offset:28672\n"
      "s_waitcnt lgkmcnt(6)\n"
      "v_mfma_f32_16x16x32_bf16 v[196:199], v[100:103], a[208:211], v[196:199]\n"
      "ds_read_b128 a[236:239], v250 offset:30720\n"
      "ds_read_b128 a[240:243], v250 offset:61440\n"
      "v_mfma_f32_16x16x32_bf16 v[200:203], v[96:99], a[212:215], v[200:203]\n"
      "s_add_u32 s70, s34, 3\n"
      "s_and_b32 s70, s70, 3\n"
      "s_lshl_b32 s70, s70, 15\n"
      "s_or_b32 s70, s70, s81\n"
      "s_xor_b32 s70, s70, 128\n"
      "s_add_u32 s70, s12, s70\n"
      "s_addc_u32 s71, s13, 0\n"
      "s_add_u32 m0, s82, 147456\n"
      "v_mfma_f32_16x16x32_bf16 v[204:207], v[100:103], a[212:215], v[204:207]\n"
      "global_load_lds_dwordx4 v240, s[70:71] offset:0\n"
      "s_waitcnt vmcnt(11)\n"
      "ds_read_b128 v[112:115], v248 offset:12288\n"
      "v_mfma_f32_16x16x32_bf16 v[208:211], v[96:99], a[216:219], v[208:211]\n"
      "ds_read_b128 v[116:119], v248 offset:14336\n"
      "ds_read_b128 v[120:123], v249 offset:12288\n"
      "v_mfma_f32_16x16x32_bf16 v[212:215], v[100:103], a[216:219], v[212:215]\n"
      "ds_read_b128 v[124:127], v249 offset:14336\n"
      "ds_read_b128 a[244:247], v251 offset:28672\n"
      "s_waitcnt lgkmcnt(10)\n"
      "v_mfma_f32_16x16x32_bf16 v[192:195], v[104:107], a[220:223], v[192:195]\n"
      "global_load_lds_dwordx4 v241, s[70:71] offset:1024\n"
      "ds_read_b128 a[248:251], v251 offset:30720\n"
      "v_mfma_f32_16x16x32_bf16 v[196:199], v[108:111], a[220:223], v[196:199]\n"
      "ds_read_b128 a[252:255], v251 offset:61440\n"
      "s_waitcnt lgkmcnt(11)\n"
      "v_mfma_f32_16x16x32_bf16 v[200:203], v[104:107], a[224:227], v[200:203]\n"
      "v_mfma_f32_16x16x32_bf16 v[204:207], v[108:111], a[224:227], v[204:207]\n"
      "global_load_lds_dwordx4 v254, s[70:71] offset:2048\n"
      "s_waitcnt lgkmcnt(10)\n"
      "v_mfma_f32_16x16x32_bf16 v[208:211], v[104:107], a[228:231], v[208:211]\n"
      "v_mfma_f32_16x16x32_bf16 v[212:215], v[108:111], a[228:231], v[212:215]\n"
      "s_waitcnt lgkmcnt(6)\n"
      "v_mfma_f32_16x16x32_bf16 v[192:195], v[112:115], a[232:235], v[192:195]\n"
      "s_waitcnt vmcnt(11)\n"
      "ds_read_b128 v[136:139], v252 offset:0\n"
      "s_waitcnt lgkmcnt(6)\n"
      "v_mfma_f32_16x16x32_bf16 v[196:199], v[116:119], a[232:235], v[196:199]\n"
      "ds_read_b128 v[140:143], v252 offset:64\n"
      "global_load_lds_dwordx4 v255, s[70:71] offset:3072\n"
      "s_waitcnt lgkmcnt(4)\n"
      "v_mfma_f32_16x16x32_bf16 v[192:195], v[120:123], a[244:247], v[192:195]\n"
      "ds_read_b128 v[144:147], v252 offset:256\n"
      "v_mfma_f32_16x16x32_bf16 v[196:199], v[124:127], a[244:247], v[196:199]\n"
      "v_mfma_f32_16x16x32_bf16 v[200:203], v[112:115], a[236:239], v[200:203]\n"
      "ds_read_b128 v[148:151], v252 offset:320\n"
      "v_mfma_f32_16x16x32_bf16 v[204:207], v[116:119], a[236:239], v[204:207]\n"
      "s_waitcnt lgkmcnt(5)\n"
      "v_mfma_f32_16x16x32_bf16 v[200:203], v[120:123], a[248:251], v[200:203]\n"
      "ds_read_b128 v[152:155], v252 offset:512\n"
      "v_cvt_pk_bf16_f32 v224, v192, v193\n"
      "v_cvt_pk_bf16_f32 v225, v194, v195\n"
      "v_mfma_f32_16x16x32_bf16 v[204:207], v[124:127], a[248:251], v[204:207]\n"
      "v_cvt_pk_bf16_f32 v226, v196, v197\n"
      "v_cvt_pk_bf16_f32 v227, v198, v199\n"
      "v_xor_b32 v224, s85, v224\n"
      "v_mfma_f32_16x16x32_bf16 v[208:211], v[112:115], a[240:243], v[208:211]\n"
      "ds_read_b128 v[156:159], v252 offset:576\n"
      "v_xor_b32 v225, s85, v225\n"
      "v_xor_b32 v226, s85, v226\n"
      "v_mfma_f32_16x16x32_bf16 v[212:215], v[116:119], a[240:243], v[212:215]\n"
      "v_xor_b32 v227, s85, v227\n"
      "s_waitcnt lgkmcnt(6)\n"
      "v_mfma_f32_16x16x32_bf16 v[208:211], v[120:123], a[252:255], v[208:211]\n"
      "ds_read_b128 v[160:163], v252 offset:768\n"
      "global_store_dwordx4 v242, v[224:227], s[62:63] offset:-4096 sc1\n"
      "v_cvt_pk_bf16_f32 v228, v200, v201\n"
      "v_mfma_f32_16x16x32_bf16 v[212:215], v[124:127], a[252:255], v[212:215]\n"
      "v_cvt_pk_bf16_f32 v229, v202, v203\n"
      "v_cvt_pk_bf16_f32 v230, v204, v205\n"
      "v_cvt_pk_bf16_f32 v231, v206, v207\n"
      "s_waitcnt lgkmcnt(6)\n"
      "v_mfma_f32_16x16x32_bf16 v[128:131], v[0:3], v[136:139], 0\n"
      "ds_read_b128 v[164:167], v252 offset:832\n"
      "v_xor_b32 v228, s85, v228\n"
      "v_xor_b32 v229, s85, v229\n"
      "v_mfma_f32_16x16x32_bf16 v[132:135], v[4:7], v[136:139], 0\n"
      "v_xor_b32 v230, s85, v230\n"
      "v_xor_b32 v231, s85, v231\n"
      "s_waitcnt lgkmcnt(6)\n"
      "v_mfma_f32_16x16x32_bf16 v[128:131], v[8:11], v[140:143], v[128:131]\n"
      "ds_read_b128 v[136:139], v252 offset:128\n"
      "global_store_dwordx4 v242, v[228:231], s[62:63] offset:-3072 sc1\n"
      "v_cvt_pk_bf16_f32 v232, v208, v209\n"
      "v_mfma_f32_16x16x32_bf16 v[132:135], v[12:15], v[140:143], v[132:135]\n"
      "v_cvt_pk_bf16_f32 v233, v210, v211\n"
      "v_cvt_pk_bf16_f32 v234, v212, v213\n"
      "v_cvt_pk_bf16_f32 v235, v214, v215\n"
      "s_waitcnt lgkmcnt(6)\n"
      "v_mfma_f32_16x16x32_bf16 v[128:131], v[16:19], v[144:147], v[128:131]\n"
      "ds_read_b128 v[140:143], v252 offset:192\n"
      "v_xor_b32 v232, s85, v232\n"
      "v_xor_b32 v233, s85, v233\n"
      "v_mfma_f32_16x16x32_bf16 v[132:135], v[20:23], v[144:147], v[132:135]\n"
      "v_xor_b32 v234, s85, v234\n"
      "v_xor_b32 v235, s85, v235\n"
      "s_waitcnt lgkmcnt(6)\n"
      "v_mfma_f32_16x16x32_bf16 v[128:131], v[24:27], v[148:151], v[128:131]\n"
      "ds_read_b128 v[144:147], v252 offset:384\n"
      "global_store_dwordx4 v242, v[232:235], s[62:63] offset:-2048 sc1\n"
      "v_mfma_f32_16x16x32_bf16 v[132:135], v[28:31], v[148:151], v[132:135]\n"
      "s_xor_b32 s70, s76, 0\n"
      "s_add_u32 s70, s8, s70\n"
      "s_addc_u32 s71, s9, 0\n"
      "s_add_u32 m0, s41, 4096\n"
      "s_waitcnt lgkmcnt(6)\n"
      "v_mfma_f32_16x16x32_bf16 v[128:131], v[32:35], v[152:155], v[128:131]\n"
      "s_add_u32 s48, s82, 32768\n"
      "ds_read_b128 v[148:151], v252 offset:448\n"
      "global_load_lds_dwordx4 v244, s[70:71] offset:0\n"
      "v_subrev_u32 v236, s41, v248\n"
      "v_mfma_f32_16x16x32_bf16 v[132:135], v[36:39], v[152:155], v[132:135]\n"
      "s_add_u32 s48, s48, 65536\n"
      "s_waitcnt vmcnt(4)\n"
      "s_barrier\n"
      "s_waitcnt lgkmcnt(6)\n"
      "v_mfma_f32_16x16x32_bf16 v[128:131], v[40:43], v[156:159], v[128:131]\n"
      "ds_read_b128 v[152:155], v252 offset:640\n"
      "ds_read_b128 a[192:195], v250 offset:34816\n"
      "v_add_u32 v236, s48, v236\n"
      "v_mfma_f32_16x16x32_bf16 v[132:135], v[44:47], v[156:159], v[132:135]\n"
      "ds_read_b128 a[196:199], v251 offset:34816\n"
      "ds_read_b128 a[200:203], v250 offset:38912\n"
      "v_xor_b32 v237, 64, v236\n"
      "s_waitcnt lgkmcnt(9)\n"
      "v_mfma_f32_16x16x32_bf16 v[128:131], v[48:51], v[160:163], v[128:131]\n"
      "ds_read_b128 v[156:159], v252 offset:704\n"
      "global_load_lds_dwordx4 v245, s[70:71] offset:1024\n"
      "v_mfma_f32_16x16x32_bf16 v[132:135], v[52:55], v[160:163], v[132:135]\n"
      "ds_read_b128 a[204:207], v251 offset:38912\n"
      "ds_read_b128 a[208:211], v250 offset:43008\n"
      "s_waitcnt lgkmcnt(11)\n"
      "v_mfma_f32_16x16x32_bf16 v[128:131], v[56:59], v[164:167], v[128:131]\n"
      "ds_read_b128 v[160:163], v252 offset:896\n"
      "ds_read_b128 a[212:215], v251 offset:43008\n"
      "v_mfma_f32_16x16x32_bf16 v[132:135], v[60:63], v[164:167], v[132:135]\n"
      "ds_read_b128 a[216:219], v250 offset:47104\n"
      "s_waitcnt lgkmcnt(13)\n"
      "v_mfma_f32_16x16x32_bf16 v[128:131], v[64:67], v[136:139], v[128:131]\n"
      "ds_read_b128 v[164:167], v252 offset:960\n"
      "global_load_lds_dwordx4 v246, s[70:71] offset:2048\n"
      "v_mfma_f32_16x16x32_bf16 v[132:135], v[68:71], v[136:139], v[132:135]\n"
      "ds_read_b128 a[220:223], v251 offset:47104\n"
      "s_waitcnt lgkmcnt(14)\n"
      "v_mfma_f32_16x16x32_bf16 v[128:131], v[72:75], v[140:143], v[128:131]\n"
      "v_mfma_f32_16x16x32_bf16 v[132:135], v[76:79], v[140:143], v[132:135]\n"
      "ds_read_b128 a[224:227], v250 offset:51200\n"
      "s_waitcnt lgkmcnt(14)\n"
      "v_mfma_f32_16x16x32_bf16 v[128:131], v[80:83], v[144:147], v[128:131]\n"
      "global_load_lds_dwordx4 v247, s[70:71] offset:3072\n"
      "v_mfma_f32_16x16x32_bf16 v[132:135], v[84:87], v[144:147], v[132:135]\n"
      "ds_read_b128 a[228:231], v251 offset:51200\n"
      "s_waitcnt lgkmcnt(14)\n"
      "v_mfma_f32_16x16x32_bf16 v[128:131], v[88:91], v[148:151], v[128:131]\n"
      "v_mfma_f32_16x16x32_bf16 v[132:135], v[92:95], v[148:151], v[132:135]\n"
      "s_add_u32 s70, s34, 4\n"
      "s_and_b32 s70, s70, 3\n"
      "s_lshl_b32 s70, s70, 15\n"
      "s_or_b32 s70, s70, s81\n"
      "s_add_u32 s70, s12, s70\n"
      "s_addc_u32 s71, s13, 0\n"
      "s_add_u32 m0, s82, 65536\n"
      "s_waitcnt lgkmcnt(13)\n"
      "v_mfma_f32_16x16x32_bf16 v[128:131], v[96:99], v[152:155], v[128:131]\n"
      "global_load_lds_dwordx4 v240, s[70:71] offset:0\n"
      "ds_read_b128 a[232:235], v250 offset:55296\n"
      "v_mfma_f32_16x16x32_bf16 v[132:135], v[100:103], v[152:155], v[132:135]\n"
      "ds_read_b128 a[236:239], v251 offset:55296\n"
      "s_waitcnt lgkmcnt(11)\n"
      "v_mfma_f32_16x16x32_bf16 v[128:131], v[104:107], v[156:159], v[128:131]\n"
      "v_mfma_f32_16x16x32_bf16 v[132:135], v[108:111], v[156:159], v[132:135]\n"
      "ds_read_b128 a[240:243], v250 offset:59392\n"
      "s_waitcnt lgkmcnt(9)\n"
      "v_mfma_f32_16x16x32_bf16 v[128:131], v[112:115], v[160:163], v[128:131]\n"
      "global_load_lds_dwordx4 v241, s[70:71] offset:1024\n"
      "v_mfma_f32_16x16x32_bf16 v[132:135], v[116:119], v[160:163], v[132:135]\n"
      "ds_read_b128 a[244:247], v251 offset:59392\n"
      "s_waitcnt lgkmcnt(7)\n"
      "v_mfma_f32_16x16x32_bf16 v[128:131], v[120:123], v[164:167], v[128:131]\n"
      "v_mfma_f32_16x16x32_bf16 v[132:135], v[124:127], v[164:167], v[132:135]\n"
      "ds_read_b128 a[248:251], v250 offset:63488\n"
      "v_mfma_f32_16x16x32_bf16 v[216:219], v[0:3], a[192:195], 0\n"
      "global_load_lds_dwordx4 v254, s[70:71] offset:2048\n"
      "v_mfma_f32_16x16x32_bf16 v[220:223], v[4:7], a[192:195], 0\n"
      "ds_read_b128 a[252:255], v251 offset:63488\n"
      "v_xor_b32 v250, 196608, v250\n"
      "v_xor_b32 v251, 196608, v251\n"
      "v_mfma_f32_16x16x32_bf16 v[216:219], v[8:11], a[196:199], v[216:219]\n"
      "s_mov_b32 s22, 0xf0f0f0f0\n"
      "s_mov_b32 s23, 0xf0f0f0f0\n"
      "v_cndmask_b32 v128, v128, v129, s[64:65]\n"
      "v_cndmask_b32 v130, v130, v131, s[64:65]\n"
      "v_mfma_f32_16x16x32_bf16 v[220:223], v[12:15], a[196:199], v[220:223]\n"
      "ds_read_b128 a[192:195], v250 offset:0\n"
      "v_cndmask_b32 v132, v132, v133, s[64:65]\n"
      "v_cndmask_b32 v134, v134, v135, s[64:65]\n"
      "v_mfma_f32_16x16x32_bf16 v[216:219], v[16:19], a[200:203], v[216:219]\n"
      "global_load_lds_dwordx4 v255, s[70:71] offset:3072\n"
      "v_cndmask_b32 v128, v128, v130, s[66:67]\n"
      "v_cndmask_b32 v132, v132, v134, s[66:67]\n"
      "v_mfma_f32_16x16x32_bf16 v[220:223], v[20:23], a[200:203], v[220:223]\n"
      "ds_read_b128 a[196:199], v250 offset:2048\n"
      "v_lshrrev_b32 v133, 4, v243\n"
      "v_cndmask_b32 v128, v128, v132, s[22:23]\n"
      "v_mfma_f32_16x16x32_bf16 v[216:219], v[24:27], a[204:207], v[216:219]\n"
      "v_and_b32 v134, 7, v243\n"
      "v_cvt_pk_bf16_f32 v128, v128, v128\n"
      "s_lshl_b32 s48, s40, 5\n"
      "v_mfma_f32_16x16x32_bf16 v[220:223], v[28:31], a[204:207], v[220:223]\n"
      "ds_read_b128 a[200:203], v251 offset:0\n"
      "v_and_b32 v128, 0xffff0000, v128\n"
      "v_lshl_or_b32 v133, v133, 3, v134\n"
      "v_mfma_f32_16x16x32_bf16 v[216:219], v[32:35], a[208:211], v[216:219]\n"
      "s_add_u32 s70, s34, 4\n"
      "s_and_b32 s70, s70, 3\n"
      "s_lshl_b32 s70, s70, 15\n"
      "s_or_b32 s70, s70, s81\n"
      "s_xor_b32 s70, s70, 128\n"
      "s_add_u32 s70, s12, s70\n"
      "s_addc_u32 s71, s13, 0\n"
      "s_add_u32 m0, s82, 81920\n"
      "v_mfma_f32_16x16x32_bf16 v[220:223], v[36:39], a[208:211], v[220:223]\n"
      "global_load_lds_dwordx4 v240, s[70:71] offset:0\n"
      "ds_read_b128 a[204:207], v251 offset:2048\n"
      "v_add_u32 v133, s48, v133\n"
      "v_mfma_f32_16x16x32_bf16 v[216:219], v[40:43], a[212:215], v[216:219]\n"
      "v_ashrrev_i32 v129, 31, v128\n"
      "v_mul_f32 v130, 0xbfb8aa3b, v128\n"
      "v_or_b32 v129, 0x80000000, v129\n"
      "v_mfma_f32_16x16x32_bf16 v[220:223], v[44:47], a[212:215], v[220:223]\n"
      "ds_read_b128 a[208:211], v250 offset:4096\n"
      "v_exp_f32 v130, v130\n"
      "v_xor_b32 v129, v129, v128\n"
      "v_mfma_f32_16x16x32_bf16 v[216:219], v[48:51], a[216:219], v[216:219]\n"
      "v_sub_u32 v132, 0xfff, v133\n"
      "v_add_f32 v130, 1.0, v130\n"
      "s_mov_b32 s49, 0xffff0000\n"
      "v_mfma_f32_16x16x32_bf16 v[220:223], v[52:55], a[216:219], v[220:223]\n"
      "global_load_lds_dwordx4 v241, s[70:71] offset:1024\n"
      "ds_read_b128 a[212:215], v250 offset:6144\n"
      "v_and_or_b32 v129, v129, s49, v132\n"
      "s_waitcnt lgkmcnt(14)\n"
      "v_mfma_f32_16x16x32_bf16 v[216:219], v[56:59], a[220:223], v[216:219]\n"
      "v_rcp_f32 v130, v130\n"
      "s_mov_b32 s64, 0x00ff00ff\n"
      "s_mov_b32 s65, 0x00ff00ff\n"
      "v_mfma_f32_16x16x32_bf16 v[220:223], v[60:63], a[220:223], v[220:223]\n"
      "ds_read_b128 a[216:219], v251 offset:4096\n"
      "v_cndmask_b32 v129, v129, 0, s[64:65]\n"
      "v_cvt_pk_bf16_f32 v130, v130, v130\n"
      "s_waitcnt lgkmcnt(14)\n"
      "v_mfma_f32_16x16x32_bf16 v[216:219], v[64:67], a[224:227], v[216:219]\n"
      "s_lshr_b32 s48, s34, 1\n"
      "v_add_u32 v238, s42, v242\n"
      "v_mov_b32 v224, s37\n"
      "v_mov_b32 v225, s37\n"
      "v_mov_b32 v226, s37\n"
      "v_mov_b32 v227, s37\n"
      "s_waitcnt vmcnt(10)\n"
      "s_mov_b64 exec, 0xff\n"
      "global_store_dwordx4 v238, v[224:227], s[58:59] offset:0 sc1\n"
      "s_mov_b64 exec, -1\n"
      "v_mfma_f32_16x16x32_bf16 v[220:223], v[68:71], a[224:227], v[220:223]\n"
      "s_lshl2_add_u32 s48, s48, s35\n"
      "ds_read_b128 a[220:223], v251 offset:6144\n"
      "v_max_u32_dpp v129, v129, v129 quad_perm:[1,0,3,2] row_mask:0xf bank_mask:0xf\n"
      "v_and_b32 v130, 0xffff0000, v130\n"
      "s_waitcnt lgkmcnt(14)\n"
      "v_mfma_f32_16x16x32_bf16 v[216:219], v[72:75], a[228:231], v[216:219]\n"
      "s_lshl_b32 s49, s48, 15\n"
      "global_load_lds_dwordx4 v254, s[70:71] offset:2048\n"
      "v_max_u32_dpp v129, v129, v129 quad_perm:[2,3,0,1] row_mask:0xf bank_mask:0xf\n"
      "s_add_u32 s49, s49, 786432\n"
      "v_mfma_f32_16x16x32_bf16 v[220:223], v[76:79], a[228:231], v[220:223]\n"
      "ds_read_b128 a[224:227], v250 offset:8192\n"
      "v_lshl_add_u32 v133, v133, 4, s49\n"
      "v_max_u32_dpp v129, v129, v129 row_half_mirror row_mask:0xf bank_mask:0xf\n"
      "s_waitcnt lgkmcnt(14)\n"
      "v_mfma_f32_16x16x32_bf16 v[216:219], v[80:83], a[232:235], v[216:219]\n"
      "s_lshl_b32 s48, s48, 6\n"
      "s_add_u32 s48, s48, s40\n"
      "v_max_u32_dpp v129, v129, v129 row_mirror row_mask:0xf bank_mask:0xf\n"
      "s_lshl_b32 s48, s48, 6\n"
      "v_mfma_f32_16x16x32_bf16 v[220:223], v[84:87], a[232:235], v[220:223]\n"
      "s_add_u32 s48, s48, 4096\n"
      "ds_read_b128 a[228:231], v250 offset:10240\n"
      "v_mov_b32 v131, v129\n"
      "v_lshl_add_u32 v132, v134, 4, s48\n"
      "s_waitcnt lgkmcnt(14)\n"
      "v_mfma_f32_16x16x32_bf16 v[216:219], v[88:91], a[236:239], v[216:219]\n"
      "global_load_lds_dwordx4 v255, s[70:71] offset:3072\n"
      "v_mov_b32 v137, s37\n"
      "v_permlane16_swap_b32 v129, v131\n"
      "v_mfma_f32_16x16x32_bf16 v[220:223], v[92:95], a[236:239], v[220:223]\n"
      "ds_read_b128 a[232:235], v251 offset:8192\n"
      "v_mov_b32 v138, s37\n"
      "v_mov_b32 v139, s37\n"
      "s_waitcnt lgkmcnt(14)\n"
      "v_mfma_f32_16x16x32_bf16 v[216:219], v[96:99], a[240:243], v[216:219]\n"
      "v_max_u32 v129, v129, v131\n"
      "v_cndmask_b32 v133, v132, v133, s[64:65]\n"
      "v_mov_b32 v131, v129\n"
      "v_mfma_f32_16x16x32_bf16 v[220:223], v[100:103], a[240:243], v[220:223]\n"
      "ds_read_b128 a[236:239], v251 offset:10240\n"
      "v_permlane32_swap_b32 v129, v131\n"
      "v_max_u32 v129, v129, v131\n"
      "s_waitcnt lgkmcnt(14)\n"
      "v_mfma_f32_16x16x32_bf16 v[216:219], v[104:107], a[244:247], v[216:219]\n"
      "s_xor_b32 s70, s77, 0\n"
      "s_add_u32 s70, s8, s70\n"
      "s_addc_u32 s71, s9, 0\n"
      "s_add_u32 m0, s41, 8192\n"
      "v_mfma_f32_16x16x32_bf16 v[220:223], v[108:111], a[244:247], v[220:223]\n"
      "global_load_lds_dwordx4 v244, s[70:71] offset:0\n"
      "ds_read_b128 a[240:243], v250 offset:12288\n"
      "v_cndmask_b32 v136, v129, v130, s[64:65]\n"
      "s_waitcnt lgkmcnt(14)\n"
      "v_mfma_f32_16x16x32_bf16 v[216:219], v[112:115], a[248:251], v[216:219]\n"
      "s_mov_b32 s64, 0x00ff0fff\n"
      "s_mov_b64 exec, s[64:65]\n"
      "global_store_dwordx4 v133, v[136:139], s[58:59] sc1\n"
      "s_mov_b64 exec, -1\n"
      "v_mfma_f32_16x16x32_bf16 v[220:223], v[116:119], a[248:251], v[220:223]\n"
      "ds_read_b128 a[244:247], v250 offset:14336\n"
      "s_waitcnt lgkmcnt(14)\n"
      "v_mfma_f32_16x16x32_bf16 v[216:219], v[120:123], a[252:255], v[216:219]\n"
      "v_mfma_f32_16x16x32_bf16 v[220:223], v[124:127], a[252:255], v[220:223]\n"
      "ds_read_b128 a[248:251], v251 offset:12288\n"
      "s_waitcnt lgkmcnt(14)\n"
      "v_mfma_f32_16x16x32_bf16 v[192:195], v[0:3], a[192:195], 0\n"
      "global_load_lds_dwordx4 v245, s[70:71] offset:1024\n"
      "v_mfma_f32_16x16x32_bf16 v[196:199], v[4:7], a[192:195], 0\n"
      "ds_read_b128 a[252:255], v251 offset:14336\n"
      "s_waitcnt vmcnt(12)\n"
      "ds_read_b128 a[0:3], v248 offset:4096\n"
      "s_waitcnt lgkmcnt(15)\n"
      "v_mfma_f32_16x16x32_bf16 v[200:203], v[0:3], a[196:199], 0\n"
      "ds_read_b128 a[4:7], v248 offset:6144\n"
      "ds_read_b128 a[32:35], v249 offset:4096\n"
      "v_cvt_pk_bf16_f32 v224, v216, v217\n"
      "v_mfma_f32_16x16x32_bf16 v[204:207], v[4:7], a[196:199], 0\n"
      "ds_read_b128 a[192:195], v250 offset:16384\n"
      "ds_read_b128 a[36:39], v249 offset:6144\n"
      "v_cvt_pk_bf16_f32 v225, v218, v219\n"
      "s_waitcnt lgkmcnt(15)\n"
      "v_mfma_f32_16x16x32_bf16 v[192:195], v[8:11], a[200:203], v[192:195]\n"
      "global_load_lds_dwordx4 v246, s[70:71] offset:2048\n"
      "v_cvt_pk_bf16_f32 v226, v220, v221\n"
      "v_cvt_pk_bf16_f32 v227, v222, v223\n"
      "v_mfma_f32_16x16x32_bf16 v[196:199], v[12:15], a[200:203], v[196:199]\n"
      "ds_read_b128 a[196:199], v250 offset:18432\n"
      "v_xor_b32 v224, s85, v224\n"
      "v_xor_b32 v225, s85, v225\n"
      "v_mfma_f32_16x16x32_bf16 v[200:203], v[8:11], a[204:207], v[200:203]\n"
      "v_xor_b32 v226, s85, v226\n"
      "v_xor_b32 v227, s85, v227\n"
      "s_lshl2_add_u32 s54, s34, s35\n"
      "v_mfma_f32_16x16x32_bf16 v[204:207], v[12:15], a[204:207], v[204:207]\n"
      "s_lshr_b32 s56, s39, 1\n"
      "ds_read_b128 a[200:203], v251 offset:16384\n"
      "s_lshl1_add_u32 s56, s33, s56\n"
      "s_lshl_b32 s56, s56, 15\n"
      "v_mfma_f32_16x16x32_bf16 v[192:195], v[16:19], a[208:211], v[192:195]\n"
      "s_mul_i32 s55, s54, 266240\n"
      "global_load_lds_dwordx4 v247, s[70:71] offset:3072\n"
      "s_add_u32 s55, s55, 4096\n"
      "s_add_u32 s14, s18, s55\n"
      "s_addc_u32 s15, s19, 0\n"
      "v_mfma_f32_16x16x32_bf16 v[196:199], v[20:23], a[208:211], v[196:199]\n"
      "s_add_u32 s10, s14, s56\n"
      "s_addc_u32 s11, s15, 0\n"
      "ds_read_b128 a[204:207], v251 offset:18432\n"
      "v_mfma_f32_16x16x32_bf16 v[200:203], v[16:19], a[212:215], v[200:203]\n"
      "s_xor_b32 s56, s56, 0x8000\n"
      "s_add_u32 s14, s14, s56\n"
      "s_addc_u32 s15, s15, 0\n"
      "s_and_b32 s55, s39, 1\n"
      "v_mfma_f32_16x16x32_bf16 v[204:207], v[20:23], a[212:215], v[204:207]\n"
      "s_lshl_b32 s56, s55, 14\n"
      "s_xor_b32 s70, s78, 0\n"
      "s_add_u32 s70, s8, s70\n"
      "s_addc_u32 s71, s9, 0\n"
      "s_add_u32 m0, s41, 12288\n"
      "s_waitcnt lgkmcnt(15)\n"
      "v_mfma_f32_16x16x32_bf16 v[192:195], v[24:27], a[216:219], v[192:195]\n"
      "global_load_lds_dwordx4 v244, s[70:71] offset:0\n"
      "ds_read_b128 a[208:211], v250 offset:20480\n"
      "v_add_u32 v240, s56, v242\n"
      "v_mfma_f32_16x16x32_bf16 v[196:199], v[28:31], a[216:219], v[196:199]\n"
      "ds_read_b128 a[212:215], v250 offset:22528\n"
      "v_xor_b32 v241, 0x4000, v240\n"
      "s_add_u32 s72, s10, 0x2000\n"
      "s_addc_u32 s73, s11, 0\n"
      "v_mfma_f32_16x16x32_bf16 v[200:203], v[24:27], a[220:223], v[200:203]\n"
      "s_add_u32 s74, s14, 0x2000\n"
      "s_addc_u32 s75, s15, 0\n"
      "s_lshl2_add_u32 s55, s54, s33\n"
      "s_lshl_b32 s55, s55, 12\n"
      "v_mfma_f32_16x16x32_bf16 v[204:207], v[28:31], a[220:223], v[204:207]\n"
      "s_add_u32 s55, s55, 1048576\n"
      "ds_read_b128 a[216:219], v251 offset:20480\n"
      "v_and_b32 v253, 7, v243\n"
      "v_lshl_add_u32 v253, v253, 9, s55\n"
      "v_mfma_f32_16x16x32_bf16 v[192:195], v[32:35], a[224:227], v[192:195]\n"
      "global_load_lds_dwordx4 v245, s[70:71] offset:1024\n"
      "v_mfma_f32_16x16x32_bf16 v[196:199], v[36:39], a[224:227], v[196:199]\n"
      "ds_read_b128 a[220:223], v251 offset:22528\n"
      "s_waitcnt vmcnt(7)\n"
      "s_barrier\n"
      "s_waitcnt lgkmcnt(15)\n"
      "v_mfma_f32_16x16x32_bf16 v[200:203], v[32:35], a[228:231], v[200:203]\n"
      "v_mfma_f32_16x16x32_bf16 v[204:207], v[36:39], a[228:231], v[204:207]\n"
      "ds_read_b128 a[224:227], v250 offset:24576\n"
      "v_mfma_f32_16x16x32_bf16 v[192:195], v[40:43], a[232:235], v[192:195]\n"
      "global_load_lds_dwordx4 v246, s[70:71] offset:2048\n"
      "v_mfma_f32_16x16x32_bf16 v[196:199], v[44:47], a[232:235], v[196:199]\n"
      "ds_read_b128 a[228:231], v250 offset:26624\n"
      "v_mfma_f32_16x16x32_bf16 v[200:203], v[40:43], a[236:239], v[200:203]\n"
      "v_mfma_f32_16x16x32_bf16 v[204:207], v[44:47], a[236:239], v[204:207]\n"
      "ds_read_b128 a[232:235], v251 offset:24576\n"
      "v_mfma_f32_16x16x32_bf16 v[192:195], v[48:51], a[240:243], v[192:195]\n"
      "global_load_lds_dwordx4 v247, s[70:71] offset:3072\n"
      "v_mfma_f32_16x16x32_bf16 v[196:199], v[52:55], a[240:243], v[196:199]\n"
      "ds_read_b128 a[236:239], v251 offset:26624\n"
      "s_waitcnt lgkmcnt(15)\n"
      "v_mfma_f32_16x16x32_bf16 v[200:203], v[48:51], a[244:247], v[200:203]\n"
      "v_mfma_f32_16x16x32_bf16 v[204:207], v[52:55], a[244:247], v[204:207]\n"
      "s_xor_b32 s70, s79, 0\n"
      "s_add_u32 s70, s8, s70\n"
      "s_addc_u32 s71, s9, 0\n"
      "s_add_u32 m0, s82, 98304\n"
      "v_mfma_f32_16x16x32_bf16 v[192:195], v[56:59], a[248:251], v[192:195]\n"
      "global_load_lds_dwordx4 v244, s[70:71] offset:0\n"
      "ds_read_b128 a[240:243], v250 offset:28672\n"
      "v_mfma_f32_16x16x32_bf16 v[196:199], v[60:63], a[248:251], v[196:199]\n"
      "ds_read_b128 a[244:247], v250 offset:30720\n"
      "v_mfma_f32_16x16x32_bf16 v[200:203], v[56:59], a[252:255], v[200:203]\n"
      "v_mfma_f32_16x16x32_bf16 v[204:207], v[60:63], a[252:255], v[204:207]\n"
      "ds_read_b128 a[248:251], v251 offset:28672\n"
      "s_waitcnt lgkmcnt(15)\n"
      "v_mfma_f32_16x16x32_bf16 v[192:195], v[64:67], a[192:195], v[192:195]\n"
      "global_load_lds_dwordx4 v245, s[70:71] offset:1024\n"
      "v_mfma_f32_16x16x32_bf16 v[196:199], v[68:71], a[192:195], v[196:199]\n"
      "ds_read_b128 a[252:255], v251 offset:30720\n"
      "v_xor_b32 v250, 196608, v250\n"
      "v_xor_b32 v251, 196608, v251\n"
      "s_waitcnt lgkmcnt(14)\n"
      "v_mfma_f32_16x16x32_bf16 v[200:203], v[64:67], a[196:199], v[200:203]\n"
      "v_mfma_f32_16x16x32_bf16 v[204:207], v[68:71], a[196:199], v[204:207]\n"
      "ds_read_b128 a[192:195], v250 offset:0\n"
      "s_waitcnt lgkmcnt(14)\n"
      "v_mfma_f32_16x16x32_bf16 v[192:195], v[72:75], a[200:203], v[192:195]\n"
      "global_load_lds_dwordx4 v246, s[70:71] offset:2048\n"
      "v_mfma_f32_16x16x32_bf16 v[196:199], v[76:79], a[200:203], v[196:199]\n"
      "ds_read_b128 a[196:199], v250 offset:2048\n"
      "s_waitcnt lgkmcnt(14)\n"
      "v_mfma_f32_16x16x32_bf16 v[200:203], v[72:75], a[204:207], v[200:203]\n"
      "v_mfma_f32_16x16x32_bf16 v[204:207], v[76:79], a[204:207], v[204:207]\n"
      "ds_read_b128 a[200:203], v251 offset:0\n"
      "s_waitcnt lgkmcnt(14)\n"
      "v_mfma_f32_16x16x32_bf16 v[192:195], v[80:83], a[208:211], v[192:195]\n"
      "global_load_lds_dwordx4 v247, s[70:71] offset:3072\n"
      "v_mfma_f32_16x16x32_bf16 v[196:199], v[84:87], a[208:211], v[196:199]\n"
      "ds_read_b128 a[204:207], v251 offset:2048\n"
      "s_waitcnt lgkmcnt(14)\n"
      "v_mfma_f32_16x16x32_bf16 v[200:203], v[80:83], a[212:215], v[200:203]\n"
      "v_mfma_f32_16x16x32_bf16 v[204:207], v[84:87], a[212:215], v[204:207]\n"
      "s_xor_b32 s70, s76, 128\n"
      "s_add_u32 s70, s8, s70\n"
      "s_addc_u32 s71, s9, 0\n"
      "s_add_u32 m0, s82, 114688\n"
      "s_waitcnt lgkmcnt(13)\n"
      "v_mfma_f32_16x16x32_bf16 v[192:195], v[88:91], a[216:219], v[192:195]\n"
      "global_load_lds_dwordx4 v244, s[70:71] offset:0\n"
      "ds_read_b128 a[208:211], v250 offset:4096\n"
      "v_mfma_f32_16x16x32_bf16 v[196:199], v[92:95], a[216:219], v[196:199]\n"
      "ds_read_b128 a[212:215], v250 offset:6144\n"
      "s_waitcnt lgkmcnt(14)\n"
      "v_mfma_f32_16x16x32_bf16 v[200:203], v[88:91], a[220:223], v[200:203]\n"
      "v_mfma_f32_16x16x32_bf16 v[204:207], v[92:95], a[220:223], v[204:207]\n"
      "ds_read_b128 a[216:219], v251 offset:4096\n"
      "s_waitcnt lgkmcnt(14)\n"
      "v_mfma_f32_16x16x32_bf16 v[192:195], v[96:99], a[224:227], v[192:195]\n"
      "global_load_lds_dwordx4 v245, s[70:71] offset:1024\n"
      "v_mfma_f32_16x16x32_bf16 v[196:199], v[100:103], a[224:227], v[196:199]\n"
      "ds_read_b128 a[220:223], v251 offset:6144\n"
      "s_waitcnt lgkmcnt(14)\n"
      "v_mfma_f32_16x16x32_bf16 v[200:203], v[96:99], a[228:231], v[200:203]\n"
      "v_mfma_f32_16x16x32_bf16 v[204:207], v[100:103], a[228:231], v[204:207]\n"
      "ds_read_b128 a[224:227], v250 offset:8192\n"
      "s_waitcnt lgkmcnt(14)\n"
      "v_mfma_f32_16x16x32_bf16 v[192:195], v[104:107], a[232:235], v[192:195]\n"
      "global_load_lds_dwordx4 v246, s[70:71] offset:2048\n"
      "v_mfma_f32_16x16x32_bf16 v[196:199], v[108:111], a[232:235], v[196:199]\n"
      "ds_read_b128 a[228:231], v250 offset:10240\n"
      "s_waitcnt vmcnt(11)\n"
      "ds_read_b128 a[8:11], v248 offset:8192\n"
      "s_waitcnt lgkmcnt(15)\n"
      "v_mfma_f32_16x16x32_bf16 v[200:203], v[104:107], a[236:239], v[200:203]\n"
      "ds_read_b128 a[12:15], v248 offset:10240\n"
      "ds_read_b128 a[40:43], v249 offset:8192\n"
      "v_mfma_f32_16x16x32_bf16 v[204:207], v[108:111], a[236:239], v[204:207]\n"
      "ds_read_b128 a[232:235], v251 offset:8192\n"
      "ds_read_b128 a[44:47], v249 offset:10240\n"
      "s_waitcnt lgkmcnt(15)\n"
      "v_mfma_f32_16x16x32_bf16 v[192:195], v[112:115], a[240:243], v[192:195]\n"
      "global_load_lds_dwordx4 v247, s[70:71] offset:3072\n"
      "v_mfma_f32_16x16x32_bf16 v[196:199], v[116:119], a[240:243], v[196:199]\n"
      "ds_read_b128 a[236:239], v251 offset:10240\n"
      "s_barrier\n"
      "v_mfma_f32_16x16x32_bf16 v[200:203], v[112:115], a[244:247], v[200:203]\n"
      "v_mfma_f32_16x16x32_bf16 v[204:207], v[116:119], a[244:247], v[204:207]\n"
      "s_xor_b32 s70, s77, 128\n"
      "s_add_u32 s70, s8, s70\n"
      "s_addc_u32 s71, s9, 0\n"
      "s_add_u32 m0, s82, 131072\n"
      "v_mfma_f32_16x16x32_bf16 v[192:195], v[120:123], a[248:251], v[192:195]\n"
      "global_load_lds_dwordx4 v244, s[70:71] offset:0\n"
      "ds_read_b128 a[240:243], v250 offset:12288\n"
      "v_mfma_f32_16x16x32_bf16 v[196:199], v[124:127], a[248:251], v[196:199]\n"
      "ds_read_b128 a[244:247], v250 offset:14336\n"
      "v_mfma_f32_16x16x32_bf16 v[200:203], v[120:123], a[252:255], v[200:203]\n"
      "v_mfma_f32_16x16x32_bf16 v[204:207], v[124:127], a[252:255], v[204:207]\n"
      "ds_read_b128 a[248:251], v251 offset:12288\n"
      "s_waitcnt lgkmcnt(15)\n"
      "v_mfma_f32_16x16x32_bf16 v[208:211], v[0:3], a[192:195], 0\n"
      "global_load_lds_dwordx4 v245, s[70:71] offset:1024\n"
      "v_mfma_f32_16x16x32_bf16 v[212:215], v[4:7], a[192:195], 0\n"
      "ds_read_b128 a[252:255], v251 offset:14336\n"
      "v_mfma_f32_16x16x32_bf16 v[216:219], v[0:3], a[196:199], 0\n"
      "v_cvt_pk_bf16_f32 v228, v192, v193\n"
      "v_cvt_pk_bf16_f32 v229, v194, v195\n"
      "v_cvt_pk_bf16_f32 v230, v196, v197\n"
      "v_mfma_f32_16x16x32_bf16 v[220:223], v[4:7], a[196:199], 0\n"
      "ds_read_b128 a[192:195], v250 offset:16384\n"
      "v_cvt_pk_bf16_f32 v231, v198, v199\n"
      "v_xor_b32 v228, s85, v228\n"
      "v_mfma_f32_16x16x32_bf16 v[208:211], v[8:11], a[200:203], v[208:211]\n"
      "global_load_lds_dwordx4 v246, s[70:71] offset:2048\n"
      "v_xor_b32 v229, s85, v229\n"
      "v_xor_b32 v230, s85, v230\n"
      "v_mfma_f32_16x16x32_bf16 v[212:215], v[12:15], a[200:203], v[212:215]\n"
      "ds_read_b128 a[196:199], v250 offset:18432\n"
      "s_waitcnt vmcnt(11)\n"
      "ds_read_b128 a[16:19], v248 offset:12288\n"
      "v_mfma_f32_16x16x32_bf16 v[216:219], v[8:11], a[204:207], v[216:219]\n"
      "ds_read_b128 a[20:23], v248 offset:14336\n"
      "ds_read_b128 a[48:51], v249 offset:12288\n"
      "v_xor_b32 v231, s85, v231\n"
      "v_mfma_f32_16x16x32_bf16 v[220:223], v[12:15], a[204:207], v[220:223]\n"
      "ds_read_b128 a[200:203], v251 offset:16384\n"
      "ds_read_b128 a[52:55], v249 offset:14336\n"
      "v_cvt_pk_bf16_f32 v232, v200, v201\n"
      "s_waitcnt lgkmcnt(15)\n"
      "v_mfma_f32_16x16x32_bf16 v[208:211], v[16:19], a[208:211], v[208:211]\n"
      "global_load_lds_dwordx4 v247, s[70:71] offset:3072\n"
      "v_cvt_pk_bf16_f32 v233, v202, v203\n"
      "v_cvt_pk_bf16_f32 v234, v204, v205\n"
      "v_mfma_f32_16x16x32_bf16 v[212:215], v[20:23], a[208:211], v[212:215]\n"
      "ds_read_b128 a[204:207], v251 offset:18432\n"
      "v_cvt_pk_bf16_f32 v235, v206, v207\n"
      "v_xor_b32 v232, s85, v232\n"
      "v_mfma_f32_16x16x32_bf16 v[216:219], v[16:19], a[212:215], v[216:219]\n"
      "v_xor_b32 v233, s85, v233\n"
      "v_xor_b32 v234, s85, v234\n"
      "v_xor_b32 v235, s85, v235\n"
      "v_mfma_f32_16x16x32_bf16 v[220:223], v[20:23], a[212:215], v[220:223]\n"
      "s_xor_b32 s70, s78, 128\n"
      "s_add_u32 s70, s8, s70\n"
      "s_addc_u32 s71, s9, 0\n"
      "s_add_u32 m0, s82, 147456\n"
      "v_mfma_f32_16x16x32_bf16 v[208:211], v[24:27], a[216:219], v[208:211]\n"
      "global_load_lds_dwordx4 v244, s[70:71] offset:0\n"
      "ds_read_b128 a[208:211], v250 offset:20480\n"
      "v_mfma_f32_16x16x32_bf16 v[212:215], v[28:31], a[216:219], v[212:215]\n"
      "ds_read_b128 a[212:215], v250 offset:22528\n"
      "v_mfma_f32_16x16x32_bf16 v[216:219], v[24:27], a[220:223], v[216:219]\n"
      "v_mfma_f32_16x16x32_bf16 v[220:223], v[28:31], a[220:223], v[220:223]\n"
      "ds_read_b128 a[216:219], v251 offset:20480\n"
      "v_mfma_f32_16x16x32_bf16 v[208:211], v[32:35], a[224:227], v[208:211]\n"
      "global_load_lds_dwordx4 v245, s[70:71] offset:1024\n"
      "v_mfma_f32_16x16x32_bf16 v[212:215], v[36:39], a[224:227], v[212:215]\n"
      "ds_read_b128 a[220:223], v251 offset:22528\n"
      "v_mfma_f32_16x16x32_bf16 v[216:219], v[32:35], a[228:231], v[216:219]\n"
      "v_mfma_f32_16x16x32_bf16 v[220:223], v[36:39], a[228:231], v[220:223]\n"
      "ds_read_b128 a[224:227], v250 offset:24576\n"
      "s_waitcnt lgkmcnt(15)\n"
      "v_mfma_f32_16x16x32_bf16 v[208:211], v[40:43], a[232:235], v[208:211]\n"
      "global_load_lds_dwordx4 v246, s[70:71] offset:2048\n"
      "v_mfma_f32_16x16x32_bf16 v[212:215], v[44:47], a[232:235], v[212:215]\n"
      "ds_read_b128 a[228:231], v250 offset:26624\n"
      "s_waitcnt vmcnt(11)\n"
      "ds_read_b128 a[24:27], v236 offset:0\n"
      "v_mfma_f32_16x16x32_bf16 v[216:219], v[40:43], a[236:239], v[216:219]\n"
      "ds_read_b128 a[28:31], v236 offset:2048\n"
      "ds_read_b128 a[56:59], v237 offset:0\n"
      "v_mfma_f32_16x16x32_bf16 v[220:223], v[44:47], a[236:239], v[220:223]\n"
      "ds_read_b128 a[232:235], v251 offset:24576\n"
      "ds_read_b128 a[60:63], v237 offset:2048\n"
      "v_mfma_f32_16x16x32_bf16 v[208:211], v[48:51], a[240:243], v[208:211]\n"
      "global_load_lds_dwordx4 v247, s[70:71] offset:3072\n"
      "v_mfma_f32_16x16x32_bf16 v[212:215], v[52:55], a[240:243], v[212:215]\n"
      "ds_read_b128 a[236:239], v251 offset:26624\n"
      "v_mfma_f32_16x16x32_bf16 v[216:219], v[48:51], a[244:247], v[216:219]\n"
      "v_mfma_f32_16x16x32_bf16 v[220:223], v[52:55], a[244:247], v[220:223]\n"
      "global_load_dword v252, v253, s[58:59] offset:0 sc1\n"
      "s_xor_b32 s70, s79, 128\n"
      "s_add_u32 s70, s8, s70\n"
      "s_addc_u32 s71, s9, 0\n"
      "s_add_u32 m0, s41, 0\n"
      "s_waitcnt lgkmcnt(15)\n"
      "v_mfma_f32_16x16x32_bf16 v[208:211], v[56:59], a[248:251], v[208:211]\n"
      "global_load_lds_dwordx4 v244, s[70:71] offset:0\n"
      "ds_read_b128 a[240:243], v250 offset:28672\n"
      "v_mfma_f32_16x16x32_bf16 v[212:215], v[60:63], a[248:251], v[212:215]\n"
      "ds_read_b128 a[244:247], v250 offset:30720\n"
      "v_mfma_f32_16x16x32_bf16 v[216:219], v[56:59], a[252:255], v[216:219]\n"
      "v_mfma_f32_16x16x32_bf16 v[220:223], v[60:63], a[252:255], v[220:223]\n"
      "ds_read_b128 a[248:251], v251 offset:28672\n"
      "v_mfma_f32_16x16x32_bf16 v[208:211], v[64:67], a[192:195], v[208:211]\n"
      "global_load_lds_dwordx4 v245, s[70:71] offset:1024\n"
      "v_mfma_f32_16x16x32_bf16 v[212:215], v[68:71], a[192:195], v[212:215]\n"
      "ds_read_b128 a[252:255], v251 offset:30720\n"
      "v_mfma_f32_16x16x32_bf16 v[216:219], v[64:67], a[196:199], v[216:219]\n"
      "v_mfma_f32_16x16x32_bf16 v[220:223], v[68:71], a[196:199], v[220:223]\n"
      "s_waitcnt lgkmcnt(15)\n"
      "v_mfma_f32_16x16x32_bf16 v[208:211], v[72:75], a[200:203], v[208:211]\n"
      "global_load_lds_dwordx4 v246, s[70:71] offset:2048\n"
      "v_mfma_f32_16x16x32_bf16 v[212:215], v[76:79], a[200:203], v[212:215]\n"
      "s_waitcnt vmcnt(12)\n"
      "ds_read_b128 a[64:67], v236 offset:16384\n"
      "v_mfma_f32_16x16x32_bf16 v[216:219], v[72:75], a[204:207], v[216:219]\n"
      "ds_read_b128 a[68:71], v236 offset:18432\n"
      "ds_read_b128 a[96:99], v237 offset:16384\n"
      "v_mfma_f32_16x16x32_bf16 v[220:223], v[76:79], a[204:207], v[220:223]\n"
      "ds_read_b128 a[100:103], v237 offset:18432\n"
      "v_mfma_f32_16x16x32_bf16 v[208:211], v[80:83], a[208:211], v[208:211]\n"
      "global_load_lds_dwordx4 v247, s[70:71] offset:3072\n"
      "v_mfma_f32_16x16x32_bf16 v[212:215], v[84:87], a[208:211], v[212:215]\n"
      "s_waitcnt lgkmcnt(15)\n"
      "v_mfma_f32_16x16x32_bf16 v[216:219], v[80:83], a[212:215], v[216:219]\n"
      "v_mfma_f32_16x16x32_bf16 v[220:223], v[84:87], a[212:215], v[220:223]\n"
      "s_waitcnt vmcnt(4)\n"
      "v_cmp_ne_u32 vcc, s37, v252\n"
      "s_nop 1\n"
      "s_cbranch_vccnz .Lp_s2_%=\n"
      ".Lp_o3_%=:\n"
      "v_mfma_f32_16x16x32_bf16 v[208:211], v[88:91], a[216:219], v[208:211]\n"
      "global_load_dwordx4 v[128:131], v240, s[10:11] offset:-4096 sc1\n"
      "v_mfma_f32_16x16x32_bf16 v[212:215], v[92:95], a[216:219], v[212:215]\n"
      "v_mfma_f32_16x16x32_bf16 v[216:219], v[88:91], a[220:223], v[216:219]\n"
      "v_mfma_f32_16x16x32_bf16 v[220:223], v[92:95], a[220:223], v[220:223]\n"
      "v_mfma_f32_16x16x32_bf16 v[208:211], v[96:99], a[224:227], v[208:211]\n"
      "global_load_dwordx4 v[132:135], v240, s[10:11] offset:-3072 sc1\n"
      "v_mfma_f32_16x16x32_bf16 v[212:215], v[100:103], a[224:227], v[212:215]\n"
      "s_waitcnt lgkmcnt(14)\n"
      "v_mfma_f32_16x16x32_bf16 v[216:219], v[96:99], a[228:231], v[216:219]\n"
      "v_mfma_f32_16x16x32_bf16 v[220:223], v[100:103], a[228:231], v[220:223]\n"
      "s_waitcnt lgkmcnt(10)\n"
      "v_mfma_f32_16x16x32_bf16 v[208:211], v[104:107], a[232:235], v[208:211]\n"
      "global_load_dwordx4 v[136:139], v240, s[10:11] offset:-2048 sc1\n"
      "v_mfma_f32_16x16x32_bf16 v[212:215], v[108:111], a[232:235], v[212:215]\n"
      "ds_read_b128 a[72:75], v236 offset:32768\n"
      "ds_read_b128 a[76:79], v236 offset:34816\n"
      "s_waitcnt lgkmcnt(10)\n"
      "v_mfma_f32_16x16x32_bf16 v[216:219], v[104:107], a[236:239], v[216:219]\n"
      "ds_read_b128 a[104:107], v237 offset:32768\n"
      "ds_read_b128 a[108:111], v237 offset:34816\n"
      "v_mfma_f32_16x16x32_bf16 v[220:223], v[108:111], a[236:239], v[220:223]\n"
      "s_waitcnt lgkmcnt(11)\n"
      "v_mfma_f32_16x16x32_bf16 v[208:211], v[112:115], a[240:243], v[208:211]\n"
      "global_store_dwordx4 v242, v[224:227], s[62:63] offset:-1024 sc1\n"
      "global_store_dwordx4 v242, v[228:231], s[62:63] offset:0 sc1\n"
      "v_mfma_f32_16x16x32_bf16 v[212:215], v[116:119], a[240:243], v[212:215]\n"
      "global_store_dwordx4 v242, v[232:235], s[62:63] offset:1024 sc1\n"
      "global_load_dwordx4 v[140:143], v240, s[72:73] offset:-4096 sc1\n"
      "s_waitcnt lgkmcnt(10)\n"
      "v_mfma_f32_16x16x32_bf16 v[216:219], v[112:115], a[244:247], v[216:219]\n"
      "v_mfma_f32_16x16x32_bf16 v[220:223], v[116:119], a[244:247], v[220:223]\n"
      "s_waitcnt lgkmcnt(9)\n"
      "v_mfma_f32_16x16x32_bf16 v[208:211], v[120:123], a[248:251], v[208:211]\n"
      "v_mfma_f32_16x16x32_bf16 v[212:215], v[124:127], a[248:251], v[212:215]\n"
      "global_load_dwordx4 v[144:147], v240, s[72:73] offset:-3072 sc1\n"
      "s_waitcnt lgkmcnt(8)\n"
      "v_mfma_f32_16x16x32_bf16 v[216:219], v[120:123], a[252:255], v[216:219]\n"
      "v_mfma_f32_16x16x32_bf16 v[220:223], v[124:127], a[252:255], v[220:223]\n"
      "s_waitcnt vmcnt(7)\n"
      "v_mfma_f32_16x16x32_bf16 v[0:3], v[128:131], a[0:3], 0\n"
      "v_mfma_f32_16x16x32_bf16 v[4:7], v[128:131], a[4:7], 0\n"
      "global_load_dwordx4 v[148:151], v240, s[72:73] offset:-2048 sc1\n"
      "v_mfma_f32_16x16x32_bf16 v[8:11], v[128:131], a[8:11], 0\n"
      "v_cvt_pk_bf16_f32 v192, v208, v209\n"
      "v_cvt_pk_bf16_f32 v193, v210, v211\n"
      "v_mfma_f32_16x16x32_bf16 v[12:15], v[128:131], a[12:15], 0\n"
      "v_cvt_pk_bf16_f32 v194, v212, v213\n"
      "v_cvt_pk_bf16_f32 v195, v214, v215\n"
      "v_mfma_f32_16x16x32_bf16 v[16:19], v[128:131], a[16:19], 0\n"
      "v_xor_b32 v192, s85, v192\n"
      "v_xor_b32 v193, s85, v193\n"
      "v_mfma_f32_16x16x32_bf16 v[20:23], v[128:131], a[20:23], 0\n"
      "ds_read_b128 a[80:83], v236 offset:49152\n"
      "v_xor_b32 v194, s85, v194\n"
      "v_mfma_f32_16x16x32_bf16 v[24:27], v[128:131], a[24:27], 0\n"
      "ds_read_b128 a[84:87], v236 offset:51200\n"
      "v_xor_b32 v195, s85, v195\n"
      "v_mfma_f32_16x16x32_bf16 v[28:31], v[128:131], a[28:31], 0\n"
      "ds_read_b128 a[112:115], v237 offset:49152\n"
      "v_cvt_pk_bf16_f32 v196, v216, v217\n"
      "s_waitcnt vmcnt(7)\n"
      "v_mfma_f32_16x16x32_bf16 v[32:35], v[132:135], a[0:3], 0\n"
      "s_xor_b32 s70, s76, 256\n"
      "s_add_u32 s70, s8, s70\n"
      "s_addc_u32 s71, s9, 0\n"
      "s_add_u32 m0, s41, 4096\n"
      "v_mfma_f32_16x16x32_bf16 v[36:39], v[132:135], a[4:7], 0\n"
      "global_load_lds_dwordx4 v244, s[70:71] offset:0\n"
      "v_cvt_pk_bf16_f32 v197, v218, v219\n"
      "v_mfma_f32_16x16x32_bf16 v[40:43], v[132:135], a[8:11], 0\n"
      "ds_read_b128 a[116:119], v237 offset:51200\n"
      "v_cvt_pk_bf16_f32 v198, v220, v221\n"
      "v_mfma_f32_16x16x32_bf16 v[44:47], v[132:135], a[12:15], 0\n"
      "v_cvt_pk_bf16_f32 v199, v222, v223\n"
      "v_xor_b32 v196, s85, v196\n"
      "v_mfma_f32_16x16x32_bf16 v[48:51], v[132:135], a[16:19], 0\n"
      "global_load_lds_dwordx4 v245, s[70:71] offset:1024\n"
      "v_xor_b32 v197, s85, v197\n"
      "v_mfma_f32_16x16x32_bf16 v[52:55], v[132:135], a[20:23], 0\n"
      "v_xor_b32 v198, s85, v198\n"
      "v_xor_b32 v199, s85, v199\n"
      "v_mfma_f32_16x16x32_bf16 v[56:59], v[132:135], a[24:27], 0\n"
      "global_store_dwordx4 v242, v[192:195], s[62:63] offset:2048 sc1\n"
      "global_store_dwordx4 v242, v[196:199], s[62:63] offset:3072 sc1\n"
      "v_mfma_f32_16x16x32_bf16 v[60:63], v[132:135], a[28:31], 0\n"
      "s_waitcnt vmcnt(6)\n"
      "global_load_lds_dwordx4 v246, s[70:71] offset:2048\n"
      "v_mfma_f32_16x16x32_bf16 v[64:67], v[136:139], a[0:3], 0\n"
      "v_mfma_f32_16x16x32_bf16 v[68:71], v[136:139], a[4:7], 0\n"
      "v_mfma_f32_16x16x32_bf16 v[72:75], v[136:139], a[8:11], 0\n"
      "global_load_lds_dwordx4 v247, s[70:71] offset:3072\n"
      "v_mfma_f32_16x16x32_bf16 v[76:79], v[136:139], a[12:15], 0\n"
      "v_mfma_f32_16x16x32_bf16 v[80:83], v[136:139], a[16:19], 0\n"
      "global_load_dwordx4 v[152:155], v241, s[10:11] offset:-4096 sc1\n"
      "v_mfma_f32_16x16x32_bf16 v[84:87], v[136:139], a[20:23], 0\n"
      "ds_read_b128 a[88:91], v248 offset:0\n"
      "ds_read_b128 a[92:95], v248 offset:2048\n"
      "v_mfma_f32_16x16x32_bf16 v[88:91], v[136:139], a[24:27], 0\n"
      "ds_read_b128 a[120:123], v249 offset:0\n"
      "ds_read_b128 a[124:127], v249 offset:2048\n"
      "v_mfma_f32_16x16x32_bf16 v[92:95], v[136:139], a[28:31], 0\n"
      "global_load_dwordx4 v[156:159], v241, s[10:11] offset:-3072 sc1\n"
      "v_mfma_f32_16x16x32_bf16 v[0:3], v[140:143], a[32:35], v[0:3]\n"
      "v_mfma_f32_16x16x32_bf16 v[4:7], v[140:143], a[36:39], v[4:7]\n"
      "v_mfma_f32_16x16x32_bf16 v[8:11], v[140:143], a[40:43], v[8:11]\n"
      "global_load_dwordx4 v[160:163], v241, s[10:11] offset:-2048 sc1\n"
      "v_mfma_f32_16x16x32_bf16 v[12:15], v[140:143], a[44:47], v[12:15]\n"
      "v_mfma_f32_16x16x32_bf16 v[16:19], v[140:143], a[48:51], v[16:19]\n"
      "v_mfma_f32_16x16x32_bf16 v[20:23], v[140:143], a[52:55], v[20:23]\n"
      "s_xor_b32 s70, s77, 256\n"
      "s_add_u32 s70, s8, s70\n"
      "s_addc_u32 s71, s9, 0\n"
      "s_add_u32 m0, s41, 8192\n"
      "v_mfma_f32_16x16x32_bf16 v[24:27], v[140:143], a[56:59], v[24:27]\n"
      "global_load_lds_dwordx4 v244, s[70:71] offset:0\n"
      "v_mfma_f32_16x16x32_bf16 v[28:31], v[140:143], a[60:63], v[28:31]\n"
      "s_waitcnt vmcnt(10)\n"
      "v_mfma_f32_16x16x32_bf16 v[32:35], v[144:147], a[32:35], v[32:35]\n"
      "v_add_u32 v238, s42, v242\n"
      "v_mov_b32 v200, s37\n"
      "v_mov_b32 v201, s37\n"
      "v_mov_b32 v202, s37\n"
      "v_mov_b32 v203, s37\n"
      "s_waitcnt vmcnt(6)\n"
      "s_mov_b64 exec, 0xff\n"
      "global_store_dwordx4 v238, v[200:203], s[58:59] offset:128 sc1\n"
      "s_mov_b64 exec, -1\n"
      "v_mfma_f32_16x16x32_bf16 v[36:39], v[144:147], a[36:39], v[36:39]\n"
      "s_lshl2_add_u32 s48, s36, s35\n"
      "global_load_lds_dwordx4 v245, s[70:71] offset:1024\n"
      "s_mul_i32 s48, s48, 147712\n"
      "v_mfma_f32_16x16x32_bf16 v[40:43], v[144:147], a[40:43], v[40:43]\n"
      "s_mul_i32 s49, s33, 3072\n"
      "s_add_u32 s48, s48, s49\n"
      "s_add_u32 s48, s48, 6291456\n"
      "v_mfma_f32_16x16x32_bf16 v[44:47], v[144:147], a[44:47], v[44:47]\n"
      "s_add_u32 s60, s18, s48\n"
      "s_addc_u32 s61, s19, 0\n"
      "s_mul_i32 s49, s34, 36864\n"
      "v_mfma_f32_16x16x32_bf16 v[48:51], v[144:147], a[48:51], v[48:51]\n"
      "s_add_u32 s62, s60, s49\n"
      "s_addc_u32 s63, s61, 0\n"
      "global_load_lds_dwordx4 v246, s[70:71] offset:2048\n"
      "v_mfma_f32_16x16x32_bf16 v[52:55], v[144:147], a[52:55], v[52:55]\n"
      "v_mfma_f32_16x16x32_bf16 v[56:59], v[144:147], a[56:59], v[56:59]\n"
      "v_mfma_f32_16x16x32_bf16 v[60:63], v[144:147], a[60:63], v[60:63]\n"
      "global_load_lds_dwordx4 v247, s[70:71] offset:3072\n"
      "v_mfma_f32_16x16x32_bf16 v[64:67], v[148:151], a[32:35], v[64:67]\n"
      "v_mfma_f32_16x16x32_bf16 v[68:71], v[148:151], a[36:39], v[68:71]\n"
      "s_xor_b32 s70, s78, 256\n"
      "s_add_u32 s70, s8, s70\n"
      "s_addc_u32 s71, s9, 0\n"
      "s_add_u32 m0, s41, 12288\n"
      "v_mfma_f32_16x16x32_bf16 v[72:75], v[148:151], a[40:43], v[72:75]\n"
      "global_load_lds_dwordx4 v244, s[70:71] offset:0\n"
      "v_mfma_f32_16x16x32_bf16 v[76:79], v[148:151], a[44:47], v[76:79]\n"
      "v_mfma_f32_16x16x32_bf16 v[80:83], v[148:151], a[48:51], v[80:83]\n"
      "v_mfma_f32_16x16x32_bf16 v[84:87], v[148:151], a[52:55], v[84:87]\n"
      "global_load_lds_dwordx4 v245, s[70:71] offset:1024\n"
      "v_mfma_f32_16x16x32_bf16 v[88:91], v[148:151], a[56:59], v[88:91]\n"
      "v_mfma_f32_16x16x32_bf16 v[92:95], v[148:151], a[60:63], v[92:95]\n"
      "s_waitcnt vmcnt(8)\n"
      "s_waitcnt lgkmcnt(15)\n"
      "v_mfma_f32_16x16x32_bf16 v[0:3], v[152:155], a[64:67], v[0:3]\n"
      "global_load_lds_dwordx4 v246, s[70:71] offset:2048\n"
      "s_waitcnt lgkmcnt(14)\n"
      "v_mfma_f32_16x16x32_bf16 v[4:7], v[152:155], a[68:71], v[4:7]\n"
      "s_waitcnt lgkmcnt(11)\n"
      "v_mfma_f32_16x16x32_bf16 v[8:11], v[152:155], a[72:75], v[8:11]\n"
      "s_waitcnt lgkmcnt(10)\n"
      "v_mfma_f32_16x16x32_bf16 v[12:15], v[152:155], a[76:79], v[12:15]\n"
      "global_load_lds_dwordx4 v247, s[70:71] offset:3072\n"
      "s_waitcnt lgkmcnt(7)\n"
      "v_mfma_f32_16x16x32_bf16 v[16:19], v[152:155], a[80:83], v[16:19]\n"
      "s_waitcnt lgkmcnt(6)\n"
      "v_mfma_f32_16x16x32_bf16 v[20:23], v[152:155], a[84:87], v[20:23]\n"
      "global_load_dwordx4 v[164:167], v241, s[72:73] offset:-4096 sc1\n"
      "s_waitcnt lgkmcnt(3)\n"
      "v_mfma_f32_16x16x32_bf16 v[24:27], v[152:155], a[88:91], v[24:27]\n"
      "s_waitcnt lgkmcnt(2)\n"
      "v_mfma_f32_16x16x32_bf16 v[28:31], v[152:155], a[92:95], v[28:31]\n"
      "v_mfma_f32_16x16x32_bf16 v[32:35], v[156:159], a[64:67], v[32:35]\n"
      "global_load_dwordx4 v[168:171], v241, s[72:73] offset:-3072 sc1\n"
      "v_mfma_f32_16x16x32_bf16 v[36:39], v[156:159], a[68:71], v[36:39]\n"
      "v_mfma_f32_16x16x32_bf16 v[40:43], v[156:159], a[72:75], v[40:43]\n"
      "v_mfma_f32_16x16x32_bf16 v[44:47], v[156:159], a[76:79], v[44:47]\n"
      "global_load_dwordx4 v[172:175], v241, s[72:73] offset:-2048 sc1\n"
      "v_mfma_f32_16x16x32_bf16 v[48:51], v[156:159], a[80:83], v[48:51]\n"
      "v_mfma_f32_16x16x32_bf16 v[52:55], v[156:159], a[84:87], v[52:55]\n"
      "v_mfma_f32_16x16x32_bf16 v[56:59], v[156:159], a[88:91], v[56:59]\n"
      "s_xor_b32 s70, s79, 256\n"
      "s_add_u32 s70, s8, s70\n"
      "s_addc_u32 s71, s9, 0\n"
      "s_add_u32 m0, s82, 98304\n"
      "v_mfma_f32_16x16x32_bf16 v[60:63], v[156:159], a[92:95], v[60:63]\n"
      "s_waitcnt vmcnt(2)\n"
      "global_load_lds_dwordx4 v244, s[70:71] offset:0\n"
      "ds_read_b128 a[128:131], v248 offset:4096\n"
      "v_mfma_f32_16x16x32_bf16 v[64:67], v[160:163], a[64:67], v[64:67]\n"
      "ds_read_b128 a[132:135], v248 offset:6144\n"
      "ds_read_b128 a[160:163], v249 offset:4096\n"
      "v_mfma_f32_16x16x32_bf16 v[68:71], v[160:163], a[68:71], v[68:71]\n"
      "ds_read_b128 a[164:167], v249 offset:6144\n"
      "v_mfma_f32_16x16x32_bf16 v[72:75], v[160:163], a[72:75], v[72:75]\n"
      "global_load_lds_dwordx4 v245, s[70:71] offset:1024\n"
      "v_mfma_f32_16x16x32_bf16 v[76:79], v[160:163], a[76:79], v[76:79]\n"
      "v_mfma_f32_16x16x32_bf16 v[80:83], v[160:163], a[80:83], v[80:83]\n"
      "v_mfma_f32_16x16x32_bf16 v[84:87], v[160:163], a[84:87], v[84:87]\n"
      "global_load_lds_dwordx4 v246, s[70:71] offset:2048\n"
      "v_mfma_f32_16x16x32_bf16 v[88:91], v[160:163], a[88:91], v[88:91]\n"
      "s_lshr_b32 s54, s34, 1\n"
      "s_lshl2_add_u32 s54, s54, s35\n"
      "s_lshl_b32 s54, s54, 12\n"
      "v_mfma_f32_16x16x32_bf16 v[92:95], v[160:163], a[92:95], v[92:95]\n"
      "s_add_u32 s54, s54, 4096\n"
      "v_lshl_add_u32 v200, v243, 6, s54\n"
      "global_load_dwordx2 v[202:203], v200, s[58:59] sc1\n"
      "v_mfma_f32_16x16x32_bf16 v[0:3], v[164:167], a[96:99], v[0:3]\n"
      "global_load_lds_dwordx4 v247, s[70:71] offset:3072\n"
      "v_mfma_f32_16x16x32_bf16 v[4:7], v[164:167], a[100:103], v[4:7]\n"
      "v_mfma_f32_16x16x32_bf16 v[8:11], v[164:167], a[104:107], v[8:11]\n"
      "global_load_dwordx4 v[176:179], v240, s[14:15] offset:-4096 sc1\n"
      "v_mfma_f32_16x16x32_bf16 v[12:15], v[164:167], a[108:111], v[12:15]\n"
      "v_mfma_f32_16x16x32_bf16 v[16:19], v[164:167], a[112:115], v[16:19]\n"
      "v_mfma_f32_16x16x32_bf16 v[20:23], v[164:167], a[116:119], v[20:23]\n"
      "global_load_dwordx4 v[180:183], v240, s[14:15] offset:-3072 sc1\n"
      "s_waitcnt lgkmcnt(5)\n"
      "v_mfma_f32_16x16x32_bf16 v[24:27], v[164:167], a[120:123], v[24:27]\n"
      "s_waitcnt lgkmcnt(4)\n"
      "v_mfma_f32_16x16x32_bf16 v[28:31], v[164:167], a[124:127], v[28:31]\n"
      "s_waitcnt vmcnt(7)\n"
      "v_mfma_f32_16x16x32_bf16 v[32:35], v[168:171], a[96:99], v[32:35]\n"
      "global_load_dwordx4 v[184:187], v240, s[14:15] offset:-2048 sc1\n"
      "global_load_dwordx4 v[188:191], v240, s[74:75] offset:-4096 sc1\n"
      "v_mfma_f32_16x16x32_bf16 v[36:39], v[168:171], a[100:103], v[36:39]\n"
      "global_load_dwordx4 v[128:131], v240, s[74:75] offset:-3072 sc1\n"
      "global_load_dwordx4 v[132:135], v240, s[74:75] offset:-2048 sc1\n"
      "v_mfma_f32_16x16x32_bf16 v[40:43], v[168:171], a[104:107], v[40:43]\n"
      "global_load_dword v252, v253, s[58:59] offset:128 sc1\n"
      "v_mfma_f32_16x16x32_bf16 v[44:47], v[168:171], a[108:111], v[44:47]\n"
      "v_mfma_f32_16x16x32_bf16 v[48:51], v[168:171], a[112:115], v[48:51]\n"
      "ds_read_b128 a[136:139], v248 offset:8192\n"
      "ds_read_b128 a[140:143], v248 offset:10240\n"
      "v_mfma_f32_16x16x32_bf16 v[52:55], v[168:171], a[116:119], v[52:55]\n"
      "ds_read_b128 a[168:171], v249 offset:8192\n"
      "ds_read_b128 a[172:175], v249 offset:10240\n"
      "v_mfma_f32_16x16x32_bf16 v[56:59], v[168:171], a[120:123], v[56:59]\n"
      "v_mfma_f32_16x16x32_bf16 v[60:63], v[168:171], a[124:127], v[60:63]\n"
      "v_mfma_f32_16x16x32_bf16 v[64:67], v[172:175], a[96:99], v[64:67]\n"
      "v_mfma_f32_16x16x32_bf16 v[68:71], v[172:175], a[100:103], v[68:71]\n"
      "v_mfma_f32_16x16x32_bf16 v[72:75], v[172:175], a[104:107], v[72:75]\n"
      "v_mfma_f32_16x16x32_bf16 v[76:79], v[172:175], a[108:111], v[76:79]\n"
      "v_mfma_f32_16x16x32_bf16 v[80:83], v[172:175], a[112:115], v[80:83]\n"
      "v_mfma_f32_16x16x32_bf16 v[84:87], v[172:175], a[116:119], v[84:87]\n"
      "v_mfma_f32_16x16x32_bf16 v[88:91], v[172:175], a[120:123], v[88:91]\n"
      "v_mfma_f32_16x16x32_bf16 v[92:95], v[172:175], a[124:127], v[92:95]\n"
      "s_waitcnt vmcnt(5)\n"
      "s_waitcnt lgkmcnt(7)\n"
      "v_mfma_f32_16x16x32_bf16 v[0:3], v[176:179], a[128:131], v[0:3]\n"
      "ds_read_b128 a[144:147], v248 offset:12288\n"
      "ds_read_b128 a[148:151], v248 offset:14336\n"
      "s_waitcnt lgkmcnt(8)\n"
      "v_mfma_f32_16x16x32_bf16 v[4:7], v[176:179], a[132:135], v[4:7]\n"
      "ds_read_b128 a[176:179], v249 offset:12288\n"
      "ds_read_b128 a[180:183], v249 offset:14336\n"
      "s_waitcnt lgkmcnt(7)\n"
      "v_mfma_f32_16x16x32_bf16 v[8:11], v[176:179], a[136:139], v[8:11]\n"
      "s_xor_b32 s70, s76, 384\n"
      "s_add_u32 s70, s8, s70\n"
      "s_addc_u32 s71, s9, 0\n"
      "s_add_u32 m0, s82, 114688\n"
      "s_waitcnt lgkmcnt(6)\n"
      "v_mfma_f32_16x16x32_bf16 v[12:15], v[176:179], a[140:143], v[12:15]\n"
      "global_load_lds_dwordx4 v244, s[70:71] offset:0\n"
      "s_waitcnt lgkmcnt(3)\n"
      "v_mfma_f32_16x16x32_bf16 v[16:19], v[176:179], a[144:147], v[16:19]\n"
      "s_waitcnt lgkmcnt(2)\n"
      "v_mfma_f32_16x16x32_bf16 v[20:23], v[176:179], a[148:151], v[20:23]\n"
      "ds_read_b128 a[152:155], v236 offset:0\n"
      "ds_read_b128 a[156:159], v236 offset:2048\n"
      "ds_read_b128 a[184:187], v237 offset:0\n"
      "ds_read_b128 a[188:191], v237 offset:2048\n"
      "s_waitcnt lgkmcnt(3)\n"
      "v_mfma_f32_16x16x32_bf16 v[24:27], v[176:179], a[152:155], v[24:27]\n"
      "global_load_lds_dwordx4 v245, s[70:71] offset:1024\n"
      "s_waitcnt lgkmcnt(2)\n"
      "v_mfma_f32_16x16x32_bf16 v[28:31], v[176:179], a[156:159], v[28:31]\n"
      "v_mfma_f32_16x16x32_bf16 v[32:35], v[180:183], a[128:131], v[32:35]\n"
      "v_mfma_f32_16x16x32_bf16 v[36:39], v[180:183], a[132:135], v[36:39]\n"
      "global_load_lds_dwordx4 v246, s[70:71] offset:2048\n"
      "v_mfma_f32_16x16x32_bf16 v[40:43], v[180:183], a[136:139], v[40:43]\n"
      "v_mfma_f32_16x16x32_bf16 v[44:47], v[180:183], a[140:143], v[44:47]\n"
      "v_mfma_f32_16x16x32_bf16 v[48:51], v[180:183], a[144:147], v[48:51]\n"
      "global_load_lds_dwordx4 v247, s[70:71] offset:3072\n"
      "v_mfma_f32_16x16x32_bf16 v[52:55], v[180:183], a[148:151], v[52:55]\n"
      "v_mfma_f32_16x16x32_bf16 v[56:59], v[180:183], a[152:155], v[56:59]\n"
      "s_xor_b32 s70, s77, 384\n"
      "s_add_u32 s70, s8, s70\n"
      "s_addc_u32 s71, s9, 0\n"
      "s_add_u32 m0, s82, 131072\n"
      "v_mfma_f32_16x16x32_bf16 v[60:63], v[180:183], a[156:159], v[60:63]\n"
      "s_waitcnt vmcnt(7)\n"
      "global_load_lds_dwordx4 v244, s[70:71] offset:0\n"
      "s_waitcnt vmcnt(5)\n"
      "v_cmp_ne_u32 vcc, s37, v252\n"
      "s_nop 1\n"
      "s_cbranch_vccnz .Lp_s5_%=\n"
      ".Lp_o6_%=:\n"
      "v_mfma_f32_16x16x32_bf16 v[64:67], v[184:187], a[128:131], v[64:67]\n"
      "global_load_dwordx4 v[136:139], v240, s[10:11] offset:-1024 sc1\n"
      "v_mfma_f32_16x16x32_bf16 v[68:71], v[184:187], a[132:135], v[68:71]\n"
      "v_mfma_f32_16x16x32_bf16 v[72:75], v[184:187], a[136:139], v[72:75]\n"
      "global_load_lds_dwordx4 v245, s[70:71] offset:1024\n"
      "v_mfma_f32_16x16x32_bf16 v[76:79], v[184:187], a[140:143], v[76:79]\n"
      "v_mfma_f32_16x16x32_bf16 v[80:83], v[184:187], a[144:147], v[80:83]\n"
      "global_load_dwordx4 v[140:143], v240, s[72:73] offset:-1024 sc1\n"
      "v_mfma_f32_16x16x32_bf16 v[84:87], v[184:187], a[148:151], v[84:87]\n"
      "global_load_lds_dwordx4 v246, s[70:71] offset:2048\n"
      "v_mfma_f32_16x16x32_bf16 v[88:91], v[184:187], a[152:155], v[88:91]\n"
      "v_mfma_f32_16x16x32_bf16 v[92:95], v[184:187], a[156:159], v[92:95]\n"
      "v_mfma_f32_16x16x32_bf16 v[0:3], v[188:191], a[160:163], v[0:3]\n"
      "global_load_lds_dwordx4 v247, s[70:71] offset:3072\n"
      "global_load_dwordx4 v[144:147], v241, s[10:11] offset:-1024 sc1\n"
      "v_mfma_f32_16x16x32_bf16 v[4:7], v[188:191], a[164:167], v[4:7]\n"
      "v_mfma_f32_16x16x32_bf16 v[8:11], v[188:191], a[168:171], v[8:11]\n"
      "s_xor_b32 s70, s78, 384\n"
      "s_add_u32 s70, s8, s70\n"
      "s_addc_u32 s71, s9, 0\n"
      "s_add_u32 m0, s82, 147456\n"
      "v_mfma_f32_16x16x32_bf16 v[12:15], v[188:191], a[172:175], v[12:15]\n"
      "global_load_lds_dwordx4 v244, s[70:71] offset:0\n"
      "v_mfma_f32_16x16x32_bf16 v[16:19], v[188:191], a[176:179], v[16:19]\n"
      "global_load_dwordx4 v[148:151], v241, s[72:73] offset:-1024 sc1\n"
      "v_mfma_f32_16x16x32_bf16 v[20:23], v[188:191], a[180:183], v[20:23]\n"
      "s_waitcnt lgkmcnt(1)\n"
      "v_mfma_f32_16x16x32_bf16 v[24:27], v[188:191], a[184:187], v[24:27]\n"
      "global_load_lds_dwordx4 v245, s[70:71] offset:1024\n"
      "s_waitcnt lgkmcnt(0)\n"
      "v_mfma_f32_16x16x32_bf16 v[28:31], v[188:191], a[188:191], v[28:31]\n"
      "v_mfma_f32_16x16x32_bf16 v[32:35], v[128:131], a[160:163], v[32:35]\n"
      "global_load_dwordx4 v[152:155], v240, s[14:15] offset:-1024 sc1\n"
      "v_mfma_f32_16x16x32_bf16 v[36:39], v[128:131], a[164:167], v[36:39]\n"
      "global_load_lds_dwordx4 v246, s[70:71] offset:2048\n"
      "v_mfma_f32_16x16x32_bf16 v[40:43], v[128:131], a[168:171], v[40:43]\n"
      "v_cmp_ne_u32 vcc, s37, v203\n"
      "s_nop 1\n"
      "s_cbranch_vccnz .Lp_s8_%=\n"
      ".Lp_o9_%=:\n"
      "v_mfma_f32_16x16x32_bf16 v[44:47], v[128:131], a[172:175], v[44:47]\n"
      "v_max_u32_dpp v202, v202, v202 quad_perm:[1,0,3,2] row_mask:0xf bank_mask:0xf\n"
      "s_nop 1\n"
      "v_max_u32_dpp v202, v202, v202 quad_perm:[2,3,0,1] row_mask:0xf bank_mask:0xf\n"
      "v_mfma_f32_16x16x32_bf16 v[48:51], v[128:131], a[176:179], v[48:51]\n"
      "global_load_lds_dwordx4 v247, s[70:71] offset:3072\n"
      "v_max_u32_dpp v202, v202, v202 row_half_mirror row_mask:0xf bank_mask:0xf\n"
      "v_mfma_f32_16x16x32_bf16 v[52:55], v[128:131], a[180:183], v[52:55]\n"
      "global_load_dwordx4 v[156:159], v241, s[14:15] offset:-4096 sc1\n"
      "v_max_u32_dpp v202, v202, v202 row_mirror row_mask:0xf bank_mask:0xf\n"
      "v_mfma_f32_16x16x32_bf16 v[56:59], v[128:131], a[184:187], v[56:59]\n"
      "s_xor_b32 s70, s79, 384\n"
      "s_add_u32 s70, s8, s70\n"
      "s_addc_u32 s71, s9, 0\n"
      "s_add_u32 m0, s41, 0\n"
      "v_mfma_f32_16x16x32_bf16 v[60:63], v[128:131], a[188:191], v[60:63]\n"
      "global_load_lds_dwordx4 v244, s[70:71] offset:0\n"
      "v_mov_b32 v201, v202\n"
      "v_mfma_f32_16x16x32_bf16 v[64:67], v[132:135], a[160:163], v[64:67]\n"
      "global_load_dwordx4 v[160:163], v241, s[74:75] offset:-4096 sc1\n"
      "v_permlane16_swap_b32 v202, v201\n"
      "v_mfma_f32_16x16x32_bf16 v[68:71], v[132:135], a[164:167], v[68:71]\n"
      "v_max_u32 v202, v202, v201\n"
      "v_mov_b32 v201, v202\n"
      "v_mfma_f32_16x16x32_bf16 v[72:75], v[132:135], a[168:171], v[72:75]\n"
      "global_load_lds_dwordx4 v245, s[70:71] offset:1024\n"
      "v_permlane32_swap_b32 v202, v201\n"
      "v_mfma_f32_16x16x32_bf16 v[76:79], v[132:135], a[172:175], v[76:79]\n"
      "v_max_u32 v202, v202, v201\n"
      "s_nop 0\n"
      "v_readfirstlane_b32 s40, v202\n"
      "v_mfma_f32_16x16x32_bf16 v[80:83], v[132:135], a[176:179], v[80:83]\n"
      "s_nop 0\n"
      "s_andn2_b32 s42, 0x7ff, s40\n"
      "global_load_dwordx4 v[164:167], v241, s[14:15] offset:-3072 sc1\n"
      "v_mfma_f32_16x16x32_bf16 v[84:87], v[132:135], a[180:183], v[84:87]\n"
      "global_load_lds_dwordx4 v246, s[70:71] offset:2048\n"
      "v_mfma_f32_16x16x32_bf16 v[88:91], v[132:135], a[184:187], v[88:91]\n"
      "v_mfma_f32_16x16x32_bf16 v[92:95], v[132:135], a[188:191], v[92:95]\n"
      "s_waitcnt vmcnt(15)\n"
      "v_mfma_f32_16x16x32_bf16 v[96:99], v[136:139], a[0:3], 0\n"
      "global_load_lds_dwordx4 v247, s[70:71] offset:3072\n"
      "global_load_dwordx4 v[168:171], v241, s[74:75] offset:-3072 sc1\n"
      "v_mfma_f32_16x16x32_bf16 v[100:103], v[136:139], a[4:7], 0\n"
      "v_mfma_f32_16x16x32_bf16 v[104:107], v[136:139], a[8:11], 0\n"
      "s_barrier\n"
      "s_lshr_b32 s80, s32, 4\n"
      "s_bfe_u32 s43, s32, 0x10002\n"
      "s_lshl1_add_u32 s80, s80, s43\n"
      "s_and_b32 s81, s32, 3\n"
      "s_cmp_lt_u32 s80, 28\n"
      "s_cselect_b32 s86, -1, 0\n"
      "s_lshr_b32 s83, s33, 1\n"
      "s_lshl1_add_u32 s83, s81, s83\n"
      "s_min_u32 s43, s80, 27\n"
      "v_lshrrev_b32 v226, 3, v243\n"
      "v_and_b32 v227, 0x70, v242\n"
      "v_lshl_or_b32 v224, v226, 10, v227\n"
      "v_add_u32 v225, 7168, v224\n"
      "v_add_u32 v226, 14336, v224\n"
      "v_add_u32 v227, 21504, v224\n"
      "s_lshr_b32 s44, s33, 1\n"
      "s_lshl1_add_u32 s44, s43, s44\n"
      "s_lshl_b32 s44, s44, 15\n"
      "s_and_b32 s45, s33, 1\n"
      "s_lshl1_add_u32 s45, s81, s45\n"
      "s_lshl_b32 s45, s45, 7\n"
      "s_add_u32 s44, s44, s45\n"
      "s_add_u32 s70, s30, s44\n"
      "s_addc_u32 s71, s31, 0\n"
      "s_lshl_b32 s43, s33, 12\n"
      "s_add_u32 m0, s43, 65536\n"
      "s_nop 0\n"
      "global_load_lds_dwordx4 v224, s[70:71] offset:0\n"
      "global_load_lds_dwordx4 v225, s[70:71] offset:1024\n"
      "global_load_lds_dwordx4 v226, s[70:71] offset:2048\n"
      "global_load_lds_dwordx4 v227, s[70:71] offset:3072\n"
      "v_mfma_f32_16x16x32_bf16 v[108:111], v[136:139], a[12:15], 0\n"
      "v_lshrrev_b32 v201, 4, v243\n"
      "v_bfe_u32 v224, v243, 2, 2\n"
      "v_mfma_f32_16x16x32_bf16 v[112:115], v[136:139], a[16:19], 0\n"
      "global_load_dwordx4 v[172:175], v241, s[14:15] offset:-2048 sc1\n"
      "v_lshl_or_b32 v201, v201, 3, v224\n"
      "v_mfma_f32_16x16x32_bf16 v[116:119], v[136:139], a[20:23], 0\n"
      "v_and_b32 v224, 3, v243\n"
      "v_lshlrev_b32 v224, 3, v224\n"
      "v_mfma_f32_16x16x32_bf16 v[120:123], v[136:139], a[24:27], 0\n"
      "v_lshl_or_b32 v201, v201, 7, v224\n"
      "s_mov_b32 s64, 0xffff0000\n"
      "v_mfma_f32_16x16x32_bf16 v[124:127], v[136:139], a[28:31], 0\n"
      "s_mov_b32 s65, 0xffff0000\n"
      "s_mov_b32 s66, -1\n"
      "s_mov_b32 s67, 0\n"
      "v_mfma_f32_16x16x32_bf16 v[96:99], v[140:143], a[32:35], v[96:99]\n"
      "global_load_dwordx4 v[176:179], v241, s[74:75] offset:-2048 sc1\n"
      "v_bfe_u32 v224, v243, 4, 1\n"
      "v_mfma_f32_16x16x32_bf16 v[100:103], v[140:143], a[36:39], v[100:103]\n"
      "ds_read_b128 a[192:195], v236 offset:16384\n"
      "v_and_b32 v203, 0xf0, v242\n"
      "v_mfma_f32_16x16x32_bf16 v[104:107], v[140:143], a[40:43], v[104:107]\n"
      "ds_read_b128 a[196:199], v236 offset:18432\n"
      "v_lshl_or_b32 v203, v224, 14, v203\n"
      "v_mfma_f32_16x16x32_bf16 v[108:111], v[140:143], a[44:47], v[108:111]\n"
      "s_lshr_b32 s54, s33, 1\n"
      "ds_read_b128 a[224:227], v237 offset:16384\n"
      "s_lshl1_add_u32 s54, s80, s54\n"
      "v_mfma_f32_16x16x32_bf16 v[112:115], v[140:143], a[48:51], v[112:115]\n"
      "s_lshl_b32 s54, s54, 12\n"
      "global_load_dwordx4 v[180:183], v240, s[74:75] offset:-1024 sc1\n"
      "s_add_u32 s54, s54, 262144\n"
      "v_mfma_f32_16x16x32_bf16 v[116:119], v[140:143], a[52:55], v[116:119]\n"
      "ds_read_b128 a[228:231], v237 offset:18432\n"
      "v_add_u32 v200, s54, v242\n"
      "v_mfma_f32_16x16x32_bf16 v[120:123], v[140:143], a[56:59], v[120:123]\n"
      "s_lshl_b32 s54, s33, 12\n"
      "s_add_u32 s54, s54, 65536\n"
      "v_add_u32 v201, s54, v201\n"
      "v_mfma_f32_16x16x32_bf16 v[124:127], v[140:143], a[60:63], v[124:127]\n"
      "s_waitcnt vmcnt(19)\n"
      "v_and_b32 v202, 15, v243\n"
      "v_lshrrev_b32 v199, 5, v243\n"
      "v_mfma_f32_16x16x32_bf16 v[96:99], v[144:147], a[64:67], v[96:99]\n"
      "global_load_dwordx4 v[184:187], v241, s[14:15] offset:-1024 sc1\n"
      "v_lshl_or_b32 v202, v199, 4, v202\n"
      "v_mfma_f32_16x16x32_bf16 v[100:103], v[144:147], a[68:71], v[100:103]\n"
      "s_add_u32 s54, s41, 12288\n"
      "v_lshl_add_u32 v202, v202, 1, s54\n"
      "v_and_b32 v199, 48, v243\n"
      "v_mfma_f32_16x16x32_bf16 v[104:107], v[144:147], a[72:75], v[104:107]\n"
      "v_add_u32 v199, s54, v199\n"
      "s_and_b32 s54, s33, 1\n"
      "v_mfma_f32_16x16x32_bf16 v[108:111], v[144:147], a[76:79], v[108:111]\n"
      "s_lshl1_add_u32 s54, s81, s54\n"
      "s_lshl_b32 s54, s54, 15\n"
      "s_lshr_b32 s55, s33, 1\n"
      "v_mfma_f32_16x16x32_bf16 v[112:115], v[144:147], a[80:83], v[112:115]\n"
      "s_lshl1_add_u32 s55, s80, s55\n"
      "global_load_dwordx4 v[188:191], v241, s[74:75] offset:-1024 sc1\n"
      "s_lshl_b32 s55, s55, 8\n"
      "v_mfma_f32_16x16x32_bf16 v[116:119], v[144:147], a[84:87], v[116:119]\n"
      "s_add_u32 s54, s54, s55\n"
      "ds_read_b128 a[200:203], v236 offset:32768\n"
      "s_add_u32 s54, s54, 524288\n"
      "v_mfma_f32_16x16x32_bf16 v[120:123], v[144:147], a[88:91], v[120:123]\n"
      "ds_read_b128 a[204:207], v236 offset:34816\n"
      "v_add_u32 v203, s54, v203\n"
      "v_mfma_f32_16x16x32_bf16 v[124:127], v[144:147], a[92:95], v[124:127]\n"
      "ds_read_b128 a[232:235], v237 offset:32768\n"
      "ds_read_b128 a[236:239], v237 offset:34816\n"
      "v_mfma_f32_16x16x32_bf16 v[96:99], v[148:151], a[96:99], v[96:99]\n"
      "global_load_dwordx4 v[128:131], v240, s[10:11] offset:0 sc1\n"
      "s_lshr_b32 s43, s42, 5\n"
      "s_lshl2_add_u32 s44, s36, s33\n"
      "s_cmp_eq_u32 s43, s44\n"
      "s_cbranch_scc0 .Lp_e10_%=\n"
      "s_lshl_b32 s43, s42, 10\n"
      "v_add_u32 v208, s43, v242\n"
      "global_load_dwordx4 v[216:219], v208, s[16:17]\n"
      "global_load_dwordx4 v[220:223], v242, s[24:25]\n"
      ".Lp_e10_%=:\n"
      "v_mfma_f32_16x16x32_bf16 v[100:103], v[148:151], a[100:103], v[100:103]\n"
      "v_mfma_f32_16x16x32_bf16 v[104:107], v[148:151], a[104:107], v[104:107]\n"
      "v_mfma_f32_16x16x32_bf16 v[108:111], v[148:151], a[108:111], v[108:111]\n"
      "v_mfma_f32_16x16x32_bf16 v[112:115], v[148:151], a[112:115], v[112:115]\n"
      "global_load_dwordx4 v[132:135], v240, s[72:73] offset:0 sc1\n"
      "v_mfma_f32_16x16x32_bf16 v[116:119], v[148:151], a[116:119], v[116:119]\n"
      "v_mfma_f32_16x16x32_bf16 v[120:123], v[148:151], a[120:123], v[120:123]\n"
      "v_mfma_f32_16x16x32_bf16 v[124:127], v[148:151], a[124:127], v[124:127]\n"
      "s_waitcnt vmcnt(18)\n"
      "v_mfma_f32_16x16x32_bf16 v[96:99], v[152:155], a[128:131], v[96:99]\n"
      "global_load_dwordx4 v[136:139], v241, s[10:11] offset:0 sc1\n"
      "v_mfma_f32_16x16x32_bf16 v[100:103], v[152:155], a[132:135], v[100:103]\n"
      "ds_read_b128 a[208:211], v236 offset:49152\n"
      "ds_read_b128 a[212:215], v236 offset:51200\n"
      "v_mfma_f32_16x16x32_bf16 v[104:107], v[152:155], a[136:139], v[104:107]\n"
      "ds_read_b128 a[240:243], v237 offset:49152\n"
      "ds_read_b128 a[244:247], v237 offset:51200\n"
      "v_mfma_f32_16x16x32_bf16 v[108:111], v[152:155], a[140:143], v[108:111]\n"
      "v_mfma_f32_16x16x32_bf16 v[112:115], v[152:155], a[144:147], v[112:115]\n"
      "global_load_dwordx4 v[140:143], v241, s[72:73] offset:0 sc1\n"
      "v_mfma_f32_16x16x32_bf16 v[116:119], v[152:155], a[148:151], v[116:119]\n"
      "v_mfma_f32_16x16x32_bf16 v[120:123], v[152:155], a[152:155], v[120:123]\n"
      "v_mfma_f32_16x16x32_bf16 v[124:127], v[152:155], a[156:159], v[124:127]\n"
      "s_waitcnt lgkmcnt(11)\n"
      "v_mfma_f32_16x16x32_bf16 v[0:3], v[156:159], a[192:195], v[0:3]\n"
      "global_load_dwordx4 v[144:147], v240, s[14:15] offset:0 sc1\n"
      "s_waitcnt lgkmcnt(10)\n"
      "v_mfma_f32_16x16x32_bf16 v[4:7], v[156:159], a[196:199], v[4:7]\n"
      "s_waitcnt lgkmcnt(7)\n"
      "v_mfma_f32_16x16x32_bf16 v[8:11], v[156:159], a[200:203], v[8:11]\n"
      "s_waitcnt lgkmcnt(6)\n"
      "v_mfma_f32_16x16x32_bf16 v[12:15], v[156:159], a[204:207], v[12:15]\n"
      "s_waitcnt lgkmcnt(3)\n"
      "v_mfma_f32_16x16x32_bf16 v[16:19], v[156:159], a[208:211], v[16:19]\n"
      "global_load_dwordx4 v[148:151], v240, s[74:75] offset:0 sc1\n"
      "s_waitcnt lgkmcnt(2)\n"
      "v_mfma_f32_16x16x32_bf16 v[20:23], v[156:159], a[212:215], v[20:23]\n"
      "s_waitcnt vmcnt(16)\n"
      "ds_read_b128 a[216:219], v248 offset:0\n"
      "ds_read_b128 a[220:223], v248 offset:2048\n"
      "ds_read_b128 a[248:251], v249 offset:0\n"
      "ds_read_b128 a[252:255], v249 offset:2048\n"
      "s_waitcnt lgkmcnt(3)\n"
      "v_mfma_f32_16x16x32_bf16 v[24:27], v[156:159], a[216:219], v[24:27]\n"
      "s_sub_u32 s48, s33, 1\n"
      "s_and_b32 s48, s48, 3\n"
      "s_lshl_b32 s48, s48, 14\n"
      "s_waitcnt lgkmcnt(2)\n"
      "v_mfma_f32_16x16x32_bf16 v[28:31], v[156:159], a[220:223], v[28:31]\n"
      "v_lshl_add_u32 v244, v243, 2, s48\n"
      "s_sub_u32 s48, s33, 2\n"
      "v_mfma_f32_16x16x32_bf16 v[0:3], v[160:163], a[224:227], v[0:3]\n"
      "s_and_b32 s48, s48, 3\n"
      "global_load_dwordx4 v[152:155], v241, s[14:15] offset:0 sc1\n"
      "s_lshl_b32 s48, s48, 14\n"
      "v_mfma_f32_16x16x32_bf16 v[4:7], v[160:163], a[228:231], v[4:7]\n"
      "s_mov_b32 m0, s41\n"
      "v_lshl_add_u32 v245, v243, 2, s48\n"
      "s_sub_u32 s48, s33, 3\n"
      "v_mfma_f32_16x16x32_bf16 v[8:11], v[160:163], a[232:235], v[8:11]\n"
      "s_and_b32 s48, s48, 3\n"
      "s_lshl_b32 s48, s48, 14\n"
      "v_lshl_add_u32 v246, v243, 2, s48\n"
      "v_mfma_f32_16x16x32_bf16 v[12:15], v[160:163], a[236:239], v[12:15]\n"
      "v_bfe_u32 v248, v243, 1, 3\n"
      "v_lshrrev_b32 v249, 4, v243\n"
      "v_mfma_f32_16x16x32_bf16 v[16:19], v[160:163], a[240:243], v[16:19]\n"
      "global_load_dwordx4 v[156:159], v241, s[74:75] offset:0 sc1\n"
      "v_lshlrev_b32 v249, 1, v249\n"
      "v_mfma_f32_16x16x32_bf16 v[20:23], v[160:163], a[244:247], v[20:23]\n"
      "v_sub_u32 v248, v248, v249\n"
      "v_and_b32 v249, 16, v242\n"
      "s_waitcnt lgkmcnt(1)\n"
      "v_mfma_f32_16x16x32_bf16 v[24:27], v[160:163], a[248:251], v[24:27]\n"
      "v_mov_b32 v254, 0x3f80\n"
      "v_lshlrev_b32 v249, v249, v254\n"
      "s_waitcnt lgkmcnt(0)\n"
      "v_mfma_f32_16x16x32_bf16 v[28:31], v[160:163], a[252:255], v[28:31]\n"
      "v_cmp_eq_u32 vcc, 0, v248\n"
      "s_nop 1\n"
      "v_cndmask_b32 v250, 0, v249, vcc\n"
      "v_mfma_f32_16x16x32_bf16 v[32:35], v[164:167], a[192:195], v[32:35]\n"
      "global_load_dwordx4 v[160:163], v240, s[10:11] offset:1024 sc1\n"
      "v_cvt_pk_bf16_f32 v8, v8, v9\n"
      "v_mfma_f32_16x16x32_bf16 v[36:39], v[164:167], a[196:199], v[36:39]\n"
      "v_cmp_eq_u32 vcc, 1, v248\n"
      "s_nop 1\n"
      "v_cndmask_b32 v251, 0, v249, vcc\n"
      "v_mfma_f32_16x16x32_bf16 v[40:43], v[164:167], a[200:203], v[40:43]\n"
      "v_mov_b32 v252, v250\n"
      "v_mov_b32 v253, v251\n"
      "v_mfma_f32_16x16x32_bf16 v[44:47], v[164:167], a[204:207], v[44:47]\n"
      "v_mov_b32 v248, 0\n"
      "v_mov_b32 v249, 0\n"
      "v_mfma_f32_16x16x32_bf16 v[48:51], v[164:167], a[208:211], v[48:51]\n"
      "v_mov_b32 v254, 0\n"
      "v_mov_b32 v255, 0\n"
      "v_mfma_f32_16x16x32_bf16 v[52:55], v[164:167], a[212:215], v[52:55]\n"
      "s_add_u32 s48, s34, 1\n"
      "s_and_b32 s48, s48, 3\n"
      "s_mul_i32 s48, s48, 36864\n"
      "v_mfma_f32_16x16x32_bf16 v[56:59], v[164:167], a[216:219], v[56:59]\n"
      "s_add_u32 s48, s48, 0\n"
      "s_add_u32 s52, s60, s48\n"
      "s_addc_u32 s53, s61, 0\n"
      "v_mfma_f32_16x16x32_bf16 v[60:63], v[164:167], a[220:223], v[60:63]\n"
      "s_waitcnt vmcnt(13)\n"
      "global_load_dwordx4 v[164:167], v240, s[72:73] offset:1024 sc1\n"
      "v_cvt_pk_bf16_f32 v9, v10, v11\n"
      "v_mfma_f32_16x16x32_bf16 v[32:35], v[168:171], a[224:227], v[32:35]\n"
      "ds_write_addtid_b32 v8 offset:0\n"
      "v_cvt_pk_bf16_f32 v10, v12, v13\n"
      "v_mfma_f32_16x16x32_bf16 v[36:39], v[168:171], a[228:231], v[36:39]\n"
      "ds_write_addtid_b32 v9 offset:256\n"
      "v_cvt_pk_bf16_f32 v11, v14, v15\n"
      "v_mfma_f32_16x16x32_bf16 v[40:43], v[168:171], a[232:235], v[40:43]\n"
      "ds_write_addtid_b32 v10 offset:512\n"
      "v_cvt_pk_bf16_f32 v12, v16, v17\n"
      "v_mfma_f32_16x16x32_bf16 v[44:47], v[168:171], a[236:239], v[44:47]\n"
      "ds_write_addtid_b32 v11 offset:768\n"
      "v_cvt_pk_bf16_f32 v13, v18, v19\n"
      "v_mfma_f32_16x16x32_bf16 v[48:51], v[168:171], a[240:243], v[48:51]\n"
      "ds_write_addtid_b32 v12 offset:1024\n"
      "v_cvt_pk_bf16_f32 v14, v20, v21\n"
      "v_mfma_f32_16x16x32_bf16 v[52:55], v[168:171], a[244:247], v[52:55]\n"
      "ds_write_addtid_b32 v13 offset:1280\n"
      "v_cvt_pk_bf16_f32 v15, v22, v23\n"
      "v_mfma_f32_16x16x32_bf16 v[56:59], v[168:171], a[248:251], v[56:59]\n"
      "ds_write_addtid_b32 v14 offset:1536\n"
      "v_cvt_pk_bf16_f32 v16, v24, v25\n"
      "v_mfma_f32_16x16x32_bf16 v[60:63], v[168:171], a[252:255], v[60:63]\n"
      "global_load_dwordx4 v[168:171], v241, s[10:11] offset:1024 sc1\n"
      "ds_write_addtid_b32 v15 offset:1792\n"
      "v_mfma_f32_16x16x32_bf16 v[64:67], v[172:175], a[192:195], v[64:67]\n"
      "v_cvt_pk_bf16_f32 v17, v26, v27\n"
      "ds_write_addtid_b32 v16 offset:2048\n"
      "v_mfma_f32_16x16x32_bf16 v[68:71], v[172:175], a[196:199], v[68:71]\n"
      "v_cvt_pk_bf16_f32 v18, v28, v29\n"
      "ds_write_addtid_b32 v17 offset:2304\n"
      "v_mfma_f32_16x16x32_bf16 v[72:75], v[172:175], a[200:203], v[72:75]\n"
      "v_cvt_pk_bf16_f32 v19, v30, v31\n"
      "ds_write_addtid_b32 v18 offset:2560\n"
      "v_mfma_f32_16x16x32_bf16 v[76:79], v[172:175], a[204:207], v[76:79]\n"
      "ds_write_addtid_b32 v19 offset:2816\n"
      "v_mfma_f32_16x16x32_bf16 v[80:83], v[172:175], a[208:211], v[80:83]\n"
      "v_mfma_f32_16x16x32_bf16 v[84:87], v[172:175], a[212:215], v[84:87]\n"
      "v_mfma_f32_16x16x32_bf16 v[88:91], v[172:175], a[216:219], v[88:91]\n"
      "s_waitcnt lgkmcnt(0)\n"
      "s_barrier\n"
      "v_mfma_f32_16x16x32_bf16 v[92:95], v[172:175], a[220:223], v[92:95]\n"
      "s_waitcnt vmcnt(13)\n"
      "global_load_dwordx4 v[172:175], v241, s[72:73] offset:1024 sc1\n"
      "ds_read2st64_b32 v[20:21], v244 offset0:0 offset1:1\n"
      "v_mfma_f32_16x16x32_bf16 v[64:67], v[176:179], a[224:227], v[64:67]\n"
      "ds_read2st64_b32 v[22:23], v245 offset0:4 offset1:5\n"
      "ds_read2st64_b32 v[24:25], v244 offset0:2 offset1:3\n"
      "v_mfma_f32_16x16x32_bf16 v[68:71], v[176:179], a[228:231], v[68:71]\n"
      "ds_read2st64_b32 v[26:27], v245 offset0:6 offset1:7\n"
      "ds_read2st64_b32 v[28:29], v246 offset0:8 offset1:9\n"
      "v_mfma_f32_16x16x32_bf16 v[72:75], v[176:179], a[232:235], v[72:75]\n"
      "ds_read2st64_b32 v[30:31], v246 offset0:10 offset1:11\n"
      "v_cvt_pk_bf16_f32 v40, v40, v41\n"
      "v_mfma_f32_16x16x32_bf16 v[76:79], v[176:179], a[236:239], v[76:79]\n"
      "v_cvt_pk_bf16_f32 v41, v42, v43\n"
      "ds_write_addtid_b32 v40 offset:3072\n"
      "v_mfma_f32_16x16x32_bf16 v[80:83], v[176:179], a[240:243], v[80:83]\n"
      "s_lshr_b32 s43, s42, 5\n"
      "s_lshl2_add_u32 s44, s36, s33\n"
      "s_cmp_eq_u32 s43, s44\n"
      "s_cbranch_scc0 .Lp_e11_%=\n"
      "s_ashr_i32 s43, s40, 31\n"
      "s_not_b32 s43, s43\n"
      "s_or_b32 s43, s43, 0x80000000\n"
      "s_xor_b32 s43, s43, s40\n"
      "s_and_b32 s43, s43, 0xffff0000\n"
      "s_waitcnt vmcnt(11)\n"
      "v_lshlrev_b32 v208, 16, v216\n"
      "s_waitcnt vmcnt(11)\n"
      "v_lshlrev_b32 v209, 16, v220\n"
      "v_and_b32 v216, 0xffff0000, v216\n"
      "v_and_b32 v220, 0xffff0000, v220\n"
      "v_mul_f32 v208, v208, v209\n"
      "v_mul_f32 v210, v216, v220\n"
      "v_cvt_pk_bf16_f32 v208, v208, v210\n"
      "v_lshlrev_b32 v209, 16, v208\n"
      "v_and_b32 v208, 0xffff0000, v208\n"
      "v_add_f32 v211, v208, v209\n"
      "v_lshlrev_b32 v208, 16, v217\n"
      "v_lshlrev_b32 v209, 16, v221\n"
      "v_and_b32 v217, 0xffff0000, v217\n"
      "v_and_b32 v221, 0xffff0000, v221\n"
      "v_mul_f32 v208, v208, v209\n"
      "v_mul_f32 v210, v217, v221\n"
      "v_cvt_pk_bf16_f32 v208, v208, v210\n"
      "v_lshlrev_b32 v209, 16, v208\n"
      "v_and_b32 v208, 0xffff0000, v208\n"
      "v_add_f32 v208, v208, v209\n"
      "v_add_f32 v211, v211, v208\n"
      "v_lshlrev_b32 v208, 16, v218\n"
      "v_lshlrev_b32 v209, 16, v222\n"
      "v_and_b32 v218, 0xffff0000, v218\n"
      "v_and_b32 v222, 0xffff0000, v222\n"
      "v_mul_f32 v208, v208, v209\n"
      "v_mul_f32 v210, v218, v222\n"
      "v_cvt_pk_bf16_f32 v208, v208, v210\n"
      "v_lshlrev_b32 v209, 16, v208\n"
      "v_and_b32 v208, 0xffff0000, v208\n"
      "v_add_f32 v208, v208, v209\n"
      "v_add_f32 v211, v211, v208\n"
      "v_lshlrev_b32 v208, 16, v219\n"
      "v_lshlrev_b32 v209, 16, v223\n"
      "v_and_b32 v219, 0xffff0000, v219\n"
      "v_and_b32 v223, 0xffff0000, v223\n"
      "v_mul_f32 v208, v208, v209\n"
      "v_mul_f32 v210, v219, v223\n"
      "v_cvt_pk_bf16_f32 v208, v208, v210\n"
      "v_lshlrev_b32 v209, 16, v208\n"
      "v_and_b32 v208, 0xffff0000, v208\n"
      "v_add_f32 v208, v208, v209\n"
      "v_add_f32 v211, v211, v208\n"
      "s_nop 1\n"
      "v_add_f32_dpp v211, v211, v211 quad_perm:[1,0,3,2] row_mask:0xf bank_mask:0xf\n"
      "s_nop 1\n"
      "v_add_f32_dpp v211, v211, v211 quad_perm:[2,3,0,1] row_mask:0xf bank_mask:0xf\n"
      "s_nop 1\n"
      "v_add_f32_dpp v211, v211, v211 row_half_mirror row_mask:0xf bank_mask:0xf\n"
      "s_nop 1\n"
      "v_add_f32_dpp v211, v211, v211 row_mirror row_mask:0xf bank_mask:0xf\n"
      "v_mov_b32 v208, v211\n"
      "s_nop 1\n"
      "v_permlane16_swap_b32 v211, v208\n"
      "v_add_f32 v211, v211, v208\n"
      "v_mov_b32 v208, v211\n"
      "s_nop 1\n"
      "v_permlane32_swap_b32 v211, v208\n"
      "v_add_f32 v211, v211, v208\n"
      "v_cvt_pk_bf16_f32 v211, v211, v211\n"
      "v_and_b32 v211, 0xffff0000, v211\n"
      "v_writelane_b32 v211, s43, 1\n"
      "v_mul_f32 v211, 0xbfb8aa3b, v211\n"
      "v_exp_f32 v211, v211\n"
      "s_nop 0\n"
      "v_add_f32 v211, 1.0, v211\n"
      "v_rcp_f32 v211, v211\n"
      "s_nop 0\n"
      "v_cvt_pk_bf16_f32 v211, v211, v211\n"
      "v_and_b32 v211, 0xffff0000, v211\n"
      "s_nop 0\n"
      "v_readfirstlane_b32 s39, v211\n"
      "v_readlane_b32 s84, v211, 1\n"
      ".Lp_e11_%=:\n"
      "v_mfma_f32_16x16x32_bf16 v[84:87], v[176:179], a[244:247], v[84:87]\n"
      "s_lshl2_add_u32 s54, s36, s33\n"
      "v_cvt_pk_bf16_f32 v42, v44, v45\n"
      "s_lshl_b32 s55, s54, 15\n"
      "v_mfma_f32_16x16x32_bf16 v[88:91], v[176:179], a[248:251], v[88:91]\n"
      "s_lshl2_add_u32 s56, s35, s34\n"
      "ds_write_addtid_b32 v41 offset:3328\n"
      "v_cvt_pk_bf16_f32 v43, v46, v47\n"
      "v_mfma_f32_16x16x32_bf16 v[92:95], v[176:179], a[252:255], v[92:95]\n"
      "s_waitcnt lgkmcnt(2)\n"
      "global_load_dwordx4 v[176:179], v240, s[14:15] offset:1024 sc1\n"
      "s_lshl_b32 s56, s56, 6\n"
      "v_mfma_f32_16x16x32_bf16 v[96:99], v[180:183], a[160:163], v[96:99]\n"
      "s_add_u32 s55, s55, s56\n"
      "v_mfma_f32_16x16x32_bf16 v[0:3], v[250:253], v[20:23], v[0:3]\n"
      "v_and_b32 v209, 15, v243\n"
      "v_mfma_f32_16x16x32_bf16 v[100:103], v[180:183], a[164:167], v[100:103]\n"
      "v_mfma_f32_16x16x32_bf16 v[4:7], v[250:253], v[24:27], v[4:7]\n"
      "v_and_b32 v208, 48, v243\n"
      "v_mfma_f32_16x16x32_bf16 v[104:107], v[180:183], a[168:171], v[104:107]\n"
      "v_mfma_f32_16x16x32_bf16 v[0:3], v[252:255], v[28:31], v[0:3]\n"
      "v_lshl_or_b32 v208, v209, 10, v208\n"
      "v_mfma_f32_16x16x32_bf16 v[108:111], v[180:183], a[172:175], v[108:111]\n"
      "v_mfma_f32_16x16x32_bf16 v[4:7], v[248:251], v[28:31], v[4:7]\n"
      "v_mov_b32 v11, s37\n"
      "v_mfma_f32_16x16x32_bf16 v[112:115], v[180:183], a[176:179], v[112:115]\n"
      "ds_write_addtid_b32 v42 offset:3584\n"
      "v_cvt_pk_bf16_f32 v44, v48, v49\n"
      "v_mfma_f32_16x16x32_bf16 v[116:119], v[180:183], a[180:183], v[116:119]\n"
      "ds_write_addtid_b32 v43 offset:3840\n"
      "v_cvt_pk_bf16_f32 v8, v0, v1\n"
      "v_mfma_f32_16x16x32_bf16 v[120:123], v[180:183], a[184:187], v[120:123]\n"
      "v_cvt_pk_bf16_f32 v9, v2, v3\n"
      "v_cvt_pk_bf16_f32 v10, v4, v5\n"
      "v_mfma_f32_16x16x32_bf16 v[124:127], v[180:183], a[188:191], v[124:127]\n"
      "s_waitcnt vmcnt(13)\n"
      "global_load_dwordx4 v[180:183], v240, s[74:75] offset:1024 sc1\n"
      "v_cvt_pk_bf16_f32 v247, v6, v7\n"
      "v_mfma_f32_16x16x32_bf16 v[96:99], v[184:187], a[192:195], v[96:99]\n"
      "global_store_dwordx4 v242, v[8:11], s[52:53] offset:0 sc1\n"
      "v_cvt_pk_bf16_f32 v45, v50, v51\n"
      "v_mfma_f32_16x16x32_bf16 v[100:103], v[184:187], a[196:199], v[100:103]\n"
      "ds_write_addtid_b32 v44 offset:4096\n"
      "v_cvt_pk_bf16_f32 v46, v52, v53\n"
      "v_mfma_f32_16x16x32_bf16 v[104:107], v[184:187], a[200:203], v[104:107]\n"
      "ds_write_addtid_b32 v45 offset:4352\n"
      "v_cvt_pk_bf16_f32 v47, v54, v55\n"
      "v_mfma_f32_16x16x32_bf16 v[108:111], v[184:187], a[204:207], v[108:111]\n"
      "ds_write_addtid_b32 v46 offset:4608\n"
      "v_cvt_pk_bf16_f32 v48, v56, v57\n"
      "v_mfma_f32_16x16x32_bf16 v[112:115], v[184:187], a[208:211], v[112:115]\n"
      "ds_write_addtid_b32 v47 offset:4864\n"
      "v_cvt_pk_bf16_f32 v49, v58, v59\n"
      "v_mfma_f32_16x16x32_bf16 v[116:119], v[184:187], a[212:215], v[116:119]\n"
      "ds_write_addtid_b32 v48 offset:5120\n"
      "v_cvt_pk_bf16_f32 v50, v60, v61\n"
      "v_mfma_f32_16x16x32_bf16 v[120:123], v[184:187], a[216:219], v[120:123]\n"
      "ds_write_addtid_b32 v49 offset:5376\n"
      "v_cvt_pk_bf16_f32 v51, v62, v63\n"
      "v_mfma_f32_16x16x32_bf16 v[124:127], v[184:187], a[220:223], v[124:127]\n"
      "global_load_dwordx4 v[184:187], v241, s[14:15] offset:1024 sc1\n"
      "v_add_u32 v208, s55, v208\n"
      "v_mfma_f32_16x16x32_bf16 v[96:99], v[188:191], a[224:227], v[96:99]\n"
      "ds_write_addtid_b32 v50 offset:5632\n"
      "v_add_u32 v210, 16384, v208\n"
      "v_mfma_f32_16x16x32_bf16 v[100:103], v[188:191], a[228:231], v[100:103]\n"
      "ds_write_addtid_b32 v51 offset:5888\n"
      "v_cvt_pk_bf16_f32 v72, v72, v73\n"
      "v_mfma_f32_16x16x32_bf16 v[104:107], v[188:191], a[232:235], v[104:107]\n"
      "global_load_dwordx4 v[216:219], v208, s[16:17]\n"
      "v_cvt_pk_bf16_f32 v73, v74, v75\n"
      "v_mfma_f32_16x16x32_bf16 v[108:111], v[188:191], a[236:239], v[108:111]\n"
      "ds_write_addtid_b32 v72 offset:6144\n"
      "v_cvt_pk_bf16_f32 v74, v76, v77\n"
      "v_mfma_f32_16x16x32_bf16 v[112:115], v[188:191], a[240:243], v[112:115]\n"
      "ds_write_addtid_b32 v73 offset:6400\n"
      "v_cvt_pk_bf16_f32 v75, v78, v79\n"
      "v_mfma_f32_16x16x32_bf16 v[116:119], v[188:191], a[244:247], v[116:119]\n"
      "ds_write_addtid_b32 v74 offset:6656\n"
      "v_cvt_pk_bf16_f32 v76, v80, v81\n"
      "v_mfma_f32_16x16x32_bf16 v[120:123], v[188:191], a[248:251], v[120:123]\n"
      "ds_write_addtid_b32 v75 offset:6912\n"
      "v_cvt_pk_bf16_f32 v77, v82, v83\n"
      "v_mfma_f32_16x16x32_bf16 v[124:127], v[188:191], a[252:255], v[124:127]\n"
      "s_waitcnt vmcnt(15)\n"
      "global_load_dwordx4 v[188:191], v241, s[74:75] offset:1024 sc1\n"
      "v_mfma_f32_16x16x32_bf16 v[0:3], v[128:131], a[0:3], 0\n"
      "ds_write_addtid_b32 v76 offset:7168\n"
      "v_cvt_pk_bf16_f32 v78, v84, v85\n"
      "v_mfma_f32_16x16x32_bf16 v[4:7], v[128:131], a[4:7], 0\n"
      "ds_write_addtid_b32 v77 offset:7424\n"
      "v_cvt_pk_bf16_f32 v79, v86, v87\n"
      "v_mfma_f32_16x16x32_bf16 v[8:11], v[128:131], a[8:11], 0\n"
      "ds_write_addtid_b32 v78 offset:7680\n"
      "v_cvt_pk_bf16_f32 v80, v88, v89\n"
      "v_mfma_f32_16x16x32_bf16 v[12:15], v[128:131], a[12:15], 0\n"
      "ds_write_addtid_b32 v79 offset:7936\n"
      "v_cvt_pk_bf16_f32 v81, v90, v91\n"
      "v_mfma_f32_16x16x32_bf16 v[16:19], v[128:131], a[16:19], 0\n"
      "global_load_dwordx4 v[224:227], v200, s[58:59] offset:0 sc1\n"
      "v_mfma_f32_16x16x32_bf16 v[20:23], v[128:131], a[20:23], 0\n"
      "global_load_dwordx4 v[228:231], v200, s[58:59] offset:1024 sc1\n"
      "v_mfma_f32_16x16x32_bf16 v[24:27], v[128:131], a[24:27], 0\n"
      "global_load_dwordx4 v[232:235], v200, s[58:59] offset:2048 sc1\n"
      "v_mfma_f32_16x16x32_bf16 v[28:31], v[128:131], a[28:31], 0\n"
      "global_load_dwordx4 v[128:131], v240, s[10:11] offset:2048 sc1\n"
      "v_mfma_f32_16x16x32_bf16 v[0:3], v[132:135], a[32:35], v[0:3]\n"
      "global_load_dwordx4 v[236:239], v200, s[58:59] offset:3072 sc1\n"
      "v_mfma_f32_16x16x32_bf16 v[4:7], v[132:135], a[36:39], v[4:7]\n"
      "ds_write_addtid_b32 v80 offset:8192\n"
      "v_cvt_pk_bf16_f32 v82, v92, v93\n"
      "v_mfma_f32_16x16x32_bf16 v[8:11], v[132:135], a[40:43], v[8:11]\n"
      "ds_write_addtid_b32 v81 offset:8448\n"
      "v_cvt_pk_bf16_f32 v83, v94, v95\n"
      "v_mfma_f32_16x16x32_bf16 v[12:15], v[132:135], a[44:47], v[12:15]\n"
      "ds_write_addtid_b32 v82 offset:8704\n"
      "v_mfma_f32_16x16x32_bf16 v[16:19], v[132:135], a[48:51], v[16:19]\n"
      "ds_write_addtid_b32 v83 offset:8960\n"
      "v_cvt_pk_bf16_f32 v104, v104, v105\n"
      "v_mfma_f32_16x16x32_bf16 v[20:23], v[132:135], a[52:55], v[20:23]\n"
      "global_load_dwordx4 v[220:223], v210, s[16:17]\n"
      "v_cvt_pk_bf16_f32 v105, v106, v107\n"
      "v_mfma_f32_16x16x32_bf16 v[24:27], v[132:135], a[56:59], v[24:27]\n"
      "s_lshr_b32 s55, s34, 1\n"
      "ds_write_addtid_b32 v104 offset:9216\n"
      "v_cvt_pk_bf16_f32 v106, v108, v109\n"
      "v_mfma_f32_16x16x32_bf16 v[28:31], v[132:135], a[60:63], v[28:31]\n"
      "s_waitcnt vmcnt(20)\n"
      "global_load_dwordx4 v[132:135], v240, s[72:73] offset:2048 sc1\n"
      "s_lshl2_add_u32 s55, s55, s35\n"
      "v_mfma_f32_16x16x32_bf16 v[0:3], v[136:139], a[64:67], v[0:3]\n"
      "s_lshl_b32 s55, s55, 15\n"
      "ds_write_addtid_b32 v105 offset:9472\n"
      "v_cvt_pk_bf16_f32 v107, v110, v111\n"
      "v_mfma_f32_16x16x32_bf16 v[4:7], v[136:139], a[68:71], v[4:7]\n"
      "s_lshl_b32 s54, s54, 9\n"
      "ds_write_addtid_b32 v106 offset:9728\n"
      "v_cvt_pk_bf16_f32 v108, v112, v113\n"
      "v_mfma_f32_16x16x32_bf16 v[8:11], v[136:139], a[72:75], v[8:11]\n"
      "s_add_u32 s54, s54, s55\n"
      "ds_write_addtid_b32 v107 offset:9984\n"
      "v_cvt_pk_bf16_f32 v109, v114, v115\n"
      "v_mfma_f32_16x16x32_bf16 v[12:15], v[136:139], a[76:79], v[12:15]\n"
      "s_add_u32 s54, s54, 786432\n"
      "ds_write_addtid_b32 v108 offset:10240\n"
      "v_cvt_pk_bf16_f32 v110, v116, v117\n"
      "v_mfma_f32_16x16x32_bf16 v[16:19], v[136:139], a[80:83], v[16:19]\n"
      "ds_write_addtid_b32 v109 offset:10496\n"
      "v_cvt_pk_bf16_f32 v111, v118, v119\n"
      "v_mfma_f32_16x16x32_bf16 v[20:23], v[136:139], a[84:87], v[20:23]\n"
      "ds_write_addtid_b32 v110 offset:10752\n"
      "v_cvt_pk_bf16_f32 v112, v120, v121\n"
      "v_mfma_f32_16x16x32_bf16 v[24:27], v[136:139], a[88:91], v[24:27]\n"
      "ds_write_addtid_b32 v111 offset:11008\n"
      "v_cvt_pk_bf16_f32 v113, v122, v123\n"
      "v_mfma_f32_16x16x32_bf16 v[28:31], v[136:139], a[92:95], v[28:31]\n"
      "global_load_dwordx4 v[136:139], v241, s[10:11] offset:2048 sc1\n"
      "v_lshl_add_u32 v209, v209, 4, s54\n"
      "v_mfma_f32_16x16x32_bf16 v[0:3], v[140:143], a[96:99], v[0:3]\n"
      "ds_write_addtid_b32 v112 offset:11264\n"
      "v_cvt_pk_bf16_f32 v114, v124, v125\n"
      "v_mfma_f32_16x16x32_bf16 v[4:7], v[140:143], a[100:103], v[4:7]\n"
      "ds_write_addtid_b32 v113 offset:11520\n"
      "v_cvt_pk_bf16_f32 v115, v126, v127\n"
      "v_mfma_f32_16x16x32_bf16 v[8:11], v[140:143], a[104:107], v[8:11]\n"
      "ds_write_addtid_b32 v114 offset:11776\n"
      "v_mfma_f32_16x16x32_bf16 v[12:15], v[140:143], a[108:111], v[12:15]\n"
      "ds_write_addtid_b32 v115 offset:12032\n"
      "v_mfma_f32_16x16x32_bf16 v[16:19], v[140:143], a[112:115], v[16:19]\n"
      "global_load_dwordx2 v[204:205], v209, s[58:59] offset:0 sc1\n"
      "v_mfma_f32_16x16x32_bf16 v[20:23], v[140:143], a[116:119], v[20:23]\n"
      "global_load_dwordx2 v[206:207], v209, s[58:59] offset:256 sc1\n"
      "s_add_u32 s68, s20, 16384\n"
      "s_addc_u32 s69, s21, 0\n"
      "v_mfma_f32_16x16x32_bf16 v[24:27], v[140:143], a[120:123], v[24:27]\n"
      "s_waitcnt lgkmcnt(0)\n"
      "s_barrier\n"
      "v_mfma_f32_16x16x32_bf16 v[28:31], v[140:143], a[124:127], v[28:31]\n"
      "s_waitcnt vmcnt(22)\n"
      "global_load_dwordx4 v[140:143], v241, s[72:73] offset:2048 sc1\n"
      "v_lshrrev_b32 v208, 10, v208\n"
      "v_mfma_f32_16x16x32_bf16 v[0:3], v[144:147], a[128:131], v[0:3]\n"
      "ds_read2st64_b32 v[52:53], v244 offset0:12 offset1:13\n"
      "v_subrev_u32 v208, s42, v208\n"
      "v_mfma_f32_16x16x32_bf16 v[4:7], v[144:147], a[132:135], v[4:7]\n"
      "ds_read2st64_b32 v[54:55], v245 offset0:16 offset1:17\n"
      "v_mfma_f32_16x16x32_bf16 v[8:11], v[144:147], a[136:139], v[8:11]\n"
      "ds_read2st64_b32 v[56:57], v244 offset0:14 offset1:15\n"
      "v_mfma_f32_16x16x32_bf16 v[12:15], v[144:147], a[140:143], v[12:15]\n"
      "ds_read2st64_b32 v[58:59], v245 offset0:18 offset1:19\n"
      "v_mfma_f32_16x16x32_bf16 v[16:19], v[144:147], a[144:147], v[16:19]\n"
      "ds_read2st64_b32 v[60:61], v246 offset0:20 offset1:21\n"
      "v_mfma_f32_16x16x32_bf16 v[20:23], v[144:147], a[148:151], v[20:23]\n"
      "ds_read2st64_b32 v[62:63], v246 offset0:22 offset1:23\n"
      "v_mfma_f32_16x16x32_bf16 v[24:27], v[144:147], a[152:155], v[24:27]\n"
      "ds_read2st64_b32 v[84:85], v244 offset0:24 offset1:25\n"
      "v_mfma_f32_16x16x32_bf16 v[28:31], v[144:147], a[156:159], v[28:31]\n"
      "global_load_dwordx4 v[144:147], v240, s[14:15] offset:2048 sc1\n"
      "v_mfma_f32_16x16x32_bf16 v[0:3], v[148:151], a[160:163], v[0:3]\n"
      "ds_read2st64_b32 v[86:87], v245 offset0:28 offset1:29\n"
      "v_mfma_f32_16x16x32_bf16 v[4:7], v[148:151], a[164:167], v[4:7]\n"
      "ds_read2st64_b32 v[88:89], v244 offset0:26 offset1:27\n"
      "v_mfma_f32_16x16x32_bf16 v[8:11], v[148:151], a[168:171], v[8:11]\n"
      "s_waitcnt lgkmcnt(3)\n"
      "v_mfma_f32_16x16x32_bf16 v[32:35], v[250:253], v[52:55], v[32:35]\n"
      "v_mfma_f32_16x16x32_bf16 v[12:15], v[148:151], a[172:175], v[12:15]\n"
      "v_mfma_f32_16x16x32_bf16 v[36:39], v[250:253], v[56:59], v[36:39]\n"
      "v_mfma_f32_16x16x32_bf16 v[16:19], v[148:151], a[176:179], v[16:19]\n"
      "v_mfma_f32_16x16x32_bf16 v[32:35], v[252:255], v[60:63], v[32:35]\n"
      "v_mfma_f32_16x16x32_bf16 v[20:23], v[148:151], a[180:183], v[20:23]\n"
      "v_mfma_f32_16x16x32_bf16 v[36:39], v[248:251], v[60:63], v[36:39]\n"
      "v_mov_b32 v43, s37\n"
      "v_mfma_f32_16x16x32_bf16 v[24:27], v[148:151], a[184:187], v[24:27]\n"
      "ds_read2st64_b32 v[90:91], v245 offset0:30 offset1:31\n"
      "v_mov_b32 v44, v247\n"
      "v_mfma_f32_16x16x32_bf16 v[28:31], v[148:151], a[188:191], v[28:31]\n"
      "s_waitcnt vmcnt(22)\n"
      "global_load_dwordx4 v[148:151], v240, s[74:75] offset:2048 sc1\n"
      "v_mov_b32 v47, s37\n"
      "v_mfma_f32_16x16x32_bf16 v[0:3], v[152:155], a[192:195], v[0:3]\n"
      "ds_read2st64_b32 v[92:93], v246 offset0:32 offset1:33\n"
      "v_mfma_f32_16x16x32_bf16 v[4:7], v[152:155], a[196:199], v[4:7]\n"
      "ds_read2st64_b32 v[94:95], v246 offset0:34 offset1:35\n"
      "v_cvt_pk_bf16_f32 v40, v32, v33\n"
      "v_mfma_f32_16x16x32_bf16 v[8:11], v[152:155], a[200:203], v[8:11]\n"
      "ds_read2st64_b32 v[116:117], v244 offset0:36 offset1:37\n"
      "v_cvt_pk_bf16_f32 v41, v34, v35\n"
      "v_mfma_f32_16x16x32_bf16 v[12:15], v[152:155], a[204:207], v[12:15]\n"
      "ds_read2st64_b32 v[118:119], v245 offset0:40 offset1:41\n"
      "v_cvt_pk_bf16_f32 v42, v36, v37\n"
      "v_mfma_f32_16x16x32_bf16 v[16:19], v[152:155], a[208:211], v[16:19]\n"
      "ds_read2st64_b32 v[120:121], v244 offset0:38 offset1:39\n"
      "v_cvt_pk_bf16_f32 v45, v38, v39\n"
      "v_mfma_f32_16x16x32_bf16 v[20:23], v[152:155], a[212:215], v[20:23]\n"
      "global_store_dwordx4 v242, v[40:43], s[52:53] offset:1024 sc1\n"
      "v_mfma_f32_16x16x32_bf16 v[24:27], v[152:155], a[216:219], v[24:27]\n"
      "global_store_dwordx4 v242, v[44:47], s[52:53] offset:2048 sc1\n"
      "s_add_u32 s48, s34, 2\n"
      "v_mfma_f32_16x16x32_bf16 v[28:31], v[152:155], a[220:223], v[28:31]\n"
      "s_and_b32 s48, s48, 3\n"
      "global_load_dwordx4 v[152:155], v241, s[14:15] offset:2048 sc1\n"
      "s_mul_i32 s48, s48, 36864\n"
      "v_mfma_f32_16x16x32_bf16 v[0:3], v[156:159], a[224:227], v[0:3]\n"
      "s_add_u32 s48, s48, 12288\n"
      "ds_read2st64_b32 v[122:123], v245 offset0:42 offset1:43\n"
      "s_add_u32 s52, s60, s48\n"
      "s_addc_u32 s53, s61, 0\n"
      "v_mfma_f32_16x16x32_bf16 v[4:7], v[156:159], a[228:231], v[4:7]\n"
      "s_waitcnt lgkmcnt(4)\n"
      "v_mfma_f32_16x16x32_bf16 v[64:67], v[250:253], v[84:87], v[64:67]\n"
      "v_mfma_f32_16x16x32_bf16 v[8:11], v[156:159], a[232:235], v[8:11]\n"
      "s_waitcnt vmcnt(11)\n"
      "v_bitop3_b32 v192, v225, v227, s37 bitop3:0x7e\n"
      "v_bitop3_b32 v192, v192, v229, s37 bitop3:0xf6\n"
      "v_bitop3_b32 v192, v192, v231, s37 bitop3:0xf6\n"
      "v_bitop3_b32 v192, v192, v233, s37 bitop3:0xf6\n"
      "v_bitop3_b32 v192, v192, v235, s37 bitop3:0xf6\n"
      "v_bitop3_b32 v192, v192, v237, s37 bitop3:0xf6\n"
      "v_bitop3_b32 v192, v192, v239, s37 bitop3:0xf6\n"
      "v_cmp_ne_u32 vcc, 0, v192\n"
      "s_nop 1\n"
      "s_cbranch_vccnz .Lp_s13_%=\n"
      ".Lp_o14_%=:\n"
      "v_mfma_f32_16x16x32_bf16 v[12:15], v[156:159], a[236:239], v[12:15]\n"
      "v_mfma_f32_16x16x32_bf16 v[68:71], v[250:253], v[88:91], v[68:71]\n"
      "v_add_f32 v192, v224, v228\n"
      "v_mfma_f32_16x16x32_bf16 v[16:19], v[156:159], a[240:243], v[16:19]\n"
      "v_mfma_f32_16x16x32_bf16 v[64:67], v[252:255], v[92:95], v[64:67]\n"
      "v_add_f32 v193, v226, v230\n"
      "v_mfma_f32_16x16x32_bf16 v[20:23], v[156:159], a[244:247], v[20:23]\n"
      "v_mfma_f32_16x16x32_bf16 v[68:71], v[248:251], v[92:95], v[68:71]\n"
      "v_add_f32 v194, v232, v236\n"
      "v_mfma_f32_16x16x32_bf16 v[24:27], v[156:159], a[248:251], v[24:27]\n"
      "ds_read2st64_b32 v[124:125], v246 offset0:44 offset1:45\n"
      "v_add_f32 v195, v234, v238\n"
      "v_mfma_f32_16x16x32_bf16 v[28:31], v[156:159], a[252:255], v[28:31]\n"
      "global_load_dwordx4 v[156:159], v241, s[74:75] offset:2048 sc1\n"
      "v_mov_b32 v75, s37\n"
      "v_mfma_f32_16x16x32_bf16 v[32:35], v[160:163], a[0:3], 0\n"
      "ds_read_b64_tr_b16 v[224:225], v201 offset:0\n"
      "v_permlane32_swap_b32 v192, v193\n"
      "v_mfma_f32_16x16x32_bf16 v[36:39], v[160:163], a[4:7], 0\n"
      "ds_read_b64_tr_b16 v[226:227], v201 offset:512\n"
      "v_permlane32_swap_b32 v194, v195\n"
      "v_mfma_f32_16x16x32_bf16 v[40:43], v[160:163], a[8:11], 0\n"
      "ds_read_b64_tr_b16 v[228:229], v201 offset:32\n"
      "v_add_f32 v192, v192, v193\n"
      "v_mfma_f32_16x16x32_bf16 v[44:47], v[160:163], a[12:15], 0\n"
      "ds_read_b64_tr_b16 v[230:231], v201 offset:544\n"
      "v_add_f32 v194, v194, v195\n"
      "v_mfma_f32_16x16x32_bf16 v[48:51], v[160:163], a[16:19], 0\n"
      "ds_read_b64_tr_b16 v[232:233], v201 offset:64\n"
      "v_mov_b32 v193, v192\n"
      "v_mfma_f32_16x16x32_bf16 v[52:55], v[160:163], a[20:23], 0\n"
      "ds_read_b64_tr_b16 v[234:235], v201 offset:576\n"
      "v_mov_b32 v195, v194\n"
      "v_mfma_f32_16x16x32_bf16 v[56:59], v[160:163], a[24:27], 0\n"
      "ds_read_b64_tr_b16 v[236:237], v201 offset:96\n"
      "v_permlane16_swap_b32 v192, v193\n"
      "v_mfma_f32_16x16x32_bf16 v[60:63], v[160:163], a[28:31], 0\n"
      "global_load_dwordx4 v[160:163], v240, s[10:11] offset:3072 sc1\n"
      "v_cvt_pk_bf16_f32 v72, v64, v65\n"
      "v_mfma_f32_16x16x32_bf16 v[32:35], v[164:167], a[32:35], v[32:35]\n"
      "ds_read_b64_tr_b16 v[238:239], v201 offset:608\n"
      "v_permlane16_swap_b32 v194, v195\n"
      "v_mfma_f32_16x16x32_bf16 v[36:39], v[164:167], a[36:39], v[36:39]\n"
      "ds_read2st64_b32 v[126:127], v246 offset0:46 offset1:47\n"
      "v_add_f32 v192, v192, v193\n"
      "v_mfma_f32_16x16x32_bf16 v[40:43], v[164:167], a[40:43], v[40:43]\n"
      "v_add_f32 v194, v194, v195\n"
      "v_cvt_pk_bf16_f32 v192, v192, v194\n"
      "v_mfma_f32_16x16x32_bf16 v[44:47], v[164:167], a[44:47], v[44:47]\n"
      "v_and_b32 v194, 0xffff0000, v192\n"
      "v_lshlrev_b32 v192, 16, v192\n"
      "v_mfma_f32_16x16x32_bf16 v[48:51], v[164:167], a[48:51], v[48:51]\n"
      "v_mul_f32 v193, 0xbfb8aa3b, v192\n"
      "v_exp_f32 v193, v193\n"
      "v_mfma_f32_16x16x32_bf16 v[52:55], v[164:167], a[52:55], v[52:55]\n"
      "v_add_f32 v193, 1.0, v193\n"
      "v_rcp_f32 v193, v193\n"
      "v_mfma_f32_16x16x32_bf16 v[56:59], v[164:167], a[56:59], v[56:59]\n"
      "v_mul_f32 v192, v192, v193\n"
      "v_cvt_pk_bf16_f32 v192, v192, v192\n"
      "v_mfma_f32_16x16x32_bf16 v[60:63], v[164:167], a[60:63], v[60:63]\n"
      "global_load_dwordx4 v[164:167], v240, s[72:73] offset:3072 sc1\n"
      "v_and_b32 v192, 0xffff0000, v192\n"
      "v_mfma_f32_16x16x32_bf16 v[32:35], v[168:171], a[64:67], v[32:35]\n"
      "v_mul_f32 v192, v192, v194\n"
      "v_cvt_pk_bf16_f32 v192, v192, v192\n"
      "v_mfma_f32_16x16x32_bf16 v[36:39], v[168:171], a[68:71], v[36:39]\n"
      "ds_write_b16 v202, v192\n"
      "v_cvt_pk_bf16_f32 v73, v66, v67\n"
      "v_mfma_f32_16x16x32_bf16 v[40:43], v[168:171], a[72:75], v[40:43]\n"
      "ds_read_b128 v[192:195], v199\n"
      "v_cvt_pk_bf16_f32 v74, v68, v69\n"
      "v_mfma_f32_16x16x32_bf16 v[44:47], v[168:171], a[76:79], v[44:47]\n"
      "s_waitcnt vmcnt(9)\n"
      "v_bitop3_b32 v210, v205, v207, s37 bitop3:0x7e\n"
      "v_cmp_ne_u32 vcc, 0, v210\n"
      "s_nop 1\n"
      "s_cbranch_vccnz .Lp_s16_%=\n"
      ".Lp_o17_%=:\n"
      "v_mfma_f32_16x16x32_bf16 v[48:51], v[168:171], a[80:83], v[48:51]\n"
      "v_cvt_pk_bf16_f32 v247, v70, v71\n"
      "v_cvt_pk_bf16_f32 v8, v8, v9\n"
      "v_mfma_f32_16x16x32_bf16 v[52:55], v[168:171], a[84:87], v[52:55]\n"
      "global_store_dwordx4 v242, v[72:75], s[52:53] offset:0 sc1\n"
      "s_waitcnt lgkmcnt(2)\n"
      "v_mfma_f32_16x16x32_bf16 v[56:59], v[168:171], a[88:91], v[56:59]\n"
      "v_mfma_f32_16x16x32_bf16 v[96:99], v[250:253], v[116:119], v[96:99]\n"
      "v_cvt_pk_bf16_f32 v9, v10, v11\n"
      "v_mfma_f32_16x16x32_bf16 v[60:63], v[168:171], a[92:95], v[60:63]\n"
      "global_load_dwordx4 v[168:171], v241, s[10:11] offset:3072 sc1\n"
      "v_mov_b32 v209, s39\n"
      "v_mfma_f32_16x16x32_bf16 v[32:35], v[172:175], a[96:99], v[32:35]\n"
      "s_waitcnt lgkmcnt(0)\n"
      "v_mfma_f32_16x16x32_bf16 v[196:199], v[192:195], v[224:227], 0\n"
      "v_mfma_f32_16x16x32_bf16 v[36:39], v[172:175], a[100:103], v[36:39]\n"
      "v_mfma_f32_16x16x32_bf16 v[224:227], v[192:195], v[228:231], 0\n"
      "v_mfma_f32_16x16x32_bf16 v[40:43], v[172:175], a[104:107], v[40:43]\n"
      "v_mfma_f32_16x16x32_bf16 v[228:231], v[192:195], v[232:235], 0\n"
      "v_mfma_f32_16x16x32_bf16 v[44:47], v[172:175], a[108:111], v[44:47]\n"
      "v_mfma_f32_16x16x32_bf16 v[232:235], v[192:195], v[236:239], 0\n"
      "v_mov_b32 v193, s37\n"
      "v_mfma_f32_16x16x32_bf16 v[48:51], v[172:175], a[112:115], v[48:51]\n"
      "v_mfma_f32_16x16x32_bf16 v[100:103], v[250:253], v[120:123], v[100:103]\n"
      "v_mov_b32 v195, s37\n"
      "v_mfma_f32_16x16x32_bf16 v[52:55], v[172:175], a[116:119], v[52:55]\n"
      "v_mfma_f32_16x16x32_bf16 v[96:99], v[252:255], v[124:127], v[96:99]\n"
      "v_mfma_f32_16x16x32_bf16 v[56:59], v[172:175], a[120:123], v[56:59]\n"
      "v_mfma_f32_16x16x32_bf16 v[100:103], v[248:251], v[124:127], v[100:103]\n"
      "v_cndmask_b32 v192, v196, v228, s[64:65]\n"
      "v_mfma_f32_16x16x32_bf16 v[60:63], v[172:175], a[124:127], v[60:63]\n"
      "global_load_dwordx4 v[172:175], v241, s[72:73] offset:3072 sc1\n"
      "v_cndmask_b32 v194, v224, v232, s[64:65]\n"
      "v_mfma_f32_16x16x32_bf16 v[40:43], v[176:179], a[136:139], v[40:43]\n"
      "ds_write_addtid_b32 v8 offset:0\n"
      "v_and_b32 v192, s86, v192\n"
      "v_mfma_f32_16x16x32_bf16 v[44:47], v[176:179], a[140:143], v[44:47]\n"
      "v_cmp_eq_u32 vcc, 0, v208\n"
      "s_nop 1\n"
      "v_cndmask_b32 v204, v204, v209, vcc\n"
      "v_mfma_f32_16x16x32_bf16 v[48:51], v[176:179], a[144:147], v[48:51]\n"
      "v_cmp_eq_u32 vcc, -16, v208\n"
      "s_nop 1\n"
      "v_cndmask_b32 v206, v206, v209, vcc\n"
      "v_mfma_f32_16x16x32_bf16 v[52:55], v[176:179], a[148:151], v[52:55]\n"
      "v_and_b32 v194, s86, v194\n"
      "v_mov_b32 v107, s37\n"
      "v_mfma_f32_16x16x32_bf16 v[56:59], v[176:179], a[152:155], v[56:59]\n"
      "s_mov_b64 exec, s[66:67]\n"
      "global_store_dwordx4 v203, v[192:195], s[58:59] sc1\n"
      "s_mov_b64 exec, -1\n"
      "v_mfma_f32_16x16x32_bf16 v[60:63], v[176:179], a[156:159], v[60:63]\n"
      "v_mov_b32 v108, v247\n"
      "v_mov_b32 v111, s37\n"
      "v_mfma_f32_16x16x32_bf16 v[40:43], v[180:183], a[168:171], v[40:43]\n"
      "v_cvt_pk_bf16_f32 v10, v12, v13\n"
      "v_sub_f32 v205, 1.0, v204\n"
      "v_mfma_f32_16x16x32_bf16 v[44:47], v[180:183], a[172:175], v[44:47]\n"
      "ds_write_addtid_b32 v9 offset:256\n"
      "v_cvt_pk_bf16_f32 v104, v96, v97\n"
      "v_mfma_f32_16x16x32_bf16 v[48:51], v[180:183], a[176:179], v[48:51]\n"
      "v_cvt_pk_bf16_f32 v105, v98, v99\n"
      "v_cvt_pk_bf16_f32 v106, v100, v101\n"
      "v_mfma_f32_16x16x32_bf16 v[52:55], v[180:183], a[180:183], v[52:55]\n"
      "v_cvt_pk_bf16_f32 v109, v102, v103\n"
      "v_cvt_pk_bf16_f32 v11, v14, v15\n"
      "v_mfma_f32_16x16x32_bf16 v[56:59], v[180:183], a[184:187], v[56:59]\n"
      "global_store_dwordx4 v242, v[104:107], s[52:53] offset:1024 sc1\n"
      "v_cvt_pk_bf16_f32 v205, v205, v205\n"
      "v_mfma_f32_16x16x32_bf16 v[60:63], v[180:183], a[188:191], v[60:63]\n"
      "global_store_dwordx4 v242, v[108:111], s[52:53] offset:2048 sc1\n"
      "s_add_u32 s48, s34, 3\n"
      "v_mfma_f32_16x16x32_bf16 v[40:43], v[184:187], a[200:203], v[40:43]\n"
      "s_and_b32 s48, s48, 3\n"
      "ds_write_addtid_b32 v10 offset:512\n"
      "s_mul_i32 s48, s48, 36864\n"
      "v_mfma_f32_16x16x32_bf16 v[44:47], v[184:187], a[204:207], v[44:47]\n"
      "s_add_u32 s48, s48, 24576\n"
      "s_add_u32 s52, s60, s48\n"
      "s_addc_u32 s53, s61, 0\n"
      "v_cvt_pk_bf16_f32 v12, v16, v17\n"
      "v_mfma_f32_16x16x32_bf16 v[48:51], v[184:187], a[208:211], v[48:51]\n"
      "ds_write_addtid_b32 v11 offset:768\n"
      "v_cvt_pk_bf16_f32 v13, v18, v19\n"
      "v_and_b32 v205, 0xffff0000, v205\n"
      "v_mfma_f32_16x16x32_bf16 v[52:55], v[184:187], a[212:215], v[52:55]\n"
      "ds_write_addtid_b32 v12 offset:1024\n"
      "v_cvt_pk_bf16_f32 v14, v20, v21\n"
      "v_sub_f32 v207, 1.0, v206\n"
      "v_mfma_f32_16x16x32_bf16 v[56:59], v[184:187], a[216:219], v[56:59]\n"
      "ds_write_addtid_b32 v13 offset:1280\n"
      "v_cvt_pk_bf16_f32 v15, v22, v23\n"
      "v_cvt_pk_bf16_f32 v207, v207, v207\n"
      "v_mfma_f32_16x16x32_bf16 v[60:63], v[184:187], a[220:223], v[60:63]\n"
      "ds_write_addtid_b32 v14 offset:1536\n"
      "v_cvt_pk_bf16_f32 v16, v24, v25\n"
      "v_and_b32 v207, 0xffff0000, v207\n"
      "v_mfma_f32_16x16x32_bf16 v[40:43], v[188:191], a[232:235], v[40:43]\n"
      "ds_write_addtid_b32 v15 offset:1792\n"
      "v_cvt_pk_bf16_f32 v17, v26, v27\n"
      "v_lshlrev_b32 v208, 16, v216\n"
      "v_mfma_f32_16x16x32_bf16 v[44:47], v[188:191], a[236:239], v[44:47]\n"
      "ds_write_addtid_b32 v16 offset:2048\n"
      "v_cvt_pk_bf16_f32 v18, v28, v29\n"
      "v_and_b32 v209, 0xffff0000, v216\n"
      "v_mfma_f32_16x16x32_bf16 v[48:51], v[188:191], a[240:243], v[48:51]\n"
      "ds_write_addtid_b32 v17 offset:2304\n"
      "v_cvt_pk_bf16_f32 v19, v30, v31\n"
      "v_mul_f32 v208, v205, v208\n"
      "v_mfma_f32_16x16x32_bf16 v[52:55], v[188:191], a[244:247], v[52:55]\n"
      "ds_write_addtid_b32 v18 offset:2560\n"
      "ds_write_addtid_b32 v19 offset:2816\n"
      "v_mul_f32 v209, v205, v209\n"
      "v_mfma_f32_16x16x32_bf16 v[56:59], v[188:191], a[248:251], v[56:59]\n"
      "v_lshlrev_b32 v212, 16, v220\n"
      "v_and_b32 v213, 0xffff0000, v220\n"
      "v_mul_f32 v212, v207, v212\n"
      "v_mfma_f32_16x16x32_bf16 v[60:63], v[188:191], a[252:255], v[60:63]\n"
      "v_mul_f32 v213, v207, v213\n"
      "v_lshlrev_b32 v210, 16, v217\n"
      "v_and_b32 v211, 0xffff0000, v217\n"
      "v_mfma_f32_16x16x32_bf16 v[32:35], v[176:179], a[128:131], v[32:35]\n"
      "s_waitcnt lgkmcnt(0)\n"
      "s_barrier\n"
      "v_mfma_f32_16x16x32_bf16 v[36:39], v[176:179], a[132:135], v[36:39]\n"
      "global_load_dwordx4 v[176:179], v240, s[14:15] offset:3072 sc1\n"
      "ds_read2st64_b32 v[20:21], v244 offset0:0 offset1:1\n"
      "v_mul_f32 v210, v205, v210\n"
      "v_mfma_f32_16x16x32_bf16 v[32:35], v[180:183], a[160:163], v[32:35]\n"
      "ds_read2st64_b32 v[22:23], v245 offset0:4 offset1:5\n"
      "ds_read2st64_b32 v[24:25], v244 offset0:2 offset1:3\n"
      "v_mul_f32 v211, v205, v211\n"
      "v_mfma_f32_16x16x32_bf16 v[36:39], v[180:183], a[164:167], v[36:39]\n"
      "ds_read2st64_b32 v[26:27], v245 offset0:6 offset1:7\n"
      "ds_read2st64_b32 v[28:29], v246 offset0:8 offset1:9\n"
      "v_lshlrev_b32 v214, 16, v221\n"
      "v_mfma_f32_16x16x32_bf16 v[32:35], v[184:187], a[192:195], v[32:35]\n"
      "ds_read2st64_b32 v[30:31], v246 offset0:10 offset1:11\n"
      "v_cvt_pk_bf16_f32 v40, v40, v41\n"
      "v_cvt_pk_bf16_f32 v41, v42, v43\n"
      "v_mfma_f32_16x16x32_bf16 v[36:39], v[184:187], a[196:199], v[36:39]\n"
      "global_load_dwordx4 v[180:183], v240, s[74:75] offset:3072 sc1\n"
      "ds_write_addtid_b32 v40 offset:3072\n"
      "v_cvt_pk_bf16_f32 v42, v44, v45\n"
      "v_mfma_f32_16x16x32_bf16 v[32:35], v[188:191], a[224:227], v[32:35]\n"
      "ds_write_addtid_b32 v41 offset:3328\n"
      "v_cvt_pk_bf16_f32 v43, v46, v47\n"
      "v_and_b32 v215, 0xffff0000, v221\n"
      "v_mfma_f32_16x16x32_bf16 v[36:39], v[188:191], a[228:231], v[36:39]\n"
      "ds_write_addtid_b32 v42 offset:3584\n"
      "v_cvt_pk_bf16_f32 v44, v48, v49\n"
      "v_mul_f32 v214, v207, v214\n"
      "v_mfma_f32_16x16x32_bf16 v[72:75], v[128:131], a[8:11], 0\n"
      "ds_write_addtid_b32 v43 offset:3840\n"
      "v_cvt_pk_bf16_f32 v45, v50, v51\n"
      "v_mul_f32 v215, v207, v215\n"
      "v_mfma_f32_16x16x32_bf16 v[76:79], v[128:131], a[12:15], 0\n"
      "s_waitcnt lgkmcnt(4)\n"
      "global_load_dwordx4 v[184:187], v241, s[14:15] offset:3072 sc1\n"
      "v_mfma_f32_16x16x32_bf16 v[0:3], v[250:253], v[20:23], v[0:3]\n"
      "v_lshlrev_b32 v216, 16, v218\n"
      "v_mfma_f32_16x16x32_bf16 v[80:83], v[128:131], a[16:19], 0\n"
      "v_mfma_f32_16x16x32_bf16 v[4:7], v[250:253], v[24:27], v[4:7]\n"
      "v_mfma_f32_16x16x32_bf16 v[0:3], v[252:255], v[28:31], v[0:3]\n"
      "v_and_b32 v217, 0xffff0000, v218\n"
      "v_mfma_f32_16x16x32_bf16 v[84:87], v[128:131], a[20:23], 0\n"
      "v_mfma_f32_16x16x32_bf16 v[4:7], v[248:251], v[28:31], v[4:7]\n"
      "ds_write_addtid_b32 v44 offset:4096\n"
      "v_mov_b32 v11, s37\n"
      "v_mfma_f32_16x16x32_bf16 v[88:91], v[128:131], a[24:27], 0\n"
      "v_cvt_pk_bf16_f32 v46, v52, v53\n"
      "v_mul_f32 v216, v205, v216\n"
      "v_mul_f32 v217, v205, v217\n"
      "v_mfma_f32_16x16x32_bf16 v[92:95], v[128:131], a[28:31], 0\n"
      "global_load_dwordx4 v[188:191], v241, s[74:75] offset:3072 sc1\n"
      "ds_write_addtid_b32 v45 offset:4352\n"
      "v_cvt_pk_bf16_f32 v8, v0, v1\n"
      "v_mfma_f32_16x16x32_bf16 v[72:75], v[132:135], a[40:43], v[72:75]\n"
      "v_cvt_pk_bf16_f32 v9, v2, v3\n"
      "v_cvt_pk_bf16_f32 v10, v4, v5\n"
      "v_cvt_pk_bf16_f32 v247, v6, v7\n"
      "v_mfma_f32_16x16x32_bf16 v[76:79], v[132:135], a[44:47], v[76:79]\n"
      "global_store_dwordx4 v242, v[8:11], s[52:53] offset:0 sc1\n"
      "v_cvt_pk_bf16_f32 v47, v54, v55\n"
      "v_lshlrev_b32 v220, 16, v222\n"
      "v_mfma_f32_16x16x32_bf16 v[80:83], v[132:135], a[48:51], v[80:83]\n"
      "ds_write_addtid_b32 v46 offset:4608\n"
      "v_cvt_pk_bf16_f32 v48, v56, v57\n"
      "v_and_b32 v221, 0xffff0000, v222\n"
      "v_mfma_f32_16x16x32_bf16 v[84:87], v[132:135], a[52:55], v[84:87]\n"
      "ds_write_addtid_b32 v47 offset:4864\n"
      "v_cvt_pk_bf16_f32 v49, v58, v59\n"
      "v_mul_f32 v220, v207, v220\n"
      "v_mfma_f32_16x16x32_bf16 v[88:91], v[132:135], a[56:59], v[88:91]\n"
      "ds_write_addtid_b32 v48 offset:5120\n"
      "v_cvt_pk_bf16_f32 v50, v60, v61\n"
      "v_mul_f32 v221, v207, v221\n"
      "v_mfma_f32_16x16x32_bf16 v[92:95], v[132:135], a[60:63], v[92:95]\n"
      "s_waitcnt vmcnt(19)\n"
      "ds_write_addtid_b32 v49 offset:5376\n"
      "v_cvt_pk_bf16_f32 v51, v62, v63\n"
      "v_lshlrev_b32 v218, 16, v219\n"
      "v_mfma_f32_16x16x32_bf16 v[72:75], v[136:139], a[72:75], v[72:75]\n"
      "ds_write_addtid_b32 v50 offset:5632\n"
      "ds_write_addtid_b32 v51 offset:5888\n"
      "v_and_b32 v219, 0xffff0000, v219\n"
      "v_mfma_f32_16x16x32_bf16 v[76:79], v[136:139], a[76:79], v[76:79]\n"
      "v_mul_f32 v218, v205, v218\n"
      "v_mul_f32 v219, v205, v219\n"
      "v_lshlrev_b32 v222, 16, v223\n"
      "v_mfma_f32_16x16x32_bf16 v[80:83], v[136:139], a[80:83], v[80:83]\n"
      "v_and_b32 v223, 0xffff0000, v223\n"
      "v_mul_f32 v222, v207, v222\n"
      "v_mul_f32 v223, v207, v223\n"
      "v_mfma_f32_16x16x32_bf16 v[84:87], v[136:139], a[84:87], v[84:87]\n"
      "s_lshl2_add_u32 s54, s36, s33\n"
      "s_waitcnt lgkmcnt(0)\n"
      "s_barrier\n"
      "v_mfma_f32_16x16x32_bf16 v[88:91], v[136:139], a[88:91], v[88:91]\n"
      "s_lshl_b32 s55, s54, 15\n"
      "ds_read2st64_b32 v[52:53], v244 offset0:12 offset1:13\n"
      "ds_read2st64_b32 v[54:55], v245 offset0:16 offset1:17\n"
      "v_add_u32 v4, 0, v242\n"
      "v_mfma_f32_16x16x32_bf16 v[92:95], v[136:139], a[92:95], v[92:95]\n"
      "s_lshl2_add_u32 s56, s35, s34\n"
      "global_load_dwordx4 v[12:15], v4, s[62:63] offset:0 sc1\n"
      "global_load_dwordx4 v[16:19], v4, s[62:63] offset:1024 sc1\n"
      "s_lshl_b32 s56, s56, 6\n"
      "v_mfma_f32_16x16x32_bf16 v[72:75], v[140:143], a[104:107], v[72:75]\n"
      "s_add_u32 s55, s55, s56\n"
      "global_load_dwordx4 v[20:23], v4, s[62:63] offset:2048 sc1\n"
      "ds_read2st64_b32 v[56:57], v244 offset0:14 offset1:15\n"
      "v_and_b32 v8, 15, v243\n"
      "v_mfma_f32_16x16x32_bf16 v[76:79], v[140:143], a[108:111], v[76:79]\n"
      "ds_read2st64_b32 v[58:59], v245 offset0:18 offset1:19\n"
      "ds_read2st64_b32 v[60:61], v246 offset0:20 offset1:21\n"
      "v_and_b32 v7, 48, v243\n"
      "v_mfma_f32_16x16x32_bf16 v[80:83], v[140:143], a[112:115], v[80:83]\n"
      "ds_read2st64_b32 v[62:63], v246 offset0:22 offset1:23\n"
      "v_lshl_or_b32 v7, v8, 10, v7\n"
      "v_add_u32 v7, s55, v7\n"
      "v_mfma_f32_16x16x32_bf16 v[84:87], v[140:143], a[116:119], v[84:87]\n"
      "v_mfma_f32_16x16x32_bf16 v[88:91], v[140:143], a[120:123], v[88:91]\n"
      "v_mfma_f32_16x16x32_bf16 v[92:95], v[140:143], a[124:127], v[92:95]\n"
      "s_waitcnt vmcnt(20)\n"
      "v_mfma_f32_16x16x32_bf16 v[72:75], v[144:147], a[136:139], v[72:75]\n"
      "v_mfma_f32_16x16x32_bf16 v[76:79], v[144:147], a[140:143], v[76:79]\n"
      "s_waitcnt lgkmcnt(0)\n"
      "v_mfma_f32_16x16x32_bf16 v[32:35], v[250:253], v[52:55], v[32:35]\n"
      "v_mfma_f32_16x16x32_bf16 v[36:39], v[250:253], v[56:59], v[36:39]\n"
      "v_mfma_f32_16x16x32_bf16 v[80:83], v[144:147], a[144:147], v[80:83]\n"
      "v_mfma_f32_16x16x32_bf16 v[32:35], v[252:255], v[60:63], v[32:35]\n"
      "v_mfma_f32_16x16x32_bf16 v[84:87], v[144:147], a[148:151], v[84:87]\n"
      "v_mfma_f32_16x16x32_bf16 v[36:39], v[248:251], v[60:63], v[36:39]\n"
      "v_mov_b32 v43, s37\n"
      "v_mfma_f32_16x16x32_bf16 v[88:91], v[144:147], a[152:155], v[88:91]\n"
      "v_mov_b32 v44, v247\n"
      "v_mov_b32 v47, s37\n"
      "v_mfma_f32_16x16x32_bf16 v[92:95], v[144:147], a[156:159], v[92:95]\n"
      "v_mfma_f32_16x16x32_bf16 v[72:75], v[148:151], a[168:171], v[72:75]\n"
      "v_cvt_pk_bf16_f32 v40, v32, v33\n"
      "v_cvt_pk_bf16_f32 v41, v34, v35\n"
      "v_mfma_f32_16x16x32_bf16 v[76:79], v[148:151], a[172:175], v[76:79]\n"
      "v_cvt_pk_bf16_f32 v42, v36, v37\n"
      "v_cvt_pk_bf16_f32 v45, v38, v39\n"
      "v_mfma_f32_16x16x32_bf16 v[80:83], v[148:151], a[176:179], v[80:83]\n"
      "global_store_dwordx4 v242, v[40:43], s[52:53] offset:1024 sc1\n"
      "v_mfma_f32_16x16x32_bf16 v[84:87], v[148:151], a[180:183], v[84:87]\n"
      "global_store_dwordx4 v242, v[44:47], s[52:53] offset:2048 sc1\n"
      "v_mfma_f32_16x16x32_bf16 v[88:91], v[148:151], a[184:187], v[88:91]\n"
      "v_mfma_f32_16x16x32_bf16 v[92:95], v[148:151], a[188:191], v[92:95]\n"
      "s_waitcnt vmcnt(18)\n"
      "v_mfma_f32_16x16x32_bf16 v[72:75], v[152:155], a[200:203], v[72:75]\n"
      "v_mfma_f32_16x16x32_bf16 v[76:79], v[152:155], a[204:207], v[76:79]\n"
      "v_mfma_f32_16x16x32_bf16 v[80:83], v[152:155], a[208:211], v[80:83]\n"
      "v_mfma_f32_16x16x32_bf16 v[84:87], v[152:155], a[212:215], v[84:87]\n"
      "v_mfma_f32_16x16x32_bf16 v[88:91], v[152:155], a[216:219], v[88:91]\n"
      "v_mfma_f32_16x16x32_bf16 v[92:95], v[152:155], a[220:223], v[92:95]\n"
      "v_mfma_f32_16x16x32_bf16 v[72:75], v[156:159], a[232:235], v[72:75]\n"
      "v_mfma_f32_16x16x32_bf16 v[76:79], v[156:159], a[236:239], v[76:79]\n"
      "v_mfma_f32_16x16x32_bf16 v[80:83], v[156:159], a[240:243], v[80:83]\n"
      "v_mfma_f32_16x16x32_bf16 v[84:87], v[156:159], a[244:247], v[84:87]\n"
      "v_mfma_f32_16x16x32_bf16 v[88:91], v[156:159], a[248:251], v[88:91]\n"
      "s_nop 3\n"
      "v_cvt_pk_bf16_f32 v72, v72, v73\n"
      "v_mfma_f32_16x16x32_bf16 v[92:95], v[156:159], a[252:255], v[92:95]\n"
      "s_waitcnt vmcnt(16)\n"
      "v_cvt_pk_bf16_f32 v73, v74, v75\n"
      "v_mfma_f32_16x16x32_bf16 v[104:107], v[160:163], a[8:11], 0\n"
      "ds_write_addtid_b32 v72 offset:6144\n"
      "v_cvt_pk_bf16_f32 v74, v76, v77\n"
      "v_mfma_f32_16x16x32_bf16 v[108:111], v[160:163], a[12:15], 0\n"
      "ds_write_addtid_b32 v73 offset:6400\n"
      "v_cvt_pk_bf16_f32 v75, v78, v79\n"
      "v_mfma_f32_16x16x32_bf16 v[112:115], v[160:163], a[16:19], 0\n"
      "ds_write_addtid_b32 v74 offset:6656\n"
      "v_cvt_pk_bf16_f32 v76, v80, v81\n"
      "v_mfma_f32_16x16x32_bf16 v[116:119], v[160:163], a[20:23], 0\n"
      "ds_write_addtid_b32 v75 offset:6912\n"
      "v_cvt_pk_bf16_f32 v77, v82, v83\n"
      "v_mfma_f32_16x16x32_bf16 v[120:123], v[160:163], a[24:27], 0\n"
      "ds_write_addtid_b32 v76 offset:7168\n"
      "v_cvt_pk_bf16_f32 v78, v84, v85\n"
      "v_mfma_f32_16x16x32_bf16 v[124:127], v[160:163], a[28:31], 0\n"
      "ds_write_addtid_b32 v77 offset:7424\n"
      "v_cvt_pk_bf16_f32 v79, v86, v87\n"
      "v_mfma_f32_16x16x32_bf16 v[104:107], v[164:167], a[40:43], v[104:107]\n"
      "ds_write_addtid_b32 v78 offset:7680\n"
      "v_cvt_pk_bf16_f32 v80, v88, v89\n"
      "v_mfma_f32_16x16x32_bf16 v[108:111], v[164:167], a[44:47], v[108:111]\n"
      "ds_write_addtid_b32 v79 offset:7936\n"
      "v_cvt_pk_bf16_f32 v81, v90, v91\n"
      "v_mfma_f32_16x16x32_bf16 v[112:115], v[164:167], a[48:51], v[112:115]\n"
      "ds_write_addtid_b32 v80 offset:8192\n"
      "v_cvt_pk_bf16_f32 v82, v92, v93\n"
      "v_mfma_f32_16x16x32_bf16 v[116:119], v[164:167], a[52:55], v[116:119]\n"
      "ds_write_addtid_b32 v81 offset:8448\n"
      "v_cvt_pk_bf16_f32 v83, v94, v95\n"
      "v_mfma_f32_16x16x32_bf16 v[120:123], v[164:167], a[56:59], v[120:123]\n"
      "ds_write_addtid_b32 v82 offset:8704\n"
      "v_mfma_f32_16x16x32_bf16 v[124:127], v[164:167], a[60:63], v[124:127]\n"
      "s_waitcnt vmcnt(13)\n"
      "ds_write_addtid_b32 v83 offset:8960\n"
      "v_mfma_f32_16x16x32_bf16 v[104:107], v[168:171], a[72:75], v[104:107]\n"
      "s_waitcnt vmcnt(2)\n"
      "v_bitop3_b32 v22, v15, v19, s37 bitop3:0x7e\n"
      "v_bitop3_b32 v22, v22, v23, s37 bitop3:0xf6\n"
      "v_cmp_ne_u32 vcc, 0, v22\n"
      "s_nop 1\n"
      "s_cbranch_vccnz .Lp_s19_%=\n"
      ".Lp_o20_%=:\n"
      "v_mfma_f32_16x16x32_bf16 v[108:111], v[168:171], a[76:79], v[108:111]\n"
      "v_mov_b32 v15, v20\n"
      "v_mov_b32 v19, v21\n"
      "v_mfma_f32_16x16x32_bf16 v[112:115], v[168:171], a[80:83], v[112:115]\n"
      "v_mfma_f32_16x16x32_bf16 v[64:67], v[252:255], v[12:15], 0\n"
      "v_mfma_f32_16x16x32_bf16 v[116:119], v[168:171], a[84:87], v[116:119]\n"
      "v_mfma_f32_16x16x32_bf16 v[68:71], v[248:251], v[12:15], 0\n"
      "v_mfma_f32_16x16x32_bf16 v[120:123], v[168:171], a[88:91], v[120:123]\n"
      "v_mfma_f32_16x16x32_bf16 v[96:99], v[252:255], v[16:19], 0\n"
      "v_mfma_f32_16x16x32_bf16 v[124:127], v[168:171], a[92:95], v[124:127]\n"
      "v_mfma_f32_16x16x32_bf16 v[100:103], v[248:251], v[16:19], 0\n"
      "v_mfma_f32_16x16x32_bf16 v[104:107], v[172:175], a[104:107], v[104:107]\n"
      "s_lshr_b32 s43, s42, 7\n"
      "s_cmp_eq_u32 s43, s36\n"
      "s_cbranch_scc0 .Lp_e21_%=\n"
      "s_lshl2_add_u32 s44, s35, s34\n"
      "s_lshl2_add_u32 s43, s44, s33\n"
      "s_lshl_b32 s43, s43, 12\n"
      "s_add_u32 s43, s43, 524288\n"
      "v_add_u32 v60, s43, v242\n"
      "global_load_dwordx4 v[24:27], v60, s[58:59] offset:0 sc1\n"
      "global_load_dwordx4 v[28:31], v60, s[58:59] offset:1024 sc1\n"
      "global_load_dwordx4 v[32:35], v60, s[58:59] offset:2048 sc1\n"
      "global_load_dwordx4 v[36:39], v60, s[58:59] offset:3072 sc1\n"
      "v_and_b32 v58, 15, v243\n"
      "v_lshrrev_b32 v59, 5, v243\n"
      "v_lshl_or_b32 v58, v59, 4, v58\n"
      "s_lshl4_add_u32 s44, s42, s44\n"
      "s_lshl_b32 s44, s44, 6\n"
      "v_lshl_add_u32 v59, v58, 1, s44\n"
      "global_load_ushort v57, v59, s[16:17]\n"
      "s_mov_b32 s44, 131072\n"
      "v_lshl_add_u32 v58, v58, 2, s44\n"
      "s_lshl_b32 s43, s33, 13\n"
      "v_add_u32 v59, s43, v58\n"
      ".Lp_e21_%=:\n"
      "v_mfma_f32_16x16x32_bf16 v[108:111], v[172:175], a[108:111], v[108:111]\n"
      "v_mfma_f32_16x16x32_bf16 v[112:115], v[172:175], a[112:115], v[112:115]\n"
      "v_mfma_f32_16x16x32_bf16 v[116:119], v[172:175], a[116:119], v[116:119]\n"
      "v_mfma_f32_16x16x32_bf16 v[120:123], v[172:175], a[120:123], v[120:123]\n"
      "v_mfma_f32_16x16x32_bf16 v[124:127], v[172:175], a[124:127], v[124:127]\n"
      "v_mfma_f32_16x16x32_bf16 v[104:107], v[176:179], a[136:139], v[104:107]\n"
      "v_mfma_f32_16x16x32_bf16 v[108:111], v[176:179], a[140:143], v[108:111]\n"
      "v_mfma_f32_16x16x32_bf16 v[112:115], v[176:179], a[144:147], v[112:115]\n"
      "v_mfma_f32_16x16x32_bf16 v[116:119], v[176:179], a[148:151], v[116:119]\n"
      "v_mfma_f32_16x16x32_bf16 v[120:123], v[176:179], a[152:155], v[120:123]\n"
      "v_mfma_f32_16x16x32_bf16 v[124:127], v[176:179], a[156:159], v[124:127]\n"
      "v_mfma_f32_16x16x32_bf16 v[104:107], v[180:183], a[168:171], v[104:107]\n"
      "v_mfma_f32_16x16x32_bf16 v[108:111], v[180:183], a[172:175], v[108:111]\n"
      "v_mfma_f32_16x16x32_bf16 v[112:115], v[180:183], a[176:179], v[112:115]\n"
      "v_mfma_f32_16x16x32_bf16 v[116:119], v[180:183], a[180:183], v[116:119]\n"
      "v_add_u32 v5, 12288, v242\n"
      "v_mfma_f32_16x16x32_bf16 v[120:123], v[180:183], a[184:187], v[120:123]\n"
      "global_load_dwordx4 v[44:47], v5, s[62:63] offset:0 sc1\n"
      "v_mfma_f32_16x16x32_bf16 v[124:127], v[180:183], a[188:191], v[124:127]\n"
      "global_load_dwordx4 v[48:51], v5, s[62:63] offset:1024 sc1\n"
      "v_mfma_f32_16x16x32_bf16 v[104:107], v[184:187], a[200:203], v[104:107]\n"
      "global_load_dwordx4 v[52:55], v5, s[62:63] offset:2048 sc1\n"
      "v_mfma_f32_16x16x32_bf16 v[108:111], v[184:187], a[204:207], v[108:111]\n"
      "v_mfma_f32_16x16x32_bf16 v[112:115], v[184:187], a[208:211], v[112:115]\n"
      "v_mfma_f32_16x16x32_bf16 v[116:119], v[184:187], a[212:215], v[116:119]\n"
      "v_mfma_f32_16x16x32_bf16 v[120:123], v[184:187], a[216:219], v[120:123]\n"
      "v_mfma_f32_16x16x32_bf16 v[124:127], v[184:187], a[220:223], v[124:127]\n"
      "v_mfma_f32_16x16x32_bf16 v[104:107], v[188:191], a[232:235], v[104:107]\n"
      "v_mfma_f32_16x16x32_bf16 v[108:111], v[188:191], a[236:239], v[108:111]\n"
      "v_mfma_f32_16x16x32_bf16 v[112:115], v[188:191], a[240:243], v[112:115]\n"
      "v_mfma_f32_16x16x32_bf16 v[116:119], v[188:191], a[244:247], v[116:119]\n"
      "v_mfma_f32_16x16x32_bf16 v[120:123], v[188:191], a[248:251], v[120:123]\n"
      "s_nop 3\n"
      "v_cvt_pk_bf16_f32 v104, v104, v105\n"
      "v_mfma_f32_16x16x32_bf16 v[124:127], v[188:191], a[252:255], v[124:127]\n"
      "v_cvt_pk_bf16_f32 v105, v106, v107\n"
      "v_mfma_f32_16x16x32_bf16 v[64:67], v[128:131], a[0:3], v[64:67]\n"
      "ds_write_addtid_b32 v104 offset:9216\n"
      "v_cvt_pk_bf16_f32 v106, v108, v109\n"
      "v_mfma_f32_16x16x32_bf16 v[68:71], v[128:131], a[4:7], v[68:71]\n"
      "ds_write_addtid_b32 v105 offset:9472\n"
      "v_cvt_pk_bf16_f32 v107, v110, v111\n"
      "v_mfma_f32_16x16x32_bf16 v[64:67], v[132:135], a[32:35], v[64:67]\n"
      "ds_write_addtid_b32 v106 offset:9728\n"
      "v_cvt_pk_bf16_f32 v108, v112, v113\n"
      "v_mfma_f32_16x16x32_bf16 v[68:71], v[132:135], a[36:39], v[68:71]\n"
      "ds_write_addtid_b32 v107 offset:9984\n"
      "v_cvt_pk_bf16_f32 v109, v114, v115\n"
      "v_mfma_f32_16x16x32_bf16 v[64:67], v[136:139], a[64:67], v[64:67]\n"
      "ds_write_addtid_b32 v108 offset:10240\n"
      "v_cvt_pk_bf16_f32 v110, v116, v117\n"
      "v_mfma_f32_16x16x32_bf16 v[68:71], v[136:139], a[68:71], v[68:71]\n"
      "ds_write_addtid_b32 v109 offset:10496\n"
      "v_cvt_pk_bf16_f32 v111, v118, v119\n"
      "v_mfma_f32_16x16x32_bf16 v[64:67], v[140:143], a[96:99], v[64:67]\n"
      "ds_write_addtid_b32 v110 offset:10752\n"
      "v_cvt_pk_bf16_f32 v112, v120, v121\n"
      "v_mfma_f32_16x16x32_bf16 v[68:71], v[140:143], a[100:103], v[68:71]\n"
      "ds_write_addtid_b32 v111 offset:11008\n"
      "v_cvt_pk_bf16_f32 v113, v122, v123\n"
      "v_mfma_f32_16x16x32_bf16 v[64:67], v[144:147], a[128:131], v[64:67]\n"
      "ds_write_addtid_b32 v112 offset:11264\n"
      "v_cvt_pk_bf16_f32 v114, v124, v125\n"
      "v_mfma_f32_16x16x32_bf16 v[68:71], v[144:147], a[132:135], v[68:71]\n"
      "ds_write_addtid_b32 v113 offset:11520\n"
      "v_cvt_pk_bf16_f32 v115, v126, v127\n"
      "v_mfma_f32_16x16x32_bf16 v[64:67], v[148:151], a[160:163], v[64:67]\n"
      "ds_write_addtid_b32 v114 offset:11776\n"
      "v_add_u32 v6, 24576, v242\n"
      "v_mfma_f32_16x16x32_bf16 v[68:71], v[148:151], a[164:167], v[68:71]\n"
      "global_load_dwordx4 v[72:75], v6, s[62:63] offset:0 sc1\n"
      "v_mfma_f32_16x16x32_bf16 v[64:67], v[152:155], a[192:195], v[64:67]\n"
      "global_load_dwordx4 v[76:79], v6, s[62:63] offset:1024 sc1\n"
      "v_mfma_f32_16x16x32_bf16 v[68:71], v[152:155], a[196:199], v[68:71]\n"
      "global_load_dwordx4 v[80:83], v6, s[62:63] offset:2048 sc1\n"
      "v_mfma_f32_16x16x32_bf16 v[64:67], v[156:159], a[224:227], v[64:67]\n"
      "ds_write_addtid_b32 v115 offset:12032\n"
      "v_mfma_f32_16x16x32_bf16 v[68:71], v[156:159], a[228:231], v[68:71]\n"
      "v_mfma_f32_16x16x32_bf16 v[96:99], v[160:163], a[0:3], v[96:99]\n"
      "v_mfma_f32_16x16x32_bf16 v[100:103], v[160:163], a[4:7], v[100:103]\n"
      "s_lshr_b32 s43, s42, 7\n"
      "s_cmp_eq_u32 s43, s36\n"
      "s_cbranch_scc0 .Lp_e22_%=\n"
      "s_waitcnt vmcnt(6)\n"
      "v_bitop3_b32 v61, v25, v27, s37 bitop3:0x7e\n"
      "v_bitop3_b32 v61, v61, v29, s37 bitop3:0xf6\n"
      "v_bitop3_b32 v61, v61, v31, s37 bitop3:0xf6\n"
      "v_bitop3_b32 v61, v61, v33, s37 bitop3:0xf6\n"
      "v_bitop3_b32 v61, v61, v35, s37 bitop3:0xf6\n"
      "v_bitop3_b32 v61, v61, v37, s37 bitop3:0xf6\n"
      "v_bitop3_b32 v61, v61, v39, s37 bitop3:0xf6\n"
      "v_cmp_ne_u32 vcc, 0, v61\n"
      "s_nop 1\n"
      "s_cbranch_vccnz .Lp_s24_%=\n"
      ".Lp_o25_%=:\n"
      "v_add_f32 v24, v24, v28\n"
      "v_add_f32 v26, v26, v30\n"
      "v_add_f32 v32, v32, v36\n"
      "v_add_f32 v34, v34, v38\n"
      "v_add_f32 v24, v24, v32\n"
      "v_add_f32 v26, v26, v34\n"
      "s_nop 1\n"
      "v_permlane32_swap_b32 v24, v26\n"
      "v_add_f32 v24, v24, v26\n"
      "v_mov_b32 v26, v24\n"
      "s_nop 1\n"
      "v_permlane16_swap_b32 v24, v26\n"
      "v_add_f32 v24, v24, v26\n"
      "ds_write_b32 v59, v24\n"
      "s_waitcnt lgkmcnt(0)\n"
      ".Lp_e22_%=:\n"
      "v_mfma_f32_16x16x32_bf16 v[96:99], v[164:167], a[32:35], v[96:99]\n"
      "s_waitcnt lgkmcnt(0)\n"
      "s_barrier\n"
      "v_mfma_f32_16x16x32_bf16 v[100:103], v[164:167], a[36:39], v[100:103]\n"
      "s_lshr_b32 s43, s42, 5\n"
      "s_lshl2_add_u32 s44, s36, s33\n"
      "s_cmp_eq_u32 s43, s44\n"
      "s_cbranch_scc0 .Lp_e26_%=\n"
      "ds_read_b32 v24, v58 offset:0\n"
      "ds_read_b32 v25, v58 offset:8192\n"
      "ds_read_b32 v26, v58 offset:16384\n"
      "ds_read_b32 v27, v58 offset:24576\n"
      "v_mov_b32 v62, 0\n"
      "v_mov_b32 v63, s37\n"
      "s_or_b32 s43, s34, s35\n"
      "s_cmp_eq_u32 s43, 0\n"
      "s_cbranch_scc0 .Lp_z27_%=\n"
      "s_mov_b64 exec, 1\n"
      "global_store_dword v62, v63, s[58:59] sc1\n"
      ".Lp_z27_%=:\n"
      "s_mov_b64 exec, -1\n"
      "s_waitcnt vmcnt(6)\n"
      "v_lshlrev_b32 v57, 16, v57\n"
      "v_and_b32 v56, 48, v243\n"
      "s_mov_b32 s44, 131072\n"
      "v_lshl_add_u32 v56, v56, 1, s44\n"
      "s_waitcnt lgkmcnt(0)\n"
      "v_add_f32 v24, v24, v25\n"
      "s_waitcnt lgkmcnt(0)\n"
      "v_add_f32 v26, v26, v27\n"
      "v_add_f32 v24, v24, v26\n"
      "v_cvt_pk_bf16_f32 v24, v24, v24\n"
      "v_and_b32 v24, 0xffff0000, v24\n"
      "v_mul_f32 v24, s84, v24\n"
      "v_cvt_pk_bf16_f32 v24, v24, v24\n"
      "v_and_b32 v24, 0xffff0000, v24\n"
      "v_add_f32 v24, v24, v57\n"
      "v_cvt_pk_bf16_f32 v24, v24, v24\n"
      "v_and_b32 v24, 0xffff0000, v24\n"
      "v_sub_f32 v24, v24, v57\n"
      "ds_write_b32 v58, v24 offset:1024\n"
      "ds_read_b128 v[28:31], v56 offset:1024\n"
      "ds_read_b128 v[32:35], v56 offset:1040\n"
      ".Lp_e26_%=:\n"
      "v_mfma_f32_16x16x32_bf16 v[96:99], v[168:171], a[64:67], v[96:99]\n"
      "ds_read2st64_b32 v[84:85], v244 offset0:24 offset1:25\n"
      "v_mfma_f32_16x16x32_bf16 v[100:103], v[168:171], a[68:71], v[100:103]\n"
      "ds_read2st64_b32 v[86:87], v245 offset0:28 offset1:29\n"
      "v_mfma_f32_16x16x32_bf16 v[96:99], v[172:175], a[96:99], v[96:99]\n"
      "ds_read2st64_b32 v[88:89], v244 offset0:26 offset1:27\n"
      "v_mfma_f32_16x16x32_bf16 v[100:103], v[172:175], a[100:103], v[100:103]\n"
      "ds_read2st64_b32 v[90:91], v245 offset0:30 offset1:31\n"
      "v_mfma_f32_16x16x32_bf16 v[96:99], v[176:179], a[128:131], v[96:99]\n"
      "ds_read2st64_b32 v[92:93], v246 offset0:32 offset1:33\n"
      "v_mfma_f32_16x16x32_bf16 v[100:103], v[176:179], a[132:135], v[100:103]\n"
      "ds_read2st64_b32 v[94:95], v246 offset0:34 offset1:35\n"
      "v_mfma_f32_16x16x32_bf16 v[96:99], v[180:183], a[160:163], v[96:99]\n"
      "ds_read2st64_b32 v[116:117], v244 offset0:36 offset1:37\n"
      "v_mfma_f32_16x16x32_bf16 v[100:103], v[180:183], a[164:167], v[100:103]\n"
      "ds_read2st64_b32 v[118:119], v245 offset0:40 offset1:41\n"
      "v_mfma_f32_16x16x32_bf16 v[96:99], v[184:187], a[192:195], v[96:99]\n"
      "ds_read2st64_b32 v[120:121], v244 offset0:38 offset1:39\n"
      "v_mfma_f32_16x16x32_bf16 v[100:103], v[184:187], a[196:199], v[100:103]\n"
      "ds_read2st64_b32 v[122:123], v245 offset0:42 offset1:43\n"
      "v_mfma_f32_16x16x32_bf16 v[96:99], v[188:191], a[224:227], v[96:99]\n"
      "s_waitcnt lgkmcnt(4)\n"
      "v_mfma_f32_16x16x32_bf16 v[64:67], v[250:253], v[84:87], v[64:67]\n"
      "v_mfma_f32_16x16x32_bf16 v[100:103], v[188:191], a[228:231], v[100:103]\n"
      "v_mfma_f32_16x16x32_bf16 v[68:71], v[250:253], v[88:91], v[68:71]\n"
      "v_mfma_f32_16x16x32_bf16 v[64:67], v[252:255], v[92:95], v[64:67]\n"
      "v_mfma_f32_16x16x32_bf16 v[68:71], v[248:251], v[92:95], v[68:71]\n"
      "ds_read2st64_b32 v[124:125], v246 offset0:44 offset1:45\n"
      "ds_read2st64_b32 v[126:127], v246 offset0:46 offset1:47\n"
      "s_waitcnt vmcnt(3)\n"
      "v_bitop3_b32 v54, v47, v51, s37 bitop3:0x7e\n"
      "v_bitop3_b32 v54, v54, v55, s37 bitop3:0xf6\n"
      "v_cmp_ne_u32 vcc, 0, v54\n"
      "s_nop 1\n"
      "s_cbranch_vccnz .Lp_s29_%=\n"
      ".Lp_o30_%=:\n"
      "v_mov_b32 v47, v52\n"
      "v_mov_b32 v51, v53\n"
      "s_nop 0\n"
      "v_mfma_f32_16x16x32_bf16 v[64:67], v[252:255], v[44:47], v[64:67]\n"
      "v_mfma_f32_16x16x32_bf16 v[68:71], v[248:251], v[44:47], v[68:71]\n"
      "v_mfma_f32_16x16x32_bf16 v[96:99], v[252:255], v[48:51], v[96:99]\n"
      "v_mfma_f32_16x16x32_bf16 v[100:103], v[248:251], v[48:51], v[100:103]\n"
      "s_waitcnt lgkmcnt(0)\n"
      "v_mfma_f32_16x16x32_bf16 v[96:99], v[250:253], v[116:119], v[96:99]\n"
      "v_mfma_f32_16x16x32_bf16 v[100:103], v[250:253], v[120:123], v[100:103]\n"
      "v_mfma_f32_16x16x32_bf16 v[96:99], v[252:255], v[124:127], v[96:99]\n"
      "v_mfma_f32_16x16x32_bf16 v[100:103], v[248:251], v[124:127], v[100:103]\n"
      "s_lshr_b32 s43, s42, 5\n"
      "s_lshl2_add_u32 s44, s36, s33\n"
      "s_cmp_eq_u32 s43, s44\n"
      "s_cbranch_scc0 .Lp_e31_%=\n"
      "v_lshrrev_b32 v24, 10, v7\n"
      "v_subrev_u32 v24, s42, v24\n"
      "v_cmp_eq_u32 vcc, 0, v24\n"
      "s_nop 1\n"
      "s_mov_b64 exec, vcc\n"
      "v_fma_f32 v208, v205, v28, v208\n"
      "v_fma_f32 v209, v205, v29, v209\n"
      "v_fma_f32 v210, v205, v30, v210\n"
      "v_fma_f32 v211, v205, v31, v211\n"
      "v_fma_f32 v216, v205, v32, v216\n"
      "v_fma_f32 v217, v205, v33, v217\n"
      "v_fma_f32 v218, v205, v34, v218\n"
      "v_fma_f32 v219, v205, v35, v219\n"
      "s_mov_b64 exec, -1\n"
      "v_cmp_eq_u32 vcc, -16, v24\n"
      "s_nop 1\n"
      "s_mov_b64 exec, vcc\n"
      "v_fma_f32 v212, v207, v28, v212\n"
      "v_fma_f32 v213, v207, v29, v213\n"
      "v_fma_f32 v214, v207, v30, v214\n"
      "v_fma_f32 v215, v207, v31, v215\n"
      "v_fma_f32 v220, v207, v32, v220\n"
      "v_fma_f32 v221, v207, v33, v221\n"
      "v_fma_f32 v222, v207, v34, v222\n"
      "v_fma_f32 v223, v207, v35, v223\n"
      "s_mov_b64 exec, -1\n"
      ".Lp_e31_%=:\n"
      "s_waitcnt vmcnt(0)\n"
      "v_bitop3_b32 v82, v75, v79, s37 bitop3:0x7e\n"
      "v_bitop3_b32 v82, v82, v83, s37 bitop3:0xf6\n"
      "v_cmp_ne_u32 vcc, 0, v82\n"
      "s_nop 1\n"
      "s_cbranch_vccnz .Lp_s33_%=\n"
      ".Lp_o34_%=:\n"
      "v_mov_b32 v75, v80\n"
      "v_mov_b32 v79, v81\n"
      "s_nop 0\n"
      "v_mfma_f32_16x16x32_bf16 v[64:67], v[252:255], v[72:75], v[64:67]\n"
      "v_mfma_f32_16x16x32_bf16 v[96:99], v[252:255], v[76:79], v[96:99]\n"
      "v_mfma_f32_16x16x32_bf16 v[68:71], v[248:251], v[72:75], v[68:71]\n"
      "v_mfma_f32_16x16x32_bf16 v[100:103], v[248:251], v[76:79], v[100:103]\n"
      "s_nop 4\n"
      "v_fma_f32 v208, v204, v64, v208\n"
      "v_fma_f32 v209, v204, v65, v209\n"
      "v_fma_f32 v210, v204, v66, v210\n"
      "v_fma_f32 v211, v204, v67, v211\n"
      "v_fma_f32 v216, v204, v96, v216\n"
      "v_fma_f32 v217, v204, v97, v217\n"
      "v_fma_f32 v218, v204, v98, v218\n"
      "v_fma_f32 v219, v204, v99, v219\n"
      "v_cvt_pk_bf16_f32 v104, v208, v209\n"
      "v_cvt_pk_bf16_f32 v105, v210, v211\n"
      "v_cvt_pk_bf16_f32 v106, v216, v217\n"
      "v_cvt_pk_bf16_f32 v107, v218, v219\n"
      "global_store_dwordx4 v7, v[104:107], s[20:21]\n"
      "v_fma_f32 v212, v206, v68, v212\n"
      "v_fma_f32 v213, v206, v69, v213\n"
      "v_fma_f32 v214, v206, v70, v214\n"
      "v_fma_f32 v215, v206, v71, v215\n"
      "v_fma_f32 v220, v206, v100, v220\n"
      "v_fma_f32 v221, v206, v101, v221\n"
      "v_fma_f32 v222, v206, v102, v222\n"
      "v_fma_f32 v223, v206, v103, v223\n"
      "v_cvt_pk_bf16_f32 v108, v212, v213\n"
      "v_cvt_pk_bf16_f32 v109, v214, v215\n"
      "v_cvt_pk_bf16_f32 v110, v220, v221\n"
      "v_cvt_pk_bf16_f32 v111, v222, v223\n"
      "global_store_dwordx4 v7, v[108:111], s[68:69]\n"
      "s_endpgm\n"
      ".Lp_s2_%=:\n"
      "s_cmp_eq_u32 s38, 0\n"
      "s_cbranch_scc1 .Lp_o3_%=\n"
      "s_sub_u32 s38, s38, 1\n"
      "s_sleep 4\n"
      "s_nop 7\n"
      "global_load_dword v252, v253, s[58:59] offset:0 sc1\n"
      "s_waitcnt vmcnt(0)\n"
      "v_cmp_ne_u32 vcc, s37, v252\n"
      "s_nop 1\n"
      "s_cbranch_vccnz .Lp_s2_%=\n"
      "s_nop 7\n"
      "s_branch .Lp_o3_%=\n"
      ".Lp_s5_%=:\n"
      "s_cmp_eq_u32 s38, 0\n"
      "s_cbranch_scc1 .Lp_o6_%=\n"
      "s_sub_u32 s38, s38, 1\n"
      "s_sleep 4\n"
      "s_nop 7\n"
      "global_load_dword v252, v253, s[58:59] offset:128 sc1\n"
      "s_waitcnt vmcnt(0)\n"
      "v_cmp_ne_u32 vcc, s37, v252\n"
      "s_nop 1\n"
      "s_cbranch_vccnz .Lp_s5_%=\n"
      "s_nop 7\n"
      "s_branch .Lp_o6_%=\n"
      ".Lp_s8_%=:\n"
      "s_cmp_eq_u32 s38, 0\n"
      "s_cbranch_scc1 .Lp_o9_%=\n"
      "s_sub_u32 s38, s38, 1\n"
      "s_sleep 2\n"
      "s_nop 7\n"
      "global_load_dwordx2 v[202:203], v200, s[58:59] sc1\n"
      "s_waitcnt vmcnt(0)\n"
      "v_cmp_ne_u32 vcc, s37, v203\n"
      "s_nop 1\n"
      "s_cbranch_vccnz .Lp_s8_%=\n"
      "s_nop 7\n"
      "s_branch .Lp_o9_%=\n"
      ".Lp_s13_%=:\n"
      "s_cmp_eq_u32 s38, 0\n"
      "s_cbranch_scc1 .Lp_o14_%=\n"
      "s_sub_u32 s38, s38, 1\n"
      "s_sleep 8\n"
      "s_nop 7\n"
      "global_load_dwordx4 v[224:227], v200, s[58:59] offset:0 sc1\n"
      "global_load_dwordx4 v[228:231], v200, s[58:59] offset:1024 sc1\n"
      "global_load_dwordx4 v[232:235], v200, s[58:59] offset:2048 sc1\n"
      "global_load_dwordx4 v[236:239], v200, s[58:59] offset:3072 sc1\n"
      "s_waitcnt vmcnt(0)\n"
      "v_bitop3_b32 v192, v225, v227, s37 bitop3:0x7e\n"
      "v_bitop3_b32 v192, v192, v229, s37 bitop3:0xf6\n"
      "v_bitop3_b32 v192, v192, v231, s37 bitop3:0xf6\n"
      "v_bitop3_b32 v192, v192, v233, s37 bitop3:0xf6\n"
      "v_bitop3_b32 v192, v192, v235, s37 bitop3:0xf6\n"
      "v_bitop3_b32 v192, v192, v237, s37 bitop3:0xf6\n"
      "v_bitop3_b32 v192, v192, v239, s37 bitop3:0xf6\n"
      "v_cmp_ne_u32 vcc, 0, v192\n"
      "s_nop 1\n"
      "s_cbranch_vccnz .Lp_s13_%=\n"
      "s_nop 7\n"
      "s_branch .Lp_o14_%=\n"
      ".Lp_s16_%=:\n"
      "s_cmp_eq_u32 s38, 0\n"
      "s_cbranch_scc1 .Lp_o17_%=\n"
      "s_sub_u32 s38, s38, 1\n"
      "s_sleep 1\n"
      "s_nop 7\n"
      "global_load_dwordx2 v[204:205], v209, s[58:59] offset:0 sc1\n"
      "global_load_dwordx2 v[206:207], v209, s[58:59] offset:256 sc1\n"
      "s_waitcnt vmcnt(0)\n"
      "v_bitop3_b32 v210, v205, v207, s37 bitop3:0x7e\n"
      "v_cmp_ne_u32 vcc, 0, v210\n"
      "s_nop 1\n"
      "s_cbranch_vccnz .Lp_s16_%=\n"
      "s_nop 7\n"
      "s_branch .Lp_o17_%=\n"
      ".Lp_s19_%=:\n"
      "s_cmp_eq_u32 s38, 0\n"
      "s_cbranch_scc1 .Lp_o20_%=\n"
      "s_sub_u32 s38, s38, 1\n"
      "s_nop 7\n"
      "global_load_dwordx4 v[12:15], v4, s[62:63] offset:0 sc1\n"
      "global_load_dwordx4 v[16:19], v4, s[62:63] offset:1024 sc1\n"
      "global_load_dwordx4 v[20:23], v4, s[62:63] offset:2048 sc1\n"
      "s_waitcnt vmcnt(0)\n"
      "v_bitop3_b32 v22, v15, v19, s37 bitop3:0x7e\n"
      "v_bitop3_b32 v22, v22, v23, s37 bitop3:0xf6\n"
      "v_cmp_ne_u32 vcc, 0, v22\n"
      "s_nop 1\n"
      "s_cbranch_vccnz .Lp_s19_%=\n"
      "s_nop 7\n"
      "s_branch .Lp_o20_%=\n"
      ".Lp_s24_%=:\n"
      "s_cmp_eq_u32 s38, 0\n"
      "s_cbranch_scc1 .Lp_o25_%=\n"
      "s_sub_u32 s38, s38, 1\n"
      "s_sleep 2\n"
      "s_nop 7\n"
      "global_load_dwordx4 v[24:27], v60, s[58:59] offset:0 sc1\n"
      "global_load_dwordx4 v[28:31], v60, s[58:59] offset:1024 sc1\n"
      "global_load_dwordx4 v[32:35], v60, s[58:59] offset:2048 sc1\n"
      "global_load_dwordx4 v[36:39], v60, s[58:59] offset:3072 sc1\n"
      "s_waitcnt vmcnt(0)\n"
      "v_bitop3_b32 v61, v25, v27, s37 bitop3:0x7e\n"
      "v_bitop3_b32 v61, v61, v29, s37 bitop3:0xf6\n"
      "v_bitop3_b32 v61, v61, v31, s37 bitop3:0xf6\n"
      "v_bitop3_b32 v61, v61, v33, s37 bitop3:0xf6\n"
      "v_bitop3_b32 v61, v61, v35, s37 bitop3:0xf6\n"
      "v_bitop3_b32 v61, v61, v37, s37 bitop3:0xf6\n"
      "v_bitop3_b32 v61, v61, v39, s37 bitop3:0xf6\n"
      "v_cmp_ne_u32 vcc, 0, v61\n"
      "s_nop 1\n"
      "s_cbranch_vccnz .Lp_s24_%=\n"
      "s_nop 7\n"
      "s_branch .Lp_o25_%=\n"
      ".Lp_s29_%=:\n"
      "s_cmp_eq_u32 s38, 0\n"
      "s_cbranch_scc1 .Lp_o30_%=\n"
      "s_sub_u32 s38, s38, 1\n"
      "s_nop 7\n"
      "global_load_dwordx4 v[44:47], v5, s[62:63] offset:0 sc1\n"
      "global_load_dwordx4 v[48:51], v5, s[62:63] offset:1024 sc1\n"
      "global_load_dwordx4 v[52:55], v5, s[62:63] offset:2048 sc1\n"
      "s_waitcnt vmcnt(0)\n"
      "v_bitop3_b32 v54, v47, v51, s37 bitop3:0x7e\n"
      "v_bitop3_b32 v54, v54, v55, s37 bitop3:0xf6\n"
      "v_cmp_ne_u32 vcc, 0, v54\n"
      "s_nop 1\n"
      "s_cbranch_vccnz .Lp_s29_%=\n"
      "s_nop 7\n"
      "s_branch .Lp_o30_%=\n"
      ".Lp_s33_%=:\n"
      "s_cmp_eq_u32 s38, 0\n"
      "s_cbranch_scc1 .Lp_o34_%=\n"
      "s_sub_u32 s38, s38, 1\n"
      "s_nop 7\n"
      "global_load_dwordx4 v[72:75], v6, s[62:63] offset:0 sc1\n"
      "global_load_dwordx4 v[76:79], v6, s[62:63] offset:1024 sc1\n"
      "global_load_dwordx4 v[80:83], v6, s[62:63] offset:2048 sc1\n"
      "s_waitcnt vmcnt(0)\n"
      "v_bitop3_b32 v82, v75, v79, s37 bitop3:0x7e\n"
      "v_bitop3_b32 v82, v82, v83, s37 bitop3:0xf6\n"
      "v_cmp_ne_u32 vcc, 0, v82\n"
      "s_nop 1\n"
      "s_cbranch_vccnz .Lp_s33_%=\n"
      "s_nop 7\n"
      "s_branch .Lp_o34_%=\n"
      ".Ls_s3_%=:\n"
      "s_cmp_eq_u32 s38, 0\n"
      "s_cbranch_scc1 .Ls_o4_%=\n"
      "s_sub_u32 s38, s38, 1\n"
      "s_sleep 4\n"
      "s_nop 7\n"
      "global_load_dword v252, v253, s[58:59] offset:0 sc1\n"
      "s_waitcnt vmcnt(0)\n"
      "v_cmp_ne_u32 vcc, s37, v252\n"
      "s_nop 1\n"
      "s_cbranch_vccnz .Ls_s3_%=\n"
      "s_nop 7\n"
      "s_branch .Ls_o4_%=\n"
      ".Ls_s6_%=:\n"
      "s_cmp_eq_u32 s38, 0\n"
      "s_cbranch_scc1 .Ls_o7_%=\n"
      "s_sub_u32 s38, s38, 1\n"
      "s_sleep 2\n"
      "s_nop 7\n"
      "global_load_dwordx2 v[206:207], v204, s[58:59] sc1\n"
      "s_waitcnt vmcnt(0)\n"
      "v_cmp_ne_u32 vcc, s37, v207\n"
      "s_nop 1\n"
      "s_cbranch_vccnz .Ls_s6_%=\n"
      "s_nop 7\n"
      "s_branch .Ls_o7_%=\n"
      ".Ls_s10_%=:\n"
      "s_cmp_eq_u32 s38, 0\n"
      "s_cbranch_scc1 .Ls_o11_%=\n"
      "s_sub_u32 s38, s38, 1\n"
      "s_sleep 4\n"
      "s_nop 7\n"
      "global_load_dword v252, v253, s[58:59] offset:128 sc1\n"
      "s_waitcnt vmcnt(0)\n"
      "v_cmp_ne_u32 vcc, s37, v252\n"
      "s_nop 1\n"
      "s_cbranch_vccnz .Ls_s10_%=\n"
      "s_nop 7\n"
      "s_branch .Ls_o11_%=\n"
      ".Ls_s13_%=:\n"
      "s_cmp_eq_u32 s38, 0\n"
      "s_cbranch_scc1 .Ls_o14_%=\n"
      "s_sub_u32 s38, s38, 1\n"
      "s_sleep 1\n"
      "s_nop 7\n"
      "global_load_dwordx2 v[204:205], v209, s[58:59] offset:0 sc1\n"
      "global_load_dwordx2 v[206:207], v209, s[58:59] offset:256 sc1\n"
      "s_waitcnt vmcnt(0)\n"
      "v_bitop3_b32 v210, v205, v207, s37 bitop3:0x7e\n"
      "v_cmp_ne_u32 vcc, 0, v210\n"
      "s_nop 1\n"
      "s_cbranch_vccnz .Ls_s13_%=\n"
      "s_nop 7\n"
      "s_branch .Ls_o14_%=\n"
      ".Ls_s16_%=:\n"
      "s_cmp_eq_u32 s38, 0\n"
      "s_cbranch_scc1 .Ls_o17_%=\n"
      "s_sub_u32 s38, s38, 1\n"
      "s_nop 7\n"
      "global_load_dwordx4 v[12:15], v4, s[62:63] offset:0 sc1\n"
      "global_load_dwordx4 v[16:19], v4, s[62:63] offset:1024 sc1\n"
      "global_load_dwordx4 v[20:23], v4, s[62:63] offset:2048 sc1\n"
      "s_waitcnt vmcnt(0)\n"
      "v_bitop3_b32 v22, v15, v19, s37 bitop3:0x7e\n"
      "v_bitop3_b32 v22, v22, v23, s37 bitop3:0xf6\n"
      "v_cmp_ne_u32 vcc, 0, v22\n"
      "s_nop 1\n"
      "s_cbranch_vccnz .Ls_s16_%=\n"
      "s_nop 7\n"
      "s_branch .Ls_o17_%=\n"
      ".Ls_s21_%=:\n"
      "s_cmp_eq_u32 s38, 0\n"
      "s_cbranch_scc1 .Ls_o22_%=\n"
      "s_sub_u32 s38, s38, 1\n"
      "s_sleep 2\n"
      "s_nop 7\n"
      "global_load_dwordx4 v[24:27], v60, s[58:59] offset:0 sc1\n"
      "global_load_dwordx4 v[28:31], v60, s[58:59] offset:1024 sc1\n"
      "global_load_dwordx4 v[32:35], v60, s[58:59] offset:2048 sc1\n"
      "global_load_dwordx4 v[36:39], v60, s[58:59] offset:3072 sc1\n"
      "s_waitcnt vmcnt(0)\n"
      "v_bitop3_b32 v61, v25, v27, s37 bitop3:0x7e\n"
      "v_bitop3_b32 v61, v61, v29, s37 bitop3:0xf6\n"
      "v_bitop3_b32 v61, v61, v31, s37 bitop3:0xf6\n"
      "v_bitop3_b32 v61, v61, v33, s37 bitop3:0xf6\n"
      "v_bitop3_b32 v61, v61, v35, s37 bitop3:0xf6\n"
      "v_bitop3_b32 v61, v61, v37, s37 bitop3:0xf6\n"
      "v_bitop3_b32 v61, v61, v39, s37 bitop3:0xf6\n"
      "v_cmp_ne_u32 vcc, 0, v61\n"
      "s_nop 1\n"
      "s_cbranch_vccnz .Ls_s21_%=\n"
      "s_nop 7\n"
      "s_branch .Ls_o22_%=\n"
      ".Ls_s26_%=:\n"
      "s_cmp_eq_u32 s38, 0\n"
      "s_cbranch_scc1 .Ls_o27_%=\n"
      "s_sub_u32 s38, s38, 1\n"
      "s_nop 7\n"
      "global_load_dwordx4 v[44:47], v5, s[62:63] offset:0 sc1\n"
      "global_load_dwordx4 v[48:51], v5, s[62:63] offset:1024 sc1\n"
      "global_load_dwordx4 v[52:55], v5, s[62:63] offset:2048 sc1\n"
      "s_waitcnt vmcnt(0)\n"
      "v_bitop3_b32 v54, v47, v51, s37 bitop3:0x7e\n"
      "v_bitop3_b32 v54, v54, v55, s37 bitop3:0xf6\n"
      "v_cmp_ne_u32 vcc, 0, v54\n"
      "s_nop 1\n"
      "s_cbranch_vccnz .Ls_s26_%=\n"
      "s_nop 7\n"
      "s_branch .Ls_o27_%=\n"
      ".Ls_s30_%=:\n"
      "s_cmp_eq_u32 s38, 0\n"
      "s_cbranch_scc1 .Ls_o31_%=\n"
      "s_sub_u32 s38, s38, 1\n"
      "s_nop 7\n"
      "global_load_dwordx4 v[72:75], v6, s[62:63] offset:0 sc1\n"
      "global_load_dwordx4 v[76:79], v6, s[62:63] offset:1024 sc1\n"
      "global_load_dwordx4 v[80:83], v6, s[62:63] offset:2048 sc1\n"
      "s_waitcnt vmcnt(0)\n"
      "v_bitop3_b32 v82, v75, v79, s37 bitop3:0x7e\n"
      "v_bitop3_b32 v82, v82, v83, s37 bitop3:0xf6\n"
      "v_cmp_ne_u32 vcc, 0, v82\n"
      "s_nop 1\n"
      "s_cbranch_vccnz .Ls_s30_%=\n"
      "s_nop 7\n"
      "s_branch .Ls_o31_%=\n"
      ".Lend_%=:\n"
      :
      : "s"(__builtin_amdgcn_kernarg_segment_ptr()), "s"(blockIdx.x), "s"(__builtin_amdgcn_readfirstlane(threadIdx.x >> 6)),
        "s"((unsigned)(size_t)smem)
      : "memory", "scc", "vcc",
        "v0", "v1", "v2", "v3", "v4", "v5", "v6", "v7", "v8", "v9", "v10", "v11", "v12", "v13", "v14", "v15", "v16", "v17", "v18", "v19", "v20", "v21", "v22", "v23", "v24", "v25", "v26", "v27", "v28", "v29", "v30", "v31", "v32", "v33", "v34", "v35", "v36", "v37", "v38", "v39", "v40", "v41", "v42", "v43", "v44", "v45", "v46", "v47", "v48", "v49", "v50", "v51", "v52", "v53", "v54", "v55", "v56", "v57", "v58", "v59", "v60", "v61", "v62", "v63", "v64", "v65", "v66", "v67", "v68", "v69", "v70", "v71", "v72", "v73", "v74", "v75", "v76", "v77", "v78", "v79", "v80", "v81", "v82", "v83", "v84", "v85", "v86", "v87", "v88", "v89", "v90", "v91", "v92", "v93", "v94", "v95", "v96", "v97", "v98", "v99", "v100", "v101", "v102", "v103", "v104", "v105", "v106", "v107", "v108", "v109", "v110", "v111", "v112", "v113", "v114", "v115", "v116", "v117", "v118", "v119", "v120", "v121", "v122", "v123", "v124", "v125", "v126", "v127", "v128", "v129", "v130", "v131", "v132", "v133", "v134", "v135", "v136", "v137", "v138", "v139", "v140", "v141", "v142", "v143", "v144", "v145", "v146", "v147", "v148", "v149", "v150", "v151", "v152", "v153", "v154", "v155", "v156", "v157", "v158", "v159", "v160", "v161", "v162", "v163", "v164", "v165", "v166", "v167", "v168", "v169", "v170", "v171", "v172", "v173", "v174", "v175", "v176", "v177", "v178", "v179", "v180", "v181", "v182", "v183", "v184", "v185", "v186", "v187", "v188", "v189", "v190", "v191", "v192", "v193", "v194", "v195", "v196", "v197", "v198", "v199", "v200", "v201", "v202", "v203", "v204", "v205", "v206", "v207", "v208", "v209", "v210", "v211", "v212", "v213", "v214", "v215", "v216", "v217", "v218", "v219", "v220", "v221", "v222", "v223", "v224", "v225", "v226", "v227", "v228", "v229", "v230", "v231", "v232", "v233", "v234", "v235", "v236", "v237", "v238", "v239", "v240", "v241", "v242", "v243", "v244", "v245", "v246", "v247", "v248", "v249", "v250", "v251", "v252", "v253", "v254", "v255",
        "a0", "a1", "a2", "a3", "a4", "a5", "a6", "a7", "a8", "a9", "a10", "a11", "a12", "a13", "a14", "a15", "a16", "a17", "a18", "a19", "a20", "a21", "a22", "a23", "a24", "a25", "a26", "a27", "a28", "a29", "a30", "a31", "a32", "a33", "a34", "a35", "a36", "a37", "a38", "a39", "a40", "a41", "a42", "a43", "a44", "a45", "a46", "a47", "a48", "a49", "a50", "a51", "a52", "a53", "a54", "a55", "a56", "a57", "a58", "a59", "a60", "a61", "a62", "a63", "a64", "a65", "a66", "a67", "a68", "a69", "a70", "a71", "a72", "a73", "a74", "a75", "a76", "a77", "a78", "a79", "a80", "a81", "a82", "a83", "a84", "a85", "a86", "a87", "a88", "a89", "a90", "a91", "a92", "a93", "a94", "a95", "a96", "a97", "a98", "a99", "a100", "a101", "a102", "a103", "a104", "a105", "a106", "a107", "a108", "a109", "a110", "a111", "a112", "a113", "a114", "a115", "a116", "a117", "a118", "a119", "a120", "a121", "a122", "a123", "a124", "a125", "a126", "a127", "a128", "a129", "a130", "a131", "a132", "a133", "a134", "a135", "a136", "a137", "a138", "a139", "a140", "a141", "a142", "a143", "a144", "a145", "a146", "a147", "a148", "a149", "a150", "a151", "a152", "a153", "a154", "a155", "a156", "a157", "a158", "a159", "a160", "a161", "a162", "a163", "a164", "a165", "a166", "a167", "a168", "a169", "a170", "a171", "a172", "a173", "a174", "a175", "a176", "a177", "a178", "a179", "a180", "a181", "a182", "a183", "a184", "a185", "a186", "a187", "a188", "a189", "a190", "a191", "a192", "a193", "a194", "a195", "a196", "a197", "a198", "a199", "a200", "a201", "a202", "a203", "a204", "a205", "a206", "a207", "a208", "a209", "a210", "a211", "a212", "a213", "a214", "a215", "a216", "a217", "a218", "a219", "a220", "a221", "a222", "a223", "a224", "a225", "a226", "a227", "a228", "a229", "a230", "a231", "a232", "a233", "a234", "a235", "a236", "a237", "a238", "a239", "a240", "a241", "a242", "a243", "a244", "a245", "a246", "a247", "a248", "a249", "a250", "a251", "a252", "a253", "a254", "a255",
        "s8", "s9", "s10", "s11", "s12", "s13", "s14", "s15", "s16", "s17", "s18", "s19", "s20", "s21", "s22", "s23", "s24", "s25", "s26", "s27", "s28", "s29", "s30", "s31", "s32", "s33", "s34", "s35", "s36", "s37", "s38", "s39", "s40", "s41", "s42", "s43", "s44", "s45", "s46", "s47", "s48", "s49", "s50", "s51", "s52", "s53", "s54", "s55", "s56", "s57", "s58", "s59", "s60", "s61", "s62", "s63", "s64", "s65", "s66", "s67", "s68", "s69", "s70", "s71", "s72", "s73", "s74", "s75", "s76", "s77", "s78", "s79", "s80", "s81", "s82", "s83", "s84", "s85", "s86", "s87");
}

extern "C" void kernel_launch(void* const* d_in, const int* in_sizes, int n_in, void* d_out, int out_size, void* d_ws, size_t ws_size,
                              hipStream_t stream) {
  fused_k<<<256, 256, 0, stream>>>((const u16*)d_in[1], (const u16*)d_in[2], (const u16*)d_in[3], (const u16*)d_in[4], (const u16*)d_in[0],
                                   (char*)d_ws, (u16*)d_out, (const u16*)d_in[6], (const u16*)d_in[10], (const u16*)d_in[7],
                                   (const u16*)d_in[8], (const u16*)d_in[9]);
}
```

```cpp
#include <hip/hip_runtime.h>

typedef unsigned short u16;

__global__ __launch_bounds__(256, 1) void fused_k(const u16* wsre, const u16* wsim, const u16* wdre, const u16* wdim, const u16* x, char* ws,
                                               u16* out, const u16* w_r, const u16* gate_w, const u16* w_gate, const u16* w_up,
                                               const u16* w_down) {
  __shared__ __attribute__((aligned(16))) char smem[163840];
  asm volatile(
      ".Lbegin_%=:\n"
      "s_load_dwordx16 s[8:23], %0, 0\n"
      "s_load_dwordx8 s[24:31], %0, 64\n"
      "v_mbcnt_lo_u32_b32 v243, -1, 0\n"
      "s_mov_b32 s32, %1\n"
      "v_mbcnt_hi_u32_b32 v243, -1, v243\n"
      "s_mov_b32 s33, %2\n"
      "s_bfe_u32 s34, s32, 0x20001\n"
      "s_bfe_u32 s35, s32, 0x20003\n"
      "v_lshrrev_b32 v236, 3, v243\n"
      "s_lshr_b32 s43, s32, 5\n"
      "v_xor_b32 v237, v243, v236\n"
      "s_and_b32 s36, s32, 1\n"
      "v_and_b32 v237, 7, v237\n"
      "s_lshl3_add_u32 s36, s36, s43\n"
      "v_lshlrev_b32 v237, 4, v237\n"
      "s_add_u32 s44, s43, s35\n"
      "v_lshlrev_b32 v242, 4, v243\n"
      "s_lshl_b32 s41, s33, 14\n"
      "s_bitcmp1_b32 s44, 0\n"
      "s_cbranch_scc1 .Lstream_%=\n"
      "v_and_b32 v238, 3, v236\n"
      "s_lshr_b32 s43, s43, 1\n"
      "v_and_b32 v239, 4, v236\n"
      "s_add_u32 s80, s43, s35\n"
      "v_lshl_or_b32 v238, v239, 1, v238\n"
      "s_lshl_b32 s80, s80, 8\n"
      "v_lshl_or_b32 v240, v238, 10, v237\n"
      "s_and_b32 s80, s80, 0x300\n"
      "v_add_u32 v241, 15360, v240\n"
      "s_lshr_b32 s83, s36, 1\n"
      "v_add_u32 v254, 2048, v240\n"
      "s_and_b32 s44, s34, 1\n"
      "v_add_u32 v255, 17408, v240\n"
      "s_lshl3_add_u32 s44, s44, s83\n"
      "s_lshl2_add_u32 s40, s44, s33\n"
      "s_lshl_b32 s44, s40, 15\n"
      "s_lshl_b32 s81, s33, 8\n"
      "s_add_u32 s81, s81, s80\n"
      "s_and_b32 s81, s81, 0x380\n"
      "s_lshl_b32 s45, s35, 17\n"
      "s_lshl_b32 s82, s33, 12\n"
      "s_add_u32 s76, s34, 1\n"
      "s_and_b32 s76, s76, 3\n"
      "s_lshl_b32 s76, s76, 15\n"
      "s_or_b32 s76, s76, s81\n"
      "s_add_u32 s76, s76, s45\n"
      "s_add_u32 s77, s34, 2\n"
      "s_and_b32 s77, s77, 3\n"
      "s_lshl_b32 s77, s77, 15\n"
      "s_or_b32 s77, s77, s81\n"
      "s_add_u32 s77, s77, s45\n"
      "s_add_u32 s78, s44, s80\n"
      "s_add_u32 s79, s80, 256\n"
      "s_and_b32 s79, s79, 0x300\n"
      "s_add_u32 s79, s79, s44\n"
      "s_add_u32 m0, s82, 65536\n"
      "s_bitcmp1_b32 s34, 1\n"
      "s_waitcnt lgkmcnt(0)\n"
      "s_cselect_b64 s[12:13], s[14:15], s[12:13]\n"
      "s_add_u32 s46, s12, s76\n"
      "s_addc_u32 s47, s13, 0\n"
      "global_load_lds_dwordx4 v240, s[46:47] offset:0\n"
      "global_load_lds_dwordx4 v241, s[46:47] offset:1024\n"
      "global_load_lds_dwordx4 v254, s[46:47] offset:2048\n"
      "global_load_lds_dwordx4 v255, s[46:47] offset:3072\n"
      "s_add_u32 s70, s16, s78\n"
      "s_addc_u32 s71, s17, 0\n"
      "s_mov_b32 m0, s41\n"
      "s_add_u32 s46, s12, s77\n"
      "s_addc_u32 s47, s13, 0\n"
      "global_load_lds_dwordx4 v240, s[70:71] offset:0\n"
      "global_load_lds_dwordx4 v241, s[70:71] offset:1024\n"
      "global_load_lds_dwordx4 v254, s[70:71] offset:2048\n"
      "global_load_lds_dwordx4 v255, s[70:71] offset:3072\n"
      "s_add_u32 m0, s82, 98304\n"
      "s_nop 0\n"
      "global_load_lds_dwordx4 v240, s[46:47] offset:0\n"
      "global_load_lds_dwordx4 v241, s[46:47] offset:1024\n"
      "s_add_u32 s70, s16, s79\n"
      "s_addc_u32 s71, s17, 0\n"
      "s_add_u32 m0, s41, 4096\n"
      "s_nop 0\n"
      "global_load_lds_dwordx4 v240, s[70:71] offset:0\n"
      "global_load_lds_dwordx4 v241, s[70:71] offset:1024\n"
      "global_load_lds_dwordx4 v254, s[70:71] offset:2048\n"
      "global_load_lds_dwordx4 v255, s[70:71] offset:3072\n"
      "s_branch .Lprod_%=\n"
      ".Lstream_%=:\n"
      "v_lshl_or_b32 v244, v236, 12, v237\n"
      "v_add_u32 v245, 31744, v244\n"
      "v_add_u32 v246, 63488, v244\n"
      "v_add_u32 v247, 95232, v244\n"
      "s_getpc_b64 s[46:47]\n"
      ".Lhere_%=:\n"
      "s_lshr_b32 s44, s32, 4\n"
      "s_lshl2_add_u32 s44, s44, s33\n"
      "s_lshl_b32 s44, s44, 10\n"
      "s_sub_u32 s46, s46, .Lhere_%=-.Lbegin_%=\n"
      "s_subb_u32 s47, s47, 0\n"
      "v_add_u32 v238, s44, v242\n"
      "v_min_u32 v238, .Lend_%=-.Lbegin_%=-16, v238\n"
      "global_load_dwordx4 v[192:195], v238, s[46:47]\n"
      "s_and_b32 s44, s34, 1\n"
      "s_lshr_b32 s45, s36, 1\n"
      "s_lshl3_add_u32 s44, s44, s45\n"
      "s_lshl2_add_u32 s44, s44, s35\n"
      "s_lshl2_add_u32 s44, s44, s33\n"
      "s_lshl_b32 s44, s44, 13\n"
      "s_add_u32 s44, s44, 128\n"
      "v_and_b32 v233, 31, v243\n"
      "v_lshl_add_u32 v233, v233, 8, s44\n"
      "s_lshr_b32 s44, s33, 1\n"
      "s_add_u32 s44, s44, s34\n"
      "s_add_u32 s44, s44, 1\n"
      "s_and_b32 s44, s44, 3\n"
      "s_lshl2_add_u32 s44, s35, s44\n"
      "s_lshr_b32 s45, s32, 6\n"
      "s_lshl2_add_u32 s44, s44, s45\n"
      "s_and_b32 s45, s33, 1\n"
      "s_lshl1_add_u32 s44, s44, s45\n"
      "s_lshl_b32 s44, s44, 12\n"
      "s_add_u32 s44, s44, 128\n"
      "v_and_b32 v234, 15, v243\n"
      "v_lshl_add_u32 v234, v234, 8, s44\n"
      "s_bitcmp1_b32 s34, 1\n"
      "s_waitcnt lgkmcnt(0)\n"
      "s_cselect_b64 s[68:69], s[14:15], s[12:13]\n"
      "s_bitcmp1_b32 s34, 1\n"
      "s_cselect_b64 s[8:9], s[10:11], s[8:9]\n"
      "s_and_b32 s45, s34, 1\n"
      "s_lshl2_add_u32 s45, s45, s33\n"
      "s_lshl_b32 s45, s45, 9\n"
      "s_lshl_b32 s76, s36, 19\n"
      "s_add_u32 s45, s45, s76\n"
      "s_add_u32 s8, s8, s45\n"
      "s_addc_u32 s9, s9, 0\n"
      "s_lshr_b32 s39, s32, 6\n"
      "s_and_b32 s45, s35, 2\n"
      "s_xor_b32 s39, s39, s45\n"
      "s_xor_b32 s39, s39, s33\n"
      "s_lshl_b32 s45, s39, 7\n"
      "s_add_u32 s76, s33, 0\n"
      "s_and_b32 s76, s76, 3\n"
      "s_lshl_b32 s76, s76, 17\n"
      "s_or_b32 s76, s76, s45\n"
      "s_add_u32 s77, s33, 1\n"
      "s_and_b32 s77, s77, 3\n"
      "s_lshl_b32 s77, s77, 17\n"
      "s_or_b32 s77, s77, s45\n"
      "s_add_u32 s78, s33, 2\n"
      "s_and_b32 s78, s78, 3\n"
      "s_lshl_b32 s78, s78, 17\n"
      "s_or_b32 s78, s78, s45\n"
      "s_add_u32 s79, s33, 3\n"
      "s_and_b32 s79, s79, 3\n"
      "s_lshl_b32 s79, s79, 17\n"
      "s_or_b32 s79, s79, s45\n"
      "v_mov_b32 v239, 0\n"
      "global_load_dword v199, v239, s[8:9]\n"
      "s_add_u32 s58, s18, 4718592\n"
      "s_addc_u32 s59, s19, 0\n"
      "global_load_dword v235, v239, s[58:59] sc1\n"
      "s_lshl2_add_u32 s43, s34, s35\n"
      "s_mul_i32 s43, s43, 266240\n"
      "s_add_u32 s43, s43, 262144\n"
      "s_lshl2_add_u32 s44, s36, s35\n"
      "s_mul_i32 s44, s44, 147712\n"
      "s_add_u32 s44, s44, 6438912\n"
      "s_bitcmp1_b32 s33, 0\n"
      "s_cselect_b32 s43, s43, s44\n"
      "s_cselect_b64 s[46:47], s[22:23], s[24:25]\n"
      "s_cselect_b64 s[70:71], s[28:29], s[26:27]\n"
      "s_lshl_b32 s44, s36, 17\n"
      "s_mul_i32 s45, s32, 7168\n"
      "s_bitcmp1_b32 s33, 1\n"
      "s_cselect_b64 s[46:47], s[46:47], s[18:19]\n"
      "s_cselect_b32 s43, 0, s43\n"
      "s_cselect_b32 s84, 0x380, 0\n"
      "v_lshlrev_b32 v238, 7, v243\n"
      "v_and_b32 v238, s84, v238\n"
      "v_add_u32 v238, s43, v238\n"
      "global_load_dword v197, v238, s[46:47] sc1\n"
      "s_cmp_eq_u32 s33, 2\n"
      "s_cselect_b64 s[70:71], s[30:31], s[70:71]\n"
      "s_cmp_eq_u32 s33, 3\n"
      "s_cselect_b64 s[70:71], s[20:21], s[70:71]\n"
      "s_cselect_b32 s44, s44, s45\n"
      "v_mov_b32 v239, s44\n"
      "global_load_dword v198, v239, s[70:71] sc1\n"
      "s_sleep 8\n"
      "global_load_dword v200, v234, s[68:69]\n"
      "global_load_dword v201, v233, s[16:17]\n"
      "s_sleep 4\n"
      "s_bitcmp1_b32 s34, 1\n"
      "s_cselect_b64 s[46:47], s[14:15], s[12:13]\n"
      "s_min_u32 s44, s33, 2\n"
      "s_add_u32 s44, s44, s34\n"
      "s_add_u32 s44, s44, 2\n"
      "s_and_b32 s44, s44, 3\n"
      "s_lshl2_add_u32 s44, s35, s44\n"
      "s_lshr_b32 s45, s32, 6\n"
      "s_lshl2_add_u32 s44, s44, s45\n"
      "s_lshl_b32 s44, s44, 13\n"
      "v_lshl_add_u32 v238, v243, 7, s44\n"
      "s_cmp_eq_u32 s33, 0\n"
      "s_cselect_b32 s44, 4096, 0\n"
      "v_or_b32 v238, s44, v238\n"
      "s_cmp_eq_u32 s33, 3\n"
      "s_cbranch_scc1 .Ls_e1_%=\n"
      "global_load_dword v196, v238, s[46:47]\n"
      ".Ls_e1_%=:\n"
      "s_xor_b32 s45, s76, 0\n"
      "s_add_u32 s46, s8, s45\n"
      "s_addc_u32 s47, s9, 0\n"
      "s_add_u32 m0, s41, 0\n"
      "s_nop 0\n"
      "global_load_lds_dwordx4 v244, s[46:47] offset:0\n"
      "global_load_lds_dwordx4 v245, s[46:47] offset:1024\n"
      "global_load_lds_dwordx4 v246, s[46:47] offset:2048\n"
      "global_load_lds_dwordx4 v247, s[46:47] offset:3072\n"
      "s_xor_b32 s45, s77, 0\n"
      "s_add_u32 s46, s8, s45\n"
      "s_addc_u32 s47, s9, 0\n"
      "s_add_u32 m0, s41, 4096\n"
      "s_nop 0\n"
      "global_load_lds_dwordx4 v244, s[46:47] offset:0\n"
      "global_load_lds_dwordx4 v245, s[46:47] offset:1024\n"
      "global_load_lds_dwordx4 v246, s[46:47] offset:2048\n"
      "global_load_lds_dwordx4 v247, s[46:47] offset:3072\n"
      "s_xor_b32 s45, s78, 0\n"
      "s_add_u32 s46, s8, s45\n"
      "s_addc_u32 s47, s9, 0\n"
      "s_add_u32 m0, s41, 8192\n"
      "s_nop 0\n"
      "global_load_lds_dwordx4 v244, s[46:47] offset:0\n"
      "global_load_lds_dwordx4 v245, s[46:47] offset:1024\n"
      "global_load_lds_dwordx4 v246, s[46:47] offset:2048\n"
      "global_load_lds_dwordx4 v247, s[46:47] offset:3072\n"
      "v_and_b32 v238, 15, v243\n"
      "v_lshrrev_b32 v239, 4, v243\n"
      "v_xor_b32 v239, v239, v238\n"
      "v_and_b32 v239, 7, v239\n"
      "v_lshlrev_b32 v239, 4, v239\n"
      "v_lshl_or_b32 v238, v238, 7, v239\n"
      "v_add_u32 v248, s41, v238\n"
      "v_xor_b32 v249, 64, v248\n"
      "s_lshl2_add_u32 s43, s34, s35\n"
      "s_lshr_b32 s45, s39, 1\n"
      "s_lshl1_add_u32 s45, s33, s45\n"
      "s_lshl_b32 s45, s45, 15\n"
      "s_mul_i32 s44, s43, 266240\n"
      "s_add_u32 s44, s44, 4096\n"
      "s_add_u32 s14, s18, s44\n"
      "s_addc_u32 s15, s19, 0\n"
      "s_add_u32 s10, s14, s45\n"
      "s_addc_u32 s11, s15, 0\n"
      "s_xor_b32 s45, s45, 0x8000\n"
      "s_add_u32 s14, s14, s45\n"
      "s_addc_u32 s15, s15, 0\n"
      "s_and_b32 s44, s39, 1\n"
      "s_lshl_b32 s45, s44, 14\n"
      "v_add_u32 v240, s45, v242\n"
      "v_xor_b32 v241, 0x4000, v240\n"
      "s_add_u32 s72, s10, 0x2000\n"
      "s_addc_u32 s73, s11, 0\n"
      "s_add_u32 s74, s14, 0x2000\n"
      "s_addc_u32 s75, s15, 0\n"
      "s_lshl2_add_u32 s44, s43, s33\n"
      "s_lshl_b32 s44, s44, 12\n"
      "s_add_u32 s44, s44, 1048576\n"
      "v_and_b32 v253, 7, v243\n"
      "v_lshl_add_u32 v253, v253, 9, s44\n"
      "s_lshl2_add_u32 s43, s36, s35\n"
      "s_mul_i32 s43, s43, 147712\n"
      "s_mul_i32 s44, s33, 3072\n"
      "s_add_u32 s43, s43, s44\n"
      "s_add_u32 s43, s43, 6291456\n"
      "s_add_u32 s60, s18, s43\n"
      "s_addc_u32 s61, s19, 0\n"
      "s_mul_i32 s44, s34, 36864\n"
      "s_add_u32 s62, s60, s44\n"
      "s_addc_u32 s63, s61, 0\n"
      "s_lshr_b32 s43, s34, 1\n"
      "s_lshl2_add_u32 s43, s43, s35\n"
      "s_lshl_b32 s43, s43, 12\n"
      "s_add_u32 s43, s43, 4096\n"
      "v_lshl_add_u32 v204, v243, 6, s43\n"
      "s_lshr_b32 s80, s32, 4\n"
      "s_bfe_u32 s43, s32, 0x10002\n"
      "s_lshl1_add_u32 s80, s80, s43\n"
      "s_and_b32 s81, s32, 3\n"
      "s_cmp_lt_u32 s80, 28\n"
      "s_cselect_b32 s86, -1, 0\n"
      "s_lshr_b32 s83, s33, 1\n"
      "s_lshl1_add_u32 s83, s81, s83\n"
      "v_add_u32 v250, 0x10000, v248\n"
      "v_add_u32 v251, 0x10000, v249\n"
      "s_xor_b32 s45, s79, 0\n"
      "s_add_u32 s46, s8, s45\n"
      "s_addc_u32 s47, s9, 0\n"
      "s_add_u32 m0, s41, 12288\n"
      "s_nop 0\n"
      "global_load_lds_dwordx4 v244, s[46:47] offset:0\n"
      "global_load_lds_dwordx4 v245, s[46:47] offset:1024\n"
      "global_load_lds_dwordx4 v246, s[46:47] offset:2048\n"
      "global_load_lds_dwordx4 v247, s[46:47] offset:3072\n"
      "s_xor_b32 s45, s76, 128\n"
      "s_add_u32 s46, s8, s45\n"
      "s_addc_u32 s47, s9, 0\n"
      "s_add_u32 m0, s41, 65536\n"
      "s_nop 0\n"
      "global_load_lds_dwordx4 v244, s[46:47] offset:0\n"
      "global_load_lds_dwordx4 v245, s[46:47] offset:1024\n"
      "global_load_lds_dwordx4 v246, s[46:47] offset:2048\n"
      "global_load_lds_dwordx4 v247, s[46:47] offset:3072\n"
      "s_waitcnt vmcnt(16)\n"
      "ds_read_b128 a[0:3], v248 offset:0\n"
      "ds_read_b128 a[4:7], v248 offset:2048\n"
      "ds_read_b128 a[32:35], v249 offset:0\n"
      "ds_read_b128 a[36:39], v249 offset:2048\n"
      "s_xor_b32 s45, s77, 128\n"
      "s_add_u32 s46, s8, s45\n"
      "s_addc_u32 s47, s9, 0\n"
      "s_add_u32 m0, s41, 69632\n"
      "s_nop 0\n"
      "global_load_lds_dwordx4 v244, s[46:47] offset:0\n"
      "global_load_lds_dwordx4 v245, s[46:47] offset:1024\n"
      "global_load_lds_dwordx4 v246, s[46:47] offset:2048\n"
      "global_load_lds_dwordx4 v247, s[46:47] offset:3072\n"
      "s_waitcnt vmcnt(16)\n"
      "ds_read_b128 a[8:11], v248 offset:4096\n"
      "ds_read_b128 a[12:15], v248 offset:6144\n"
      "ds_read_b128 a[40:43], v249 offset:4096\n"
      "ds_read_b128 a[44:47], v249 offset:6144\n"
      "s_xor_b32 s45, s78, 128\n"
      "s_add_u32 s46, s8, s45\n"
      "s_addc_u32 s47, s9, 0\n"
      "s_add_u32 m0, s41, 73728\n"
      "s_nop 0\n"
      "global_load_lds_dwordx4 v244, s[46:47] offset:0\n"
      "global_load_lds_dwordx4 v245, s[46:47] offset:1024\n"
      "global_load_lds_dwordx4 v246, s[46:47] offset:2048\n"
      "global_load_lds_dwordx4 v247, s[46:47] offset:3072\n"
      "s_waitcnt vmcnt(16)\n"
      "ds_read_b128 a[16:19], v248 offset:8192\n"
      "ds_read_b128 a[20:23], v248 offset:10240\n"
      "ds_read_b128 a[48:51], v249 offset:8192\n"
      "ds_read_b128 a[52:55], v249 offset:10240\n"
      "v_readfirstlane_b32 s37, v235\n"
      "s_nop 1\n"
      "s_mul_i32 s37, s37, 0x9E3779B1\n"
      "s_add_u32 s37, s37, 0x7F4A7C15\n"
      "s_mov_b32 s38, 0x8000\n"
      "s_waitcnt lgkmcnt(8)\n"
      "s_xor_b32 s45, s79, 128\n"
      "s_add_u32 s46, s8, s45\n"
      "s_addc_u32 s47, s9, 0\n"
      "s_add_u32 m0, s41, 0\n"
      "s_nop 0\n"
      "global_load_lds_dwordx4 v244, s[46:47] offset:0\n"
      "global_load_lds_dwordx4 v245, s[46:47] offset:1024\n"
      "global_load_lds_dwordx4 v246, s[46:47] offset:2048\n"
      "global_load_lds_dwordx4 v247, s[46:47] offset:3072\n"
      "s_barrier\n"
      "s_waitcnt vmcnt(16)\n"
      "ds_read_b128 a[24:27], v248 offset:12288\n"
      "ds_read_b128 a[28:31], v248 offset:14336\n"
      "ds_read_b128 a[56:59], v249 offset:12288\n"
      "ds_read_b128 a[60:63], v249 offset:14336\n"
      "s_waitcnt lgkmcnt(8)\n"
      "s_xor_b32 s45, s76, 256\n"
      "s_add_u32 s46, s8, s45\n"
      "s_addc_u32 s47, s9, 0\n"
      "s_add_u32 m0, s41, 4096\n"
      "s_nop 0\n"
      "global_load_lds_dwordx4 v244, s[46:47] offset:0\n"
      "global_load_lds_dwordx4 v245, s[46:47] offset:1024\n"
      "global_load_lds_dwordx4 v246, s[46:47] offset:2048\n"
      "global_load_lds_dwordx4 v247, s[46:47] offset:3072\n"
      "s_waitcnt vmcnt(16)\n"
      "ds_read_b128 a[64:67], v250 offset:0\n"
      "ds_read_b128 a[68:71], v250 offset:2048\n"
      "ds_read_b128 a[96:99], v251 offset:0\n"
      "ds_read_b128 a[100:103], v251 offset:2048\n"
      "s_waitcnt lgkmcnt(8)\n"
      "s_xor_b32 s45, s77, 256\n"
      "s_add_u32 s46, s8, s45\n"
      "s_addc_u32 s47, s9, 0\n"
      "s_add_u32 m0, s41, 8192\n"
      "s_nop 0\n"
      "global_load_lds_dwordx4 v244, s[46:47] offset:0\n"
      "global_load_lds_dwordx4 v245, s[46:47] offset:1024\n"
      "global_load_lds_dwordx4 v246, s[46:47] offset:2048\n"
      "global_load_lds_dwordx4 v247, s[46:47] offset:3072\n"
      "s_waitcnt vmcnt(16)\n"
      "ds_read_b128 a[72:75], v250 offset:4096\n"
      "ds_read_b128 a[76:79], v250 offset:6144\n"
      "ds_read_b128 a[104:107], v251 offset:4096\n"
      "ds_read_b128 a[108:111], v251 offset:6144\n"
      "s_waitcnt lgkmcnt(8)\n"
      "s_xor_b32 s45, s78, 256\n"
      "s_add_u32 s46, s8, s45\n"
      "s_addc_u32 s47, s9, 0\n"
      "s_add_u32 m0, s41, 12288\n"
      "s_nop 0\n"
      "global_load_lds_dwordx4 v244, s[46:47] offset:0\n"
      "global_load_lds_dwordx4 v245, s[46:47] offset:1024\n"
      "global_load_lds_dwordx4 v246, s[46:47] offset:2048\n"
      "global_load_lds_dwordx4 v247, s[46:47] offset:3072\n"
      "s_waitcnt vmcnt(16)\n"
      "ds_read_b128 a[80:83], v250 offset:8192\n"
      "ds_read_b128 a[84:87], v250 offset:10240\n"
      "ds_read_b128 a[112:115], v251 offset:8192\n"
      "ds_read_b128 a[116:119], v251 offset:10240\n"
      "s_waitcnt vmcnt(12)\n"
      "ds_read_b128 a[88:91], v248 offset:0\n"
      "ds_read_b128 a[92:95], v248 offset:2048\n"
      "ds_read_b128 a[120:123], v249 offset:0\n"
      "ds_read_b128 a[124:127], v249 offset:2048\n"
      "global_load_dword v252, v253, s[58:59] offset:0 sc1\n"
      "s_waitcnt lgkmcnt(12)\n"
      "s_xor_b32 s45, s79, 256\n"
      "s_add_u32 s46, s8, s45\n"
      "s_addc_u32 s47, s9, 0\n"
      "s_add_u32 m0, s41, 65536\n"
      "s_nop 0\n"
      "global_load_lds_dwordx4 v244, s[46:47] offset:0\n"
      "global_load_lds_dwordx4 v245, s[46:47] offset:1024\n"
      "global_load_lds_dwordx4 v246, s[46:47] offset:2048\n"
      "global_load_lds_dwordx4 v247, s[46:47] offset:3072\n"
      "s_waitcnt lgkmcnt(8)\n"
      "s_xor_b32 s45, s76, 384\n"
      "s_add_u32 s46, s8, s45\n"
      "s_addc_u32 s47, s9, 0\n"
      "s_add_u32 m0, s41, 69632\n"
      "s_nop 0\n"
      "global_load_lds_dwordx4 v244, s[46:47] offset:0\n"
      "global_load_lds_dwordx4 v245, s[46:47] offset:1024\n"
      "global_load_lds_dwordx4 v246, s[46:47] offset:2048\n"
      "global_load_lds_dwordx4 v247, s[46:47] offset:3072\n"
      "s_waitcnt vmcnt(17)\n"
      "ds_read_b128 a[128:131], v248 offset:4096\n"
      "ds_read_b128 a[132:135], v248 offset:6144\n"
      "ds_read_b128 a[160:163], v249 offset:4096\n"
      "ds_read_b128 a[164:167], v249 offset:6144\n"
      "s_waitcnt vmcnt(13)\n"
      "ds_read_b128 a[136:139], v248 offset:8192\n"
      "ds_read_b128 a[140:143], v248 offset:10240\n"
      "ds_read_b128 a[168:171], v249 offset:8192\n"
      "ds_read_b128 a[172:175], v249 offset:10240\n"
      "s_barrier\n"
      "s_waitcnt vmcnt(8)\n"
      "v_cmp_ne_u32 vcc, s37, v252\n"
      "s_nop 1\n"
      "s_cbranch_vccnz .Ls_s3_%=\n"
      ".Ls_o4_%=:\n"
      "global_load_dwordx2 v[206:207], v204, s[58:59] sc1\n"
      "global_load_dwordx4 v[128:131], v240, s[10:11] offset:-4096 sc1\n"
      "global_load_dwordx4 v[132:135], v240, s[10:11] offset:-3072 sc1\n"
      "global_load_dwordx4 v[136:139], v240, s[10:11] offset:-2048 sc1\n"
      "global_load_dwordx4 v[140:143], v240, s[72:73] offset:-4096 sc1\n"
      "global_load_dwordx4 v[144:147], v240, s[72:73] offset:-3072 sc1\n"
      "global_load_dwordx4 v[148:151], v240, s[72:73] offset:-2048 sc1\n"
      "s_waitcnt vmcnt(5)\n"
      "v_mfma_f32_16x16x32_bf16 v[0:3], v[128:131], a[0:3], 0\n"
      "global_load_dwordx4 v[152:155], v241, s[10:11] offset:-4096 sc1\n"
      "v_cmp_ne_u32 vcc, s37, v207\n"
      "s_nop 1\n"
      "s_cbranch_vccnz .Ls_s6_%=\n"
      ".Ls_o7_%=:\n"
      "v_mfma_f32_16x16x32_bf16 v[4:7], v[128:131], a[4:7], 0\n"
      "v_max_u32_dpp v206, v206, v206 quad_perm:[1,0,3,2] row_mask:0xf bank_mask:0xf\n"
      "s_nop 1\n"
      "v_max_u32_dpp v206, v206, v206 quad_perm:[2,3,0,1] row_mask:0xf bank_mask:0xf\n"
      "v_mfma_f32_16x16x32_bf16 v[8:11], v[128:131], a[8:11], 0\n"
      "global_load_dwordx4 v[156:159], v241, s[10:11] offset:-3072 sc1\n"
      "v_max_u32_dpp v206, v206, v206 row_half_mirror row_mask:0xf bank_mask:0xf\n"
      "v_mfma_f32_16x16x32_bf16 v[12:15], v[128:131], a[12:15], 0\n"
      "s_nop 0\n"
      "v_max_u32_dpp v206, v206, v206 row_mirror row_mask:0xf bank_mask:0xf\n"
      "v_mfma_f32_16x16x32_bf16 v[16:19], v[128:131], a[16:19], 0\n"
      "global_load_dwordx4 v[160:163], v241, s[10:11] offset:-2048 sc1\n"
      "v_mov_b32 v205, v206\n"
      "v_mfma_f32_16x16x32_bf16 v[20:23], v[128:131], a[20:23], 0\n"
      "s_nop 0\n"
      "v_permlane16_swap_b32 v206, v205\n"
      "v_mfma_f32_16x16x32_bf16 v[24:27], v[128:131], a[24:27], 0\n"
      "global_load_dwordx4 v[164:167], v241, s[72:73] offset:-4096 sc1\n"
      "v_max_u32 v206, v206, v205\n"
      "v_mfma_f32_16x16x32_bf16 v[28:31], v[128:131], a[28:31], 0\n"
      "v_mov_b32 v205, v206\n"
      "s_nop 1\n"
      "v_permlane32_swap_b32 v206, v205\n"
      "s_waitcnt vmcnt(8)\n"
      "v_mfma_f32_16x16x32_bf16 v[32:35], v[132:135], a[0:3], 0\n"
      "global_load_dwordx4 v[168:171], v241, s[72:73] offset:-3072 sc1\n"
      "v_max_u32 v206, v206, v205\n"
      "v_mfma_f32_16x16x32_bf16 v[36:39], v[132:135], a[4:7], 0\n"
      "v_readfirstlane_b32 s40, v206\n"
      "s_nop 1\n"
      "s_andn2_b32 s42, 0x7ff, s40\n"
      "v_mfma_f32_16x16x32_bf16 v[40:43], v[132:135], a[8:11], 0\n"
      "global_load_dwordx4 v[172:175], v241, s[72:73] offset:-2048 sc1\n"
      "s_lshr_b32 s43, s42, 5\n"
      "s_lshl2_add_u32 s44, s36, s33\n"
      "s_cmp_eq_u32 s43, s44\n"
      "s_cbranch_scc0 .Ls_e8_%=\n"
      "s_lshl_b32 s43, s42, 10\n"
      "v_add_u32 v208, s43, v242\n"
      "global_load_dwordx4 v[216:219], v208, s[16:17]\n"
      "global_load_dwordx4 v[220:223], v242, s[24:25]\n"
      ".Ls_e8_%=:\n"
      "v_mfma_f32_16x16x32_bf16 v[44:47], v[132:135], a[12:15], 0\n"
      "v_mfma_f32_16x16x32_bf16 v[48:51], v[132:135], a[16:19], 0\n"
      "global_load_dwordx4 v[176:179], v240, s[14:15] offset:-4096 sc1\n"
      "v_mfma_f32_16x16x32_bf16 v[52:55], v[132:135], a[20:23], 0\n"
      "v_mfma_f32_16x16x32_bf16 v[56:59], v[132:135], a[24:27], 0\n"
      "global_load_dwordx4 v[180:183], v240, s[14:15] offset:-3072 sc1\n"
      "v_mfma_f32_16x16x32_bf16 v[60:63], v[132:135], a[28:31], 0\n"
      "s_waitcnt vmcnt(10)\n"
      "v_mfma_f32_16x16x32_bf16 v[64:67], v[136:139], a[0:3], 0\n"
      "global_load_dwordx4 v[184:187], v240, s[14:15] offset:-2048 sc1\n"
      "global_load_dwordx4 v[188:191], v240, s[74:75] offset:-4096 sc1\n"
      "v_mfma_f32_16x16x32_bf16 v[68:71], v[136:139], a[4:7], 0\n"
      "global_load_dwordx4 v[128:131], v240, s[74:75] offset:-3072 sc1\n"
      "global_load_dwordx4 v[132:135], v240, s[74:75] offset:-2048 sc1\n"
      "v_mfma_f32_16x16x32_bf16 v[72:75], v[136:139], a[8:11], 0\n"
      "s_waitcnt lgkmcnt(12)\n"
      "s_xor_b32 s70, s77, 384\n"
      "s_add_u32 s70, s8, s70\n"
      "s_addc_u32 s71, s9, 0\n"
      "s_add_u32 m0, s41, 73728\n"
      "v_mfma_f32_16x16x32_bf16 v[76:79], v[136:139], a[12:15], 0\n"
      "global_load_lds_dwordx4 v244, s[70:71] offset:0\n"
      "v_mfma_f32_16x16x32_bf16 v[80:83], v[136:139], a[16:19], 0\n"
      "v_mfma_f32_16x16x32_bf16 v[84:87], v[136:139], a[20:23], 0\n"
      "global_load_lds_dwordx4 v245, s[70:71] offset:1024\n"
      "v_mfma_f32_16x16x32_bf16 v[88:91], v[136:139], a[24:27], 0\n"
      "v_mfma_f32_16x16x32_bf16 v[92:95], v[136:139], a[28:31], 0\n"
      "global_load_lds_dwordx4 v246, s[70:71] offset:2048\n"
      "global_load_dwordx4 v[136:139], v241, s[14:15] offset:-4096 sc1\n"
      "v_mfma_f32_16x16x32_bf16 v[0:3], v[140:143], a[32:35], v[0:3]\n"
      "v_mfma_f32_16x16x32_bf16 v[4:7], v[140:143], a[36:39], v[4:7]\n"
      "global_load_lds_dwordx4 v247, s[70:71] offset:3072\n"
      "v_mfma_f32_16x16x32_bf16 v[8:11], v[140:143], a[40:43], v[8:11]\n"
      "s_waitcnt lgkmcnt(8)\n"
      "s_xor_b32 s70, s78, 384\n"
      "s_add_u32 s70, s8, s70\n"
      "s_addc_u32 s71, s9, 0\n"
      "s_add_u32 m0, s41, 0\n"
      "v_mfma_f32_16x16x32_bf16 v[12:15], v[140:143], a[44:47], v[12:15]\n"
      "global_load_lds_dwordx4 v244, s[70:71] offset:0\n"
      "v_mfma_f32_16x16x32_bf16 v[16:19], v[140:143], a[48:51], v[16:19]\n"
      "v_mfma_f32_16x16x32_bf16 v[20:23], v[140:143], a[52:55], v[20:23]\n"
      "global_load_lds_dwordx4 v245, s[70:71] offset:1024\n"
      "ds_read_b128 a[144:147], v248 offset:12288\n"
      "v_mfma_f32_16x16x32_bf16 v[24:27], v[140:143], a[56:59], v[24:27]\n"
      "ds_read_b128 a[148:151], v248 offset:14336\n"
      "ds_read_b128 a[176:179], v249 offset:12288\n"
      "v_mfma_f32_16x16x32_bf16 v[28:31], v[140:143], a[60:63], v[28:31]\n"
      "s_waitcnt vmcnt(19)\n"
      "global_load_lds_dwordx4 v246, s[70:71] offset:2048\n"
      "global_load_dwordx4 v[140:143], v241, s[74:75] offset:-4096 sc1\n"
      "v_mfma_f32_16x16x32_bf16 v[32:35], v[144:147], a[32:35], v[32:35]\n"
      "ds_read_b128 a[180:183], v249 offset:14336\n"
      "ds_read_b128 a[152:155], v250 offset:0\n"
      "v_mfma_f32_16x16x32_bf16 v[36:39], v[144:147], a[36:39], v[36:39]\n"
      "global_load_lds_dwordx4 v247, s[70:71] offset:3072\n"
      "ds_read_b128 a[156:159], v250 offset:2048\n"
      "v_mfma_f32_16x16x32_bf16 v[40:43], v[144:147], a[40:43], v[40:43]\n"
      "s_waitcnt lgkmcnt(10)\n"
      "s_xor_b32 s70, s79, 384\n"
      "s_add_u32 s70, s8, s70\n"
      "s_addc_u32 s71, s9, 0\n"
      "s_add_u32 m0, s41, 4096\n"
      "v_mfma_f32_16x16x32_bf16 v[44:47], v[144:147], a[44:47], v[44:47]\n"
      "global_load_lds_dwordx4 v244, s[70:71] offset:0\n"
      "ds_read_b128 a[184:187], v251 offset:0\n"
      "v_mfma_f32_16x16x32_bf16 v[48:51], v[144:147], a[48:51], v[48:51]\n"
      "ds_read_b128 a[188:191], v251 offset:2048\n"
      "ds_read_b128 a[192:195], v250 offset:4096\n"
      "v_mfma_f32_16x16x32_bf16 v[52:55], v[144:147], a[52:55], v[52:55]\n"
      "global_load_lds_dwordx4 v245, s[70:71] offset:1024\n"
      "ds_read_b128 a[196:199], v250 offset:6144\n"
      "v_mfma_f32_16x16x32_bf16 v[56:59], v[144:147], a[56:59], v[56:59]\n"
      "ds_read_b128 a[224:227], v251 offset:4096\n"
      "ds_read_b128 a[228:231], v251 offset:6144\n"
      "v_mfma_f32_16x16x32_bf16 v[60:63], v[144:147], a[60:63], v[60:63]\n"
      "global_load_lds_dwordx4 v246, s[70:71] offset:2048\n"
      "global_load_dwordx4 v[144:147], v241, s[14:15] offset:-3072 sc1\n"
      "v_mfma_f32_16x16x32_bf16 v[64:67], v[148:151], a[32:35], v[64:67]\n"
      "v_mfma_f32_16x16x32_bf16 v[68:71], v[148:151], a[36:39], v[68:71]\n"
      "global_load_lds_dwordx4 v247, s[70:71] offset:3072\n"
      "v_mfma_f32_16x16x32_bf16 v[72:75], v[148:151], a[40:43], v[72:75]\n"
      "v_mfma_f32_16x16x32_bf16 v[76:79], v[148:151], a[44:47], v[76:79]\n"
      "v_mfma_f32_16x16x32_bf16 v[80:83], v[148:151], a[48:51], v[80:83]\n"
      "v_mfma_f32_16x16x32_bf16 v[84:87], v[148:151], a[52:55], v[84:87]\n"
      "v_mfma_f32_16x16x32_bf16 v[88:91], v[148:151], a[56:59], v[88:91]\n"
      "v_mfma_f32_16x16x32_bf16 v[92:95], v[148:151], a[60:63], v[92:95]\n"
      "s_waitcnt vmcnt(25)\n"
      "global_load_dwordx4 v[148:151], v241, s[74:75] offset:-3072 sc1\n"
      "v_mfma_f32_16x16x32_bf16 v[0:3], v[152:155], a[64:67], v[0:3]\n"
      "v_mfma_f32_16x16x32_bf16 v[4:7], v[152:155], a[68:71], v[4:7]\n"
      "v_mfma_f32_16x16x32_bf16 v[8:11], v[152:155], a[72:75], v[8:11]\n"
      "v_mfma_f32_16x16x32_bf16 v[12:15], v[152:155], a[76:79], v[12:15]\n"
      "v_mfma_f32_16x16x32_bf16 v[16:19], v[152:155], a[80:83], v[16:19]\n"
      "v_mfma_f32_16x16x32_bf16 v[20:23], v[152:155], a[84:87], v[20:23]\n"
      "v_mfma_f32_16x16x32_bf16 v[24:27], v[152:155], a[88:91], v[24:27]\n"
      "v_mfma_f32_16x16x32_bf16 v[28:31], v[152:155], a[92:95], v[28:31]\n"
      "global_load_dwordx4 v[152:155], v241, s[14:15] offset:-2048 sc1\n"
      "s_waitcnt vmcnt(12)\n"
      "ds_read_b128 a[200:203], v250 offset:8192\n"
      "v_mfma_f32_16x16x32_bf16 v[32:35], v[156:159], a[64:67], v[32:35]\n"
      "ds_read_b128 a[204:207], v250 offset:10240\n"
      "ds_read_b128 a[232:235], v251 offset:8192\n"
      "v_mfma_f32_16x16x32_bf16 v[36:39], v[156:159], a[68:71], v[36:39]\n"
      "ds_read_b128 a[236:239], v251 offset:10240\n"
      "v_mfma_f32_16x16x32_bf16 v[40:43], v[156:159], a[72:75], v[40:43]\n"
      "v_mfma_f32_16x16x32_bf16 v[44:47], v[156:159], a[76:79], v[44:47]\n"
      "v_mfma_f32_16x16x32_bf16 v[48:51], v[156:159], a[80:83], v[48:51]\n"
      "s_lshl_b32 s54, s42, 10\n"
      "s_lshl_b32 s55, s83, 7\n"
      "s_add_u32 s54, s54, s55\n"
      "v_mfma_f32_16x16x32_bf16 v[52:55], v[156:159], a[84:87], v[52:55]\n"
      "v_and_b32 v224, 48, v243\n"
      "v_add_u32 v224, s54, v224\n"
      "v_mfma_f32_16x16x32_bf16 v[56:59], v[156:159], a[88:91], v[56:59]\n"
      "global_load_dwordx4 v[192:195], v224, s[16:17]\n"
      "global_load_dwordx4 v[196:199], v224, s[16:17] offset:64\n"
      "v_mfma_f32_16x16x32_bf16 v[60:63], v[156:159], a[92:95], v[60:63]\n"
      "global_load_dwordx4 v[156:159], v241, s[74:75] offset:-2048 sc1\n"
      "s_waitcnt vmcnt(10)\n"
      "ds_read_b128 a[208:211], v248 offset:0\n"
      "v_mfma_f32_16x16x32_bf16 v[64:67], v[160:163], a[64:67], v[64:67]\n"
      "ds_read_b128 a[212:215], v248 offset:2048\n"
      "ds_read_b128 a[240:243], v249 offset:0\n"
      "v_mfma_f32_16x16x32_bf16 v[68:71], v[160:163], a[68:71], v[68:71]\n"
      "ds_read_b128 a[244:247], v249 offset:2048\n"
      "v_mfma_f32_16x16x32_bf16 v[72:75], v[160:163], a[72:75], v[72:75]\n"
      "v_mfma_f32_16x16x32_bf16 v[76:79], v[160:163], a[76:79], v[76:79]\n"
      "v_mfma_f32_16x16x32_bf16 v[80:83], v[160:163], a[80:83], v[80:83]\n"
      "v_mfma_f32_16x16x32_bf16 v[84:87], v[160:163], a[84:87], v[84:87]\n"
      "v_mfma_f32_16x16x32_bf16 v[88:91], v[160:163], a[88:91], v[88:91]\n"
      "s_ashr_i32 s84, s40, 31\n"
      "s_min_u32 s43, s80, 27\n"
      "v_lshrrev_b32 v202, 3, v243\n"
      "v_and_b32 v203, 0x70, v242\n"
      "s_movk_i32 s44, 3584\n"
      "v_mad_u32_u24 v200, v202, s44, v203\n"
      "v_add_u32 v201, 27648, v200\n"
      "v_add_u32 v202, 55296, v200\n"
      "v_add_u32 v203, 82944, v200\n"
      "s_bitcmp1_b32 s33, 0\n"
      "s_cselect_b64 s[70:71], s[28:29], s[26:27]\n"
      "s_mul_i32 s44, s83, 229376\n"
      "s_lshl_b32 s45, s43, 7\n"
      "s_add_u32 s44, s44, s45\n"
      "s_add_u32 s70, s70, s44\n"
      "s_addc_u32 s71, s71, 0\n"
      "s_lshl_b32 s45, s33, 13\n"
      "s_add_u32 m0, s45, 131072\n"
      "v_mfma_f32_16x16x32_bf16 v[92:95], v[160:163], a[92:95], v[92:95]\n"
      "s_not_b32 s84, s84\n"
      "global_load_lds_dwordx4 v200, s[70:71] offset:0\n"
      "s_or_b32 s84, s84, 0x80000000\n"
      "v_mfma_f32_16x16x32_bf16 v[0:3], v[164:167], a[96:99], v[0:3]\n"
      "s_xor_b32 s84, s84, s40\n"
      "global_load_lds_dwordx4 v201, s[70:71] offset:1024\n"
      "s_and_b32 s84, s84, 0xffff0000\n"
      "v_mfma_f32_16x16x32_bf16 v[4:7], v[164:167], a[100:103], v[4:7]\n"
      "global_load_lds_dwordx4 v202, s[70:71] offset:2048\n"
      "v_lshlrev_b32 v208, 16, v216\n"
      "v_mfma_f32_16x16x32_bf16 v[8:11], v[164:167], a[104:107], v[8:11]\n"
      "global_load_lds_dwordx4 v203, s[70:71] offset:3072\n"
      "v_lshlrev_b32 v209, 16, v220\n"
      "v_mfma_f32_16x16x32_bf16 v[12:15], v[164:167], a[108:111], v[12:15]\n"
      "s_add_u32 s70, s70, 114688\n"
      "s_addc_u32 s71, s71, 0\n"
      "s_add_u32 m0, m0, 4096\n"
      "v_mfma_f32_16x16x32_bf16 v[16:19], v[164:167], a[112:115], v[16:19]\n"
      "global_load_lds_dwordx4 v200, s[70:71] offset:0\n"
      "v_and_b32 v216, 0xffff0000, v216\n"
      "v_mfma_f32_16x16x32_bf16 v[20:23], v[164:167], a[116:119], v[20:23]\n"
      "global_load_lds_dwordx4 v201, s[70:71] offset:1024\n"
      "v_and_b32 v220, 0xffff0000, v220\n"
      "v_mfma_f32_16x16x32_bf16 v[24:27], v[164:167], a[120:123], v[24:27]\n"
      "global_load_lds_dwordx4 v202, s[70:71] offset:2048\n"
      "v_mul_f32 v208, v208, v209\n"
      "v_mfma_f32_16x16x32_bf16 v[28:31], v[164:167], a[124:127], v[28:31]\n"
      "global_load_lds_dwordx4 v203, s[70:71] offset:3072\n"
      "v_mul_f32 v210, v216, v220\n"
      "v_mfma_f32_16x16x32_bf16 v[32:35], v[168:171], a[96:99], v[32:35]\n"
      "s_min_u32 s43, s80, 27\n"
      "s_lshr_b32 s44, s33, 1\n"
      "s_lshl1_add_u32 s44, s43, s44\n"
      "s_lshl_b32 s44, s44, 15\n"
      "s_and_b32 s45, s33, 1\n"
      "s_lshl1_add_u32 s45, s81, s45\n"
      "s_lshl_b32 s45, s45, 7\n"
      "s_add_u32 s44, s44, s45\n"
      "v_and_b32 v239, 31, v243\n"
      "v_lshl_add_u32 v239, v239, 10, s44\n"
      "global_load_dword v207, v239, s[30:31]\n"
      "v_mfma_f32_16x16x32_bf16 v[36:39], v[168:171], a[100:103], v[36:39]\n"
      "s_waitcnt vmcnt(14)\n"
      "ds_read_b128 a[216:219], v248 offset:4096\n"
      "v_mfma_f32_16x16x32_bf16 v[40:43], v[168:171], a[104:107], v[40:43]\n"
      "ds_read_b128 a[220:223], v248 offset:6144\n"
      "v_lshrrev_b32 v201, 4, v243\n"
      "v_mfma_f32_16x16x32_bf16 v[44:47], v[168:171], a[108:111], v[44:47]\n"
      "ds_read_b128 a[248:251], v249 offset:4096\n"
      "v_bfe_u32 v224, v243, 2, 2\n"
      "v_mfma_f32_16x16x32_bf16 v[48:51], v[168:171], a[112:115], v[48:51]\n"
      "ds_read_b128 a[252:255], v249 offset:6144\n"
      "v_lshl_or_b32 v201, v201, 3, v224\n"
      "v_mfma_f32_16x16x32_bf16 v[52:55], v[168:171], a[116:119], v[52:55]\n"
      "s_sub_u32 s48, s33, 1\n"
      "s_and_b32 s48, s48, 3\n"
      "v_mfma_f32_16x16x32_bf16 v[56:59], v[168:171], a[120:123], v[56:59]\n"
      "s_lshl_b32 s48, s48, 14\n"
      "v_lshl_add_u32 v244, v243, 2, s48\n"
      "s_sub_u32 s48, s33, 2\n"
      "v_mfma_f32_16x16x32_bf16 v[60:63], v[168:171], a[124:127], v[60:63]\n"
      "s_and_b32 s48, s48, 3\n"
      "s_lshl_b32 s48, s48, 14\n"
      "v_lshl_add_u32 v245, v243, 2, s48\n"
      "v_mfma_f32_16x16x32_bf16 v[64:67], v[172:175], a[96:99], v[64:67]\n"
      "s_sub_u32 s48, s33, 3\n"
      "s_and_b32 s48, s48, 3\n"
      "s_lshl_b32 s48, s48, 14\n"
      "v_mfma_f32_16x16x32_bf16 v[68:71], v[172:175], a[100:103], v[68:71]\n"
      "s_mov_b32 m0, s41\n"
      "v_lshl_add_u32 v246, v243, 2, s48\n"
      "v_and_b32 v224, 3, v243\n"
      "v_mfma_f32_16x16x32_bf16 v[72:75], v[172:175], a[104:107], v[72:75]\n"
      "v_lshlrev_b32 v224, 3, v224\n"
      "v_lshl_or_b32 v201, v201, 7, v224\n"
      "v_mfma_f32_16x16x32_bf16 v[76:79], v[172:175], a[108:111], v[76:79]\n"
      "s_mov_b32 s64, 0xffff0000\n"
      "s_mov_b32 s65, 0xffff0000\n"
      "s_mov_b32 s66, -1\n"
      "v_mfma_f32_16x16x32_bf16 v[80:83], v[172:175], a[112:115], v[80:83]\n"
      "s_mov_b32 s67, 0\n"
      "s_lshl_b32 s54, s33, 13\n"
      "s_add_u32 s54, s54, 131072\n"
      "v_mfma_f32_16x16x32_bf16 v[84:87], v[172:175], a[116:119], v[84:87]\n"
      "v_add_u32 v202, s54, v201\n"
      "s_and_b32 s54, s33, 1\n"
      "v_mfma_f32_16x16x32_bf16 v[88:91], v[172:175], a[120:123], v[88:91]\n"
      "s_lshl3_add_u32 s54, s54, s83\n"
      "s_lshl_b32 s54, s54, 8\n"
      "s_lshl_b32 s55, s80, 13\n"
      "v_mfma_f32_16x16x32_bf16 v[92:95], v[172:175], a[124:127], v[92:95]\n"
      "global_load_dword v252, v253, s[58:59] offset:128 sc1\n"
      "s_add_u32 s55, s55, 262144\n"
      "v_mfma_f32_16x16x32_bf16 v[0:3], v[176:179], a[128:131], v[0:3]\n"
      "s_add_u32 s54, s54, s55\n"
      "v_bfe_u32 v224, v243, 4, 1\n"
      "v_and_b32 v203, 0xf0, v242\n"
      "v_mfma_f32_16x16x32_bf16 v[4:7], v[176:179], a[132:135], v[4:7]\n"
      "v_lshl_or_b32 v200, v224, 12, v203\n"
      "v_add_u32 v200, s54, v200\n"
      "s_waitcnt lgkmcnt(15)\n"
      "v_mfma_f32_16x16x32_bf16 v[8:11], v[176:179], a[136:139], v[8:11]\n"
      "v_lshl_or_b32 v203, v224, 14, v203\n"
      "v_cvt_pk_bf16_f32 v208, v208, v210\n"
      "v_mfma_f32_16x16x32_bf16 v[12:15], v[176:179], a[140:143], v[12:15]\n"
      "v_lshlrev_b32 v209, 16, v208\n"
      "v_and_b32 v208, 0xffff0000, v208\n"
      "v_mfma_f32_16x16x32_bf16 v[16:19], v[176:179], a[144:147], v[16:19]\n"
      "v_add_f32 v211, v208, v209\n"
      "v_lshlrev_b32 v208, 16, v217\n"
      "v_mfma_f32_16x16x32_bf16 v[20:23], v[176:179], a[148:151], v[20:23]\n"
      "v_lshlrev_b32 v209, 16, v221\n"
      "v_and_b32 v217, 0xffff0000, v217\n"
      "v_mfma_f32_16x16x32_bf16 v[24:27], v[176:179], a[152:155], v[24:27]\n"
      "v_and_b32 v221, 0xffff0000, v221\n"
      "v_mul_f32 v208, v208, v209\n"
      "v_mfma_f32_16x16x32_bf16 v[28:31], v[176:179], a[156:159], v[28:31]\n"
      "v_mul_f32 v210, v217, v221\n"
      "v_cvt_pk_bf16_f32 v208, v208, v210\n"
      "v_mfma_f32_16x16x32_bf16 v[32:35], v[180:183], a[128:131], v[32:35]\n"
      "v_lshlrev_b32 v209, 16, v208\n"
      "v_and_b32 v208, 0xffff0000, v208\n"
      "v_mfma_f32_16x16x32_bf16 v[36:39], v[180:183], a[132:135], v[36:39]\n"
      "v_add_f32 v208, v208, v209\n"
      "v_add_f32 v211, v211, v208\n"
      "v_mfma_f32_16x16x32_bf16 v[40:43], v[180:183], a[136:139], v[40:43]\n"
      "v_lshlrev_b32 v208, 16, v218\n"
      "v_lshlrev_b32 v209, 16, v222\n"
      "v_mfma_f32_16x16x32_bf16 v[44:47], v[180:183], a[140:143], v[44:47]\n"
      "v_and_b32 v218, 0xffff0000, v218\n"
      "v_and_b32 v222, 0xffff0000, v222\n"
      "v_mfma_f32_16x16x32_bf16 v[48:51], v[180:183], a[144:147], v[48:51]\n"
      "v_mul_f32 v208, v208, v209\n"
      "v_mul_f32 v210, v218, v222\n"
      "v_mfma_f32_16x16x32_bf16 v[52:55], v[180:183], a[148:151], v[52:55]\n"
      "v_cvt_pk_bf16_f32 v208, v208, v210\n"
      "v_lshlrev_b32 v209, 16, v208\n"
      "v_mfma_f32_16x16x32_bf16 v[56:59], v[180:183], a[152:155], v[56:59]\n"
      "v_and_b32 v208, 0xffff0000, v208\n"
      "v_add_f32 v208, v208, v209\n"
      "v_mfma_f32_16x16x32_bf16 v[60:63], v[180:183], a[156:159], v[60:63]\n"
      "v_add_f32 v211, v211, v208\n"
      "v_lshlrev_b32 v208, 16, v219\n"
      "v_mfma_f32_16x16x32_bf16 v[64:67], v[184:187], a[128:131], v[64:67]\n"
      "v_lshlrev_b32 v209, 16, v223\n"
      "v_and_b32 v219, 0xffff0000, v219\n"
      "v_mfma_f32_16x16x32_bf16 v[68:71], v[184:187], a[132:135], v[68:71]\n"
      "v_and_b32 v223, 0xffff0000, v223\n"
      "v_mul_f32 v208, v208, v209\n"
      "v_mfma_f32_16x16x32_bf16 v[72:75], v[184:187], a[136:139], v[72:75]\n"
      "v_mul_f32 v210, v219, v223\n"
      "v_cvt_pk_bf16_f32 v208, v208, v210\n"
      "v_mfma_f32_16x16x32_bf16 v[76:79], v[184:187], a[140:143], v[76:79]\n"
      "v_lshlrev_b32 v209, 16, v208\n"
      "v_and_b32 v208, 0xffff0000, v208\n"
      "v_mfma_f32_16x16x32_bf16 v[80:83], v[184:187], a[144:147], v[80:83]\n"
      "v_add_f32 v208, v208, v209\n"
      "v_add_f32 v211, v211, v208\n"
      "v_mfma_f32_16x16x32_bf16 v[84:87], v[184:187], a[148:151], v[84:87]\n"
      "s_nop 0\n"
      "v_add_f32_dpp v211, v211, v211 quad_perm:[1,0,3,2] row_mask:0xf bank_mask:0xf\n"
      "v_mfma_f32_16x16x32_bf16 v[88:91], v[184:187], a[152:155], v[88:91]\n"
      "s_nop 0\n"
      "v_add_f32_dpp v211, v211, v211 quad_perm:[2,3,0,1] row_mask:0xf bank_mask:0xf\n"
      "v_mfma_f32_16x16x32_bf16 v[92:95], v[184:187], a[156:159], v[92:95]\n"
      "s_nop 0\n"
      "v_add_f32_dpp v211, v211, v211 row_half_mirror row_mask:0xf bank_mask:0xf\n"
      "v_mfma_f32_16x16x32_bf16 v[0:3], v[188:191], a[160:163], v[0:3]\n"
      "s_nop 0\n"
      "v_add_f32_dpp v211, v211, v211 row_mirror row_mask:0xf bank_mask:0xf\n"
      "v_mfma_f32_16x16x32_bf16 v[4:7], v[188:191], a[164:167], v[4:7]\n"
      "v_mov_b32 v208, v211\n"
      "v_mfma_f32_16x16x32_bf16 v[8:11], v[188:191], a[168:171], v[8:11]\n"
      "s_nop 0\n"
      "v_permlane16_swap_b32 v211, v208\n"
      "v_mfma_f32_16x16x32_bf16 v[12:15], v[188:191], a[172:175], v[12:15]\n"
      "v_add_f32 v211, v211, v208\n"
      "v_mov_b32 v208, v211\n"
      "v_mfma_f32_16x16x32_bf16 v[16:19], v[188:191], a[176:179], v[16:19]\n"
      "s_nop 0\n"
      "v_permlane32_swap_b32 v211, v208\n"
      "v_mfma_f32_16x16x32_bf16 v[20:23], v[188:191], a[180:183], v[20:23]\n"
      "v_add_f32 v211, v211, v208\n"
      "v_cvt_pk_bf16_f32 v211, v211, v211\n"
      "v_mfma_f32_16x16x32_bf16 v[24:27], v[188:191], a[184:187], v[24:27]\n"
      "v_and_b32 v211, 0xffff0000, v211\n"
      "v_writelane_b32 v211, s84, 1\n"
      "v_mfma_f32_16x16x32_bf16 v[28:31], v[188:191], a[188:191], v[28:31]\n"
      "v_mul_f32 v211, 0xbfb8aa3b, v211\n"
      "v_exp_f32 v211, v211\n"
      "v_mfma_f32_16x16x32_bf16 v[32:35], v[128:131], a[160:163], v[32:35]\n"
      "v_add_f32 v211, 1.0, v211\n"
      "v_rcp_f32 v211, v211\n"
      "v_mfma_f32_16x16x32_bf16 v[36:39], v[128:131], a[164:167], v[36:39]\n"
      "v_cvt_pk_bf16_f32 v211, v211, v211\n"
      "v_and_b32 v211, 0xffff0000, v211\n"
      "v_mfma_f32_16x16x32_bf16 v[40:43], v[128:131], a[168:171], v[40:43]\n"
      "v_readfirstlane_b32 s39, v211\n"
      "v_readlane_b32 s84, v211, 1\n"
      "v_mfma_f32_16x16x32_bf16 v[44:47], v[128:131], a[172:175], v[44:47]\n"
      "v_mfma_f32_16x16x32_bf16 v[48:51], v[128:131], a[176:179], v[48:51]\n"
      "v_mfma_f32_16x16x32_bf16 v[52:55], v[128:131], a[180:183], v[52:55]\n"
      "v_mfma_f32_16x16x32_bf16 v[56:59], v[128:131], a[184:187], v[56:59]\n"
      "v_mfma_f32_16x16x32_bf16 v[60:63], v[128:131], a[188:191], v[60:63]\n"
      "v_mfma_f32_16x16x32_bf16 v[64:67], v[132:135], a[160:163], v[64:67]\n"
      "v_mfma_f32_16x16x32_bf16 v[68:71], v[132:135], a[164:167], v[68:71]\n"
      "v_mfma_f32_16x16x32_bf16 v[72:75], v[132:135], a[168:171], v[72:75]\n"
      "v_mfma_f32_16x16x32_bf16 v[76:79], v[132:135], a[172:175], v[76:79]\n"
      "v_mfma_f32_16x16x32_bf16 v[80:83], v[132:135], a[176:179], v[80:83]\n"
      "v_mfma_f32_16x16x32_bf16 v[84:87], v[132:135], a[180:183], v[84:87]\n"
      "v_mfma_f32_16x16x32_bf16 v[88:91], v[132:135], a[184:187], v[88:91]\n"
      "v_mfma_f32_16x16x32_bf16 v[92:95], v[132:135], a[188:191], v[92:95]\n"
      "v_mfma_f32_16x16x32_bf16 v[0:3], v[136:139], a[192:195], v[0:3]\n"
      "s_waitcnt lgkmcnt(14)\n"
      "v_mfma_f32_16x16x32_bf16 v[4:7], v[136:139], a[196:199], v[4:7]\n"
      "s_waitcnt lgkmcnt(11)\n"
      "v_mfma_f32_16x16x32_bf16 v[8:11], v[136:139], a[200:203], v[8:11]\n"
      "s_waitcnt lgkmcnt(10)\n"
      "v_mfma_f32_16x16x32_bf16 v[12:15], v[136:139], a[204:207], v[12:15]\n"
      "s_waitcnt lgkmcnt(7)\n"
      "v_mfma_f32_16x16x32_bf16 v[16:19], v[136:139], a[208:211], v[16:19]\n"
      "s_waitcnt lgkmcnt(6)\n"
      "v_mfma_f32_16x16x32_bf16 v[20:23], v[136:139], a[212:215], v[20:23]\n"
      "s_waitcnt vmcnt(2)\n"
      "ds_read_b64_tr_b16 v[224:225], v202 offset:0\n"
      "s_waitcnt lgkmcnt(4)\n"
      "v_mfma_f32_16x16x32_bf16 v[24:27], v[136:139], a[216:219], v[24:27]\n"
      "ds_read_b64_tr_b16 v[226:227], v202 offset:512\n"
      "ds_read_b64_tr_b16 v[228:229], v202 offset:4096\n"
      "s_waitcnt lgkmcnt(5)\n"
      "v_mfma_f32_16x16x32_bf16 v[28:31], v[136:139], a[220:223], v[28:31]\n"
      "ds_read_b64_tr_b16 v[230:231], v202 offset:4608\n"
      "ds_read_b64_tr_b16 v[232:233], v202 offset:64\n"
      "v_mfma_f32_16x16x32_bf16 v[0:3], v[140:143], a[224:227], v[0:3]\n"
      "ds_read_b64_tr_b16 v[234:235], v202 offset:576\n"
      "ds_read_b64_tr_b16 v[236:237], v202 offset:4160\n"
      "v_mfma_f32_16x16x32_bf16 v[4:7], v[140:143], a[228:231], v[4:7]\n"
      "s_waitcnt vmcnt(0)\n"
      "v_cmp_ne_u32 vcc, s37, v252\n"
      "s_nop 1\n"
      "s_cbranch_vccnz .Ls_s10_%=\n"
      ".Ls_o11_%=:\n"
      "v_mfma_f32_16x16x32_bf16 v[8:11], v[140:143], a[232:235], v[8:11]\n"
      "ds_read_b64_tr_b16 v[238:239], v202 offset:4672\n"
      "v_mfma_f32_16x16x32_bf16 v[12:15], v[140:143], a[236:239], v[12:15]\n"
      "global_load_dwordx4 v[160:163], v240, s[10:11] offset:-1024 sc1\n"
      "global_load_dwordx4 v[164:167], v240, s[72:73] offset:-1024 sc1\n"
      "v_mfma_f32_16x16x32_bf16 v[16:19], v[140:143], a[240:243], v[16:19]\n"
      "global_load_dwordx4 v[168:171], v241, s[10:11] offset:-1024 sc1\n"
      "global_load_dwordx4 v[172:175], v241, s[72:73] offset:-1024 sc1\n"
      "v_mfma_f32_16x16x32_bf16 v[20:23], v[140:143], a[244:247], v[20:23]\n"
      "global_load_dwordx4 v[176:179], v240, s[14:15] offset:-1024 sc1\n"
      "global_load_dwordx4 v[180:183], v240, s[74:75] offset:-1024 sc1\n"
      "s_waitcnt lgkmcnt(9)\n"
      "v_mfma_f32_16x16x32_bf16 v[24:27], v[140:143], a[248:251], v[24:27]\n"
      "global_load_dwordx4 v[184:187], v241, s[14:15] offset:-1024 sc1\n"
      "v_cvt_pk_bf16_f32 v8, v8, v9\n"
      "s_waitcnt lgkmcnt(8)\n"
      "v_mfma_f32_16x16x32_bf16 v[28:31], v[140:143], a[252:255], v[28:31]\n"
      "global_load_dwordx4 v[188:191], v241, s[74:75] offset:-1024 sc1\n"
      "v_cvt_pk_bf16_f32 v9, v10, v11\n"
      "v_mfma_f32_16x16x32_bf16 v[32:35], v[144:147], a[192:195], v[32:35]\n"
      "global_load_dwordx4 v[128:131], v240, s[10:11] offset:0 sc1\n"
      "global_load_dwordx4 v[132:135], v240, s[72:73] offset:0 sc1\n"
      "v_mfma_f32_16x16x32_bf16 v[36:39], v[144:147], a[196:199], v[36:39]\n"
      "global_load_dwordx4 v[136:139], v241, s[10:11] offset:0 sc1\n"
      "global_load_dwordx4 v[140:143], v241, s[72:73] offset:0 sc1\n"
      "v_mfma_f32_16x16x32_bf16 v[40:43], v[144:147], a[200:203], v[40:43]\n"
      "s_waitcnt lgkmcnt(6)\n"
      "v_mfma_f32_16x16x32_bf16 v[248:251], v[192:195], v[224:227], 0\n"
      "v_mfma_f32_16x16x32_bf16 v[44:47], v[144:147], a[204:207], v[44:47]\n"
      "s_waitcnt lgkmcnt(4)\n"
      "v_mfma_f32_16x16x32_bf16 v[248:251], v[196:199], v[228:231], v[248:251]\n"
      "v_mfma_f32_16x16x32_bf16 v[48:51], v[144:147], a[208:211], v[48:51]\n"
      "s_waitcnt lgkmcnt(2)\n"
      "v_mfma_f32_16x16x32_bf16 v[224:227], v[192:195], v[232:235], 0\n"
      "v_mfma_f32_16x16x32_bf16 v[52:55], v[144:147], a[212:215], v[52:55]\n"
      "s_waitcnt lgkmcnt(0)\n"
      "v_mfma_f32_16x16x32_bf16 v[224:227], v[196:199], v[236:239], v[224:227]\n"
      "v_mfma_f32_16x16x32_bf16 v[56:59], v[144:147], a[216:219], v[56:59]\n"
      "ds_write_addtid_b32 v8 offset:0\n"
      "v_cvt_pk_bf16_f32 v10, v12, v13\n"
      "v_mfma_f32_16x16x32_bf16 v[60:63], v[144:147], a[220:223], v[60:63]\n"
      "global_load_dwordx4 v[144:147], v240, s[14:15] offset:0 sc1\n"
      "ds_write_addtid_b32 v9 offset:256\n"
      "v_mfma_f32_16x16x32_bf16 v[32:35], v[148:151], a[224:227], v[32:35]\n"
      "s_nop 0\n"
      "v_cndmask_b32 v247, v248, v224, s[64:65]\n"
      "v_mfma_f32_16x16x32_bf16 v[36:39], v[148:151], a[228:231], v[36:39]\n"
      "ds_read_b64_tr_b16 v[224:225], v202 offset:32\n"
      "v_cvt_pk_bf16_f32 v11, v14, v15\n"
      "v_mfma_f32_16x16x32_bf16 v[40:43], v[148:151], a[232:235], v[40:43]\n"
      "ds_read_b64_tr_b16 v[226:227], v202 offset:544\n"
      "ds_read_b64_tr_b16 v[228:229], v202 offset:4128\n"
      "v_mfma_f32_16x16x32_bf16 v[44:47], v[148:151], a[236:239], v[44:47]\n"
      "ds_read_b64_tr_b16 v[230:231], v202 offset:4640\n"
      "ds_read_b64_tr_b16 v[232:233], v202 offset:96\n"
      "v_mfma_f32_16x16x32_bf16 v[48:51], v[148:151], a[240:243], v[48:51]\n"
      "ds_read_b64_tr_b16 v[234:235], v202 offset:608\n"
      "ds_read_b64_tr_b16 v[236:237], v202 offset:4192\n"
      "v_mfma_f32_16x16x32_bf16 v[52:55], v[148:151], a[244:247], v[52:55]\n"
      "ds_read_b64_tr_b16 v[238:239], v202 offset:4704\n"
      "ds_write_addtid_b32 v10 offset:512\n"
      "v_mfma_f32_16x16x32_bf16 v[56:59], v[148:151], a[248:251], v[56:59]\n"
      "v_cvt_pk_bf16_f32 v12, v16, v17\n"
      "ds_write_addtid_b32 v11 offset:768\n"
      "v_mfma_f32_16x16x32_bf16 v[60:63], v[148:151], a[252:255], v[60:63]\n"
      "global_load_dwordx4 v[148:151], v240, s[74:75] offset:0 sc1\n"
      "v_cvt_pk_bf16_f32 v13, v18, v19\n"
      "v_mfma_f32_16x16x32_bf16 v[64:67], v[152:155], a[192:195], v[64:67]\n"
      "s_waitcnt lgkmcnt(8)\n"
      "v_mfma_f32_16x16x32_bf16 v[248:251], v[192:195], v[224:227], 0\n"
      "v_mfma_f32_16x16x32_bf16 v[68:71], v[152:155], a[196:199], v[68:71]\n"
      "s_waitcnt lgkmcnt(6)\n"
      "v_mfma_f32_16x16x32_bf16 v[248:251], v[196:199], v[228:231], v[248:251]\n"
      "v_mfma_f32_16x16x32_bf16 v[72:75], v[152:155], a[200:203], v[72:75]\n"
      "s_waitcnt lgkmcnt(4)\n"
      "v_mfma_f32_16x16x32_bf16 v[224:227], v[192:195], v[232:235], 0\n"
      "v_mfma_f32_16x16x32_bf16 v[76:79], v[152:155], a[204:207], v[76:79]\n"
      "s_waitcnt lgkmcnt(2)\n"
      "v_mfma_f32_16x16x32_bf16 v[224:227], v[196:199], v[236:239], v[224:227]\n"
      "v_mfma_f32_16x16x32_bf16 v[80:83], v[152:155], a[208:211], v[80:83]\n"
      "ds_write_addtid_b32 v12 offset:1024\n"
      "v_cvt_pk_bf16_f32 v14, v20, v21\n"
      "v_mfma_f32_16x16x32_bf16 v[84:87], v[152:155], a[212:215], v[84:87]\n"
      "ds_write_addtid_b32 v13 offset:1280\n"
      "v_cvt_pk_bf16_f32 v15, v22, v23\n"
      "v_mfma_f32_16x16x32_bf16 v[88:91], v[152:155], a[216:219], v[88:91]\n"
      "ds_write_addtid_b32 v14 offset:1536\n"
      "v_cndmask_b32 v250, v248, v224, s[64:65]\n"
      "v_mfma_f32_16x16x32_bf16 v[92:95], v[152:155], a[220:223], v[92:95]\n"
      "global_load_dwordx4 v[152:155], v241, s[14:15] offset:0 sc1\n"
      "v_and_b32 v248, s86, v247\n"
      "v_mfma_f32_16x16x32_bf16 v[64:67], v[156:159], a[224:227], v[64:67]\n"
      "v_mov_b32 v249, s37\n"
      "v_and_b32 v250, s86, v250\n"
      "v_mfma_f32_16x16x32_bf16 v[68:71], v[156:159], a[228:231], v[68:71]\n"
      "v_mov_b32 v251, s37\n"
      "v_cvt_pk_bf16_f32 v16, v24, v25\n"
      "v_mfma_f32_16x16x32_bf16 v[72:75], v[156:159], a[232:235], v[72:75]\n"
      "s_mov_b64 exec, s[66:67]\n"
      "global_store_dwordx4 v200, v[248:251], s[58:59] sc1\n"
      "s_mov_b64 exec, -1\n"
      "v_mfma_f32_16x16x32_bf16 v[76:79], v[156:159], a[236:239], v[76:79]\n"
      "ds_write_addtid_b32 v15 offset:1792\n"
      "v_bfe_u32 v248, v243, 1, 3\n"
      "v_mfma_f32_16x16x32_bf16 v[80:83], v[156:159], a[240:243], v[80:83]\n"
      "v_lshrrev_b32 v249, 4, v243\n"
      "v_lshlrev_b32 v249, 1, v249\n"
      "v_mfma_f32_16x16x32_bf16 v[84:87], v[156:159], a[244:247], v[84:87]\n"
      "v_sub_u32 v248, v248, v249\n"
      "v_and_b32 v249, 16, v242\n"
      "v_mfma_f32_16x16x32_bf16 v[88:91], v[156:159], a[248:251], v[88:91]\n"
      "v_mov_b32 v254, 0x3f80\n"
      "v_lshlrev_b32 v249, v249, v254\n"
      "v_mfma_f32_16x16x32_bf16 v[92:95], v[156:159], a[252:255], v[92:95]\n"
      "s_waitcnt vmcnt(14)\n"
      "global_load_dwordx4 v[156:159], v241, s[74:75] offset:0 sc1\n"
      "v_cvt_pk_bf16_f32 v17, v26, v27\n"
      "v_mfma_f32_16x16x32_bf16 v[96:99], v[160:163], a[0:3], 0\n"
      "v_cmp_eq_u32 vcc, 0, v248\n"
      "s_nop 1\n"
      "v_cndmask_b32 v250, 0, v249, vcc\n"
      "v_mfma_f32_16x16x32_bf16 v[100:103], v[160:163], a[4:7], 0\n"
      "v_cmp_eq_u32 vcc, 1, v248\n"
      "s_nop 1\n"
      "v_cndmask_b32 v251, 0, v249, vcc\n"
      "v_mfma_f32_16x16x32_bf16 v[104:107], v[160:163], a[8:11], 0\n"
      "ds_write_addtid_b32 v16 offset:2048\n"
      "v_mov_b32 v252, v250\n"
      "v_mfma_f32_16x16x32_bf16 v[108:111], v[160:163], a[12:15], 0\n"
      "v_mov_b32 v253, v251\n"
      "v_mov_b32 v248, 0\n"
      "v_mfma_f32_16x16x32_bf16 v[112:115], v[160:163], a[16:19], 0\n"
      "v_mov_b32 v249, 0\n"
      "v_mov_b32 v254, 0\n"
      "v_mfma_f32_16x16x32_bf16 v[116:119], v[160:163], a[20:23], 0\n"
      "v_mov_b32 v255, 0\n"
      "s_add_u32 s48, s34, 1\n"
      "v_mfma_f32_16x16x32_bf16 v[120:123], v[160:163], a[24:27], 0\n"
      "s_and_b32 s48, s48, 3\n"
      "s_mul_i32 s48, s48, 36864\n"
      "s_add_u32 s48, s48, 0\n"
      "v_mfma_f32_16x16x32_bf16 v[124:127], v[160:163], a[28:31], 0\n"
      "s_add_u32 s52, s60, s48\n"
      "s_addc_u32 s53, s61, 0\n"
      "global_load_dwordx4 v[160:163], v240, s[10:11] offset:1024 sc1\n"
      "v_mfma_f32_16x16x32_bf16 v[96:99], v[164:167], a[32:35], v[96:99]\n"
      "s_lshl2_add_u32 s54, s36, s33\n"
      "v_cvt_pk_bf16_f32 v18, v28, v29\n"
      "s_lshl_b32 s55, s54, 15\n"
      "v_mfma_f32_16x16x32_bf16 v[100:103], v[164:167], a[36:39], v[100:103]\n"
      "s_lshl2_add_u32 s56, s35, s34\n"
      "ds_write_addtid_b32 v17 offset:2304\n"
      "v_cvt_pk_bf16_f32 v19, v30, v31\n"
      "v_mfma_f32_16x16x32_bf16 v[104:107], v[164:167], a[40:43], v[104:107]\n"
      "s_lshl_b32 s56, s56, 6\n"
      "ds_write_addtid_b32 v18 offset:2560\n"
      "s_add_u32 s55, s55, s56\n"
      "v_mfma_f32_16x16x32_bf16 v[108:111], v[164:167], a[44:47], v[108:111]\n"
      "ds_write_addtid_b32 v19 offset:2816\n"
      "v_cvt_pk_bf16_f32 v40, v40, v41\n"
      "v_mfma_f32_16x16x32_bf16 v[112:115], v[164:167], a[48:51], v[112:115]\n"
      "v_cvt_pk_bf16_f32 v41, v42, v43\n"
      "v_and_b32 v209, 15, v243\n"
      "v_mfma_f32_16x16x32_bf16 v[116:119], v[164:167], a[52:55], v[116:119]\n"
      "ds_write_addtid_b32 v40 offset:3072\n"
      "v_cvt_pk_bf16_f32 v42, v44, v45\n"
      "v_mfma_f32_16x16x32_bf16 v[120:123], v[164:167], a[56:59], v[120:123]\n"
      "ds_write_addtid_b32 v41 offset:3328\n"
      "v_cvt_pk_bf16_f32 v43, v46, v47\n"
      "v_mfma_f32_16x16x32_bf16 v[124:127], v[164:167], a[60:63], v[124:127]\n"
      "s_waitcnt vmcnt(14)\n"
      "global_load_dwordx4 v[164:167], v240, s[72:73] offset:1024 sc1\n"
      "v_and_b32 v208, 48, v243\n"
      "v_mfma_f32_16x16x32_bf16 v[96:99], v[168:171], a[64:67], v[96:99]\n"
      "ds_write_addtid_b32 v42 offset:3584\n"
      "v_cvt_pk_bf16_f32 v44, v48, v49\n"
      "v_mfma_f32_16x16x32_bf16 v[100:103], v[168:171], a[68:71], v[100:103]\n"
      "ds_write_addtid_b32 v43 offset:3840\n"
      "v_cvt_pk_bf16_f32 v45, v50, v51\n"
      "v_mfma_f32_16x16x32_bf16 v[104:107], v[168:171], a[72:75], v[104:107]\n"
      "ds_write_addtid_b32 v44 offset:4096\n"
      "v_cvt_pk_bf16_f32 v46, v52, v53\n"
      "v_mfma_f32_16x16x32_bf16 v[108:111], v[168:171], a[76:79], v[108:111]\n"
      "ds_write_addtid_b32 v45 offset:4352\n"
      "v_cvt_pk_bf16_f32 v47, v54, v55\n"
      "v_mfma_f32_16x16x32_bf16 v[112:115], v[168:171], a[80:83], v[112:115]\n"
      "ds_write_addtid_b32 v46 offset:4608\n"
      "v_cvt_pk_bf16_f32 v48, v56, v57\n"
      "v_mfma_f32_16x16x32_bf16 v[116:119], v[168:171], a[84:87], v[116:119]\n"
      "ds_write_addtid_b32 v47 offset:4864\n"
      "v_cvt_pk_bf16_f32 v49, v58, v59\n"
      "v_mfma_f32_16x16x32_bf16 v[120:123], v[168:171], a[88:91], v[120:123]\n"
      "ds_write_addtid_b32 v48 offset:5120\n"
      "v_cvt_pk_bf16_f32 v50, v60, v61\n"
      "v_mfma_f32_16x16x32_bf16 v[124:127], v[168:171], a[92:95], v[124:127]\n"
      "global_load_dwordx4 v[168:171], v241, s[10:11] offset:1024 sc1\n"
      "v_lshl_or_b32 v208, v209, 10, v208\n"
      "v_mfma_f32_16x16x32_bf16 v[96:99], v[172:175], a[96:99], v[96:99]\n"
      "ds_write_addtid_b32 v49 offset:5376\n"
      "v_cvt_pk_bf16_f32 v51, v62, v63\n"
      "v_mfma_f32_16x16x32_bf16 v[100:103], v[172:175], a[100:103], v[100:103]\n"
      "ds_write_addtid_b32 v50 offset:5632\n"
      "v_add_u32 v208, s55, v208\n"
      "v_mfma_f32_16x16x32_bf16 v[104:107], v[172:175], a[104:107], v[104:107]\n"
      "ds_write_addtid_b32 v51 offset:5888\n"
      "v_add_u32 v210, 16384, v208\n"
      "v_mfma_f32_16x16x32_bf16 v[108:111], v[172:175], a[108:111], v[108:111]\n"
      "global_load_dwordx4 v[216:219], v208, s[16:17]\n"
      "global_load_dwordx4 v[220:223], v210, s[16:17]\n"
      "v_mfma_f32_16x16x32_bf16 v[112:115], v[172:175], a[112:115], v[112:115]\n"
      "s_lshr_b32 s55, s34, 1\n"
      "s_lshl2_add_u32 s55, s55, s35\n"
      "s_lshl_b32 s55, s55, 15\n"
      "v_mfma_f32_16x16x32_bf16 v[116:119], v[172:175], a[116:119], v[116:119]\n"
      "s_lshl_b32 s54, s54, 9\n"
      "s_waitcnt lgkmcnt(0)\n"
      "s_barrier\n"
      "v_mfma_f32_16x16x32_bf16 v[120:123], v[172:175], a[120:123], v[120:123]\n"
      "s_add_u32 s54, s54, s55\n"
      "ds_read2st64_b32 v[20:21], v244 offset0:0 offset1:1\n"
      "s_add_u32 s54, s54, 786432\n"
      "v_mfma_f32_16x16x32_bf16 v[124:127], v[172:175], a[124:127], v[124:127]\n"
      "s_waitcnt vmcnt(16)\n"
      "global_load_dwordx4 v[172:175], v241, s[72:73] offset:1024 sc1\n"
      "v_lshl_add_u32 v209, v209, 4, s54\n"
      "v_mfma_f32_16x16x32_bf16 v[96:99], v[176:179], a[128:131], v[96:99]\n"
      "ds_read2st64_b32 v[22:23], v245 offset0:4 offset1:5\n"
      "ds_read2st64_b32 v[24:25], v244 offset0:2 offset1:3\n"
      "v_mfma_f32_16x16x32_bf16 v[100:103], v[176:179], a[132:135], v[100:103]\n"
      "ds_read2st64_b32 v[26:27], v245 offset0:6 offset1:7\n"
      "ds_read2st64_b32 v[28:29], v246 offset0:8 offset1:9\n"
      "v_mfma_f32_16x16x32_bf16 v[104:107], v[176:179], a[136:139], v[104:107]\n"
      "ds_read2st64_b32 v[30:31], v246 offset0:10 offset1:11\n"
      "ds_read2st64_b32 v[52:53], v244 offset0:12 offset1:13\n"
      "v_mfma_f32_16x16x32_bf16 v[108:111], v[176:179], a[140:143], v[108:111]\n"
      "ds_read2st64_b32 v[54:55], v245 offset0:16 offset1:17\n"
      "ds_read2st64_b32 v[56:57], v244 offset0:14 offset1:15\n"
      "v_mfma_f32_16x16x32_bf16 v[112:115], v[176:179], a[144:147], v[112:115]\n"
      "ds_read2st64_b32 v[58:59], v245 offset0:18 offset1:19\n"
      "ds_read2st64_b32 v[60:61], v246 offset0:20 offset1:21\n"
      "v_mfma_f32_16x16x32_bf16 v[116:119], v[176:179], a[148:151], v[116:119]\n"
      "ds_read2st64_b32 v[62:63], v246 offset0:22 offset1:23\n"
      "v_cvt_pk_bf16_f32 v72, v72, v73\n"
      "v_mfma_f32_16x16x32_bf16 v[120:123], v[176:179], a[152:155], v[120:123]\n"
      "global_load_dwordx2 v[204:205], v209, s[58:59] offset:0 sc1\n"
      "v_cvt_pk_bf16_f32 v73, v74, v75\n"
      "v_mfma_f32_16x16x32_bf16 v[124:127], v[176:179], a[156:159], v[124:127]\n"
      "s_waitcnt lgkmcnt(6)\n"
      "global_load_dwordx4 v[176:179], v240, s[14:15] offset:1024 sc1\n"
      "v_mfma_f32_16x16x32_bf16 v[96:99], v[180:183], a[160:163], v[96:99]\n"
      "v_mfma_f32_16x16x32_bf16 v[0:3], v[250:253], v[20:23], v[0:3]\n"
      "v_mfma_f32_16x16x32_bf16 v[100:103], v[180:183], a[164:167], v[100:103]\n"
      "v_mfma_f32_16x16x32_bf16 v[4:7], v[250:253], v[24:27], v[4:7]\n"
      "v_mfma_f32_16x16x32_bf16 v[104:107], v[180:183], a[168:171], v[104:107]\n"
      "v_mfma_f32_16x16x32_bf16 v[0:3], v[252:255], v[28:31], v[0:3]\n"
      "v_mfma_f32_16x16x32_bf16 v[108:111], v[180:183], a[172:175], v[108:111]\n"
      "v_mfma_f32_16x16x32_bf16 v[4:7], v[248:251], v[28:31], v[4:7]\n"
      "v_mov_b32 v11, s37\n"
      "v_mfma_f32_16x16x32_bf16 v[112:115], v[180:183], a[176:179], v[112:115]\n"
      "ds_write_addtid_b32 v72 offset:6144\n"
      "v_cvt_pk_bf16_f32 v74, v76, v77\n"
      "v_mfma_f32_16x16x32_bf16 v[116:119], v[180:183], a[180:183], v[116:119]\n"
      "ds_write_addtid_b32 v73 offset:6400\n"
      "v_cvt_pk_bf16_f32 v8, v0, v1\n"
      "v_mfma_f32_16x16x32_bf16 v[120:123], v[180:183], a[184:187], v[120:123]\n"
      "global_load_dwordx2 v[206:207], v209, s[58:59] offset:256 sc1\n"
      "v_cvt_pk_bf16_f32 v9, v2, v3\n"
      "v_mfma_f32_16x16x32_bf16 v[124:127], v[180:183], a[188:191], v[124:127]\n"
      "s_waitcnt vmcnt(18)\n"
      "global_load_dwordx4 v[180:183], v240, s[74:75] offset:1024 sc1\n"
      "v_cvt_pk_bf16_f32 v10, v4, v5\n"
      "v_mfma_f32_16x16x32_bf16 v[96:99], v[184:187], a[192:195], v[96:99]\n"
      "s_add_u32 s68, s20, 16384\n"
      "s_addc_u32 s69, s21, 0\n"
      "v_cvt_pk_bf16_f32 v247, v6, v7\n"
      "v_mfma_f32_16x16x32_bf16 v[100:103], v[184:187], a[196:199], v[100:103]\n"
      "global_store_dwordx4 v242, v[8:11], s[52:53] offset:0 sc1\n"
      "s_waitcnt lgkmcnt(2)\n"
      "v_mfma_f32_16x16x32_bf16 v[104:107], v[184:187], a[200:203], v[104:107]\n"
      "v_mfma_f32_16x16x32_bf16 v[32:35], v[250:253], v[52:55], v[32:35]\n"
      "v_cvt_pk_bf16_f32 v75, v78, v79\n"
      "v_mfma_f32_16x16x32_bf16 v[108:111], v[184:187], a[204:207], v[108:111]\n"
      "v_mfma_f32_16x16x32_bf16 v[36:39], v[250:253], v[56:59], v[36:39]\n"
      "v_lshrrev_b32 v208, 10, v208\n"
      "v_mfma_f32_16x16x32_bf16 v[112:115], v[184:187], a[208:211], v[112:115]\n"
      "v_mfma_f32_16x16x32_bf16 v[32:35], v[252:255], v[60:63], v[32:35]\n"
      "v_subrev_u32 v208, s42, v208\n"
      "v_mfma_f32_16x16x32_bf16 v[116:119], v[184:187], a[212:215], v[116:119]\n"
      "v_mfma_f32_16x16x32_bf16 v[36:39], v[248:251], v[60:63], v[36:39]\n"
      "v_mov_b32 v43, s37\n"
      "v_mfma_f32_16x16x32_bf16 v[120:123], v[184:187], a[216:219], v[120:123]\n"
      "ds_write_addtid_b32 v74 offset:6656\n"
      "v_mov_b32 v44, v247\n"
      "v_mfma_f32_16x16x32_bf16 v[124:127], v[184:187], a[220:223], v[124:127]\n"
      "global_load_dwordx4 v[184:187], v241, s[14:15] offset:1024 sc1\n"
      "v_mov_b32 v47, s37\n"
      "v_mfma_f32_16x16x32_bf16 v[96:99], v[188:191], a[224:227], v[96:99]\n"
      "v_cvt_pk_bf16_f32 v76, v80, v81\n"
      "v_mfma_f32_16x16x32_bf16 v[100:103], v[188:191], a[228:231], v[100:103]\n"
      "ds_write_addtid_b32 v75 offset:6912\n"
      "v_cvt_pk_bf16_f32 v40, v32, v33\n"
      "v_mfma_f32_16x16x32_bf16 v[104:107], v[188:191], a[232:235], v[104:107]\n"
      "v_cvt_pk_bf16_f32 v41, v34, v35\n"
      "v_cvt_pk_bf16_f32 v42, v36, v37\n"
      "v_mfma_f32_16x16x32_bf16 v[108:111], v[188:191], a[236:239], v[108:111]\n"
      "v_cvt_pk_bf16_f32 v45, v38, v39\n"
      "v_cvt_pk_bf16_f32 v77, v82, v83\n"
      "v_mfma_f32_16x16x32_bf16 v[112:115], v[188:191], a[240:243], v[112:115]\n"
      "global_store_dwordx4 v242, v[40:43], s[52:53] offset:1024 sc1\n"
      "v_mfma_f32_16x16x32_bf16 v[116:119], v[188:191], a[244:247], v[116:119]\n"
      "global_store_dwordx4 v242, v[44:47], s[52:53] offset:2048 sc1\n"
      "s_add_u32 s48, s34, 2\n"
      "v_mfma_f32_16x16x32_bf16 v[120:123], v[188:191], a[248:251], v[120:123]\n"
      "s_and_b32 s48, s48, 3\n"
      "ds_write_addtid_b32 v76 offset:7168\n"
      "s_mul_i32 s48, s48, 36864\n"
      "v_mfma_f32_16x16x32_bf16 v[124:127], v[188:191], a[252:255], v[124:127]\n"
      "s_waitcnt vmcnt(21)\n"
      "global_load_dwordx4 v[188:191], v241, s[74:75] offset:1024 sc1\n"
      "s_add_u32 s48, s48, 12288\n"
      "v_mfma_f32_16x16x32_bf16 v[0:3], v[128:131], a[0:3], 0\n"
      "s_add_u32 s52, s60, s48\n"
      "s_addc_u32 s53, s61, 0\n"
      "v_cvt_pk_bf16_f32 v78, v84, v85\n"
      "v_mfma_f32_16x16x32_bf16 v[4:7], v[128:131], a[4:7], 0\n"
      "ds_write_addtid_b32 v77 offset:7424\n"
      "v_cvt_pk_bf16_f32 v79, v86, v87\n"
      "v_mfma_f32_16x16x32_bf16 v[8:11], v[128:131], a[8:11], 0\n"
      "ds_write_addtid_b32 v78 offset:7680\n"
      "v_cvt_pk_bf16_f32 v80, v88, v89\n"
      "v_mfma_f32_16x16x32_bf16 v[12:15], v[128:131], a[12:15], 0\n"
      "ds_write_addtid_b32 v79 offset:7936\n"
      "v_cvt_pk_bf16_f32 v81, v90, v91\n"
      "v_mfma_f32_16x16x32_bf16 v[16:19], v[128:131], a[16:19], 0\n"
      "ds_write_addtid_b32 v80 offset:8192\n"
      "v_cvt_pk_bf16_f32 v82, v92, v93\n"
      "v_mfma_f32_16x16x32_bf16 v[20:23], v[128:131], a[20:23], 0\n"
      "ds_write_addtid_b32 v81 offset:8448\n"
      "v_cvt_pk_bf16_f32 v83, v94, v95\n"
      "v_mfma_f32_16x16x32_bf16 v[24:27], v[128:131], a[24:27], 0\n"
      "ds_write_addtid_b32 v82 offset:8704\n"
      "v_mfma_f32_16x16x32_bf16 v[28:31], v[128:131], a[28:31], 0\n"
      "global_load_dwordx4 v[128:131], v240, s[10:11] offset:2048 sc1\n"
      "v_mfma_f32_16x16x32_bf16 v[0:3], v[132:135], a[32:35], v[0:3]\n"
      "ds_write_addtid_b32 v83 offset:8960\n"
      "v_cvt_pk_bf16_f32 v104, v104, v105\n"
      "v_mfma_f32_16x16x32_bf16 v[4:7], v[132:135], a[36:39], v[4:7]\n"
      "v_cvt_pk_bf16_f32 v105, v106, v107\n"
      "v_mfma_f32_16x16x32_bf16 v[8:11], v[132:135], a[40:43], v[8:11]\n"
      "ds_write_addtid_b32 v104 offset:9216\n"
      "v_cvt_pk_bf16_f32 v106, v108, v109\n"
      "v_mfma_f32_16x16x32_bf16 v[12:15], v[132:135], a[44:47], v[12:15]\n"
      "ds_write_addtid_b32 v105 offset:9472\n"
      "v_cvt_pk_bf16_f32 v107, v110, v111\n"
      "v_mfma_f32_16x16x32_bf16 v[16:19], v[132:135], a[48:51], v[16:19]\n"
      "ds_write_addtid_b32 v106 offset:9728\n"
      "v_cvt_pk_bf16_f32 v108, v112, v113\n"
      "v_mfma_f32_16x16x32_bf16 v[20:23], v[132:135], a[52:55], v[20:23]\n"
      "ds_write_addtid_b32 v107 offset:9984\n"
      "v_cvt_pk_bf16_f32 v109, v114, v115\n"
      "v_mfma_f32_16x16x32_bf16 v[24:27], v[132:135], a[56:59], v[24:27]\n"
      "ds_write_addtid_b32 v108 offset:10240\n"
      "v_cvt_pk_bf16_f32 v110, v116, v117\n"
      "v_mfma_f32_16x16x32_bf16 v[28:31], v[132:135], a[60:63], v[28:31]\n"
      "s_waitcnt vmcnt(21)\n"
      "global_load_dwordx4 v[132:135], v240, s[72:73] offset:2048 sc1\n"
      "v_mfma_f32_16x16x32_bf16 v[0:3], v[136:139], a[64:67], v[0:3]\n"
      "ds_write_addtid_b32 v109 offset:10496\n"
      "v_cvt_pk_bf16_f32 v111, v118, v119\n"
      "v_mfma_f32_16x16x32_bf16 v[4:7], v[136:139], a[68:71], v[4:7]\n"
      "ds_write_addtid_b32 v110 offset:10752\n"
      "v_cvt_pk_bf16_f32 v112, v120, v121\n"
      "v_mfma_f32_16x16x32_bf16 v[8:11], v[136:139], a[72:75], v[8:11]\n"
      "ds_write_addtid_b32 v111 offset:11008\n"
      "v_cvt_pk_bf16_f32 v113, v122, v123\n"
      "v_mfma_f32_16x16x32_bf16 v[12:15], v[136:139], a[76:79], v[12:15]\n"
      "ds_write_addtid_b32 v112 offset:11264\n"
      "v_cvt_pk_bf16_f32 v114, v124, v125\n"
      "v_mfma_f32_16x16x32_bf16 v[16:19], v[136:139], a[80:83], v[16:19]\n"
      "ds_write_addtid_b32 v113 offset:11520\n"
      "v_cvt_pk_bf16_f32 v115, v126, v127\n"
      "v_mfma_f32_16x16x32_bf16 v[20:23], v[136:139], a[84:87], v[20:23]\n"
      "ds_write_addtid_b32 v114 offset:11776\n"
      "v_mfma_f32_16x16x32_bf16 v[24:27], v[136:139], a[88:91], v[24:27]\n"
      "ds_write_addtid_b32 v115 offset:12032\n"
      "v_mfma_f32_16x16x32_bf16 v[28:31], v[136:139], a[92:95], v[28:31]\n"
      "global_load_dwordx4 v[136:139], v241, s[10:11] offset:2048 sc1\n"
      "v_mfma_f32_16x16x32_bf16 v[0:3], v[140:143], a[96:99], v[0:3]\n"
      "v_mfma_f32_16x16x32_bf16 v[4:7], v[140:143], a[100:103], v[4:7]\n"
      "s_waitcnt lgkmcnt(0)\n"
      "s_barrier\n"
      "v_mfma_f32_16x16x32_bf16 v[8:11], v[140:143], a[104:107], v[8:11]\n"
      "ds_read2st64_b32 v[84:85], v244 offset0:24 offset1:25\n"
      "v_mfma_f32_16x16x32_bf16 v[12:15], v[140:143], a[108:111], v[12:15]\n"
      "ds_read2st64_b32 v[86:87], v245 offset0:28 offset1:29\n"
      "v_mfma_f32_16x16x32_bf16 v[16:19], v[140:143], a[112:115], v[16:19]\n"
      "ds_read2st64_b32 v[88:89], v244 offset0:26 offset1:27\n"
      "v_mfma_f32_16x16x32_bf16 v[20:23], v[140:143], a[116:119], v[20:23]\n"
      "ds_read2st64_b32 v[90:91], v245 offset0:30 offset1:31\n"
      "v_mfma_f32_16x16x32_bf16 v[24:27], v[140:143], a[120:123], v[24:27]\n"
      "ds_read2st64_b32 v[92:93], v246 offset0:32 offset1:33\n"
      "v_mfma_f32_16x16x32_bf16 v[28:31], v[140:143], a[124:127], v[28:31]\n"
      "s_waitcnt vmcnt(21)\n"
      "global_load_dwordx4 v[140:143], v241, s[72:73] offset:2048 sc1\n"
      "v_mfma_f32_16x16x32_bf16 v[0:3], v[144:147], a[128:131], v[0:3]\n"
      "ds_read2st64_b32 v[94:95], v246 offset0:34 offset1:35\n"
      "v_mfma_f32_16x16x32_bf16 v[4:7], v[144:147], a[132:135], v[4:7]\n"
      "ds_read2st64_b32 v[116:117], v244 offset0:36 offset1:37\n"
      "v_mfma_f32_16x16x32_bf16 v[8:11], v[144:147], a[136:139], v[8:11]\n"
      "ds_read2st64_b32 v[118:119], v245 offset0:40 offset1:41\n"
      "v_mfma_f32_16x16x32_bf16 v[12:15], v[144:147], a[140:143], v[12:15]\n"
      "ds_read2st64_b32 v[120:121], v244 offset0:38 offset1:39\n"
      "v_mfma_f32_16x16x32_bf16 v[16:19], v[144:147], a[144:147], v[16:19]\n"
      "ds_read2st64_b32 v[122:123], v245 offset0:42 offset1:43\n"
      "v_mfma_f32_16x16x32_bf16 v[20:23], v[144:147], a[148:151], v[20:23]\n"
      "s_waitcnt lgkmcnt(4)\n"
      "v_mfma_f32_16x16x32_bf16 v[64:67], v[250:253], v[84:87], v[64:67]\n"
      "v_mfma_f32_16x16x32_bf16 v[24:27], v[144:147], a[152:155], v[24:27]\n"
      "v_mfma_f32_16x16x32_bf16 v[68:71], v[250:253], v[88:91], v[68:71]\n"
      "v_mfma_f32_16x16x32_bf16 v[28:31], v[144:147], a[156:159], v[28:31]\n"
      "global_load_dwordx4 v[144:147], v240, s[14:15] offset:2048 sc1\n"
      "v_mfma_f32_16x16x32_bf16 v[0:3], v[148:151], a[160:163], v[0:3]\n"
      "v_mfma_f32_16x16x32_bf16 v[64:67], v[252:255], v[92:95], v[64:67]\n"
      "v_mfma_f32_16x16x32_bf16 v[4:7], v[148:151], a[164:167], v[4:7]\n"
      "v_mfma_f32_16x16x32_bf16 v[68:71], v[248:251], v[92:95], v[68:71]\n"
      "v_mov_b32 v75, s37\n"
      "v_mfma_f32_16x16x32_bf16 v[8:11], v[148:151], a[168:171], v[8:11]\n"
      "ds_read2st64_b32 v[124:125], v246 offset0:44 offset1:45\n"
      "v_mfma_f32_16x16x32_bf16 v[12:15], v[148:151], a[172:175], v[12:15]\n"
      "ds_read2st64_b32 v[126:127], v246 offset0:46 offset1:47\n"
      "s_nop 0\n"
      "v_cvt_pk_bf16_f32 v72, v64, v65\n"
      "v_mfma_f32_16x16x32_bf16 v[16:19], v[148:151], a[176:179], v[16:19]\n"
      "s_waitcnt vmcnt(11)\n"
      "v_bitop3_b32 v210, v205, v207, s37 bitop3:0x7e\n"
      "v_cmp_ne_u32 vcc, 0, v210\n"
      "s_nop 1\n"
      "s_cbranch_vccnz .Ls_s13_%=\n"
      ".Ls_o14_%=:\n"
      "v_mfma_f32_16x16x32_bf16 v[20:23], v[148:151], a[180:183], v[20:23]\n"
      "v_cvt_pk_bf16_f32 v73, v66, v67\n"
      "v_cvt_pk_bf16_f32 v74, v68, v69\n"
      "v_mfma_f32_16x16x32_bf16 v[24:27], v[148:151], a[184:187], v[24:27]\n"
      "v_cvt_pk_bf16_f32 v247, v70, v71\n"
      "v_mov_b32 v209, s39\n"
      "v_mfma_f32_16x16x32_bf16 v[28:31], v[148:151], a[188:191], v[28:31]\n"
      "global_load_dwordx4 v[148:151], v240, s[74:75] offset:2048 sc1\n"
      "v_mfma_f32_16x16x32_bf16 v[0:3], v[152:155], a[192:195], v[0:3]\n"
      "global_store_dwordx4 v242, v[72:75], s[52:53] offset:0 sc1\n"
      "s_waitcnt lgkmcnt(0)\n"
      "v_mfma_f32_16x16x32_bf16 v[4:7], v[152:155], a[196:199], v[4:7]\n"
      "v_mfma_f32_16x16x32_bf16 v[96:99], v[250:253], v[116:119], v[96:99]\n"
      "v_mfma_f32_16x16x32_bf16 v[8:11], v[152:155], a[200:203], v[8:11]\n"
      "v_mfma_f32_16x16x32_bf16 v[100:103], v[250:253], v[120:123], v[100:103]\n"
      "v_mfma_f32_16x16x32_bf16 v[12:15], v[152:155], a[204:207], v[12:15]\n"
      "v_mfma_f32_16x16x32_bf16 v[96:99], v[252:255], v[124:127], v[96:99]\n"
      "v_mfma_f32_16x16x32_bf16 v[16:19], v[152:155], a[208:211], v[16:19]\n"
      "v_mfma_f32_16x16x32_bf16 v[100:103], v[248:251], v[124:127], v[100:103]\n"
      "v_mov_b32 v107, s37\n"
      "v_mfma_f32_16x16x32_bf16 v[20:23], v[152:155], a[212:215], v[20:23]\n"
      "v_cmp_eq_u32 vcc, 0, v208\n"
      "s_nop 1\n"
      "v_cndmask_b32 v204, v204, v209, vcc\n"
      "v_mfma_f32_16x16x32_bf16 v[24:27], v[152:155], a[216:219], v[24:27]\n"
      "v_cmp_eq_u32 vcc, -16, v208\n"
      "s_nop 1\n"
      "v_cndmask_b32 v206, v206, v209, vcc\n"
      "v_mfma_f32_16x16x32_bf16 v[28:31], v[152:155], a[220:223], v[28:31]\n"
      "global_load_dwordx4 v[152:155], v241, s[14:15] offset:2048 sc1\n"
      "v_mov_b32 v108, v247\n"
      "v_mfma_f32_16x16x32_bf16 v[0:3], v[156:159], a[224:227], v[0:3]\n"
      "v_mov_b32 v111, s37\n"
      "v_sub_f32 v205, 1.0, v204\n"
      "v_mfma_f32_16x16x32_bf16 v[4:7], v[156:159], a[228:231], v[4:7]\n"
      "v_cvt_pk_bf16_f32 v205, v205, v205\n"
      "v_and_b32 v205, 0xffff0000, v205\n"
      "v_mfma_f32_16x16x32_bf16 v[8:11], v[156:159], a[232:235], v[8:11]\n"
      "v_cvt_pk_bf16_f32 v104, v96, v97\n"
      "v_cvt_pk_bf16_f32 v105, v98, v99\n"
      "v_mfma_f32_16x16x32_bf16 v[12:15], v[156:159], a[236:239], v[12:15]\n"
      "v_cvt_pk_bf16_f32 v106, v100, v101\n"
      "v_cvt_pk_bf16_f32 v109, v102, v103\n"
      "v_mfma_f32_16x16x32_bf16 v[16:19], v[156:159], a[240:243], v[16:19]\n"
      "global_store_dwordx4 v242, v[104:107], s[52:53] offset:1024 sc1\n"
      "v_sub_f32 v207, 1.0, v206\n"
      "v_mfma_f32_16x16x32_bf16 v[20:23], v[156:159], a[244:247], v[20:23]\n"
      "global_store_dwordx4 v242, v[108:111], s[52:53] offset:2048 sc1\n"
      "s_add_u32 s48, s34, 3\n"
      "v_mfma_f32_16x16x32_bf16 v[24:27], v[156:159], a[248:251], v[24:27]\n"
      "s_and_b32 s48, s48, 3\n"
      "s_mul_i32 s48, s48, 36864\n"
      "s_add_u32 s48, s48, 24576\n"
      "v_mfma_f32_16x16x32_bf16 v[28:31], v[156:159], a[252:255], v[28:31]\n"
      "global_load_dwordx4 v[156:159], v241, s[74:75] offset:2048 sc1\n"
      "s_add_u32 s52, s60, s48\n"
      "s_addc_u32 s53, s61, 0\n"
      "v_mfma_f32_16x16x32_bf16 v[32:35], v[160:163], a[0:3], 0\n"
      "v_cvt_pk_bf16_f32 v8, v8, v9\n"
      "v_cvt_pk_bf16_f32 v9, v10, v11\n"
      "v_mfma_f32_16x16x32_bf16 v[36:39], v[160:163], a[4:7], 0\n"
      "ds_write_addtid_b32 v8 offset:0\n"
      "v_cvt_pk_bf16_f32 v10, v12, v13\n"
      "v_mfma_f32_16x16x32_bf16 v[40:43], v[160:163], a[8:11], 0\n"
      "ds_write_addtid_b32 v9 offset:256\n"
      "v_cvt_pk_bf16_f32 v11, v14, v15\n"
      "v_mfma_f32_16x16x32_bf16 v[44:47], v[160:163], a[12:15], 0\n"
      "ds_write_addtid_b32 v10 offset:512\n"
      "v_cvt_pk_bf16_f32 v12, v16, v17\n"
      "v_mfma_f32_16x16x32_bf16 v[48:51], v[160:163], a[16:19], 0\n"
      "ds_write_addtid_b32 v11 offset:768\n"
      "v_cvt_pk_bf16_f32 v13, v18, v19\n"
      "v_mfma_f32_16x16x32_bf16 v[52:55], v[160:163], a[20:23], 0\n"
      "ds_write_addtid_b32 v12 offset:1024\n"
      "v_cvt_pk_bf16_f32 v14, v20, v21\n"
      "v_mfma_f32_16x16x32_bf16 v[56:59], v[160:163], a[24:27], 0\n"
      "ds_write_addtid_b32 v13 offset:1280\n"
      "v_cvt_pk_bf16_f32 v15, v22, v23\n"
      "v_mfma_f32_16x16x32_bf16 v[60:63], v[160:163], a[28:31], 0\n"
      "global_load_dwordx4 v[160:163], v240, s[10:11] offset:3072 sc1\n"
      "v_cvt_pk_bf16_f32 v207, v207, v207\n"
      "v_mfma_f32_16x16x32_bf16 v[32:35], v[164:167], a[32:35], v[32:35]\n"
      "ds_write_addtid_b32 v14 offset:1536\n"
      "v_cvt_pk_bf16_f32 v16, v24, v25\n"
      "v_mfma_f32_16x16x32_bf16 v[36:39], v[164:167], a[36:39], v[36:39]\n"
      "ds_write_addtid_b32 v15 offset:1792\n"
      "v_cvt_pk_bf16_f32 v17, v26, v27\n"
      "v_mfma_f32_16x16x32_bf16 v[40:43], v[164:167], a[40:43], v[40:43]\n"
      "ds_write_addtid_b32 v16 offset:2048\n"
      "v_cvt_pk_bf16_f32 v18, v28, v29\n"
      "v_mfma_f32_16x16x32_bf16 v[44:47], v[164:167], a[44:47], v[44:47]\n"
      "ds_write_addtid_b32 v17 offset:2304\n"
      "v_cvt_pk_bf16_f32 v19, v30, v31\n"
      "v_mfma_f32_16x16x32_bf16 v[48:51], v[164:167], a[48:51], v[48:51]\n"
      "ds_write_addtid_b32 v18 offset:2560\n"
      "v_and_b32 v207, 0xffff0000, v207\n"
      "v_mfma_f32_16x16x32_bf16 v[52:55], v[164:167], a[52:55], v[52:55]\n"
      "ds_write_addtid_b32 v19 offset:2816\n"
      "v_lshlrev_b32 v208, 16, v216\n"
      "v_mfma_f32_16x16x32_bf16 v[56:59], v[164:167], a[56:59], v[56:59]\n"
      "v_and_b32 v209, 0xffff0000, v216\n"
      "v_mul_f32 v208, v205, v208\n"
      "v_mfma_f32_16x16x32_bf16 v[60:63], v[164:167], a[60:63], v[60:63]\n"
      "global_load_dwordx4 v[164:167], v240, s[72:73] offset:3072 sc1\n"
      "v_mul_f32 v209, v205, v209\n"
      "v_mfma_f32_16x16x32_bf16 v[32:35], v[168:171], a[64:67], v[32:35]\n"
      "s_waitcnt lgkmcnt(0)\n"
      "s_barrier\n"
      "v_mfma_f32_16x16x32_bf16 v[36:39], v[168:171], a[68:71], v[36:39]\n"
      "ds_read2st64_b32 v[20:21], v244 offset0:0 offset1:1\n"
      "v_lshlrev_b32 v212, 16, v220\n"
      "v_mfma_f32_16x16x32_bf16 v[40:43], v[168:171], a[72:75], v[40:43]\n"
      "ds_read2st64_b32 v[22:23], v245 offset0:4 offset1:5\n"
      "v_and_b32 v213, 0xffff0000, v220\n"
      "v_mfma_f32_16x16x32_bf16 v[44:47], v[168:171], a[76:79], v[44:47]\n"
      "ds_read2st64_b32 v[24:25], v244 offset0:2 offset1:3\n"
      "v_mul_f32 v212, v207, v212\n"
      "v_mfma_f32_16x16x32_bf16 v[48:51], v[168:171], a[80:83], v[48:51]\n"
      "ds_read2st64_b32 v[26:27], v245 offset0:6 offset1:7\n"
      "v_mul_f32 v213, v207, v213\n"
      "v_mfma_f32_16x16x32_bf16 v[52:55], v[168:171], a[84:87], v[52:55]\n"
      "ds_read2st64_b32 v[28:29], v246 offset0:8 offset1:9\n"
      "v_lshlrev_b32 v210, 16, v217\n"
      "v_mfma_f32_16x16x32_bf16 v[56:59], v[168:171], a[88:91], v[56:59]\n"
      "ds_read2st64_b32 v[30:31], v246 offset0:10 offset1:11\n"
      "v_and_b32 v211, 0xffff0000, v217\n"
      "v_mfma_f32_16x16x32_bf16 v[60:63], v[168:171], a[92:95], v[60:63]\n"
      "global_load_dwordx4 v[168:171], v241, s[10:11] offset:3072 sc1\n"
      "v_mul_f32 v210, v205, v210\n"
      "v_mfma_f32_16x16x32_bf16 v[32:35], v[172:175], a[96:99], v[32:35]\n"
      "v_mul_f32 v211, v205, v211\n"
      "v_lshlrev_b32 v214, 16, v221\n"
      "v_mfma_f32_16x16x32_bf16 v[36:39], v[172:175], a[100:103], v[36:39]\n"
      "v_and_b32 v215, 0xffff0000, v221\n"
      "v_mul_f32 v214, v207, v214\n"
      "v_mfma_f32_16x16x32_bf16 v[40:43], v[172:175], a[104:107], v[40:43]\n"
      "v_mul_f32 v215, v207, v215\n"
      "v_lshlrev_b32 v216, 16, v218\n"
      "v_mfma_f32_16x16x32_bf16 v[44:47], v[172:175], a[108:111], v[44:47]\n"
      "s_waitcnt lgkmcnt(0)\n"
      "v_mfma_f32_16x16x32_bf16 v[0:3], v[250:253], v[20:23], v[0:3]\n"
      "v_and_b32 v217, 0xffff0000, v218\n"
      "v_mfma_f32_16x16x32_bf16 v[48:51], v[172:175], a[112:115], v[48:51]\n"
      "v_mfma_f32_16x16x32_bf16 v[4:7], v[250:253], v[24:27], v[4:7]\n"
      "v_mul_f32 v216, v205, v216\n"
      "v_mfma_f32_16x16x32_bf16 v[52:55], v[172:175], a[116:119], v[52:55]\n"
      "v_mfma_f32_16x16x32_bf16 v[0:3], v[252:255], v[28:31], v[0:3]\n"
      "v_mul_f32 v217, v205, v217\n"
      "v_mfma_f32_16x16x32_bf16 v[56:59], v[172:175], a[120:123], v[56:59]\n"
      "v_mfma_f32_16x16x32_bf16 v[4:7], v[248:251], v[28:31], v[4:7]\n"
      "v_mov_b32 v11, s37\n"
      "v_mfma_f32_16x16x32_bf16 v[60:63], v[172:175], a[124:127], v[60:63]\n"
      "s_waitcnt vmcnt(19)\n"
      "global_load_dwordx4 v[172:175], v241, s[72:73] offset:3072 sc1\n"
      "v_lshlrev_b32 v220, 16, v222\n"
      "v_mfma_f32_16x16x32_bf16 v[40:43], v[176:179], a[136:139], v[40:43]\n"
      "v_cvt_pk_bf16_f32 v8, v0, v1\n"
      "v_cvt_pk_bf16_f32 v9, v2, v3\n"
      "v_mfma_f32_16x16x32_bf16 v[44:47], v[176:179], a[140:143], v[44:47]\n"
      "v_cvt_pk_bf16_f32 v10, v4, v5\n"
      "v_cvt_pk_bf16_f32 v247, v6, v7\n"
      "v_mfma_f32_16x16x32_bf16 v[48:51], v[176:179], a[144:147], v[48:51]\n"
      "global_store_dwordx4 v242, v[8:11], s[52:53] offset:0 sc1\n"
      "v_and_b32 v221, 0xffff0000, v222\n"
      "v_mfma_f32_16x16x32_bf16 v[52:55], v[176:179], a[148:151], v[52:55]\n"
      "v_mul_f32 v220, v207, v220\n"
      "v_mul_f32 v221, v207, v221\n"
      "v_mfma_f32_16x16x32_bf16 v[56:59], v[176:179], a[152:155], v[56:59]\n"
      "v_lshlrev_b32 v218, 16, v219\n"
      "v_and_b32 v219, 0xffff0000, v219\n"
      "v_mfma_f32_16x16x32_bf16 v[60:63], v[176:179], a[156:159], v[60:63]\n"
      "v_mul_f32 v218, v205, v218\n"
      "v_mul_f32 v219, v205, v219\n"
      "v_mfma_f32_16x16x32_bf16 v[40:43], v[180:183], a[168:171], v[40:43]\n"
      "v_lshlrev_b32 v222, 16, v223\n"
      "v_and_b32 v223, 0xffff0000, v223\n"
      "v_mfma_f32_16x16x32_bf16 v[44:47], v[180:183], a[172:175], v[44:47]\n"
      "v_mul_f32 v222, v207, v222\n"
      "v_mul_f32 v223, v207, v223\n"
      "v_mfma_f32_16x16x32_bf16 v[48:51], v[180:183], a[176:179], v[48:51]\n"
      "s_lshl2_add_u32 s54, s36, s33\n"
      "s_lshl_b32 s55, s54, 15\n"
      "s_lshl2_add_u32 s56, s35, s34\n"
      "v_mfma_f32_16x16x32_bf16 v[52:55], v[180:183], a[180:183], v[52:55]\n"
      "s_lshl_b32 s56, s56, 6\n"
      "s_add_u32 s55, s55, s56\n"
      "v_and_b32 v8, 15, v243\n"
      "v_mfma_f32_16x16x32_bf16 v[56:59], v[180:183], a[184:187], v[56:59]\n"
      "v_and_b32 v7, 48, v243\n"
      "v_lshl_or_b32 v7, v8, 10, v7\n"
      "v_mfma_f32_16x16x32_bf16 v[60:63], v[180:183], a[188:191], v[60:63]\n"
      "s_waitcnt vmcnt(16)\n"
      "v_add_u32 v7, s55, v7\n"
      "v_mfma_f32_16x16x32_bf16 v[40:43], v[184:187], a[200:203], v[40:43]\n"
      "v_mfma_f32_16x16x32_bf16 v[44:47], v[184:187], a[204:207], v[44:47]\n"
      "v_mfma_f32_16x16x32_bf16 v[48:51], v[184:187], a[208:211], v[48:51]\n"
      "v_mfma_f32_16x16x32_bf16 v[52:55], v[184:187], a[212:215], v[52:55]\n"
      "v_mfma_f32_16x16x32_bf16 v[56:59], v[184:187], a[216:219], v[56:59]\n"
      "v_mfma_f32_16x16x32_bf16 v[60:63], v[184:187], a[220:223], v[60:63]\n"
      "v_mfma_f32_16x16x32_bf16 v[40:43], v[188:191], a[232:235], v[40:43]\n"
      "v_mfma_f32_16x16x32_bf16 v[44:47], v[188:191], a[236:239], v[44:47]\n"
      "v_mfma_f32_16x16x32_bf16 v[48:51], v[188:191], a[240:243], v[48:51]\n"
      "v_mfma_f32_16x16x32_bf16 v[52:55], v[188:191], a[244:247], v[52:55]\n"
      "v_mfma_f32_16x16x32_bf16 v[56:59], v[188:191], a[248:251], v[56:59]\n"
      "s_nop 3\n"
      "v_cvt_pk_bf16_f32 v40, v40, v41\n"
      "v_cvt_pk_bf16_f32 v41, v42, v43\n"
      "v_mfma_f32_16x16x32_bf16 v[60:63], v[188:191], a[252:255], v[60:63]\n"
      "ds_write_addtid_b32 v40 offset:3072\n"
      "v_cvt_pk_bf16_f32 v42, v44, v45\n"
      "v_mfma_f32_16x16x32_bf16 v[32:35], v[176:179], a[128:131], v[32:35]\n"
      "ds_write_addtid_b32 v41 offset:3328\n"
      "v_cvt_pk_bf16_f32 v43, v46, v47\n"
      "v_mfma_f32_16x16x32_bf16 v[36:39], v[176:179], a[132:135], v[36:39]\n"
      "global_load_dwordx4 v[176:179], v240, s[14:15] offset:3072 sc1\n"
      "ds_write_addtid_b32 v42 offset:3584\n"
      "v_cvt_pk_bf16_f32 v44, v48, v49\n"
      "v_mfma_f32_16x16x32_bf16 v[32:35], v[180:183], a[160:163], v[32:35]\n"
      "ds_write_addtid_b32 v43 offset:3840\n"
      "v_cvt_pk_bf16_f32 v45, v50, v51\n"
      "v_mfma_f32_16x16x32_bf16 v[36:39], v[180:183], a[164:167], v[36:39]\n"
      "global_load_dwordx4 v[180:183], v240, s[74:75] offset:3072 sc1\n"
      "ds_write_addtid_b32 v44 offset:4096\n"
      "v_cvt_pk_bf16_f32 v46, v52, v53\n"
      "v_mfma_f32_16x16x32_bf16 v[32:35], v[184:187], a[192:195], v[32:35]\n"
      "ds_write_addtid_b32 v45 offset:4352\n"
      "v_cvt_pk_bf16_f32 v47, v54, v55\n"
      "v_mfma_f32_16x16x32_bf16 v[36:39], v[184:187], a[196:199], v[36:39]\n"
      "global_load_dwordx4 v[184:187], v241, s[14:15] offset:3072 sc1\n"
      "ds_write_addtid_b32 v46 offset:4608\n"
      "v_cvt_pk_bf16_f32 v48, v56, v57\n"
      "v_mfma_f32_16x16x32_bf16 v[32:35], v[188:191], a[224:227], v[32:35]\n"
      "ds_write_addtid_b32 v47 offset:4864\n"
      "v_cvt_pk_bf16_f32 v49, v58, v59\n"
      "v_mfma_f32_16x16x32_bf16 v[36:39], v[188:191], a[228:231], v[36:39]\n"
      "s_waitcnt vmcnt(17)\n"
      "global_load_dwordx4 v[188:191], v241, s[74:75] offset:3072 sc1\n"
      "ds_write_addtid_b32 v48 offset:5120\n"
      "v_cvt_pk_bf16_f32 v50, v60, v61\n"
      "v_mfma_f32_16x16x32_bf16 v[72:75], v[128:131], a[8:11], 0\n"
      "ds_write_addtid_b32 v49 offset:5376\n"
      "v_cvt_pk_bf16_f32 v51, v62, v63\n"
      "v_mfma_f32_16x16x32_bf16 v[76:79], v[128:131], a[12:15], 0\n"
      "ds_write_addtid_b32 v50 offset:5632\n"
      "ds_write_addtid_b32 v51 offset:5888\n"
      "v_mfma_f32_16x16x32_bf16 v[80:83], v[128:131], a[16:19], 0\n"
      "v_mfma_f32_16x16x32_bf16 v[84:87], v[128:131], a[20:23], 0\n"
      "v_mfma_f32_16x16x32_bf16 v[88:91], v[128:131], a[24:27], 0\n"
      "s_waitcnt lgkmcnt(0)\n"
      "s_barrier\n"
      "v_mfma_f32_16x16x32_bf16 v[92:95], v[128:131], a[28:31], 0\n"
      "ds_read2st64_b32 v[52:53], v244 offset0:12 offset1:13\n"
      "ds_read2st64_b32 v[54:55], v245 offset0:16 offset1:17\n"
      "v_mfma_f32_16x16x32_bf16 v[72:75], v[132:135], a[40:43], v[72:75]\n"
      "ds_read2st64_b32 v[56:57], v244 offset0:14 offset1:15\n"
      "ds_read2st64_b32 v[58:59], v245 offset0:18 offset1:19\n"
      "v_mfma_f32_16x16x32_bf16 v[76:79], v[132:135], a[44:47], v[76:79]\n"
      "ds_read2st64_b32 v[60:61], v246 offset0:20 offset1:21\n"
      "ds_read2st64_b32 v[62:63], v246 offset0:22 offset1:23\n"
      "v_mfma_f32_16x16x32_bf16 v[80:83], v[132:135], a[48:51], v[80:83]\n"
      "v_mfma_f32_16x16x32_bf16 v[84:87], v[132:135], a[52:55], v[84:87]\n"
      "v_mfma_f32_16x16x32_bf16 v[88:91], v[132:135], a[56:59], v[88:91]\n"
      "v_mfma_f32_16x16x32_bf16 v[92:95], v[132:135], a[60:63], v[92:95]\n"
      "s_waitcnt vmcnt(16)\n"
      "v_mfma_f32_16x16x32_bf16 v[72:75], v[136:139], a[72:75], v[72:75]\n"
      "s_waitcnt lgkmcnt(0)\n"
      "v_mfma_f32_16x16x32_bf16 v[32:35], v[250:253], v[52:55], v[32:35]\n"
      "v_mfma_f32_16x16x32_bf16 v[36:39], v[250:253], v[56:59], v[36:39]\n"
      "v_mfma_f32_16x16x32_bf16 v[76:79], v[136:139], a[76:79], v[76:79]\n"
      "v_mfma_f32_16x16x32_bf16 v[32:35], v[252:255], v[60:63], v[32:35]\n"
      "v_mfma_f32_16x16x32_bf16 v[36:39], v[248:251], v[60:63], v[36:39]\n"
      "v_mov_b32 v43, s37\n"
      "v_mfma_f32_16x16x32_bf16 v[80:83], v[136:139], a[80:83], v[80:83]\n"
      "v_mov_b32 v44, v247\n"
      "v_mov_b32 v47, s37\n"
      "v_mfma_f32_16x16x32_bf16 v[84:87], v[136:139], a[84:87], v[84:87]\n"
      "v_mfma_f32_16x16x32_bf16 v[88:91], v[136:139], a[88:91], v[88:91]\n"
      "v_add_u32 v4, 0, v242\n"
      "v_cvt_pk_bf16_f32 v40, v32, v33\n"
      "v_cvt_pk_bf16_f32 v41, v34, v35\n"
      "v_mfma_f32_16x16x32_bf16 v[92:95], v[136:139], a[92:95], v[92:95]\n"
      "global_load_dwordx4 v[12:15], v4, s[62:63] offset:0 sc1\n"
      "global_load_dwordx4 v[16:19], v4, s[62:63] offset:1024 sc1\n"
      "v_cvt_pk_bf16_f32 v42, v36, v37\n"
      "v_mfma_f32_16x16x32_bf16 v[72:75], v[140:143], a[104:107], v[72:75]\n"
      "global_load_dwordx4 v[20:23], v4, s[62:63] offset:2048 sc1\n"
      "v_cvt_pk_bf16_f32 v45, v38, v39\n"
      "v_mfma_f32_16x16x32_bf16 v[76:79], v[140:143], a[108:111], v[76:79]\n"
      "global_store_dwordx4 v242, v[40:43], s[52:53] offset:1024 sc1\n"
      "global_store_dwordx4 v242, v[44:47], s[52:53] offset:2048 sc1\n"
      "v_mfma_f32_16x16x32_bf16 v[80:83], v[140:143], a[112:115], v[80:83]\n"
      "v_mfma_f32_16x16x32_bf16 v[84:87], v[140:143], a[116:119], v[84:87]\n"
      "v_mfma_f32_16x16x32_bf16 v[88:91], v[140:143], a[120:123], v[88:91]\n"
      "v_mfma_f32_16x16x32_bf16 v[92:95], v[140:143], a[124:127], v[92:95]\n"
      "s_waitcnt vmcnt(19)\n"
      "v_mfma_f32_16x16x32_bf16 v[72:75], v[144:147], a[136:139], v[72:75]\n"
      "v_mfma_f32_16x16x32_bf16 v[76:79], v[144:147], a[140:143], v[76:79]\n"
      "v_mfma_f32_16x16x32_bf16 v[80:83], v[144:147], a[144:147], v[80:83]\n"
      "v_mfma_f32_16x16x32_bf16 v[84:87], v[144:147], a[148:151], v[84:87]\n"
      "v_mfma_f32_16x16x32_bf16 v[88:91], v[144:147], a[152:155], v[88:91]\n"
      "v_mfma_f32_16x16x32_bf16 v[92:95], v[144:147], a[156:159], v[92:95]\n"
      "v_mfma_f32_16x16x32_bf16 v[72:75], v[148:151], a[168:171], v[72:75]\n"
      "v_mfma_f32_16x16x32_bf16 v[76:79], v[148:151], a[172:175], v[76:79]\n"
      "v_mfma_f32_16x16x32_bf16 v[80:83], v[148:151], a[176:179], v[80:83]\n"
      "v_mfma_f32_16x16x32_bf16 v[84:87], v[148:151], a[180:183], v[84:87]\n"
      "v_mfma_f32_16x16x32_bf16 v[88:91], v[148:151], a[184:187], v[88:91]\n"
      "v_mfma_f32_16x16x32_bf16 v[92:95], v[148:151], a[188:191], v[92:95]\n"
      "s_waitcnt vmcnt(14)\n"
      "v_mfma_f32_16x16x32_bf16 v[72:75], v[152:155], a[200:203], v[72:75]\n"
      "v_mfma_f32_16x16x32_bf16 v[76:79], v[152:155], a[204:207], v[76:79]\n"
      "v_mfma_f32_16x16x32_bf16 v[80:83], v[152:155], a[208:211], v[80:83]\n"
      "v_mfma_f32_16x16x32_bf16 v[84:87], v[152:155], a[212:215], v[84:87]\n"
      "v_mfma_f32_16x16x32_bf16 v[88:91], v[152:155], a[216:219], v[88:91]\n"
      "v_mfma_f32_16x16x32_bf16 v[92:95], v[152:155], a[220:223], v[92:95]\n"
      "v_mfma_f32_16x16x32_bf16 v[72:75], v[156:159], a[232:235], v[72:75]\n"
      "v_mfma_f32_16x16x32_bf16 v[76:79], v[156:159], a[236:239], v[76:79]\n"
      "v_mfma_f32_16x16x32_bf16 v[80:83], v[156:159], a[240:243], v[80:83]\n"
      "v_mfma_f32_16x16x32_bf16 v[84:87], v[156:159], a[244:247], v[84:87]\n"
      "v_mfma_f32_16x16x32_bf16 v[88:91], v[156:159], a[248:251], v[88:91]\n"
      "s_nop 3\n"
      "v_cvt_pk_bf16_f32 v72, v72, v73\n"
      "v_mfma_f32_16x16x32_bf16 v[92:95], v[156:159], a[252:255], v[92:95]\n"
      "s_waitcnt vmcnt(12)\n"
      "v_cvt_pk_bf16_f32 v73, v74, v75\n"
      "v_mfma_f32_16x16x32_bf16 v[104:107], v[160:163], a[8:11], 0\n"
      "ds_write_addtid_b32 v72 offset:6144\n"
      "v_cvt_pk_bf16_f32 v74, v76, v77\n"
      "v_mfma_f32_16x16x32_bf16 v[108:111], v[160:163], a[12:15], 0\n"
      "ds_write_addtid_b32 v73 offset:6400\n"
      "v_cvt_pk_bf16_f32 v75, v78, v79\n"
      "v_mfma_f32_16x16x32_bf16 v[112:115], v[160:163], a[16:19], 0\n"
      "ds_write_addtid_b32 v74 offset:6656\n"
      "v_cvt_pk_bf16_f32 v76, v80, v81\n"
      "v_mfma_f32_16x16x32_bf16 v[116:119], v[160:163], a[20:23], 0\n"
      "ds_write_addtid_b32 v75 offset:6912\n"
      "v_cvt_pk_bf16_f32 v77, v82, v83\n"
      "v_mfma_f32_16x16x32_bf16 v[120:123], v[160:163], a[24:27], 0\n"
      "ds_write_addtid_b32 v76 offset:7168\n"
      "v_cvt_pk_bf16_f32 v78, v84, v85\n"
      "v_mfma_f32_16x16x32_bf16 v[124:127], v[160:163], a[28:31], 0\n"
      "ds_write_addtid_b32 v77 offset:7424\n"
      "v_cvt_pk_bf16_f32 v79, v86, v87\n"
      "v_mfma_f32_16x16x32_bf16 v[104:107], v[164:167], a[40:43], v[104:107]\n"
      "ds_write_addtid_b32 v78 offset:7680\n"
      "v_cvt_pk_bf16_f32 v80, v88, v89\n"
      "v_mfma_f32_16x16x32_bf16 v[108:111], v[164:167], a[44:47], v[108:111]\n"
      "ds_write_addtid_b32 v79 offset:7936\n"
      "v_cvt_pk_bf16_f32 v81, v90, v91\n"
      "v_mfma_f32_16x16x32_bf16 v[112:115], v[164:167], a[48:51], v[112:115]\n"
      "ds_write_addtid_b32 v80 offset:8192\n"
      "v_cvt_pk_bf16_f32 v82, v92, v93\n"
      "v_mfma_f32_16x16x32_bf16 v[116:119], v[164:167], a[52:55], v[116:119]\n"
      "ds_write_addtid_b32 v81 offset:8448\n"
      "v_cvt_pk_bf16_f32 v83, v94, v95\n"
      "v_mfma_f32_16x16x32_bf16 v[120:123], v[164:167], a[56:59], v[120:123]\n"
      "ds_write_addtid_b32 v82 offset:8704\n"
      "v_mfma_f32_16x16x32_bf16 v[124:127], v[164:167], a[60:63], v[124:127]\n"
      "s_waitcnt vmcnt(10)\n"
      "ds_write_addtid_b32 v83 offset:8960\n"
      "v_mfma_f32_16x16x32_bf16 v[104:107], v[168:171], a[72:75], v[104:107]\n"
      "s_waitcnt vmcnt(2)\n"
      "v_bitop3_b32 v22, v15, v19, s37 bitop3:0x7e\n"
      "v_bitop3_b32 v22, v22, v23, s37 bitop3:0xf6\n"
      "v_cmp_ne_u32 vcc, 0, v22\n"
      "s_nop 1\n"
      "s_cbranch_vccnz .Ls_s16_%=\n"
      ".Ls_o17_%=:\n"
      "v_mfma_f32_16x16x32_bf16 v[108:111], v[168:171], a[76:79], v[108:111]\n"
      "v_mov_b32 v15, v20\n"
      "v_mov_b32 v19, v21\n"
      "v_mfma_f32_16x16x32_bf16 v[112:115], v[168:171], a[80:83], v[112:115]\n"
      "s_waitcnt lgkmcnt(0)\n"
      "s_barrier\n"
      "v_mfma_f32_16x16x32_bf16 v[116:119], v[168:171], a[84:87], v[116:119]\n"
      "ds_read2st64_b32 v[84:85], v244 offset0:24 offset1:25\n"
      "v_mfma_f32_16x16x32_bf16 v[120:123], v[168:171], a[88:91], v[120:123]\n"
      "ds_read2st64_b32 v[86:87], v245 offset0:28 offset1:29\n"
      "v_mfma_f32_16x16x32_bf16 v[124:127], v[168:171], a[92:95], v[124:127]\n"
      "ds_read2st64_b32 v[88:89], v244 offset0:26 offset1:27\n"
      "v_mfma_f32_16x16x32_bf16 v[104:107], v[172:175], a[104:107], v[104:107]\n"
      "ds_read2st64_b32 v[90:91], v245 offset0:30 offset1:31\n"
      "v_mfma_f32_16x16x32_bf16 v[108:111], v[172:175], a[108:111], v[108:111]\n"
      "ds_read2st64_b32 v[92:93], v246 offset0:32 offset1:33\n"
      "v_mfma_f32_16x16x32_bf16 v[112:115], v[172:175], a[112:115], v[112:115]\n"
      "ds_read2st64_b32 v[94:95], v246 offset0:34 offset1:35\n"
      "v_mfma_f32_16x16x32_bf16 v[116:119], v[172:175], a[116:119], v[116:119]\n"
      "s_lshr_b32 s43, s42, 7\n"
      "s_cmp_eq_u32 s43, s36\n"
      "s_cbranch_scc0 .Ls_e18_%=\n"
      "s_lshl2_add_u32 s44, s35, s34\n"
      "s_lshl2_add_u32 s43, s44, s33\n"
      "s_lshl_b32 s43, s43, 12\n"
      "s_add_u32 s43, s43, 524288\n"
      "v_add_u32 v60, s43, v242\n"
      "global_load_dwordx4 v[24:27], v60, s[58:59] offset:0 sc1\n"
      "global_load_dwordx4 v[28:31], v60, s[58:59] offset:1024 sc1\n"
      "global_load_dwordx4 v[32:35], v60, s[58:59] offset:2048 sc1\n"
      "global_load_dwordx4 v[36:39], v60, s[58:59] offset:3072 sc1\n"
      "v_and_b32 v58, 15, v243\n"
      "v_lshrrev_b32 v59, 5, v243\n"
      "v_lshl_or_b32 v58, v59, 4, v58\n"
      "s_lshl4_add_u32 s44, s42, s44\n"
      "s_lshl_b32 s44, s44, 6\n"
      "v_lshl_add_u32 v59, v58, 1, s44\n"
      "global_load_ushort v57, v59, s[16:17]\n"
      "s_mov_b32 s44, 131072\n"
      "v_lshl_add_u32 v58, v58, 2, s44\n"
      "s_lshl_b32 s43, s33, 13\n"
      "v_add_u32 v59, s43, v58\n"
      ".Ls_e18_%=:\n"
      "v_mfma_f32_16x16x32_bf16 v[120:123], v[172:175], a[120:123], v[120:123]\n"
      "v_mfma_f32_16x16x32_bf16 v[64:67], v[252:255], v[12:15], 0\n"
      "v_mfma_f32_16x16x32_bf16 v[124:127], v[172:175], a[124:127], v[124:127]\n"
      "v_mfma_f32_16x16x32_bf16 v[68:71], v[248:251], v[12:15], 0\n"
      "v_mfma_f32_16x16x32_bf16 v[104:107], v[176:179], a[136:139], v[104:107]\n"
      "v_mfma_f32_16x16x32_bf16 v[96:99], v[252:255], v[16:19], 0\n"
      "v_mfma_f32_16x16x32_bf16 v[108:111], v[176:179], a[140:143], v[108:111]\n"
      "s_waitcnt lgkmcnt(0)\n"
      "v_mfma_f32_16x16x32_bf16 v[64:67], v[250:253], v[84:87], v[64:67]\n"
      "v_mfma_f32_16x16x32_bf16 v[112:115], v[176:179], a[144:147], v[112:115]\n"
      "v_mfma_f32_16x16x32_bf16 v[68:71], v[250:253], v[88:91], v[68:71]\n"
      "v_mfma_f32_16x16x32_bf16 v[116:119], v[176:179], a[148:151], v[116:119]\n"
      "v_mfma_f32_16x16x32_bf16 v[64:67], v[252:255], v[92:95], v[64:67]\n"
      "v_mfma_f32_16x16x32_bf16 v[120:123], v[176:179], a[152:155], v[120:123]\n"
      "v_mfma_f32_16x16x32_bf16 v[68:71], v[248:251], v[92:95], v[68:71]\n"
      "v_mfma_f32_16x16x32_bf16 v[124:127], v[176:179], a[156:159], v[124:127]\n"
      "v_mfma_f32_16x16x32_bf16 v[100:103], v[248:251], v[16:19], 0\n"
      "v_mfma_f32_16x16x32_bf16 v[104:107], v[180:183], a[168:171], v[104:107]\n"
      "v_mfma_f32_16x16x32_bf16 v[108:111], v[180:183], a[172:175], v[108:111]\n"
      "v_mfma_f32_16x16x32_bf16 v[112:115], v[180:183], a[176:179], v[112:115]\n"
      "v_mfma_f32_16x16x32_bf16 v[116:119], v[180:183], a[180:183], v[116:119]\n"
      "v_add_u32 v5, 12288, v242\n"
      "v_mfma_f32_16x16x32_bf16 v[120:123], v[180:183], a[184:187], v[120:123]\n"
      "global_load_dwordx4 v[44:47], v5, s[62:63] offset:0 sc1\n"
      "v_mfma_f32_16x16x32_bf16 v[124:127], v[180:183], a[188:191], v[124:127]\n"
      "global_load_dwordx4 v[48:51], v5, s[62:63] offset:1024 sc1\n"
      "v_mfma_f32_16x16x32_bf16 v[104:107], v[184:187], a[200:203], v[104:107]\n"
      "global_load_dwordx4 v[52:55], v5, s[62:63] offset:2048 sc1\n"
      "v_mfma_f32_16x16x32_bf16 v[108:111], v[184:187], a[204:207], v[108:111]\n"
      "v_mfma_f32_16x16x32_bf16 v[112:115], v[184:187], a[208:211], v[112:115]\n"
      "v_mfma_f32_16x16x32_bf16 v[116:119], v[184:187], a[212:215], v[116:119]\n"
      "v_mfma_f32_16x16x32_bf16 v[120:123], v[184:187], a[216:219], v[120:123]\n"
      "v_mfma_f32_16x16x32_bf16 v[124:127], v[184:187], a[220:223], v[124:127]\n"
      "v_mfma_f32_16x16x32_bf16 v[104:107], v[188:191], a[232:235], v[104:107]\n"
      "v_mfma_f32_16x16x32_bf16 v[108:111], v[188:191], a[236:239], v[108:111]\n"
      "v_mfma_f32_16x16x32_bf16 v[112:115], v[188:191], a[240:243], v[112:115]\n"
      "v_mfma_f32_16x16x32_bf16 v[116:119], v[188:191], a[244:247], v[116:119]\n"
      "v_mfma_f32_16x16x32_bf16 v[120:123], v[188:191], a[248:251], v[120:123]\n"
      "s_nop 3\n"
      "v_cvt_pk_bf16_f32 v104, v104, v105\n"
      "v_mfma_f32_16x16x32_bf16 v[124:127], v[188:191], a[252:255], v[124:127]\n"
      "v_cvt_pk_bf16_f32 v105, v106, v107\n"
      "v_mfma_f32_16x16x32_bf16 v[64:67], v[128:131], a[0:3], v[64:67]\n"
      "ds_write_addtid_b32 v104 offset:9216\n"
      "v_cvt_pk_bf16_f32 v106, v108, v109\n"
      "v_mfma_f32_16x16x32_bf16 v[68:71], v[128:131], a[4:7], v[68:71]\n"
      "ds_write_addtid_b32 v105 offset:9472\n"
      "v_cvt_pk_bf16_f32 v107, v110, v111\n"
      "v_mfma_f32_16x16x32_bf16 v[64:67], v[132:135], a[32:35], v[64:67]\n"
      "ds_write_addtid_b32 v106 offset:9728\n"
      "v_cvt_pk_bf16_f32 v108, v112, v113\n"
      "v_mfma_f32_16x16x32_bf16 v[68:71], v[132:135], a[36:39], v[68:71]\n"
      "ds_write_addtid_b32 v107 offset:9984\n"
      "v_cvt_pk_bf16_f32 v109, v114, v115\n"
      "v_mfma_f32_16x16x32_bf16 v[64:67], v[136:139], a[64:67], v[64:67]\n"
      "ds_write_addtid_b32 v108 offset:10240\n"
      "v_cvt_pk_bf16_f32 v110, v116, v117\n"
      "v_mfma_f32_16x16x32_bf16 v[68:71], v[136:139], a[68:71], v[68:71]\n"
      "ds_write_addtid_b32 v109 offset:10496\n"
      "v_cvt_pk_bf16_f32 v111, v118, v119\n"
      "v_mfma_f32_16x16x32_bf16 v[64:67], v[140:143], a[96:99], v[64:67]\n"
      "ds_write_addtid_b32 v110 offset:10752\n"
      "v_cvt_pk_bf16_f32 v112, v120, v121\n"
      "v_mfma_f32_16x16x32_bf16 v[68:71], v[140:143], a[100:103], v[68:71]\n"
      "ds_write_addtid_b32 v111 offset:11008\n"
      "v_cvt_pk_bf16_f32 v113, v122, v123\n"
      "v_mfma_f32_16x16x32_bf16 v[64:67], v[144:147], a[128:131], v[64:67]\n"
      "ds_write_addtid_b32 v112 offset:11264\n"
      "v_cvt_pk_bf16_f32 v114, v124, v125\n"
      "v_mfma_f32_16x16x32_bf16 v[68:71], v[144:147], a[132:135], v[68:71]\n"
      "ds_write_addtid_b32 v113 offset:11520\n"
      "v_cvt_pk_bf16_f32 v115, v126, v127\n"
      "v_mfma_f32_16x16x32_bf16 v[64:67], v[148:151], a[160:163], v[64:67]\n"
      "ds_write_addtid_b32 v114 offset:11776\n"
      "v_add_u32 v6, 24576, v242\n"
      "v_mfma_f32_16x16x32_bf16 v[68:71], v[148:151], a[164:167], v[68:71]\n"
      "global_load_dwordx4 v[72:75], v6, s[62:63] offset:0 sc1\n"
      "v_mfma_f32_16x16x32_bf16 v[64:67], v[152:155], a[192:195], v[64:67]\n"
      "global_load_dwordx4 v[76:79], v6, s[62:63] offset:1024 sc1\n"
      "v_mfma_f32_16x16x32_bf16 v[68:71], v[152:155], a[196:199], v[68:71]\n"
      "global_load_dwordx4 v[80:83], v6, s[62:63] offset:2048 sc1\n"
      "v_mfma_f32_16x16x32_bf16 v[64:67], v[156:159], a[224:227], v[64:67]\n"
      "ds_write_addtid_b32 v115 offset:12032\n"
      "v_mfma_f32_16x16x32_bf16 v[68:71], v[156:159], a[228:231], v[68:71]\n"
      "v_mfma_f32_16x16x32_bf16 v[96:99], v[160:163], a[0:3], v[96:99]\n"
      "v_mfma_f32_16x16x32_bf16 v[100:103], v[160:163], a[4:7], v[100:103]\n"
      "s_lshr_b32 s43, s42, 7\n"
      "s_cmp_eq_u32 s43, s36\n"
      "s_cbranch_scc0 .Ls_e19_%=\n"
      "s_waitcnt vmcnt(6)\n"
      "v_bitop3_b32 v61, v25, v27, s37 bitop3:0x7e\n"
      "v_bitop3_b32 v61, v61, v29, s37 bitop3:0xf6\n"
      "v_bitop3_b32 v61, v61, v31, s37 bitop3:0xf6\n"
      "v_bitop3_b32 v61, v61, v33, s37 bitop3:0xf6\n"
      "v_bitop3_b32 v61, v61, v35, s37 bitop3:0xf6\n"
      "v_bitop3_b32 v61, v61, v37, s37 bitop3:0xf6\n"
      "v_bitop3_b32 v61, v61, v39, s37 bitop3:0xf6\n"
      "v_cmp_ne_u32 vcc, 0, v61\n"
      "s_nop 1\n"
      "s_cbranch_vccnz .Ls_s21_%=\n"
      ".Ls_o22_%=:\n"
      "v_add_f32 v24, v24, v28\n"
      "v_add_f32 v26, v26, v30\n"
      "v_add_f32 v32, v32, v36\n"
      "v_add_f32 v34, v34, v38\n"
      "v_add_f32 v24, v24, v32\n"
      "v_add_f32 v26, v26, v34\n"
      "s_nop 1\n"
      "v_permlane32_swap_b32 v24, v26\n"
      "v_add_f32 v24, v24, v26\n"
      "v_mov_b32 v26, v24\n"
      "s_nop 1\n"
      "v_permlane16_swap_b32 v24, v26\n"
      "v_add_f32 v24, v24, v26\n"
      "ds_write_b32 v59, v24\n"
      "s_waitcnt lgkmcnt(0)\n"
      ".Ls_e19_%=:\n"
      "v_mfma_f32_16x16x32_bf16 v[96:99], v[164:167], a[32:35], v[96:99]\n"
      "s_waitcnt lgkmcnt(0)\n"
      "s_barrier\n"
      "v_mfma_f32_16x16x32_bf16 v[100:103], v[164:167], a[36:39], v[100:103]\n"
      "s_lshr_b32 s43, s42, 5\n"
      "s_lshl2_add_u32 s44, s36, s33\n"
      "s_cmp_eq_u32 s43, s44\n"
      "s_cbranch_scc0 .Ls_e23_%=\n"
      "ds_read_b32 v24, v58 offset:0\n"
      "ds_read_b32 v25, v58 offset:8192\n"
      "ds_read_b32 v26, v58 offset:16384\n"
      "ds_read_b32 v27, v58 offset:24576\n"
      "v_mov_b32 v62, 0\n"
      "v_mov_b32 v63, s37\n"
      "s_or_b32 s43, s34, s35\n"
      "s_cmp_eq_u32 s43, 0\n"
      "s_cbranch_scc0 .Ls_z24_%=\n"
      "s_mov_b64 exec, 1\n"
      "global_store_dword v62, v63, s[58:59] sc1\n"
      ".Ls_z24_%=:\n"
      "s_mov_b64 exec, -1\n"
      "s_waitcnt vmcnt(6)\n"
      "v_lshlrev_b32 v57, 16, v57\n"
      "v_and_b32 v56, 48, v243\n"
      "s_mov_b32 s44, 131072\n"
      "v_lshl_add_u32 v56, v56, 1, s44\n"
      "s_waitcnt lgkmcnt(0)\n"
      "v_add_f32 v24, v24, v25\n"
      "s_waitcnt lgkmcnt(0)\n"
      "v_add_f32 v26, v26, v27\n"
      "v_add_f32 v24, v24, v26\n"
      "v_cvt_pk_bf16_f32 v24, v24, v24\n"
      "v_and_b32 v24, 0xffff0000, v24\n"
      "v_mul_f32 v24, s84, v24\n"
      "v_cvt_pk_bf16_f32 v24, v24, v24\n"
      "v_and_b32 v24, 0xffff0000, v24\n"
      "v_add_f32 v24, v24, v57\n"
      "v_cvt_pk_bf16_f32 v24, v24, v24\n"
      "v_and_b32 v24, 0xffff0000, v24\n"
      "v_sub_f32 v24, v24, v57\n"
      "ds_write_b32 v58, v24 offset:1024\n"
      "ds_read_b128 v[28:31], v56 offset:1024\n"
      "ds_read_b128 v[32:35], v56 offset:1040\n"
      ".Ls_e23_%=:\n"
      "v_mfma_f32_16x16x32_bf16 v[96:99], v[168:171], a[64:67], v[96:99]\n"
      "ds_read2st64_b32 v[116:117], v244 offset0:36 offset1:37\n"
      "v_mfma_f32_16x16x32_bf16 v[100:103], v[168:171], a[68:71], v[100:103]\n"
      "ds_read2st64_b32 v[118:119], v245 offset0:40 offset1:41\n"
      "v_mfma_f32_16x16x32_bf16 v[96:99], v[172:175], a[96:99], v[96:99]\n"
      "ds_read2st64_b32 v[120:121], v244 offset0:38 offset1:39\n"
      "v_mfma_f32_16x16x32_bf16 v[100:103], v[172:175], a[100:103], v[100:103]\n"
      "ds_read2st64_b32 v[122:123], v245 offset0:42 offset1:43\n"
      "v_mfma_f32_16x16x32_bf16 v[96:99], v[176:179], a[128:131], v[96:99]\n"
      "ds_read2st64_b32 v[124:125], v246 offset0:44 offset1:45\n"
      "v_mfma_f32_16x16x32_bf16 v[100:103], v[176:179], a[132:135], v[100:103]\n"
      "ds_read2st64_b32 v[126:127], v246 offset0:46 offset1:47\n"
      "v_mfma_f32_16x16x32_bf16 v[96:99], v[180:183], a[160:163], v[96:99]\n"
      "v_mfma_f32_16x16x32_bf16 v[100:103], v[180:183], a[164:167], v[100:103]\n"
      "v_mfma_f32_16x16x32_bf16 v[96:99], v[184:187], a[192:195], v[96:99]\n"
      "s_waitcnt vmcnt(3)\n"
      "v_bitop3_b32 v54, v47, v51, s37 bitop3:0x7e\n"
      "v_bitop3_b32 v54, v54, v55, s37 bitop3:0xf6\n"
      "v_cmp_ne_u32 vcc, 0, v54\n"
      "s_nop 1\n"
      "s_cbranch_vccnz .Ls_s26_%=\n"
      ".Ls_o27_%=:\n"
      "v_mfma_f32_16x16x32_bf16 v[100:103], v[184:187], a[196:199], v[100:103]\n"
      "v_mov_b32 v47, v52\n"
      "v_mov_b32 v51, v53\n"
      "v_mfma_f32_16x16x32_bf16 v[96:99], v[188:191], a[224:227], v[96:99]\n"
      "s_waitcnt lgkmcnt(0)\n"
      "v_mfma_f32_16x16x32_bf16 v[96:99], v[250:253], v[116:119], v[96:99]\n"
      "v_mfma_f32_16x16x32_bf16 v[100:103], v[188:191], a[228:231], v[100:103]\n"
      "v_mfma_f32_16x16x32_bf16 v[100:103], v[250:253], v[120:123], v[100:103]\n"
      "v_mfma_f32_16x16x32_bf16 v[96:99], v[252:255], v[124:127], v[96:99]\n"
      "v_mfma_f32_16x16x32_bf16 v[100:103], v[248:251], v[124:127], v[100:103]\n"
      "v_mfma_f32_16x16x32_bf16 v[64:67], v[252:255], v[44:47], v[64:67]\n"
      "v_mfma_f32_16x16x32_bf16 v[68:71], v[248:251], v[44:47], v[68:71]\n"
      "v_mfma_f32_16x16x32_bf16 v[96:99], v[252:255], v[48:51], v[96:99]\n"
      "v_mfma_f32_16x16x32_bf16 v[100:103], v[248:251], v[48:51], v[100:103]\n"
      "s_lshr_b32 s43, s42, 5\n"
      "s_lshl2_add_u32 s44, s36, s33\n"
      "s_cmp_eq_u32 s43, s44\n"
      "s_cbranch_scc0 .Ls_e28_%=\n"
      "v_lshrrev_b32 v24, 10, v7\n"
      "v_subrev_u32 v24, s42, v24\n"
      "v_cmp_eq_u32 vcc, 0, v24\n"
      "s_nop 1\n"
      "s_mov_b64 exec, vcc\n"
      "v_fma_f32 v208, v205, v28, v208\n"
      "v_fma_f32 v209, v205, v29, v209\n"
      "v_fma_f32 v210, v205, v30, v210\n"
      "v_fma_f32 v211, v205, v31, v211\n"
      "v_fma_f32 v216, v205, v32, v216\n"
      "v_fma_f32 v217, v205, v33, v217\n"
      "v_fma_f32 v218, v205, v34, v218\n"
      "v_fma_f32 v219, v205, v35, v219\n"
      "s_mov_b64 exec, -1\n"
      "v_cmp_eq_u32 vcc, -16, v24\n"
      "s_nop 1\n"
      "s_mov_b64 exec, vcc\n"
      "v_fma_f32 v212, v207, v28, v212\n"
      "v_fma_f32 v213, v207, v29, v213\n"
      "v_fma_f32 v214, v207, v30, v214\n"
      "v_fma_f32 v215, v207, v31, v215\n"
      "v_fma_f32 v220, v207, v32, v220\n"
      "v_fma_f32 v221, v207, v33, v221\n"
      "v_fma_f32 v222, v207, v34, v222\n"
      "v_fma_f32 v223, v207, v35, v223\n"
      "s_mov_b64 exec, -1\n"
      ".Ls_e28_%=:\n"
      "s_waitcnt vmcnt(0)\n"
      "v_bitop3_b32 v82, v75, v79, s37 bitop3:0x7e\n"
      "v_bitop3_b32 v82, v82, v83, s37 bitop3:0xf6\n"
      "v_cmp_ne_u32 vcc, 0, v82\n"
      "s_nop 1\n"
      "s_cbranch_vccnz .Ls_s30_%=\n"
      ".Ls_o31_%=:\n"
      "v_mov_b32 v75, v80\n"
      "v_mov_b32 v79, v81\n"
      "s_nop 0\n"
      "v_mfma_f32_16x16x32_bf16 v[64:67], v[252:255], v[72:75], v[64:67]\n"
      "v_mfma_f32_16x16x32_bf16 v[96:99], v[252:255], v[76:79], v[96:99]\n"
      "v_mfma_f32_16x16x32_bf16 v[68:71], v[248:251], v[72:75], v[68:71]\n"
      "v_mfma_f32_16x16x32_bf16 v[100:103], v[248:251], v[76:79], v[100:103]\n"
      "s_nop 4\n"
      "v_fma_f32 v208, v204, v64, v208\n"
      "v_fma_f32 v209, v204, v65, v209\n"
      "v_fma_f32 v210, v204, v66, v210\n"
      "v_fma_f32 v211, v204, v67, v211\n"
      "v_fma_f32 v216, v204, v96, v216\n"
      "v_fma_f32 v217, v204, v97, v217\n"
      "v_fma_f32 v218, v204, v98, v218\n"
      "v_fma_f32 v219, v204, v99, v219\n"
      "v_cvt_pk_bf16_f32 v104, v208, v209\n"
      "v_cvt_pk_bf16_f32 v105, v210, v211\n"
      "v_cvt_pk_bf16_f32 v106, v216, v217\n"
      "v_cvt_pk_bf16_f32 v107, v218, v219\n"
      "global_store_dwordx4 v7, v[104:107], s[20:21]\n"
      "v_fma_f32 v212, v206, v68, v212\n"
      "v_fma_f32 v213, v206, v69, v213\n"
      "v_fma_f32 v214, v206, v70, v214\n"
      "v_fma_f32 v215, v206, v71, v215\n"
      "v_fma_f32 v220, v206, v100, v220\n"
      "v_fma_f32 v221, v206, v101, v221\n"
      "v_fma_f32 v222, v206, v102, v222\n"
      "v_fma_f32 v223, v206, v103, v223\n"
      "v_cvt_pk_bf16_f32 v108, v212, v213\n"
      "v_cvt_pk_bf16_f32 v109, v214, v215\n"
      "v_cvt_pk_bf16_f32 v110, v220, v221\n"
      "v_cvt_pk_bf16_f32 v111, v222, v223\n"
      "global_store_dwordx4 v7, v[108:111], s[68:69]\n"
      "s_endpgm\n"
      ".Lprod_%=:\n"
      "s_waitcnt lgkmcnt(0)\n"
      "s_add_u32 s68, s16, s44\n"
      "s_addc_u32 s69, s17, 0\n"
      "s_add_u32 s12, s12, s45\n"
      "s_addc_u32 s13, s13, 0\n"
      "s_add_u32 s58, s18, 4718592\n"
      "s_addc_u32 s59, s19, 0\n"
      "v_mov_b32 v235, 0\n"
      "global_load_dword v235, v235, s[58:59] sc1\n"
      "v_and_b32 v238, 15, v243\n"
      "v_lshrrev_b32 v239, 4, v243\n"
      "v_xor_b32 v239, v239, v238\n"
      "v_and_b32 v239, 7, v239\n"
      "v_lshlrev_b32 v239, 4, v239\n"
      "v_lshl_or_b32 v238, v238, 7, v239\n"
      "v_add_u32 v248, s41, v238\n"
      "v_xor_b32 v249, 64, v248\n"
      "v_add_u32 v250, 65536, v238\n"
      "v_xor_b32 v251, 64, v250\n"
      "v_and_b32 v252, 8, v243\n"
      "v_lshlrev_b32 v252, 7, v252\n"
      "v_and_or_b32 v252, v243, 48, v252\n"
      "v_add_u32 v252, s41, v252\n"
      "v_add_u32 v238, s80, v242\n"
      "v_and_b32 v238, 0x3ff, v238\n"
      "v_lshl_or_b32 v244, v236, 12, v237\n"
      "v_add_u32 v245, 31744, v244\n"
      "v_add_u32 v246, 63488, v244\n"
      "v_add_u32 v247, 95232, v244\n"
      "s_bitcmp1_b32 s34, 1\n"
      "s_cselect_b64 s[8:9], s[10:11], s[8:9]\n"
      "s_and_b32 s45, s34, 1\n"
      "s_lshl2_add_u32 s45, s45, s33\n"
      "s_lshl_b32 s45, s45, 9\n"
      "s_lshl_b32 s76, s36, 19\n"
      "s_add_u32 s45, s45, s76\n"
      "s_add_u32 s8, s8, s45\n"
      "s_addc_u32 s9, s9, 0\n"
      "s_lshr_b32 s39, s32, 6\n"
      "s_and_b32 s45, s35, 2\n"
      "s_xor_b32 s39, s39, s45\n"
      "s_xor_b32 s39, s39, s33\n"
      "s_lshl_b32 s45, s39, 7\n"
      "s_add_u32 s76, s33, 0\n"
      "s_and_b32 s76, s76, 3\n"
      "s_lshl_b32 s76, s76, 17\n"
      "s_or_b32 s76, s76, s45\n"
      "s_add_u32 s77, s33, 1\n"
      "s_and_b32 s77, s77, 3\n"
      "s_lshl_b32 s77, s77, 17\n"
      "s_or_b32 s77, s77, s45\n"
      "s_add_u32 s78, s33, 2\n"
      "s_and_b32 s78, s78, 3\n"
      "s_lshl_b32 s78, s78, 17\n"
      "s_or_b32 s78, s78, s45\n"
      "s_add_u32 s79, s33, 3\n"
      "s_and_b32 s79, s79, 3\n"
      "s_lshl_b32 s79, s79, 17\n"
      "s_or_b32 s79, s79, s45\n"
      "s_lshl2_add_u32 s43, s34, s35\n"
      "s_and_b32 s44, s40, 31\n"
      "s_mul_i32 s45, s43, 266240\n"
      "s_lshl_b32 s42, s44, 13\n"
      "s_add_u32 s45, s45, s42\n"
      "s_add_u32 s45, s45, 4096\n"
      "s_add_u32 s62, s18, s45\n"
      "s_addc_u32 s63, s19, 0\n"
      "s_bitcmp1_b32 s34, 1\n"
      "s_cselect_b32 s85, 0x80008000, 0\n"
      "s_lshl3_add_u32 s42, s43, s83\n"
      "s_lshl2_add_u32 s42, s42, s33\n"
      "s_lshl_b32 s42, s42, 9\n"
      "s_add_u32 s42, s42, 1048576\n"
      "s_mov_b32 s64, 0xaaaaaaaa\n"
      "s_mov_b32 s65, 0xaaaaaaaa\n"
      "s_mov_b32 s66, 0xcccccccc\n"
      "s_mov_b32 s67, 0xcccccccc\n"
      "s_add_u32 s45, s80, 512\n"
      "s_and_b32 s45, s45, 0x380\n"
      "s_add_u32 s46, s68, s45\n"
      "s_addc_u32 s47, s69, 0\n"
      "s_add_u32 m0, s41, 8192\n"
      "s_nop 0\n"
      "global_load_lds_dwordx4 v240, s[46:47] offset:0\n"
      "global_load_lds_dwordx4 v241, s[46:47] offset:1024\n"
      "global_load_lds_dwordx4 v254, s[46:47] offset:2048\n"
      "global_load_lds_dwordx4 v255, s[46:47] offset:3072\n"
      "s_add_u32 s45, s80, 768\n"
      "s_and_b32 s45, s45, 0x380\n"
      "s_add_u32 s46, s68, s45\n"
      "s_addc_u32 s47, s69, 0\n"
      "s_add_u32 m0, s41, 12288\n"
      "s_nop 0\n"
      "global_load_lds_dwordx4 v240, s[46:47] offset:0\n"
      "global_load_lds_dwordx4 v241, s[46:47] offset:1024\n"
      "global_load_lds_dwordx4 v254, s[46:47] offset:2048\n"
      "global_load_lds_dwordx4 v255, s[46:47] offset:3072\n"
      "s_waitcnt vmcnt(13)\n"
      "s_barrier\n"
      "ds_read_b128 v[0:3], v248 offset:0\n"
      "ds_read_b128 v[4:7], v248 offset:2048\n"
      "ds_read_b128 v[8:11], v249 offset:0\n"
      "ds_read_b128 v[12:15], v249 offset:2048\n"
      "ds_read_b128 a[192:195], v250 offset:0\n"
      "ds_read_b128 a[196:199], v250 offset:2048\n"
      "ds_read_b128 a[200:203], v250 offset:32768\n"
      "ds_read_b128 a[204:207], v251 offset:0\n"
      "ds_read_b128 a[208:211], v251 offset:2048\n"
      "ds_read_b128 a[212:215], v251 offset:32768\n"
      "s_waitcnt lgkmcnt(5)\n"
      "v_mfma_f32_16x16x32_bf16 v[192:195], v[0:3], a[192:195], 0\n"
      "s_add_u32 s70, s34, 1\n"
      "s_and_b32 s70, s70, 3\n"
      "s_lshl_b32 s70, s70, 15\n"
      "s_or_b32 s70, s70, s81\n"
      "s_xor_b32 s70, s70, 128\n"
      "s_add_u32 s70, s12, s70\n"
      "s_addc_u32 s71, s13, 0\n"
      "s_add_u32 m0, s82, 81920\n"
      "v_mfma_f32_16x16x32_bf16 v[196:199], v[4:7], a[192:195], 0\n"
      "global_load_lds_dwordx4 v240, s[70:71] offset:0\n"
      "ds_read_b128 a[216:219], v250 offset:4096\n"
      "s_waitcnt lgkmcnt(5)\n"
      "v_mfma_f32_16x16x32_bf16 v[200:203], v[0:3], a[196:199], 0\n"
      "ds_read_b128 a[220:223], v250 offset:6144\n"
      "ds_read_b128 a[224:227], v250 offset:36864\n"
      "v_mfma_f32_16x16x32_bf16 v[204:207], v[4:7], a[196:199], 0\n"
      "global_load_lds_dwordx4 v241, s[70:71] offset:1024\n"
      "s_waitcnt vmcnt(11)\n"
      "ds_read_b128 v[16:19], v248 offset:4096\n"
      "s_waitcnt lgkmcnt(7)\n"
      "v_mfma_f32_16x16x32_bf16 v[208:211], v[0:3], a[200:203], 0\n"
      "ds_read_b128 v[20:23], v248 offset:6144\n"
      "ds_read_b128 v[24:27], v249 offset:4096\n"
      "v_mfma_f32_16x16x32_bf16 v[212:215], v[4:7], a[200:203], 0\n"
      "global_load_lds_dwordx4 v254, s[70:71] offset:2048\n"
      "ds_read_b128 v[28:31], v249 offset:6144\n"
      "s_waitcnt lgkmcnt(9)\n"
      "v_mfma_f32_16x16x32_bf16 v[192:195], v[8:11], a[204:207], v[192:195]\n"
      "ds_read_b128 a[228:231], v251 offset:4096\n"
      "ds_read_b128 a[232:235], v251 offset:6144\n"
      "v_mfma_f32_16x16x32_bf16 v[196:199], v[12:15], a[204:207], v[196:199]\n"
      "global_load_lds_dwordx4 v255, s[70:71] offset:3072\n"
      "ds_read_b128 a[236:239], v251 offset:36864\n"
      "s_waitcnt lgkmcnt(11)\n"
      "v_mfma_f32_16x16x32_bf16 v[200:203], v[8:11], a[208:211], v[200:203]\n"
      "s_add_u32 s70, s34, 2\n"
      "s_and_b32 s70, s70, 3\n"
      "s_lshl_b32 s70, s70, 15\n"
      "s_or_b32 s70, s70, s81\n"
      "s_xor_b32 s70, s70, 128\n"
      "s_add_u32 s70, s12, s70\n"
      "s_addc_u32 s71, s13, 0\n"
      "s_add_u32 m0, s82, 114688\n"
      "v_mfma_f32_16x16x32_bf16 v[204:207], v[12:15], a[208:211], v[204:207]\n"
      "global_load_lds_dwordx4 v240, s[70:71] offset:0\n"
      "s_waitcnt lgkmcnt(10)\n"
      "v_mfma_f32_16x16x32_bf16 v[208:211], v[8:11], a[212:215], v[208:211]\n"
      "v_mfma_f32_16x16x32_bf16 v[212:215], v[12:15], a[212:215], v[212:215]\n"
      "global_load_lds_dwordx4 v241, s[70:71] offset:1024\n"
      "s_waitcnt lgkmcnt(6)\n"
      "v_mfma_f32_16x16x32_bf16 v[192:195], v[16:19], a[216:219], v[192:195]\n"
      "s_add_u32 s70, s80, 128\n"
      "s_and_b32 s70, s70, 0x380\n"
      "s_add_u32 s70, s68, s70\n"
      "s_addc_u32 s71, s69, 0\n"
      "s_add_u32 m0, s41, 0\n"
      "s_waitcnt lgkmcnt(5)\n"
      "v_mfma_f32_16x16x32_bf16 v[196:199], v[20:23], a[216:219], v[196:199]\n"
      "global_load_lds_dwordx4 v240, s[70:71] offset:0\n"
      "ds_read_b128 a[240:243], v250 offset:8192\n"
      "v_mfma_f32_16x16x32_bf16 v[200:203], v[16:19], a[220:223], v[200:203]\n"
      "ds_read_b128 a[244:247], v250 offset:10240\n"
      "ds_read_b128 a[248:251], v250 offset:40960\n"
      "v_mfma_f32_16x16x32_bf16 v[204:207], v[20:23], a[220:223], v[204:207]\n"
      "global_load_lds_dwordx4 v241, s[70:71] offset:1024\n"
      "s_waitcnt vmcnt(12)\n"
      "ds_read_b128 v[32:35], v248 offset:8192\n"
      "v_mfma_f32_16x16x32_bf16 v[208:211], v[16:19], a[224:227], v[208:211]\n"
      "ds_read_b128 v[36:39], v248 offset:10240\n"
      "ds_read_b128 v[40:43], v249 offset:8192\n"
      "v_mfma_f32_16x16x32_bf16 v[212:215], v[20:23], a[224:227], v[212:215]\n"
      "global_load_lds_dwordx4 v254, s[70:71] offset:2048\n"
      "ds_read_b128 v[44:47], v249 offset:10240\n"
      "s_waitcnt lgkmcnt(9)\n"
      "v_mfma_f32_16x16x32_bf16 v[192:195], v[24:27], a[228:231], v[192:195]\n"
      "ds_read_b128 a[252:255], v251 offset:8192\n"
      "ds_read_b128 a[192:195], v251 offset:10240\n"
      "v_mfma_f32_16x16x32_bf16 v[196:199], v[28:31], a[228:231], v[196:199]\n"
      "global_load_lds_dwordx4 v255, s[70:71] offset:3072\n"
      "ds_read_b128 a[196:199], v251 offset:40960\n"
      "s_waitcnt lgkmcnt(11)\n"
      "v_mfma_f32_16x16x32_bf16 v[200:203], v[24:27], a[232:235], v[200:203]\n"
      "s_add_u32 s70, s80, 384\n"
      "s_and_b32 s70, s70, 0x380\n"
      "s_add_u32 s70, s68, s70\n"
      "s_addc_u32 s71, s69, 0\n"
      "s_add_u32 m0, s41, 4096\n"
      "v_mfma_f32_16x16x32_bf16 v[204:207], v[28:31], a[232:235], v[204:207]\n"
      "global_load_lds_dwordx4 v240, s[70:71] offset:0\n"
      "s_waitcnt lgkmcnt(10)\n"
      "v_mfma_f32_16x16x32_bf16 v[208:211], v[24:27], a[236:239], v[208:211]\n"
      "v_mfma_f32_16x16x32_bf16 v[212:215], v[28:31], a[236:239], v[212:215]\n"
      "global_load_lds_dwordx4 v241, s[70:71] offset:1024\n"
      "s_waitcnt lgkmcnt(6)\n"
      "v_mfma_f32_16x16x32_bf16 v[192:195], v[32:35], a[240:243], v[192:195]\n"
      "ds_read_b128 a[200:203], v250 offset:12288\n"
      "ds_read_b128 a[204:207], v250 offset:14336\n"
      "s_waitcnt lgkmcnt(7)\n"
      "v_mfma_f32_16x16x32_bf16 v[196:199], v[36:39], a[240:243], v[196:199]\n"
      "global_load_lds_dwordx4 v254, s[70:71] offset:2048\n"
      "ds_read_b128 a[208:211], v250 offset:45056\n"
      "v_mfma_f32_16x16x32_bf16 v[200:203], v[32:35], a[244:247], v[200:203]\n"
      "s_waitcnt vmcnt(13)\n"
      "ds_read_b128 v[48:51], v248 offset:12288\n"
      "v_mfma_f32_16x16x32_bf16 v[204:207], v[36:39], a[244:247], v[204:207]\n"
      "global_load_lds_dwordx4 v255, s[70:71] offset:3072\n"
      "ds_read_b128 v[52:55], v248 offset:14336\n"
      "v_mfma_f32_16x16x32_bf16 v[208:211], v[32:35], a[248:251], v[208:211]\n"
      "s_add_u32 s70, s80, 640\n"
      "s_and_b32 s70, s70, 0x380\n"
      "s_add_u32 s70, s68, s70\n"
      "s_addc_u32 s71, s69, 0\n"
      "s_waitcnt lgkmcnt(8)\n"
      "s_add_u32 m0, s41, 8192\n"
      "v_mfma_f32_16x16x32_bf16 v[212:215], v[36:39], a[248:251], v[212:215]\n"
      "global_load_lds_dwordx4 v240, s[70:71] offset:0\n"
      "ds_read_b128 v[56:59], v249 offset:12288\n"
      "s_waitcnt lgkmcnt(8)\n"
      "v_mfma_f32_16x16x32_bf16 v[192:195], v[40:43], a[252:255], v[192:195]\n"
      "ds_read_b128 v[60:63], v249 offset:14336\n"
      "ds_read_b128 a[212:215], v251 offset:12288\n"
      "v_readfirstlane_b32 s37, v235\n"
      "v_mfma_f32_16x16x32_bf16 v[196:199], v[44:47], a[252:255], v[196:199]\n"
      "s_nop 0\n"
      "s_mul_i32 s37, s37, 0x9E3779B1\n"
      "global_load_lds_dwordx4 v241, s[70:71] offset:1024\n"
      "s_waitcnt lgkmcnt(9)\n"
      "v_mfma_f32_16x16x32_bf16 v[200:203], v[40:43], a[192:195], v[200:203]\n"
      "s_add_u32 s37, s37, 0x7F4A7C15\n"
      "ds_read_b128 a[216:219], v251 offset:14336\n"
      "ds_read_b128 a[220:223], v251 offset:45056\n"
      "s_mov_b32 s38, 0x8000\n"
      "v_mfma_f32_16x16x32_bf16 v[204:207], v[44:47], a[192:195], v[204:207]\n"
      "global_load_lds_dwordx4 v254, s[70:71] offset:2048\n"
      "s_waitcnt lgkmcnt(10)\n"
      "v_mfma_f32_16x16x32_bf16 v[208:211], v[40:43], a[196:199], v[208:211]\n"
      "v_mfma_f32_16x16x32_bf16 v[212:215], v[44:47], a[196:199], v[212:215]\n"
      "global_load_lds_dwordx4 v255, s[70:71] offset:3072\n"
      "s_waitcnt lgkmcnt(6)\n"
      "v_mfma_f32_16x16x32_bf16 v[192:195], v[48:51], a[200:203], v[192:195]\n"
      "s_add_u32 s70, s80, 896\n"
      "s_and_b32 s70, s70, 0x380\n"
      "s_add_u32 s70, s68, s70\n"
      "s_addc_u32 s71, s69, 0\n"
      "s_waitcnt lgkmcnt(3)\n"
      "s_add_u32 m0, s41, 12288\n"
      "v_mfma_f32_16x16x32_bf16 v[196:199], v[52:55], a[200:203], v[196:199]\n"
      "global_load_lds_dwordx4 v240, s[70:71] offset:0\n"
      "s_waitcnt vmcnt(13)\n"
      "s_barrier\n"
      "v_mfma_f32_16x16x32_bf16 v[200:203], v[48:51], a[204:207], v[200:203]\n"
      "ds_read_b128 a[224:227], v250 offset:16384\n"
      "ds_read_b128 a[228:231], v250 offset:18432\n"
      "v_mfma_f32_16x16x32_bf16 v[204:207], v[52:55], a[204:207], v[204:207]\n"
      "global_load_lds_dwordx4 v241, s[70:71] offset:1024\n"
      "ds_read_b128 a[232:235], v250 offset:49152\n"
      "v_mfma_f32_16x16x32_bf16 v[208:211], v[48:51], a[208:211], v[208:211]\n"
      "s_waitcnt vmcnt(10)\n"
      "ds_read_b128 v[64:67], v248 offset:0\n"
      "v_mfma_f32_16x16x32_bf16 v[212:215], v[52:55], a[208:211], v[212:215]\n"
      "global_load_lds_dwordx4 v254, s[70:71] offset:2048\n"
      "ds_read_b128 v[68:71], v248 offset:2048\n"
      "s_waitcnt lgkmcnt(7)\n"
      "v_mfma_f32_16x16x32_bf16 v[192:195], v[56:59], a[212:215], v[192:195]\n"
      "ds_read_b128 v[72:75], v249 offset:0\n"
      "ds_read_b128 v[76:79], v249 offset:2048\n"
      "v_mfma_f32_16x16x32_bf16 v[196:199], v[60:63], a[212:215], v[196:199]\n"
      "global_load_lds_dwordx4 v255, s[70:71] offset:3072\n"
      "ds_read_b128 a[236:239], v251 offset:16384\n"
      "s_waitcnt lgkmcnt(9)\n"
      "v_mfma_f32_16x16x32_bf16 v[200:203], v[56:59], a[216:219], v[200:203]\n"
      "ds_read_b128 a[240:243], v251 offset:18432\n"
      "ds_read_b128 a[244:247], v251 offset:49152\n"
      "v_mfma_f32_16x16x32_bf16 v[204:207], v[60:63], a[216:219], v[204:207]\n"
      "s_waitcnt lgkmcnt(10)\n"
      "v_mfma_f32_16x16x32_bf16 v[208:211], v[56:59], a[220:223], v[208:211]\n"
      "v_mfma_f32_16x16x32_bf16 v[212:215], v[60:63], a[220:223], v[212:215]\n"
      "s_waitcnt lgkmcnt(6)\n"
      "v_mfma_f32_16x16x32_bf16 v[192:195], v[64:67], a[224:227], v[192:195]\n"
      "s_waitcnt lgkmcnt(3)\n"
      "s_add_u32 m0, s41, 0\n"
      "s_nop 0\n"
      "global_load_lds_dwordx4 v238, s[24:25]\n"
      "s_add_u32 m0, s41, 1024\n"
      "s_nop 0\n"
      "global_load_lds_dwordx4 v238, s[22:23]\n"
      "v_mfma_f32_16x16x32_bf16 v[196:199], v[68:71], a[224:227], v[196:199]\n"
      "ds_read_b128 a[248:251], v250 offset:20480\n"
      "ds_read_b128 a[252:255], v250 offset:22528\n"
      "v_mfma_f32_16x16x32_bf16 v[200:203], v[64:67], a[228:231], v[200:203]\n"
      "s_add_u32 s70, s34, 2\n"
      "s_and_b32 s70, s70, 3\n"
      "s_lshl_b32 s70, s70, 15\n"
      "s_or_b32 s70, s70, s81\n"
      "s_add_u32 s70, s12, s70\n"
      "s_addc_u32 s71, s13, 0\n"
      "s_add_u32 m0, s82, 98304\n"
      "v_mfma_f32_16x16x32_bf16 v[204:207], v[68:71], a[228:231], v[204:207]\n"
      "global_load_lds_dwordx4 v254, s[70:71] offset:2048\n"
      "ds_read_b128 a[192:195], v250 offset:53248\n"
      "v_mfma_f32_16x16x32_bf16 v[208:211], v[64:67], a[232:235], v[208:211]\n"
      "s_waitcnt vmcnt(11)\n"
      "ds_read_b128 v[80:83], v248 offset:4096\n"
      "v_mfma_f32_16x16x32_bf16 v[212:215], v[68:71], a[232:235], v[212:215]\n"
      "ds_read_b128 v[84:87], v248 offset:6144\n"
      "ds_read_b128 v[88:91], v249 offset:4096\n"
      "s_waitcnt lgkmcnt(8)\n"
      "v_mfma_f32_16x16x32_bf16 v[192:195], v[72:75], a[236:239], v[192:195]\n"
      "global_load_lds_dwordx4 v255, s[70:71] offset:3072\n"
      "ds_read_b128 v[92:95], v249 offset:6144\n"
      "v_mfma_f32_16x16x32_bf16 v[196:199], v[76:79], a[236:239], v[196:199]\n"
      "ds_read_b128 a[196:199], v251 offset:20480\n"
      "ds_read_b128 a[200:203], v251 offset:22528\n"
      "s_waitcnt lgkmcnt(10)\n"
      "v_mfma_f32_16x16x32_bf16 v[200:203], v[72:75], a[240:243], v[200:203]\n"
      "s_add_u32 s70, s34, 2\n"
      "s_and_b32 s70, s70, 3\n"
      "s_lshl_b32 s70, s70, 15\n"
      "s_or_b32 s70, s70, s81\n"
      "s_xor_b32 s70, s70, 128\n"
      "s_add_u32 s70, s12, s70\n"
      "s_addc_u32 s71, s13, 0\n"
      "s_add_u32 m0, s82, 114688\n"
      "v_mfma_f32_16x16x32_bf16 v[204:207], v[76:79], a[240:243], v[204:207]\n"
      "global_load_lds_dwordx4 v254, s[70:71] offset:2048\n"
      "ds_read_b128 a[204:207], v251 offset:53248\n"
      "s_waitcnt lgkmcnt(10)\n"
      "v_mfma_f32_16x16x32_bf16 v[208:211], v[72:75], a[244:247], v[208:211]\n"
      "v_mfma_f32_16x16x32_bf16 v[212:215], v[76:79], a[244:247], v[212:215]\n"
      "s_waitcnt lgkmcnt(6)\n"
      "v_mfma_f32_16x16x32_bf16 v[192:195], v[80:83], a[248:251], v[192:195]\n"
      "global_load_lds_dwordx4 v255, s[70:71] offset:3072\n"
      "ds_read_b128 a[208:211], v250 offset:24576\n"
      "s_waitcnt lgkmcnt(6)\n"
      "v_mfma_f32_16x16x32_bf16 v[196:199], v[84:87], a[248:251], v[196:199]\n"
      "ds_read_b128 a[212:215], v250 offset:26624\n"
      "ds_read_b128 a[216:219], v250 offset:57344\n"
      "v_mfma_f32_16x16x32_bf16 v[200:203], v[80:83], a[252:255], v[200:203]\n"
      "s_add_u32 s70, s34, 3\n"
      "s_and_b32 s70, s70, 3\n"
      "s_lshl_b32 s70, s70, 15\n"
      "s_or_b32 s70, s70, s81\n"
      "s_add_u32 s70, s12, s70\n"
      "s_addc_u32 s71, s13, 0\n"
      "s_add_u32 m0, s82, 131072\n"
      "v_mfma_f32_16x16x32_bf16 v[204:207], v[84:87], a[252:255], v[204:207]\n"
      "global_load_lds_dwordx4 v240, s[70:71] offset:0\n"
      "s_waitcnt vmcnt(11)\n"
      "ds_read_b128 v[96:99], v248 offset:8192\n"
      "v_mfma_f32_16x16x32_bf16 v[208:211], v[80:83], a[192:195], v[208:211]\n"
      "ds_read_b128 v[100:103], v248 offset:10240\n"
      "ds_read_b128 v[104:107], v249 offset:8192\n"
      "v_mfma_f32_16x16x32_bf16 v[212:215], v[84:87], a[192:195], v[212:215]\n"
      "ds_read_b128 v[108:111], v249 offset:10240\n"
      "ds_read_b128 a[220:223], v251 offset:24576\n"
      "s_waitcnt lgkmcnt(10)\n"
      "v_mfma_f32_16x16x32_bf16 v[192:195], v[88:91], a[196:199], v[192:195]\n"
      "global_load_lds_dwordx4 v241, s[70:71] offset:1024\n"
      "ds_read_b128 a[224:227], v251 offset:26624\n"
      "v_mfma_f32_16x16x32_bf16 v[196:199], v[92:95], a[196:199], v[196:199]\n"
      "ds_read_b128 a[228:231], v251 offset:57344\n"
      "s_waitcnt lgkmcnt(11)\n"
      "v_mfma_f32_16x16x32_bf16 v[200:203], v[88:91], a[200:203], v[200:203]\n"
      "v_mfma_f32_16x16x32_bf16 v[204:207], v[92:95], a[200:203], v[204:207]\n"
      "global_load_lds_dwordx4 v254, s[70:71] offset:2048\n"
      "s_waitcnt lgkmcnt(10)\n"
      "v_mfma_f32_16x16x32_bf16 v[208:211], v[88:91], a[204:207], v[208:211]\n"
      "v_mfma_f32_16x16x32_bf16 v[212:215], v[92:95], a[204:207], v[212:215]\n"
      "s_waitcnt lgkmcnt(6)\n"
      "v_mfma_f32_16x16x32_bf16 v[192:195], v[96:99], a[208:211], v[192:195]\n"
      "global_load_lds_dwordx4 v255, s[70:71] offset:3072\n"
      "ds_read_b128 a[232:235], v250 offset:28672\n"
      "s_waitcnt lgkmcnt(6)\n"
      "v_mfma_f32_16x16x32_bf16 v[196:199], v[100:103], a[208:211], v[196:199]\n"
      "ds_read_b128 a[236:239], v250 offset:30720\n"
      "ds_read_b128 a[240:243], v250 offset:61440\n"
      "v_mfma_f32_16x16x32_bf16 v[200:203], v[96:99], a[212:215], v[200:203]\n"
      "s_add_u32 s70, s34, 3\n"
      "s_and_b32 s70, s70, 3\n"
      "s_lshl_b32 s70, s70, 15\n"
      "s_or_b32 s70, s70, s81\n"
      "s_xor_b32 s70, s70, 128\n"
      "s_add_u32 s70, s12, s70\n"
      "s_addc_u32 s71, s13, 0\n"
      "s_add_u32 m0, s82, 147456\n"
      "v_mfma_f32_16x16x32_bf16 v[204:207], v[100:103], a[212:215], v[204:207]\n"
      "global_load_lds_dwordx4 v240, s[70:71] offset:0\n"
      "s_waitcnt vmcnt(11)\n"
      "ds_read_b128 v[112:115], v248 offset:12288\n"
      "v_mfma_f32_16x16x32_bf16 v[208:211], v[96:99], a[216:219], v[208:211]\n"
      "ds_read_b128 v[116:119], v248 offset:14336\n"
      "ds_read_b128 v[120:123], v249 offset:12288\n"
      "v_mfma_f32_16x16x32_bf16 v[212:215], v[100:103], a[216:219], v[212:215]\n"
      "ds_read_b128 v[124:127], v249 offset:14336\n"
      "ds_read_b128 a[244:247], v251 offset:28672\n"
      "s_waitcnt lgkmcnt(10)\n"
      "v_mfma_f32_16x16x32_bf16 v[192:195], v[104:107], a[220:223], v[192:195]\n"
      "global_load_lds_dwordx4 v241, s[70:71] offset:1024\n"
      "ds_read_b128 a[248:251], v251 offset:30720\n"
      "v_mfma_f32_16x16x32_bf16 v[196:199], v[108:111], a[220:223], v[196:199]\n"
      "ds_read_b128 a[252:255], v251 offset:61440\n"
      "s_waitcnt lgkmcnt(11)\n"
      "v_mfma_f32_16x16x32_bf16 v[200:203], v[104:107], a[224:227], v[200:203]\n"
      "v_mfma_f32_16x16x32_bf16 v[204:207], v[108:111], a[224:227], v[204:207]\n"
      "global_load_lds_dwordx4 v254, s[70:71] offset:2048\n"
      "s_waitcnt lgkmcnt(10)\n"
      "v_mfma_f32_16x16x32_bf16 v[208:211], v[104:107], a[228:231], v[208:211]\n"
      "v_mfma_f32_16x16x32_bf16 v[212:215], v[108:111], a[228:231], v[212:215]\n"
      "s_waitcnt lgkmcnt(6)\n"
      "v_mfma_f32_16x16x32_bf16 v[192:195], v[112:115], a[232:235], v[192:195]\n"
      "s_waitcnt vmcnt(11)\n"
      "ds_read_b128 v[136:139], v252 offset:0\n"
      "s_waitcnt lgkmcnt(6)\n"
      "v_mfma_f32_16x16x32_bf16 v[196:199], v[116:119], a[232:235], v[196:199]\n"
      "ds_read_b128 v[140:143], v252 offset:64\n"
      "global_load_lds_dwordx4 v255, s[70:71] offset:3072\n"
      "s_waitcnt lgkmcnt(4)\n"
      "v_mfma_f32_16x16x32_bf16 v[192:195], v[120:123], a[244:247], v[192:195]\n"
      "ds_read_b128 v[144:147], v252 offset:256\n"
      "v_mfma_f32_16x16x32_bf16 v[196:199], v[124:127], a[244:247], v[196:199]\n"
      "v_mfma_f32_16x16x32_bf16 v[200:203], v[112:115], a[236:239], v[200:203]\n"
      "ds_read_b128 v[148:151], v252 offset:320\n"
      "v_mfma_f32_16x16x32_bf16 v[204:207], v[116:119], a[236:239], v[204:207]\n"
      "s_waitcnt lgkmcnt(5)\n"
      "v_mfma_f32_16x16x32_bf16 v[200:203], v[120:123], a[248:251], v[200:203]\n"
      "ds_read_b128 v[152:155], v252 offset:512\n"
      "v_cvt_pk_bf16_f32 v224, v192, v193\n"
      "v_cvt_pk_bf16_f32 v225, v194, v195\n"
      "v_mfma_f32_16x16x32_bf16 v[204:207], v[124:127], a[248:251], v[204:207]\n"
      "v_cvt_pk_bf16_f32 v226, v196, v197\n"
      "v_cvt_pk_bf16_f32 v227, v198, v199\n"
      "v_xor_b32 v224, s85, v224\n"
      "v_mfma_f32_16x16x32_bf16 v[208:211], v[112:115], a[240:243], v[208:211]\n"
      "ds_read_b128 v[156:159], v252 offset:576\n"
      "v_xor_b32 v225, s85, v225\n"
      "v_xor_b32 v226, s85, v226\n"
      "v_mfma_f32_16x16x32_bf16 v[212:215], v[116:119], a[240:243], v[212:215]\n"
      "v_xor_b32 v227, s85, v227\n"
      "s_waitcnt lgkmcnt(6)\n"
      "v_mfma_f32_16x16x32_bf16 v[208:211], v[120:123], a[252:255], v[208:211]\n"
      "ds_read_b128 v[160:163], v252 offset:768\n"
      "global_store_dwordx4 v242, v[224:227], s[62:63] offset:-4096 sc1\n"
      "v_cvt_pk_bf16_f32 v228, v200, v201\n"
      "v_mfma_f32_16x16x32_bf16 v[212:215], v[124:127], a[252:255], v[212:215]\n"
      "v_cvt_pk_bf16_f32 v229, v202, v203\n"
      "v_cvt_pk_bf16_f32 v230, v204, v205\n"
      "v_cvt_pk_bf16_f32 v231, v206, v207\n"
      "s_waitcnt lgkmcnt(6)\n"
      "v_mfma_f32_16x16x32_bf16 v[128:131], v[0:3], v[136:139], 0\n"
      "ds_read_b128 v[164:167], v252 offset:832\n"
      "v_xor_b32 v228, s85, v228\n"
      "v_xor_b32 v229, s85, v229\n"
      "v_mfma_f32_16x16x32_bf16 v[132:135], v[4:7], v[136:139], 0\n"
      "v_xor_b32 v230, s85, v230\n"
      "v_xor_b32 v231, s85, v231\n"
      "s_waitcnt lgkmcnt(6)\n"
      "v_mfma_f32_16x16x32_bf16 v[128:131], v[8:11], v[140:143], v[128:131]\n"
      "ds_read_b128 v[136:139], v252 offset:128\n"
      "global_store_dwordx4 v242, v[228:231], s[62:63] offset:-3072 sc1\n"
      "v_cvt_pk_bf16_f32 v232, v208, v209\n"
      "v_mfma_f32_16x16x32_bf16 v[132:135], v[12:15], v[140:143], v[132:135]\n"
      "v_cvt_pk_bf16_f32 v233, v210, v211\n"
      "v_cvt_pk_bf16_f32 v234, v212, v213\n"
      "v_cvt_pk_bf16_f32 v235, v214, v215\n"
      "s_waitcnt lgkmcnt(6)\n"
      "v_mfma_f32_16x16x32_bf16 v[128:131], v[16:19], v[144:147], v[128:131]\n"
      "ds_read_b128 v[140:143], v252 offset:192\n"
      "v_xor_b32 v232, s85, v232\n"
      "v_xor_b32 v233, s85, v233\n"
      "v_mfma_f32_16x16x32_bf16 v[132:135], v[20:23], v[144:147], v[132:135]\n"
      "v_xor_b32 v234, s85, v234\n"
      "v_xor_b32 v235, s85, v235\n"
      "s_waitcnt lgkmcnt(6)\n"
      "v_mfma_f32_16x16x32_bf16 v[128:131], v[24:27], v[148:151], v[128:131]\n"
      "ds_read_b128 v[144:147], v252 offset:384\n"
      "global_store_dwordx4 v242, v[232:235], s[62:63] offset:-2048 sc1\n"
      "v_mfma_f32_16x16x32_bf16 v[132:135], v[28:31], v[148:151], v[132:135]\n"
      "s_xor_b32 s70, s76, 0\n"
      "s_add_u32 s70, s8, s70\n"
      "s_addc_u32 s71, s9, 0\n"
      "s_add_u32 m0, s41, 4096\n"
      "s_waitcnt lgkmcnt(6)\n"
      "v_mfma_f32_16x16x32_bf16 v[128:131], v[32:35], v[152:155], v[128:131]\n"
      "s_add_u32 s48, s82, 32768\n"
      "ds_read_b128 v[148:151], v252 offset:448\n"
      "global_load_lds_dwordx4 v244, s[70:71] offset:0\n"
      "v_subrev_u32 v236, s41, v248\n"
      "v_mfma_f32_16x16x32_bf16 v[132:135], v[36:39], v[152:155], v[132:135]\n"
      "s_add_u32 s48, s48, 65536\n"
      "global_load_lds_dwordx4 v245, s[70:71] offset:1024\n"
      "v_add_u32 v236, s48, v236\n"
      "v_xor_b32 v237, 64, v236\n"
      "s_waitcnt lgkmcnt(6)\n"
      "v_mfma_f32_16x16x32_bf16 v[128:131], v[40:43], v[156:159], v[128:131]\n"
      "ds_read_b128 v[152:155], v252 offset:640\n"
      "global_load_lds_dwordx4 v246, s[70:71] offset:2048\n"
      "v_mfma_f32_16x16x32_bf16 v[132:135], v[44:47], v[156:159], v[132:135]\n"
      "global_load_lds_dwordx4 v247, s[70:71] offset:3072\n"
      "s_xor_b32 s70, s77, 0\n"
      "s_add_u32 s70, s8, s70\n"
      "s_addc_u32 s71, s9, 0\n"
      "s_add_u32 m0, s41, 8192\n"
      "s_waitcnt lgkmcnt(6)\n"
      "v_mfma_f32_16x16x32_bf16 v[128:131], v[48:51], v[160:163], v[128:131]\n"
      "ds_read_b128 v[156:159], v252 offset:704\n"
      "global_load_lds_dwordx4 v244, s[70:71] offset:0\n"
      "v_mfma_f32_16x16x32_bf16 v[132:135], v[52:55], v[160:163], v[132:135]\n"
      "global_load_lds_dwordx4 v245, s[70:71] offset:1024\n"
      "s_waitcnt lgkmcnt(6)\n"
      "v_mfma_f32_16x16x32_bf16 v[128:131], v[56:59], v[164:167], v[128:131]\n"
      "ds_read_b128 v[160:163], v252 offset:896\n"
      "global_load_lds_dwordx4 v246, s[70:71] offset:2048\n"
      "v_mfma_f32_16x16x32_bf16 v[132:135], v[60:63], v[164:167], v[132:135]\n"
      "global_load_lds_dwordx4 v247, s[70:71] offset:3072\n"
      "s_waitcnt lgkmcnt(6)\n"
      "v_mfma_f32_16x16x32_bf16 v[128:131], v[64:67], v[136:139], v[128:131]\n"
      "ds_read_b128 v[164:167], v252 offset:960\n"
      "v_mfma_f32_16x16x32_bf16 v[132:135], v[68:71], v[136:139], v[132:135]\n"
      "s_waitcnt lgkmcnt(6)\n"
      "v_mfma_f32_16x16x32_bf16 v[128:131], v[72:75], v[140:143], v[128:131]\n"
      "s_waitcnt vmcnt(11)\n"
      "s_barrier\n"
      "v_mfma_f32_16x16x32_bf16 v[132:135], v[76:79], v[140:143], v[132:135]\n"
      "s_add_u32 s70, s34, 4\n"
      "s_and_b32 s70, s70, 3\n"
      "s_lshl_b32 s70, s70, 15\n"
      "s_or_b32 s70, s70, s81\n"
      "s_add_u32 s70, s12, s70\n"
      "s_addc_u32 s71, s13, 0\n"
      "s_add_u32 m0, s82, 65536\n"
      "s_waitcnt lgkmcnt(5)\n"
      "v_mfma_f32_16x16x32_bf16 v[128:131], v[80:83], v[144:147], v[128:131]\n"
      "global_load_lds_dwordx4 v240, s[70:71] offset:0\n"
      "ds_read_b128 a[192:195], v250 offset:34816\n"
      "v_mfma_f32_16x16x32_bf16 v[132:135], v[84:87], v[144:147], v[132:135]\n"
      "ds_read_b128 a[196:199], v251 offset:34816\n"
      "ds_read_b128 a[200:203], v250 offset:38912\n"
      "s_waitcnt lgkmcnt(7)\n"
      "v_mfma_f32_16x16x32_bf16 v[128:131], v[88:91], v[148:151], v[128:131]\n"
      "ds_read_b128 a[204:207], v251 offset:38912\n"
      "ds_read_b128 a[208:211], v250 offset:43008\n"
      "v_mfma_f32_16x16x32_bf16 v[132:135], v[92:95], v[148:151], v[132:135]\n"
      "ds_read_b128 a[212:215], v251 offset:43008\n"
      "ds_read_b128 a[216:219], v250 offset:47104\n"
      "s_waitcnt lgkmcnt(10)\n"
      "v_mfma_f32_16x16x32_bf16 v[128:131], v[96:99], v[152:155], v[128:131]\n"
      "global_load_lds_dwordx4 v241, s[70:71] offset:1024\n"
      "ds_read_b128 a[220:223], v251 offset:47104\n"
      "v_mfma_f32_16x16x32_bf16 v[132:135], v[100:103], v[152:155], v[132:135]\n"
      "ds_read_b128 a[224:227], v250 offset:51200\n"
      "ds_read_b128 a[228:231], v251 offset:51200\n"
      "s_waitcnt lgkmcnt(12)\n"
      "v_mfma_f32_16x16x32_bf16 v[128:131], v[104:107], v[156:159], v[128:131]\n"
      "ds_read_b128 a[232:235], v250 offset:55296\n"
      "ds_read_b128 a[236:239], v251 offset:55296\n"
      "v_mfma_f32_16x16x32_bf16 v[132:135], v[108:111], v[156:159], v[132:135]\n"
      "ds_read_b128 a[240:243], v250 offset:59392\n"
      "s_waitcnt lgkmcnt(14)\n"
      "v_mfma_f32_16x16x32_bf16 v[128:131], v[112:115], v[160:163], v[128:131]\n"
      "global_load_lds_dwordx4 v254, s[70:71] offset:2048\n"
      "v_mfma_f32_16x16x32_bf16 v[132:135], v[116:119], v[160:163], v[132:135]\n"
      "ds_read_b128 a[244:247], v251 offset:59392\n"
      "s_waitcnt lgkmcnt(14)\n"
      "v_mfma_f32_16x16x32_bf16 v[128:131], v[120:123], v[164:167], v[128:131]\n"
      "v_mfma_f32_16x16x32_bf16 v[132:135], v[124:127], v[164:167], v[132:135]\n"
      "ds_read_b128 a[248:251], v250 offset:63488\n"
      "s_waitcnt lgkmcnt(14)\n"
      "v_mfma_f32_16x16x32_bf16 v[216:219], v[0:3], a[192:195], 0\n"
      "global_load_lds_dwordx4 v255, s[70:71] offset:3072\n"
      "v_mfma_f32_16x16x32_bf16 v[220:223], v[4:7], a[192:195], 0\n"
      "ds_read_b128 a[252:255], v251 offset:63488\n"
      "v_xor_b32 v250, 196608, v250\n"
      "v_xor_b32 v251, 196608, v251\n"
      "s_waitcnt lgkmcnt(14)\n"
      "v_mfma_f32_16x16x32_bf16 v[216:219], v[8:11], a[196:199], v[216:219]\n"
      "s_mov_b32 s22, 0xf0f0f0f0\n"
      "s_mov_b32 s23, 0xf0f0f0f0\n"
      "v_cndmask_b32 v128, v128, v129, s[64:65]\n"
      "v_cndmask_b32 v130, v130, v131, s[64:65]\n"
      "v_mfma_f32_16x16x32_bf16 v[220:223], v[12:15], a[196:199], v[220:223]\n"
      "ds_read_b128 a[192:195], v250 offset:0\n"
      "v_cndmask_b32 v132, v132, v133, s[64:65]\n"
      "v_cndmask_b32 v134, v134, v135, s[64:65]\n"
      "s_waitcnt lgkmcnt(14)\n"
      "v_mfma_f32_16x16x32_bf16 v[216:219], v[16:19], a[200:203], v[216:219]\n"
      "s_add_u32 s70, s34, 4\n"
      "s_and_b32 s70, s70, 3\n"
      "s_lshl_b32 s70, s70, 15\n"
      "s_or_b32 s70, s70, s81\n"
      "s_xor_b32 s70, s70, 128\n"
      "s_add_u32 s70, s12, s70\n"
      "s_addc_u32 s71, s13, 0\n"
      "s_add_u32 m0, s82, 81920\n"
      "v_mfma_f32_16x16x32_bf16 v[220:223], v[20:23], a[200:203], v[220:223]\n"
      "global_load_lds_dwordx4 v240, s[70:71] offset:0\n"
      "ds_read_b128 a[196:199], v250 offset:2048\n"
      "v_cndmask_b32 v128, v128, v130, s[66:67]\n"
      "s_waitcnt lgkmcnt(14)\n"
      "v_mfma_f32_16x16x32_bf16 v[216:219], v[24:27], a[204:207], v[216:219]\n"
      "v_cndmask_b32 v132, v132, v134, s[66:67]\n"
      "v_lshrrev_b32 v133, 4, v243\n"
      "v_cndmask_b32 v128, v128, v132, s[22:23]\n"
      "v_mfma_f32_16x16x32_bf16 v[220:223], v[28:31], a[204:207], v[220:223]\n"
      "ds_read_b128 a[200:203], v251 offset:0\n"
      "v_and_b32 v134, 7, v243\n"
      "v_cvt_pk_bf16_f32 v128, v128, v128\n"
      "s_waitcnt lgkmcnt(14)\n"
      "v_mfma_f32_16x16x32_bf16 v[216:219], v[32:35], a[208:211], v[216:219]\n"
      "s_lshl_b32 s48, s40, 5\n"
      "v_and_b32 v128, 0xffff0000, v128\n"
      "v_lshl_or_b32 v133, v133, 3, v134\n"
      "v_add_u32 v133, s48, v133\n"
      "v_mfma_f32_16x16x32_bf16 v[220:223], v[36:39], a[208:211], v[220:223]\n"
      "global_load_lds_dwordx4 v241, s[70:71] offset:1024\n"
      "ds_read_b128 a[204:207], v251 offset:2048\n"
      "v_ashrrev_i32 v129, 31, v128\n"
      "s_waitcnt lgkmcnt(14)\n"
      "v_mfma_f32_16x16x32_bf16 v[216:219], v[40:43], a[212:215], v[216:219]\n"
      "v_mul_f32 v130, 0xbfb8aa3b, v128\n"
      "v_or_b32 v129, 0x80000000, v129\n"
      "v_exp_f32 v130, v130\n"
      "v_mfma_f32_16x16x32_bf16 v[220:223], v[44:47], a[212:215], v[220:223]\n"
      "ds_read_b128 a[208:211], v250 offset:4096\n"
      "v_xor_b32 v129, v129, v128\n"
      "v_sub_u32 v132, 0xfff, v133\n"
      "s_waitcnt lgkmcnt(14)\n"
      "v_mfma_f32_16x16x32_bf16 v[216:219], v[48:51], a[216:219], v[216:219]\n"
      "v_add_f32 v130, 1.0, v130\n"
      "s_mov_b32 s49, 0xffff0000\n"
      "v_and_or_b32 v129, v129, s49, v132\n"
      "v_mfma_f32_16x16x32_bf16 v[220:223], v[52:55], a[216:219], v[220:223]\n"
      "global_load_lds_dwordx4 v254, s[70:71] offset:2048\n"
      "ds_read_b128 a[212:215], v250 offset:6144\n"
      "v_rcp_f32 v130, v130\n"
      "s_waitcnt lgkmcnt(14)\n"
      "v_mfma_f32_16x16x32_bf16 v[216:219], v[56:59], a[220:223], v[216:219]\n"
      "s_mov_b32 s64, 0x00ff00ff\n"
      "s_mov_b32 s65, 0x00ff00ff\n"
      "v_cndmask_b32 v129, v129, 0, s[64:65]\n"
      "v_cvt_pk_bf16_f32 v130, v130, v130\n"
      "v_mfma_f32_16x16x32_bf16 v[220:223], v[60:63], a[220:223], v[220:223]\n"
      "s_lshr_b32 s48, s34, 1\n"
      "ds_read_b128 a[216:219], v251 offset:4096\n"
      "s_lshl2_add_u32 s48, s48, s35\n"
      "v_max_u32_dpp v129, v129, v129 quad_perm:[1,0,3,2] row_mask:0xf bank_mask:0xf\n"
      "s_waitcnt lgkmcnt(14)\n"
      "v_mfma_f32_16x16x32_bf16 v[216:219], v[64:67], a[224:227], v[216:219]\n"
      "v_add_u32 v238, s42, v242\n"
      "v_mov_b32 v224, s37\n"
      "v_mov_b32 v225, s37\n"
      "v_mov_b32 v226, s37\n"
      "v_mov_b32 v227, s37\n"
      "s_waitcnt vmcnt(15)\n"
      "s_mov_b64 exec, 0xff\n"
      "global_store_dwordx4 v238, v[224:227], s[58:59] offset:0 sc1\n"
      "s_mov_b64 exec, -1\n"
      "v_mfma_f32_16x16x32_bf16 v[220:223], v[68:71], a[224:227], v[220:223]\n"
      "global_load_lds_dwordx4 v255, s[70:71] offset:3072\n"
      "ds_read_b128 a[220:223], v251 offset:6144\n"
      "v_and_b32 v130, 0xffff0000, v130\n"
      "s_waitcnt lgkmcnt(14)\n"
      "v_mfma_f32_16x16x32_bf16 v[216:219], v[72:75], a[228:231], v[216:219]\n"
      "s_lshl_b32 s49, s48, 15\n"
      "v_max_u32_dpp v129, v129, v129 quad_perm:[2,3,0,1] row_mask:0xf bank_mask:0xf\n"
      "s_add_u32 s49, s49, 786432\n"
      "v_lshl_add_u32 v133, v133, 4, s49\n"
      "v_mfma_f32_16x16x32_bf16 v[220:223], v[76:79], a[228:231], v[220:223]\n"
      "ds_read_b128 a[224:227], v250 offset:8192\n"
      "v_max_u32_dpp v129, v129, v129 row_half_mirror row_mask:0xf bank_mask:0xf\n"
      "s_lshl_b32 s48, s48, 6\n"
      "s_waitcnt lgkmcnt(14)\n"
      "v_mfma_f32_16x16x32_bf16 v[216:219], v[80:83], a[232:235], v[216:219]\n"
      "s_add_u32 s48, s48, s40\n"
      "v_max_u32_dpp v129, v129, v129 row_mirror row_mask:0xf bank_mask:0xf\n"
      "s_lshl_b32 s48, s48, 6\n"
      "s_add_u32 s48, s48, 4096\n"
      "v_mfma_f32_16x16x32_bf16 v[220:223], v[84:87], a[232:235], v[220:223]\n"
      "s_xor_b32 s70, s78, 0\n"
      "s_add_u32 s70, s8, s70\n"
      "s_addc_u32 s71, s9, 0\n"
      "s_add_u32 m0, s41, 12288\n"
      "s_waitcnt lgkmcnt(13)\n"
      "v_mfma_f32_16x16x32_bf16 v[216:219], v[88:91], a[236:239], v[216:219]\n"
      "global_load_lds_dwordx4 v244, s[70:71] offset:0\n"
      "ds_read_b128 a[228:231], v250 offset:10240\n"
      "v_mov_b32 v131, v129\n"
      "v_mfma_f32_16x16x32_bf16 v[220:223], v[92:95], a[236:239], v[220:223]\n"
      "ds_read_b128 a[232:235], v251 offset:8192\n"
      "v_lshl_add_u32 v132, v134, 4, s48\n"
      "v_mov_b32 v137, s37\n"
      "s_waitcnt lgkmcnt(14)\n"
      "v_mfma_f32_16x16x32_bf16 v[216:219], v[96:99], a[240:243], v[216:219]\n"
      "v_permlane16_swap_b32 v129, v131\n"
      "v_mov_b32 v138, s37\n"
      "v_mov_b32 v139, s37\n"
      "v_mfma_f32_16x16x32_bf16 v[220:223], v[100:103], a[240:243], v[220:223]\n"
      "ds_read_b128 a[236:239], v251 offset:10240\n"
      "v_max_u32 v129, v129, v131\n"
      "v_cndmask_b32 v133, v132, v133, s[64:65]\n"
      "s_waitcnt lgkmcnt(14)\n"
      "v_mfma_f32_16x16x32_bf16 v[216:219], v[104:107], a[244:247], v[216:219]\n"
      "global_load_lds_dwordx4 v245, s[70:71] offset:1024\n"
      "v_mov_b32 v131, v129\n"
      "s_nop 1\n"
      "v_permlane32_swap_b32 v129, v131\n"
      "v_mfma_f32_16x16x32_bf16 v[220:223], v[108:111], a[244:247], v[220:223]\n"
      "ds_read_b128 a[240:243], v250 offset:12288\n"
      "s_waitcnt vmcnt(15)\n"
      "ds_read_b128 a[0:3], v248 offset:4096\n"
      "s_waitcnt lgkmcnt(15)\n"
      "v_mfma_f32_16x16x32_bf16 v[216:219], v[112:115], a[248:251], v[216:219]\n"
      "ds_read_b128 a[4:7], v248 offset:6144\n"
      "ds_read_b128 a[32:35], v249 offset:4096\n"
      "v_max_u32 v129, v129, v131\n"
      "v_mfma_f32_16x16x32_bf16 v[220:223], v[116:119], a[248:251], v[220:223]\n"
      "ds_read_b128 a[244:247], v250 offset:14336\n"
      "ds_read_b128 a[36:39], v249 offset:6144\n"
      "v_cndmask_b32 v136, v129, v130, s[64:65]\n"
      "s_waitcnt lgkmcnt(15)\n"
      "v_mfma_f32_16x16x32_bf16 v[216:219], v[120:123], a[252:255], v[216:219]\n"
      "s_mov_b32 s64, 0x00ff0fff\n"
      "s_mov_b64 exec, s[64:65]\n"
      "global_store_dwordx4 v133, v[136:139], s[58:59] sc1\n"
      "s_mov_b64 exec, -1\n"
      "v_mfma_f32_16x16x32_bf16 v[220:223], v[124:127], a[252:255], v[220:223]\n"
      "global_load_lds_dwordx4 v246, s[70:71] offset:2048\n"
      "ds_read_b128 a[248:251], v251 offset:12288\n"
      "v_mfma_f32_16x16x32_bf16 v[192:195], v[0:3], a[192:195], 0\n"
      "s_waitcnt vmcnt(13)\n"
      "ds_read_b128 a[8:11], v248 offset:8192\n"
      "v_mfma_f32_16x16x32_bf16 v[196:199], v[4:7], a[192:195], 0\n"
      "ds_read_b128 a[252:255], v251 offset:14336\n"
      "ds_read_b128 a[12:15], v248 offset:10240\n"
      "v_mfma_f32_16x16x32_bf16 v[200:203], v[0:3], a[196:199], 0\n"
      "ds_read_b128 a[40:43], v249 offset:8192\n"
      "ds_read_b128 a[44:47], v249 offset:10240\n"
      "v_cvt_pk_bf16_f32 v224, v216, v217\n"
      "v_mfma_f32_16x16x32_bf16 v[204:207], v[4:7], a[196:199], 0\n"
      "global_load_lds_dwordx4 v247, s[70:71] offset:3072\n"
      "ds_read_b128 a[192:195], v250 offset:16384\n"
      "v_cvt_pk_bf16_f32 v225, v218, v219\n"
      "v_mfma_f32_16x16x32_bf16 v[192:195], v[8:11], a[200:203], v[192:195]\n"
      "v_cvt_pk_bf16_f32 v226, v220, v221\n"
      "v_cvt_pk_bf16_f32 v227, v222, v223\n"
      "v_xor_b32 v224, s85, v224\n"
      "v_mfma_f32_16x16x32_bf16 v[196:199], v[12:15], a[200:203], v[196:199]\n"
      "ds_read_b128 a[196:199], v250 offset:18432\n"
      "v_xor_b32 v225, s85, v225\n"
      "v_xor_b32 v226, s85, v226\n"
      "s_waitcnt lgkmcnt(15)\n"
      "v_mfma_f32_16x16x32_bf16 v[200:203], v[8:11], a[204:207], v[200:203]\n"
      "s_xor_b32 s70, s79, 0\n"
      "s_add_u32 s70, s8, s70\n"
      "s_addc_u32 s71, s9, 0\n"
      "s_add_u32 m0, s41, 0\n"
      "v_mfma_f32_16x16x32_bf16 v[204:207], v[12:15], a[204:207], v[204:207]\n"
      "global_load_lds_dwordx4 v244, s[70:71] offset:0\n"
      "ds_read_b128 a[200:203], v251 offset:16384\n"
      "v_xor_b32 v227, s85, v227\n"
      "v_mfma_f32_16x16x32_bf16 v[192:195], v[16:19], a[208:211], v[192:195]\n"
      "s_lshl2_add_u32 s54, s34, s35\n"
      "s_lshr_b32 s56, s39, 1\n"
      "s_lshl1_add_u32 s56, s33, s56\n"
      "s_lshl_b32 s56, s56, 15\n"
      "v_mfma_f32_16x16x32_bf16 v[196:199], v[20:23], a[208:211], v[196:199]\n"
      "s_mul_i32 s55, s54, 266240\n"
      "ds_read_b128 a[204:207], v251 offset:18432\n"
      "s_add_u32 s55, s55, 4096\n"
      "s_add_u32 s14, s18, s55\n"
      "s_addc_u32 s15, s19, 0\n"
      "v_mfma_f32_16x16x32_bf16 v[200:203], v[16:19], a[212:215], v[200:203]\n"
      "s_add_u32 s10, s14, s56\n"
      "s_addc_u32 s11, s15, 0\n"
      "s_xor_b32 s56, s56, 0x8000\n"
      "s_add_u32 s14, s14, s56\n"
      "s_addc_u32 s15, s15, 0\n"
      "v_mfma_f32_16x16x32_bf16 v[204:207], v[20:23], a[212:215], v[204:207]\n"
      "s_and_b32 s55, s39, 1\n"
      "global_load_lds_dwordx4 v245, s[70:71] offset:1024\n"
      "ds_read_b128 a[208:211], v250 offset:20480\n"
      "s_lshl_b32 s56, s55, 14\n"
      "v_mfma_f32_16x16x32_bf16 v[192:195], v[24:27], a[216:219], v[192:195]\n"
      "v_add_u32 v240, s56, v242\n"
      "v_xor_b32 v241, 0x4000, v240\n"
      "s_add_u32 s72, s10, 0x2000\n"
      "s_addc_u32 s73, s11, 0\n"
      "v_mfma_f32_16x16x32_bf16 v[196:199], v[28:31], a[216:219], v[196:199]\n"
      "s_add_u32 s74, s14, 0x2000\n"
      "s_addc_u32 s75, s15, 0\n"
      "ds_read_b128 a[212:215], v250 offset:22528\n"
      "v_mfma_f32_16x16x32_bf16 v[200:203], v[24:27], a[220:223], v[200:203]\n"
      "s_lshl2_add_u32 s55, s54, s33\n"
      "s_lshl_b32 s55, s55, 12\n"
      "s_add_u32 s55, s55, 1048576\n"
      "v_and_b32 v253, 7, v243\n"
      "v_mfma_f32_16x16x32_bf16 v[204:207], v[28:31], a[220:223], v[204:207]\n"
      "global_load_lds_dwordx4 v246, s[70:71] offset:2048\n"
      "ds_read_b128 a[216:219], v251 offset:20480\n"
      "v_lshl_add_u32 v253, v253, 9, s55\n"
      "v_mfma_f32_16x16x32_bf16 v[192:195], v[32:35], a[224:227], v[192:195]\n"
      "v_mfma_f32_16x16x32_bf16 v[196:199], v[36:39], a[224:227], v[196:199]\n"
      "ds_read_b128 a[220:223], v251 offset:22528\n"
      "s_waitcnt vmcnt(8)\n"
      "s_barrier\n"
      "v_mfma_f32_16x16x32_bf16 v[200:203], v[32:35], a[228:231], v[200:203]\n"
      "v_mfma_f32_16x16x32_bf16 v[204:207], v[36:39], a[228:231], v[204:207]\n"
      "global_load_lds_dwordx4 v247, s[70:71] offset:3072\n"
      "ds_read_b128 a[224:227], v250 offset:24576\n"
      "v_mfma_f32_16x16x32_bf16 v[192:195], v[40:43], a[232:235], v[192:195]\n"
      "v_mfma_f32_16x16x32_bf16 v[196:199], v[44:47], a[232:235], v[196:199]\n"
      "ds_read_b128 a[228:231], v250 offset:26624\n"
      "s_waitcnt lgkmcnt(15)\n"
      "v_mfma_f32_16x16x32_bf16 v[200:203], v[40:43], a[236:239], v[200:203]\n"
      "s_xor_b32 s70, s76, 128\n"
      "s_add_u32 s70, s8, s70\n"
      "s_addc_u32 s71, s9, 0\n"
      "s_add_u32 m0, s82, 98304\n"
      "v_mfma_f32_16x16x32_bf16 v[204:207], v[44:47], a[236:239], v[204:207]\n"
      "global_load_lds_dwordx4 v244, s[70:71] offset:0\n"
      "ds_read_b128 a[232:235], v251 offset:24576\n"
      "v_mfma_f32_16x16x32_bf16 v[192:195], v[48:51], a[240:243], v[192:195]\n"
      "v_mfma_f32_16x16x32_bf16 v[196:199], v[52:55], a[240:243], v[196:199]\n"
      "ds_read_b128 a[236:239], v251 offset:26624\n"
      "v_mfma_f32_16x16x32_bf16 v[200:203], v[48:51], a[244:247], v[200:203]\n"
      "v_mfma_f32_16x16x32_bf16 v[204:207], v[52:55], a[244:247], v[204:207]\n"
      "global_load_lds_dwordx4 v245, s[70:71] offset:1024\n"
      "ds_read_b128 a[240:243], v250 offset:28672\n"
      "v_mfma_f32_16x16x32_bf16 v[192:195], v[56:59], a[248:251], v[192:195]\n"
      "v_mfma_f32_16x16x32_bf16 v[196:199], v[60:63], a[248:251], v[196:199]\n"
      "ds_read_b128 a[244:247], v250 offset:30720\n"
      "s_waitcnt lgkmcnt(15)\n"
      "v_mfma_f32_16x16x32_bf16 v[200:203], v[56:59], a[252:255], v[200:203]\n"
      "v_mfma_f32_16x16x32_bf16 v[204:207], v[60:63], a[252:255], v[204:207]\n"
      "global_load_lds_dwordx4 v246, s[70:71] offset:2048\n"
      "ds_read_b128 a[248:251], v251 offset:28672\n"
      "s_waitcnt lgkmcnt(14)\n"
      "v_mfma_f32_16x16x32_bf16 v[192:195], v[64:67], a[192:195], v[192:195]\n"
      "v_mfma_f32_16x16x32_bf16 v[196:199], v[68:71], a[192:195], v[196:199]\n"
      "ds_read_b128 a[252:255], v251 offset:30720\n"
      "v_xor_b32 v250, 196608, v250\n"
      "v_xor_b32 v251, 196608, v251\n"
      "s_waitcnt lgkmcnt(14)\n"
      "v_mfma_f32_16x16x32_bf16 v[200:203], v[64:67], a[196:199], v[200:203]\n"
      "v_mfma_f32_16x16x32_bf16 v[204:207], v[68:71], a[196:199], v[204:207]\n"
      "global_load_lds_dwordx4 v247, s[70:71] offset:3072\n"
      "ds_read_b128 a[192:195], v250 offset:0\n"
      "s_waitcnt lgkmcnt(14)\n"
      "v_mfma_f32_16x16x32_bf16 v[192:195], v[72:75], a[200:203], v[192:195]\n"
      "v_mfma_f32_16x16x32_bf16 v[196:199], v[76:79], a[200:203], v[196:199]\n"
      "ds_read_b128 a[196:199], v250 offset:2048\n"
      "s_waitcnt lgkmcnt(14)\n"
      "v_mfma_f32_16x16x32_bf16 v[200:203], v[72:75], a[204:207], v[200:203]\n"
      "s_xor_b32 s70, s77, 128\n"
      "s_add_u32 s70, s8, s70\n"
      "s_addc_u32 s71, s9, 0\n"
      "s_add_u32 m0, s82, 114688\n"
      "v_mfma_f32_16x16x32_bf16 v[204:207], v[76:79], a[204:207], v[204:207]\n"
      "global_load_lds_dwordx4 v244, s[70:71] offset:0\n"
      "ds_read_b128 a[200:203], v251 offset:0\n"
      "s_waitcnt lgkmcnt(14)\n"
      "v_mfma_f32_16x16x32_bf16 v[192:195], v[80:83], a[208:211], v[192:195]\n"
      "v_mfma_f32_16x16x32_bf16 v[196:199], v[84:87], a[208:211], v[196:199]\n"
      "ds_read_b128 a[204:207], v251 offset:2048\n"
      "s_waitcnt lgkmcnt(14)\n"
      "v_mfma_f32_16x16x32_bf16 v[200:203], v[80:83], a[212:215], v[200:203]\n"
      "v_mfma_f32_16x16x32_bf16 v[204:207], v[84:87], a[212:215], v[204:207]\n"
      "global_load_lds_dwordx4 v245, s[70:71] offset:1024\n"
      "ds_read_b128 a[208:211], v250 offset:4096\n"
      "s_waitcnt lgkmcnt(14)\n"
      "v_mfma_f32_16x16x32_bf16 v[192:195], v[88:91], a[216:219], v[192:195]\n"
      "v_mfma_f32_16x16x32_bf16 v[196:199], v[92:95], a[216:219], v[196:199]\n"
      "ds_read_b128 a[212:215], v250 offset:6144\n"
      "s_waitcnt lgkmcnt(14)\n"
      "v_mfma_f32_16x16x32_bf16 v[200:203], v[88:91], a[220:223], v[200:203]\n"
      "v_mfma_f32_16x16x32_bf16 v[204:207], v[92:95], a[220:223], v[204:207]\n"
      "global_load_lds_dwordx4 v246, s[70:71] offset:2048\n"
      "ds_read_b128 a[216:219], v251 offset:4096\n"
      "s_waitcnt lgkmcnt(14)\n"
      "v_mfma_f32_16x16x32_bf16 v[192:195], v[96:99], a[224:227], v[192:195]\n"
      "s_waitcnt vmcnt(11)\n"
      "ds_read_b128 a[16:19], v248 offset:12288\n"
      "v_mfma_f32_16x16x32_bf16 v[196:199], v[100:103], a[224:227], v[196:199]\n"
      "ds_read_b128 a[220:223], v251 offset:6144\n"
      "ds_read_b128 a[20:23], v248 offset:14336\n"
      "s_waitcnt lgkmcnt(15)\n"
      "v_mfma_f32_16x16x32_bf16 v[200:203], v[96:99], a[228:231], v[200:203]\n"
      "ds_read_b128 a[48:51], v249 offset:12288\n"
      "ds_read_b128 a[52:55], v249 offset:14336\n"
      "v_mfma_f32_16x16x32_bf16 v[204:207], v[100:103], a[228:231], v[204:207]\n"
      "global_load_lds_dwordx4 v247, s[70:71] offset:3072\n"
      "ds_read_b128 a[224:227], v250 offset:8192\n"
      "v_mfma_f32_16x16x32_bf16 v[192:195], v[104:107], a[232:235], v[192:195]\n"
      "v_mfma_f32_16x16x32_bf16 v[196:199], v[108:111], a[232:235], v[196:199]\n"
      "ds_read_b128 a[228:231], v250 offset:10240\n"
      "s_waitcnt lgkmcnt(15)\n"
      "v_mfma_f32_16x16x32_bf16 v[200:203], v[104:107], a[236:239], v[200:203]\n"
      "s_xor_b32 s70, s78, 128\n"
      "s_add_u32 s70, s8, s70\n"
      "s_addc_u32 s71, s9, 0\n"
      "s_add_u32 m0, s41, 4096\n"
      "v_mfma_f32_16x16x32_bf16 v[204:207], v[108:111], a[236:239], v[204:207]\n"
      "global_load_lds_dwordx4 v244, s[70:71] offset:0\n"
      "ds_read_b128 a[232:235], v251 offset:8192\n"
      "v_mfma_f32_16x16x32_bf16 v[192:195], v[112:115], a[240:243], v[192:195]\n"
      "v_mfma_f32_16x16x32_bf16 v[196:199], v[116:119], a[240:243], v[196:199]\n"
      "ds_read_b128 a[236:239], v251 offset:10240\n"
      "s_waitcnt lgkmcnt(15)\n"
      "s_barrier\n"
      "v_mfma_f32_16x16x32_bf16 v[200:203], v[112:115], a[244:247], v[200:203]\n"
      "v_mfma_f32_16x16x32_bf16 v[204:207], v[116:119], a[244:247], v[204:207]\n"
      "global_load_lds_dwordx4 v245, s[70:71] offset:1024\n"
      "ds_read_b128 a[240:243], v250 offset:12288\n"
      "v_mfma_f32_16x16x32_bf16 v[192:195], v[120:123], a[248:251], v[192:195]\n"
      "v_mfma_f32_16x16x32_bf16 v[196:199], v[124:127], a[248:251], v[196:199]\n"
      "ds_read_b128 a[244:247], v250 offset:14336\n"
      "v_mfma_f32_16x16x32_bf16 v[200:203], v[120:123], a[252:255], v[200:203]\n"
      "v_mfma_f32_16x16x32_bf16 v[204:207], v[124:127], a[252:255], v[204:207]\n"
      "global_load_lds_dwordx4 v246, s[70:71] offset:2048\n"
      "ds_read_b128 a[248:251], v251 offset:12288\n"
      "v_mfma_f32_16x16x32_bf16 v[208:211], v[0:3], a[192:195], 0\n"
      "s_waitcnt vmcnt(11)\n"
      "ds_read_b128 a[24:27], v248 offset:0\n"
      "v_mfma_f32_16x16x32_bf16 v[212:215], v[4:7], a[192:195], 0\n"
      "ds_read_b128 a[252:255], v251 offset:14336\n"
      "ds_read_b128 a[28:31], v248 offset:2048\n"
      "s_waitcnt lgkmcnt(15)\n"
      "v_mfma_f32_16x16x32_bf16 v[216:219], v[0:3], a[196:199], 0\n"
      "ds_read_b128 a[56:59], v249 offset:0\n"
      "ds_read_b128 a[60:63], v249 offset:2048\n"
      "v_cvt_pk_bf16_f32 v228, v192, v193\n"
      "v_mfma_f32_16x16x32_bf16 v[220:223], v[4:7], a[196:199], 0\n"
      "global_load_lds_dwordx4 v247, s[70:71] offset:3072\n"
      "ds_read_b128 a[192:195], v250 offset:16384\n"
      "v_cvt_pk_bf16_f32 v229, v194, v195\n"
      "v_mfma_f32_16x16x32_bf16 v[208:211], v[8:11], a[200:203], v[208:211]\n"
      "v_cvt_pk_bf16_f32 v230, v196, v197\n"
      "v_cvt_pk_bf16_f32 v231, v198, v199\n"
      "v_xor_b32 v228, s85, v228\n"
      "v_mfma_f32_16x16x32_bf16 v[212:215], v[12:15], a[200:203], v[212:215]\n"
      "ds_read_b128 a[196:199], v250 offset:18432\n"
      "v_xor_b32 v229, s85, v229\n"
      "v_xor_b32 v230, s85, v230\n"
      "v_mfma_f32_16x16x32_bf16 v[216:219], v[8:11], a[204:207], v[216:219]\n"
      "global_load_dword v252, v253, s[58:59] offset:0 sc1\n"
      "s_xor_b32 s70, s79, 128\n"
      "s_add_u32 s70, s8, s70\n"
      "s_addc_u32 s71, s9, 0\n"
      "s_add_u32 m0, s82, 131072\n"
      "v_mfma_f32_16x16x32_bf16 v[220:223], v[12:15], a[204:207], v[220:223]\n"
      "global_load_lds_dwordx4 v244, s[70:71] offset:0\n"
      "ds_read_b128 a[200:203], v251 offset:16384\n"
      "v_xor_b32 v231, s85, v231\n"
      "v_mfma_f32_16x16x32_bf16 v[208:211], v[16:19], a[208:211], v[208:211]\n"
      "v_cvt_pk_bf16_f32 v232, v200, v201\n"
      "v_cvt_pk_bf16_f32 v233, v202, v203\n"
      "v_cvt_pk_bf16_f32 v234, v204, v205\n"
      "v_mfma_f32_16x16x32_bf16 v[212:215], v[20:23], a[208:211], v[212:215]\n"
      "ds_read_b128 a[204:207], v251 offset:18432\n"
      "v_cvt_pk_bf16_f32 v235, v206, v207\n"
      "v_xor_b32 v232, s85, v232\n"
      "v_mfma_f32_16x16x32_bf16 v[216:219], v[16:19], a[212:215], v[216:219]\n"
      "v_xor_b32 v233, s85, v233\n"
      "v_xor_b32 v234, s85, v234\n"
      "v_xor_b32 v235, s85, v235\n"
      "v_mfma_f32_16x16x32_bf16 v[220:223], v[20:23], a[212:215], v[220:223]\n"
      "global_load_lds_dwordx4 v245, s[70:71] offset:1024\n"
      "ds_read_b128 a[208:211], v250 offset:20480\n"
      "v_mfma_f32_16x16x32_bf16 v[208:211], v[24:27], a[216:219], v[208:211]\n"
      "v_mfma_f32_16x16x32_bf16 v[212:215], v[28:31], a[216:219], v[212:215]\n"
      "ds_read_b128 a[212:215], v250 offset:22528\n"
      "s_waitcnt lgkmcnt(15)\n"
      "v_mfma_f32_16x16x32_bf16 v[216:219], v[24:27], a[220:223], v[216:219]\n"
      "v_mfma_f32_16x16x32_bf16 v[220:223], v[28:31], a[220:223], v[220:223]\n"
      "global_load_lds_dwordx4 v246, s[70:71] offset:2048\n"
      "ds_read_b128 a[216:219], v251 offset:20480\n"
      "v_mfma_f32_16x16x32_bf16 v[208:211], v[32:35], a[224:227], v[208:211]\n"
      "s_waitcnt vmcnt(12)\n"
      "ds_read_b128 a[64:67], v236 offset:0\n"
      "v_mfma_f32_16x16x32_bf16 v[212:215], v[36:39], a[224:227], v[212:215]\n"
      "ds_read_b128 a[220:223], v251 offset:22528\n"
      "ds_read_b128 a[68:71], v236 offset:2048\n"
      "v_mfma_f32_16x16x32_bf16 v[216:219], v[32:35], a[228:231], v[216:219]\n"
      "ds_read_b128 a[96:99], v237 offset:0\n"
      "ds_read_b128 a[100:103], v237 offset:2048\n"
      "v_mfma_f32_16x16x32_bf16 v[220:223], v[36:39], a[228:231], v[220:223]\n"
      "global_load_lds_dwordx4 v247, s[70:71] offset:3072\n"
      "ds_read_b128 a[224:227], v250 offset:24576\n"
      "v_mfma_f32_16x16x32_bf16 v[208:211], v[40:43], a[232:235], v[208:211]\n"
      "v_mfma_f32_16x16x32_bf16 v[212:215], v[44:47], a[232:235], v[212:215]\n"
      "ds_read_b128 a[228:231], v250 offset:26624\n"
      "s_waitcnt lgkmcnt(15)\n"
      "v_mfma_f32_16x16x32_bf16 v[216:219], v[40:43], a[236:239], v[216:219]\n"
      "s_xor_b32 s70, s76, 256\n"
      "s_add_u32 s70, s8, s70\n"
      "s_addc_u32 s71, s9, 0\n"
      "s_add_u32 m0, s82, 147456\n"
      "v_mfma_f32_16x16x32_bf16 v[220:223], v[44:47], a[236:239], v[220:223]\n"
      "global_load_lds_dwordx4 v244, s[70:71] offset:0\n"
      "ds_read_b128 a[232:235], v251 offset:24576\n"
      "v_mfma_f32_16x16x32_bf16 v[208:211], v[48:51], a[240:243], v[208:211]\n"
      "v_mfma_f32_16x16x32_bf16 v[212:215], v[52:55], a[240:243], v[212:215]\n"
      "ds_read_b128 a[236:239], v251 offset:26624\n"
      "v_mfma_f32_16x16x32_bf16 v[216:219], v[48:51], a[244:247], v[216:219]\n"
      "v_mfma_f32_16x16x32_bf16 v[220:223], v[52:55], a[244:247], v[220:223]\n"
      "global_load_lds_dwordx4 v245, s[70:71] offset:1024\n"
      "ds_read_b128 a[240:243], v250 offset:28672\n"
      "v_mfma_f32_16x16x32_bf16 v[208:211], v[56:59], a[248:251], v[208:211]\n"
      "v_mfma_f32_16x16x32_bf16 v[212:215], v[60:63], a[248:251], v[212:215]\n"
      "ds_read_b128 a[244:247], v250 offset:30720\n"
      "v_mfma_f32_16x16x32_bf16 v[216:219], v[56:59], a[252:255], v[216:219]\n"
      "v_mfma_f32_16x16x32_bf16 v[220:223], v[60:63], a[252:255], v[220:223]\n"
      "global_load_lds_dwordx4 v246, s[70:71] offset:2048\n"
      "ds_read_b128 a[248:251], v251 offset:28672\n"
      "s_waitcnt lgkmcnt(15)\n"
      "v_mfma_f32_16x16x32_bf16 v[208:211], v[64:67], a[192:195], v[208:211]\n"
      "s_waitcnt vmcnt(12)\n"
      "ds_read_b128 a[72:75], v236 offset:16384\n"
      "v_mfma_f32_16x16x32_bf16 v[212:215], v[68:71], a[192:195], v[212:215]\n"
      "ds_read_b128 a[252:255], v251 offset:30720\n"
      "ds_read_b128 a[76:79], v236 offset:18432\n"
      "v_mfma_f32_16x16x32_bf16 v[216:219], v[64:67], a[196:199], v[216:219]\n"
      "ds_read_b128 a[104:107], v237 offset:16384\n"
      "ds_read_b128 a[108:111], v237 offset:18432\n"
      "v_mfma_f32_16x16x32_bf16 v[220:223], v[68:71], a[196:199], v[220:223]\n"
      "global_load_lds_dwordx4 v247, s[70:71] offset:3072\n"
      "v_mfma_f32_16x16x32_bf16 v[208:211], v[72:75], a[200:203], v[208:211]\n"
      "v_mfma_f32_16x16x32_bf16 v[212:215], v[76:79], a[200:203], v[212:215]\n"
      "v_mfma_f32_16x16x32_bf16 v[216:219], v[72:75], a[204:207], v[216:219]\n"
      "s_waitcnt vmcnt(8)\n"
      "v_cmp_ne_u32 vcc, s37, v252\n"
      "s_nop 1\n"
      "s_cbranch_vccnz .Lp_s2_%=\n"
      ".Lp_o3_%=:\n"
      "v_mfma_f32_16x16x32_bf16 v[220:223], v[76:79], a[204:207], v[220:223]\n"
      "global_load_dwordx4 v[128:131], v240, s[10:11] offset:-4096 sc1\n"
      "s_waitcnt lgkmcnt(15)\n"
      "v_mfma_f32_16x16x32_bf16 v[208:211], v[80:83], a[208:211], v[208:211]\n"
      "v_mfma_f32_16x16x32_bf16 v[212:215], v[84:87], a[208:211], v[212:215]\n"
      "v_mfma_f32_16x16x32_bf16 v[216:219], v[80:83], a[212:215], v[216:219]\n"
      "v_mfma_f32_16x16x32_bf16 v[220:223], v[84:87], a[212:215], v[220:223]\n"
      "global_load_dwordx4 v[132:135], v240, s[10:11] offset:-3072 sc1\n"
      "v_mfma_f32_16x16x32_bf16 v[208:211], v[88:91], a[216:219], v[208:211]\n"
      "v_mfma_f32_16x16x32_bf16 v[212:215], v[92:95], a[216:219], v[212:215]\n"
      "v_mfma_f32_16x16x32_bf16 v[216:219], v[88:91], a[220:223], v[216:219]\n"
      "v_mfma_f32_16x16x32_bf16 v[220:223], v[92:95], a[220:223], v[220:223]\n"
      "global_load_dwordx4 v[136:139], v240, s[10:11] offset:-2048 sc1\n"
      "s_waitcnt lgkmcnt(11)\n"
      "v_mfma_f32_16x16x32_bf16 v[208:211], v[96:99], a[224:227], v[208:211]\n"
      "ds_read_b128 a[80:83], v248 offset:4096\n"
      "ds_read_b128 a[84:87], v248 offset:6144\n"
      "v_mfma_f32_16x16x32_bf16 v[212:215], v[100:103], a[224:227], v[212:215]\n"
      "ds_read_b128 a[112:115], v249 offset:4096\n"
      "ds_read_b128 a[116:119], v249 offset:6144\n"
      "s_waitcnt lgkmcnt(14)\n"
      "v_mfma_f32_16x16x32_bf16 v[216:219], v[96:99], a[228:231], v[216:219]\n"
      "v_mfma_f32_16x16x32_bf16 v[220:223], v[100:103], a[228:231], v[220:223]\n"
      "global_load_dwordx4 v[140:143], v240, s[72:73] offset:-4096 sc1\n"
      "s_waitcnt lgkmcnt(13)\n"
      "v_mfma_f32_16x16x32_bf16 v[208:211], v[104:107], a[232:235], v[208:211]\n"
      "v_mfma_f32_16x16x32_bf16 v[212:215], v[108:111], a[232:235], v[212:215]\n"
      "s_waitcnt lgkmcnt(12)\n"
      "v_mfma_f32_16x16x32_bf16 v[216:219], v[104:107], a[236:239], v[216:219]\n"
      "v_mfma_f32_16x16x32_bf16 v[220:223], v[108:111], a[236:239], v[220:223]\n"
      "global_load_dwordx4 v[144:147], v240, s[72:73] offset:-3072 sc1\n"
      "s_waitcnt lgkmcnt(11)\n"
      "v_mfma_f32_16x16x32_bf16 v[208:211], v[112:115], a[240:243], v[208:211]\n"
      "global_store_dwordx4 v242, v[224:227], s[62:63] offset:-1024 sc1\n"
      "global_store_dwordx4 v242, v[228:231], s[62:63] offset:0 sc1\n"
      "v_mfma_f32_16x16x32_bf16 v[212:215], v[116:119], a[240:243], v[212:215]\n"
      "global_store_dwordx4 v242, v[232:235], s[62:63] offset:1024 sc1\n"
      "s_waitcnt lgkmcnt(10)\n"
      "v_mfma_f32_16x16x32_bf16 v[216:219], v[112:115], a[244:247], v[216:219]\n"
      "v_mfma_f32_16x16x32_bf16 v[220:223], v[116:119], a[244:247], v[220:223]\n"
      "global_load_dwordx4 v[148:151], v240, s[72:73] offset:-2048 sc1\n"
      "s_waitcnt lgkmcnt(9)\n"
      "v_mfma_f32_16x16x32_bf16 v[208:211], v[120:123], a[248:251], v[208:211]\n"
      "v_mfma_f32_16x16x32_bf16 v[212:215], v[124:127], a[248:251], v[212:215]\n"
      "s_waitcnt lgkmcnt(7)\n"
      "v_mfma_f32_16x16x32_bf16 v[216:219], v[120:123], a[252:255], v[216:219]\n"
      "v_mfma_f32_16x16x32_bf16 v[220:223], v[124:127], a[252:255], v[220:223]\n"
      "s_waitcnt vmcnt(8)\n"
      "v_mfma_f32_16x16x32_bf16 v[0:3], v[128:131], a[0:3], 0\n"
      "ds_read_b128 a[88:91], v236 offset:32768\n"
      "ds_read_b128 a[92:95], v236 offset:34816\n"
      "v_mfma_f32_16x16x32_bf16 v[4:7], v[128:131], a[4:7], 0\n"
      "ds_read_b128 a[120:123], v237 offset:32768\n"
      "ds_read_b128 a[124:127], v237 offset:34816\n"
      "v_mfma_f32_16x16x32_bf16 v[8:11], v[128:131], a[8:11], 0\n"
      "v_cvt_pk_bf16_f32 v192, v208, v209\n"
      "v_cvt_pk_bf16_f32 v193, v210, v211\n"
      "v_mfma_f32_16x16x32_bf16 v[12:15], v[128:131], a[12:15], 0\n"
      "v_cvt_pk_bf16_f32 v194, v212, v213\n"
      "v_cvt_pk_bf16_f32 v195, v214, v215\n"
      "v_mfma_f32_16x16x32_bf16 v[16:19], v[128:131], a[16:19], 0\n"
      "v_xor_b32 v192, s85, v192\n"
      "v_xor_b32 v193, s85, v193\n"
      "v_mfma_f32_16x16x32_bf16 v[20:23], v[128:131], a[20:23], 0\n"
      "v_xor_b32 v194, s85, v194\n"
      "v_xor_b32 v195, s85, v195\n"
      "v_mfma_f32_16x16x32_bf16 v[24:27], v[128:131], a[24:27], 0\n"
      "v_cvt_pk_bf16_f32 v196, v216, v217\n"
      "v_cvt_pk_bf16_f32 v197, v218, v219\n"
      "v_mfma_f32_16x16x32_bf16 v[28:31], v[128:131], a[28:31], 0\n"
      "v_cvt_pk_bf16_f32 v198, v220, v221\n"
      "v_cvt_pk_bf16_f32 v199, v222, v223\n"
      "s_waitcnt vmcnt(7)\n"
      "v_mfma_f32_16x16x32_bf16 v[32:35], v[132:135], a[0:3], 0\n"
      "s_xor_b32 s70, s77, 256\n"
      "s_add_u32 s70, s8, s70\n"
      "s_addc_u32 s71, s9, 0\n"
      "s_add_u32 m0, s41, 8192\n"
      "v_mfma_f32_16x16x32_bf16 v[36:39], v[132:135], a[4:7], 0\n"
      "global_load_lds_dwordx4 v244, s[70:71] offset:0\n"
      "v_xor_b32 v196, s85, v196\n"
      "v_mfma_f32_16x16x32_bf16 v[40:43], v[132:135], a[8:11], 0\n"
      "v_xor_b32 v197, s85, v197\n"
      "v_xor_b32 v198, s85, v198\n"
      "v_mfma_f32_16x16x32_bf16 v[44:47], v[132:135], a[12:15], 0\n"
      "v_xor_b32 v199, s85, v199\n"
      "global_store_dwordx4 v242, v[192:195], s[62:63] offset:2048 sc1\n"
      "v_mfma_f32_16x16x32_bf16 v[48:51], v[132:135], a[16:19], 0\n"
      "global_load_lds_dwordx4 v245, s[70:71] offset:1024\n"
      "global_store_dwordx4 v242, v[196:199], s[62:63] offset:3072 sc1\n"
      "v_mfma_f32_16x16x32_bf16 v[52:55], v[132:135], a[20:23], 0\n"
      "v_mfma_f32_16x16x32_bf16 v[56:59], v[132:135], a[24:27], 0\n"
      "v_mfma_f32_16x16x32_bf16 v[60:63], v[132:135], a[28:31], 0\n"
      "s_waitcnt vmcnt(9)\n"
      "global_load_lds_dwordx4 v246, s[70:71] offset:2048\n"
      "v_mfma_f32_16x16x32_bf16 v[64:67], v[136:139], a[0:3], 0\n"
      "ds_read_b128 a[128:131], v236 offset:49152\n"
      "ds_read_b128 a[132:135], v236 offset:51200\n"
      "v_mfma_f32_16x16x32_bf16 v[68:71], v[136:139], a[4:7], 0\n"
      "ds_read_b128 a[160:163], v237 offset:49152\n"
      "ds_read_b128 a[164:167], v237 offset:51200\n"
      "v_mfma_f32_16x16x32_bf16 v[72:75], v[136:139], a[8:11], 0\n"
      "global_load_lds_dwordx4 v247, s[70:71] offset:3072\n"
      "v_mfma_f32_16x16x32_bf16 v[76:79], v[136:139], a[12:15], 0\n"
      "v_mfma_f32_16x16x32_bf16 v[80:83], v[136:139], a[16:19], 0\n"
      "global_load_dwordx4 v[152:155], v241, s[10:11] offset:-4096 sc1\n"
      "v_mfma_f32_16x16x32_bf16 v[84:87], v[136:139], a[20:23], 0\n"
      "v_mfma_f32_16x16x32_bf16 v[88:91], v[136:139], a[24:27], 0\n"
      "v_mfma_f32_16x16x32_bf16 v[92:95], v[136:139], a[28:31], 0\n"
      "global_load_dwordx4 v[156:159], v241, s[10:11] offset:-3072 sc1\n"
      "v_mfma_f32_16x16x32_bf16 v[0:3], v[140:143], a[32:35], v[0:3]\n"
      "v_mfma_f32_16x16x32_bf16 v[4:7], v[140:143], a[36:39], v[4:7]\n"
      "v_mfma_f32_16x16x32_bf16 v[8:11], v[140:143], a[40:43], v[8:11]\n"
      "global_load_dwordx4 v[160:163], v241, s[10:11] offset:-2048 sc1\n"
      "v_mfma_f32_16x16x32_bf16 v[12:15], v[140:143], a[44:47], v[12:15]\n"
      "v_mfma_f32_16x16x32_bf16 v[16:19], v[140:143], a[48:51], v[16:19]\n"
      "v_mfma_f32_16x16x32_bf16 v[20:23], v[140:143], a[52:55], v[20:23]\n"
      "s_xor_b32 s70, s78, 256\n"
      "s_add_u32 s70, s8, s70\n"
      "s_addc_u32 s71, s9, 0\n"
      "s_add_u32 m0, s41, 12288\n"
      "v_mfma_f32_16x16x32_bf16 v[24:27], v[140:143], a[56:59], v[24:27]\n"
      "global_load_lds_dwordx4 v244, s[70:71] offset:0\n"
      "v_mfma_f32_16x16x32_bf16 v[28:31], v[140:143], a[60:63], v[28:31]\n"
      "s_waitcnt vmcnt(10)\n"
      "v_mfma_f32_16x16x32_bf16 v[32:35], v[144:147], a[32:35], v[32:35]\n"
      "v_add_u32 v238, s42, v242\n"
      "v_mov_b32 v200, s37\n"
      "v_mov_b32 v201, s37\n"
      "v_mov_b32 v202, s37\n"
      "v_mov_b32 v203, s37\n"
      "s_waitcnt vmcnt(6)\n"
      "s_mov_b64 exec, 0xff\n"
      "global_store_dwordx4 v238, v[200:203], s[58:59] offset:128 sc1\n"
      "s_mov_b64 exec, -1\n"
      "v_mfma_f32_16x16x32_bf16 v[36:39], v[144:147], a[36:39], v[36:39]\n"
      "s_lshl2_add_u32 s48, s36, s35\n"
      "global_load_lds_dwordx4 v245, s[70:71] offset:1024\n"
      "s_mul_i32 s48, s48, 147712\n"
      "v_mfma_f32_16x16x32_bf16 v[40:43], v[144:147], a[40:43], v[40:43]\n"
      "s_mul_i32 s49, s33, 3072\n"
      "s_add_u32 s48, s48, s49\n"
      "s_add_u32 s48, s48, 6291456\n"
      "v_mfma_f32_16x16x32_bf16 v[44:47], v[144:147], a[44:47], v[44:47]\n"
      "s_add_u32 s60, s18, s48\n"
      "s_addc_u32 s61, s19, 0\n"
      "s_mul_i32 s49, s34, 36864\n"
      "v_mfma_f32_16x16x32_bf16 v[48:51], v[144:147], a[48:51], v[48:51]\n"
      "s_add_u32 s62, s60, s49\n"
      "s_addc_u32 s63, s61, 0\n"
      "global_load_lds_dwordx4 v246, s[70:71] offset:2048\n"
      "v_mfma_f32_16x16x32_bf16 v[52:55], v[144:147], a[52:55], v[52:55]\n"
      "v_mfma_f32_16x16x32_bf16 v[56:59], v[144:147], a[56:59], v[56:59]\n"
      "v_mfma_f32_16x16x32_bf16 v[60:63], v[144:147], a[60:63], v[60:63]\n"
      "global_load_lds_dwordx4 v247, s[70:71] offset:3072\n"
      "v_mfma_f32_16x16x32_bf16 v[64:67], v[148:151], a[32:35], v[64:67]\n"
      "v_mfma_f32_16x16x32_bf16 v[68:71], v[148:151], a[36:39], v[68:71]\n"
      "s_xor_b32 s70, s79, 256\n"
      "s_add_u32 s70, s8, s70\n"
      "s_addc_u32 s71, s9, 0\n"
      "s_add_u32 m0, s41, 0\n"
      "v_mfma_f32_16x16x32_bf16 v[72:75], v[148:151], a[40:43], v[72:75]\n"
      "global_load_lds_dwordx4 v244, s[70:71] offset:0\n"
      "v_mfma_f32_16x16x32_bf16 v[76:79], v[148:151], a[44:47], v[76:79]\n"
      "v_mfma_f32_16x16x32_bf16 v[80:83], v[148:151], a[48:51], v[80:83]\n"
      "v_mfma_f32_16x16x32_bf16 v[84:87], v[148:151], a[52:55], v[84:87]\n"
      "global_load_lds_dwordx4 v245, s[70:71] offset:1024\n"
      "v_mfma_f32_16x16x32_bf16 v[88:91], v[148:151], a[56:59], v[88:91]\n"
      "v_mfma_f32_16x16x32_bf16 v[92:95], v[148:151], a[60:63], v[92:95]\n"
      "s_waitcnt vmcnt(8)\n"
      "v_mfma_f32_16x16x32_bf16 v[0:3], v[152:155], a[64:67], v[0:3]\n"
      "global_load_lds_dwordx4 v246, s[70:71] offset:2048\n"
      "v_mfma_f32_16x16x32_bf16 v[4:7], v[152:155], a[68:71], v[4:7]\n"
      "v_mfma_f32_16x16x32_bf16 v[8:11], v[152:155], a[72:75], v[8:11]\n"
      "s_waitcnt lgkmcnt(14)\n"
      "v_mfma_f32_16x16x32_bf16 v[12:15], v[152:155], a[76:79], v[12:15]\n"
      "global_load_lds_dwordx4 v247, s[70:71] offset:3072\n"
      "s_waitcnt lgkmcnt(11)\n"
      "v_mfma_f32_16x16x32_bf16 v[16:19], v[152:155], a[80:83], v[16:19]\n"
      "s_waitcnt lgkmcnt(10)\n"
      "v_mfma_f32_16x16x32_bf16 v[20:23], v[152:155], a[84:87], v[20:23]\n"
      "global_load_dwordx4 v[164:167], v241, s[72:73] offset:-4096 sc1\n"
      "s_waitcnt lgkmcnt(7)\n"
      "v_mfma_f32_16x16x32_bf16 v[24:27], v[152:155], a[88:91], v[24:27]\n"
      "s_waitcnt lgkmcnt(6)\n"
      "v_mfma_f32_16x16x32_bf16 v[28:31], v[152:155], a[92:95], v[28:31]\n"
      "v_mfma_f32_16x16x32_bf16 v[32:35], v[156:159], a[64:67], v[32:35]\n"
      "global_load_dwordx4 v[168:171], v241, s[72:73] offset:-3072 sc1\n"
      "v_mfma_f32_16x16x32_bf16 v[36:39], v[156:159], a[68:71], v[36:39]\n"
      "v_mfma_f32_16x16x32_bf16 v[40:43], v[156:159], a[72:75], v[40:43]\n"
      "v_mfma_f32_16x16x32_bf16 v[44:47], v[156:159], a[76:79], v[44:47]\n"
      "global_load_dwordx4 v[172:175], v241, s[72:73] offset:-2048 sc1\n"
      "v_mfma_f32_16x16x32_bf16 v[48:51], v[156:159], a[80:83], v[48:51]\n"
      "v_mfma_f32_16x16x32_bf16 v[52:55], v[156:159], a[84:87], v[52:55]\n"
      "v_mfma_f32_16x16x32_bf16 v[56:59], v[156:159], a[88:91], v[56:59]\n"
      "global_load_dwordx4 v[176:179], v240, s[14:15] offset:-4096 sc1\n"
      "v_mfma_f32_16x16x32_bf16 v[60:63], v[156:159], a[92:95], v[60:63]\n"
      "s_waitcnt vmcnt(3)\n"
      "ds_read_b128 a[136:139], v248 offset:8192\n"
      "ds_read_b128 a[140:143], v248 offset:10240\n"
      "v_mfma_f32_16x16x32_bf16 v[64:67], v[160:163], a[64:67], v[64:67]\n"
      "ds_read_b128 a[168:171], v249 offset:8192\n"
      "ds_read_b128 a[172:175], v249 offset:10240\n"
      "v_mfma_f32_16x16x32_bf16 v[68:71], v[160:163], a[68:71], v[68:71]\n"
      "global_load_dwordx4 v[180:183], v240, s[14:15] offset:-3072 sc1\n"
      "v_mfma_f32_16x16x32_bf16 v[72:75], v[160:163], a[72:75], v[72:75]\n"
      "v_mfma_f32_16x16x32_bf16 v[76:79], v[160:163], a[76:79], v[76:79]\n"
      "v_mfma_f32_16x16x32_bf16 v[80:83], v[160:163], a[80:83], v[80:83]\n"
      "global_load_dwordx4 v[184:187], v240, s[14:15] offset:-2048 sc1\n"
      "global_load_dwordx4 v[188:191], v240, s[74:75] offset:-4096 sc1\n"
      "v_mfma_f32_16x16x32_bf16 v[84:87], v[160:163], a[84:87], v[84:87]\n"
      "global_load_dwordx4 v[128:131], v240, s[74:75] offset:-3072 sc1\n"
      "global_load_dword v252, v253, s[58:59] offset:128 sc1\n"
      "v_mfma_f32_16x16x32_bf16 v[88:91], v[160:163], a[88:91], v[88:91]\n"
      "s_lshr_b32 s54, s34, 1\n"
      "global_load_dwordx4 v[132:135], v240, s[74:75] offset:-2048 sc1\n"
      "s_lshl2_add_u32 s54, s54, s35\n"
      "v_mfma_f32_16x16x32_bf16 v[92:95], v[160:163], a[92:95], v[92:95]\n"
      "s_lshl_b32 s54, s54, 12\n"
      "s_add_u32 s54, s54, 4096\n"
      "v_lshl_add_u32 v200, v243, 6, s54\n"
      "v_mfma_f32_16x16x32_bf16 v[0:3], v[164:167], a[96:99], v[0:3]\n"
      "global_load_dwordx2 v[202:203], v200, s[58:59] sc1\n"
      "v_mfma_f32_16x16x32_bf16 v[4:7], v[164:167], a[100:103], v[4:7]\n"
      "v_mfma_f32_16x16x32_bf16 v[8:11], v[164:167], a[104:107], v[8:11]\n"
      "v_mfma_f32_16x16x32_bf16 v[12:15], v[164:167], a[108:111], v[12:15]\n"
      "v_mfma_f32_16x16x32_bf16 v[16:19], v[164:167], a[112:115], v[16:19]\n"
      "v_mfma_f32_16x16x32_bf16 v[20:23], v[164:167], a[116:119], v[20:23]\n"
      "s_waitcnt lgkmcnt(9)\n"
      "v_mfma_f32_16x16x32_bf16 v[24:27], v[164:167], a[120:123], v[24:27]\n"
      "s_waitcnt lgkmcnt(8)\n"
      "v_mfma_f32_16x16x32_bf16 v[28:31], v[164:167], a[124:127], v[28:31]\n"
      "s_waitcnt vmcnt(8)\n"
      "v_mfma_f32_16x16x32_bf16 v[32:35], v[168:171], a[96:99], v[32:35]\n"
      "v_mfma_f32_16x16x32_bf16 v[36:39], v[168:171], a[100:103], v[36:39]\n"
      "v_mfma_f32_16x16x32_bf16 v[40:43], v[168:171], a[104:107], v[40:43]\n"
      "v_mfma_f32_16x16x32_bf16 v[44:47], v[168:171], a[108:111], v[44:47]\n"
      "v_mfma_f32_16x16x32_bf16 v[48:51], v[168:171], a[112:115], v[48:51]\n"
      "ds_read_b128 a[144:147], v248 offset:12288\n"
      "ds_read_b128 a[148:151], v248 offset:14336\n"
      "v_mfma_f32_16x16x32_bf16 v[52:55], v[168:171], a[116:119], v[52:55]\n"
      "ds_read_b128 a[176:179], v249 offset:12288\n"
      "ds_read_b128 a[180:183], v249 offset:14336\n"
      "v_mfma_f32_16x16x32_bf16 v[56:59], v[168:171], a[120:123], v[56:59]\n"
      "v_mfma_f32_16x16x32_bf16 v[60:63], v[168:171], a[124:127], v[60:63]\n"
      "v_mfma_f32_16x16x32_bf16 v[64:67], v[172:175], a[96:99], v[64:67]\n"
      "s_xor_b32 s70, s76, 384\n"
      "s_add_u32 s70, s8, s70\n"
      "s_addc_u32 s71, s9, 0\n"
      "s_add_u32 m0, s82, 98304\n"
      "v_mfma_f32_16x16x32_bf16 v[68:71], v[172:175], a[100:103], v[68:71]\n"
      "global_load_lds_dwordx4 v244, s[70:71] offset:0\n"
      "v_mfma_f32_16x16x32_bf16 v[72:75], v[172:175], a[104:107], v[72:75]\n"
      "v_mfma_f32_16x16x32_bf16 v[76:79], v[172:175], a[108:111], v[76:79]\n"
      "v_mfma_f32_16x16x32_bf16 v[80:83], v[172:175], a[112:115], v[80:83]\n"
      "global_load_lds_dwordx4 v245, s[70:71] offset:1024\n"
      "v_mfma_f32_16x16x32_bf16 v[84:87], v[172:175], a[116:119], v[84:87]\n"
      "v_mfma_f32_16x16x32_bf16 v[88:91], v[172:175], a[120:123], v[88:91]\n"
      "v_mfma_f32_16x16x32_bf16 v[92:95], v[172:175], a[124:127], v[92:95]\n"
      "s_waitcnt vmcnt(8)\n"
      "global_load_lds_dwordx4 v246, s[70:71] offset:2048\n"
      "s_waitcnt lgkmcnt(11)\n"
      "v_mfma_f32_16x16x32_bf16 v[0:3], v[176:179], a[128:131], v[0:3]\n"
      "ds_read_b128 a[152:155], v248 offset:0\n"
      "ds_read_b128 a[156:159], v248 offset:2048\n"
      "s_waitcnt lgkmcnt(12)\n"
      "v_mfma_f32_16x16x32_bf16 v[4:7], v[176:179], a[132:135], v[4:7]\n"
      "ds_read_b128 a[184:187], v249 offset:0\n"
      "ds_read_b128 a[188:191], v249 offset:2048\n"
      "s_waitcnt lgkmcnt(11)\n"
      "v_mfma_f32_16x16x32_bf16 v[8:11], v[176:179], a[136:139], v[8:11]\n"
      "global_load_lds_dwordx4 v247, s[70:71] offset:3072\n"
      "s_waitcnt lgkmcnt(10)\n"
      "v_mfma_f32_16x16x32_bf16 v[12:15], v[176:179], a[140:143], v[12:15]\n"
      "s_waitcnt lgkmcnt(7)\n"
      "v_mfma_f32_16x16x32_bf16 v[16:19], v[176:179], a[144:147], v[16:19]\n"
      "s_xor_b32 s70, s77, 384\n"
      "s_add_u32 s70, s8, s70\n"
      "s_addc_u32 s71, s9, 0\n"
      "s_add_u32 m0, s82, 114688\n"
      "s_waitcnt lgkmcnt(6)\n"
      "v_mfma_f32_16x16x32_bf16 v[20:23], v[176:179], a[148:151], v[20:23]\n"
      "global_load_lds_dwordx4 v244, s[70:71] offset:0\n"
      "s_waitcnt lgkmcnt(3)\n"
      "v_mfma_f32_16x16x32_bf16 v[24:27], v[176:179], a[152:155], v[24:27]\n"
      "s_waitcnt lgkmcnt(2)\n"
      "v_mfma_f32_16x16x32_bf16 v[28:31], v[176:179], a[156:159], v[28:31]\n"
      "v_mfma_f32_16x16x32_bf16 v[32:35], v[180:183], a[128:131], v[32:35]\n"
      "global_load_lds_dwordx4 v245, s[70:71] offset:1024\n"
      "v_mfma_f32_16x16x32_bf16 v[36:39], v[180:183], a[132:135], v[36:39]\n"
      "v_mfma_f32_16x16x32_bf16 v[40:43], v[180:183], a[136:139], v[40:43]\n"
      "v_mfma_f32_16x16x32_bf16 v[44:47], v[180:183], a[140:143], v[44:47]\n"
      "global_load_lds_dwordx4 v246, s[70:71] offset:2048\n"
      "v_mfma_f32_16x16x32_bf16 v[48:51], v[180:183], a[144:147], v[48:51]\n"
      "v_mfma_f32_16x16x32_bf16 v[52:55], v[180:183], a[148:151], v[52:55]\n"
      "v_mfma_f32_16x16x32_bf16 v[56:59], v[180:183], a[152:155], v[56:59]\n"
      "global_load_lds_dwordx4 v247, s[70:71] offset:3072\n"
      "s_waitcnt vmcnt(10)\n"
      "v_cmp_ne_u32 vcc, s37, v252\n"
      "s_nop 1\n"
      "s_cbranch_vccnz .Lp_s5_%=\n"
      ".Lp_o6_%=:\n"
      "v_mfma_f32_16x16x32_bf16 v[60:63], v[180:183], a[156:159], v[60:63]\n"
      "v_mfma_f32_16x16x32_bf16 v[64:67], v[184:187], a[128:131], v[64:67]\n"
      "s_xor_b32 s70, s78, 384\n"
      "s_add_u32 s70, s8, s70\n"
      "s_addc_u32 s71, s9, 0\n"
      "s_add_u32 m0, s41, 4096\n"
      "v_mfma_f32_16x16x32_bf16 v[68:71], v[184:187], a[132:135], v[68:71]\n"
      "global_load_lds_dwordx4 v244, s[70:71] offset:0\n"
      "global_load_dwordx4 v[136:139], v240, s[10:11] offset:-1024 sc1\n"
      "v_mfma_f32_16x16x32_bf16 v[72:75], v[184:187], a[136:139], v[72:75]\n"
      "v_mfma_f32_16x16x32_bf16 v[76:79], v[184:187], a[140:143], v[76:79]\n"
      "v_mfma_f32_16x16x32_bf16 v[80:83], v[184:187], a[144:147], v[80:83]\n"
      "global_load_lds_dwordx4 v245, s[70:71] offset:1024\n"
      "global_load_dwordx4 v[140:143], v240, s[72:73] offset:-1024 sc1\n"
      "v_mfma_f32_16x16x32_bf16 v[84:87], v[184:187], a[148:151], v[84:87]\n"
      "v_mfma_f32_16x16x32_bf16 v[88:91], v[184:187], a[152:155], v[88:91]\n"
      "v_mfma_f32_16x16x32_bf16 v[92:95], v[184:187], a[156:159], v[92:95]\n"
      "global_load_lds_dwordx4 v246, s[70:71] offset:2048\n"
      "v_mfma_f32_16x16x32_bf16 v[0:3], v[188:191], a[160:163], v[0:3]\n"
      "global_load_dwordx4 v[144:147], v241, s[10:11] offset:-1024 sc1\n"
      "v_mfma_f32_16x16x32_bf16 v[4:7], v[188:191], a[164:167], v[4:7]\n"
      "v_mfma_f32_16x16x32_bf16 v[8:11], v[188:191], a[168:171], v[8:11]\n"
      "global_load_lds_dwordx4 v247, s[70:71] offset:3072\n"
      "v_mfma_f32_16x16x32_bf16 v[12:15], v[188:191], a[172:175], v[12:15]\n"
      "v_mfma_f32_16x16x32_bf16 v[16:19], v[188:191], a[176:179], v[16:19]\n"
      "s_xor_b32 s70, s79, 384\n"
      "s_add_u32 s70, s8, s70\n"
      "s_addc_u32 s71, s9, 0\n"
      "s_add_u32 m0, s82, 131072\n"
      "v_mfma_f32_16x16x32_bf16 v[20:23], v[188:191], a[180:183], v[20:23]\n"
      "global_load_lds_dwordx4 v244, s[70:71] offset:0\n"
      "global_load_dwordx4 v[148:151], v241, s[72:73] offset:-1024 sc1\n"
      "s_waitcnt lgkmcnt(1)\n"
      "v_mfma_f32_16x16x32_bf16 v[24:27], v[188:191], a[184:187], v[24:27]\n"
      "s_waitcnt lgkmcnt(0)\n"
      "v_mfma_f32_16x16x32_bf16 v[28:31], v[188:191], a[188:191], v[28:31]\n"
      "s_waitcnt vmcnt(18)\n"
      "v_mfma_f32_16x16x32_bf16 v[32:35], v[128:131], a[160:163], v[32:35]\n"
      "global_load_lds_dwordx4 v245, s[70:71] offset:1024\n"
      "global_load_dwordx4 v[152:155], v240, s[14:15] offset:-1024 sc1\n"
      "v_mfma_f32_16x16x32_bf16 v[36:39], v[128:131], a[164:167], v[36:39]\n"
      "v_mfma_f32_16x16x32_bf16 v[40:43], v[128:131], a[168:171], v[40:43]\n"
      "s_waitcnt vmcnt(19)\n"
      "v_cmp_ne_u32 vcc, s37, v203\n"
      "s_nop 1\n"
      "s_cbranch_vccnz .Lp_s8_%=\n"
      ".Lp_o9_%=:\n"
      "v_mfma_f32_16x16x32_bf16 v[44:47], v[128:131], a[172:175], v[44:47]\n"
      "global_load_lds_dwordx4 v246, s[70:71] offset:2048\n"
      "v_max_u32_dpp v202, v202, v202 quad_perm:[1,0,3,2] row_mask:0xf bank_mask:0xf\n"
      "v_mfma_f32_16x16x32_bf16 v[48:51], v[128:131], a[176:179], v[48:51]\n"
      "global_load_dwordx4 v[156:159], v241, s[14:15] offset:-4096 sc1\n"
      "v_max_u32_dpp v202, v202, v202 quad_perm:[2,3,0,1] row_mask:0xf bank_mask:0xf\n"
      "v_mfma_f32_16x16x32_bf16 v[52:55], v[128:131], a[180:183], v[52:55]\n"
      "s_nop 0\n"
      "v_max_u32_dpp v202, v202, v202 row_half_mirror row_mask:0xf bank_mask:0xf\n"
      "v_mfma_f32_16x16x32_bf16 v[56:59], v[128:131], a[184:187], v[56:59]\n"
      "global_load_lds_dwordx4 v247, s[70:71] offset:3072\n"
      "v_max_u32_dpp v202, v202, v202 row_mirror row_mask:0xf bank_mask:0xf\n"
      "v_mfma_f32_16x16x32_bf16 v[60:63], v[128:131], a[188:191], v[60:63]\n"
      "v_mov_b32 v201, v202\n"
      "s_nop 1\n"
      "v_permlane16_swap_b32 v202, v201\n"
      "v_mfma_f32_16x16x32_bf16 v[64:67], v[132:135], a[160:163], v[64:67]\n"
      "s_barrier\n"
      "s_lshr_b32 s80, s32, 4\n"
      "s_bfe_u32 s43, s32, 0x10002\n"
      "s_lshl1_add_u32 s80, s80, s43\n"
      "s_and_b32 s81, s32, 3\n"
      "s_cmp_lt_u32 s80, 28\n"
      "s_cselect_b32 s86, -1, 0\n"
      "s_lshr_b32 s83, s33, 1\n"
      "s_lshl1_add_u32 s83, s81, s83\n"
      "s_min_u32 s43, s80, 27\n"
      "v_lshrrev_b32 v226, 3, v243\n"
      "v_and_b32 v227, 0x70, v242\n"
      "v_lshl_or_b32 v224, v226, 10, v227\n"
      "v_add_u32 v225, 7168, v224\n"
      "v_add_u32 v226, 14336, v224\n"
      "v_add_u32 v227, 21504, v224\n"
      "s_lshr_b32 s44, s33, 1\n"
      "s_lshl1_add_u32 s44, s43, s44\n"
      "s_lshl_b32 s44, s44, 15\n"
      "s_and_b32 s45, s33, 1\n"
      "s_lshl1_add_u32 s45, s81, s45\n"
      "s_lshl_b32 s45, s45, 7\n"
      "s_add_u32 s44, s44, s45\n"
      "s_add_u32 s70, s30, s44\n"
      "s_addc_u32 s71, s31, 0\n"
      "s_lshl_b32 s43, s33, 12\n"
      "s_add_u32 m0, s43, 65536\n"
      "s_nop 0\n"
      "global_load_lds_dwordx4 v224, s[70:71] offset:0\n"
      "global_load_lds_dwordx4 v225, s[70:71] offset:1024\n"
      "global_load_lds_dwordx4 v226, s[70:71] offset:2048\n"
      "global_load_lds_dwordx4 v227, s[70:71] offset:3072\n"
      "v_mfma_f32_16x16x32_bf16 v[68:71], v[132:135], a[164:167], v[68:71]\n"
      "global_load_dwordx4 v[160:163], v241, s[74:75] offset:-4096 sc1\n"
      "v_max_u32 v202, v202, v201\n"
      "v_mfma_f32_16x16x32_bf16 v[72:75], v[132:135], a[168:171], v[72:75]\n"
      "v_mov_b32 v201, v202\n"
      "s_nop 1\n"
      "v_permlane32_swap_b32 v202, v201\n"
      "v_mfma_f32_16x16x32_bf16 v[76:79], v[132:135], a[172:175], v[76:79]\n"
      "v_max_u32 v202, v202, v201\n"
      "s_nop 0\n"
      "v_readfirstlane_b32 s40, v202\n"
      "v_mfma_f32_16x16x32_bf16 v[80:83], v[132:135], a[176:179], v[80:83]\n"
      "s_nop 0\n"
      "s_andn2_b32 s42, 0x7ff, s40\n"
      "global_load_dwordx4 v[164:167], v241, s[14:15] offset:-3072 sc1\n"
      "v_mfma_f32_16x16x32_bf16 v[84:87], v[132:135], a[180:183], v[84:87]\n"
      "s_lshr_b32 s43, s42, 5\n"
      "s_lshl2_add_u32 s44, s36, s33\n"
      "s_cmp_eq_u32 s43, s44\n"
      "s_cbranch_scc0 .Lp_e10_%=\n"
      "s_lshl_b32 s43, s42, 10\n"
      "v_add_u32 v228, s43, v242\n"
      "global_load_dwordx4 v[192:195], v228, s[16:17]\n"
      "global_load_dwordx4 v[196:199], v242, s[24:25]\n"
      ".Lp_e10_%=:\n"
      "v_mfma_f32_16x16x32_bf16 v[88:91], v[132:135], a[184:187], v[88:91]\n"
      "v_mfma_f32_16x16x32_bf16 v[92:95], v[132:135], a[188:191], v[92:95]\n"
      "s_waitcnt vmcnt(16)\n"
      "ds_read_b128 a[192:195], v236 offset:0\n"
      "ds_read_b128 a[196:199], v236 offset:2048\n"
      "v_mfma_f32_16x16x32_bf16 v[96:99], v[136:139], a[0:3], 0\n"
      "global_load_dwordx4 v[168:171], v241, s[74:75] offset:-3072 sc1\n"
      "ds_read_b128 a[224:227], v237 offset:0\n"
      "v_mfma_f32_16x16x32_bf16 v[100:103], v[136:139], a[4:7], 0\n"
      "ds_read_b128 a[228:231], v237 offset:2048\n"
      "v_mfma_f32_16x16x32_bf16 v[104:107], v[136:139], a[8:11], 0\n"
      "v_mfma_f32_16x16x32_bf16 v[108:111], v[136:139], a[12:15], 0\n"
      "v_mfma_f32_16x16x32_bf16 v[112:115], v[136:139], a[16:19], 0\n"
      "global_load_dwordx4 v[172:175], v241, s[14:15] offset:-2048 sc1\n"
      "v_mfma_f32_16x16x32_bf16 v[116:119], v[136:139], a[20:23], 0\n"
      "v_mfma_f32_16x16x32_bf16 v[120:123], v[136:139], a[24:27], 0\n"
      "v_mfma_f32_16x16x32_bf16 v[124:127], v[136:139], a[28:31], 0\n"
      "v_mfma_f32_16x16x32_bf16 v[96:99], v[140:143], a[32:35], v[96:99]\n"
      "v_mfma_f32_16x16x32_bf16 v[100:103], v[140:143], a[36:39], v[100:103]\n"
      "v_mfma_f32_16x16x32_bf16 v[104:107], v[140:143], a[40:43], v[104:107]\n"
      "global_load_dwordx4 v[176:179], v241, s[74:75] offset:-2048 sc1\n"
      "v_mfma_f32_16x16x32_bf16 v[108:111], v[140:143], a[44:47], v[108:111]\n"
      "ds_read_b128 a[200:203], v236 offset:16384\n"
      "ds_read_b128 a[204:207], v236 offset:18432\n"
      "v_mfma_f32_16x16x32_bf16 v[112:115], v[140:143], a[48:51], v[112:115]\n"
      "ds_read_b128 a[232:235], v237 offset:16384\n"
      "ds_read_b128 a[236:239], v237 offset:18432\n"
      "v_mfma_f32_16x16x32_bf16 v[116:119], v[140:143], a[52:55], v[116:119]\n"
      "v_mfma_f32_16x16x32_bf16 v[120:123], v[140:143], a[56:59], v[120:123]\n"
      "v_mfma_f32_16x16x32_bf16 v[124:127], v[140:143], a[60:63], v[124:127]\n"
      "s_waitcnt vmcnt(14)\n"
      "v_mfma_f32_16x16x32_bf16 v[96:99], v[144:147], a[64:67], v[96:99]\n"
      "v_mfma_f32_16x16x32_bf16 v[100:103], v[144:147], a[68:71], v[100:103]\n"
      "v_mfma_f32_16x16x32_bf16 v[104:107], v[144:147], a[72:75], v[104:107]\n"
      "global_load_dwordx4 v[180:183], v240, s[74:75] offset:-1024 sc1\n"
      "v_mfma_f32_16x16x32_bf16 v[108:111], v[144:147], a[76:79], v[108:111]\n"
      "v_mfma_f32_16x16x32_bf16 v[112:115], v[144:147], a[80:83], v[112:115]\n"
      "v_mfma_f32_16x16x32_bf16 v[116:119], v[144:147], a[84:87], v[116:119]\n"
      "s_lshl2_add_u32 s54, s36, s33\n"
      "s_lshl_b32 s55, s54, 15\n"
      "s_lshl2_add_u32 s56, s35, s34\n"
      "v_mfma_f32_16x16x32_bf16 v[120:123], v[144:147], a[88:91], v[120:123]\n"
      "s_lshl_b32 s56, s56, 6\n"
      "s_add_u32 s55, s55, s56\n"
      "v_and_b32 v209, 15, v243\n"
      "v_mfma_f32_16x16x32_bf16 v[124:127], v[144:147], a[92:95], v[124:127]\n"
      "ds_read_b128 a[208:211], v248 offset:4096\n"
      "v_and_b32 v208, 48, v243\n"
      "v_mfma_f32_16x16x32_bf16 v[96:99], v[148:151], a[96:99], v[96:99]\n"
      "ds_read_b128 a[212:215], v248 offset:6144\n"
      "v_lshl_or_b32 v208, v209, 10, v208\n"
      "v_mfma_f32_16x16x32_bf16 v[100:103], v[148:151], a[100:103], v[100:103]\n"
      "ds_read_b128 a[240:243], v249 offset:4096\n"
      "v_add_u32 v208, s55, v208\n"
      "v_mfma_f32_16x16x32_bf16 v[104:107], v[148:151], a[104:107], v[104:107]\n"
      "global_load_dwordx4 v[184:187], v241, s[14:15] offset:-1024 sc1\n"
      "v_add_u32 v210, 16384, v208\n"
      "v_mfma_f32_16x16x32_bf16 v[108:111], v[148:151], a[108:111], v[108:111]\n"
      "ds_read_b128 a[244:247], v249 offset:6144\n"
      "global_load_dwordx4 v[216:219], v208, s[16:17]\n"
      "v_mfma_f32_16x16x32_bf16 v[112:115], v[148:151], a[112:115], v[112:115]\n"
      "global_load_dwordx4 v[220:223], v210, s[16:17]\n"
      "s_lshr_b32 s55, s34, 1\n"
      "v_mfma_f32_16x16x32_bf16 v[116:119], v[148:151], a[116:119], v[116:119]\n"
      "s_lshl2_add_u32 s55, s55, s35\n"
      "s_lshl_b32 s55, s55, 15\n"
      "s_lshl_b32 s54, s54, 9\n"
      "v_mfma_f32_16x16x32_bf16 v[120:123], v[148:151], a[120:123], v[120:123]\n"
      "s_add_u32 s54, s54, s55\n"
      "s_add_u32 s54, s54, 786432\n"
      "v_lshl_add_u32 v209, v209, 4, s54\n"
      "v_mfma_f32_16x16x32_bf16 v[124:127], v[148:151], a[124:127], v[124:127]\n"
      "s_waitcnt vmcnt(14)\n"
      "global_load_dwordx2 v[204:205], v209, s[58:59] offset:0 sc1\n"
      "global_load_dwordx2 v[206:207], v209, s[58:59] offset:256 sc1\n"
      "v_mfma_f32_16x16x32_bf16 v[96:99], v[152:155], a[128:131], v[96:99]\n"
      "s_add_u32 s68, s20, 16384\n"
      "s_addc_u32 s69, s21, 0\n"
      "v_lshrrev_b32 v208, 10, v208\n"
      "v_mfma_f32_16x16x32_bf16 v[100:103], v[152:155], a[132:135], v[100:103]\n"
      "v_subrev_u32 v208, s42, v208\n"
      "v_mfma_f32_16x16x32_bf16 v[104:107], v[152:155], a[136:139], v[104:107]\n"
      "global_load_dwordx4 v[188:191], v241, s[74:75] offset:-1024 sc1\n"
      "v_mfma_f32_16x16x32_bf16 v[108:111], v[152:155], a[140:143], v[108:111]\n"
      "s_waitcnt vmcnt(16)\n"
      "ds_read_b128 a[216:219], v236 offset:32768\n"
      "v_mfma_f32_16x16x32_bf16 v[112:115], v[152:155], a[144:147], v[112:115]\n"
      "ds_read_b128 a[220:223], v236 offset:34816\n"
      "ds_read_b128 a[248:251], v237 offset:32768\n"
      "v_mfma_f32_16x16x32_bf16 v[116:119], v[152:155], a[148:151], v[116:119]\n"
      "ds_read_b128 a[252:255], v237 offset:34816\n"
      "s_sub_u32 s48, s33, 1\n"
      "v_mfma_f32_16x16x32_bf16 v[120:123], v[152:155], a[152:155], v[120:123]\n"
      "s_and_b32 s48, s48, 3\n"
      "s_lshl_b32 s48, s48, 14\n"
      "v_lshl_add_u32 v244, v243, 2, s48\n"
      "v_mfma_f32_16x16x32_bf16 v[124:127], v[152:155], a[156:159], v[124:127]\n"
      "s_sub_u32 s48, s33, 2\n"
      "s_and_b32 s48, s48, 3\n"
      "s_lshl_b32 s48, s48, 14\n"
      "s_waitcnt lgkmcnt(15)\n"
      "v_mfma_f32_16x16x32_bf16 v[0:3], v[156:159], a[192:195], v[0:3]\n"
      "s_mov_b32 m0, s41\n"
      "v_lshl_add_u32 v245, v243, 2, s48\n"
      "s_sub_u32 s48, s33, 3\n"
      "s_waitcnt lgkmcnt(14)\n"
      "v_mfma_f32_16x16x32_bf16 v[4:7], v[156:159], a[196:199], v[4:7]\n"
      "s_and_b32 s48, s48, 3\n"
      "s_lshl_b32 s48, s48, 14\n"
      "v_lshl_add_u32 v246, v243, 2, s48\n"
      "s_waitcnt lgkmcnt(11)\n"
      "v_mfma_f32_16x16x32_bf16 v[8:11], v[156:159], a[200:203], v[8:11]\n"
      "global_load_dwordx4 v[128:131], v240, s[10:11] offset:0 sc1\n"
      "v_bfe_u32 v248, v243, 1, 3\n"
      "s_waitcnt lgkmcnt(10)\n"
      "v_mfma_f32_16x16x32_bf16 v[12:15], v[156:159], a[204:207], v[12:15]\n"
      "v_lshrrev_b32 v249, 4, v243\n"
      "v_lshlrev_b32 v249, 1, v249\n"
      "s_waitcnt lgkmcnt(7)\n"
      "v_mfma_f32_16x16x32_bf16 v[16:19], v[156:159], a[208:211], v[16:19]\n"
      "v_sub_u32 v248, v248, v249\n"
      "v_and_b32 v249, 16, v242\n"
      "s_waitcnt lgkmcnt(6)\n"
      "v_mfma_f32_16x16x32_bf16 v[20:23], v[156:159], a[212:215], v[20:23]\n"
      "v_mov_b32 v254, 0x3f80\n"
      "v_lshlrev_b32 v249, v249, v254\n"
      "s_waitcnt lgkmcnt(3)\n"
      "v_mfma_f32_16x16x32_bf16 v[24:27], v[156:159], a[216:219], v[24:27]\n"
      "v_cmp_eq_u32 vcc, 0, v248\n"
      "s_nop 1\n"
      "v_cndmask_b32 v250, 0, v249, vcc\n"
      "s_waitcnt lgkmcnt(2)\n"
      "v_mfma_f32_16x16x32_bf16 v[28:31], v[156:159], a[220:223], v[28:31]\n"
      "s_waitcnt vmcnt(11)\n"
      "v_cmp_eq_u32 vcc, 1, v248\n"
      "s_nop 1\n"
      "v_cndmask_b32 v251, 0, v249, vcc\n"
      "v_mfma_f32_16x16x32_bf16 v[0:3], v[160:163], a[224:227], v[0:3]\n"
      "v_mov_b32 v252, v250\n"
      "v_mov_b32 v253, v251\n"
      "v_mfma_f32_16x16x32_bf16 v[4:7], v[160:163], a[228:231], v[4:7]\n"
      "v_mov_b32 v248, 0\n"
      "v_mov_b32 v249, 0\n"
      "v_mfma_f32_16x16x32_bf16 v[8:11], v[160:163], a[232:235], v[8:11]\n"
      "global_load_dwordx4 v[132:135], v240, s[72:73] offset:0 sc1\n"
      "v_mov_b32 v254, 0\n"
      "v_mfma_f32_16x16x32_bf16 v[12:15], v[160:163], a[236:239], v[12:15]\n"
      "v_mov_b32 v255, 0\n"
      "s_add_u32 s48, s34, 1\n"
      "v_mfma_f32_16x16x32_bf16 v[16:19], v[160:163], a[240:243], v[16:19]\n"
      "s_and_b32 s48, s48, 3\n"
      "s_mul_i32 s48, s48, 36864\n"
      "s_add_u32 s48, s48, 0\n"
      "v_mfma_f32_16x16x32_bf16 v[20:23], v[160:163], a[244:247], v[20:23]\n"
      "s_add_u32 s52, s60, s48\n"
      "s_addc_u32 s53, s61, 0\n"
      "s_waitcnt lgkmcnt(1)\n"
      "v_mfma_f32_16x16x32_bf16 v[24:27], v[160:163], a[248:251], v[24:27]\n"
      "v_cvt_pk_bf16_f32 v8, v8, v9\n"
      "v_cvt_pk_bf16_f32 v9, v10, v11\n"
      "s_waitcnt lgkmcnt(0)\n"
      "v_mfma_f32_16x16x32_bf16 v[28:31], v[160:163], a[252:255], v[28:31]\n"
      "ds_write_addtid_b32 v8 offset:0\n"
      "v_cvt_pk_bf16_f32 v10, v12, v13\n"
      "v_mfma_f32_16x16x32_bf16 v[32:35], v[164:167], a[192:195], v[32:35]\n"
      "ds_write_addtid_b32 v9 offset:256\n"
      "v_cvt_pk_bf16_f32 v11, v14, v15\n"
      "v_mfma_f32_16x16x32_bf16 v[36:39], v[164:167], a[196:199], v[36:39]\n"
      "ds_write_addtid_b32 v10 offset:512\n"
      "v_cvt_pk_bf16_f32 v12, v16, v17\n"
      "v_mfma_f32_16x16x32_bf16 v[40:43], v[164:167], a[200:203], v[40:43]\n"
      "global_load_dwordx4 v[136:139], v241, s[10:11] offset:0 sc1\n"
      "ds_write_addtid_b32 v11 offset:768\n"
      "v_mfma_f32_16x16x32_bf16 v[44:47], v[164:167], a[204:207], v[44:47]\n"
      "s_lshr_b32 s43, s42, 5\n"
      "s_lshl2_add_u32 s44, s36, s33\n"
      "s_cmp_eq_u32 s43, s44\n"
      "s_cbranch_scc0 .Lp_e11_%=\n"
      "s_ashr_i32 s43, s40, 31\n"
      "s_not_b32 s43, s43\n"
      "s_or_b32 s43, s43, 0x80000000\n"
      "s_xor_b32 s43, s43, s40\n"
      "s_and_b32 s43, s43, 0xffff0000\n"
      "s_waitcnt vmcnt(13)\n"
      "v_lshlrev_b32 v228, 16, v192\n"
      "s_waitcnt vmcnt(13)\n"
      "v_lshlrev_b32 v229, 16, v196\n"
      "v_and_b32 v192, 0xffff0000, v192\n"
      "v_and_b32 v196, 0xffff0000, v196\n"
      "v_mul_f32 v228, v228, v229\n"
      "v_mul_f32 v230, v192, v196\n"
      "v_cvt_pk_bf16_f32 v228, v228, v230\n"
      "v_lshlrev_b32 v229, 16, v228\n"
      "v_and_b32 v228, 0xffff0000, v228\n"
      "v_add_f32 v231, v228, v229\n"
      "v_lshlrev_b32 v228, 16, v193\n"
      "v_lshlrev_b32 v229, 16, v197\n"
      "v_and_b32 v193, 0xffff0000, v193\n"
      "v_and_b32 v197, 0xffff0000, v197\n"
      "v_mul_f32 v228, v228, v229\n"
      "v_mul_f32 v230, v193, v197\n"
      "v_cvt_pk_bf16_f32 v228, v228, v230\n"
      "v_lshlrev_b32 v229, 16, v228\n"
      "v_and_b32 v228, 0xffff0000, v228\n"
      "v_add_f32 v228, v228, v229\n"
      "v_add_f32 v231, v231, v228\n"
      "v_lshlrev_b32 v228, 16, v194\n"
      "v_lshlrev_b32 v229, 16, v198\n"
      "v_and_b32 v194, 0xffff0000, v194\n"
      "v_and_b32 v198, 0xffff0000, v198\n"
      "v_mul_f32 v228, v228, v229\n"
      "v_mul_f32 v230, v194, v198\n"
      "v_cvt_pk_bf16_f32 v228, v228, v230\n"
      "v_lshlrev_b32 v229, 16, v228\n"
      "v_and_b32 v228, 0xffff0000, v228\n"
      "v_add_f32 v228, v228, v229\n"
      "v_add_f32 v231, v231, v228\n"
      "v_lshlrev_b32 v228, 16, v195\n"
      "v_lshlrev_b32 v229, 16, v199\n"
      "v_and_b32 v195, 0xffff0000, v195\n"
      "v_and_b32 v199, 0xffff0000, v199\n"
      "v_mul_f32 v228, v228, v229\n"
      "v_mul_f32 v230, v195, v199\n"
      "v_cvt_pk_bf16_f32 v228, v228, v230\n"
      "v_lshlrev_b32 v229, 16, v228\n"
      "v_and_b32 v228, 0xffff0000, v228\n"
      "v_add_f32 v228, v228, v229\n"
      "v_add_f32 v231, v231, v228\n"
      "s_nop 1\n"
      "v_add_f32_dpp v231, v231, v231 quad_perm:[1,0,3,2] row_mask:0xf bank_mask:0xf\n"
      "s_nop 1\n"
      "v_add_f32_dpp v231, v231, v231 quad_perm:[2,3,0,1] row_mask:0xf bank_mask:0xf\n"
      "s_nop 1\n"
      "v_add_f32_dpp v231, v231, v231 row_half_mirror row_mask:0xf bank_mask:0xf\n"
      "s_nop 1\n"
      "v_add_f32_dpp v231, v231, v231 row_mirror row_mask:0xf bank_mask:0xf\n"
      "v_mov_b32 v228, v231\n"
      "s_nop 1\n"
      "v_permlane16_swap_b32 v231, v228\n"
      "v_add_f32 v231, v231, v228\n"
      "v_mov_b32 v228, v231\n"
      "s_nop 1\n"
      "v_permlane32_swap_b32 v231, v228\n"
      "v_add_f32 v231, v231, v228\n"
      "v_cvt_pk_bf16_f32 v231, v231, v231\n"
      "v_and_b32 v231, 0xffff0000, v231\n"
      "v_writelane_b32 v231, s43, 1\n"
      "v_mul_f32 v231, 0xbfb8aa3b, v231\n"
      "v_exp_f32 v231, v231\n"
      "s_nop 0\n"
      "v_add_f32 v231, 1.0, v231\n"
      "v_rcp_f32 v231, v231\n"
      "s_nop 0\n"
      "v_cvt_pk_bf16_f32 v231, v231, v231\n"
      "v_and_b32 v231, 0xffff0000, v231\n"
      "s_nop 0\n"
      "v_readfirstlane_b32 s39, v231\n"
      "v_readlane_b32 s84, v231, 1\n"
      ".Lp_e11_%=:\n"
      "v_mfma_f32_16x16x32_bf16 v[48:51], v[164:167], a[208:211], v[48:51]\n"
      "v_lshrrev_b32 v201, 4, v243\n"
      "v_bfe_u32 v224, v243, 2, 2\n"
      "v_mfma_f32_16x16x32_bf16 v[52:55], v[164:167], a[212:215], v[52:55]\n"
      "v_lshl_or_b32 v201, v201, 3, v224\n"
      "v_and_b32 v224, 3, v243\n"
      "v_mfma_f32_16x16x32_bf16 v[56:59], v[164:167], a[216:219], v[56:59]\n"
      "global_load_dwordx4 v[140:143], v241, s[72:73] offset:0 sc1\n"
      "v_lshlrev_b32 v224, 3, v224\n"
      "v_mfma_f32_16x16x32_bf16 v[60:63], v[164:167], a[220:223], v[60:63]\n"
      "s_waitcnt vmcnt(12)\n"
      "v_lshl_or_b32 v201, v201, 7, v224\n"
      "s_mov_b32 s64, 0xffff0000\n"
      "v_mfma_f32_16x16x32_bf16 v[32:35], v[168:171], a[224:227], v[32:35]\n"
      "s_mov_b32 s65, 0xffff0000\n"
      "s_mov_b32 s66, -1\n"
      "s_mov_b32 s67, 0\n"
      "v_mfma_f32_16x16x32_bf16 v[36:39], v[168:171], a[228:231], v[36:39]\n"
      "v_bfe_u32 v224, v243, 4, 1\n"
      "v_and_b32 v203, 0xf0, v242\n"
      "v_mfma_f32_16x16x32_bf16 v[40:43], v[168:171], a[232:235], v[40:43]\n"
      "global_load_dwordx4 v[144:147], v240, s[14:15] offset:0 sc1\n"
      "v_lshl_or_b32 v203, v224, 14, v203\n"
      "v_mfma_f32_16x16x32_bf16 v[44:47], v[168:171], a[236:239], v[44:47]\n"
      "s_lshr_b32 s54, s33, 1\n"
      "s_lshl1_add_u32 s54, s80, s54\n"
      "s_lshl_b32 s54, s54, 12\n"
      "v_mfma_f32_16x16x32_bf16 v[48:51], v[168:171], a[240:243], v[48:51]\n"
      "s_add_u32 s54, s54, 262144\n"
      "v_add_u32 v200, s54, v242\n"
      "s_lshl_b32 s54, s33, 12\n"
      "v_mfma_f32_16x16x32_bf16 v[52:55], v[168:171], a[244:247], v[52:55]\n"
      "s_add_u32 s54, s54, 65536\n"
      "v_add_u32 v201, s54, v201\n"
      "v_and_b32 v202, 15, v243\n"
      "v_mfma_f32_16x16x32_bf16 v[56:59], v[168:171], a[248:251], v[56:59]\n"
      "global_load_dwordx4 v[148:151], v240, s[74:75] offset:0 sc1\n"
      "v_lshrrev_b32 v199, 5, v243\n"
      "v_mfma_f32_16x16x32_bf16 v[60:63], v[168:171], a[252:255], v[60:63]\n"
      "v_lshl_or_b32 v202, v199, 4, v202\n"
      "s_add_u32 s54, s41, 12288\n"
      "v_mfma_f32_16x16x32_bf16 v[64:67], v[172:175], a[192:195], v[64:67]\n"
      "v_lshl_add_u32 v202, v202, 1, s54\n"
      "v_and_b32 v199, 48, v243\n"
      "v_mfma_f32_16x16x32_bf16 v[68:71], v[172:175], a[196:199], v[68:71]\n"
      "v_add_u32 v199, s54, v199\n"
      "s_and_b32 s54, s33, 1\n"
      "v_mfma_f32_16x16x32_bf16 v[72:75], v[172:175], a[200:203], v[72:75]\n"
      "s_lshl1_add_u32 s54, s81, s54\n"
      "global_load_dwordx4 v[152:155], v241, s[14:15] offset:0 sc1\n"
      "s_lshl_b32 s54, s54, 15\n"
      "v_mfma_f32_16x16x32_bf16 v[76:79], v[172:175], a[204:207], v[76:79]\n"
      "s_lshr_b32 s55, s33, 1\n"
      "s_lshl1_add_u32 s55, s80, s55\n"
      "s_lshl_b32 s55, s55, 8\n"
      "v_mfma_f32_16x16x32_bf16 v[80:83], v[172:175], a[208:211], v[80:83]\n"
      "s_add_u32 s54, s54, s55\n"
      "s_add_u32 s54, s54, 524288\n"
      "v_add_u32 v203, s54, v203\n"
      "v_mfma_f32_16x16x32_bf16 v[84:87], v[172:175], a[212:215], v[84:87]\n"
      "v_cvt_pk_bf16_f32 v13, v18, v19\n"
      "ds_write_addtid_b32 v12 offset:1024\n"
      "v_mfma_f32_16x16x32_bf16 v[88:91], v[172:175], a[216:219], v[88:91]\n"
      "global_load_dwordx4 v[156:159], v241, s[74:75] offset:0 sc1\n"
      "v_cvt_pk_bf16_f32 v14, v20, v21\n"
      "v_mfma_f32_16x16x32_bf16 v[92:95], v[172:175], a[220:223], v[92:95]\n"
      "s_waitcnt vmcnt(14)\n"
      "ds_write_addtid_b32 v13 offset:1280\n"
      "v_cvt_pk_bf16_f32 v15, v22, v23\n"
      "v_mfma_f32_16x16x32_bf16 v[64:67], v[176:179], a[224:227], v[64:67]\n"
      "ds_write_addtid_b32 v14 offset:1536\n"
      "v_cvt_pk_bf16_f32 v16, v24, v25\n"
      "v_mfma_f32_16x16x32_bf16 v[68:71], v[176:179], a[228:231], v[68:71]\n"
      "ds_write_addtid_b32 v15 offset:1792\n"
      "v_cvt_pk_bf16_f32 v17, v26, v27\n"
      "v_mfma_f32_16x16x32_bf16 v[72:75], v[176:179], a[232:235], v[72:75]\n"
      "global_load_dwordx4 v[160:163], v240, s[10:11] offset:1024 sc1\n"
      "ds_write_addtid_b32 v16 offset:2048\n"
      "v_mfma_f32_16x16x32_bf16 v[76:79], v[176:179], a[236:239], v[76:79]\n"
      "s_waitcnt vmcnt(10)\n"
      "v_bitop3_b32 v210, v205, v207, s37 bitop3:0x7e\n"
      "v_cmp_ne_u32 vcc, 0, v210\n"
      "s_nop 1\n"
      "s_cbranch_vccnz .Lp_s13_%=\n"
      ".Lp_o14_%=:\n"
      "v_mfma_f32_16x16x32_bf16 v[80:83], v[176:179], a[240:243], v[80:83]\n"
      "v_cvt_pk_bf16_f32 v18, v28, v29\n"
      "v_mov_b32 v209, s39\n"
      "v_mfma_f32_16x16x32_bf16 v[84:87], v[176:179], a[244:247], v[84:87]\n"
      "ds_write_addtid_b32 v17 offset:2304\n"
      "v_cvt_pk_bf16_f32 v19, v30, v31\n"
      "v_mfma_f32_16x16x32_bf16 v[88:91], v[176:179], a[248:251], v[88:91]\n"
      "global_load_dwordx4 v[164:167], v240, s[72:73] offset:1024 sc1\n"
      "v_mfma_f32_16x16x32_bf16 v[92:95], v[176:179], a[252:255], v[92:95]\n"
      "ds_write_addtid_b32 v18 offset:2560\n"
      "v_mfma_f32_16x16x32_bf16 v[96:99], v[180:183], a[160:163], v[96:99]\n"
      "ds_write_addtid_b32 v19 offset:2816\n"
      "v_mfma_f32_16x16x32_bf16 v[100:103], v[180:183], a[164:167], v[100:103]\n"
      "v_cmp_eq_u32 vcc, 0, v208\n"
      "s_nop 1\n"
      "v_cndmask_b32 v204, v204, v209, vcc\n"
      "v_mfma_f32_16x16x32_bf16 v[104:107], v[180:183], a[168:171], v[104:107]\n"
      "global_load_dwordx4 v[168:171], v241, s[10:11] offset:1024 sc1\n"
      "v_mfma_f32_16x16x32_bf16 v[108:111], v[180:183], a[172:175], v[108:111]\n"
      "s_waitcnt lgkmcnt(0)\n"
      "s_barrier\n"
      "v_mfma_f32_16x16x32_bf16 v[112:115], v[180:183], a[176:179], v[112:115]\n"
      "ds_read2st64_b32 v[20:21], v244 offset0:0 offset1:1\n"
      "v_mfma_f32_16x16x32_bf16 v[116:119], v[180:183], a[180:183], v[116:119]\n"
      "ds_read2st64_b32 v[22:23], v245 offset0:4 offset1:5\n"
      "v_mfma_f32_16x16x32_bf16 v[120:123], v[180:183], a[184:187], v[120:123]\n"
      "global_load_dwordx4 v[172:175], v241, s[72:73] offset:1024 sc1\n"
      "v_mfma_f32_16x16x32_bf16 v[124:127], v[180:183], a[188:191], v[124:127]\n"
      "s_waitcnt vmcnt(12)\n"
      "ds_read2st64_b32 v[24:25], v244 offset0:2 offset1:3\n"
      "v_mfma_f32_16x16x32_bf16 v[96:99], v[184:187], a[192:195], v[96:99]\n"
      "ds_read2st64_b32 v[26:27], v245 offset0:6 offset1:7\n"
      "v_mfma_f32_16x16x32_bf16 v[100:103], v[184:187], a[196:199], v[100:103]\n"
      "ds_read2st64_b32 v[28:29], v246 offset0:8 offset1:9\n"
      "v_mfma_f32_16x16x32_bf16 v[104:107], v[184:187], a[200:203], v[104:107]\n"
      "global_load_dwordx4 v[176:179], v240, s[14:15] offset:1024 sc1\n"
      "v_mfma_f32_16x16x32_bf16 v[108:111], v[184:187], a[204:207], v[108:111]\n"
      "ds_read2st64_b32 v[30:31], v246 offset0:10 offset1:11\n"
      "v_cvt_pk_bf16_f32 v40, v40, v41\n"
      "v_mfma_f32_16x16x32_bf16 v[112:115], v[184:187], a[208:211], v[112:115]\n"
      "v_cmp_eq_u32 vcc, -16, v208\n"
      "s_nop 1\n"
      "v_cndmask_b32 v206, v206, v209, vcc\n"
      "v_mfma_f32_16x16x32_bf16 v[116:119], v[184:187], a[212:215], v[116:119]\n"
      "v_cvt_pk_bf16_f32 v41, v42, v43\n"
      "v_sub_f32 v205, 1.0, v204\n"
      "v_mfma_f32_16x16x32_bf16 v[120:123], v[184:187], a[216:219], v[120:123]\n"
      "global_load_dwordx4 v[180:183], v240, s[74:75] offset:1024 sc1\n"
      "v_cvt_pk_bf16_f32 v205, v205, v205\n"
      "v_mfma_f32_16x16x32_bf16 v[124:127], v[184:187], a[220:223], v[124:127]\n"
      "ds_write_addtid_b32 v40 offset:3072\n"
      "v_cvt_pk_bf16_f32 v42, v44, v45\n"
      "v_mfma_f32_16x16x32_bf16 v[96:99], v[188:191], a[224:227], v[96:99]\n"
      "s_waitcnt lgkmcnt(1)\n"
      "v_mfma_f32_16x16x32_bf16 v[0:3], v[250:253], v[20:23], v[0:3]\n"
      "v_and_b32 v205, 0xffff0000, v205\n"
      "v_mfma_f32_16x16x32_bf16 v[100:103], v[188:191], a[228:231], v[100:103]\n"
      "v_mfma_f32_16x16x32_bf16 v[4:7], v[250:253], v[24:27], v[4:7]\n"
      "v_sub_f32 v207, 1.0, v206\n"
      "v_mfma_f32_16x16x32_bf16 v[104:107], v[188:191], a[232:235], v[104:107]\n"
      "global_load_dwordx4 v[184:187], v241, s[14:15] offset:1024 sc1\n"
      "v_cvt_pk_bf16_f32 v207, v207, v207\n"
      "v_mfma_f32_16x16x32_bf16 v[108:111], v[188:191], a[236:239], v[108:111]\n"
      "v_mfma_f32_16x16x32_bf16 v[0:3], v[252:255], v[28:31], v[0:3]\n"
      "v_and_b32 v207, 0xffff0000, v207\n"
      "v_mfma_f32_16x16x32_bf16 v[112:115], v[188:191], a[240:243], v[112:115]\n"
      "v_mfma_f32_16x16x32_bf16 v[4:7], v[248:251], v[28:31], v[4:7]\n"
      "v_mov_b32 v11, s37\n"
      "v_mfma_f32_16x16x32_bf16 v[116:119], v[188:191], a[244:247], v[116:119]\n"
      "ds_write_addtid_b32 v41 offset:3328\n"
      "v_cvt_pk_bf16_f32 v43, v46, v47\n"
      "v_mfma_f32_16x16x32_bf16 v[120:123], v[188:191], a[248:251], v[120:123]\n"
      "ds_write_addtid_b32 v42 offset:3584\n"
      "v_cvt_pk_bf16_f32 v8, v0, v1\n"
      "v_mfma_f32_16x16x32_bf16 v[124:127], v[188:191], a[252:255], v[124:127]\n"
      "s_waitcnt vmcnt(13)\n"
      "global_load_dwordx4 v[188:191], v241, s[74:75] offset:1024 sc1\n"
      "v_cvt_pk_bf16_f32 v9, v2, v3\n"
      "v_cvt_pk_bf16_f32 v10, v4, v5\n"
      "v_cvt_pk_bf16_f32 v247, v6, v7\n"
      "global_store_dwordx4 v242, v[8:11], s[52:53] offset:0 sc1\n"
      "v_mfma_f32_16x16x32_bf16 v[0:3], v[128:131], a[0:3], 0\n"
      "v_cvt_pk_bf16_f32 v44, v48, v49\n"
      "v_lshlrev_b32 v208, 16, v216\n"
      "v_mfma_f32_16x16x32_bf16 v[4:7], v[128:131], a[4:7], 0\n"
      "ds_write_addtid_b32 v43 offset:3840\n"
      "v_cvt_pk_bf16_f32 v45, v50, v51\n"
      "v_mfma_f32_16x16x32_bf16 v[8:11], v[128:131], a[8:11], 0\n"
      "ds_write_addtid_b32 v44 offset:4096\n"
      "v_cvt_pk_bf16_f32 v46, v52, v53\n"
      "v_mfma_f32_16x16x32_bf16 v[12:15], v[128:131], a[12:15], 0\n"
      "ds_write_addtid_b32 v45 offset:4352\n"
      "v_cvt_pk_bf16_f32 v47, v54, v55\n"
      "v_mfma_f32_16x16x32_bf16 v[16:19], v[128:131], a[16:19], 0\n"
      "ds_write_addtid_b32 v46 offset:4608\n"
      "v_cvt_pk_bf16_f32 v48, v56, v57\n"
      "v_mfma_f32_16x16x32_bf16 v[20:23], v[128:131], a[20:23], 0\n"
      "ds_write_addtid_b32 v47 offset:4864\n"
      "v_cvt_pk_bf16_f32 v49, v58, v59\n"
      "v_mfma_f32_16x16x32_bf16 v[24:27], v[128:131], a[24:27], 0\n"
      "ds_write_addtid_b32 v48 offset:5120\n"
      "v_cvt_pk_bf16_f32 v50, v60, v61\n"
      "v_mfma_f32_16x16x32_bf16 v[28:31], v[128:131], a[28:31], 0\n"
      "global_load_dwordx4 v[128:131], v240, s[10:11] offset:2048 sc1\n"
      "v_and_b32 v209, 0xffff0000, v216\n"
      "v_mfma_f32_16x16x32_bf16 v[0:3], v[132:135], a[32:35], v[0:3]\n"
      "ds_write_addtid_b32 v49 offset:5376\n"
      "v_cvt_pk_bf16_f32 v51, v62, v63\n"
      "v_mfma_f32_16x16x32_bf16 v[4:7], v[132:135], a[36:39], v[4:7]\n"
      "ds_write_addtid_b32 v50 offset:5632\n"
      "v_mul_f32 v208, v205, v208\n"
      "v_mfma_f32_16x16x32_bf16 v[8:11], v[132:135], a[40:43], v[8:11]\n"
      "ds_write_addtid_b32 v51 offset:5888\n"
      "v_cvt_pk_bf16_f32 v72, v72, v73\n"
      "v_mfma_f32_16x16x32_bf16 v[12:15], v[132:135], a[44:47], v[12:15]\n"
      "v_cvt_pk_bf16_f32 v73, v74, v75\n"
      "v_mul_f32 v209, v205, v209\n"
      "v_mfma_f32_16x16x32_bf16 v[16:19], v[132:135], a[48:51], v[16:19]\n"
      "ds_write_addtid_b32 v72 offset:6144\n"
      "v_cvt_pk_bf16_f32 v74, v76, v77\n"
      "v_mfma_f32_16x16x32_bf16 v[20:23], v[132:135], a[52:55], v[20:23]\n"
      "ds_write_addtid_b32 v73 offset:6400\n"
      "v_cvt_pk_bf16_f32 v75, v78, v79\n"
      "v_mfma_f32_16x16x32_bf16 v[24:27], v[132:135], a[56:59], v[24:27]\n"
      "ds_write_addtid_b32 v74 offset:6656\n"
      "v_cvt_pk_bf16_f32 v76, v80, v81\n"
      "v_mfma_f32_16x16x32_bf16 v[28:31], v[132:135], a[60:63], v[28:31]\n"
      "s_waitcnt vmcnt(14)\n"
      "global_load_dwordx4 v[132:135], v240, s[72:73] offset:2048 sc1\n"
      "v_lshlrev_b32 v212, 16, v220\n"
      "v_mfma_f32_16x16x32_bf16 v[0:3], v[136:139], a[64:67], v[0:3]\n"
      "ds_write_addtid_b32 v75 offset:6912\n"
      "v_cvt_pk_bf16_f32 v77, v82, v83\n"
      "v_mfma_f32_16x16x32_bf16 v[4:7], v[136:139], a[68:71], v[4:7]\n"
      "ds_write_addtid_b32 v76 offset:7168\n"
      "v_cvt_pk_bf16_f32 v78, v84, v85\n"
      "v_mfma_f32_16x16x32_bf16 v[8:11], v[136:139], a[72:75], v[8:11]\n"
      "ds_write_addtid_b32 v77 offset:7424\n"
      "v_cvt_pk_bf16_f32 v79, v86, v87\n"
      "v_mfma_f32_16x16x32_bf16 v[12:15], v[136:139], a[76:79], v[12:15]\n"
      "ds_write_addtid_b32 v78 offset:7680\n"
      "v_cvt_pk_bf16_f32 v80, v88, v89\n"
      "v_mfma_f32_16x16x32_bf16 v[16:19], v[136:139], a[80:83], v[16:19]\n"
      "ds_write_addtid_b32 v79 offset:7936\n"
      "v_cvt_pk_bf16_f32 v81, v90, v91\n"
      "v_mfma_f32_16x16x32_bf16 v[20:23], v[136:139], a[84:87], v[20:23]\n"
      "ds_write_addtid_b32 v80 offset:8192\n"
      "v_cvt_pk_bf16_f32 v82, v92, v93\n"
      "v_mfma_f32_16x16x32_bf16 v[24:27], v[136:139], a[88:91], v[24:27]\n"
      "ds_write_addtid_b32 v81 offset:8448\n"
      "v_cvt_pk_bf16_f32 v83, v94, v95\n"
      "v_mfma_f32_16x16x32_bf16 v[28:31], v[136:139], a[92:95], v[28:31]\n"
      "global_load_dwordx4 v[136:139], v241, s[10:11] offset:2048 sc1\n"
      "v_and_b32 v213, 0xffff0000, v220\n"
      "v_mfma_f32_16x16x32_bf16 v[0:3], v[140:143], a[96:99], v[0:3]\n"
      "ds_write_addtid_b32 v82 offset:8704\n"
      "v_mul_f32 v212, v207, v212\n"
      "v_mfma_f32_16x16x32_bf16 v[4:7], v[140:143], a[100:103], v[4:7]\n"
      "ds_write_addtid_b32 v83 offset:8960\n"
      "v_cvt_pk_bf16_f32 v104, v104, v105\n"
      "v_mfma_f32_16x16x32_bf16 v[8:11], v[140:143], a[104:107], v[8:11]\n"
      "v_cvt_pk_bf16_f32 v105, v106, v107\n"
      "v_mul_f32 v213, v207, v213\n"
      "v_mfma_f32_16x16x32_bf16 v[12:15], v[140:143], a[108:111], v[12:15]\n"
      "ds_write_addtid_b32 v104 offset:9216\n"
      "v_cvt_pk_bf16_f32 v106, v108, v109\n"
      "v_mfma_f32_16x16x32_bf16 v[16:19], v[140:143], a[112:115], v[16:19]\n"
      "ds_write_addtid_b32 v105 offset:9472\n"
      "v_cvt_pk_bf16_f32 v107, v110, v111\n"
      "v_mfma_f32_16x16x32_bf16 v[20:23], v[140:143], a[116:119], v[20:23]\n"
      "ds_write_addtid_b32 v106 offset:9728\n"
      "v_cvt_pk_bf16_f32 v108, v112, v113\n"
      "v_mfma_f32_16x16x32_bf16 v[24:27], v[140:143], a[120:123], v[24:27]\n"
      "ds_write_addtid_b32 v107 offset:9984\n"
      "v_cvt_pk_bf16_f32 v109, v114, v115\n"
      "v_mfma_f32_16x16x32_bf16 v[28:31], v[140:143], a[124:127], v[28:31]\n"
      "s_waitcnt vmcnt(14)\n"
      "global_load_dwordx4 v[140:143], v241, s[72:73] offset:2048 sc1\n"
      "v_lshlrev_b32 v210, 16, v217\n"
      "v_mfma_f32_16x16x32_bf16 v[0:3], v[144:147], a[128:131], v[0:3]\n"
      "ds_write_addtid_b32 v108 offset:10240\n"
      "v_cvt_pk_bf16_f32 v110, v116, v117\n"
      "v_mfma_f32_16x16x32_bf16 v[4:7], v[144:147], a[132:135], v[4:7]\n"
      "ds_write_addtid_b32 v109 offset:10496\n"
      "v_cvt_pk_bf16_f32 v111, v118, v119\n"
      "v_mfma_f32_16x16x32_bf16 v[8:11], v[144:147], a[136:139], v[8:11]\n"
      "global_load_dwordx4 v[224:227], v200, s[58:59] offset:0 sc1\n"
      "v_and_b32 v211, 0xffff0000, v217\n"
      "v_mfma_f32_16x16x32_bf16 v[12:15], v[144:147], a[140:143], v[12:15]\n"
      "global_load_dwordx4 v[228:231], v200, s[58:59] offset:1024 sc1\n"
      "v_mul_f32 v210, v205, v210\n"
      "v_mfma_f32_16x16x32_bf16 v[16:19], v[144:147], a[144:147], v[16:19]\n"
      "global_load_dwordx4 v[232:235], v200, s[58:59] offset:2048 sc1\n"
      "v_mul_f32 v211, v205, v211\n"
      "v_mfma_f32_16x16x32_bf16 v[20:23], v[144:147], a[148:151], v[20:23]\n"
      "global_load_dwordx4 v[236:239], v200, s[58:59] offset:3072 sc1\n"
      "v_lshlrev_b32 v214, 16, v221\n"
      "v_mfma_f32_16x16x32_bf16 v[24:27], v[144:147], a[152:155], v[24:27]\n"
      "ds_write_addtid_b32 v110 offset:10752\n"
      "v_cvt_pk_bf16_f32 v112, v120, v121\n"
      "v_mfma_f32_16x16x32_bf16 v[28:31], v[144:147], a[156:159], v[28:31]\n"
      "global_load_dwordx4 v[144:147], v240, s[14:15] offset:2048 sc1\n"
      "v_and_b32 v215, 0xffff0000, v221\n"
      "v_mfma_f32_16x16x32_bf16 v[0:3], v[148:151], a[160:163], v[0:3]\n"
      "ds_write_addtid_b32 v111 offset:11008\n"
      "v_cvt_pk_bf16_f32 v113, v122, v123\n"
      "v_mfma_f32_16x16x32_bf16 v[4:7], v[148:151], a[164:167], v[4:7]\n"
      "ds_write_addtid_b32 v112 offset:11264\n"
      "v_cvt_pk_bf16_f32 v114, v124, v125\n"
      "v_mfma_f32_16x16x32_bf16 v[8:11], v[148:151], a[168:171], v[8:11]\n"
      "ds_write_addtid_b32 v113 offset:11520\n"
      "v_cvt_pk_bf16_f32 v115, v126, v127\n"
      "v_mfma_f32_16x16x32_bf16 v[12:15], v[148:151], a[172:175], v[12:15]\n"
      "ds_write_addtid_b32 v114 offset:11776\n"
      "v_mul_f32 v214, v207, v214\n"
      "v_mfma_f32_16x16x32_bf16 v[16:19], v[148:151], a[176:179], v[16:19]\n"
      "ds_write_addtid_b32 v115 offset:12032\n"
      "v_mul_f32 v215, v207, v215\n"
      "v_mfma_f32_16x16x32_bf16 v[20:23], v[148:151], a[180:183], v[20:23]\n"
      "v_lshlrev_b32 v216, 16, v218\n"
      "v_and_b32 v217, 0xffff0000, v218\n"
      "v_mfma_f32_16x16x32_bf16 v[24:27], v[148:151], a[184:187], v[24:27]\n"
      "v_mul_f32 v216, v205, v216\n"
      "v_mul_f32 v217, v205, v217\n"
      "v_mfma_f32_16x16x32_bf16 v[28:31], v[148:151], a[188:191], v[28:31]\n"
      "s_waitcnt vmcnt(18)\n"
      "global_load_dwordx4 v[148:151], v240, s[74:75] offset:2048 sc1\n"
      "v_lshlrev_b32 v220, 16, v222\n"
      "v_mfma_f32_16x16x32_bf16 v[0:3], v[152:155], a[192:195], v[0:3]\n"
      "s_waitcnt lgkmcnt(0)\n"
      "s_barrier\n"
      "v_mfma_f32_16x16x32_bf16 v[4:7], v[152:155], a[196:199], v[4:7]\n"
      "ds_read2st64_b32 v[52:53], v244 offset0:12 offset1:13\n"
      "v_and_b32 v221, 0xffff0000, v222\n"
      "v_mfma_f32_16x16x32_bf16 v[8:11], v[152:155], a[200:203], v[8:11]\n"
      "ds_read2st64_b32 v[54:55], v245 offset0:16 offset1:17\n"
      "v_mul_f32 v220, v207, v220\n"
      "v_mfma_f32_16x16x32_bf16 v[12:15], v[152:155], a[204:207], v[12:15]\n"
      "ds_read2st64_b32 v[56:57], v244 offset0:14 offset1:15\n"
      "v_mul_f32 v221, v207, v221\n"
      "v_mfma_f32_16x16x32_bf16 v[16:19], v[152:155], a[208:211], v[16:19]\n"
      "ds_read2st64_b32 v[58:59], v245 offset0:18 offset1:19\n"
      "v_lshlrev_b32 v218, 16, v219\n"
      "v_mfma_f32_16x16x32_bf16 v[20:23], v[152:155], a[212:215], v[20:23]\n"
      "ds_read2st64_b32 v[60:61], v246 offset0:20 offset1:21\n"
      "v_and_b32 v219, 0xffff0000, v219\n"
      "v_mfma_f32_16x16x32_bf16 v[24:27], v[152:155], a[216:219], v[24:27]\n"
      "ds_read2st64_b32 v[62:63], v246 offset0:22 offset1:23\n"
      "v_mul_f32 v218, v205, v218\n"
      "v_mfma_f32_16x16x32_bf16 v[28:31], v[152:155], a[220:223], v[28:31]\n"
      "global_load_dwordx4 v[152:155], v241, s[14:15] offset:2048 sc1\n"
      "v_mul_f32 v219, v205, v219\n"
      "v_mfma_f32_16x16x32_bf16 v[0:3], v[156:159], a[224:227], v[0:3]\n"
      "ds_read2st64_b32 v[84:85], v244 offset0:24 offset1:25\n"
      "v_lshlrev_b32 v222, 16, v223\n"
      "v_mfma_f32_16x16x32_bf16 v[4:7], v[156:159], a[228:231], v[4:7]\n"
      "ds_read2st64_b32 v[86:87], v245 offset0:28 offset1:29\n"
      "v_and_b32 v223, 0xffff0000, v223\n"
      "v_mfma_f32_16x16x32_bf16 v[8:11], v[156:159], a[232:235], v[8:11]\n"
      "ds_read2st64_b32 v[88:89], v244 offset0:26 offset1:27\n"
      "v_mul_f32 v222, v207, v222\n"
      "v_mfma_f32_16x16x32_bf16 v[12:15], v[156:159], a[236:239], v[12:15]\n"
      "s_waitcnt lgkmcnt(3)\n"
      "v_mfma_f32_16x16x32_bf16 v[32:35], v[250:253], v[52:55], v[32:35]\n"
      "v_mul_f32 v223, v207, v223\n"
      "v_mfma_f32_16x16x32_bf16 v[16:19], v[156:159], a[240:243], v[16:19]\n"
      "v_mfma_f32_16x16x32_bf16 v[36:39], v[250:253], v[56:59], v[36:39]\n"
      "v_mfma_f32_16x16x32_bf16 v[20:23], v[156:159], a[244:247], v[20:23]\n"
      "v_mfma_f32_16x16x32_bf16 v[32:35], v[252:255], v[60:63], v[32:35]\n"
      "v_mfma_f32_16x16x32_bf16 v[24:27], v[156:159], a[248:251], v[24:27]\n"
      "v_mfma_f32_16x16x32_bf16 v[36:39], v[248:251], v[60:63], v[36:39]\n"
      "v_mov_b32 v43, s37\n"
      "v_mfma_f32_16x16x32_bf16 v[28:31], v[156:159], a[252:255], v[28:31]\n"
      "s_waitcnt vmcnt(18)\n"
      "global_load_dwordx4 v[156:159], v241, s[74:75] offset:2048 sc1\n"
      "v_mov_b32 v44, v247\n"
      "v_mov_b32 v47, s37\n"
      "v_cvt_pk_bf16_f32 v40, v32, v33\n"
      "v_cvt_pk_bf16_f32 v41, v34, v35\n"
      "v_cvt_pk_bf16_f32 v42, v36, v37\n"
      "v_cvt_pk_bf16_f32 v45, v38, v39\n"
      "global_store_dwordx4 v242, v[40:43], s[52:53] offset:1024 sc1\n"
      "global_store_dwordx4 v242, v[44:47], s[52:53] offset:2048 sc1\n"
      "v_mfma_f32_16x16x32_bf16 v[32:35], v[160:163], a[0:3], 0\n"
      "s_add_u32 s48, s34, 2\n"
      "ds_read2st64_b32 v[90:91], v245 offset0:30 offset1:31\n"
      "s_and_b32 s48, s48, 3\n"
      "v_mfma_f32_16x16x32_bf16 v[36:39], v[160:163], a[4:7], 0\n"
      "s_mul_i32 s48, s48, 36864\n"
      "ds_read2st64_b32 v[92:93], v246 offset0:32 offset1:33\n"
      "s_add_u32 s48, s48, 12288\n"
      "v_mfma_f32_16x16x32_bf16 v[40:43], v[160:163], a[8:11], 0\n"
      "s_add_u32 s52, s60, s48\n"
      "s_addc_u32 s53, s61, 0\n"
      "ds_read2st64_b32 v[94:95], v246 offset0:34 offset1:35\n"
      "v_mfma_f32_16x16x32_bf16 v[44:47], v[160:163], a[12:15], 0\n"
      "ds_read2st64_b32 v[116:117], v244 offset0:36 offset1:37\n"
      "v_mfma_f32_16x16x32_bf16 v[48:51], v[160:163], a[16:19], 0\n"
      "ds_read2st64_b32 v[118:119], v245 offset0:40 offset1:41\n"
      "v_mfma_f32_16x16x32_bf16 v[52:55], v[160:163], a[20:23], 0\n"
      "ds_read2st64_b32 v[120:121], v244 offset0:38 offset1:39\n"
      "v_mfma_f32_16x16x32_bf16 v[56:59], v[160:163], a[24:27], 0\n"
      "ds_read2st64_b32 v[122:123], v245 offset0:42 offset1:43\n"
      "v_mfma_f32_16x16x32_bf16 v[60:63], v[160:163], a[28:31], 0\n"
      "s_waitcnt lgkmcnt(4)\n"
      "global_load_dwordx4 v[160:163], v240, s[10:11] offset:3072 sc1\n"
      "v_mfma_f32_16x16x32_bf16 v[32:35], v[164:167], a[32:35], v[32:35]\n"
      "v_mfma_f32_16x16x32_bf16 v[64:67], v[250:253], v[84:87], v[64:67]\n"
      "v_mfma_f32_16x16x32_bf16 v[36:39], v[164:167], a[36:39], v[36:39]\n"
      "v_mfma_f32_16x16x32_bf16 v[68:71], v[250:253], v[88:91], v[68:71]\n"
      "v_mfma_f32_16x16x32_bf16 v[40:43], v[164:167], a[40:43], v[40:43]\n"
      "v_mfma_f32_16x16x32_bf16 v[64:67], v[252:255], v[92:95], v[64:67]\n"
      "v_mfma_f32_16x16x32_bf16 v[44:47], v[164:167], a[44:47], v[44:47]\n"
      "v_mfma_f32_16x16x32_bf16 v[68:71], v[248:251], v[92:95], v[68:71]\n"
      "v_mov_b32 v75, s37\n"
      "v_mfma_f32_16x16x32_bf16 v[48:51], v[164:167], a[48:51], v[48:51]\n"
      "ds_read2st64_b32 v[124:125], v246 offset0:44 offset1:45\n"
      "v_mfma_f32_16x16x32_bf16 v[52:55], v[164:167], a[52:55], v[52:55]\n"
      "s_waitcnt vmcnt(7)\n"
      "v_bitop3_b32 v192, v225, v227, s37 bitop3:0x7e\n"
      "v_bitop3_b32 v192, v192, v229, s37 bitop3:0xf6\n"
      "v_bitop3_b32 v192, v192, v231, s37 bitop3:0xf6\n"
      "v_bitop3_b32 v192, v192, v233, s37 bitop3:0xf6\n"
      "v_bitop3_b32 v192, v192, v235, s37 bitop3:0xf6\n"
      "v_bitop3_b32 v192, v192, v237, s37 bitop3:0xf6\n"
      "v_bitop3_b32 v192, v192, v239, s37 bitop3:0xf6\n"
      "v_cmp_ne_u32 vcc, 0, v192\n"
      "s_nop 1\n"
      "s_cbranch_vccnz .Lp_s16_%=\n"
      ".Lp_o17_%=:\n"
      "v_mfma_f32_16x16x32_bf16 v[56:59], v[164:167], a[56:59], v[56:59]\n"
      "ds_read2st64_b32 v[126:127], v246 offset0:46 offset1:47\n"
      "v_add_f32 v192, v224, v228\n"
      "v_mfma_f32_16x16x32_bf16 v[60:63], v[164:167], a[60:63], v[60:63]\n"
      "global_load_dwordx4 v[164:167], v240, s[72:73] offset:3072 sc1\n"
      "v_add_f32 v193, v226, v230\n"
      "v_mfma_f32_16x16x32_bf16 v[32:35], v[168:171], a[64:67], v[32:35]\n"
      "v_add_f32 v194, v232, v236\n"
      "v_add_f32 v195, v234, v238\n"
      "v_mfma_f32_16x16x32_bf16 v[36:39], v[168:171], a[68:71], v[36:39]\n"
      "ds_read_b64_tr_b16 v[224:225], v201 offset:0\n"
      "v_permlane32_swap_b32 v192, v193\n"
      "v_mfma_f32_16x16x32_bf16 v[40:43], v[168:171], a[72:75], v[40:43]\n"
      "ds_read_b64_tr_b16 v[226:227], v201 offset:512\n"
      "v_permlane32_swap_b32 v194, v195\n"
      "v_mfma_f32_16x16x32_bf16 v[44:47], v[168:171], a[76:79], v[44:47]\n"
      "ds_read_b64_tr_b16 v[228:229], v201 offset:32\n"
      "v_add_f32 v192, v192, v193\n"
      "v_mfma_f32_16x16x32_bf16 v[48:51], v[168:171], a[80:83], v[48:51]\n"
      "ds_read_b64_tr_b16 v[230:231], v201 offset:544\n"
      "v_add_f32 v194, v194, v195\n"
      "v_mfma_f32_16x16x32_bf16 v[52:55], v[168:171], a[84:87], v[52:55]\n"
      "ds_read_b64_tr_b16 v[232:233], v201 offset:64\n"
      "v_mov_b32 v193, v192\n"
      "v_mfma_f32_16x16x32_bf16 v[56:59], v[168:171], a[88:91], v[56:59]\n"
      "ds_read_b64_tr_b16 v[234:235], v201 offset:576\n"
      "v_mov_b32 v195, v194\n"
      "v_mfma_f32_16x16x32_bf16 v[60:63], v[168:171], a[92:95], v[60:63]\n"
      "global_load_dwordx4 v[168:171], v241, s[10:11] offset:3072 sc1\n"
      "v_cvt_pk_bf16_f32 v72, v64, v65\n"
      "v_mfma_f32_16x16x32_bf16 v[32:35], v[172:175], a[96:99], v[32:35]\n"
      "ds_read_b64_tr_b16 v[236:237], v201 offset:96\n"
      "v_permlane16_swap_b32 v192, v193\n"
      "v_mfma_f32_16x16x32_bf16 v[36:39], v[172:175], a[100:103], v[36:39]\n"
      "ds_read_b64_tr_b16 v[238:239], v201 offset:608\n"
      "v_permlane16_swap_b32 v194, v195\n"
      "v_mfma_f32_16x16x32_bf16 v[40:43], v[172:175], a[104:107], v[40:43]\n"
      "v_add_f32 v192, v192, v193\n"
      "v_add_f32 v194, v194, v195\n"
      "v_mfma_f32_16x16x32_bf16 v[44:47], v[172:175], a[108:111], v[44:47]\n"
      "v_cvt_pk_bf16_f32 v192, v192, v194\n"
      "v_and_b32 v194, 0xffff0000, v192\n"
      "v_mfma_f32_16x16x32_bf16 v[48:51], v[172:175], a[112:115], v[48:51]\n"
      "v_lshlrev_b32 v192, 16, v192\n"
      "v_mul_f32 v193, 0xbfb8aa3b, v192\n"
      "v_mfma_f32_16x16x32_bf16 v[52:55], v[172:175], a[116:119], v[52:55]\n"
      "v_exp_f32 v193, v193\n"
      "s_nop 0\n"
      "v_add_f32 v193, 1.0, v193\n"
      "v_mfma_f32_16x16x32_bf16 v[56:59], v[172:175], a[120:123], v[56:59]\n"
      "v_rcp_f32 v193, v193\n"
      "s_nop 0\n"
      "v_mul_f32 v192, v192, v193\n"
      "v_mfma_f32_16x16x32_bf16 v[60:63], v[172:175], a[124:127], v[60:63]\n"
      "global_load_dwordx4 v[172:175], v241, s[72:73] offset:3072 sc1\n"
      "v_cvt_pk_bf16_f32 v192, v192, v192\n"
      "v_mfma_f32_16x16x32_bf16 v[40:43], v[176:179], a[136:139], v[40:43]\n"
      "v_and_b32 v192, 0xffff0000, v192\n"
      "v_mul_f32 v192, v192, v194\n"
      "v_mfma_f32_16x16x32_bf16 v[44:47], v[176:179], a[140:143], v[44:47]\n"
      "v_cvt_pk_bf16_f32 v192, v192, v192\n"
      "v_cvt_pk_bf16_f32 v73, v66, v67\n"
      "v_mfma_f32_16x16x32_bf16 v[48:51], v[176:179], a[144:147], v[48:51]\n"
      "ds_write_b16 v202, v192\n"
      "v_cvt_pk_bf16_f32 v74, v68, v69\n"
      "v_mfma_f32_16x16x32_bf16 v[52:55], v[176:179], a[148:151], v[52:55]\n"
      "ds_read_b128 v[192:195], v199\n"
      "v_cvt_pk_bf16_f32 v247, v70, v71\n"
      "v_mfma_f32_16x16x32_bf16 v[56:59], v[176:179], a[152:155], v[56:59]\n"
      "global_store_dwordx4 v242, v[72:75], s[52:53] offset:0 sc1\n"
      "s_waitcnt lgkmcnt(10)\n"
      "v_mfma_f32_16x16x32_bf16 v[60:63], v[176:179], a[156:159], v[60:63]\n"
      "v_mfma_f32_16x16x32_bf16 v[96:99], v[250:253], v[116:119], v[96:99]\n"
      "v_cvt_pk_bf16_f32 v8, v8, v9\n"
      "v_mfma_f32_16x16x32_bf16 v[40:43], v[180:183], a[168:171], v[40:43]\n"
      "v_mfma_f32_16x16x32_bf16 v[100:103], v[250:253], v[120:123], v[100:103]\n"
      "v_cvt_pk_bf16_f32 v9, v10, v11\n"
      "v_mfma_f32_16x16x32_bf16 v[44:47], v[180:183], a[172:175], v[44:47]\n"
      "v_mfma_f32_16x16x32_bf16 v[96:99], v[252:255], v[124:127], v[96:99]\n"
      "v_mfma_f32_16x16x32_bf16 v[48:51], v[180:183], a[176:179], v[48:51]\n"
      "v_mfma_f32_16x16x32_bf16 v[100:103], v[248:251], v[124:127], v[100:103]\n"
      "v_mov_b32 v107, s37\n"
      "v_mfma_f32_16x16x32_bf16 v[52:55], v[180:183], a[180:183], v[52:55]\n"
      "s_waitcnt lgkmcnt(0)\n"
      "v_mfma_f32_16x16x32_bf16 v[196:199], v[192:195], v[224:227], 0\n"
      "v_mfma_f32_16x16x32_bf16 v[56:59], v[180:183], a[184:187], v[56:59]\n"
      "v_mfma_f32_16x16x32_bf16 v[224:227], v[192:195], v[228:231], 0\n"
      "v_mov_b32 v108, v247\n"
      "v_mfma_f32_16x16x32_bf16 v[60:63], v[180:183], a[188:191], v[60:63]\n"
      "v_mfma_f32_16x16x32_bf16 v[228:231], v[192:195], v[232:235], 0\n"
      "v_mov_b32 v111, s37\n"
      "v_mfma_f32_16x16x32_bf16 v[40:43], v[184:187], a[200:203], v[40:43]\n"
      "v_mfma_f32_16x16x32_bf16 v[232:235], v[192:195], v[236:239], 0\n"
      "v_mov_b32 v193, s37\n"
      "v_mfma_f32_16x16x32_bf16 v[44:47], v[184:187], a[204:207], v[44:47]\n"
      "ds_write_addtid_b32 v8 offset:0\n"
      "v_mov_b32 v195, s37\n"
      "v_cvt_pk_bf16_f32 v104, v96, v97\n"
      "v_mfma_f32_16x16x32_bf16 v[48:51], v[184:187], a[208:211], v[48:51]\n"
      "v_cvt_pk_bf16_f32 v105, v98, v99\n"
      "v_cvt_pk_bf16_f32 v106, v100, v101\n"
      "v_cvt_pk_bf16_f32 v109, v102, v103\n"
      "v_mfma_f32_16x16x32_bf16 v[52:55], v[184:187], a[212:215], v[52:55]\n"
      "global_store_dwordx4 v242, v[104:107], s[52:53] offset:1024 sc1\n"
      "global_store_dwordx4 v242, v[108:111], s[52:53] offset:2048 sc1\n"
      "v_cndmask_b32 v192, v196, v228, s[64:65]\n"
      "v_mfma_f32_16x16x32_bf16 v[56:59], v[184:187], a[216:219], v[56:59]\n"
      "s_add_u32 s48, s34, 3\n"
      "v_cndmask_b32 v194, v224, v232, s[64:65]\n"
      "v_and_b32 v192, s86, v192\n"
      "v_and_b32 v194, s86, v194\n"
      "v_mfma_f32_16x16x32_bf16 v[60:63], v[184:187], a[220:223], v[60:63]\n"
      "s_and_b32 s48, s48, 3\n"
      "s_mov_b64 exec, s[66:67]\n"
      "global_store_dwordx4 v203, v[192:195], s[58:59] sc1\n"
      "s_mov_b64 exec, -1\n"
      "v_mfma_f32_16x16x32_bf16 v[40:43], v[188:191], a[232:235], v[40:43]\n"
      "s_mul_i32 s48, s48, 36864\n"
      "s_add_u32 s48, s48, 24576\n"
      "s_add_u32 s52, s60, s48\n"
      "s_addc_u32 s53, s61, 0\n"
      "v_mfma_f32_16x16x32_bf16 v[44:47], v[188:191], a[236:239], v[44:47]\n"
      "v_cvt_pk_bf16_f32 v10, v12, v13\n"
      "v_mfma_f32_16x16x32_bf16 v[48:51], v[188:191], a[240:243], v[48:51]\n"
      "ds_write_addtid_b32 v9 offset:256\n"
      "v_cvt_pk_bf16_f32 v11, v14, v15\n"
      "v_mfma_f32_16x16x32_bf16 v[52:55], v[188:191], a[244:247], v[52:55]\n"
      "ds_write_addtid_b32 v10 offset:512\n"
      "v_cvt_pk_bf16_f32 v12, v16, v17\n"
      "v_mfma_f32_16x16x32_bf16 v[56:59], v[188:191], a[248:251], v[56:59]\n"
      "ds_write_addtid_b32 v11 offset:768\n"
      "v_cvt_pk_bf16_f32 v13, v18, v19\n"
      "v_mfma_f32_16x16x32_bf16 v[60:63], v[188:191], a[252:255], v[60:63]\n"
      "ds_write_addtid_b32 v12 offset:1024\n"
      "v_cvt_pk_bf16_f32 v14, v20, v21\n"
      "v_mfma_f32_16x16x32_bf16 v[32:35], v[176:179], a[128:131], v[32:35]\n"
      "ds_write_addtid_b32 v13 offset:1280\n"
      "v_cvt_pk_bf16_f32 v15, v22, v23\n"
      "v_mfma_f32_16x16x32_bf16 v[36:39], v[176:179], a[132:135], v[36:39]\n"
      "global_load_dwordx4 v[176:179], v240, s[14:15] offset:3072 sc1\n"
      "ds_write_addtid_b32 v14 offset:1536\n"
      "v_cvt_pk_bf16_f32 v16, v24, v25\n"
      "v_mfma_f32_16x16x32_bf16 v[32:35], v[180:183], a[160:163], v[32:35]\n"
      "ds_write_addtid_b32 v15 offset:1792\n"
      "v_cvt_pk_bf16_f32 v17, v26, v27\n"
      "v_mfma_f32_16x16x32_bf16 v[36:39], v[180:183], a[164:167], v[36:39]\n"
      "ds_write_addtid_b32 v16 offset:2048\n"
      "v_cvt_pk_bf16_f32 v18, v28, v29\n"
      "v_mfma_f32_16x16x32_bf16 v[32:35], v[184:187], a[192:195], v[32:35]\n"
      "ds_write_addtid_b32 v17 offset:2304\n"
      "v_cvt_pk_bf16_f32 v19, v30, v31\n"
      "v_mfma_f32_16x16x32_bf16 v[36:39], v[184:187], a[196:199], v[36:39]\n"
      "global_load_dwordx4 v[180:183], v240, s[74:75] offset:3072 sc1\n"
      "ds_write_addtid_b32 v18 offset:2560\n"
      "v_mfma_f32_16x16x32_bf16 v[32:35], v[188:191], a[224:227], v[32:35]\n"
      "ds_write_addtid_b32 v19 offset:2816\n"
      "v_mfma_f32_16x16x32_bf16 v[36:39], v[188:191], a[228:231], v[36:39]\n"
      "v_mfma_f32_16x16x32_bf16 v[72:75], v[128:131], a[8:11], 0\n"
      "v_mfma_f32_16x16x32_bf16 v[76:79], v[128:131], a[12:15], 0\n"
      "global_load_dwordx4 v[184:187], v241, s[14:15] offset:3072 sc1\n"
      "s_waitcnt lgkmcnt(0)\n"
      "s_barrier\n"
      "v_mfma_f32_16x16x32_bf16 v[80:83], v[128:131], a[16:19], 0\n"
      "ds_read2st64_b32 v[20:21], v244 offset0:0 offset1:1\n"
      "ds_read2st64_b32 v[22:23], v245 offset0:4 offset1:5\n"
      "v_mfma_f32_16x16x32_bf16 v[84:87], v[128:131], a[20:23], 0\n"
      "ds_read2st64_b32 v[24:25], v244 offset0:2 offset1:3\n"
      "ds_read2st64_b32 v[26:27], v245 offset0:6 offset1:7\n"
      "v_mfma_f32_16x16x32_bf16 v[88:91], v[128:131], a[24:27], 0\n"
      "ds_read2st64_b32 v[28:29], v246 offset0:8 offset1:9\n"
      "ds_read2st64_b32 v[30:31], v246 offset0:10 offset1:11\n"
      "v_cvt_pk_bf16_f32 v40, v40, v41\n"
      "v_mfma_f32_16x16x32_bf16 v[92:95], v[128:131], a[28:31], 0\n"
      "global_load_dwordx4 v[188:191], v241, s[74:75] offset:3072 sc1\n"
      "v_cvt_pk_bf16_f32 v41, v42, v43\n"
      "v_mfma_f32_16x16x32_bf16 v[72:75], v[132:135], a[40:43], v[72:75]\n"
      "ds_write_addtid_b32 v40 offset:3072\n"
      "v_cvt_pk_bf16_f32 v42, v44, v45\n"
      "v_mfma_f32_16x16x32_bf16 v[76:79], v[132:135], a[44:47], v[76:79]\n"
      "ds_write_addtid_b32 v41 offset:3328\n"
      "v_cvt_pk_bf16_f32 v43, v46, v47\n"
      "v_mfma_f32_16x16x32_bf16 v[80:83], v[132:135], a[48:51], v[80:83]\n"
      "ds_write_addtid_b32 v42 offset:3584\n"
      "v_cvt_pk_bf16_f32 v44, v48, v49\n"
      "v_mfma_f32_16x16x32_bf16 v[84:87], v[132:135], a[52:55], v[84:87]\n"
      "s_waitcnt lgkmcnt(3)\n"
      "v_mfma_f32_16x16x32_bf16 v[0:3], v[250:253], v[20:23], v[0:3]\n"
      "v_mfma_f32_16x16x32_bf16 v[4:7], v[250:253], v[24:27], v[4:7]\n"
      "v_mfma_f32_16x16x32_bf16 v[88:91], v[132:135], a[56:59], v[88:91]\n"
      "v_mfma_f32_16x16x32_bf16 v[0:3], v[252:255], v[28:31], v[0:3]\n"
      "v_mfma_f32_16x16x32_bf16 v[4:7], v[248:251], v[28:31], v[4:7]\n"
      "v_mov_b32 v11, s37\n"
      "v_mfma_f32_16x16x32_bf16 v[92:95], v[132:135], a[60:63], v[92:95]\n"
      "ds_write_addtid_b32 v43 offset:3840\n"
      "v_cvt_pk_bf16_f32 v45, v50, v51\n"
      "v_mfma_f32_16x16x32_bf16 v[72:75], v[136:139], a[72:75], v[72:75]\n"
      "ds_write_addtid_b32 v44 offset:4096\n"
      "s_nop 0\n"
      "v_cvt_pk_bf16_f32 v8, v0, v1\n"
      "v_mfma_f32_16x16x32_bf16 v[76:79], v[136:139], a[76:79], v[76:79]\n"
      "v_cvt_pk_bf16_f32 v9, v2, v3\n"
      "v_cvt_pk_bf16_f32 v10, v4, v5\n"
      "v_cvt_pk_bf16_f32 v247, v6, v7\n"
      "v_mfma_f32_16x16x32_bf16 v[80:83], v[136:139], a[80:83], v[80:83]\n"
      "global_store_dwordx4 v242, v[8:11], s[52:53] offset:0 sc1\n"
      "v_cvt_pk_bf16_f32 v46, v52, v53\n"
      "s_lshl2_add_u32 s54, s36, s33\n"
      "v_mfma_f32_16x16x32_bf16 v[84:87], v[136:139], a[84:87], v[84:87]\n"
      "s_lshl_b32 s55, s54, 15\n"
      "ds_write_addtid_b32 v45 offset:4352\n"
      "v_cvt_pk_bf16_f32 v47, v54, v55\n"
      "s_lshl2_add_u32 s56, s35, s34\n"
      "v_mfma_f32_16x16x32_bf16 v[88:91], v[136:139], a[88:91], v[88:91]\n"
      "s_lshl_b32 s56, s56, 6\n"
      "ds_write_addtid_b32 v46 offset:4608\n"
      "v_add_u32 v4, 0, v242\n"
      "v_cvt_pk_bf16_f32 v48, v56, v57\n"
      "v_mfma_f32_16x16x32_bf16 v[92:95], v[136:139], a[92:95], v[92:95]\n"
      "s_add_u32 s55, s55, s56\n"
      "global_load_dwordx4 v[12:15], v4, s[62:63] offset:0 sc1\n"
      "global_load_dwordx4 v[16:19], v4, s[62:63] offset:1024 sc1\n"
      "v_and_b32 v8, 15, v243\n"
      "v_mfma_f32_16x16x32_bf16 v[72:75], v[140:143], a[104:107], v[72:75]\n"
      "global_load_dwordx4 v[20:23], v4, s[62:63] offset:2048 sc1\n"
      "ds_write_addtid_b32 v47 offset:4864\n"
      "v_cvt_pk_bf16_f32 v49, v58, v59\n"
      "v_mfma_f32_16x16x32_bf16 v[76:79], v[140:143], a[108:111], v[76:79]\n"
      "ds_write_addtid_b32 v48 offset:5120\n"
      "v_cvt_pk_bf16_f32 v50, v60, v61\n"
      "v_and_b32 v7, 48, v243\n"
      "v_mfma_f32_16x16x32_bf16 v[80:83], v[140:143], a[112:115], v[80:83]\n"
      "ds_write_addtid_b32 v49 offset:5376\n"
      "v_cvt_pk_bf16_f32 v51, v62, v63\n"
      "v_lshl_or_b32 v7, v8, 10, v7\n"
      "v_mfma_f32_16x16x32_bf16 v[84:87], v[140:143], a[116:119], v[84:87]\n"
      "ds_write_addtid_b32 v50 offset:5632\n"
      "ds_write_addtid_b32 v51 offset:5888\n"
      "v_add_u32 v7, s55, v7\n"
      "v_mfma_f32_16x16x32_bf16 v[88:91], v[140:143], a[120:123], v[88:91]\n"
      "v_mfma_f32_16x16x32_bf16 v[92:95], v[140:143], a[124:127], v[92:95]\n"
      "s_waitcnt vmcnt(20)\n"
      "v_mfma_f32_16x16x32_bf16 v[72:75], v[144:147], a[136:139], v[72:75]\n"
      "s_waitcnt lgkmcnt(0)\n"
      "s_barrier\n"
      "v_mfma_f32_16x16x32_bf16 v[76:79], v[144:147], a[140:143], v[76:79]\n"
      "ds_read2st64_b32 v[52:53], v244 offset0:12 offset1:13\n"
      "ds_read2st64_b32 v[54:55], v245 offset0:16 offset1:17\n"
      "v_mfma_f32_16x16x32_bf16 v[80:83], v[144:147], a[144:147], v[80:83]\n"
      "ds_read2st64_b32 v[56:57], v244 offset0:14 offset1:15\n"
      "v_mfma_f32_16x16x32_bf16 v[84:87], v[144:147], a[148:151], v[84:87]\n"
      "ds_read2st64_b32 v[58:59], v245 offset0:18 offset1:19\n"
      "v_mfma_f32_16x16x32_bf16 v[88:91], v[144:147], a[152:155], v[88:91]\n"
      "ds_read2st64_b32 v[60:61], v246 offset0:20 offset1:21\n"
      "v_mfma_f32_16x16x32_bf16 v[92:95], v[144:147], a[156:159], v[92:95]\n"
      "ds_read2st64_b32 v[62:63], v246 offset0:22 offset1:23\n"
      "v_mfma_f32_16x16x32_bf16 v[72:75], v[148:151], a[168:171], v[72:75]\n"
      "v_mfma_f32_16x16x32_bf16 v[76:79], v[148:151], a[172:175], v[76:79]\n"
      "v_mfma_f32_16x16x32_bf16 v[80:83], v[148:151], a[176:179], v[80:83]\n"
      "v_mfma_f32_16x16x32_bf16 v[84:87], v[148:151], a[180:183], v[84:87]\n"
      "v_mfma_f32_16x16x32_bf16 v[88:91], v[148:151], a[184:187], v[88:91]\n"
      "s_waitcnt lgkmcnt(0)\n"
      "v_mfma_f32_16x16x32_bf16 v[32:35], v[250:253], v[52:55], v[32:35]\n"
      "v_mfma_f32_16x16x32_bf16 v[92:95], v[148:151], a[188:191], v[92:95]\n"
      "s_waitcnt vmcnt(18)\n"
      "v_mfma_f32_16x16x32_bf16 v[36:39], v[250:253], v[56:59], v[36:39]\n"
      "v_mfma_f32_16x16x32_bf16 v[72:75], v[152:155], a[200:203], v[72:75]\n"
      "v_mfma_f32_16x16x32_bf16 v[32:35], v[252:255], v[60:63], v[32:35]\n"
      "v_mfma_f32_16x16x32_bf16 v[76:79], v[152:155], a[204:207], v[76:79]\n"
      "v_mfma_f32_16x16x32_bf16 v[36:39], v[248:251], v[60:63], v[36:39]\n"
      "v_mov_b32 v43, s37\n"
      "v_mfma_f32_16x16x32_bf16 v[80:83], v[152:155], a[208:211], v[80:83]\n"
      "v_mov_b32 v44, v247\n"
      "v_mov_b32 v47, s37\n"
      "v_mfma_f32_16x16x32_bf16 v[84:87], v[152:155], a[212:215], v[84:87]\n"
      "v_mfma_f32_16x16x32_bf16 v[88:91], v[152:155], a[216:219], v[88:91]\n"
      "v_cvt_pk_bf16_f32 v40, v32, v33\n"
      "v_cvt_pk_bf16_f32 v41, v34, v35\n"
      "v_mfma_f32_16x16x32_bf16 v[92:95], v[152:155], a[220:223], v[92:95]\n"
      "v_cvt_pk_bf16_f32 v42, v36, v37\n"
      "v_cvt_pk_bf16_f32 v45, v38, v39\n"
      "v_mfma_f32_16x16x32_bf16 v[72:75], v[156:159], a[232:235], v[72:75]\n"
      "global_store_dwordx4 v242, v[40:43], s[52:53] offset:1024 sc1\n"
      "v_mfma_f32_16x16x32_bf16 v[76:79], v[156:159], a[236:239], v[76:79]\n"
      "global_store_dwordx4 v242, v[44:47], s[52:53] offset:2048 sc1\n"
      "v_mfma_f32_16x16x32_bf16 v[80:83], v[156:159], a[240:243], v[80:83]\n"
      "v_mfma_f32_16x16x32_bf16 v[84:87], v[156:159], a[244:247], v[84:87]\n"
      "v_mfma_f32_16x16x32_bf16 v[88:91], v[156:159], a[248:251], v[88:91]\n"
      "s_nop 1\n"
      "v_cvt_pk_bf16_f32 v72, v72, v73\n"
      "v_mfma_f32_16x16x32_bf16 v[92:95], v[156:159], a[252:255], v[92:95]\n"
      "s_waitcnt vmcnt(16)\n"
      "v_cvt_pk_bf16_f32 v73, v74, v75\n"
      "v_mfma_f32_16x16x32_bf16 v[104:107], v[160:163], a[8:11], 0\n"
      "ds_write_addtid_b32 v72 offset:6144\n"
      "v_cvt_pk_bf16_f32 v74, v76, v77\n"
      "v_mfma_f32_16x16x32_bf16 v[108:111], v[160:163], a[12:15], 0\n"
      "ds_write_addtid_b32 v73 offset:6400\n"
      "v_cvt_pk_bf16_f32 v75, v78, v79\n"
      "v_mfma_f32_16x16x32_bf16 v[112:115], v[160:163], a[16:19], 0\n"
      "ds_write_addtid_b32 v74 offset:6656\n"
      "v_cvt_pk_bf16_f32 v76, v80, v81\n"
      "v_mfma_f32_16x16x32_bf16 v[116:119], v[160:163], a[20:23], 0\n"
      "ds_write_addtid_b32 v75 offset:6912\n"
      "v_cvt_pk_bf16_f32 v77, v82, v83\n"
      "v_mfma_f32_16x16x32_bf16 v[120:123], v[160:163], a[24:27], 0\n"
      "ds_write_addtid_b32 v76 offset:7168\n"
      "v_cvt_pk_bf16_f32 v78, v84, v85\n"
      "v_mfma_f32_16x16x32_bf16 v[124:127], v[160:163], a[28:31], 0\n"
      "ds_write_addtid_b32 v77 offset:7424\n"
      "v_cvt_pk_bf16_f32 v79, v86, v87\n"
      "v_mfma_f32_16x16x32_bf16 v[104:107], v[164:167], a[40:43], v[104:107]\n"
      "ds_write_addtid_b32 v78 offset:7680\n"
      "v_cvt_pk_bf16_f32 v80, v88, v89\n"
      "v_mfma_f32_16x16x32_bf16 v[108:111], v[164:167], a[44:47], v[108:111]\n"
      "ds_write_addtid_b32 v79 offset:7936\n"
      "v_cvt_pk_bf16_f32 v81, v90, v91\n"
      "v_mfma_f32_16x16x32_bf16 v[112:115], v[164:167], a[48:51], v[112:115]\n"
      "ds_write_addtid_b32 v80 offset:8192\n"
      "v_cvt_pk_bf16_f32 v82, v92, v93\n"
      "v_mfma_f32_16x16x32_bf16 v[116:119], v[164:167], a[52:55], v[116:119]\n"
      "ds_write_addtid_b32 v81 offset:8448\n"
      "v_cvt_pk_bf16_f32 v83, v94, v95\n"
      "v_mfma_f32_16x16x32_bf16 v[120:123], v[164:167], a[56:59], v[120:123]\n"
      "ds_write_addtid_b32 v82 offset:8704\n"
      "v_mfma_f32_16x16x32_bf16 v[124:127], v[164:167], a[60:63], v[124:127]\n"
      "s_waitcnt vmcnt(14)\n"
      "ds_write_addtid_b32 v83 offset:8960\n"
      "v_mfma_f32_16x16x32_bf16 v[104:107], v[168:171], a[72:75], v[104:107]\n"
      "v_mfma_f32_16x16x32_bf16 v[108:111], v[168:171], a[76:79], v[108:111]\n"
      "v_mfma_f32_16x16x32_bf16 v[112:115], v[168:171], a[80:83], v[112:115]\n"
      "v_mfma_f32_16x16x32_bf16 v[116:119], v[168:171], a[84:87], v[116:119]\n"
      "v_mfma_f32_16x16x32_bf16 v[120:123], v[168:171], a[88:91], v[120:123]\n"
      "v_mfma_f32_16x16x32_bf16 v[124:127], v[168:171], a[92:95], v[124:127]\n"
      "v_mfma_f32_16x16x32_bf16 v[104:107], v[172:175], a[104:107], v[104:107]\n"
      "s_lshr_b32 s43, s42, 7\n"
      "s_cmp_eq_u32 s43, s36\n"
      "s_cbranch_scc0 .Lp_e18_%=\n"
      "s_lshl2_add_u32 s44, s35, s34\n"
      "s_lshl2_add_u32 s43, s44, s33\n"
      "s_lshl_b32 s43, s43, 12\n"
      "s_add_u32 s43, s43, 524288\n"
      "v_add_u32 v60, s43, v242\n"
      "global_load_dwordx4 v[24:27], v60, s[58:59] offset:0 sc1\n"
      "global_load_dwordx4 v[28:31], v60, s[58:59] offset:1024 sc1\n"
      "global_load_dwordx4 v[32:35], v60, s[58:59] offset:2048 sc1\n"
      "global_load_dwordx4 v[36:39], v60, s[58:59] offset:3072 sc1\n"
      "v_and_b32 v58, 15, v243\n"
      "v_lshrrev_b32 v59, 5, v243\n"
      "v_lshl_or_b32 v58, v59, 4, v58\n"
      "s_lshl4_add_u32 s44, s42, s44\n"
      "s_lshl_b32 s44, s44, 6\n"
      "v_lshl_add_u32 v59, v58, 1, s44\n"
      "global_load_ushort v57, v59, s[16:17]\n"
      "s_mov_b32 s44, 131072\n"
      "v_lshl_add_u32 v58, v58, 2, s44\n"
      "s_lshl_b32 s43, s33, 13\n"
      "v_add_u32 v59, s43, v58\n"
      ".Lp_e18_%=:\n"
      "v_mfma_f32_16x16x32_bf16 v[108:111], v[172:175], a[108:111], v[108:111]\n"
      "v_mfma_f32_16x16x32_bf16 v[112:115], v[172:175], a[112:115], v[112:115]\n"
      "v_mfma_f32_16x16x32_bf16 v[116:119], v[172:175], a[116:119], v[116:119]\n"
      "v_mfma_f32_16x16x32_bf16 v[120:123], v[172:175], a[120:123], v[120:123]\n"
      "v_mfma_f32_16x16x32_bf16 v[124:127], v[172:175], a[124:127], v[124:127]\n"
      "s_waitcnt vmcnt(8)\n"
      "v_mfma_f32_16x16x32_bf16 v[104:107], v[176:179], a[136:139], v[104:107]\n"
      "v_mfma_f32_16x16x32_bf16 v[108:111], v[176:179], a[140:143], v[108:111]\n"
      "v_mfma_f32_16x16x32_bf16 v[112:115], v[176:179], a[144:147], v[112:115]\n"
      "v_mfma_f32_16x16x32_bf16 v[116:119], v[176:179], a[148:151], v[116:119]\n"
      "v_mfma_f32_16x16x32_bf16 v[120:123], v[176:179], a[152:155], v[120:123]\n"
      "v_mfma_f32_16x16x32_bf16 v[124:127], v[176:179], a[156:159], v[124:127]\n"
      "v_mfma_f32_16x16x32_bf16 v[104:107], v[180:183], a[168:171], v[104:107]\n"
      "v_mfma_f32_16x16x32_bf16 v[108:111], v[180:183], a[172:175], v[108:111]\n"
      "v_mfma_f32_16x16x32_bf16 v[112:115], v[180:183], a[176:179], v[112:115]\n"
      "v_mfma_f32_16x16x32_bf16 v[116:119], v[180:183], a[180:183], v[116:119]\n"
      "v_add_u32 v5, 12288, v242\n"
      "v_mfma_f32_16x16x32_bf16 v[120:123], v[180:183], a[184:187], v[120:123]\n"
      "global_load_dwordx4 v[44:47], v5, s[62:63] offset:0 sc1\n"
      "v_mfma_f32_16x16x32_bf16 v[124:127], v[180:183], a[188:191], v[124:127]\n"
      "s_waitcnt vmcnt(7)\n"
      "global_load_dwordx4 v[48:51], v5, s[62:63] offset:1024 sc1\n"
      "v_mfma_f32_16x16x32_bf16 v[104:107], v[184:187], a[200:203], v[104:107]\n"
      "global_load_dwordx4 v[52:55], v5, s[62:63] offset:2048 sc1\n"
      "v_mfma_f32_16x16x32_bf16 v[108:111], v[184:187], a[204:207], v[108:111]\n"
      "v_mfma_f32_16x16x32_bf16 v[112:115], v[184:187], a[208:211], v[112:115]\n"
      "v_mfma_f32_16x16x32_bf16 v[116:119], v[184:187], a[212:215], v[116:119]\n"
      "v_mfma_f32_16x16x32_bf16 v[120:123], v[184:187], a[216:219], v[120:123]\n"
      "v_mfma_f32_16x16x32_bf16 v[124:127], v[184:187], a[220:223], v[124:127]\n"
      "v_mfma_f32_16x16x32_bf16 v[104:107], v[188:191], a[232:235], v[104:107]\n"
      "v_mfma_f32_16x16x32_bf16 v[108:111], v[188:191], a[236:239], v[108:111]\n"
      "v_mfma_f32_16x16x32_bf16 v[112:115], v[188:191], a[240:243], v[112:115]\n"
      "v_mfma_f32_16x16x32_bf16 v[116:119], v[188:191], a[244:247], v[116:119]\n"
      "s_waitcnt vmcnt(5)\n"
      "v_bitop3_b32 v22, v15, v19, s37 bitop3:0x7e\n"
      "v_bitop3_b32 v22, v22, v23, s37 bitop3:0xf6\n"
      "v_cmp_ne_u32 vcc, 0, v22\n"
      "s_nop 1\n"
      "s_cbranch_vccnz .Lp_s20_%=\n"
      ".Lp_o21_%=:\n"
      "v_mfma_f32_16x16x32_bf16 v[120:123], v[188:191], a[248:251], v[120:123]\n"
      "v_cvt_pk_bf16_f32 v104, v104, v105\n"
      "v_cvt_pk_bf16_f32 v105, v106, v107\n"
      "v_mfma_f32_16x16x32_bf16 v[124:127], v[188:191], a[252:255], v[124:127]\n"
      "ds_write_addtid_b32 v104 offset:9216\n"
      "v_cvt_pk_bf16_f32 v106, v108, v109\n"
      "v_mfma_f32_16x16x32_bf16 v[64:67], v[128:131], a[0:3], 0\n"
      "ds_write_addtid_b32 v105 offset:9472\n"
      "v_cvt_pk_bf16_f32 v107, v110, v111\n"
      "v_mfma_f32_16x16x32_bf16 v[68:71], v[128:131], a[4:7], 0\n"
      "ds_write_addtid_b32 v106 offset:9728\n"
      "v_cvt_pk_bf16_f32 v108, v112, v113\n"
      "v_mfma_f32_16x16x32_bf16 v[64:67], v[132:135], a[32:35], v[64:67]\n"
      "ds_write_addtid_b32 v107 offset:9984\n"
      "v_cvt_pk_bf16_f32 v109, v114, v115\n"
      "v_mfma_f32_16x16x32_bf16 v[68:71], v[132:135], a[36:39], v[68:71]\n"
      "ds_write_addtid_b32 v108 offset:10240\n"
      "v_cvt_pk_bf16_f32 v110, v116, v117\n"
      "v_mfma_f32_16x16x32_bf16 v[64:67], v[136:139], a[64:67], v[64:67]\n"
      "ds_write_addtid_b32 v109 offset:10496\n"
      "v_cvt_pk_bf16_f32 v111, v118, v119\n"
      "v_mfma_f32_16x16x32_bf16 v[68:71], v[136:139], a[68:71], v[68:71]\n"
      "ds_write_addtid_b32 v110 offset:10752\n"
      "v_cvt_pk_bf16_f32 v112, v120, v121\n"
      "v_mfma_f32_16x16x32_bf16 v[64:67], v[140:143], a[96:99], v[64:67]\n"
      "ds_write_addtid_b32 v111 offset:11008\n"
      "v_cvt_pk_bf16_f32 v113, v122, v123\n"
      "v_mfma_f32_16x16x32_bf16 v[68:71], v[140:143], a[100:103], v[68:71]\n"
      "ds_write_addtid_b32 v112 offset:11264\n"
      "v_cvt_pk_bf16_f32 v114, v124, v125\n"
      "v_mfma_f32_16x16x32_bf16 v[64:67], v[144:147], a[128:131], v[64:67]\n"
      "ds_write_addtid_b32 v113 offset:11520\n"
      "v_cvt_pk_bf16_f32 v115, v126, v127\n"
      "v_mfma_f32_16x16x32_bf16 v[68:71], v[144:147], a[132:135], v[68:71]\n"
      "ds_write_addtid_b32 v114 offset:11776\n"
      "v_mov_b32 v15, v20\n"
      "v_mfma_f32_16x16x32_bf16 v[64:67], v[148:151], a[160:163], v[64:67]\n"
      "ds_write_addtid_b32 v115 offset:12032\n"
      "v_add_u32 v6, 24576, v242\n"
      "v_mfma_f32_16x16x32_bf16 v[68:71], v[148:151], a[164:167], v[68:71]\n"
      "global_load_dwordx4 v[72:75], v6, s[62:63] offset:0 sc1\n"
      "v_mov_b32 v19, v21\n"
      "v_mfma_f32_16x16x32_bf16 v[64:67], v[152:155], a[192:195], v[64:67]\n"
      "global_load_dwordx4 v[76:79], v6, s[62:63] offset:1024 sc1\n"
      "v_mfma_f32_16x16x32_bf16 v[68:71], v[152:155], a[196:199], v[68:71]\n"
      "global_load_dwordx4 v[80:83], v6, s[62:63] offset:2048 sc1\n"
      "v_mfma_f32_16x16x32_bf16 v[64:67], v[156:159], a[224:227], v[64:67]\n"
      "s_lshr_b32 s43, s42, 7\n"
      "s_cmp_eq_u32 s43, s36\n"
      "s_cbranch_scc0 .Lp_e22_%=\n"
      "s_waitcnt vmcnt(6)\n"
      "v_bitop3_b32 v61, v25, v27, s37 bitop3:0x7e\n"
      "v_bitop3_b32 v61, v61, v29, s37 bitop3:0xf6\n"
      "v_bitop3_b32 v61, v61, v31, s37 bitop3:0xf6\n"
      "v_bitop3_b32 v61, v61, v33, s37 bitop3:0xf6\n"
      "v_bitop3_b32 v61, v61, v35, s37 bitop3:0xf6\n"
      "v_bitop3_b32 v61, v61, v37, s37 bitop3:0xf6\n"
      "v_bitop3_b32 v61, v61, v39, s37 bitop3:0xf6\n"
      "v_cmp_ne_u32 vcc, 0, v61\n"
      "s_nop 1\n"
      "s_cbranch_vccnz .Lp_s24_%=\n"
      ".Lp_o25_%=:\n"
      "v_add_f32 v24, v24, v28\n"
      "v_add_f32 v26, v26, v30\n"
      "v_add_f32 v32, v32, v36\n"
      "v_add_f32 v34, v34, v38\n"
      "v_add_f32 v24, v24, v32\n"
      "v_add_f32 v26, v26, v34\n"
      "s_nop 1\n"
      "v_permlane32_swap_b32 v24, v26\n"
      "v_add_f32 v24, v24, v26\n"
      "v_mov_b32 v26, v24\n"
      "s_nop 1\n"
      "v_permlane16_swap_b32 v24, v26\n"
      "v_add_f32 v24, v24, v26\n"
      "ds_write_b32 v59, v24\n"
      "s_waitcnt lgkmcnt(0)\n"
      ".Lp_e22_%=:\n"
      "v_mfma_f32_16x16x32_bf16 v[68:71], v[156:159], a[228:231], v[68:71]\n"
      "s_waitcnt lgkmcnt(0)\n"
      "s_barrier\n"
      "v_mfma_f32_16x16x32_bf16 v[96:99], v[160:163], a[0:3], 0\n"
      "s_lshr_b32 s43, s42, 5\n"
      "s_lshl2_add_u32 s44, s36, s33\n"
      "s_cmp_eq_u32 s43, s44\n"
      "s_cbranch_scc0 .Lp_e26_%=\n"
      "ds_read_b32 v24, v58 offset:0\n"
      "ds_read_b32 v25, v58 offset:8192\n"
      "ds_read_b32 v26, v58 offset:16384\n"
      "ds_read_b32 v27, v58 offset:24576\n"
      "v_mov_b32 v62, 0\n"
      "v_mov_b32 v63, s37\n"
      "s_or_b32 s43, s34, s35\n"
      "s_cmp_eq_u32 s43, 0\n"
      "s_cbranch_scc0 .Lp_z27_%=\n"
      "s_mov_b64 exec, 1\n"
      "global_store_dword v62, v63, s[58:59] sc1\n"
      ".Lp_z27_%=:\n"
      "s_mov_b64 exec, -1\n"
      "s_waitcnt vmcnt(6)\n"
      "v_lshlrev_b32 v57, 16, v57\n"
      "v_and_b32 v56, 48, v243\n"
      "s_mov_b32 s44, 131072\n"
      "v_lshl_add_u32 v56, v56, 1, s44\n"
      "s_waitcnt lgkmcnt(0)\n"
      "v_add_f32 v24, v24, v25\n"
      "s_waitcnt lgkmcnt(0)\n"
      "v_add_f32 v26, v26, v27\n"
      "v_add_f32 v24, v24, v26\n"
      "v_cvt_pk_bf16_f32 v24, v24, v24\n"
      "v_and_b32 v24, 0xffff0000, v24\n"
      "v_mul_f32 v24, s84, v24\n"
      "v_cvt_pk_bf16_f32 v24, v24, v24\n"
      "v_and_b32 v24, 0xffff0000, v24\n"
      "v_add_f32 v24, v24, v57\n"
      "v_cvt_pk_bf16_f32 v24, v24, v24\n"
      "v_and_b32 v24, 0xffff0000, v24\n"
      "v_sub_f32 v24, v24, v57\n"
      "ds_write_b32 v58, v24 offset:1024\n"
      "ds_read_b128 v[28:31], v56 offset:1024\n"
      "ds_read_b128 v[32:35], v56 offset:1040\n"
      ".Lp_e26_%=:\n"
      "v_mfma_f32_16x16x32_bf16 v[100:103], v[160:163], a[4:7], 0\n"
      "ds_read2st64_b32 v[84:85], v244 offset0:24 offset1:25\n"
      "v_mfma_f32_16x16x32_bf16 v[96:99], v[164:167], a[32:35], v[96:99]\n"
      "ds_read2st64_b32 v[86:87], v245 offset0:28 offset1:29\n"
      "v_mfma_f32_16x16x32_bf16 v[100:103], v[164:167], a[36:39], v[100:103]\n"
      "ds_read2st64_b32 v[88:89], v244 offset0:26 offset1:27\n"
      "v_mfma_f32_16x16x32_bf16 v[96:99], v[168:171], a[64:67], v[96:99]\n"
      "ds_read2st64_b32 v[90:91], v245 offset0:30 offset1:31\n"
      "v_mfma_f32_16x16x32_bf16 v[100:103], v[168:171], a[68:71], v[100:103]\n"
      "ds_read2st64_b32 v[92:93], v246 offset0:32 offset1:33\n"
      "v_mfma_f32_16x16x32_bf16 v[96:99], v[172:175], a[96:99], v[96:99]\n"
      "ds_read2st64_b32 v[94:95], v246 offset0:34 offset1:35\n"
      "v_mfma_f32_16x16x32_bf16 v[100:103], v[172:175], a[100:103], v[100:103]\n"
      "ds_read2st64_b32 v[116:117], v244 offset0:36 offset1:37\n"
      "v_mfma_f32_16x16x32_bf16 v[96:99], v[176:179], a[128:131], v[96:99]\n"
      "ds_read2st64_b32 v[118:119], v245 offset0:40 offset1:41\n"
      "v_mfma_f32_16x16x32_bf16 v[100:103], v[176:179], a[132:135], v[100:103]\n"
      "ds_read2st64_b32 v[120:121], v244 offset0:38 offset1:39\n"
      "v_mfma_f32_16x16x32_bf16 v[96:99], v[180:183], a[160:163], v[96:99]\n"
      "ds_read2st64_b32 v[122:123], v245 offset0:42 offset1:43\n"
      "v_mfma_f32_16x16x32_bf16 v[100:103], v[180:183], a[164:167], v[100:103]\n"
      "s_waitcnt lgkmcnt(4)\n"
      "v_mfma_f32_16x16x32_bf16 v[64:67], v[250:253], v[84:87], v[64:67]\n"
      "v_mfma_f32_16x16x32_bf16 v[96:99], v[184:187], a[192:195], v[96:99]\n"
      "v_mfma_f32_16x16x32_bf16 v[68:71], v[250:253], v[88:91], v[68:71]\n"
      "v_mfma_f32_16x16x32_bf16 v[100:103], v[184:187], a[196:199], v[100:103]\n"
      "v_mfma_f32_16x16x32_bf16 v[64:67], v[252:255], v[92:95], v[64:67]\n"
      "v_mfma_f32_16x16x32_bf16 v[96:99], v[188:191], a[224:227], v[96:99]\n"
      "v_mfma_f32_16x16x32_bf16 v[68:71], v[248:251], v[92:95], v[68:71]\n"
      "v_mfma_f32_16x16x32_bf16 v[100:103], v[188:191], a[228:231], v[100:103]\n"
      "ds_read2st64_b32 v[124:125], v246 offset0:44 offset1:45\n"
      "ds_read2st64_b32 v[126:127], v246 offset0:46 offset1:47\n"
      "v_mfma_f32_16x16x32_bf16 v[64:67], v[252:255], v[12:15], v[64:67]\n"
      "v_mfma_f32_16x16x32_bf16 v[68:71], v[248:251], v[12:15], v[68:71]\n"
      "v_mfma_f32_16x16x32_bf16 v[96:99], v[252:255], v[16:19], v[96:99]\n"
      "v_mfma_f32_16x16x32_bf16 v[100:103], v[248:251], v[16:19], v[100:103]\n"
      "s_waitcnt vmcnt(3)\n"
      "v_bitop3_b32 v54, v47, v51, s37 bitop3:0x7e\n"
      "v_bitop3_b32 v54, v54, v55, s37 bitop3:0xf6\n"
      "v_cmp_ne_u32 vcc, 0, v54\n"
      "s_nop 1\n"
      "s_cbranch_vccnz .Lp_s29_%=\n"
      ".Lp_o30_%=:\n"
      "v_mov_b32 v47, v52\n"
      "v_mov_b32 v51, v53\n"
      "s_nop 0\n"
      "v_mfma_f32_16x16x32_bf16 v[64:67], v[252:255], v[44:47], v[64:67]\n"
      "v_mfma_f32_16x16x32_bf16 v[68:71], v[248:251], v[44:47], v[68:71]\n"
      "v_mfma_f32_16x16x32_bf16 v[96:99], v[252:255], v[48:51], v[96:99]\n"
      "v_mfma_f32_16x16x32_bf16 v[100:103], v[248:251], v[48:51], v[100:103]\n"
      "s_waitcnt lgkmcnt(0)\n"
      "v_mfma_f32_16x16x32_bf16 v[96:99], v[250:253], v[116:119], v[96:99]\n"
      "v_mfma_f32_16x16x32_bf16 v[100:103], v[250:253], v[120:123], v[100:103]\n"
      "v_mfma_f32_16x16x32_bf16 v[96:99], v[252:255], v[124:127], v[96:99]\n"
      "v_mfma_f32_16x16x32_bf16 v[100:103], v[248:251], v[124:127], v[100:103]\n"
      "s_lshr_b32 s43, s42, 5\n"
      "s_lshl2_add_u32 s44, s36, s33\n"
      "s_cmp_eq_u32 s43, s44\n"
      "s_cbranch_scc0 .Lp_e31_%=\n"
      "v_lshrrev_b32 v24, 10, v7\n"
      "v_subrev_u32 v24, s42, v24\n"
      "v_cmp_eq_u32 vcc, 0, v24\n"
      "s_nop 1\n"
      "s_mov_b64 exec, vcc\n"
      "v_fma_f32 v208, v205, v28, v208\n"
      "v_fma_f32 v209, v205, v29, v209\n"
      "v_fma_f32 v210, v205, v30, v210\n"
      "v_fma_f32 v211, v205, v31, v211\n"
      "v_fma_f32 v216, v205, v32, v216\n"
      "v_fma_f32 v217, v205, v33, v217\n"
      "v_fma_f32 v218, v205, v34, v218\n"
      "v_fma_f32 v219, v205, v35, v219\n"
      "s_mov_b64 exec, -1\n"
      "v_cmp_eq_u32 vcc, -16, v24\n"
      "s_nop 1\n"
      "s_mov_b64 exec, vcc\n"
      "v_fma_f32 v212, v207, v28, v212\n"
      "v_fma_f32 v213, v207, v29, v213\n"
      "v_fma_f32 v214, v207, v30, v214\n"
      "v_fma_f32 v215, v207, v31, v215\n"
      "v_fma_f32 v220, v207, v32, v220\n"
      "v_fma_f32 v221, v207, v33, v221\n"
      "v_fma_f32 v222, v207, v34, v222\n"
      "v_fma_f32 v223, v207, v35, v223\n"
      "s_mov_b64 exec, -1\n"
      ".Lp_e31_%=:\n"
      "s_waitcnt vmcnt(0)\n"
      "v_bitop3_b32 v82, v75, v79, s37 bitop3:0x7e\n"
      "v_bitop3_b32 v82, v82, v83, s37 bitop3:0xf6\n"
      "v_cmp_ne_u32 vcc, 0, v82\n"
      "s_nop 1\n"
      "s_cbranch_vccnz .Lp_s33_%=\n"
      ".Lp_o34_%=:\n"
      "v_mov_b32 v75, v80\n"
      "v_mov_b32 v79, v81\n"
      "s_nop 0\n"
      "v_mfma_f32_16x16x32_bf16 v[64:67], v[252:255], v[72:75], v[64:67]\n"
      "v_mfma_f32_16x16x32_bf16 v[96:99], v[252:255], v[76:79], v[96:99]\n"
      "v_mfma_f32_16x16x32_bf16 v[68:71], v[248:251], v[72:75], v[68:71]\n"
      "v_mfma_f32_16x16x32_bf16 v[100:103], v[248:251], v[76:79], v[100:103]\n"
      "s_nop 4\n"
      "v_fma_f32 v208, v204, v64, v208\n"
      "v_fma_f32 v209, v204, v65, v209\n"
      "v_fma_f32 v210, v204, v66, v210\n"
      "v_fma_f32 v211, v204, v67, v211\n"
      "v_fma_f32 v216, v204, v96, v216\n"
      "v_fma_f32 v217, v204, v97, v217\n"
      "v_fma_f32 v218, v204, v98, v218\n"
      "v_fma_f32 v219, v204, v99, v219\n"
      "v_cvt_pk_bf16_f32 v104, v208, v209\n"
      "v_cvt_pk_bf16_f32 v105, v210, v211\n"
      "v_cvt_pk_bf16_f32 v106, v216, v217\n"
      "v_cvt_pk_bf16_f32 v107, v218, v219\n"
      "global_store_dwordx4 v7, v[104:107], s[20:21]\n"
      "v_fma_f32 v212, v206, v68, v212\n"
      "v_fma_f32 v213, v206, v69, v213\n"
      "v_fma_f32 v214, v206, v70, v214\n"
      "v_fma_f32 v215, v206, v71, v215\n"
      "v_fma_f32 v220, v206, v100, v220\n"
      "v_fma_f32 v221, v206, v101, v221\n"
      "v_fma_f32 v222, v206, v102, v222\n"
      "v_fma_f32 v223, v206, v103, v223\n"
      "v_cvt_pk_bf16_f32 v108, v212, v213\n"
      "v_cvt_pk_bf16_f32 v109, v214, v215\n"
      "v_cvt_pk_bf16_f32 v110, v220, v221\n"
      "v_cvt_pk_bf16_f32 v111, v222, v223\n"
      "global_store_dwordx4 v7, v[108:111], s[68:69]\n"
      "s_endpgm\n"
      ".Lp_s2_%=:\n"
      "s_cmp_eq_u32 s38, 0\n"
      "s_cbranch_scc1 .Lp_o3_%=\n"
      "s_sub_u32 s38, s38, 1\n"
      "s_sleep 4\n"
      "s_nop 7\n"
      "global_load_dword v252, v253, s[58:59] offset:0 sc1\n"
      "s_waitcnt vmcnt(0)\n"
      "v_cmp_ne_u32 vcc, s37, v252\n"
      "s_nop 1\n"
      "s_cbranch_vccnz .Lp_s2_%=\n"
      "s_nop 7\n"
      "s_branch .Lp_o3_%=\n"
      ".Lp_s5_%=:\n"
      "s_cmp_eq_u32 s38, 0\n"
      "s_cbranch_scc1 .Lp_o6_%=\n"
      "s_sub_u32 s38, s38, 1\n"
      "s_sleep 4\n"
      "s_nop 7\n"
      "global_load_dword v252, v253, s[58:59] offset:128 sc1\n"
      "s_waitcnt vmcnt(0)\n"
      "v_cmp_ne_u32 vcc, s37, v252\n"
      "s_nop 1\n"
      "s_cbranch_vccnz .Lp_s5_%=\n"
      "s_nop 7\n"
      "s_branch .Lp_o6_%=\n"
      ".Lp_s8_%=:\n"
      "s_cmp_eq_u32 s38, 0\n"
      "s_cbranch_scc1 .Lp_o9_%=\n"
      "s_sub_u32 s38, s38, 1\n"
      "s_sleep 2\n"
      "s_nop 7\n"
      "global_load_dwordx2 v[202:203], v200, s[58:59] sc1\n"
      "s_waitcnt vmcnt(0)\n"
      "v_cmp_ne_u32 vcc, s37, v203\n"
      "s_nop 1\n"
      "s_cbranch_vccnz .Lp_s8_%=\n"
      "s_nop 7\n"
      "s_branch .Lp_o9_%=\n"
      ".Lp_s13_%=:\n"
      "s_cmp_eq_u32 s38, 0\n"
      "s_cbranch_scc1 .Lp_o14_%=\n"
      "s_sub_u32 s38, s38, 1\n"
      "s_sleep 1\n"
      "s_nop 7\n"
      "global_load_dwordx2 v[204:205], v209, s[58:59] offset:0 sc1\n"
      "global_load_dwordx2 v[206:207], v209, s[58:59] offset:256 sc1\n"
      "s_waitcnt vmcnt(0)\n"
      "v_bitop3_b32 v210, v205, v207, s37 bitop3:0x7e\n"
      "v_cmp_ne_u32 vcc, 0, v210\n"
      "s_nop 1\n"
      "s_cbranch_vccnz .Lp_s13_%=\n"
      "s_nop 7\n"
      "s_branch .Lp_o14_%=\n"
      ".Lp_s16_%=:\n"
      "s_cmp_eq_u32 s38, 0\n"
      "s_cbranch_scc1 .Lp_o17_%=\n"
      "s_sub_u32 s38, s38, 1\n"
      "s_sleep 8\n"
      "s_nop 7\n"
      "global_load_dwordx4 v[224:227], v200, s[58:59] offset:0 sc1\n"
      "global_load_dwordx4 v[228:231], v200, s[58:59] offset:1024 sc1\n"
      "global_load_dwordx4 v[232:235], v200, s[58:59] offset:2048 sc1\n"
      "global_load_dwordx4 v[236:239], v200, s[58:59] offset:3072 sc1\n"
      "s_waitcnt vmcnt(0)\n"
      "v_bitop3_b32 v192, v225, v227, s37 bitop3:0x7e\n"
      "v_bitop3_b32 v192, v192, v229, s37 bitop3:0xf6\n"
      "v_bitop3_b32 v192, v192, v231, s37 bitop3:0xf6\n"
      "v_bitop3_b32 v192, v192, v233, s37 bitop3:0xf6\n"
      "v_bitop3_b32 v192, v192, v235, s37 bitop3:0xf6\n"
      "v_bitop3_b32 v192, v192, v237, s37 bitop3:0xf6\n"
      "v_bitop3_b32 v192, v192, v239, s37 bitop3:0xf6\n"
      "v_cmp_ne_u32 vcc, 0, v192\n"
      "s_nop 1\n"
      "s_cbranch_vccnz .Lp_s16_%=\n"
      "s_nop 7\n"
      "s_branch .Lp_o17_%=\n"
      ".Lp_s20_%=:\n"
      "s_cmp_eq_u32 s38, 0\n"
      "s_cbranch_scc1 .Lp_o21_%=\n"
      "s_sub_u32 s38, s38, 1\n"
      "s_nop 7\n"
      "global_load_dwordx4 v[12:15], v4, s[62:63] offset:0 sc1\n"
      "global_load_dwordx4 v[16:19], v4, s[62:63] offset:1024 sc1\n"
      "global_load_dwordx4 v[20:23], v4, s[62:63] offset:2048 sc1\n"
      "s_waitcnt vmcnt(0)\n"
      "v_bitop3_b32 v22, v15, v19, s37 bitop3:0x7e\n"
      "v_bitop3_b32 v22, v22, v23, s37 bitop3:0xf6\n"
      "v_cmp_ne_u32 vcc, 0, v22\n"
      "s_nop 1\n"
      "s_cbranch_vccnz .Lp_s20_%=\n"
      "s_nop 7\n"
      "s_branch .Lp_o21_%=\n"
      ".Lp_s24_%=:\n"
      "s_cmp_eq_u32 s38, 0\n"
      "s_cbranch_scc1 .Lp_o25_%=\n"
      "s_sub_u32 s38, s38, 1\n"
      "s_sleep 2\n"
      "s_nop 7\n"
      "global_load_dwordx4 v[24:27], v60, s[58:59] offset:0 sc1\n"
      "global_load_dwordx4 v[28:31], v60, s[58:59] offset:1024 sc1\n"
      "global_load_dwordx4 v[32:35], v60, s[58:59] offset:2048 sc1\n"
      "global_load_dwordx4 v[36:39], v60, s[58:59] offset:3072 sc1\n"
      "s_waitcnt vmcnt(0)\n"
      "v_bitop3_b32 v61, v25, v27, s37 bitop3:0x7e\n"
      "v_bitop3_b32 v61, v61, v29, s37 bitop3:0xf6\n"
      "v_bitop3_b32 v61, v61, v31, s37 bitop3:0xf6\n"
      "v_bitop3_b32 v61, v61, v33, s37 bitop3:0xf6\n"
      "v_bitop3_b32 v61, v61, v35, s37 bitop3:0xf6\n"
      "v_bitop3_b32 v61, v61, v37, s37 bitop3:0xf6\n"
      "v_bitop3_b32 v61, v61, v39, s37 bitop3:0xf6\n"
      "v_cmp_ne_u32 vcc, 0, v61\n"
      "s_nop 1\n"
      "s_cbranch_vccnz .Lp_s24_%=\n"
      "s_nop 7\n"
      "s_branch .Lp_o25_%=\n"
      ".Lp_s29_%=:\n"
      "s_cmp_eq_u32 s38, 0\n"
      "s_cbranch_scc1 .Lp_o30_%=\n"
      "s_sub_u32 s38, s38, 1\n"
      "s_nop 7\n"
      "global_load_dwordx4 v[44:47], v5, s[62:63] offset:0 sc1\n"
      "global_load_dwordx4 v[48:51], v5, s[62:63] offset:1024 sc1\n"
      "global_load_dwordx4 v[52:55], v5, s[62:63] offset:2048 sc1\n"
      "s_waitcnt vmcnt(0)\n"
      "v_bitop3_b32 v54, v47, v51, s37 bitop3:0x7e\n"
      "v_bitop3_b32 v54, v54, v55, s37 bitop3:0xf6\n"
      "v_cmp_ne_u32 vcc, 0, v54\n"
      "s_nop 1\n"
      "s_cbranch_vccnz .Lp_s29_%=\n"
      "s_nop 7\n"
      "s_branch .Lp_o30_%=\n"
      ".Lp_s33_%=:\n"
      "s_cmp_eq_u32 s38, 0\n"
      "s_cbranch_scc1 .Lp_o34_%=\n"
      "s_sub_u32 s38, s38, 1\n"
      "s_nop 7\n"
      "global_load_dwordx4 v[72:75], v6, s[62:63] offset:0 sc1\n"
      "global_load_dwordx4 v[76:79], v6, s[62:63] offset:1024 sc1\n"
      "global_load_dwordx4 v[80:83], v6, s[62:63] offset:2048 sc1\n"
      "s_waitcnt vmcnt(0)\n"
      "v_bitop3_b32 v82, v75, v79, s37 bitop3:0x7e\n"
      "v_bitop3_b32 v82, v82, v83, s37 bitop3:0xf6\n"
      "v_cmp_ne_u32 vcc, 0, v82\n"
      "s_nop 1\n"
      "s_cbranch_vccnz .Lp_s33_%=\n"
      "s_nop 7\n"
      "s_branch .Lp_o34_%=\n"
      ".Ls_s3_%=:\n"
      "s_cmp_eq_u32 s38, 0\n"
      "s_cbranch_scc1 .Ls_o4_%=\n"
      "s_sub_u32 s38, s38, 1\n"
      "s_sleep 4\n"
      "s_nop 7\n"
      "global_load_dword v252, v253, s[58:59] offset:0 sc1\n"
      "s_waitcnt vmcnt(0)\n"
      "v_cmp_ne_u32 vcc, s37, v252\n"
      "s_nop 1\n"
      "s_cbranch_vccnz .Ls_s3_%=\n"
      "s_nop 7\n"
      "s_branch .Ls_o4_%=\n"
      ".Ls_s6_%=:\n"
      "s_cmp_eq_u32 s38, 0\n"
      "s_cbranch_scc1 .Ls_o7_%=\n"
      "s_sub_u32 s38, s38, 1\n"
      "s_sleep 2\n"
      "s_nop 7\n"
      "global_load_dwordx2 v[206:207], v204, s[58:59] sc1\n"
      "s_waitcnt vmcnt(0)\n"
      "v_cmp_ne_u32 vcc, s37, v207\n"
      "s_nop 1\n"
      "s_cbranch_vccnz .Ls_s6_%=\n"
      "s_nop 7\n"
      "s_branch .Ls_o7_%=\n"
      ".Ls_s10_%=:\n"
      "s_cmp_eq_u32 s38, 0\n"
      "s_cbranch_scc1 .Ls_o11_%=\n"
      "s_sub_u32 s38, s38, 1\n"
      "s_sleep 4\n"
      "s_nop 7\n"
      "global_load_dword v252, v253, s[58:59] offset:128 sc1\n"
      "s_waitcnt vmcnt(0)\n"
      "v_cmp_ne_u32 vcc, s37, v252\n"
      "s_nop 1\n"
      "s_cbranch_vccnz .Ls_s10_%=\n"
      "s_nop 7\n"
      "s_branch .Ls_o11_%=\n"
      ".Ls_s13_%=:\n"
      "s_cmp_eq_u32 s38, 0\n"
      "s_cbranch_scc1 .Ls_o14_%=\n"
      "s_sub_u32 s38, s38, 1\n"
      "s_sleep 1\n"
      "s_nop 7\n"
      "global_load_dwordx2 v[204:205], v209, s[58:59] offset:0 sc1\n"
      "global_load_dwordx2 v[206:207], v209, s[58:59] offset:256 sc1\n"
      "s_waitcnt vmcnt(0)\n"
      "v_bitop3_b32 v210, v205, v207, s37 bitop3:0x7e\n"
      "v_cmp_ne_u32 vcc, 0, v210\n"
      "s_nop 1\n"
      "s_cbranch_vccnz .Ls_s13_%=\n"
      "s_nop 7\n"
      "s_branch .Ls_o14_%=\n"
      ".Ls_s16_%=:\n"
      "s_cmp_eq_u32 s38, 0\n"
      "s_cbranch_scc1 .Ls_o17_%=\n"
      "s_sub_u32 s38, s38, 1\n"
      "s_nop 7\n"
      "global_load_dwordx4 v[12:15], v4, s[62:63] offset:0 sc1\n"
      "global_load_dwordx4 v[16:19], v4, s[62:63] offset:1024 sc1\n"
      "global_load_dwordx4 v[20:23], v4, s[62:63] offset:2048 sc1\n"
      "s_waitcnt vmcnt(0)\n"
      "v_bitop3_b32 v22, v15, v19, s37 bitop3:0x7e\n"
      "v_bitop3_b32 v22, v22, v23, s37 bitop3:0xf6\n"
      "v_cmp_ne_u32 vcc, 0, v22\n"
      "s_nop 1\n"
      "s_cbranch_vccnz .Ls_s16_%=\n"
      "s_nop 7\n"
      "s_branch .Ls_o17_%=\n"
      ".Ls_s21_%=:\n"
      "s_cmp_eq_u32 s38, 0\n"
      "s_cbranch_scc1 .Ls_o22_%=\n"
      "s_sub_u32 s38, s38, 1\n"
      "s_sleep 2\n"
      "s_nop 7\n"
      "global_load_dwordx4 v[24:27], v60, s[58:59] offset:0 sc1\n"
      "global_load_dwordx4 v[28:31], v60, s[58:59] offset:1024 sc1\n"
      "global_load_dwordx4 v[32:35], v60, s[58:59] offset:2048 sc1\n"
      "global_load_dwordx4 v[36:39], v60, s[58:59] offset:3072 sc1\n"
      "s_waitcnt vmcnt(0)\n"
      "v_bitop3_b32 v61, v25, v27, s37 bitop3:0x7e\n"
      "v_bitop3_b32 v61, v61, v29, s37 bitop3:0xf6\n"
      "v_bitop3_b32 v61, v61, v31, s37 bitop3:0xf6\n"
      "v_bitop3_b32 v61, v61, v33, s37 bitop3:0xf6\n"
      "v_bitop3_b32 v61, v61, v35, s37 bitop3:0xf6\n"
      "v_bitop3_b32 v61, v61, v37, s37 bitop3:0xf6\n"
      "v_bitop3_b32 v61, v61, v39, s37 bitop3:0xf6\n"
      "v_cmp_ne_u32 vcc, 0, v61\n"
      "s_nop 1\n"
      "s_cbranch_vccnz .Ls_s21_%=\n"
      "s_nop 7\n"
      "s_branch .Ls_o22_%=\n"
      ".Ls_s26_%=:\n"
      "s_cmp_eq_u32 s38, 0\n"
      "s_cbranch_scc1 .Ls_o27_%=\n"
      "s_sub_u32 s38, s38, 1\n"
      "s_nop 7\n"
      "global_load_dwordx4 v[44:47], v5, s[62:63] offset:0 sc1\n"
      "global_load_dwordx4 v[48:51], v5, s[62:63] offset:1024 sc1\n"
      "global_load_dwordx4 v[52:55], v5, s[62:63] offset:2048 sc1\n"
      "s_waitcnt vmcnt(0)\n"
      "v_bitop3_b32 v54, v47, v51, s37 bitop3:0x7e\n"
      "v_bitop3_b32 v54, v54, v55, s37 bitop3:0xf6\n"
      "v_cmp_ne_u32 vcc, 0, v54\n"
      "s_nop 1\n"
      "s_cbranch_vccnz .Ls_s26_%=\n"
      "s_nop 7\n"
      "s_branch .Ls_o27_%=\n"
      ".Ls_s30_%=:\n"
      "s_cmp_eq_u32 s38, 0\n"
      "s_cbranch_scc1 .Ls_o31_%=\n"
      "s_sub_u32 s38, s38, 1\n"
      "s_nop 7\n"
      "global_load_dwordx4 v[72:75], v6, s[62:63] offset:0 sc1\n"
      "global_load_dwordx4 v[76:79], v6, s[62:63] offset:1024 sc1\n"
      "global_load_dwordx4 v[80:83], v6, s[62:63] offset:2048 sc1\n"
      "s_waitcnt vmcnt(0)\n"
      "v_bitop3_b32 v82, v75, v79, s37 bitop3:0x7e\n"
      "v_bitop3_b32 v82, v82, v83, s37 bitop3:0xf6\n"
      "v_cmp_ne_u32 vcc, 0, v82\n"
      "s_nop 1\n"
      "s_cbranch_vccnz .Ls_s30_%=\n"
      "s_nop 7\n"
      "s_branch .Ls_o31_%=\n"
      ".Lend_%=:\n"
      :
      : "s"(__builtin_amdgcn_kernarg_segment_ptr()), "s"(blockIdx.x), "s"(__builtin_amdgcn_readfirstlane(threadIdx.x >> 6)),
        "s"((unsigned)(size_t)smem)
      : "memory", "scc", "vcc",
        "v0", "v1", "v2", "v3", "v4", "v5", "v6", "v7", "v8", "v9", "v10", "v11", "v12", "v13", "v14", "v15", "v16", "v17", "v18", "v19", "v20", "v21", "v22", "v23", "v24", "v25", "v26", "v27", "v28", "v29", "v30", "v31", "v32", "v33", "v34", "v35", "v36", "v37", "v38", "v39", "v40", "v41", "v42", "v43", "v44", "v45", "v46", "v47", "v48", "v49", "v50", "v51", "v52", "v53", "v54", "v55", "v56", "v57", "v58", "v59", "v60", "v61", "v62", "v63", "v64", "v65", "v66", "v67", "v68", "v69", "v70", "v71", "v72", "v73", "v74", "v75", "v76", "v77", "v78", "v79", "v80", "v81", "v82", "v83", "v84", "v85", "v86", "v87", "v88", "v89", "v90", "v91", "v92", "v93", "v94", "v95", "v96", "v97", "v98", "v99", "v100", "v101", "v102", "v103", "v104", "v105", "v106", "v107", "v108", "v109", "v110", "v111", "v112", "v113", "v114", "v115", "v116", "v117", "v118", "v119", "v120", "v121", "v122", "v123", "v124", "v125", "v126", "v127", "v128", "v129", "v130", "v131", "v132", "v133", "v134", "v135", "v136", "v137", "v138", "v139", "v140", "v141", "v142", "v143", "v144", "v145", "v146", "v147", "v148", "v149", "v150", "v151", "v152", "v153", "v154", "v155", "v156", "v157", "v158", "v159", "v160", "v161", "v162", "v163", "v164", "v165", "v166", "v167", "v168", "v169", "v170", "v171", "v172", "v173", "v174", "v175", "v176", "v177", "v178", "v179", "v180", "v181", "v182", "v183", "v184", "v185", "v186", "v187", "v188", "v189", "v190", "v191", "v192", "v193", "v194", "v195", "v196", "v197", "v198", "v199", "v200", "v201", "v202", "v203", "v204", "v205", "v206", "v207", "v208", "v209", "v210", "v211", "v212", "v213", "v214", "v215", "v216", "v217", "v218", "v219", "v220", "v221", "v222", "v223", "v224", "v225", "v226", "v227", "v228", "v229", "v230", "v231", "v232", "v233", "v234", "v235", "v236", "v237", "v238", "v239", "v240", "v241", "v242", "v243", "v244", "v245", "v246", "v247", "v248", "v249", "v250", "v251", "v252", "v253", "v254", "v255",
        "a0", "a1", "a2", "a3", "a4", "a5", "a6", "a7", "a8", "a9", "a10", "a11", "a12", "a13", "a14", "a15", "a16", "a17", "a18", "a19", "a20", "a21", "a22", "a23", "a24", "a25", "a26", "a27", "a28", "a29", "a30", "a31", "a32", "a33", "a34", "a35", "a36", "a37", "a38", "a39", "a40", "a41", "a42", "a43", "a44", "a45", "a46", "a47", "a48", "a49", "a50", "a51", "a52", "a53", "a54", "a55", "a56", "a57", "a58", "a59", "a60", "a61", "a62", "a63", "a64", "a65", "a66", "a67", "a68", "a69", "a70", "a71", "a72", "a73", "a74", "a75", "a76", "a77", "a78", "a79", "a80", "a81", "a82", "a83", "a84", "a85", "a86", "a87", "a88", "a89", "a90", "a91", "a92", "a93", "a94", "a95", "a96", "a97", "a98", "a99", "a100", "a101", "a102", "a103", "a104", "a105", "a106", "a107", "a108", "a109", "a110", "a111", "a112", "a113", "a114", "a115", "a116", "a117", "a118", "a119", "a120", "a121", "a122", "a123", "a124", "a125", "a126", "a127", "a128", "a129", "a130", "a131", "a132", "a133", "a134", "a135", "a136", "a137", "a138", "a139", "a140", "a141", "a142", "a143", "a144", "a145", "a146", "a147", "a148", "a149", "a150", "a151", "a152", "a153", "a154", "a155", "a156", "a157", "a158", "a159", "a160", "a161", "a162", "a163", "a164", "a165", "a166", "a167", "a168", "a169", "a170", "a171", "a172", "a173", "a174", "a175", "a176", "a177", "a178", "a179", "a180", "a181", "a182", "a183", "a184", "a185", "a186", "a187", "a188", "a189", "a190", "a191", "a192", "a193", "a194", "a195", "a196", "a197", "a198", "a199", "a200", "a201", "a202", "a203", "a204", "a205", "a206", "a207", "a208", "a209", "a210", "a211", "a212", "a213", "a214", "a215", "a216", "a217", "a218", "a219", "a220", "a221", "a222", "a223", "a224", "a225", "a226", "a227", "a228", "a229", "a230", "a231", "a232", "a233", "a234", "a235", "a236", "a237", "a238", "a239", "a240", "a241", "a242", "a243", "a244", "a245", "a246", "a247", "a248", "a249", "a250", "a251", "a252", "a253", "a254", "a255",
        "s8", "s9", "s10", "s11", "s12", "s13", "s14", "s15", "s16", "s17", "s18", "s19", "s20", "s21", "s22", "s23", "s24", "s25", "s26", "s27", "s28", "s29", "s30", "s31", "s32", "s33", "s34", "s35", "s36", "s37", "s38", "s39", "s40", "s41", "s42", "s43", "s44", "s45", "s46", "s47", "s48", "s49", "s50", "s51", "s52", "s53", "s54", "s55", "s56", "s57", "s58", "s59", "s60", "s61", "s62", "s63", "s64", "s65", "s66", "s67", "s68", "s69", "s70", "s71", "s72", "s73", "s74", "s75", "s76", "s77", "s78", "s79", "s80", "s81", "s82", "s83", "s84", "s85", "s86", "s87");
}

extern "C" void kernel_launch(void* const* d_in, const int* in_sizes, int n_in, void* d_out, int out_size, void* d_ws, size_t ws_size,
                              hipStream_t stream) {
  fused_k<<<256, 256, 0, stream>>>((const u16*)d_in[1], (const u16*)d_in[2], (const u16*)d_in[3], (const u16*)d_in[4], (const u16*)d_in[0],
                                   (char*)d_ws, (u16*)d_out, (const u16*)d_in[6], (const u16*)d_in[10], (const u16*)d_in[7],
                                   (const u16*)d_in[8], (const u16*)d_in[9]);
}
```

```cpp
#include <hip/hip_runtime.h>

typedef unsigned short u16;

__global__ __launch_bounds__(256, 1) void fused_k(const u16* wsre, const u16* wsim, const u16* wdre, const u16* wdim, const u16* x, char* ws,
                                               u16* out, const u16* w_r, const u16* gate_w, const u16* w_gate, const u16* w_up,
                                               const u16* w_down) {
  __shared__ __attribute__((aligned(16))) char smem[163840];
  asm volatile(
      ".Lbegin_%=:\n"
      "s_load_dwordx16 s[8:23], %0, 0\n"
      "s_load_dwordx8 s[24:31], %0, 64\n"
      "v_mbcnt_lo_u32_b32 v243, -1, 0\n"
      "s_mov_b32 s32, %1\n"
      "v_mbcnt_hi_u32_b32 v243, -1, v243\n"
      "s_mov_b32 s33, %2\n"
      "s_bfe_u32 s34, s32, 0x20001\n"
      "s_bfe_u32 s35, s32, 0x20003\n"
      "v_lshrrev_b32 v236, 3, v243\n"
      "s_lshr_b32 s43, s32, 5\n"
      "v_xor_b32 v237, v243, v236\n"
      "s_and_b32 s36, s32, 1\n"
      "v_and_b32 v237, 7, v237\n"
      "s_lshl3_add_u32 s36, s36, s43\n"
      "v_lshlrev_b32 v237, 4, v237\n"
      "s_add_u32 s44, s43, s35\n"
      "v_lshlrev_b32 v242, 4, v243\n"
      "s_lshl_b32 s41, s33, 14\n"
      "s_bitcmp1_b32 s44, 0\n"
      "s_cbranch_scc1 .Lstream_%=\n"
      "v_and_b32 v238, 3, v236\n"
      "s_lshr_b32 s43, s43, 1\n"
      "v_and_b32 v239, 4, v236\n"
      "s_add_u32 s80, s43, s35\n"
      "v_lshl_or_b32 v238, v239, 1, v238\n"
      "s_lshl_b32 s80, s80, 8\n"
      "v_lshl_or_b32 v240, v238, 10, v237\n"
      "s_and_b32 s80, s80, 0x300\n"
      "v_add_u32 v241, 15360, v240\n"
      "s_lshr_b32 s83, s36, 1\n"
      "v_add_u32 v254, 2048, v240\n"
      "s_and_b32 s44, s34, 1\n"
      "v_add_u32 v255, 17408, v240\n"
      "s_lshl3_add_u32 s44, s44, s83\n"
      "s_lshl2_add_u32 s40, s44, s33\n"
      "s_lshl_b32 s44, s40, 15\n"
      "s_lshl_b32 s81, s33, 8\n"
      "s_add_u32 s81, s81, s80\n"
      "s_and_b32 s81, s81, 0x380\n"
      "s_lshl_b32 s45, s35, 17\n"
      "s_lshl_b32 s82, s33, 12\n"
      "s_add_u32 s76, s34, 1\n"
      "s_and_b32 s76, s76, 3\n"
      "s_lshl_b32 s76, s76, 15\n"
      "s_or_b32 s76, s76, s81\n"
      "s_add_u32 s76, s76, s45\n"
      "s_add_u32 s77, s34, 2\n"
      "s_and_b32 s77, s77, 3\n"
      "s_lshl_b32 s77, s77, 15\n"
      "s_or_b32 s77, s77, s81\n"
      "s_add_u32 s77, s77, s45\n"
      "s_add_u32 s78, s44, s80\n"
      "s_add_u32 s79, s80, 256\n"
      "s_and_b32 s79, s79, 0x300\n"
      "s_add_u32 s79, s79, s44\n"
      "s_add_u32 m0, s82, 65536\n"
      "s_bitcmp1_b32 s34, 1\n"
      "s_waitcnt lgkmcnt(0)\n"
      "s_cselect_b64 s[12:13], s[14:15], s[12:13]\n"
      "s_add_u32 s46, s12, s76\n"
      "s_addc_u32 s47, s13, 0\n"
      "global_load_lds_dwordx4 v240, s[46:47] offset:0\n"
      "global_load_lds_dwordx4 v241, s[46:47] offset:1024\n"
      "global_load_lds_dwordx4 v254, s[46:47] offset:2048\n"
      "global_load_lds_dwordx4 v255, s[46:47] offset:3072\n"
      "s_add_u32 s70, s16, s78\n"
      "s_addc_u32 s71, s17, 0\n"
      "s_mov_b32 m0, s41\n"
      "s_add_u32 s46, s12, s77\n"
      "s_addc_u32 s47, s13, 0\n"
      "global_load_lds_dwordx4 v240, s[70:71] offset:0\n"
      "global_load_lds_dwordx4 v241, s[70:71] offset:1024\n"
      "global_load_lds_dwordx4 v254, s[70:71] offset:2048\n"
      "global_load_lds_dwordx4 v255, s[70:71] offset:3072\n"
      "s_add_u32 m0, s82, 98304\n"
      "s_nop 0\n"
      "global_load_lds_dwordx4 v240, s[46:47] offset:0\n"
      "global_load_lds_dwordx4 v241, s[46:47] offset:1024\n"
      "s_add_u32 s70, s16, s79\n"
      "s_addc_u32 s71, s17, 0\n"
      "s_add_u32 m0, s41, 4096\n"
      "s_nop 0\n"
      "global_load_lds_dwordx4 v240, s[70:71] offset:0\n"
      "global_load_lds_dwordx4 v241, s[70:71] offset:1024\n"
      "global_load_lds_dwordx4 v254, s[70:71] offset:2048\n"
      "global_load_lds_dwordx4 v255, s[70:71] offset:3072\n"
      "s_branch .Lprod_%=\n"
      ".Lstream_%=:\n"
      "v_lshl_or_b32 v244, v236, 12, v237\n"
      "v_add_u32 v245, 31744, v244\n"
      "v_add_u32 v246, 63488, v244\n"
      "v_add_u32 v247, 95232, v244\n"
      "s_getpc_b64 s[46:47]\n"
      ".Lhere_%=:\n"
      "s_lshr_b32 s44, s32, 4\n"
      "s_lshl2_add_u32 s44, s44, s33\n"
      "s_lshl_b32 s44, s44, 10\n"
      "s_sub_u32 s46, s46, .Lhere_%=-.Lbegin_%=\n"
      "s_subb_u32 s47, s47, 0\n"
      "v_add_u32 v238, s44, v242\n"
      "v_min_u32 v238, .Lend_%=-.Lbegin_%=-16, v238\n"
      "global_load_dwordx4 v[192:195], v238, s[46:47]\n"
      "s_and_b32 s44, s34, 1\n"
      "s_lshr_b32 s45, s36, 1\n"
      "s_lshl3_add_u32 s44, s44, s45\n"
      "s_lshl2_add_u32 s44, s44, s35\n"
      "s_lshl2_add_u32 s44, s44, s33\n"
      "s_lshl_b32 s44, s44, 13\n"
      "s_add_u32 s44, s44, 128\n"
      "v_and_b32 v233, 31, v243\n"
      "v_lshl_add_u32 v233, v233, 8, s44\n"
      "s_lshr_b32 s44, s33, 1\n"
      "s_add_u32 s44, s44, s34\n"
      "s_add_u32 s44, s44, 1\n"
      "s_and_b32 s44, s44, 3\n"
      "s_lshl2_add_u32 s44, s35, s44\n"
      "s_lshr_b32 s45, s32, 6\n"
      "s_lshl2_add_u32 s44, s44, s45\n"
      "s_and_b32 s45, s33, 1\n"
      "s_lshl1_add_u32 s44, s44, s45\n"
      "s_lshl_b32 s44, s44, 12\n"
      "s_add_u32 s44, s44, 128\n"
      "v_and_b32 v234, 15, v243\n"
      "v_lshl_add_u32 v234, v234, 8, s44\n"
      "s_bitcmp1_b32 s34, 1\n"
      "s_waitcnt lgkmcnt(0)\n"
      "s_cselect_b64 s[68:69], s[14:15], s[12:13]\n"
      "s_bitcmp1_b32 s34, 1\n"
      "s_cselect_b64 s[8:9], s[10:11], s[8:9]\n"
      "s_and_b32 s45, s34, 1\n"
      "s_lshl2_add_u32 s45, s45, s33\n"
      "s_lshl_b32 s45, s45, 9\n"
      "s_lshl_b32 s76, s36, 19\n"
      "s_add_u32 s45, s45, s76\n"
      "s_add_u32 s8, s8, s45\n"
      "s_addc_u32 s9, s9, 0\n"
      "s_lshr_b32 s39, s32, 6\n"
      "s_and_b32 s45, s35, 2\n"
      "s_xor_b32 s39, s39, s45\n"
      "s_xor_b32 s39, s39, s33\n"
      "s_lshl_b32 s45, s39, 7\n"
      "s_add_u32 s76, s33, 0\n"
      "s_and_b32 s76, s76, 3\n"
      "s_lshl_b32 s76, s76, 17\n"
      "s_or_b32 s76, s76, s45\n"
      "s_add_u32 s77, s33, 1\n"
      "s_and_b32 s77, s77, 3\n"
      "s_lshl_b32 s77, s77, 17\n"
      "s_or_b32 s77, s77, s45\n"
      "s_add_u32 s78, s33, 2\n"
      "s_and_b32 s78, s78, 3\n"
      "s_lshl_b32 s78, s78, 17\n"
      "s_or_b32 s78, s78, s45\n"
      "s_add_u32 s79, s33, 3\n"
      "s_and_b32 s79, s79, 3\n"
      "s_lshl_b32 s79, s79, 17\n"
      "s_or_b32 s79, s79, s45\n"
      "v_mov_b32 v239, 0\n"
      "global_load_dword v199, v239, s[8:9]\n"
      "s_add_u32 s58, s18, 4718592\n"
      "s_addc_u32 s59, s19, 0\n"
      "global_load_dword v235, v239, s[58:59] sc1\n"
      "s_lshl2_add_u32 s43, s34, s35\n"
      "s_mul_i32 s43, s43, 266240\n"
      "s_add_u32 s43, s43, 262144\n"
      "s_lshl2_add_u32 s44, s36, s35\n"
      "s_mul_i32 s44, s44, 147712\n"
      "s_add_u32 s44, s44, 6438912\n"
      "s_bitcmp1_b32 s33, 0\n"
      "s_cselect_b32 s43, s43, s44\n"
      "s_cselect_b64 s[46:47], s[22:23], s[24:25]\n"
      "s_cselect_b64 s[70:71], s[28:29], s[26:27]\n"
      "s_lshl_b32 s44, s36, 17\n"
      "s_mul_i32 s45, s32, 7168\n"
      "s_bitcmp1_b32 s33, 1\n"
      "s_cselect_b64 s[46:47], s[46:47], s[18:19]\n"
      "s_cselect_b32 s43, 0, s43\n"
      "s_cselect_b32 s84, 0x380, 0\n"
      "v_lshlrev_b32 v238, 7, v243\n"
      "v_and_b32 v238, s84, v238\n"
      "v_add_u32 v238, s43, v238\n"
      "global_load_dword v197, v238, s[46:47] sc1\n"
      "s_cmp_eq_u32 s33, 2\n"
      "s_cselect_b64 s[70:71], s[30:31], s[70:71]\n"
      "s_cmp_eq_u32 s33, 3\n"
      "s_cselect_b64 s[70:71], s[20:21], s[70:71]\n"
      "s_cselect_b32 s44, s44, s45\n"
      "v_mov_b32 v239, s44\n"
      "global_load_dword v198, v239, s[70:71] sc1\n"
      "s_sleep 8\n"
      "global_load_dword v200, v234, s[68:69]\n"
      "global_load_dword v201, v233, s[16:17]\n"
      "s_sleep 4\n"
      "s_bitcmp1_b32 s34, 1\n"
      "s_cselect_b64 s[46:47], s[14:15], s[12:13]\n"
      "s_min_u32 s44, s33, 2\n"
      "s_add_u32 s44, s44, s34\n"
      "s_add_u32 s44, s44, 2\n"
      "s_and_b32 s44, s44, 3\n"
      "s_lshl2_add_u32 s44, s35, s44\n"
      "s_lshr_b32 s45, s32, 6\n"
      "s_lshl2_add_u32 s44, s44, s45\n"
      "s_lshl_b32 s44, s44, 13\n"
      "v_lshl_add_u32 v238, v243, 7, s44\n"
      "s_cmp_eq_u32 s33, 0\n"
      "s_cselect_b32 s44, 4096, 0\n"
      "v_or_b32 v238, s44, v238\n"
      "s_cmp_eq_u32 s33, 3\n"
      "s_cbranch_scc1 .Ls_e1_%=\n"
      "global_load_dword v196, v238, s[46:47]\n"
      ".Ls_e1_%=:\n"
      "s_xor_b32 s45, s76, 0\n"
      "s_add_u32 s46, s8, s45\n"
      "s_addc_u32 s47, s9, 0\n"
      "s_add_u32 m0, s41, 0\n"
      "s_nop 0\n"
      "global_load_lds_dwordx4 v244, s[46:47] offset:0\n"
      "global_load_lds_dwordx4 v245, s[46:47] offset:1024\n"
      "global_load_lds_dwordx4 v246, s[46:47] offset:2048\n"
      "global_load_lds_dwordx4 v247, s[46:47] offset:3072\n"
      "s_xor_b32 s45, s77, 0\n"
      "s_add_u32 s46, s8, s45\n"
      "s_addc_u32 s47, s9, 0\n"
      "s_add_u32 m0, s41, 4096\n"
      "s_nop 0\n"
      "global_load_lds_dwordx4 v244, s[46:47] offset:0\n"
      "global_load_lds_dwordx4 v245, s[46:47] offset:1024\n"
      "global_load_lds_dwordx4 v246, s[46:47] offset:2048\n"
      "global_load_lds_dwordx4 v247, s[46:47] offset:3072\n"
      "s_xor_b32 s45, s78, 0\n"
      "s_add_u32 s46, s8, s45\n"
      "s_addc_u32 s47, s9, 0\n"
      "s_add_u32 m0, s41, 8192\n"
      "s_nop 0\n"
      "global_load_lds_dwordx4 v244, s[46:47] offset:0\n"
      "global_load_lds_dwordx4 v245, s[46:47] offset:1024\n"
      "global_load_lds_dwordx4 v246, s[46:47] offset:2048\n"
      "global_load_lds_dwordx4 v247, s[46:47] offset:3072\n"
      "v_and_b32 v238, 15, v243\n"
      "v_lshrrev_b32 v239, 4, v243\n"
      "v_xor_b32 v239, v239, v238\n"
      "v_and_b32 v239, 7, v239\n"
      "v_lshlrev_b32 v239, 4, v239\n"
      "v_lshl_or_b32 v238, v238, 7, v239\n"
      "v_add_u32 v248, s41, v238\n"
      "v_xor_b32 v249, 64, v248\n"
      "s_lshl2_add_u32 s43, s34, s35\n"
      "s_lshr_b32 s45, s39, 1\n"
      "s_lshl1_add_u32 s45, s33, s45\n"
      "s_lshl_b32 s45, s45, 15\n"
      "s_mul_i32 s44, s43, 266240\n"
      "s_add_u32 s44, s44, 4096\n"
      "s_add_u32 s14, s18, s44\n"
      "s_addc_u32 s15, s19, 0\n"
      "s_add_u32 s10, s14, s45\n"
      "s_addc_u32 s11, s15, 0\n"
      "s_xor_b32 s45, s45, 0x8000\n"
      "s_add_u32 s14, s14, s45\n"
      "s_addc_u32 s15, s15, 0\n"
      "s_and_b32 s44, s39, 1\n"
      "s_lshl_b32 s45, s44, 14\n"
      "v_add_u32 v240, s45, v242\n"
      "v_xor_b32 v241, 0x4000, v240\n"
      "s_add_u32 s72, s10, 0x2000\n"
      "s_addc_u32 s73, s11, 0\n"
      "s_add_u32 s74, s14, 0x2000\n"
      "s_addc_u32 s75, s15, 0\n"
      "s_lshl2_add_u32 s44, s43, s33\n"
      "s_lshl_b32 s44, s44, 12\n"
      "s_add_u32 s44, s44, 1048576\n"
      "v_and_b32 v253, 7, v243\n"
      "v_lshl_add_u32 v253, v253, 9, s44\n"
      "s_lshl2_add_u32 s43, s36, s35\n"
      "s_mul_i32 s43, s43, 147712\n"
      "s_mul_i32 s44, s33, 3072\n"
      "s_add_u32 s43, s43, s44\n"
      "s_add_u32 s43, s43, 6291456\n"
      "s_add_u32 s60, s18, s43\n"
      "s_addc_u32 s61, s19, 0\n"
      "s_mul_i32 s44, s34, 36864\n"
      "s_add_u32 s62, s60, s44\n"
      "s_addc_u32 s63, s61, 0\n"
      "s_lshr_b32 s43, s34, 1\n"
      "s_lshl2_add_u32 s43, s43, s35\n"
      "s_lshl_b32 s43, s43, 12\n"
      "s_add_u32 s43, s43, 4096\n"
      "v_lshl_add_u32 v204, v243, 6, s43\n"
      "s_lshr_b32 s80, s32, 4\n"
      "s_bfe_u32 s43, s32, 0x10002\n"
      "s_lshl1_add_u32 s80, s80, s43\n"
      "s_and_b32 s81, s32, 3\n"
      "s_cmp_lt_u32 s80, 28\n"
      "s_cselect_b32 s86, -1, 0\n"
      "s_lshr_b32 s83, s33, 1\n"
      "s_lshl1_add_u32 s83, s81, s83\n"
      "v_add_u32 v250, 0x10000, v248\n"
      "v_add_u32 v251, 0x10000, v249\n"
      "s_xor_b32 s45, s79, 0\n"
      "s_add_u32 s46, s8, s45\n"
      "s_addc_u32 s47, s9, 0\n"
      "s_add_u32 m0, s41, 12288\n"
      "s_nop 0\n"
      "global_load_lds_dwordx4 v244, s[46:47] offset:0\n"
      "global_load_lds_dwordx4 v245, s[46:47] offset:1024\n"
      "global_load_lds_dwordx4 v246, s[46:47] offset:2048\n"
      "global_load_lds_dwordx4 v247, s[46:47] offset:3072\n"
      "s_xor_b32 s45, s76, 128\n"
      "s_add_u32 s46, s8, s45\n"
      "s_addc_u32 s47, s9, 0\n"
      "s_add_u32 m0, s41, 65536\n"
      "s_nop 0\n"
      "global_load_lds_dwordx4 v244, s[46:47] offset:0\n"
      "global_load_lds_dwordx4 v245, s[46:47] offset:1024\n"
      "global_load_lds_dwordx4 v246, s[46:47] offset:2048\n"
      "global_load_lds_dwordx4 v247, s[46:47] offset:3072\n"
      "s_waitcnt vmcnt(16)\n"
      "ds_read_b128 a[0:3], v248 offset:0\n"
      "ds_read_b128 a[4:7], v248 offset:2048\n"
      "ds_read_b128 a[32:35], v249 offset:0\n"
      "ds_read_b128 a[36:39], v249 offset:2048\n"
      "s_xor_b32 s45, s77, 128\n"
      "s_add_u32 s46, s8, s45\n"
      "s_addc_u32 s47, s9, 0\n"
      "s_add_u32 m0, s41, 69632\n"
      "s_nop 0\n"
      "global_load_lds_dwordx4 v244, s[46:47] offset:0\n"
      "global_load_lds_dwordx4 v245, s[46:47] offset:1024\n"
      "global_load_lds_dwordx4 v246, s[46:47] offset:2048\n"
      "global_load_lds_dwordx4 v247, s[46:47] offset:3072\n"
      "s_waitcnt vmcnt(16)\n"
      "ds_read_b128 a[8:11], v248 offset:4096\n"
      "ds_read_b128 a[12:15], v248 offset:6144\n"
      "ds_read_b128 a[40:43], v249 offset:4096\n"
      "ds_read_b128 a[44:47], v249 offset:6144\n"
      "s_xor_b32 s45, s78, 128\n"
      "s_add_u32 s46, s8, s45\n"
      "s_addc_u32 s47, s9, 0\n"
      "s_add_u32 m0, s41, 73728\n"
      "s_nop 0\n"
      "global_load_lds_dwordx4 v244, s[46:47] offset:0\n"
      "global_load_lds_dwordx4 v245, s[46:47] offset:1024\n"
      "global_load_lds_dwordx4 v246, s[46:47] offset:2048\n"
      "global_load_lds_dwordx4 v247, s[46:47] offset:3072\n"
      "s_waitcnt vmcnt(16)\n"
      "ds_read_b128 a[16:19], v248 offset:8192\n"
      "ds_read_b128 a[20:23], v248 offset:10240\n"
      "ds_read_b128 a[48:51], v249 offset:8192\n"
      "ds_read_b128 a[52:55], v249 offset:10240\n"
      "v_readfirstlane_b32 s37, v235\n"
      "s_nop 1\n"
      "s_mul_i32 s37, s37, 0x9E3779B1\n"
      "s_add_u32 s37, s37, 0x7F4A7C15\n"
      "s_mov_b32 s38, 0x8000\n"
      "s_waitcnt lgkmcnt(8)\n"
      "s_xor_b32 s45, s79, 128\n"
      "s_add_u32 s46, s8, s45\n"
      "s_addc_u32 s47, s9, 0\n"
      "s_add_u32 m0, s41, 0\n"
      "s_nop 0\n"
      "global_load_lds_dwordx4 v244, s[46:47] offset:0\n"
      "global_load_lds_dwordx4 v245, s[46:47] offset:1024\n"
      "global_load_lds_dwordx4 v246, s[46:47] offset:2048\n"
      "global_load_lds_dwordx4 v247, s[46:47] offset:3072\n"
      "s_barrier\n"
      "s_waitcnt vmcnt(16)\n"
      "ds_read_b128 a[24:27], v248 offset:12288\n"
      "ds_read_b128 a[28:31], v248 offset:14336\n"
      "ds_read_b128 a[56:59], v249 offset:12288\n"
      "ds_read_b128 a[60:63], v249 offset:14336\n"
      "s_waitcnt lgkmcnt(8)\n"
      "s_xor_b32 s45, s76, 256\n"
      "s_add_u32 s46, s8, s45\n"
      "s_addc_u32 s47, s9, 0\n"
      "s_add_u32 m0, s41, 4096\n"
      "s_nop 0\n"
      "global_load_lds_dwordx4 v244, s[46:47] offset:0\n"
      "global_load_lds_dwordx4 v245, s[46:47] offset:1024\n"
      "global_load_lds_dwordx4 v246, s[46:47] offset:2048\n"
      "global_load_lds_dwordx4 v247, s[46:47] offset:3072\n"
      "s_waitcnt vmcnt(16)\n"
      "ds_read_b128 a[64:67], v250 offset:0\n"
      "ds_read_b128 a[68:71], v250 offset:2048\n"
      "ds_read_b128 a[96:99], v251 offset:0\n"
      "ds_read_b128 a[100:103], v251 offset:2048\n"
      "s_waitcnt lgkmcnt(8)\n"
      "s_xor_b32 s45, s77, 256\n"
      "s_add_u32 s46, s8, s45\n"
      "s_addc_u32 s47, s9, 0\n"
      "s_add_u32 m0, s41, 8192\n"
      "s_nop 0\n"
      "global_load_lds_dwordx4 v244, s[46:47] offset:0\n"
      "global_load_lds_dwordx4 v245, s[46:47] offset:1024\n"
      "global_load_lds_dwordx4 v246, s[46:47] offset:2048\n"
      "global_load_lds_dwordx4 v247, s[46:47] offset:3072\n"
      "s_waitcnt vmcnt(16)\n"
      "ds_read_b128 a[72:75], v250 offset:4096\n"
      "ds_read_b128 a[76:79], v250 offset:6144\n"
      "ds_read_b128 a[104:107], v251 offset:4096\n"
      "ds_read_b128 a[108:111], v251 offset:6144\n"
      "s_waitcnt lgkmcnt(8)\n"
      "s_xor_b32 s45, s78, 256\n"
      "s_add_u32 s46, s8, s45\n"
      "s_addc_u32 s47, s9, 0\n"
      "s_add_u32 m0, s41, 12288\n"
      "s_nop 0\n"
      "global_load_lds_dwordx4 v244, s[46:47] offset:0\n"
      "global_load_lds_dwordx4 v245, s[46:47] offset:1024\n"
      "global_load_lds_dwordx4 v246, s[46:47] offset:2048\n"
      "global_load_lds_dwordx4 v247, s[46:47] offset:3072\n"
      "s_waitcnt vmcnt(16)\n"
      "ds_read_b128 a[80:83], v250 offset:8192\n"
      "ds_read_b128 a[84:87], v250 offset:10240\n"
      "ds_read_b128 a[112:115], v251 offset:8192\n"
      "ds_read_b128 a[116:119], v251 offset:10240\n"
      "s_waitcnt vmcnt(12)\n"
      "ds_read_b128 a[88:91], v248 offset:0\n"
      "ds_read_b128 a[92:95], v248 offset:2048\n"
      "ds_read_b128 a[120:123], v249 offset:0\n"
      "ds_read_b128 a[124:127], v249 offset:2048\n"
      "global_load_dword v252, v253, s[58:59] offset:0 sc1\n"
      "s_waitcnt lgkmcnt(12)\n"
      "s_xor_b32 s45, s79, 256\n"
      "s_add_u32 s46, s8, s45\n"
      "s_addc_u32 s47, s9, 0\n"
      "s_add_u32 m0, s41, 65536\n"
      "s_nop 0\n"
      "global_load_lds_dwordx4 v244, s[46:47] offset:0\n"
      "global_load_lds_dwordx4 v245, s[46:47] offset:1024\n"
      "global_load_lds_dwordx4 v246, s[46:47] offset:2048\n"
      "global_load_lds_dwordx4 v247, s[46:47] offset:3072\n"
      "s_waitcnt lgkmcnt(8)\n"
      "s_xor_b32 s45, s76, 384\n"
      "s_add_u32 s46, s8, s45\n"
      "s_addc_u32 s47, s9, 0\n"
      "s_add_u32 m0, s41, 69632\n"
      "s_nop 0\n"
      "global_load_lds_dwordx4 v244, s[46:47] offset:0\n"
      "global_load_lds_dwordx4 v245, s[46:47] offset:1024\n"
      "global_load_lds_dwordx4 v246, s[46:47] offset:2048\n"
      "global_load_lds_dwordx4 v247, s[46:47] offset:3072\n"
      "s_waitcnt vmcnt(17)\n"
      "ds_read_b128 a[128:131], v248 offset:4096\n"
      "ds_read_b128 a[132:135], v248 offset:6144\n"
      "ds_read_b128 a[160:163], v249 offset:4096\n"
      "ds_read_b128 a[164:167], v249 offset:6144\n"
      "s_waitcnt vmcnt(13)\n"
      "ds_read_b128 a[136:139], v248 offset:8192\n"
      "ds_read_b128 a[140:143], v248 offset:10240\n"
      "ds_read_b128 a[168:171], v249 offset:8192\n"
      "ds_read_b128 a[172:175], v249 offset:10240\n"
      "s_barrier\n"
      "s_waitcnt vmcnt(8)\n"
      "v_cmp_ne_u32 vcc, s37, v252\n"
      "s_nop 1\n"
      "s_cbranch_vccnz .Ls_s3_%=\n"
      ".Ls_o4_%=:\n"
      "global_load_dwordx2 v[206:207], v204, s[58:59] sc1\n"
      "global_load_dwordx4 v[128:131], v240, s[10:11] offset:-4096 sc1\n"
      "global_load_dwordx4 v[132:135], v240, s[10:11] offset:-3072 sc1\n"
      "global_load_dwordx4 v[136:139], v240, s[10:11] offset:-2048 sc1\n"
      "global_load_dwordx4 v[140:143], v240, s[72:73] offset:-4096 sc1\n"
      "global_load_dwordx4 v[144:147], v240, s[72:73] offset:-3072 sc1\n"
      "global_load_dwordx4 v[148:151], v240, s[72:73] offset:-2048 sc1\n"
      "s_waitcnt lgkmcnt(12)\n"
      "s_xor_b32 s45, s77, 384\n"
      "s_add_u32 s46, s8, s45\n"
      "s_addc_u32 s47, s9, 0\n"
      "s_add_u32 m0, s41, 73728\n"
      "s_nop 0\n"
      "global_load_lds_dwordx4 v244, s[46:47] offset:0\n"
      "global_load_lds_dwordx4 v245, s[46:47] offset:1024\n"
      "global_load_lds_dwordx4 v246, s[46:47] offset:2048\n"
      "global_load_lds_dwordx4 v247, s[46:47] offset:3072\n"
      "s_waitcnt vmcnt(9)\n"
      "v_mfma_f32_16x16x32_bf16 v[0:3], v[128:131], a[0:3], 0\n"
      "global_load_dwordx4 v[152:155], v241, s[10:11] offset:-4096 sc1\n"
      "v_cmp_ne_u32 vcc, s37, v207\n"
      "s_nop 1\n"
      "s_cbranch_vccnz .Ls_s6_%=\n"
      ".Ls_o7_%=:\n"
      "v_mfma_f32_16x16x32_bf16 v[4:7], v[128:131], a[4:7], 0\n"
      "v_max_u32_dpp v206, v206, v206 quad_perm:[1,0,3,2] row_mask:0xf bank_mask:0xf\n"
      "s_nop 1\n"
      "v_max_u32_dpp v206, v206, v206 quad_perm:[2,3,0,1] row_mask:0xf bank_mask:0xf\n"
      "v_mfma_f32_16x16x32_bf16 v[8:11], v[128:131], a[8:11], 0\n"
      "global_load_dwordx4 v[156:159], v241, s[10:11] offset:-3072 sc1\n"
      "v_max_u32_dpp v206, v206, v206 row_half_mirror row_mask:0xf bank_mask:0xf\n"
      "v_mfma_f32_16x16x32_bf16 v[12:15], v[128:131], a[12:15], 0\n"
      "s_nop 0\n"
      "v_max_u32_dpp v206, v206, v206 row_mirror row_mask:0xf bank_mask:0xf\n"
      "v_mfma_f32_16x16x32_bf16 v[16:19], v[128:131], a[16:19], 0\n"
      "global_load_dwordx4 v[160:163], v241, s[10:11] offset:-2048 sc1\n"
      "v_mov_b32 v205, v206\n"
      "v_mfma_f32_16x16x32_bf16 v[20:23], v[128:131], a[20:23], 0\n"
      "s_nop 0\n"
      "v_permlane16_swap_b32 v206, v205\n"
      "v_mfma_f32_16x16x32_bf16 v[24:27], v[128:131], a[24:27], 0\n"
      "global_load_dwordx4 v[164:167], v241, s[72:73] offset:-4096 sc1\n"
      "v_max_u32 v206, v206, v205\n"
      "v_mfma_f32_16x16x32_bf16 v[28:31], v[128:131], a[28:31], 0\n"
      "v_mov_b32 v205, v206\n"
      "s_nop 1\n"
      "v_permlane32_swap_b32 v206, v205\n"
      "s_waitcnt vmcnt(12)\n"
      "v_mfma_f32_16x16x32_bf16 v[32:35], v[132:135], a[0:3], 0\n"
      "global_load_dwordx4 v[168:171], v241, s[72:73] offset:-3072 sc1\n"
      "v_max_u32 v206, v206, v205\n"
      "v_mfma_f32_16x16x32_bf16 v[36:39], v[132:135], a[4:7], 0\n"
      "v_readfirstlane_b32 s40, v206\n"
      "s_nop 1\n"
      "s_andn2_b32 s42, 0x7ff, s40\n"
      "v_mfma_f32_16x16x32_bf16 v[40:43], v[132:135], a[8:11], 0\n"
      "global_load_dwordx4 v[172:175], v241, s[72:73] offset:-2048 sc1\n"
      "s_lshl2_add_u32 s43, s32, s33\n"
      "s_cmp_eq_u32 s43, 960\n"
      "s_cbranch_scc0 .Ls_e8_%=\n"
      "s_lshl_b32 s43, s42, 10\n"
      "v_add_u32 v208, s43, v242\n"
      "global_load_dwordx4 v[216:219], v208, s[16:17]\n"
      "global_load_dwordx4 v[220:223], v242, s[24:25]\n"
      ".Ls_e8_%=:\n"
      "v_mfma_f32_16x16x32_bf16 v[44:47], v[132:135], a[12:15], 0\n"
      "v_mfma_f32_16x16x32_bf16 v[48:51], v[132:135], a[16:19], 0\n"
      "global_load_dwordx4 v[176:179], v240, s[14:15] offset:-4096 sc1\n"
      "v_mfma_f32_16x16x32_bf16 v[52:55], v[132:135], a[20:23], 0\n"
      "v_mfma_f32_16x16x32_bf16 v[56:59], v[132:135], a[24:27], 0\n"
      "global_load_dwordx4 v[180:183], v240, s[14:15] offset:-3072 sc1\n"
      "v_mfma_f32_16x16x32_bf16 v[60:63], v[132:135], a[28:31], 0\n"
      "s_waitcnt vmcnt(14)\n"
      "v_mfma_f32_16x16x32_bf16 v[64:67], v[136:139], a[0:3], 0\n"
      "global_load_dwordx4 v[184:187], v240, s[14:15] offset:-2048 sc1\n"
      "global_load_dwordx4 v[188:191], v240, s[74:75] offset:-4096 sc1\n"
      "v_mfma_f32_16x16x32_bf16 v[68:71], v[136:139], a[4:7], 0\n"
      "global_load_dwordx4 v[128:131], v240, s[74:75] offset:-3072 sc1\n"
      "global_load_dwordx4 v[132:135], v240, s[74:75] offset:-2048 sc1\n"
      "v_mfma_f32_16x16x32_bf16 v[72:75], v[136:139], a[8:11], 0\n"
      "s_waitcnt lgkmcnt(8)\n"
      "s_xor_b32 s70, s78, 384\n"
      "s_add_u32 s70, s8, s70\n"
      "s_addc_u32 s71, s9, 0\n"
      "s_add_u32 m0, s41, 0\n"
      "v_mfma_f32_16x16x32_bf16 v[76:79], v[136:139], a[12:15], 0\n"
      "global_load_lds_dwordx4 v244, s[70:71] offset:0\n"
      "v_mfma_f32_16x16x32_bf16 v[80:83], v[136:139], a[16:19], 0\n"
      "v_mfma_f32_16x16x32_bf16 v[84:87], v[136:139], a[20:23], 0\n"
      "global_load_lds_dwordx4 v245, s[70:71] offset:1024\n"
      "ds_read_b128 a[144:147], v248 offset:12288\n"
      "v_mfma_f32_16x16x32_bf16 v[88:91], v[136:139], a[24:27], 0\n"
      "ds_read_b128 a[148:151], v248 offset:14336\n"
      "ds_read_b128 a[176:179], v249 offset:12288\n"
      "v_mfma_f32_16x16x32_bf16 v[92:95], v[136:139], a[28:31], 0\n"
      "global_load_lds_dwordx4 v246, s[70:71] offset:2048\n"
      "global_load_dwordx4 v[136:139], v241, s[14:15] offset:-4096 sc1\n"
      "v_mfma_f32_16x16x32_bf16 v[0:3], v[140:143], a[32:35], v[0:3]\n"
      "ds_read_b128 a[180:183], v249 offset:14336\n"
      "ds_read_b128 a[152:155], v250 offset:0\n"
      "v_mfma_f32_16x16x32_bf16 v[4:7], v[140:143], a[36:39], v[4:7]\n"
      "global_load_lds_dwordx4 v247, s[70:71] offset:3072\n"
      "ds_read_b128 a[156:159], v250 offset:2048\n"
      "v_mfma_f32_16x16x32_bf16 v[8:11], v[140:143], a[40:43], v[8:11]\n"
      "s_waitcnt lgkmcnt(10)\n"
      "s_xor_b32 s70, s79, 384\n"
      "s_add_u32 s70, s8, s70\n"
      "s_addc_u32 s71, s9, 0\n"
      "s_add_u32 m0, s41, 4096\n"
      "v_mfma_f32_16x16x32_bf16 v[12:15], v[140:143], a[44:47], v[12:15]\n"
      "global_load_lds_dwordx4 v244, s[70:71] offset:0\n"
      "ds_read_b128 a[184:187], v251 offset:0\n"
      "v_mfma_f32_16x16x32_bf16 v[16:19], v[140:143], a[48:51], v[16:19]\n"
      "ds_read_b128 a[188:191], v251 offset:2048\n"
      "ds_read_b128 a[192:195], v250 offset:4096\n"
      "v_mfma_f32_16x16x32_bf16 v[20:23], v[140:143], a[52:55], v[20:23]\n"
      "global_load_lds_dwordx4 v245, s[70:71] offset:1024\n"
      "ds_read_b128 a[196:199], v250 offset:6144\n"
      "v_mfma_f32_16x16x32_bf16 v[24:27], v[140:143], a[56:59], v[24:27]\n"
      "ds_read_b128 a[224:227], v251 offset:4096\n"
      "ds_read_b128 a[228:231], v251 offset:6144\n"
      "v_mfma_f32_16x16x32_bf16 v[28:31], v[140:143], a[60:63], v[28:31]\n"
      "s_waitcnt vmcnt(23)\n"
      "global_load_lds_dwordx4 v246, s[70:71] offset:2048\n"
      "global_load_dwordx4 v[140:143], v241, s[74:75] offset:-4096 sc1\n"
      "v_mfma_f32_16x16x32_bf16 v[32:35], v[144:147], a[32:35], v[32:35]\n"
      "s_waitcnt vmcnt(21)\n"
      "ds_read_b128 a[200:203], v250 offset:8192\n"
      "v_mfma_f32_16x16x32_bf16 v[36:39], v[144:147], a[36:39], v[36:39]\n"
      "global_load_lds_dwordx4 v247, s[70:71] offset:3072\n"
      "ds_read_b128 a[204:207], v250 offset:10240\n"
      "v_mfma_f32_16x16x32_bf16 v[40:43], v[144:147], a[40:43], v[40:43]\n"
      "ds_read_b128 a[232:235], v251 offset:8192\n"
      "ds_read_b128 a[236:239], v251 offset:10240\n"
      "v_mfma_f32_16x16x32_bf16 v[44:47], v[144:147], a[44:47], v[44:47]\n"
      "v_mfma_f32_16x16x32_bf16 v[48:51], v[144:147], a[48:51], v[48:51]\n"
      "v_mfma_f32_16x16x32_bf16 v[52:55], v[144:147], a[52:55], v[52:55]\n"
      "v_mfma_f32_16x16x32_bf16 v[56:59], v[144:147], a[56:59], v[56:59]\n"
      "v_mfma_f32_16x16x32_bf16 v[60:63], v[144:147], a[60:63], v[60:63]\n"
      "global_load_dwordx4 v[144:147], v241, s[14:15] offset:-3072 sc1\n"
      "v_mfma_f32_16x16x32_bf16 v[64:67], v[148:151], a[32:35], v[64:67]\n"
      "v_mfma_f32_16x16x32_bf16 v[68:71], v[148:151], a[36:39], v[68:71]\n"
      "v_mfma_f32_16x16x32_bf16 v[72:75], v[148:151], a[40:43], v[72:75]\n"
      "v_mfma_f32_16x16x32_bf16 v[76:79], v[148:151], a[44:47], v[76:79]\n"
      "v_mfma_f32_16x16x32_bf16 v[80:83], v[148:151], a[48:51], v[80:83]\n"
      "v_mfma_f32_16x16x32_bf16 v[84:87], v[148:151], a[52:55], v[84:87]\n"
      "v_mfma_f32_16x16x32_bf16 v[88:91], v[148:151], a[56:59], v[88:91]\n"
      "v_mfma_f32_16x16x32_bf16 v[92:95], v[148:151], a[60:63], v[92:95]\n"
      "s_waitcnt vmcnt(21)\n"
      "global_load_dwordx4 v[148:151], v241, s[74:75] offset:-3072 sc1\n"
      "s_waitcnt vmcnt(7)\n"
      "ds_read_b128 a[208:211], v248 offset:0\n"
      "v_mfma_f32_16x16x32_bf16 v[0:3], v[152:155], a[64:67], v[0:3]\n"
      "ds_read_b128 a[212:215], v248 offset:2048\n"
      "ds_read_b128 a[240:243], v249 offset:0\n"
      "v_mfma_f32_16x16x32_bf16 v[4:7], v[152:155], a[68:71], v[4:7]\n"
      "ds_read_b128 a[244:247], v249 offset:2048\n"
      "v_mfma_f32_16x16x32_bf16 v[8:11], v[152:155], a[72:75], v[8:11]\n"
      "v_mfma_f32_16x16x32_bf16 v[12:15], v[152:155], a[76:79], v[12:15]\n"
      "v_mfma_f32_16x16x32_bf16 v[16:19], v[152:155], a[80:83], v[16:19]\n"
      "v_mfma_f32_16x16x32_bf16 v[20:23], v[152:155], a[84:87], v[20:23]\n"
      "v_mfma_f32_16x16x32_bf16 v[24:27], v[152:155], a[88:91], v[24:27]\n"
      "v_mfma_f32_16x16x32_bf16 v[28:31], v[152:155], a[92:95], v[28:31]\n"
      "global_load_dwordx4 v[152:155], v241, s[14:15] offset:-2048 sc1\n"
      "s_waitcnt vmcnt(3)\n"
      "ds_read_b128 a[216:219], v248 offset:4096\n"
      "v_mfma_f32_16x16x32_bf16 v[32:35], v[156:159], a[64:67], v[32:35]\n"
      "ds_read_b128 a[220:223], v248 offset:6144\n"
      "ds_read_b128 a[248:251], v249 offset:4096\n"
      "v_mfma_f32_16x16x32_bf16 v[36:39], v[156:159], a[68:71], v[36:39]\n"
      "ds_read_b128 a[252:255], v249 offset:6144\n"
      "v_mfma_f32_16x16x32_bf16 v[40:43], v[156:159], a[72:75], v[40:43]\n"
      "v_mfma_f32_16x16x32_bf16 v[44:47], v[156:159], a[76:79], v[44:47]\n"
      "v_mfma_f32_16x16x32_bf16 v[48:51], v[156:159], a[80:83], v[48:51]\n"
      "s_lshl_b32 s54, s42, 10\n"
      "s_lshl_b32 s55, s83, 7\n"
      "s_add_u32 s54, s54, s55\n"
      "v_mfma_f32_16x16x32_bf16 v[52:55], v[156:159], a[84:87], v[52:55]\n"
      "v_and_b32 v224, 48, v243\n"
      "v_add_u32 v224, s54, v224\n"
      "v_mfma_f32_16x16x32_bf16 v[56:59], v[156:159], a[88:91], v[56:59]\n"
      "global_load_dwordx4 v[192:195], v224, s[16:17]\n"
      "global_load_dwordx4 v[196:199], v224, s[16:17] offset:64\n"
      "v_mfma_f32_16x16x32_bf16 v[60:63], v[156:159], a[92:95], v[60:63]\n"
      "global_load_dwordx4 v[156:159], v241, s[74:75] offset:-2048 sc1\n"
      "v_mfma_f32_16x16x32_bf16 v[64:67], v[160:163], a[64:67], v[64:67]\n"
      "v_mfma_f32_16x16x32_bf16 v[68:71], v[160:163], a[68:71], v[68:71]\n"
      "v_mfma_f32_16x16x32_bf16 v[72:75], v[160:163], a[72:75], v[72:75]\n"
      "v_mfma_f32_16x16x32_bf16 v[76:79], v[160:163], a[76:79], v[76:79]\n"
      "v_mfma_f32_16x16x32_bf16 v[80:83], v[160:163], a[80:83], v[80:83]\n"
      "v_mfma_f32_16x16x32_bf16 v[84:87], v[160:163], a[84:87], v[84:87]\n"
      "v_mfma_f32_16x16x32_bf16 v[88:91], v[160:163], a[88:91], v[88:91]\n"
      "s_min_u32 s43, s80, 27\n"
      "v_lshrrev_b32 v202, 3, v243\n"
      "v_and_b32 v203, 0x70, v242\n"
      "s_movk_i32 s44, 3584\n"
      "v_mad_u32_u24 v200, v202, s44, v203\n"
      "v_add_u32 v201, 27648, v200\n"
      "v_add_u32 v202, 55296, v200\n"
      "v_add_u32 v203, 82944, v200\n"
      "s_bitcmp1_b32 s33, 0\n"
      "s_cselect_b64 s[70:71], s[28:29], s[26:27]\n"
      "s_mul_i32 s44, s83, 229376\n"
      "s_lshl_b32 s45, s43, 7\n"
      "s_add_u32 s44, s44, s45\n"
      "s_add_u32 s70, s70, s44\n"
      "s_addc_u32 s71, s71, 0\n"
      "s_lshl_b32 s45, s33, 13\n"
      "s_add_u32 m0, s45, 131072\n"
      "v_mfma_f32_16x16x32_bf16 v[92:95], v[160:163], a[92:95], v[92:95]\n"
      "global_load_lds_dwordx4 v200, s[70:71] offset:0\n"
      "s_lshl2_add_u32 s43, s32, s33\n"
      "s_cmp_eq_u32 s43, 960\n"
      "s_cbranch_scc0 .Ls_e9_%=\n"
      "s_ashr_i32 s43, s40, 31\n"
      "s_not_b32 s43, s43\n"
      "s_or_b32 s43, s43, 0x80000000\n"
      "s_xor_b32 s43, s43, s40\n"
      "s_and_b32 s43, s43, 0xffff0000\n"
      "v_lshlrev_b32 v208, 16, v216\n"
      "v_lshlrev_b32 v209, 16, v220\n"
      "v_and_b32 v216, 0xffff0000, v216\n"
      "v_and_b32 v220, 0xffff0000, v220\n"
      "v_mul_f32 v208, v208, v209\n"
      "v_mul_f32 v210, v216, v220\n"
      "v_cvt_pk_bf16_f32 v208, v208, v210\n"
      "v_lshlrev_b32 v209, 16, v208\n"
      "v_and_b32 v208, 0xffff0000, v208\n"
      "v_add_f32 v211, v208, v209\n"
      "v_lshlrev_b32 v208, 16, v217\n"
      "v_lshlrev_b32 v209, 16, v221\n"
      "v_and_b32 v217, 0xffff0000, v217\n"
      "v_and_b32 v221, 0xffff0000, v221\n"
      "v_mul_f32 v208, v208, v209\n"
      "v_mul_f32 v210, v217, v221\n"
      "v_cvt_pk_bf16_f32 v208, v208, v210\n"
      "v_lshlrev_b32 v209, 16, v208\n"
      "v_and_b32 v208, 0xffff0000, v208\n"
      "v_add_f32 v208, v208, v209\n"
      "v_add_f32 v211, v211, v208\n"
      "v_lshlrev_b32 v208, 16, v218\n"
      "v_lshlrev_b32 v209, 16, v222\n"
      "v_and_b32 v218, 0xffff0000, v218\n"
      "v_and_b32 v222, 0xffff0000, v222\n"
      "v_mul_f32 v208, v208, v209\n"
      "v_mul_f32 v210, v218, v222\n"
      "v_cvt_pk_bf16_f32 v208, v208, v210\n"
      "v_lshlrev_b32 v209, 16, v208\n"
      "v_and_b32 v208, 0xffff0000, v208\n"
      "v_add_f32 v208, v208, v209\n"
      "v_add_f32 v211, v211, v208\n"
      "v_lshlrev_b32 v208, 16, v219\n"
      "v_lshlrev_b32 v209, 16, v223\n"
      "v_and_b32 v219, 0xffff0000, v219\n"
      "v_and_b32 v223, 0xffff0000, v223\n"
      "v_mul_f32 v208, v208, v209\n"
      "v_mul_f32 v210, v219, v223\n"
      "v_cvt_pk_bf16_f32 v208, v208, v210\n"
      "v_lshlrev_b32 v209, 16, v208\n"
      "v_and_b32 v208, 0xffff0000, v208\n"
      "v_add_f32 v208, v208, v209\n"
      "v_add_f32 v211, v211, v208\n"
      "s_nop 1\n"
      "v_add_f32_dpp v211, v211, v211 quad_perm:[1,0,3,2] row_mask:0xf bank_mask:0xf\n"
      "s_nop 1\n"
      "v_add_f32_dpp v211, v211, v211 quad_perm:[2,3,0,1] row_mask:0xf bank_mask:0xf\n"
      "s_nop 1\n"
      "v_add_f32_dpp v211, v211, v211 row_half_mirror row_mask:0xf bank_mask:0xf\n"
      "s_nop 1\n"
      "v_add_f32_dpp v211, v211, v211 row_mirror row_mask:0xf bank_mask:0xf\n"
      "v_mov_b32 v208, v211\n"
      "s_nop 1\n"
      "v_permlane16_swap_b32 v211, v208\n"
      "v_add_f32 v211, v211, v208\n"
      "v_mov_b32 v208, v211\n"
      "s_nop 1\n"
      "v_permlane32_swap_b32 v211, v208\n"
      "v_add_f32 v211, v211, v208\n"
      "v_cvt_pk_bf16_f32 v211, v211, v211\n"
      "v_and_b32 v211, 0xffff0000, v211\n"
      "v_writelane_b32 v211, s43, 1\n"
      "v_mul_f32 v211, 0xbfb8aa3b, v211\n"
      "v_exp_f32 v211, v211\n"
      "s_nop 0\n"
      "v_add_f32 v211, 1.0, v211\n"
      "v_rcp_f32 v211, v211\n"
      "s_nop 0\n"
      "v_cvt_pk_bf16_f32 v211, v211, v211\n"
      "v_and_b32 v211, 0xffff0000, v211\n"
      "s_nop 0\n"
      "v_readfirstlane_b32 s39, v211\n"
      "v_readlane_b32 s84, v211, 1\n"
      "s_nop 0\n"
      "v_mov_b32 v216, s39\n"
      "v_mov_b32 v217, s84\n"
      "v_mov_b32 v218, s37\n"
      "v_mov_b32 v219, s37\n"
      "s_mov_b64 exec, 0xff\n"
      "global_store_dwordx4 v242, v[216:219], s[58:59] offset:1024 sc1\n"
      "s_mov_b64 exec, -1\n"
      ".Ls_e9_%=:\n"
      "v_mfma_f32_16x16x32_bf16 v[0:3], v[164:167], a[96:99], v[0:3]\n"
      "global_load_lds_dwordx4 v201, s[70:71] offset:1024\n"
      "v_mfma_f32_16x16x32_bf16 v[4:7], v[164:167], a[100:103], v[4:7]\n"
      "global_load_lds_dwordx4 v202, s[70:71] offset:2048\n"
      "v_mfma_f32_16x16x32_bf16 v[8:11], v[164:167], a[104:107], v[8:11]\n"
      "global_load_lds_dwordx4 v203, s[70:71] offset:3072\n"
      "v_mfma_f32_16x16x32_bf16 v[12:15], v[164:167], a[108:111], v[12:15]\n"
      "s_add_u32 s70, s70, 114688\n"
      "s_addc_u32 s71, s71, 0\n"
      "s_add_u32 m0, m0, 4096\n"
      "v_mfma_f32_16x16x32_bf16 v[16:19], v[164:167], a[112:115], v[16:19]\n"
      "global_load_lds_dwordx4 v200, s[70:71] offset:0\n"
      "v_mfma_f32_16x16x32_bf16 v[20:23], v[164:167], a[116:119], v[20:23]\n"
      "global_load_lds_dwordx4 v201, s[70:71] offset:1024\n"
      "v_mfma_f32_16x16x32_bf16 v[24:27], v[164:167], a[120:123], v[24:27]\n"
      "global_load_lds_dwordx4 v202, s[70:71] offset:2048\n"
      "v_mfma_f32_16x16x32_bf16 v[28:31], v[164:167], a[124:127], v[28:31]\n"
      "global_load_lds_dwordx4 v203, s[70:71] offset:3072\n"
      "v_mfma_f32_16x16x32_bf16 v[32:35], v[168:171], a[96:99], v[32:35]\n"
      "s_min_u32 s43, s80, 27\n"
      "s_lshr_b32 s44, s33, 1\n"
      "s_lshl1_add_u32 s44, s43, s44\n"
      "s_lshl_b32 s44, s44, 15\n"
      "s_and_b32 s45, s33, 1\n"
      "s_lshl1_add_u32 s45, s81, s45\n"
      "s_lshl_b32 s45, s45, 7\n"
      "s_add_u32 s44, s44, s45\n"
      "v_and_b32 v239, 31, v243\n"
      "v_lshl_add_u32 v239, v239, 10, s44\n"
      "global_load_dword v207, v239, s[30:31]\n"
      "v_mfma_f32_16x16x32_bf16 v[36:39], v[168:171], a[100:103], v[36:39]\n"
      "s_sub_u32 s48, s33, 1\n"
      "s_and_b32 s48, s48, 3\n"
      "s_lshl_b32 s48, s48, 14\n"
      "v_mfma_f32_16x16x32_bf16 v[40:43], v[168:171], a[104:107], v[40:43]\n"
      "s_mov_b32 m0, s41\n"
      "v_lshl_add_u32 v244, v243, 2, s48\n"
      "s_sub_u32 s48, s33, 2\n"
      "v_mfma_f32_16x16x32_bf16 v[44:47], v[168:171], a[108:111], v[44:47]\n"
      "s_and_b32 s48, s48, 3\n"
      "s_lshl_b32 s48, s48, 14\n"
      "v_lshl_add_u32 v245, v243, 2, s48\n"
      "v_mfma_f32_16x16x32_bf16 v[48:51], v[168:171], a[112:115], v[48:51]\n"
      "s_sub_u32 s48, s33, 3\n"
      "s_and_b32 s48, s48, 3\n"
      "s_lshl_b32 s48, s48, 14\n"
      "v_mfma_f32_16x16x32_bf16 v[52:55], v[168:171], a[116:119], v[52:55]\n"
      "v_lshl_add_u32 v246, v243, 2, s48\n"
      "v_lshrrev_b32 v201, 4, v243\n"
      "v_mfma_f32_16x16x32_bf16 v[56:59], v[168:171], a[120:123], v[56:59]\n"
      "v_bfe_u32 v224, v243, 2, 2\n"
      "v_lshl_or_b32 v201, v201, 3, v224\n"
      "v_mfma_f32_16x16x32_bf16 v[60:63], v[168:171], a[124:127], v[60:63]\n"
      "v_and_b32 v224, 3, v243\n"
      "v_lshlrev_b32 v224, 3, v224\n"
      "v_mfma_f32_16x16x32_bf16 v[64:67], v[172:175], a[96:99], v[64:67]\n"
      "v_lshl_or_b32 v201, v201, 7, v224\n"
      "s_mov_b32 s64, 0xffff0000\n"
      "v_mfma_f32_16x16x32_bf16 v[68:71], v[172:175], a[100:103], v[68:71]\n"
      "s_mov_b32 s65, 0xffff0000\n"
      "s_mov_b32 s66, -1\n"
      "s_mov_b32 s67, 0\n"
      "v_mfma_f32_16x16x32_bf16 v[72:75], v[172:175], a[104:107], v[72:75]\n"
      "s_lshl_b32 s54, s33, 13\n"
      "s_add_u32 s54, s54, 131072\n"
      "v_add_u32 v202, s54, v201\n"
      "v_mfma_f32_16x16x32_bf16 v[76:79], v[172:175], a[108:111], v[76:79]\n"
      "s_and_b32 s54, s33, 1\n"
      "s_lshl3_add_u32 s54, s54, s83\n"
      "s_lshl_b32 s54, s54, 8\n"
      "v_mfma_f32_16x16x32_bf16 v[80:83], v[172:175], a[112:115], v[80:83]\n"
      "s_lshl_b32 s55, s80, 13\n"
      "s_add_u32 s55, s55, 262144\n"
      "s_add_u32 s54, s54, s55\n"
      "v_mfma_f32_16x16x32_bf16 v[84:87], v[172:175], a[116:119], v[84:87]\n"
      "v_bfe_u32 v224, v243, 4, 1\n"
      "v_and_b32 v203, 0xf0, v242\n"
      "v_mfma_f32_16x16x32_bf16 v[88:91], v[172:175], a[120:123], v[88:91]\n"
      "v_lshl_or_b32 v200, v224, 12, v203\n"
      "v_add_u32 v200, s54, v200\n"
      "v_mfma_f32_16x16x32_bf16 v[92:95], v[172:175], a[124:127], v[92:95]\n"
      "global_load_dword v252, v253, s[58:59] offset:128 sc1\n"
      "v_lshl_or_b32 v203, v224, 14, v203\n"
      "v_mfma_f32_16x16x32_bf16 v[0:3], v[176:179], a[128:131], v[0:3]\n"
      "v_mfma_f32_16x16x32_bf16 v[4:7], v[176:179], a[132:135], v[4:7]\n"
      "s_waitcnt lgkmcnt(15)\n"
      "v_mfma_f32_16x16x32_bf16 v[8:11], v[176:179], a[136:139], v[8:11]\n"
      "v_mfma_f32_16x16x32_bf16 v[12:15], v[176:179], a[140:143], v[12:15]\n"
      "v_mfma_f32_16x16x32_bf16 v[16:19], v[176:179], a[144:147], v[16:19]\n"
      "v_mfma_f32_16x16x32_bf16 v[20:23], v[176:179], a[148:151], v[20:23]\n"
      "v_mfma_f32_16x16x32_bf16 v[24:27], v[176:179], a[152:155], v[24:27]\n"
      "v_mfma_f32_16x16x32_bf16 v[28:31], v[176:179], a[156:159], v[28:31]\n"
      "v_mfma_f32_16x16x32_bf16 v[32:35], v[180:183], a[128:131], v[32:35]\n"
      "v_mfma_f32_16x16x32_bf16 v[36:39], v[180:183], a[132:135], v[36:39]\n"
      "v_mfma_f32_16x16x32_bf16 v[40:43], v[180:183], a[136:139], v[40:43]\n"
      "v_mfma_f32_16x16x32_bf16 v[44:47], v[180:183], a[140:143], v[44:47]\n"
      "v_mfma_f32_16x16x32_bf16 v[48:51], v[180:183], a[144:147], v[48:51]\n"
      "v_mfma_f32_16x16x32_bf16 v[52:55], v[180:183], a[148:151], v[52:55]\n"
      "v_mfma_f32_16x16x32_bf16 v[56:59], v[180:183], a[152:155], v[56:59]\n"
      "v_mfma_f32_16x16x32_bf16 v[60:63], v[180:183], a[156:159], v[60:63]\n"
      "v_mfma_f32_16x16x32_bf16 v[64:67], v[184:187], a[128:131], v[64:67]\n"
      "v_mfma_f32_16x16x32_bf16 v[68:71], v[184:187], a[132:135], v[68:71]\n"
      "v_mfma_f32_16x16x32_bf16 v[72:75], v[184:187], a[136:139], v[72:75]\n"
      "v_mfma_f32_16x16x32_bf16 v[76:79], v[184:187], a[140:143], v[76:79]\n"
      "v_mfma_f32_16x16x32_bf16 v[80:83], v[184:187], a[144:147], v[80:83]\n"
      "v_mfma_f32_16x16x32_bf16 v[84:87], v[184:187], a[148:151], v[84:87]\n"
      "v_mfma_f32_16x16x32_bf16 v[88:91], v[184:187], a[152:155], v[88:91]\n"
      "v_mfma_f32_16x16x32_bf16 v[92:95], v[184:187], a[156:159], v[92:95]\n"
      "v_mfma_f32_16x16x32_bf16 v[0:3], v[188:191], a[160:163], v[0:3]\n"
      "v_mfma_f32_16x16x32_bf16 v[4:7], v[188:191], a[164:167], v[4:7]\n"
      "v_mfma_f32_16x16x32_bf16 v[8:11], v[188:191], a[168:171], v[8:11]\n"
      "v_mfma_f32_16x16x32_bf16 v[12:15], v[188:191], a[172:175], v[12:15]\n"
      "v_mfma_f32_16x16x32_bf16 v[16:19], v[188:191], a[176:179], v[16:19]\n"
      "v_mfma_f32_16x16x32_bf16 v[20:23], v[188:191], a[180:183], v[20:23]\n"
      "v_mfma_f32_16x16x32_bf16 v[24:27], v[188:191], a[184:187], v[24:27]\n"
      "v_mfma_f32_16x16x32_bf16 v[28:31], v[188:191], a[188:191], v[28:31]\n"
      "v_mfma_f32_16x16x32_bf16 v[32:35], v[128:131], a[160:163], v[32:35]\n"
      "v_mfma_f32_16x16x32_bf16 v[36:39], v[128:131], a[164:167], v[36:39]\n"
      "v_mfma_f32_16x16x32_bf16 v[40:43], v[128:131], a[168:171], v[40:43]\n"
      "v_mfma_f32_16x16x32_bf16 v[44:47], v[128:131], a[172:175], v[44:47]\n"
      "v_mfma_f32_16x16x32_bf16 v[48:51], v[128:131], a[176:179], v[48:51]\n"
      "v_mfma_f32_16x16x32_bf16 v[52:55], v[128:131], a[180:183], v[52:55]\n"
      "v_mfma_f32_16x16x32_bf16 v[56:59], v[128:131], a[184:187], v[56:59]\n"
      "v_mfma_f32_16x16x32_bf16 v[60:63], v[128:131], a[188:191], v[60:63]\n"
      "v_mfma_f32_16x16x32_bf16 v[64:67], v[132:135], a[160:163], v[64:67]\n"
      "v_mfma_f32_16x16x32_bf16 v[68:71], v[132:135], a[164:167], v[68:71]\n"
      "v_mfma_f32_16x16x32_bf16 v[72:75], v[132:135], a[168:171], v[72:75]\n"
      "v_mfma_f32_16x16x32_bf16 v[76:79], v[132:135], a[172:175], v[76:79]\n"
      "v_mfma_f32_16x16x32_bf16 v[80:83], v[132:135], a[176:179], v[80:83]\n"
      "v_mfma_f32_16x16x32_bf16 v[84:87], v[132:135], a[180:183], v[84:87]\n"
      "v_mfma_f32_16x16x32_bf16 v[88:91], v[132:135], a[184:187], v[88:91]\n"
      "v_mfma_f32_16x16x32_bf16 v[92:95], v[132:135], a[188:191], v[92:95]\n"
      "v_mfma_f32_16x16x32_bf16 v[0:3], v[136:139], a[192:195], v[0:3]\n"
      "s_waitcnt lgkmcnt(14)\n"
      "v_mfma_f32_16x16x32_bf16 v[4:7], v[136:139], a[196:199], v[4:7]\n"
      "s_waitcnt lgkmcnt(11)\n"
      "v_mfma_f32_16x16x32_bf16 v[8:11], v[136:139], a[200:203], v[8:11]\n"
      "s_waitcnt lgkmcnt(10)\n"
      "v_mfma_f32_16x16x32_bf16 v[12:15], v[136:139], a[204:207], v[12:15]\n"
      "s_waitcnt lgkmcnt(7)\n"
      "v_mfma_f32_16x16x32_bf16 v[16:19], v[136:139], a[208:211], v[16:19]\n"
      "s_waitcnt lgkmcnt(6)\n"
      "v_mfma_f32_16x16x32_bf16 v[20:23], v[136:139], a[212:215], v[20:23]\n"
      "s_waitcnt vmcnt(2)\n"
      "ds_read_b64_tr_b16 v[224:225], v202 offset:0\n"
      "s_waitcnt lgkmcnt(4)\n"
      "v_mfma_f32_16x16x32_bf16 v[24:27], v[136:139], a[216:219], v[24:27]\n"
      "ds_read_b64_tr_b16 v[226:227], v202 offset:512\n"
      "ds_read_b64_tr_b16 v[228:229], v202 offset:4096\n"
      "s_waitcnt lgkmcnt(5)\n"
      "v_mfma_f32_16x16x32_bf16 v[28:31], v[136:139], a[220:223], v[28:31]\n"
      "ds_read_b64_tr_b16 v[230:231], v202 offset:4608\n"
      "ds_read_b64_tr_b16 v[232:233], v202 offset:64\n"
      "v_mfma_f32_16x16x32_bf16 v[0:3], v[140:143], a[224:227], v[0:3]\n"
      "ds_read_b64_tr_b16 v[234:235], v202 offset:576\n"
      "ds_read_b64_tr_b16 v[236:237], v202 offset:4160\n"
      "v_mfma_f32_16x16x32_bf16 v[4:7], v[140:143], a[228:231], v[4:7]\n"
      "s_waitcnt vmcnt(0)\n"
      "v_cmp_ne_u32 vcc, s37, v252\n"
      "s_nop 1\n"
      "s_cbranch_vccnz .Ls_s11_%=\n"
      ".Ls_o12_%=:\n"
      "v_mfma_f32_16x16x32_bf16 v[8:11], v[140:143], a[232:235], v[8:11]\n"
      "ds_read_b64_tr_b16 v[238:239], v202 offset:4672\n"
      "v_mfma_f32_16x16x32_bf16 v[12:15], v[140:143], a[236:239], v[12:15]\n"
      "global_load_dwordx4 v[160:163], v240, s[10:11] offset:-1024 sc1\n"
      "global_load_dwordx4 v[164:167], v240, s[72:73] offset:-1024 sc1\n"
      "v_mfma_f32_16x16x32_bf16 v[16:19], v[140:143], a[240:243], v[16:19]\n"
      "global_load_dwordx4 v[168:171], v241, s[10:11] offset:-1024 sc1\n"
      "global_load_dwordx4 v[172:175], v241, s[72:73] offset:-1024 sc1\n"
      "v_mfma_f32_16x16x32_bf16 v[20:23], v[140:143], a[244:247], v[20:23]\n"
      "global_load_dwordx4 v[176:179], v240, s[14:15] offset:-1024 sc1\n"
      "global_load_dwordx4 v[180:183], v240, s[74:75] offset:-1024 sc1\n"
      "s_waitcnt lgkmcnt(9)\n"
      "v_mfma_f32_16x16x32_bf16 v[24:27], v[140:143], a[248:251], v[24:27]\n"
      "global_load_dwordx4 v[184:187], v241, s[14:15] offset:-1024 sc1\n"
      "v_cvt_pk_bf16_f32 v8, v8, v9\n"
      "s_waitcnt lgkmcnt(8)\n"
      "v_mfma_f32_16x16x32_bf16 v[28:31], v[140:143], a[252:255], v[28:31]\n"
      "global_load_dwordx4 v[188:191], v241, s[74:75] offset:-1024 sc1\n"
      "v_cvt_pk_bf16_f32 v9, v10, v11\n"
      "v_mfma_f32_16x16x32_bf16 v[32:35], v[144:147], a[192:195], v[32:35]\n"
      "global_load_dwordx4 v[128:131], v240, s[10:11] offset:0 sc1\n"
      "global_load_dwordx4 v[132:135], v240, s[72:73] offset:0 sc1\n"
      "v_mfma_f32_16x16x32_bf16 v[36:39], v[144:147], a[196:199], v[36:39]\n"
      "global_load_dwordx4 v[136:139], v241, s[10:11] offset:0 sc1\n"
      "global_load_dwordx4 v[140:143], v241, s[72:73] offset:0 sc1\n"
      "v_mfma_f32_16x16x32_bf16 v[40:43], v[144:147], a[200:203], v[40:43]\n"
      "s_waitcnt lgkmcnt(6)\n"
      "v_mfma_f32_16x16x32_bf16 v[248:251], v[192:195], v[224:227], 0\n"
      "v_mfma_f32_16x16x32_bf16 v[44:47], v[144:147], a[204:207], v[44:47]\n"
      "s_waitcnt lgkmcnt(4)\n"
      "v_mfma_f32_16x16x32_bf16 v[248:251], v[196:199], v[228:231], v[248:251]\n"
      "v_mfma_f32_16x16x32_bf16 v[48:51], v[144:147], a[208:211], v[48:51]\n"
      "s_waitcnt lgkmcnt(2)\n"
      "v_mfma_f32_16x16x32_bf16 v[224:227], v[192:195], v[232:235], 0\n"
      "v_mfma_f32_16x16x32_bf16 v[52:55], v[144:147], a[212:215], v[52:55]\n"
      "s_waitcnt lgkmcnt(0)\n"
      "v_mfma_f32_16x16x32_bf16 v[224:227], v[196:199], v[236:239], v[224:227]\n"
      "v_mfma_f32_16x16x32_bf16 v[56:59], v[144:147], a[216:219], v[56:59]\n"
      "ds_write_addtid_b32 v8 offset:0\n"
      "v_cvt_pk_bf16_f32 v10, v12, v13\n"
      "v_mfma_f32_16x16x32_bf16 v[60:63], v[144:147], a[220:223], v[60:63]\n"
      "global_load_dwordx4 v[144:147], v240, s[14:15] offset:0 sc1\n"
      "ds_write_addtid_b32 v9 offset:256\n"
      "v_mfma_f32_16x16x32_bf16 v[32:35], v[148:151], a[224:227], v[32:35]\n"
      "s_nop 0\n"
      "v_cndmask_b32 v247, v248, v224, s[64:65]\n"
      "v_mfma_f32_16x16x32_bf16 v[36:39], v[148:151], a[228:231], v[36:39]\n"
      "ds_read_b64_tr_b16 v[224:225], v202 offset:32\n"
      "v_cvt_pk_bf16_f32 v11, v14, v15\n"
      "v_mfma_f32_16x16x32_bf16 v[40:43], v[148:151], a[232:235], v[40:43]\n"
      "ds_read_b64_tr_b16 v[226:227], v202 offset:544\n"
      "ds_read_b64_tr_b16 v[228:229], v202 offset:4128\n"
      "v_mfma_f32_16x16x32_bf16 v[44:47], v[148:151], a[236:239], v[44:47]\n"
      "ds_read_b64_tr_b16 v[230:231], v202 offset:4640\n"
      "ds_read_b64_tr_b16 v[232:233], v202 offset:96\n"
      "v_mfma_f32_16x16x32_bf16 v[48:51], v[148:151], a[240:243], v[48:51]\n"
      "ds_read_b64_tr_b16 v[234:235], v202 offset:608\n"
      "ds_read_b64_tr_b16 v[236:237], v202 offset:4192\n"
      "v_mfma_f32_16x16x32_bf16 v[52:55], v[148:151], a[244:247], v[52:55]\n"
      "ds_read_b64_tr_b16 v[238:239], v202 offset:4704\n"
      "ds_write_addtid_b32 v10 offset:512\n"
      "v_mfma_f32_16x16x32_bf16 v[56:59], v[148:151], a[248:251], v[56:59]\n"
      "v_cvt_pk_bf16_f32 v12, v16, v17\n"
      "ds_write_addtid_b32 v11 offset:768\n"
      "v_mfma_f32_16x16x32_bf16 v[60:63], v[148:151], a[252:255], v[60:63]\n"
      "global_load_dwordx4 v[148:151], v240, s[74:75] offset:0 sc1\n"
      "v_cvt_pk_bf16_f32 v13, v18, v19\n"
      "v_mfma_f32_16x16x32_bf16 v[64:67], v[152:155], a[192:195], v[64:67]\n"
      "s_waitcnt lgkmcnt(8)\n"
      "v_mfma_f32_16x16x32_bf16 v[248:251], v[192:195], v[224:227], 0\n"
      "v_mfma_f32_16x16x32_bf16 v[68:71], v[152:155], a[196:199], v[68:71]\n"
      "s_waitcnt lgkmcnt(6)\n"
      "v_mfma_f32_16x16x32_bf16 v[248:251], v[196:199], v[228:231], v[248:251]\n"
      "v_mfma_f32_16x16x32_bf16 v[72:75], v[152:155], a[200:203], v[72:75]\n"
      "s_waitcnt lgkmcnt(4)\n"
      "v_mfma_f32_16x16x32_bf16 v[224:227], v[192:195], v[232:235], 0\n"
      "v_mfma_f32_16x16x32_bf16 v[76:79], v[152:155], a[204:207], v[76:79]\n"
      "s_waitcnt lgkmcnt(2)\n"
      "v_mfma_f32_16x16x32_bf16 v[224:227], v[196:199], v[236:239], v[224:227]\n"
      "v_mfma_f32_16x16x32_bf16 v[80:83], v[152:155], a[208:211], v[80:83]\n"
      "ds_write_addtid_b32 v12 offset:1024\n"
      "v_cvt_pk_bf16_f32 v14, v20, v21\n"
      "v_mfma_f32_16x16x32_bf16 v[84:87], v[152:155], a[212:215], v[84:87]\n"
      "ds_write_addtid_b32 v13 offset:1280\n"
      "v_cvt_pk_bf16_f32 v15, v22, v23\n"
      "v_mfma_f32_16x16x32_bf16 v[88:91], v[152:155], a[216:219], v[88:91]\n"
      "ds_write_addtid_b32 v14 offset:1536\n"
      "v_cndmask_b32 v250, v248, v224, s[64:65]\n"
      "v_mfma_f32_16x16x32_bf16 v[92:95], v[152:155], a[220:223], v[92:95]\n"
      "global_load_dwordx4 v[152:155], v241, s[14:15] offset:0 sc1\n"
      "v_and_b32 v248, s86, v247\n"
      "v_mfma_f32_16x16x32_bf16 v[64:67], v[156:159], a[224:227], v[64:67]\n"
      "v_mov_b32 v249, s37\n"
      "v_and_b32 v250, s86, v250\n"
      "v_mfma_f32_16x16x32_bf16 v[68:71], v[156:159], a[228:231], v[68:71]\n"
      "v_mov_b32 v251, s37\n"
      "v_cvt_pk_bf16_f32 v16, v24, v25\n"
      "v_mfma_f32_16x16x32_bf16 v[72:75], v[156:159], a[232:235], v[72:75]\n"
      "s_mov_b64 exec, s[66:67]\n"
      "global_store_dwordx4 v200, v[248:251], s[58:59] sc1\n"
      "s_mov_b64 exec, -1\n"
      "v_mfma_f32_16x16x32_bf16 v[76:79], v[156:159], a[236:239], v[76:79]\n"
      "ds_write_addtid_b32 v15 offset:1792\n"
      "v_bfe_u32 v248, v243, 1, 3\n"
      "v_mfma_f32_16x16x32_bf16 v[80:83], v[156:159], a[240:243], v[80:83]\n"
      "v_lshrrev_b32 v249, 4, v243\n"
      "v_lshlrev_b32 v249, 1, v249\n"
      "v_mfma_f32_16x16x32_bf16 v[84:87], v[156:159], a[244:247], v[84:87]\n"
      "v_sub_u32 v248, v248, v249\n"
      "v_and_b32 v249, 16, v242\n"
      "v_mfma_f32_16x16x32_bf16 v[88:91], v[156:159], a[248:251], v[88:91]\n"
      "v_mov_b32 v254, 0x3f80\n"
      "v_lshlrev_b32 v249, v249, v254\n"
      "v_mfma_f32_16x16x32_bf16 v[92:95], v[156:159], a[252:255], v[92:95]\n"
      "s_waitcnt vmcnt(14)\n"
      "global_load_dwordx4 v[156:159], v241, s[74:75] offset:0 sc1\n"
      "v_cvt_pk_bf16_f32 v17, v26, v27\n"
      "v_mfma_f32_16x16x32_bf16 v[96:99], v[160:163], a[0:3], 0\n"
      "v_cmp_eq_u32 vcc, 0, v248\n"
      "s_nop 1\n"
      "v_cndmask_b32 v250, 0, v249, vcc\n"
      "v_mfma_f32_16x16x32_bf16 v[100:103], v[160:163], a[4:7], 0\n"
      "v_cmp_eq_u32 vcc, 1, v248\n"
      "s_nop 1\n"
      "v_cndmask_b32 v251, 0, v249, vcc\n"
      "v_mfma_f32_16x16x32_bf16 v[104:107], v[160:163], a[8:11], 0\n"
      "ds_write_addtid_b32 v16 offset:2048\n"
      "v_mov_b32 v252, v250\n"
      "v_mfma_f32_16x16x32_bf16 v[108:111], v[160:163], a[12:15], 0\n"
      "v_mov_b32 v253, v251\n"
      "v_mov_b32 v248, 0\n"
      "v_mfma_f32_16x16x32_bf16 v[112:115], v[160:163], a[16:19], 0\n"
      "v_mov_b32 v249, 0\n"
      "v_mov_b32 v254, 0\n"
      "v_mfma_f32_16x16x32_bf16 v[116:119], v[160:163], a[20:23], 0\n"
      "v_mov_b32 v255, 0\n"
      "s_add_u32 s48, s34, 1\n"
      "v_mfma_f32_16x16x32_bf16 v[120:123], v[160:163], a[24:27], 0\n"
      "s_and_b32 s48, s48, 3\n"
      "s_mul_i32 s48, s48, 36864\n"
      "s_add_u32 s48, s48, 0\n"
      "v_mfma_f32_16x16x32_bf16 v[124:127], v[160:163], a[28:31], 0\n"
      "s_add_u32 s52, s60, s48\n"
      "s_addc_u32 s53, s61, 0\n"
      "global_load_dwordx4 v[160:163], v240, s[10:11] offset:1024 sc1\n"
      "v_mfma_f32_16x16x32_bf16 v[96:99], v[164:167], a[32:35], v[96:99]\n"
      "s_lshl2_add_u32 s54, s36, s33\n"
      "v_cvt_pk_bf16_f32 v18, v28, v29\n"
      "s_lshl_b32 s55, s54, 15\n"
      "v_mfma_f32_16x16x32_bf16 v[100:103], v[164:167], a[36:39], v[100:103]\n"
      "s_lshl2_add_u32 s56, s35, s34\n"
      "ds_write_addtid_b32 v17 offset:2304\n"
      "v_cvt_pk_bf16_f32 v19, v30, v31\n"
      "v_mfma_f32_16x16x32_bf16 v[104:107], v[164:167], a[40:43], v[104:107]\n"
      "s_lshl_b32 s56, s56, 6\n"
      "ds_write_addtid_b32 v18 offset:2560\n"
      "s_add_u32 s55, s55, s56\n"
      "v_mfma_f32_16x16x32_bf16 v[108:111], v[164:167], a[44:47], v[108:111]\n"
      "ds_write_addtid_b32 v19 offset:2816\n"
      "v_cvt_pk_bf16_f32 v40, v40, v41\n"
      "v_mfma_f32_16x16x32_bf16 v[112:115], v[164:167], a[48:51], v[112:115]\n"
      "v_cvt_pk_bf16_f32 v41, v42, v43\n"
      "v_and_b32 v209, 15, v243\n"
      "v_mfma_f32_16x16x32_bf16 v[116:119], v[164:167], a[52:55], v[116:119]\n"
      "ds_write_addtid_b32 v40 offset:3072\n"
      "v_cvt_pk_bf16_f32 v42, v44, v45\n"
      "v_mfma_f32_16x16x32_bf16 v[120:123], v[164:167], a[56:59], v[120:123]\n"
      "ds_write_addtid_b32 v41 offset:3328\n"
      "v_cvt_pk_bf16_f32 v43, v46, v47\n"
      "v_mfma_f32_16x16x32_bf16 v[124:127], v[164:167], a[60:63], v[124:127]\n"
      "s_waitcnt vmcnt(14)\n"
      "global_load_dwordx4 v[164:167], v240, s[72:73] offset:1024 sc1\n"
      "v_and_b32 v208, 48, v243\n"
      "v_mfma_f32_16x16x32_bf16 v[96:99], v[168:171], a[64:67], v[96:99]\n"
      "ds_write_addtid_b32 v42 offset:3584\n"
      "v_cvt_pk_bf16_f32 v44, v48, v49\n"
      "v_mfma_f32_16x16x32_bf16 v[100:103], v[168:171], a[68:71], v[100:103]\n"
      "ds_write_addtid_b32 v43 offset:3840\n"
      "v_cvt_pk_bf16_f32 v45, v50, v51\n"
      "v_mfma_f32_16x16x32_bf16 v[104:107], v[168:171], a[72:75], v[104:107]\n"
      "ds_write_addtid_b32 v44 offset:4096\n"
      "v_cvt_pk_bf16_f32 v46, v52, v53\n"
      "v_mfma_f32_16x16x32_bf16 v[108:111], v[168:171], a[76:79], v[108:111]\n"
      "ds_write_addtid_b32 v45 offset:4352\n"
      "v_cvt_pk_bf16_f32 v47, v54, v55\n"
      "v_mfma_f32_16x16x32_bf16 v[112:115], v[168:171], a[80:83], v[112:115]\n"
      "ds_write_addtid_b32 v46 offset:4608\n"
      "v_cvt_pk_bf16_f32 v48, v56, v57\n"
      "v_mfma_f32_16x16x32_bf16 v[116:119], v[168:171], a[84:87], v[116:119]\n"
      "ds_write_addtid_b32 v47 offset:4864\n"
      "v_cvt_pk_bf16_f32 v49, v58, v59\n"
      "v_mfma_f32_16x16x32_bf16 v[120:123], v[168:171], a[88:91], v[120:123]\n"
      "ds_write_addtid_b32 v48 offset:5120\n"
      "v_cvt_pk_bf16_f32 v50, v60, v61\n"
      "v_mfma_f32_16x16x32_bf16 v[124:127], v[168:171], a[92:95], v[124:127]\n"
      "global_load_dwordx4 v[168:171], v241, s[10:11] offset:1024 sc1\n"
      "v_lshl_or_b32 v208, v209, 10, v208\n"
      "v_mfma_f32_16x16x32_bf16 v[96:99], v[172:175], a[96:99], v[96:99]\n"
      "ds_write_addtid_b32 v49 offset:5376\n"
      "v_cvt_pk_bf16_f32 v51, v62, v63\n"
      "v_mfma_f32_16x16x32_bf16 v[100:103], v[172:175], a[100:103], v[100:103]\n"
      "ds_write_addtid_b32 v50 offset:5632\n"
      "v_add_u32 v208, s55, v208\n"
      "v_mfma_f32_16x16x32_bf16 v[104:107], v[172:175], a[104:107], v[104:107]\n"
      "ds_write_addtid_b32 v51 offset:5888\n"
      "v_add_u32 v210, 16384, v208\n"
      "v_mfma_f32_16x16x32_bf16 v[108:111], v[172:175], a[108:111], v[108:111]\n"
      "global_load_dwordx4 v[216:219], v208, s[16:17]\n"
      "global_load_dwordx4 v[220:223], v210, s[16:17]\n"
      "v_mfma_f32_16x16x32_bf16 v[112:115], v[172:175], a[112:115], v[112:115]\n"
      "s_lshr_b32 s55, s34, 1\n"
      "s_lshl2_add_u32 s55, s55, s35\n"
      "s_lshl_b32 s55, s55, 15\n"
      "v_mfma_f32_16x16x32_bf16 v[116:119], v[172:175], a[116:119], v[116:119]\n"
      "s_lshl_b32 s54, s54, 9\n"
      "s_waitcnt lgkmcnt(0)\n"
      "s_barrier\n"
      "v_mfma_f32_16x16x32_bf16 v[120:123], v[172:175], a[120:123], v[120:123]\n"
      "s_add_u32 s54, s54, s55\n"
      "ds_read2st64_b32 v[20:21], v244 offset0:0 offset1:1\n"
      "s_add_u32 s54, s54, 786432\n"
      "v_mfma_f32_16x16x32_bf16 v[124:127], v[172:175], a[124:127], v[124:127]\n"
      "s_waitcnt vmcnt(16)\n"
      "global_load_dwordx4 v[172:175], v241, s[72:73] offset:1024 sc1\n"
      "v_lshl_add_u32 v209, v209, 4, s54\n"
      "v_mfma_f32_16x16x32_bf16 v[96:99], v[176:179], a[128:131], v[96:99]\n"
      "ds_read2st64_b32 v[22:23], v245 offset0:4 offset1:5\n"
      "ds_read2st64_b32 v[24:25], v244 offset0:2 offset1:3\n"
      "v_mfma_f32_16x16x32_bf16 v[100:103], v[176:179], a[132:135], v[100:103]\n"
      "ds_read2st64_b32 v[26:27], v245 offset0:6 offset1:7\n"
      "ds_read2st64_b32 v[28:29], v246 offset0:8 offset1:9\n"
      "v_mfma_f32_16x16x32_bf16 v[104:107], v[176:179], a[136:139], v[104:107]\n"
      "ds_read2st64_b32 v[30:31], v246 offset0:10 offset1:11\n"
      "ds_read2st64_b32 v[52:53], v244 offset0:12 offset1:13\n"
      "v_mfma_f32_16x16x32_bf16 v[108:111], v[176:179], a[140:143], v[108:111]\n"
      "ds_read2st64_b32 v[54:55], v245 offset0:16 offset1:17\n"
      "ds_read2st64_b32 v[56:57], v244 offset0:14 offset1:15\n"
      "v_mfma_f32_16x16x32_bf16 v[112:115], v[176:179], a[144:147], v[112:115]\n"
      "ds_read2st64_b32 v[58:59], v245 offset0:18 offset1:19\n"
      "ds_read2st64_b32 v[60:61], v246 offset0:20 offset1:21\n"
      "v_mfma_f32_16x16x32_bf16 v[116:119], v[176:179], a[148:151], v[116:119]\n"
      "ds_read2st64_b32 v[62:63], v246 offset0:22 offset1:23\n"
      "v_cvt_pk_bf16_f32 v72, v72, v73\n"
      "v_mfma_f32_16x16x32_bf16 v[120:123], v[176:179], a[152:155], v[120:123]\n"
      "s_waitcnt lgkmcnt(6)\n"
      "v_mfma_f32_16x16x32_bf16 v[0:3], v[250:253], v[20:23], v[0:3]\n"
      "v_cvt_pk_bf16_f32 v73, v74, v75\n"
      "v_mfma_f32_16x16x32_bf16 v[124:127], v[176:179], a[156:159], v[124:127]\n"
      "global_load_dwordx4 v[176:179], v240, s[14:15] offset:1024 sc1\n"
      "v_mfma_f32_16x16x32_bf16 v[96:99], v[180:183], a[160:163], v[96:99]\n"
      "v_mfma_f32_16x16x32_bf16 v[4:7], v[250:253], v[24:27], v[4:7]\n"
      "v_mfma_f32_16x16x32_bf16 v[100:103], v[180:183], a[164:167], v[100:103]\n"
      "v_mfma_f32_16x16x32_bf16 v[0:3], v[252:255], v[28:31], v[0:3]\n"
      "v_mfma_f32_16x16x32_bf16 v[104:107], v[180:183], a[168:171], v[104:107]\n"
      "v_mfma_f32_16x16x32_bf16 v[4:7], v[248:251], v[28:31], v[4:7]\n"
      "v_mov_b32 v11, s37\n"
      "v_mfma_f32_16x16x32_bf16 v[108:111], v[180:183], a[172:175], v[108:111]\n"
      "ds_write_addtid_b32 v72 offset:6144\n"
      "v_cvt_pk_bf16_f32 v74, v76, v77\n"
      "v_mfma_f32_16x16x32_bf16 v[112:115], v[180:183], a[176:179], v[112:115]\n"
      "ds_write_addtid_b32 v73 offset:6400\n"
      "v_cvt_pk_bf16_f32 v8, v0, v1\n"
      "v_mfma_f32_16x16x32_bf16 v[116:119], v[180:183], a[180:183], v[116:119]\n"
      "global_load_dwordx2 v[204:205], v209, s[58:59] offset:0 sc1\n"
      "v_cvt_pk_bf16_f32 v9, v2, v3\n"
      "v_mfma_f32_16x16x32_bf16 v[120:123], v[180:183], a[184:187], v[120:123]\n"
      "global_load_dwordx2 v[206:207], v209, s[58:59] offset:256 sc1\n"
      "v_cvt_pk_bf16_f32 v10, v4, v5\n"
      "v_mfma_f32_16x16x32_bf16 v[124:127], v[180:183], a[188:191], v[124:127]\n"
      "s_waitcnt vmcnt(18)\n"
      "global_load_dwordx4 v[180:183], v240, s[74:75] offset:1024 sc1\n"
      "v_cvt_pk_bf16_f32 v247, v6, v7\n"
      "v_mfma_f32_16x16x32_bf16 v[96:99], v[184:187], a[192:195], v[96:99]\n"
      "s_add_u32 s68, s20, 16384\n"
      "s_addc_u32 s69, s21, 0\n"
      "global_store_dwordx4 v242, v[8:11], s[52:53] offset:0 sc1\n"
      "v_mfma_f32_16x16x32_bf16 v[100:103], v[184:187], a[196:199], v[100:103]\n"
      "s_waitcnt lgkmcnt(2)\n"
      "v_mfma_f32_16x16x32_bf16 v[32:35], v[250:253], v[52:55], v[32:35]\n"
      "v_cvt_pk_bf16_f32 v75, v78, v79\n"
      "v_mfma_f32_16x16x32_bf16 v[104:107], v[184:187], a[200:203], v[104:107]\n"
      "v_mfma_f32_16x16x32_bf16 v[36:39], v[250:253], v[56:59], v[36:39]\n"
      "v_mfma_f32_16x16x32_bf16 v[108:111], v[184:187], a[204:207], v[108:111]\n"
      "v_mfma_f32_16x16x32_bf16 v[32:35], v[252:255], v[60:63], v[32:35]\n"
      "v_mfma_f32_16x16x32_bf16 v[112:115], v[184:187], a[208:211], v[112:115]\n"
      "v_mfma_f32_16x16x32_bf16 v[36:39], v[248:251], v[60:63], v[36:39]\n"
      "v_mov_b32 v43, s37\n"
      "v_mfma_f32_16x16x32_bf16 v[116:119], v[184:187], a[212:215], v[116:119]\n"
      "ds_write_addtid_b32 v74 offset:6656\n"
      "v_mov_b32 v44, v247\n"
      "v_mfma_f32_16x16x32_bf16 v[120:123], v[184:187], a[216:219], v[120:123]\n"
      "v_mov_b32 v47, s37\n"
      "v_cvt_pk_bf16_f32 v76, v80, v81\n"
      "v_mfma_f32_16x16x32_bf16 v[124:127], v[184:187], a[220:223], v[124:127]\n"
      "global_load_dwordx4 v[184:187], v241, s[14:15] offset:1024 sc1\n"
      "v_mfma_f32_16x16x32_bf16 v[96:99], v[188:191], a[224:227], v[96:99]\n"
      "ds_write_addtid_b32 v75 offset:6912\n"
      "v_cvt_pk_bf16_f32 v40, v32, v33\n"
      "v_mfma_f32_16x16x32_bf16 v[100:103], v[188:191], a[228:231], v[100:103]\n"
      "v_cvt_pk_bf16_f32 v41, v34, v35\n"
      "v_cvt_pk_bf16_f32 v42, v36, v37\n"
      "v_mfma_f32_16x16x32_bf16 v[104:107], v[188:191], a[232:235], v[104:107]\n"
      "v_cvt_pk_bf16_f32 v45, v38, v39\n"
      "v_cvt_pk_bf16_f32 v77, v82, v83\n"
      "v_mfma_f32_16x16x32_bf16 v[108:111], v[188:191], a[236:239], v[108:111]\n"
      "global_store_dwordx4 v242, v[40:43], s[52:53] offset:1024 sc1\n"
      "v_mfma_f32_16x16x32_bf16 v[112:115], v[188:191], a[240:243], v[112:115]\n"
      "global_store_dwordx4 v242, v[44:47], s[52:53] offset:2048 sc1\n"
      "s_add_u32 s48, s34, 2\n"
      "v_mfma_f32_16x16x32_bf16 v[116:119], v[188:191], a[244:247], v[116:119]\n"
      "s_and_b32 s48, s48, 3\n"
      "ds_write_addtid_b32 v76 offset:7168\n"
      "s_mul_i32 s48, s48, 36864\n"
      "v_mfma_f32_16x16x32_bf16 v[120:123], v[188:191], a[248:251], v[120:123]\n"
      "s_add_u32 s48, s48, 12288\n"
      "s_add_u32 s52, s60, s48\n"
      "s_addc_u32 s53, s61, 0\n"
      "v_mfma_f32_16x16x32_bf16 v[124:127], v[188:191], a[252:255], v[124:127]\n"
      "s_waitcnt vmcnt(21)\n"
      "global_load_dwordx4 v[188:191], v241, s[74:75] offset:1024 sc1\n"
      "v_cvt_pk_bf16_f32 v78, v84, v85\n"
      "v_mfma_f32_16x16x32_bf16 v[0:3], v[128:131], a[0:3], 0\n"
      "ds_write_addtid_b32 v77 offset:7424\n"
      "v_cvt_pk_bf16_f32 v79, v86, v87\n"
      "v_mfma_f32_16x16x32_bf16 v[4:7], v[128:131], a[4:7], 0\n"
      "ds_write_addtid_b32 v78 offset:7680\n"
      "v_cvt_pk_bf16_f32 v80, v88, v89\n"
      "v_mfma_f32_16x16x32_bf16 v[8:11], v[128:131], a[8:11], 0\n"
      "ds_write_addtid_b32 v79 offset:7936\n"
      "v_cvt_pk_bf16_f32 v81, v90, v91\n"
      "v_mfma_f32_16x16x32_bf16 v[12:15], v[128:131], a[12:15], 0\n"
      "ds_write_addtid_b32 v80 offset:8192\n"
      "v_cvt_pk_bf16_f32 v82, v92, v93\n"
      "v_mfma_f32_16x16x32_bf16 v[16:19], v[128:131], a[16:19], 0\n"
      "ds_write_addtid_b32 v81 offset:8448\n"
      "v_cvt_pk_bf16_f32 v83, v94, v95\n"
      "v_mfma_f32_16x16x32_bf16 v[20:23], v[128:131], a[20:23], 0\n"
      "ds_write_addtid_b32 v82 offset:8704\n"
      "v_mfma_f32_16x16x32_bf16 v[24:27], v[128:131], a[24:27], 0\n"
      "ds_write_addtid_b32 v83 offset:8960\n"
      "v_cvt_pk_bf16_f32 v104, v104, v105\n"
      "v_mfma_f32_16x16x32_bf16 v[28:31], v[128:131], a[28:31], 0\n"
      "global_load_dwordx4 v[128:131], v240, s[10:11] offset:2048 sc1\n"
      "v_cvt_pk_bf16_f32 v105, v106, v107\n"
      "v_mfma_f32_16x16x32_bf16 v[0:3], v[132:135], a[32:35], v[0:3]\n"
      "ds_write_addtid_b32 v104 offset:9216\n"
      "v_cvt_pk_bf16_f32 v106, v108, v109\n"
      "v_mfma_f32_16x16x32_bf16 v[4:7], v[132:135], a[36:39], v[4:7]\n"
      "ds_write_addtid_b32 v105 offset:9472\n"
      "v_cvt_pk_bf16_f32 v107, v110, v111\n"
      "v_mfma_f32_16x16x32_bf16 v[8:11], v[132:135], a[40:43], v[8:11]\n"
      "ds_write_addtid_b32 v106 offset:9728\n"
      "v_cvt_pk_bf16_f32 v108, v112, v113\n"
      "v_mfma_f32_16x16x32_bf16 v[12:15], v[132:135], a[44:47], v[12:15]\n"
      "ds_write_addtid_b32 v107 offset:9984\n"
      "v_cvt_pk_bf16_f32 v109, v114, v115\n"
      "v_mfma_f32_16x16x32_bf16 v[16:19], v[132:135], a[48:51], v[16:19]\n"
      "ds_write_addtid_b32 v108 offset:10240\n"
      "v_cvt_pk_bf16_f32 v110, v116, v117\n"
      "v_mfma_f32_16x16x32_bf16 v[20:23], v[132:135], a[52:55], v[20:23]\n"
      "ds_write_addtid_b32 v109 offset:10496\n"
      "v_cvt_pk_bf16_f32 v111, v118, v119\n"
      "v_mfma_f32_16x16x32_bf16 v[24:27], v[132:135], a[56:59], v[24:27]\n"
      "ds_write_addtid_b32 v110 offset:10752\n"
      "v_cvt_pk_bf16_f32 v112, v120, v121\n"
      "v_mfma_f32_16x16x32_bf16 v[28:31], v[132:135], a[60:63], v[28:31]\n"
      "s_waitcnt vmcnt(21)\n"
      "global_load_dwordx4 v[132:135], v240, s[72:73] offset:2048 sc1\n"
      "v_mfma_f32_16x16x32_bf16 v[0:3], v[136:139], a[64:67], v[0:3]\n"
      "ds_write_addtid_b32 v111 offset:11008\n"
      "v_cvt_pk_bf16_f32 v113, v122, v123\n"
      "v_mfma_f32_16x16x32_bf16 v[4:7], v[136:139], a[68:71], v[4:7]\n"
      "ds_write_addtid_b32 v112 offset:11264\n"
      "v_cvt_pk_bf16_f32 v114, v124, v125\n"
      "v_mfma_f32_16x16x32_bf16 v[8:11], v[136:139], a[72:75], v[8:11]\n"
      "ds_write_addtid_b32 v113 offset:11520\n"
      "v_cvt_pk_bf16_f32 v115, v126, v127\n"
      "v_mfma_f32_16x16x32_bf16 v[12:15], v[136:139], a[76:79], v[12:15]\n"
      "ds_write_addtid_b32 v114 offset:11776\n"
      "v_mfma_f32_16x16x32_bf16 v[16:19], v[136:139], a[80:83], v[16:19]\n"
      "ds_write_addtid_b32 v115 offset:12032\n"
      "v_mfma_f32_16x16x32_bf16 v[20:23], v[136:139], a[84:87], v[20:23]\n"
      "v_mfma_f32_16x16x32_bf16 v[24:27], v[136:139], a[88:91], v[24:27]\n"
      "v_mfma_f32_16x16x32_bf16 v[28:31], v[136:139], a[92:95], v[28:31]\n"
      "global_load_dwordx4 v[136:139], v241, s[10:11] offset:2048 sc1\n"
      "v_mfma_f32_16x16x32_bf16 v[0:3], v[140:143], a[96:99], v[0:3]\n"
      "s_waitcnt lgkmcnt(0)\n"
      "s_barrier\n"
      "v_mfma_f32_16x16x32_bf16 v[4:7], v[140:143], a[100:103], v[4:7]\n"
      "ds_read2st64_b32 v[84:85], v244 offset0:24 offset1:25\n"
      "v_mfma_f32_16x16x32_bf16 v[8:11], v[140:143], a[104:107], v[8:11]\n"
      "ds_read2st64_b32 v[86:87], v245 offset0:28 offset1:29\n"
      "v_mfma_f32_16x16x32_bf16 v[12:15], v[140:143], a[108:111], v[12:15]\n"
      "ds_read2st64_b32 v[88:89], v244 offset0:26 offset1:27\n"
      "v_mfma_f32_16x16x32_bf16 v[16:19], v[140:143], a[112:115], v[16:19]\n"
      "ds_read2st64_b32 v[90:91], v245 offset0:30 offset1:31\n"
      "v_mfma_f32_16x16x32_bf16 v[20:23], v[140:143], a[116:119], v[20:23]\n"
      "ds_read2st64_b32 v[92:93], v246 offset0:32 offset1:33\n"
      "v_mfma_f32_16x16x32_bf16 v[24:27], v[140:143], a[120:123], v[24:27]\n"
      "ds_read2st64_b32 v[94:95], v246 offset0:34 offset1:35\n"
      "v_mfma_f32_16x16x32_bf16 v[28:31], v[140:143], a[124:127], v[28:31]\n"
      "s_waitcnt vmcnt(21)\n"
      "global_load_dwordx4 v[140:143], v241, s[72:73] offset:2048 sc1\n"
      "v_mfma_f32_16x16x32_bf16 v[0:3], v[144:147], a[128:131], v[0:3]\n"
      "ds_read2st64_b32 v[116:117], v244 offset0:36 offset1:37\n"
      "v_mfma_f32_16x16x32_bf16 v[4:7], v[144:147], a[132:135], v[4:7]\n"
      "ds_read2st64_b32 v[118:119], v245 offset0:40 offset1:41\n"
      "v_mfma_f32_16x16x32_bf16 v[8:11], v[144:147], a[136:139], v[8:11]\n"
      "s_waitcnt lgkmcnt(2)\n"
      "v_mfma_f32_16x16x32_bf16 v[64:67], v[250:253], v[84:87], v[64:67]\n"
      "v_mfma_f32_16x16x32_bf16 v[12:15], v[144:147], a[140:143], v[12:15]\n"
      "v_mfma_f32_16x16x32_bf16 v[68:71], v[250:253], v[88:91], v[68:71]\n"
      "v_mfma_f32_16x16x32_bf16 v[16:19], v[144:147], a[144:147], v[16:19]\n"
      "v_mfma_f32_16x16x32_bf16 v[64:67], v[252:255], v[92:95], v[64:67]\n"
      "v_mfma_f32_16x16x32_bf16 v[20:23], v[144:147], a[148:151], v[20:23]\n"
      "v_mfma_f32_16x16x32_bf16 v[68:71], v[248:251], v[92:95], v[68:71]\n"
      "v_mov_b32 v75, s37\n"
      "v_mfma_f32_16x16x32_bf16 v[24:27], v[144:147], a[152:155], v[24:27]\n"
      "ds_read2st64_b32 v[120:121], v244 offset0:38 offset1:39\n"
      "v_mfma_f32_16x16x32_bf16 v[28:31], v[144:147], a[156:159], v[28:31]\n"
      "global_load_dwordx4 v[144:147], v240, s[14:15] offset:2048 sc1\n"
      "s_nop 0\n"
      "v_cvt_pk_bf16_f32 v72, v64, v65\n"
      "v_mfma_f32_16x16x32_bf16 v[0:3], v[148:151], a[160:163], v[0:3]\n"
      "ds_read2st64_b32 v[122:123], v245 offset0:42 offset1:43\n"
      "v_cvt_pk_bf16_f32 v73, v66, v67\n"
      "v_mfma_f32_16x16x32_bf16 v[4:7], v[148:151], a[164:167], v[4:7]\n"
      "ds_read2st64_b32 v[124:125], v246 offset0:44 offset1:45\n"
      "v_cvt_pk_bf16_f32 v74, v68, v69\n"
      "v_mfma_f32_16x16x32_bf16 v[8:11], v[148:151], a[168:171], v[8:11]\n"
      "ds_read2st64_b32 v[126:127], v246 offset0:46 offset1:47\n"
      "v_cvt_pk_bf16_f32 v247, v70, v71\n"
      "v_mfma_f32_16x16x32_bf16 v[12:15], v[148:151], a[172:175], v[12:15]\n"
      "global_store_dwordx4 v242, v[72:75], s[52:53] offset:0 sc1\n"
      "v_mfma_f32_16x16x32_bf16 v[16:19], v[148:151], a[176:179], v[16:19]\n"
      "s_waitcnt vmcnt(12)\n"
      "v_bitop3_b32 v210, v205, v207, s37 bitop3:0x7e\n"
      "v_cmp_ne_u32 vcc, 0, v210\n"
      "s_nop 1\n"
      "s_cbranch_vccnz .Ls_s14_%=\n"
      ".Ls_o15_%=:\n"
      "v_mfma_f32_16x16x32_bf16 v[20:23], v[148:151], a[180:183], v[20:23]\n"
      "v_sub_f32 v205, 1.0, v204\n"
      "v_cvt_pk_bf16_f32 v205, v205, v205\n"
      "v_mfma_f32_16x16x32_bf16 v[24:27], v[148:151], a[184:187], v[24:27]\n"
      "s_waitcnt lgkmcnt(0)\n"
      "v_mfma_f32_16x16x32_bf16 v[96:99], v[250:253], v[116:119], v[96:99]\n"
      "v_and_b32 v205, 0xffff0000, v205\n"
      "v_mfma_f32_16x16x32_bf16 v[28:31], v[148:151], a[188:191], v[28:31]\n"
      "global_load_dwordx4 v[148:151], v240, s[74:75] offset:2048 sc1\n"
      "v_sub_f32 v207, 1.0, v206\n"
      "v_mfma_f32_16x16x32_bf16 v[0:3], v[152:155], a[192:195], v[0:3]\n"
      "v_mfma_f32_16x16x32_bf16 v[100:103], v[250:253], v[120:123], v[100:103]\n"
      "v_cvt_pk_bf16_f32 v207, v207, v207\n"
      "v_mfma_f32_16x16x32_bf16 v[4:7], v[152:155], a[196:199], v[4:7]\n"
      "v_mfma_f32_16x16x32_bf16 v[96:99], v[252:255], v[124:127], v[96:99]\n"
      "v_and_b32 v207, 0xffff0000, v207\n"
      "v_mfma_f32_16x16x32_bf16 v[8:11], v[152:155], a[200:203], v[8:11]\n"
      "v_mfma_f32_16x16x32_bf16 v[100:103], v[248:251], v[124:127], v[100:103]\n"
      "v_mov_b32 v107, s37\n"
      "v_mfma_f32_16x16x32_bf16 v[12:15], v[152:155], a[204:207], v[12:15]\n"
      "v_mov_b32 v108, v247\n"
      "v_mov_b32 v111, s37\n"
      "v_mfma_f32_16x16x32_bf16 v[16:19], v[152:155], a[208:211], v[16:19]\n"
      "v_lshlrev_b32 v208, 16, v216\n"
      "v_and_b32 v209, 0xffff0000, v216\n"
      "v_mfma_f32_16x16x32_bf16 v[20:23], v[152:155], a[212:215], v[20:23]\n"
      "v_cvt_pk_bf16_f32 v104, v96, v97\n"
      "v_cvt_pk_bf16_f32 v105, v98, v99\n"
      "v_mfma_f32_16x16x32_bf16 v[24:27], v[152:155], a[216:219], v[24:27]\n"
      "v_cvt_pk_bf16_f32 v106, v100, v101\n"
      "v_cvt_pk_bf16_f32 v109, v102, v103\n"
      "v_mfma_f32_16x16x32_bf16 v[28:31], v[152:155], a[220:223], v[28:31]\n"
      "global_load_dwordx4 v[152:155], v241, s[14:15] offset:2048 sc1\n"
      "v_mul_f32 v208, v205, v208\n"
      "v_mfma_f32_16x16x32_bf16 v[0:3], v[156:159], a[224:227], v[0:3]\n"
      "global_store_dwordx4 v242, v[104:107], s[52:53] offset:1024 sc1\n"
      "v_mul_f32 v209, v205, v209\n"
      "v_mfma_f32_16x16x32_bf16 v[4:7], v[156:159], a[228:231], v[4:7]\n"
      "global_store_dwordx4 v242, v[108:111], s[52:53] offset:2048 sc1\n"
      "s_add_u32 s48, s34, 3\n"
      "v_mfma_f32_16x16x32_bf16 v[8:11], v[156:159], a[232:235], v[8:11]\n"
      "s_and_b32 s48, s48, 3\n"
      "s_mul_i32 s48, s48, 36864\n"
      "s_add_u32 s48, s48, 24576\n"
      "v_mfma_f32_16x16x32_bf16 v[12:15], v[156:159], a[236:239], v[12:15]\n"
      "s_add_u32 s52, s60, s48\n"
      "s_addc_u32 s53, s61, 0\n"
      "v_lshlrev_b32 v212, 16, v220\n"
      "v_mfma_f32_16x16x32_bf16 v[16:19], v[156:159], a[240:243], v[16:19]\n"
      "v_and_b32 v213, 0xffff0000, v220\n"
      "v_mul_f32 v212, v207, v212\n"
      "v_mfma_f32_16x16x32_bf16 v[20:23], v[156:159], a[244:247], v[20:23]\n"
      "v_mul_f32 v213, v207, v213\n"
      "v_lshlrev_b32 v210, 16, v217\n"
      "v_mfma_f32_16x16x32_bf16 v[24:27], v[156:159], a[248:251], v[24:27]\n"
      "v_cvt_pk_bf16_f32 v8, v8, v9\n"
      "v_cvt_pk_bf16_f32 v9, v10, v11\n"
      "v_mfma_f32_16x16x32_bf16 v[28:31], v[156:159], a[252:255], v[28:31]\n"
      "global_load_dwordx4 v[156:159], v241, s[74:75] offset:2048 sc1\n"
      "v_and_b32 v211, 0xffff0000, v217\n"
      "v_mfma_f32_16x16x32_bf16 v[32:35], v[160:163], a[0:3], 0\n"
      "ds_write_addtid_b32 v8 offset:0\n"
      "v_cvt_pk_bf16_f32 v10, v12, v13\n"
      "v_mfma_f32_16x16x32_bf16 v[36:39], v[160:163], a[4:7], 0\n"
      "ds_write_addtid_b32 v9 offset:256\n"
      "v_cvt_pk_bf16_f32 v11, v14, v15\n"
      "v_mfma_f32_16x16x32_bf16 v[40:43], v[160:163], a[8:11], 0\n"
      "ds_write_addtid_b32 v10 offset:512\n"
      "v_cvt_pk_bf16_f32 v12, v16, v17\n"
      "v_mfma_f32_16x16x32_bf16 v[44:47], v[160:163], a[12:15], 0\n"
      "ds_write_addtid_b32 v11 offset:768\n"
      "v_cvt_pk_bf16_f32 v13, v18, v19\n"
      "v_mfma_f32_16x16x32_bf16 v[48:51], v[160:163], a[16:19], 0\n"
      "ds_write_addtid_b32 v12 offset:1024\n"
      "v_cvt_pk_bf16_f32 v14, v20, v21\n"
      "v_mfma_f32_16x16x32_bf16 v[52:55], v[160:163], a[20:23], 0\n"
      "ds_write_addtid_b32 v13 offset:1280\n"
      "v_cvt_pk_bf16_f32 v15, v22, v23\n"
      "v_mfma_f32_16x16x32_bf16 v[56:59], v[160:163], a[24:27], 0\n"
      "ds_write_addtid_b32 v14 offset:1536\n"
      "v_cvt_pk_bf16_f32 v16, v24, v25\n"
      "v_mfma_f32_16x16x32_bf16 v[60:63], v[160:163], a[28:31], 0\n"
      "global_load_dwordx4 v[160:163], v240, s[10:11] offset:3072 sc1\n"
      "v_mul_f32 v210, v205, v210\n"
      "v_mfma_f32_16x16x32_bf16 v[32:35], v[164:167], a[32:35], v[32:35]\n"
      "ds_write_addtid_b32 v15 offset:1792\n"
      "v_cvt_pk_bf16_f32 v17, v26, v27\n"
      "v_mfma_f32_16x16x32_bf16 v[36:39], v[164:167], a[36:39], v[36:39]\n"
      "ds_write_addtid_b32 v16 offset:2048\n"
      "v_cvt_pk_bf16_f32 v18, v28, v29\n"
      "v_mfma_f32_16x16x32_bf16 v[40:43], v[164:167], a[40:43], v[40:43]\n"
      "ds_write_addtid_b32 v17 offset:2304\n"
      "v_cvt_pk_bf16_f32 v19, v30, v31\n"
      "v_mfma_f32_16x16x32_bf16 v[44:47], v[164:167], a[44:47], v[44:47]\n"
      "ds_write_addtid_b32 v18 offset:2560\n"
      "v_mul_f32 v211, v205, v211\n"
      "v_mfma_f32_16x16x32_bf16 v[48:51], v[164:167], a[48:51], v[48:51]\n"
      "ds_write_addtid_b32 v19 offset:2816\n"
      "v_lshlrev_b32 v214, 16, v221\n"
      "v_mfma_f32_16x16x32_bf16 v[52:55], v[164:167], a[52:55], v[52:55]\n"
      "v_and_b32 v215, 0xffff0000, v221\n"
      "v_mul_f32 v214, v207, v214\n"
      "v_mfma_f32_16x16x32_bf16 v[56:59], v[164:167], a[56:59], v[56:59]\n"
      "v_mul_f32 v215, v207, v215\n"
      "v_lshlrev_b32 v216, 16, v218\n"
      "v_mfma_f32_16x16x32_bf16 v[60:63], v[164:167], a[60:63], v[60:63]\n"
      "global_load_dwordx4 v[164:167], v240, s[72:73] offset:3072 sc1\n"
      "v_and_b32 v217, 0xffff0000, v218\n"
      "v_mfma_f32_16x16x32_bf16 v[32:35], v[168:171], a[64:67], v[32:35]\n"
      "s_waitcnt lgkmcnt(0)\n"
      "s_barrier\n"
      "v_mfma_f32_16x16x32_bf16 v[36:39], v[168:171], a[68:71], v[36:39]\n"
      "ds_read2st64_b32 v[20:21], v244 offset0:0 offset1:1\n"
      "v_mul_f32 v216, v205, v216\n"
      "v_mfma_f32_16x16x32_bf16 v[40:43], v[168:171], a[72:75], v[40:43]\n"
      "ds_read2st64_b32 v[22:23], v245 offset0:4 offset1:5\n"
      "v_mul_f32 v217, v205, v217\n"
      "v_mfma_f32_16x16x32_bf16 v[44:47], v[168:171], a[76:79], v[44:47]\n"
      "ds_read2st64_b32 v[24:25], v244 offset0:2 offset1:3\n"
      "v_lshlrev_b32 v220, 16, v222\n"
      "v_mfma_f32_16x16x32_bf16 v[48:51], v[168:171], a[80:83], v[48:51]\n"
      "ds_read2st64_b32 v[26:27], v245 offset0:6 offset1:7\n"
      "v_and_b32 v221, 0xffff0000, v222\n"
      "v_mfma_f32_16x16x32_bf16 v[52:55], v[168:171], a[84:87], v[52:55]\n"
      "ds_read2st64_b32 v[28:29], v246 offset0:8 offset1:9\n"
      "v_mul_f32 v220, v207, v220\n"
      "v_mfma_f32_16x16x32_bf16 v[56:59], v[168:171], a[88:91], v[56:59]\n"
      "ds_read2st64_b32 v[30:31], v246 offset0:10 offset1:11\n"
      "v_mul_f32 v221, v207, v221\n"
      "v_mfma_f32_16x16x32_bf16 v[60:63], v[168:171], a[92:95], v[60:63]\n"
      "global_load_dwordx4 v[168:171], v241, s[10:11] offset:3072 sc1\n"
      "v_lshlrev_b32 v218, 16, v219\n"
      "v_mfma_f32_16x16x32_bf16 v[32:35], v[172:175], a[96:99], v[32:35]\n"
      "v_and_b32 v219, 0xffff0000, v219\n"
      "v_mul_f32 v218, v205, v218\n"
      "v_mfma_f32_16x16x32_bf16 v[36:39], v[172:175], a[100:103], v[36:39]\n"
      "v_mul_f32 v219, v205, v219\n"
      "v_lshlrev_b32 v222, 16, v223\n"
      "v_mfma_f32_16x16x32_bf16 v[40:43], v[172:175], a[104:107], v[40:43]\n"
      "s_waitcnt lgkmcnt(0)\n"
      "v_mfma_f32_16x16x32_bf16 v[0:3], v[250:253], v[20:23], v[0:3]\n"
      "v_and_b32 v223, 0xffff0000, v223\n"
      "v_mfma_f32_16x16x32_bf16 v[44:47], v[172:175], a[108:111], v[44:47]\n"
      "v_mfma_f32_16x16x32_bf16 v[4:7], v[250:253], v[24:27], v[4:7]\n"
      "v_mul_f32 v222, v207, v222\n"
      "v_mfma_f32_16x16x32_bf16 v[48:51], v[172:175], a[112:115], v[48:51]\n"
      "v_mfma_f32_16x16x32_bf16 v[0:3], v[252:255], v[28:31], v[0:3]\n"
      "v_mul_f32 v223, v207, v223\n"
      "v_mfma_f32_16x16x32_bf16 v[52:55], v[172:175], a[116:119], v[52:55]\n"
      "v_mfma_f32_16x16x32_bf16 v[4:7], v[248:251], v[28:31], v[4:7]\n"
      "v_mov_b32 v11, s37\n"
      "v_mfma_f32_16x16x32_bf16 v[56:59], v[172:175], a[120:123], v[56:59]\n"
      "v_mfma_f32_16x16x32_bf16 v[60:63], v[172:175], a[124:127], v[60:63]\n"
      "s_waitcnt vmcnt(19)\n"
      "global_load_dwordx4 v[172:175], v241, s[72:73] offset:3072 sc1\n"
      "v_cvt_pk_bf16_f32 v8, v0, v1\n"
      "v_mfma_f32_16x16x32_bf16 v[32:35], v[176:179], a[128:131], v[32:35]\n"
      "v_cvt_pk_bf16_f32 v9, v2, v3\n"
      "v_cvt_pk_bf16_f32 v10, v4, v5\n"
      "v_mfma_f32_16x16x32_bf16 v[36:39], v[176:179], a[132:135], v[36:39]\n"
      "v_cvt_pk_bf16_f32 v247, v6, v7\n"
      "v_mfma_f32_16x16x32_bf16 v[40:43], v[176:179], a[136:139], v[40:43]\n"
      "global_store_dwordx4 v242, v[8:11], s[52:53] offset:0 sc1\n"
      "s_lshl2_add_u32 s54, s36, s33\n"
      "v_mfma_f32_16x16x32_bf16 v[44:47], v[176:179], a[140:143], v[44:47]\n"
      "s_lshl_b32 s55, s54, 15\n"
      "s_lshl2_add_u32 s56, s35, s34\n"
      "s_lshl_b32 s56, s56, 6\n"
      "v_mfma_f32_16x16x32_bf16 v[48:51], v[176:179], a[144:147], v[48:51]\n"
      "s_add_u32 s55, s55, s56\n"
      "v_and_b32 v8, 15, v243\n"
      "v_and_b32 v7, 48, v243\n"
      "v_mfma_f32_16x16x32_bf16 v[52:55], v[176:179], a[148:151], v[52:55]\n"
      "v_lshl_or_b32 v7, v8, 10, v7\n"
      "v_add_u32 v7, s55, v7\n"
      "v_mfma_f32_16x16x32_bf16 v[56:59], v[176:179], a[152:155], v[56:59]\n"
      "v_mfma_f32_16x16x32_bf16 v[60:63], v[176:179], a[156:159], v[60:63]\n"
      "global_load_dwordx4 v[176:179], v240, s[14:15] offset:3072 sc1\n"
      "v_mfma_f32_16x16x32_bf16 v[32:35], v[180:183], a[160:163], v[32:35]\n"
      "v_mfma_f32_16x16x32_bf16 v[36:39], v[180:183], a[164:167], v[36:39]\n"
      "v_mfma_f32_16x16x32_bf16 v[40:43], v[180:183], a[168:171], v[40:43]\n"
      "v_mfma_f32_16x16x32_bf16 v[44:47], v[180:183], a[172:175], v[44:47]\n"
      "v_mfma_f32_16x16x32_bf16 v[48:51], v[180:183], a[176:179], v[48:51]\n"
      "v_mfma_f32_16x16x32_bf16 v[52:55], v[180:183], a[180:183], v[52:55]\n"
      "v_mfma_f32_16x16x32_bf16 v[56:59], v[180:183], a[184:187], v[56:59]\n"
      "v_mfma_f32_16x16x32_bf16 v[60:63], v[180:183], a[188:191], v[60:63]\n"
      "s_waitcnt vmcnt(17)\n"
      "global_load_dwordx4 v[180:183], v240, s[74:75] offset:3072 sc1\n"
      "v_mfma_f32_16x16x32_bf16 v[40:43], v[184:187], a[200:203], v[40:43]\n"
      "v_mfma_f32_16x16x32_bf16 v[44:47], v[184:187], a[204:207], v[44:47]\n"
      "v_mfma_f32_16x16x32_bf16 v[48:51], v[184:187], a[208:211], v[48:51]\n"
      "v_mfma_f32_16x16x32_bf16 v[52:55], v[184:187], a[212:215], v[52:55]\n"
      "v_mfma_f32_16x16x32_bf16 v[56:59], v[184:187], a[216:219], v[56:59]\n"
      "v_mfma_f32_16x16x32_bf16 v[60:63], v[184:187], a[220:223], v[60:63]\n"
      "v_mfma_f32_16x16x32_bf16 v[40:43], v[188:191], a[232:235], v[40:43]\n"
      "v_mfma_f32_16x16x32_bf16 v[44:47], v[188:191], a[236:239], v[44:47]\n"
      "v_mfma_f32_16x16x32_bf16 v[48:51], v[188:191], a[240:243], v[48:51]\n"
      "v_mfma_f32_16x16x32_bf16 v[52:55], v[188:191], a[244:247], v[52:55]\n"
      "v_mfma_f32_16x16x32_bf16 v[56:59], v[188:191], a[248:251], v[56:59]\n"
      "s_nop 3\n"
      "v_cvt_pk_bf16_f32 v40, v40, v41\n"
      "v_cvt_pk_bf16_f32 v41, v42, v43\n"
      "v_mfma_f32_16x16x32_bf16 v[60:63], v[188:191], a[252:255], v[60:63]\n"
      "ds_write_addtid_b32 v40 offset:3072\n"
      "v_cvt_pk_bf16_f32 v42, v44, v45\n"
      "v_mfma_f32_16x16x32_bf16 v[32:35], v[184:187], a[192:195], v[32:35]\n"
      "ds_write_addtid_b32 v41 offset:3328\n"
      "v_cvt_pk_bf16_f32 v43, v46, v47\n"
      "v_mfma_f32_16x16x32_bf16 v[36:39], v[184:187], a[196:199], v[36:39]\n"
      "global_load_dwordx4 v[184:187], v241, s[14:15] offset:3072 sc1\n"
      "ds_write_addtid_b32 v42 offset:3584\n"
      "v_cvt_pk_bf16_f32 v44, v48, v49\n"
      "v_mfma_f32_16x16x32_bf16 v[32:35], v[188:191], a[224:227], v[32:35]\n"
      "ds_write_addtid_b32 v43 offset:3840\n"
      "v_cvt_pk_bf16_f32 v45, v50, v51\n"
      "v_mfma_f32_16x16x32_bf16 v[36:39], v[188:191], a[228:231], v[36:39]\n"
      "s_waitcnt vmcnt(17)\n"
      "global_load_dwordx4 v[188:191], v241, s[74:75] offset:3072 sc1\n"
      "ds_write_addtid_b32 v44 offset:4096\n"
      "v_cvt_pk_bf16_f32 v46, v52, v53\n"
      "v_mfma_f32_16x16x32_bf16 v[72:75], v[128:131], a[8:11], 0\n"
      "ds_write_addtid_b32 v45 offset:4352\n"
      "v_cvt_pk_bf16_f32 v47, v54, v55\n"
      "v_mfma_f32_16x16x32_bf16 v[76:79], v[128:131], a[12:15], 0\n"
      "ds_write_addtid_b32 v46 offset:4608\n"
      "v_cvt_pk_bf16_f32 v48, v56, v57\n"
      "v_mfma_f32_16x16x32_bf16 v[80:83], v[128:131], a[16:19], 0\n"
      "ds_write_addtid_b32 v47 offset:4864\n"
      "v_cvt_pk_bf16_f32 v49, v58, v59\n"
      "v_mfma_f32_16x16x32_bf16 v[84:87], v[128:131], a[20:23], 0\n"
      "ds_write_addtid_b32 v48 offset:5120\n"
      "v_cvt_pk_bf16_f32 v50, v60, v61\n"
      "v_mfma_f32_16x16x32_bf16 v[88:91], v[128:131], a[24:27], 0\n"
      "ds_write_addtid_b32 v49 offset:5376\n"
      "v_cvt_pk_bf16_f32 v51, v62, v63\n"
      "v_mfma_f32_16x16x32_bf16 v[92:95], v[128:131], a[28:31], 0\n"
      "ds_write_addtid_b32 v50 offset:5632\n"
      "ds_write_addtid_b32 v51 offset:5888\n"
      "v_mfma_f32_16x16x32_bf16 v[72:75], v[132:135], a[40:43], v[72:75]\n"
      "v_mfma_f32_16x16x32_bf16 v[76:79], v[132:135], a[44:47], v[76:79]\n"
      "v_mfma_f32_16x16x32_bf16 v[80:83], v[132:135], a[48:51], v[80:83]\n"
      "s_waitcnt lgkmcnt(0)\n"
      "s_barrier\n"
      "v_mfma_f32_16x16x32_bf16 v[84:87], v[132:135], a[52:55], v[84:87]\n"
      "ds_read2st64_b32 v[52:53], v244 offset0:12 offset1:13\n"
      "ds_read2st64_b32 v[54:55], v245 offset0:16 offset1:17\n"
      "v_mfma_f32_16x16x32_bf16 v[88:91], v[132:135], a[56:59], v[88:91]\n"
      "ds_read2st64_b32 v[56:57], v244 offset0:14 offset1:15\n"
      "ds_read2st64_b32 v[58:59], v245 offset0:18 offset1:19\n"
      "v_mfma_f32_16x16x32_bf16 v[92:95], v[132:135], a[60:63], v[92:95]\n"
      "s_waitcnt vmcnt(16)\n"
      "ds_read2st64_b32 v[60:61], v246 offset0:20 offset1:21\n"
      "ds_read2st64_b32 v[62:63], v246 offset0:22 offset1:23\n"
      "v_mfma_f32_16x16x32_bf16 v[72:75], v[136:139], a[72:75], v[72:75]\n"
      "v_mfma_f32_16x16x32_bf16 v[76:79], v[136:139], a[76:79], v[76:79]\n"
      "v_mfma_f32_16x16x32_bf16 v[80:83], v[136:139], a[80:83], v[80:83]\n"
      "v_mfma_f32_16x16x32_bf16 v[84:87], v[136:139], a[84:87], v[84:87]\n"
      "s_waitcnt lgkmcnt(0)\n"
      "v_mfma_f32_16x16x32_bf16 v[32:35], v[250:253], v[52:55], v[32:35]\n"
      "v_mfma_f32_16x16x32_bf16 v[36:39], v[250:253], v[56:59], v[36:39]\n"
      "v_mfma_f32_16x16x32_bf16 v[88:91], v[136:139], a[88:91], v[88:91]\n"
      "v_mfma_f32_16x16x32_bf16 v[32:35], v[252:255], v[60:63], v[32:35]\n"
      "v_mfma_f32_16x16x32_bf16 v[36:39], v[248:251], v[60:63], v[36:39]\n"
      "v_add_u32 v4, 0, v242\n"
      "v_mfma_f32_16x16x32_bf16 v[92:95], v[136:139], a[92:95], v[92:95]\n"
      "global_load_dwordx4 v[12:15], v4, s[62:63] offset:0 sc1\n"
      "global_load_dwordx4 v[16:19], v4, s[62:63] offset:1024 sc1\n"
      "v_mov_b32 v43, s37\n"
      "v_mfma_f32_16x16x32_bf16 v[72:75], v[140:143], a[104:107], v[72:75]\n"
      "global_load_dwordx4 v[20:23], v4, s[62:63] offset:2048 sc1\n"
      "v_mov_b32 v44, v247\n"
      "v_mov_b32 v47, s37\n"
      "v_mfma_f32_16x16x32_bf16 v[76:79], v[140:143], a[108:111], v[76:79]\n"
      "v_mfma_f32_16x16x32_bf16 v[80:83], v[140:143], a[112:115], v[80:83]\n"
      "v_cvt_pk_bf16_f32 v40, v32, v33\n"
      "v_cvt_pk_bf16_f32 v41, v34, v35\n"
      "v_cvt_pk_bf16_f32 v42, v36, v37\n"
      "v_mfma_f32_16x16x32_bf16 v[84:87], v[140:143], a[116:119], v[84:87]\n"
      "v_cvt_pk_bf16_f32 v45, v38, v39\n"
      "v_mfma_f32_16x16x32_bf16 v[88:91], v[140:143], a[120:123], v[88:91]\n"
      "global_store_dwordx4 v242, v[40:43], s[52:53] offset:1024 sc1\n"
      "global_store_dwordx4 v242, v[44:47], s[52:53] offset:2048 sc1\n"
      "v_mfma_f32_16x16x32_bf16 v[92:95], v[140:143], a[124:127], v[92:95]\n"
      "s_waitcnt vmcnt(18)\n"
      "v_mfma_f32_16x16x32_bf16 v[72:75], v[144:147], a[136:139], v[72:75]\n"
      "v_mfma_f32_16x16x32_bf16 v[76:79], v[144:147], a[140:143], v[76:79]\n"
      "v_mfma_f32_16x16x32_bf16 v[80:83], v[144:147], a[144:147], v[80:83]\n"
      "v_mfma_f32_16x16x32_bf16 v[84:87], v[144:147], a[148:151], v[84:87]\n"
      "v_mfma_f32_16x16x32_bf16 v[88:91], v[144:147], a[152:155], v[88:91]\n"
      "v_mfma_f32_16x16x32_bf16 v[92:95], v[144:147], a[156:159], v[92:95]\n"
      "v_mfma_f32_16x16x32_bf16 v[72:75], v[148:151], a[168:171], v[72:75]\n"
      "v_mfma_f32_16x16x32_bf16 v[76:79], v[148:151], a[172:175], v[76:79]\n"
      "v_mfma_f32_16x16x32_bf16 v[80:83], v[148:151], a[176:179], v[80:83]\n"
      "v_mfma_f32_16x16x32_bf16 v[84:87], v[148:151], a[180:183], v[84:87]\n"
      "v_mfma_f32_16x16x32_bf16 v[88:91], v[148:151], a[184:187], v[88:91]\n"
      "v_mfma_f32_16x16x32_bf16 v[92:95], v[148:151], a[188:191], v[92:95]\n"
      "s_waitcnt vmcnt(14)\n"
      "v_mfma_f32_16x16x32_bf16 v[72:75], v[152:155], a[200:203], v[72:75]\n"
      "v_mfma_f32_16x16x32_bf16 v[76:79], v[152:155], a[204:207], v[76:79]\n"
      "v_mfma_f32_16x16x32_bf16 v[80:83], v[152:155], a[208:211], v[80:83]\n"
      "v_mfma_f32_16x16x32_bf16 v[84:87], v[152:155], a[212:215], v[84:87]\n"
      "v_mfma_f32_16x16x32_bf16 v[88:91], v[152:155], a[216:219], v[88:91]\n"
      "v_mfma_f32_16x16x32_bf16 v[92:95], v[152:155], a[220:223], v[92:95]\n"
      "v_mfma_f32_16x16x32_bf16 v[72:75], v[156:159], a[232:235], v[72:75]\n"
      "v_mfma_f32_16x16x32_bf16 v[76:79], v[156:159], a[236:239], v[76:79]\n"
      "v_mfma_f32_16x16x32_bf16 v[80:83], v[156:159], a[240:243], v[80:83]\n"
      "v_mfma_f32_16x16x32_bf16 v[84:87], v[156:159], a[244:247], v[84:87]\n"
      "v_mfma_f32_16x16x32_bf16 v[88:91], v[156:159], a[248:251], v[88:91]\n"
      "s_nop 3\n"
      "v_cvt_pk_bf16_f32 v72, v72, v73\n"
      "v_mfma_f32_16x16x32_bf16 v[92:95], v[156:159], a[252:255], v[92:95]\n"
      "s_waitcnt vmcnt(12)\n"
      "v_cvt_pk_bf16_f32 v73, v74, v75\n"
      "v_mfma_f32_16x16x32_bf16 v[104:107], v[160:163], a[8:11], 0\n"
      "ds_write_addtid_b32 v72 offset:6144\n"
      "v_cvt_pk_bf16_f32 v74, v76, v77\n"
      "v_mfma_f32_16x16x32_bf16 v[108:111], v[160:163], a[12:15], 0\n"
      "ds_write_addtid_b32 v73 offset:6400\n"
      "v_cvt_pk_bf16_f32 v75, v78, v79\n"
      "v_mfma_f32_16x16x32_bf16 v[112:115], v[160:163], a[16:19], 0\n"
      "ds_write_addtid_b32 v74 offset:6656\n"
      "v_cvt_pk_bf16_f32 v76, v80, v81\n"
      "v_mfma_f32_16x16x32_bf16 v[116:119], v[160:163], a[20:23], 0\n"
      "ds_write_addtid_b32 v75 offset:6912\n"
      "v_cvt_pk_bf16_f32 v77, v82, v83\n"
      "v_mfma_f32_16x16x32_bf16 v[120:123], v[160:163], a[24:27], 0\n"
      "ds_write_addtid_b32 v76 offset:7168\n"
      "v_cvt_pk_bf16_f32 v78, v84, v85\n"
      "v_mfma_f32_16x16x32_bf16 v[124:127], v[160:163], a[28:31], 0\n"
      "ds_write_addtid_b32 v77 offset:7424\n"
      "v_cvt_pk_bf16_f32 v79, v86, v87\n"
      "v_mfma_f32_16x16x32_bf16 v[104:107], v[164:167], a[40:43], v[104:107]\n"
      "ds_write_addtid_b32 v78 offset:7680\n"
      "v_cvt_pk_bf16_f32 v80, v88, v89\n"
      "v_mfma_f32_16x16x32_bf16 v[108:111], v[164:167], a[44:47], v[108:111]\n"
      "ds_write_addtid_b32 v79 offset:7936\n"
      "v_cvt_pk_bf16_f32 v81, v90, v91\n"
      "v_mfma_f32_16x16x32_bf16 v[112:115], v[164:167], a[48:51], v[112:115]\n"
      "ds_write_addtid_b32 v80 offset:8192\n"
      "v_cvt_pk_bf16_f32 v82, v92, v93\n"
      "v_mfma_f32_16x16x32_bf16 v[116:119], v[164:167], a[52:55], v[116:119]\n"
      "ds_write_addtid_b32 v81 offset:8448\n"
      "v_cvt_pk_bf16_f32 v83, v94, v95\n"
      "v_mfma_f32_16x16x32_bf16 v[120:123], v[164:167], a[56:59], v[120:123]\n"
      "ds_write_addtid_b32 v82 offset:8704\n"
      "v_mfma_f32_16x16x32_bf16 v[124:127], v[164:167], a[60:63], v[124:127]\n"
      "s_waitcnt vmcnt(10)\n"
      "ds_write_addtid_b32 v83 offset:8960\n"
      "v_mfma_f32_16x16x32_bf16 v[104:107], v[168:171], a[72:75], v[104:107]\n"
      "s_waitcnt vmcnt(2)\n"
      "v_bitop3_b32 v22, v15, v19, s37 bitop3:0x7e\n"
      "v_bitop3_b32 v22, v22, v23, s37 bitop3:0xf6\n"
      "v_cmp_ne_u32 vcc, 0, v22\n"
      "s_nop 1\n"
      "s_cbranch_vccnz .Ls_s17_%=\n"
      ".Ls_o18_%=:\n"
      "v_mfma_f32_16x16x32_bf16 v[108:111], v[168:171], a[76:79], v[108:111]\n"
      "v_mov_b32 v15, v20\n"
      "v_mov_b32 v19, v21\n"
      "v_mfma_f32_16x16x32_bf16 v[112:115], v[168:171], a[80:83], v[112:115]\n"
      "s_waitcnt lgkmcnt(0)\n"
      "s_barrier\n"
      "v_mfma_f32_16x16x32_bf16 v[116:119], v[168:171], a[84:87], v[116:119]\n"
      "ds_read2st64_b32 v[84:85], v244 offset0:24 offset1:25\n"
      "v_mfma_f32_16x16x32_bf16 v[120:123], v[168:171], a[88:91], v[120:123]\n"
      "ds_read2st64_b32 v[86:87], v245 offset0:28 offset1:29\n"
      "v_mfma_f32_16x16x32_bf16 v[124:127], v[168:171], a[92:95], v[124:127]\n"
      "ds_read2st64_b32 v[88:89], v244 offset0:26 offset1:27\n"
      "v_mfma_f32_16x16x32_bf16 v[104:107], v[172:175], a[104:107], v[104:107]\n"
      "ds_read2st64_b32 v[90:91], v245 offset0:30 offset1:31\n"
      "v_mfma_f32_16x16x32_bf16 v[108:111], v[172:175], a[108:111], v[108:111]\n"
      "ds_read2st64_b32 v[92:93], v246 offset0:32 offset1:33\n"
      "v_mfma_f32_16x16x32_bf16 v[112:115], v[172:175], a[112:115], v[112:115]\n"
      "ds_read2st64_b32 v[94:95], v246 offset0:34 offset1:35\n"
      "v_mfma_f32_16x16x32_bf16 v[116:119], v[172:175], a[116:119], v[116:119]\n"
      "s_lshr_b32 s43, s42, 7\n"
      "s_cmp_eq_u32 s43, s36\n"
      "s_cbranch_scc0 .Ls_e19_%=\n"
      "s_lshl2_add_u32 s44, s35, s34\n"
      "s_lshl2_add_u32 s43, s44, s33\n"
      "s_lshl_b32 s43, s43, 12\n"
      "s_add_u32 s43, s43, 524288\n"
      "v_add_u32 v60, s43, v242\n"
      "v_mov_b32 v62, 0\n"
      "global_load_dwordx4 v[24:27], v60, s[58:59] offset:0 sc1\n"
      "global_load_dwordx4 v[28:31], v60, s[58:59] offset:1024 sc1\n"
      "global_load_dwordx4 v[32:35], v60, s[58:59] offset:2048 sc1\n"
      "global_load_dwordx4 v[36:39], v60, s[58:59] offset:3072 sc1\n"
      "global_load_dwordx4 v[40:43], v62, s[58:59] offset:1024 sc1\n"
      "v_and_b32 v58, 15, v243\n"
      "v_lshrrev_b32 v59, 5, v243\n"
      "v_lshl_or_b32 v58, v59, 4, v58\n"
      "s_lshl4_add_u32 s44, s42, s44\n"
      "s_lshl_b32 s44, s44, 6\n"
      "v_lshl_add_u32 v59, v58, 1, s44\n"
      "global_load_ushort v57, v59, s[16:17]\n"
      "s_mov_b32 s44, 131072\n"
      "v_lshl_add_u32 v58, v58, 2, s44\n"
      "s_lshl_b32 s43, s33, 13\n"
      "v_add_u32 v59, s43, v58\n"
      ".Ls_e19_%=:\n"
      "v_mfma_f32_16x16x32_bf16 v[120:123], v[172:175], a[120:123], v[120:123]\n"
      "v_mfma_f32_16x16x32_bf16 v[64:67], v[252:255], v[12:15], 0\n"
      "v_mfma_f32_16x16x32_bf16 v[124:127], v[172:175], a[124:127], v[124:127]\n"
      "v_mfma_f32_16x16x32_bf16 v[68:71], v[248:251], v[12:15], 0\n"
      "v_mfma_f32_16x16x32_bf16 v[104:107], v[176:179], a[136:139], v[104:107]\n"
      "s_waitcnt lgkmcnt(0)\n"
      "v_mfma_f32_16x16x32_bf16 v[64:67], v[250:253], v[84:87], v[64:67]\n"
      "v_mfma_f32_16x16x32_bf16 v[108:111], v[176:179], a[140:143], v[108:111]\n"
      "v_mfma_f32_16x16x32_bf16 v[68:71], v[250:253], v[88:91], v[68:71]\n"
      "v_mfma_f32_16x16x32_bf16 v[112:115], v[176:179], a[144:147], v[112:115]\n"
      "v_mfma_f32_16x16x32_bf16 v[64:67], v[252:255], v[92:95], v[64:67]\n"
      "v_mfma_f32_16x16x32_bf16 v[116:119], v[176:179], a[148:151], v[116:119]\n"
      "v_mfma_f32_16x16x32_bf16 v[68:71], v[248:251], v[92:95], v[68:71]\n"
      "v_mfma_f32_16x16x32_bf16 v[120:123], v[176:179], a[152:155], v[120:123]\n"
      "v_mfma_f32_16x16x32_bf16 v[96:99], v[252:255], v[16:19], 0\n"
      "v_mfma_f32_16x16x32_bf16 v[124:127], v[176:179], a[156:159], v[124:127]\n"
      "v_mfma_f32_16x16x32_bf16 v[100:103], v[248:251], v[16:19], 0\n"
      "v_mfma_f32_16x16x32_bf16 v[104:107], v[180:183], a[168:171], v[104:107]\n"
      "v_mfma_f32_16x16x32_bf16 v[108:111], v[180:183], a[172:175], v[108:111]\n"
      "v_mfma_f32_16x16x32_bf16 v[112:115], v[180:183], a[176:179], v[112:115]\n"
      "v_mfma_f32_16x16x32_bf16 v[116:119], v[180:183], a[180:183], v[116:119]\n"
      "v_add_u32 v5, 12288, v242\n"
      "v_mfma_f32_16x16x32_bf16 v[120:123], v[180:183], a[184:187], v[120:123]\n"
      "global_load_dwordx4 v[44:47], v5, s[62:63] offset:0 sc1\n"
      "v_mfma_f32_16x16x32_bf16 v[124:127], v[180:183], a[188:191], v[124:127]\n"
      "global_load_dwordx4 v[48:51], v5, s[62:63] offset:1024 sc1\n"
      "v_mfma_f32_16x16x32_bf16 v[104:107], v[184:187], a[200:203], v[104:107]\n"
      "global_load_dwordx4 v[52:55], v5, s[62:63] offset:2048 sc1\n"
      "v_mfma_f32_16x16x32_bf16 v[108:111], v[184:187], a[204:207], v[108:111]\n"
      "v_mfma_f32_16x16x32_bf16 v[112:115], v[184:187], a[208:211], v[112:115]\n"
      "v_mfma_f32_16x16x32_bf16 v[116:119], v[184:187], a[212:215], v[116:119]\n"
      "v_mfma_f32_16x16x32_bf16 v[120:123], v[184:187], a[216:219], v[120:123]\n"
      "v_mfma_f32_16x16x32_bf16 v[124:127], v[184:187], a[220:223], v[124:127]\n"
      "v_mfma_f32_16x16x32_bf16 v[104:107], v[188:191], a[232:235], v[104:107]\n"
      "v_mfma_f32_16x16x32_bf16 v[108:111], v[188:191], a[236:239], v[108:111]\n"
      "v_mfma_f32_16x16x32_bf16 v[112:115], v[188:191], a[240:243], v[112:115]\n"
      "v_mfma_f32_16x16x32_bf16 v[116:119], v[188:191], a[244:247], v[116:119]\n"
      "v_mfma_f32_16x16x32_bf16 v[120:123], v[188:191], a[248:251], v[120:123]\n"
      "s_nop 3\n"
      "v_cvt_pk_bf16_f32 v104, v104, v105\n"
      "v_mfma_f32_16x16x32_bf16 v[124:127], v[188:191], a[252:255], v[124:127]\n"
      "v_cvt_pk_bf16_f32 v105, v106, v107\n"
      "v_mfma_f32_16x16x32_bf16 v[64:67], v[128:131], a[0:3], v[64:67]\n"
      "ds_write_addtid_b32 v104 offset:9216\n"
      "v_cvt_pk_bf16_f32 v106, v108, v109\n"
      "v_mfma_f32_16x16x32_bf16 v[68:71], v[128:131], a[4:7], v[68:71]\n"
      "ds_write_addtid_b32 v105 offset:9472\n"
      "v_cvt_pk_bf16_f32 v107, v110, v111\n"
      "v_mfma_f32_16x16x32_bf16 v[64:67], v[132:135], a[32:35], v[64:67]\n"
      "ds_write_addtid_b32 v106 offset:9728\n"
      "v_cvt_pk_bf16_f32 v108, v112, v113\n"
      "v_mfma_f32_16x16x32_bf16 v[68:71], v[132:135], a[36:39], v[68:71]\n"
      "ds_write_addtid_b32 v107 offset:9984\n"
      "v_cvt_pk_bf16_f32 v109, v114, v115\n"
      "v_mfma_f32_16x16x32_bf16 v[64:67], v[136:139], a[64:67], v[64:67]\n"
      "ds_write_addtid_b32 v108 offset:10240\n"
      "v_cvt_pk_bf16_f32 v110, v116, v117\n"
      "v_mfma_f32_16x16x32_bf16 v[68:71], v[136:139], a[68:71], v[68:71]\n"
      "ds_write_addtid_b32 v109 offset:10496\n"
      "v_cvt_pk_bf16_f32 v111, v118, v119\n"
      "v_mfma_f32_16x16x32_bf16 v[64:67], v[140:143], a[96:99], v[64:67]\n"
      "ds_write_addtid_b32 v110 offset:10752\n"
      "v_cvt_pk_bf16_f32 v112, v120, v121\n"
      "v_mfma_f32_16x16x32_bf16 v[68:71], v[140:143], a[100:103], v[68:71]\n"
      "ds_write_addtid_b32 v111 offset:11008\n"
      "v_cvt_pk_bf16_f32 v113, v122, v123\n"
      "v_mfma_f32_16x16x32_bf16 v[64:67], v[144:147], a[128:131], v[64:67]\n"
      "ds_write_addtid_b32 v112 offset:11264\n"
      "v_cvt_pk_bf16_f32 v114, v124, v125\n"
      "v_mfma_f32_16x16x32_bf16 v[68:71], v[144:147], a[132:135], v[68:71]\n"
      "ds_write_addtid_b32 v113 offset:11520\n"
      "v_cvt_pk_bf16_f32 v115, v126, v127\n"
      "v_mfma_f32_16x16x32_bf16 v[64:67], v[148:151], a[160:163], v[64:67]\n"
      "ds_write_addtid_b32 v114 offset:11776\n"
      "v_add_u32 v6, 24576, v242\n"
      "v_mfma_f32_16x16x32_bf16 v[68:71], v[148:151], a[164:167], v[68:71]\n"
      "global_load_dwordx4 v[72:75], v6, s[62:63] offset:0 sc1\n"
      "v_mfma_f32_16x16x32_bf16 v[64:67], v[152:155], a[192:195], v[64:67]\n"
      "global_load_dwordx4 v[76:79], v6, s[62:63] offset:1024 sc1\n"
      "v_mfma_f32_16x16x32_bf16 v[68:71], v[152:155], a[196:199], v[68:71]\n"
      "global_load_dwordx4 v[80:83], v6, s[62:63] offset:2048 sc1\n"
      "v_mfma_f32_16x16x32_bf16 v[64:67], v[156:159], a[224:227], v[64:67]\n"
      "ds_write_addtid_b32 v115 offset:12032\n"
      "v_mfma_f32_16x16x32_bf16 v[68:71], v[156:159], a[228:231], v[68:71]\n"
      "v_mfma_f32_16x16x32_bf16 v[96:99], v[160:163], a[0:3], v[96:99]\n"
      "v_mfma_f32_16x16x32_bf16 v[100:103], v[160:163], a[4:7], v[100:103]\n"
      "s_lshr_b32 s43, s42, 7\n"
      "s_cmp_eq_u32 s43, s36\n"
      "s_cbranch_scc0 .Ls_e20_%=\n"
      "s_waitcnt vmcnt(6)\n"
      "v_bitop3_b32 v61, v25, v27, s37 bitop3:0x7e\n"
      "v_bitop3_b32 v61, v61, v29, s37 bitop3:0xf6\n"
      "v_bitop3_b32 v61, v61, v31, s37 bitop3:0xf6\n"
      "v_bitop3_b32 v61, v61, v33, s37 bitop3:0xf6\n"
      "v_bitop3_b32 v61, v61, v35, s37 bitop3:0xf6\n"
      "v_bitop3_b32 v61, v61, v37, s37 bitop3:0xf6\n"
      "v_bitop3_b32 v61, v61, v39, s37 bitop3:0xf6\n"
      "v_bitop3_b32 v61, v61, v43, s37 bitop3:0xf6\n"
      "v_cmp_ne_u32 vcc, 0, v61\n"
      "s_nop 1\n"
      "s_cbranch_vccnz .Ls_s22_%=\n"
      ".Ls_o23_%=:\n"
      "v_add_f32 v24, v24, v28\n"
      "v_add_f32 v26, v26, v30\n"
      "v_add_f32 v32, v32, v36\n"
      "v_add_f32 v34, v34, v38\n"
      "v_add_f32 v24, v24, v32\n"
      "v_add_f32 v26, v26, v34\n"
      "s_nop 1\n"
      "v_permlane32_swap_b32 v24, v26\n"
      "v_add_f32 v24, v24, v26\n"
      "v_mov_b32 v26, v24\n"
      "s_nop 1\n"
      "v_permlane16_swap_b32 v24, v26\n"
      "v_add_f32 v24, v24, v26\n"
      "ds_write_b32 v59, v24\n"
      "s_waitcnt lgkmcnt(0)\n"
      ".Ls_e20_%=:\n"
      "v_mfma_f32_16x16x32_bf16 v[96:99], v[164:167], a[32:35], v[96:99]\n"
      "s_waitcnt lgkmcnt(0)\n"
      "s_barrier\n"
      "v_mfma_f32_16x16x32_bf16 v[100:103], v[164:167], a[36:39], v[100:103]\n"
      "s_lshr_b32 s43, s42, 5\n"
      "s_lshl2_add_u32 s44, s36, s33\n"
      "s_cmp_eq_u32 s43, s44\n"
      "s_cbranch_scc0 .Ls_e24_%=\n"
      "ds_read_b32 v24, v58 offset:0\n"
      "ds_read_b32 v25, v58 offset:8192\n"
      "ds_read_b32 v26, v58 offset:16384\n"
      "ds_read_b32 v27, v58 offset:24576\n"
      "v_mov_b32 v63, s37\n"
      "s_or_b32 s43, s34, s35\n"
      "s_cmp_eq_u32 s43, 0\n"
      "s_cbranch_scc0 .Ls_z25_%=\n"
      "s_mov_b64 exec, 1\n"
      "global_store_dword v62, v63, s[58:59] sc1\n"
      ".Ls_z25_%=:\n"
      "s_mov_b64 exec, -1\n"
      "s_waitcnt vmcnt(6)\n"
      "v_lshlrev_b32 v57, 16, v57\n"
      "v_and_b32 v56, 48, v243\n"
      "s_mov_b32 s44, 131072\n"
      "v_lshl_add_u32 v56, v56, 1, s44\n"
      "s_waitcnt lgkmcnt(0)\n"
      "v_add_f32 v24, v24, v25\n"
      "s_waitcnt lgkmcnt(0)\n"
      "v_add_f32 v26, v26, v27\n"
      "v_add_f32 v24, v24, v26\n"
      "v_cvt_pk_bf16_f32 v24, v24, v24\n"
      "v_and_b32 v24, 0xffff0000, v24\n"
      "v_mul_f32 v24, v41, v24\n"
      "v_cvt_pk_bf16_f32 v24, v24, v24\n"
      "v_and_b32 v24, 0xffff0000, v24\n"
      "v_add_f32 v24, v24, v57\n"
      "v_cvt_pk_bf16_f32 v24, v24, v24\n"
      "v_and_b32 v24, 0xffff0000, v24\n"
      "v_sub_f32 v42, 1.0, v40\n"
      "v_cvt_pk_bf16_f32 v42, v42, v42\n"
      "v_and_b32 v42, 0xffff0000, v42\n"
      "v_mul_f32 v24, v42, v24\n"
      "ds_write_b32 v58, v24 offset:1024\n"
      "ds_read_b128 v[28:31], v56 offset:1024\n"
      "ds_read_b128 v[32:35], v56 offset:1040\n"
      ".Ls_e24_%=:\n"
      "v_mfma_f32_16x16x32_bf16 v[96:99], v[168:171], a[64:67], v[96:99]\n"
      "ds_read2st64_b32 v[116:117], v244 offset0:36 offset1:37\n"
      "v_mfma_f32_16x16x32_bf16 v[100:103], v[168:171], a[68:71], v[100:103]\n"
      "ds_read2st64_b32 v[118:119], v245 offset0:40 offset1:41\n"
      "v_mfma_f32_16x16x32_bf16 v[96:99], v[172:175], a[96:99], v[96:99]\n"
      "ds_read2st64_b32 v[120:121], v244 offset0:38 offset1:39\n"
      "v_mfma_f32_16x16x32_bf16 v[100:103], v[172:175], a[100:103], v[100:103]\n"
      "ds_read2st64_b32 v[122:123], v245 offset0:42 offset1:43\n"
      "v_mfma_f32_16x16x32_bf16 v[96:99], v[176:179], a[128:131], v[96:99]\n"
      "ds_read2st64_b32 v[124:125], v246 offset0:44 offset1:45\n"
      "v_mfma_f32_16x16x32_bf16 v[100:103], v[176:179], a[132:135], v[100:103]\n"
      "ds_read2st64_b32 v[126:127], v246 offset0:46 offset1:47\n"
      "v_mfma_f32_16x16x32_bf16 v[96:99], v[180:183], a[160:163], v[96:99]\n"
      "v_mfma_f32_16x16x32_bf16 v[100:103], v[180:183], a[164:167], v[100:103]\n"
      "v_mfma_f32_16x16x32_bf16 v[96:99], v[184:187], a[192:195], v[96:99]\n"
      "s_waitcnt vmcnt(3)\n"
      "v_bitop3_b32 v54, v47, v51, s37 bitop3:0x7e\n"
      "v_bitop3_b32 v54, v54, v55, s37 bitop3:0xf6\n"
      "v_cmp_ne_u32 vcc, 0, v54\n"
      "s_nop 1\n"
      "s_cbranch_vccnz .Ls_s27_%=\n"
      ".Ls_o28_%=:\n"
      "v_mfma_f32_16x16x32_bf16 v[100:103], v[184:187], a[196:199], v[100:103]\n"
      "s_waitcnt lgkmcnt(0)\n"
      "v_mfma_f32_16x16x32_bf16 v[96:99], v[250:253], v[116:119], v[96:99]\n"
      "v_mov_b32 v47, v52\n"
      "v_mfma_f32_16x16x32_bf16 v[96:99], v[188:191], a[224:227], v[96:99]\n"
      "v_mfma_f32_16x16x32_bf16 v[100:103], v[250:253], v[120:123], v[100:103]\n"
      "v_mov_b32 v51, v53\n"
      "v_mfma_f32_16x16x32_bf16 v[100:103], v[188:191], a[228:231], v[100:103]\n"
      "v_mfma_f32_16x16x32_bf16 v[96:99], v[252:255], v[124:127], v[96:99]\n"
      "v_mfma_f32_16x16x32_bf16 v[100:103], v[248:251], v[124:127], v[100:103]\n"
      "v_mfma_f32_16x16x32_bf16 v[64:67], v[252:255], v[44:47], v[64:67]\n"
      "v_mfma_f32_16x16x32_bf16 v[68:71], v[248:251], v[44:47], v[68:71]\n"
      "v_mfma_f32_16x16x32_bf16 v[96:99], v[252:255], v[48:51], v[96:99]\n"
      "v_mfma_f32_16x16x32_bf16 v[100:103], v[248:251], v[48:51], v[100:103]\n"
      "s_lshr_b32 s43, s42, 5\n"
      "s_lshl2_add_u32 s44, s36, s33\n"
      "s_cmp_eq_u32 s43, s44\n"
      "s_cbranch_scc0 .Ls_e29_%=\n"
      "v_lshrrev_b32 v24, 10, v7\n"
      "v_subrev_u32 v24, s42, v24\n"
      "v_cmp_eq_u32 vcc, 0, v24\n"
      "s_nop 1\n"
      "s_mov_b64 exec, vcc\n"
      "v_mov_b32 v204, v40\n"
      "v_mov_b32 v208, v28\n"
      "v_mov_b32 v209, v29\n"
      "v_mov_b32 v210, v30\n"
      "v_mov_b32 v211, v31\n"
      "v_mov_b32 v216, v32\n"
      "v_mov_b32 v217, v33\n"
      "v_mov_b32 v218, v34\n"
      "v_mov_b32 v219, v35\n"
      "s_mov_b64 exec, -1\n"
      "v_cmp_eq_u32 vcc, -16, v24\n"
      "s_nop 1\n"
      "s_mov_b64 exec, vcc\n"
      "v_mov_b32 v206, v40\n"
      "v_mov_b32 v212, v28\n"
      "v_mov_b32 v213, v29\n"
      "v_mov_b32 v214, v30\n"
      "v_mov_b32 v215, v31\n"
      "v_mov_b32 v220, v32\n"
      "v_mov_b32 v221, v33\n"
      "v_mov_b32 v222, v34\n"
      "v_mov_b32 v223, v35\n"
      "s_mov_b64 exec, -1\n"
      ".Ls_e29_%=:\n"
      "s_waitcnt vmcnt(0)\n"
      "v_bitop3_b32 v82, v75, v79, s37 bitop3:0x7e\n"
      "v_bitop3_b32 v82, v82, v83, s37 bitop3:0xf6\n"
      "v_cmp_ne_u32 vcc, 0, v82\n"
      "s_nop 1\n"
      "s_cbranch_vccnz .Ls_s31_%=\n"
      ".Ls_o32_%=:\n"
      "v_mov_b32 v75, v80\n"
      "v_mov_b32 v79, v81\n"
      "s_nop 0\n"
      "v_mfma_f32_16x16x32_bf16 v[64:67], v[252:255], v[72:75], v[64:67]\n"
      "v_mfma_f32_16x16x32_bf16 v[96:99], v[252:255], v[76:79], v[96:99]\n"
      "v_mfma_f32_16x16x32_bf16 v[68:71], v[248:251], v[72:75], v[68:71]\n"
      "v_mfma_f32_16x16x32_bf16 v[100:103], v[248:251], v[76:79], v[100:103]\n"
      "s_nop 4\n"
      "v_fma_f32 v208, v204, v64, v208\n"
      "v_fma_f32 v209, v204, v65, v209\n"
      "v_fma_f32 v210, v204, v66, v210\n"
      "v_fma_f32 v211, v204, v67, v211\n"
      "v_fma_f32 v216, v204, v96, v216\n"
      "v_fma_f32 v217, v204, v97, v217\n"
      "v_fma_f32 v218, v204, v98, v218\n"
      "v_fma_f32 v219, v204, v99, v219\n"
      "v_cvt_pk_bf16_f32 v104, v208, v209\n"
      "v_cvt_pk_bf16_f32 v105, v210, v211\n"
      "v_cvt_pk_bf16_f32 v106, v216, v217\n"
      "v_cvt_pk_bf16_f32 v107, v218, v219\n"
      "global_store_dwordx4 v7, v[104:107], s[20:21]\n"
      "v_fma_f32 v212, v206, v68, v212\n"
      "v_fma_f32 v213, v206, v69, v213\n"
      "v_fma_f32 v214, v206, v70, v214\n"
      "v_fma_f32 v215, v206, v71, v215\n"
      "v_fma_f32 v220, v206, v100, v220\n"
      "v_fma_f32 v221, v206, v101, v221\n"
      "v_fma_f32 v222, v206, v102, v222\n"
      "v_fma_f32 v223, v206, v103, v223\n"
      "v_cvt_pk_bf16_f32 v108, v212, v213\n"
      "v_cvt_pk_bf16_f32 v109, v214, v215\n"
      "v_cvt_pk_bf16_f32 v110, v220, v221\n"
      "v_cvt_pk_bf16_f32 v111, v222, v223\n"
      "global_store_dwordx4 v7, v[108:111], s[68:69]\n"
      "s_endpgm\n"
      ".Lprod_%=:\n"
      "s_waitcnt lgkmcnt(0)\n"
      "s_add_u32 s68, s16, s44\n"
      "s_addc_u32 s69, s17, 0\n"
      "s_add_u32 s12, s12, s45\n"
      "s_addc_u32 s13, s13, 0\n"
      "s_add_u32 s58, s18, 4718592\n"
      "s_addc_u32 s59, s19, 0\n"
      "v_mov_b32 v235, 0\n"
      "global_load_dword v235, v235, s[58:59] sc1\n"
      "v_and_b32 v238, 15, v243\n"
      "v_lshrrev_b32 v239, 4, v243\n"
      "v_xor_b32 v239, v239, v238\n"
      "v_and_b32 v239, 7, v239\n"
      "v_lshlrev_b32 v239, 4, v239\n"
      "v_lshl_or_b32 v238, v238, 7, v239\n"
      "v_add_u32 v248, s41, v238\n"
      "v_xor_b32 v249, 64, v248\n"
      "v_add_u32 v250, 65536, v238\n"
      "v_xor_b32 v251, 64, v250\n"
      "v_and_b32 v252, 8, v243\n"
      "v_lshlrev_b32 v252, 7, v252\n"
      "v_and_or_b32 v252, v243, 48, v252\n"
      "v_add_u32 v252, s41, v252\n"
      "v_add_u32 v238, s80, v242\n"
      "v_and_b32 v238, 0x3ff, v238\n"
      "v_lshl_or_b32 v244, v236, 12, v237\n"
      "v_add_u32 v245, 31744, v244\n"
      "v_add_u32 v246, 63488, v244\n"
      "v_add_u32 v247, 95232, v244\n"
      "s_bitcmp1_b32 s34, 1\n"
      "s_cselect_b64 s[8:9], s[10:11], s[8:9]\n"
      "s_and_b32 s45, s34, 1\n"
      "s_lshl2_add_u32 s45, s45, s33\n"
      "s_lshl_b32 s45, s45, 9\n"
      "s_lshl_b32 s76, s36, 19\n"
      "s_add_u32 s45, s45, s76\n"
      "s_add_u32 s8, s8, s45\n"
      "s_addc_u32 s9, s9, 0\n"
      "s_lshr_b32 s39, s32, 6\n"
      "s_and_b32 s45, s35, 2\n"
      "s_xor_b32 s39, s39, s45\n"
      "s_xor_b32 s39, s39, s33\n"
      "s_lshl_b32 s45, s39, 7\n"
      "s_add_u32 s76, s33, 0\n"
      "s_and_b32 s76, s76, 3\n"
      "s_lshl_b32 s76, s76, 17\n"
      "s_or_b32 s76, s76, s45\n"
      "s_add_u32 s77, s33, 1\n"
      "s_and_b32 s77, s77, 3\n"
      "s_lshl_b32 s77, s77, 17\n"
      "s_or_b32 s77, s77, s45\n"
      "s_add_u32 s78, s33, 2\n"
      "s_and_b32 s78, s78, 3\n"
      "s_lshl_b32 s78, s78, 17\n"
      "s_or_b32 s78, s78, s45\n"
      "s_add_u32 s79, s33, 3\n"
      "s_and_b32 s79, s79, 3\n"
      "s_lshl_b32 s79, s79, 17\n"
      "s_or_b32 s79, s79, s45\n"
      "s_lshl2_add_u32 s43, s34, s35\n"
      "s_and_b32 s44, s40, 31\n"
      "s_mul_i32 s45, s43, 266240\n"
      "s_lshl_b32 s42, s44, 13\n"
      "s_add_u32 s45, s45, s42\n"
      "s_add_u32 s45, s45, 4096\n"
      "s_add_u32 s62, s18, s45\n"
      "s_addc_u32 s63, s19, 0\n"
      "s_bitcmp1_b32 s34, 1\n"
      "s_cselect_b32 s85, 0x80008000, 0\n"
      "s_lshl3_add_u32 s42, s43, s83\n"
      "s_lshl2_add_u32 s42, s42, s33\n"
      "s_lshl_b32 s42, s42, 9\n"
      "s_add_u32 s42, s42, 1048576\n"
      "s_mov_b32 s64, 0xaaaaaaaa\n"
      "s_mov_b32 s65, 0xaaaaaaaa\n"
      "s_mov_b32 s66, 0xcccccccc\n"
      "s_mov_b32 s67, 0xcccccccc\n"
      "s_add_u32 s45, s80, 512\n"
      "s_and_b32 s45, s45, 0x380\n"
      "s_add_u32 s46, s68, s45\n"
      "s_addc_u32 s47, s69, 0\n"
      "s_add_u32 m0, s41, 8192\n"
      "s_nop 0\n"
      "global_load_lds_dwordx4 v240, s[46:47] offset:0\n"
      "global_load_lds_dwordx4 v241, s[46:47] offset:1024\n"
      "global_load_lds_dwordx4 v254, s[46:47] offset:2048\n"
      "global_load_lds_dwordx4 v255, s[46:47] offset:3072\n"
      "s_add_u32 s45, s80, 768\n"
      "s_and_b32 s45, s45, 0x380\n"
      "s_add_u32 s46, s68, s45\n"
      "s_addc_u32 s47, s69, 0\n"
      "s_add_u32 m0, s41, 12288\n"
      "s_nop 0\n"
      "global_load_lds_dwordx4 v240, s[46:47] offset:0\n"
      "global_load_lds_dwordx4 v241, s[46:47] offset:1024\n"
      "global_load_lds_dwordx4 v254, s[46:47] offset:2048\n"
      "global_load_lds_dwordx4 v255, s[46:47] offset:3072\n"
      "s_waitcnt vmcnt(13)\n"
      "s_barrier\n"
      "ds_read_b128 v[0:3], v248 offset:0\n"
      "ds_read_b128 v[4:7], v248 offset:2048\n"
      "ds_read_b128 v[8:11], v249 offset:0\n"
      "ds_read_b128 v[12:15], v249 offset:2048\n"
      "ds_read_b128 a[192:195], v250 offset:0\n"
      "ds_read_b128 a[196:199], v250 offset:2048\n"
      "ds_read_b128 a[200:203], v250 offset:32768\n"
      "ds_read_b128 a[204:207], v251 offset:0\n"
      "ds_read_b128 a[208:211], v251 offset:2048\n"
      "ds_read_b128 a[212:215], v251 offset:32768\n"
      "s_waitcnt lgkmcnt(5)\n"
      "v_mfma_f32_16x16x32_bf16 v[192:195], v[0:3], a[192:195], 0\n"
      "s_add_u32 s70, s34, 1\n"
      "s_and_b32 s70, s70, 3\n"
      "s_lshl_b32 s70, s70, 15\n"
      "s_or_b32 s70, s70, s81\n"
      "s_xor_b32 s70, s70, 128\n"
      "s_add_u32 s70, s12, s70\n"
      "s_addc_u32 s71, s13, 0\n"
      "s_add_u32 m0, s82, 81920\n"
      "v_mfma_f32_16x16x32_bf16 v[196:199], v[4:7], a[192:195], 0\n"
      "global_load_lds_dwordx4 v240, s[70:71] offset:0\n"
      "ds_read_b128 a[216:219], v250 offset:4096\n"
      "s_waitcnt lgkmcnt(5)\n"
      "v_mfma_f32_16x16x32_bf16 v[200:203], v[0:3], a[196:199], 0\n"
      "ds_read_b128 a[220:223], v250 offset:6144\n"
      "ds_read_b128 a[224:227], v250 offset:36864\n"
      "v_mfma_f32_16x16x32_bf16 v[204:207], v[4:7], a[196:199], 0\n"
      "global_load_lds_dwordx4 v241, s[70:71] offset:1024\n"
      "s_waitcnt vmcnt(11)\n"
      "ds_read_b128 v[16:19], v248 offset:4096\n"
      "s_waitcnt lgkmcnt(7)\n"
      "v_mfma_f32_16x16x32_bf16 v[208:211], v[0:3], a[200:203], 0\n"
      "ds_read_b128 v[20:23], v248 offset:6144\n"
      "ds_read_b128 v[24:27], v249 offset:4096\n"
      "v_mfma_f32_16x16x32_bf16 v[212:215], v[4:7], a[200:203], 0\n"
      "global_load_lds_dwordx4 v254, s[70:71] offset:2048\n"
      "ds_read_b128 v[28:31], v249 offset:6144\n"
      "s_waitcnt lgkmcnt(9)\n"
      "v_mfma_f32_16x16x32_bf16 v[192:195], v[8:11], a[204:207], v[192:195]\n"
      "ds_read_b128 a[228:231], v251 offset:4096\n"
      "ds_read_b128 a[232:235], v251 offset:6144\n"
      "v_mfma_f32_16x16x32_bf16 v[196:199], v[12:15], a[204:207], v[196:199]\n"
      "global_load_lds_dwordx4 v255, s[70:71] offset:3072\n"
      "ds_read_b128 a[236:239], v251 offset:36864\n"
      "s_waitcnt lgkmcnt(11)\n"
      "v_mfma_f32_16x16x32_bf16 v[200:203], v[8:11], a[208:211], v[200:203]\n"
      "s_add_u32 s70, s34, 2\n"
      "s_and_b32 s70, s70, 3\n"
      "s_lshl_b32 s70, s70, 15\n"
      "s_or_b32 s70, s70, s81\n"
      "s_xor_b32 s70, s70, 128\n"
      "s_add_u32 s70, s12, s70\n"
      "s_addc_u32 s71, s13, 0\n"
      "s_add_u32 m0, s82, 114688\n"
      "v_mfma_f32_16x16x32_bf16 v[204:207], v[12:15], a[208:211], v[204:207]\n"
      "global_load_lds_dwordx4 v240, s[70:71] offset:0\n"
      "s_waitcnt lgkmcnt(10)\n"
      "v_mfma_f32_16x16x32_bf16 v[208:211], v[8:11], a[212:215], v[208:211]\n"
      "v_mfma_f32_16x16x32_bf16 v[212:215], v[12:15], a[212:215], v[212:215]\n"
      "global_load_lds_dwordx4 v241, s[70:71] offset:1024\n"
      "s_waitcnt lgkmcnt(6)\n"
      "v_mfma_f32_16x16x32_bf16 v[192:195], v[16:19], a[216:219], v[192:195]\n"
      "s_add_u32 s70, s80, 128\n"
      "s_and_b32 s70, s70, 0x380\n"
      "s_add_u32 s70, s68, s70\n"
      "s_addc_u32 s71, s69, 0\n"
      "s_add_u32 m0, s41, 0\n"
      "s_waitcnt lgkmcnt(5)\n"
      "v_mfma_f32_16x16x32_bf16 v[196:199], v[20:23], a[216:219], v[196:199]\n"
      "global_load_lds_dwordx4 v240, s[70:71] offset:0\n"
      "ds_read_b128 a[240:243], v250 offset:8192\n"
      "v_mfma_f32_16x16x32_bf16 v[200:203], v[16:19], a[220:223], v[200:203]\n"
      "ds_read_b128 a[244:247], v250 offset:10240\n"
      "ds_read_b128 a[248:251], v250 offset:40960\n"
      "v_mfma_f32_16x16x32_bf16 v[204:207], v[20:23], a[220:223], v[204:207]\n"
      "global_load_lds_dwordx4 v241, s[70:71] offset:1024\n"
      "s_waitcnt vmcnt(12)\n"
      "ds_read_b128 v[32:35], v248 offset:8192\n"
      "v_mfma_f32_16x16x32_bf16 v[208:211], v[16:19], a[224:227], v[208:211]\n"
      "ds_read_b128 v[36:39], v248 offset:10240\n"
      "ds_read_b128 v[40:43], v249 offset:8192\n"
      "v_mfma_f32_16x16x32_bf16 v[212:215], v[20:23], a[224:227], v[212:215]\n"
      "global_load_lds_dwordx4 v254, s[70:71] offset:2048\n"
      "ds_read_b128 v[44:47], v249 offset:10240\n"
      "s_waitcnt lgkmcnt(9)\n"
      "v_mfma_f32_16x16x32_bf16 v[192:195], v[24:27], a[228:231], v[192:195]\n"
      "ds_read_b128 a[252:255], v251 offset:8192\n"
      "ds_read_b128 a[192:195], v251 offset:10240\n"
      "v_mfma_f32_16x16x32_bf16 v[196:199], v[28:31], a[228:231], v[196:199]\n"
      "global_load_lds_dwordx4 v255, s[70:71] offset:3072\n"
      "ds_read_b128 a[196:199], v251 offset:40960\n"
      "s_waitcnt lgkmcnt(11)\n"
      "v_mfma_f32_16x16x32_bf16 v[200:203], v[24:27], a[232:235], v[200:203]\n"
      "s_add_u32 s70, s80, 384\n"
      "s_and_b32 s70, s70, 0x380\n"
      "s_add_u32 s70, s68, s70\n"
      "s_addc_u32 s71, s69, 0\n"
      "s_add_u32 m0, s41, 4096\n"
      "v_mfma_f32_16x16x32_bf16 v[204:207], v[28:31], a[232:235], v[204:207]\n"
      "global_load_lds_dwordx4 v240, s[70:71] offset:0\n"
      "s_waitcnt lgkmcnt(10)\n"
      "v_mfma_f32_16x16x32_bf16 v[208:211], v[24:27], a[236:239], v[208:211]\n"
      "v_mfma_f32_16x16x32_bf16 v[212:215], v[28:31], a[236:239], v[212:215]\n"
      "global_load_lds_dwordx4 v241, s[70:71] offset:1024\n"
      "s_waitcnt lgkmcnt(6)\n"
      "v_mfma_f32_16x16x32_bf16 v[192:195], v[32:35], a[240:243], v[192:195]\n"
      "ds_read_b128 a[200:203], v250 offset:12288\n"
      "ds_read_b128 a[204:207], v250 offset:14336\n"
      "s_waitcnt lgkmcnt(7)\n"
      "v_mfma_f32_16x16x32_bf16 v[196:199], v[36:39], a[240:243], v[196:199]\n"
      "global_load_lds_dwordx4 v254, s[70:71] offset:2048\n"
      "ds_read_b128 a[208:211], v250 offset:45056\n"
      "v_mfma_f32_16x16x32_bf16 v[200:203], v[32:35], a[244:247], v[200:203]\n"
      "s_waitcnt vmcnt(13)\n"
      "ds_read_b128 v[48:51], v248 offset:12288\n"
      "v_mfma_f32_16x16x32_bf16 v[204:207], v[36:39], a[244:247], v[204:207]\n"
      "global_load_lds_dwordx4 v255, s[70:71] offset:3072\n"
      "ds_read_b128 v[52:55], v248 offset:14336\n"
      "v_mfma_f32_16x16x32_bf16 v[208:211], v[32:35], a[248:251], v[208:211]\n"
      "s_add_u32 s70, s80, 640\n"
      "s_and_b32 s70, s70, 0x380\n"
      "s_add_u32 s70, s68, s70\n"
      "s_addc_u32 s71, s69, 0\n"
      "s_waitcnt lgkmcnt(8)\n"
      "s_add_u32 m0, s41, 8192\n"
      "v_mfma_f32_16x16x32_bf16 v[212:215], v[36:39], a[248:251], v[212:215]\n"
      "global_load_lds_dwordx4 v240, s[70:71] offset:0\n"
      "ds_read_b128 v[56:59], v249 offset:12288\n"
      "s_waitcnt lgkmcnt(8)\n"
      "v_mfma_f32_16x16x32_bf16 v[192:195], v[40:43], a[252:255], v[192:195]\n"
      "ds_read_b128 v[60:63], v249 offset:14336\n"
      "ds_read_b128 a[212:215], v251 offset:12288\n"
      "v_readfirstlane_b32 s37, v235\n"
      "v_mfma_f32_16x16x32_bf16 v[196:199], v[44:47], a[252:255], v[196:199]\n"
      "s_nop 0\n"
      "s_mul_i32 s37, s37, 0x9E3779B1\n"
      "global_load_lds_dwordx4 v241, s[70:71] offset:1024\n"
      "s_waitcnt lgkmcnt(9)\n"
      "v_mfma_f32_16x16x32_bf16 v[200:203], v[40:43], a[192:195], v[200:203]\n"
      "s_add_u32 s37, s37, 0x7F4A7C15\n"
      "ds_read_b128 a[216:219], v251 offset:14336\n"
      "ds_read_b128 a[220:223], v251 offset:45056\n"
      "s_mov_b32 s38, 0x8000\n"
      "v_mfma_f32_16x16x32_bf16 v[204:207], v[44:47], a[192:195], v[204:207]\n"
      "global_load_lds_dwordx4 v254, s[70:71] offset:2048\n"
      "s_waitcnt lgkmcnt(10)\n"
      "v_mfma_f32_16x16x32_bf16 v[208:211], v[40:43], a[196:199], v[208:211]\n"
      "v_mfma_f32_16x16x32_bf16 v[212:215], v[44:47], a[196:199], v[212:215]\n"
      "global_load_lds_dwordx4 v255, s[70:71] offset:3072\n"
      "s_waitcnt lgkmcnt(6)\n"
      "v_mfma_f32_16x16x32_bf16 v[192:195], v[48:51], a[200:203], v[192:195]\n"
      "s_add_u32 s70, s80, 896\n"
      "s_and_b32 s70, s70, 0x380\n"
      "s_add_u32 s70, s68, s70\n"
      "s_addc_u32 s71, s69, 0\n"
      "s_waitcnt lgkmcnt(3)\n"
      "s_add_u32 m0, s41, 12288\n"
      "v_mfma_f32_16x16x32_bf16 v[196:199], v[52:55], a[200:203], v[196:199]\n"
      "global_load_lds_dwordx4 v240, s[70:71] offset:0\n"
      "s_waitcnt vmcnt(13)\n"
      "s_barrier\n"
      "v_mfma_f32_16x16x32_bf16 v[200:203], v[48:51], a[204:207], v[200:203]\n"
      "ds_read_b128 a[224:227], v250 offset:16384\n"
      "ds_read_b128 a[228:231], v250 offset:18432\n"
      "v_mfma_f32_16x16x32_bf16 v[204:207], v[52:55], a[204:207], v[204:207]\n"
      "global_load_lds_dwordx4 v241, s[70:71] offset:1024\n"
      "ds_read_b128 a[232:235], v250 offset:49152\n"
      "v_mfma_f32_16x16x32_bf16 v[208:211], v[48:51], a[208:211], v[208:211]\n"
      "s_waitcnt vmcnt(10)\n"
      "ds_read_b128 v[64:67], v248 offset:0\n"
      "v_mfma_f32_16x16x32_bf16 v[212:215], v[52:55], a[208:211], v[212:215]\n"
      "global_load_lds_dwordx4 v254, s[70:71] offset:2048\n"
      "ds_read_b128 v[68:71], v248 offset:2048\n"
      "s_waitcnt lgkmcnt(7)\n"
      "v_mfma_f32_16x16x32_bf16 v[192:195], v[56:59], a[212:215], v[192:195]\n"
      "ds_read_b128 v[72:75], v249 offset:0\n"
      "ds_read_b128 v[76:79], v249 offset:2048\n"
      "v_mfma_f32_16x16x32_bf16 v[196:199], v[60:63], a[212:215], v[196:199]\n"
      "global_load_lds_dwordx4 v255, s[70:71] offset:3072\n"
      "ds_read_b128 a[236:239], v251 offset:16384\n"
      "s_waitcnt lgkmcnt(9)\n"
      "v_mfma_f32_16x16x32_bf16 v[200:203], v[56:59], a[216:219], v[200:203]\n"
      "ds_read_b128 a[240:243], v251 offset:18432\n"
      "ds_read_b128 a[244:247], v251 offset:49152\n"
      "v_mfma_f32_16x16x32_bf16 v[204:207], v[60:63], a[216:219], v[204:207]\n"
      "s_waitcnt lgkmcnt(10)\n"
      "v_mfma_f32_16x16x32_bf16 v[208:211], v[56:59], a[220:223], v[208:211]\n"
      "v_mfma_f32_16x16x32_bf16 v[212:215], v[60:63], a[220:223], v[212:215]\n"
      "s_waitcnt lgkmcnt(6)\n"
      "v_mfma_f32_16x16x32_bf16 v[192:195], v[64:67], a[224:227], v[192:195]\n"
      "s_waitcnt lgkmcnt(3)\n"
      "s_add_u32 m0, s41, 0\n"
      "s_nop 0\n"
      "global_load_lds_dwordx4 v238, s[24:25]\n"
      "s_add_u32 m0, s41, 1024\n"
      "s_nop 0\n"
      "global_load_lds_dwordx4 v238, s[22:23]\n"
      "v_mfma_f32_16x16x32_bf16 v[196:199], v[68:71], a[224:227], v[196:199]\n"
      "ds_read_b128 a[248:251], v250 offset:20480\n"
      "ds_read_b128 a[252:255], v250 offset:22528\n"
      "v_mfma_f32_16x16x32_bf16 v[200:203], v[64:67], a[228:231], v[200:203]\n"
      "s_add_u32 s70, s34, 2\n"
      "s_and_b32 s70, s70, 3\n"
      "s_lshl_b32 s70, s70, 15\n"
      "s_or_b32 s70, s70, s81\n"
      "s_add_u32 s70, s12, s70\n"
      "s_addc_u32 s71, s13, 0\n"
      "s_add_u32 m0, s82, 98304\n"
      "v_mfma_f32_16x16x32_bf16 v[204:207], v[68:71], a[228:231], v[204:207]\n"
      "global_load_lds_dwordx4 v254, s[70:71] offset:2048\n"
      "ds_read_b128 a[192:195], v250 offset:53248\n"
      "v_mfma_f32_16x16x32_bf16 v[208:211], v[64:67], a[232:235], v[208:211]\n"
      "s_waitcnt vmcnt(11)\n"
      "ds_read_b128 v[80:83], v248 offset:4096\n"
      "v_mfma_f32_16x16x32_bf16 v[212:215], v[68:71], a[232:235], v[212:215]\n"
      "ds_read_b128 v[84:87], v248 offset:6144\n"
      "ds_read_b128 v[88:91], v249 offset:4096\n"
      "s_waitcnt lgkmcnt(8)\n"
      "v_mfma_f32_16x16x32_bf16 v[192:195], v[72:75], a[236:239], v[192:195]\n"
      "global_load_lds_dwordx4 v255, s[70:71] offset:3072\n"
      "ds_read_b128 v[92:95], v249 offset:6144\n"
      "v_mfma_f32_16x16x32_bf16 v[196:199], v[76:79], a[236:239], v[196:199]\n"
      "ds_read_b128 a[196:199], v251 offset:20480\n"
      "ds_read_b128 a[200:203], v251 offset:22528\n"
      "s_waitcnt lgkmcnt(10)\n"
      "v_mfma_f32_16x16x32_bf16 v[200:203], v[72:75], a[240:243], v[200:203]\n"
      "s_add_u32 s70, s34, 2\n"
      "s_and_b32 s70, s70, 3\n"
      "s_lshl_b32 s70, s70, 15\n"
      "s_or_b32 s70, s70, s81\n"
      "s_xor_b32 s70, s70, 128\n"
      "s_add_u32 s70, s12, s70\n"
      "s_addc_u32 s71, s13, 0\n"
      "s_add_u32 m0, s82, 114688\n"
      "v_mfma_f32_16x16x32_bf16 v[204:207], v[76:79], a[240:243], v[204:207]\n"
      "global_load_lds_dwordx4 v254, s[70:71] offset:2048\n"
      "ds_read_b128 a[204:207], v251 offset:53248\n"
      "s_waitcnt lgkmcnt(10)\n"
      "v_mfma_f32_16x16x32_bf16 v[208:211], v[72:75], a[244:247], v[208:211]\n"
      "v_mfma_f32_16x16x32_bf16 v[212:215], v[76:79], a[244:247], v[212:215]\n"
      "s_waitcnt lgkmcnt(6)\n"
      "v_mfma_f32_16x16x32_bf16 v[192:195], v[80:83], a[248:251], v[192:195]\n"
      "global_load_lds_dwordx4 v255, s[70:71] offset:3072\n"
      "ds_read_b128 a[208:211], v250 offset:24576\n"
      "s_waitcnt lgkmcnt(6)\n"
      "v_mfma_f32_16x16x32_bf16 v[196:199], v[84:87], a[248:251], v[196:199]\n"
      "ds_read_b128 a[212:215], v250 offset:26624\n"
      "ds_read_b128 a[216:219], v250 offset:57344\n"
      "v_mfma_f32_16x16x32_bf16 v[200:203], v[80:83], a[252:255], v[200:203]\n"
      "s_add_u32 s70, s34, 3\n"
      "s_and_b32 s70, s70, 3\n"
      "s_lshl_b32 s70, s70, 15\n"
      "s_or_b32 s70, s70, s81\n"
      "s_add_u32 s70, s12, s70\n"
      "s_addc_u32 s71, s13, 0\n"
      "s_add_u32 m0, s82, 131072\n"
      "v_mfma_f32_16x16x32_bf16 v[204:207], v[84:87], a[252:255], v[204:207]\n"
      "global_load_lds_dwordx4 v240, s[70:71] offset:0\n"
      "s_waitcnt vmcnt(11)\n"
      "ds_read_b128 v[96:99], v248 offset:8192\n"
      "v_mfma_f32_16x16x32_bf16 v[208:211], v[80:83], a[192:195], v[208:211]\n"
      "ds_read_b128 v[100:103], v248 offset:10240\n"
      "ds_read_b128 v[104:107], v249 offset:8192\n"
      "v_mfma_f32_16x16x32_bf16 v[212:215], v[84:87], a[192:195], v[212:215]\n"
      "ds_read_b128 v[108:111], v249 offset:10240\n"
      "ds_read_b128 a[220:223], v251 offset:24576\n"
      "s_waitcnt lgkmcnt(10)\n"
      "v_mfma_f32_16x16x32_bf16 v[192:195], v[88:91], a[196:199], v[192:195]\n"
      "global_load_lds_dwordx4 v241, s[70:71] offset:1024\n"
      "ds_read_b128 a[224:227], v251 offset:26624\n"
      "v_mfma_f32_16x16x32_bf16 v[196:199], v[92:95], a[196:199], v[196:199]\n"
      "ds_read_b128 a[228:231], v251 offset:57344\n"
      "s_waitcnt lgkmcnt(11)\n"
      "v_mfma_f32_16x16x32_bf16 v[200:203], v[88:91], a[200:203], v[200:203]\n"
      "v_mfma_f32_16x16x32_bf16 v[204:207], v[92:95], a[200:203], v[204:207]\n"
      "global_load_lds_dwordx4 v254, s[70:71] offset:2048\n"
      "s_waitcnt lgkmcnt(10)\n"
      "v_mfma_f32_16x16x32_bf16 v[208:211], v[88:91], a[204:207], v[208:211]\n"
      "v_mfma_f32_16x16x32_bf16 v[212:215], v[92:95], a[204:207], v[212:215]\n"
      "s_waitcnt lgkmcnt(6)\n"
      "v_mfma_f32_16x16x32_bf16 v[192:195], v[96:99], a[208:211], v[192:195]\n"
      "global_load_lds_dwordx4 v255, s[70:71] offset:3072\n"
      "ds_read_b128 a[232:235], v250 offset:28672\n"
      "s_waitcnt lgkmcnt(6)\n"
      "v_mfma_f32_16x16x32_bf16 v[196:199], v[100:103], a[208:211], v[196:199]\n"
      "ds_read_b128 a[236:239], v250 offset:30720\n"
      "ds_read_b128 a[240:243], v250 offset:61440\n"
      "v_mfma_f32_16x16x32_bf16 v[200:203], v[96:99], a[212:215], v[200:203]\n"
      "s_add_u32 s70, s34, 3\n"
      "s_and_b32 s70, s70, 3\n"
      "s_lshl_b32 s70, s70, 15\n"
      "s_or_b32 s70, s70, s81\n"
      "s_xor_b32 s70, s70, 128\n"
      "s_add_u32 s70, s12, s70\n"
      "s_addc_u32 s71, s13, 0\n"
      "s_add_u32 m0, s82, 147456\n"
      "v_mfma_f32_16x16x32_bf16 v[204:207], v[100:103], a[212:215], v[204:207]\n"
      "global_load_lds_dwordx4 v240, s[70:71] offset:0\n"
      "s_waitcnt vmcnt(11)\n"
      "ds_read_b128 v[112:115], v248 offset:12288\n"
      "v_mfma_f32_16x16x32_bf16 v[208:211], v[96:99], a[216:219], v[208:211]\n"
      "ds_read_b128 v[116:119], v248 offset:14336\n"
      "ds_read_b128 v[120:123], v249 offset:12288\n"
      "v_mfma_f32_16x16x32_bf16 v[212:215], v[100:103], a[216:219], v[212:215]\n"
      "ds_read_b128 v[124:127], v249 offset:14336\n"
      "ds_read_b128 a[244:247], v251 offset:28672\n"
      "s_waitcnt lgkmcnt(10)\n"
      "v_mfma_f32_16x16x32_bf16 v[192:195], v[104:107], a[220:223], v[192:195]\n"
      "global_load_lds_dwordx4 v241, s[70:71] offset:1024\n"
      "ds_read_b128 a[248:251], v251 offset:30720\n"
      "v_mfma_f32_16x16x32_bf16 v[196:199], v[108:111], a[220:223], v[196:199]\n"
      "ds_read_b128 a[252:255], v251 offset:61440\n"
      "s_waitcnt lgkmcnt(11)\n"
      "v_mfma_f32_16x16x32_bf16 v[200:203], v[104:107], a[224:227], v[200:203]\n"
      "v_mfma_f32_16x16x32_bf16 v[204:207], v[108:111], a[224:227], v[204:207]\n"
      "global_load_lds_dwordx4 v254, s[70:71] offset:2048\n"
      "s_waitcnt lgkmcnt(10)\n"
      "v_mfma_f32_16x16x32_bf16 v[208:211], v[104:107], a[228:231], v[208:211]\n"
      "v_mfma_f32_16x16x32_bf16 v[212:215], v[108:111], a[228:231], v[212:215]\n"
      "s_waitcnt lgkmcnt(6)\n"
      "v_mfma_f32_16x16x32_bf16 v[192:195], v[112:115], a[232:235], v[192:195]\n"
      "s_waitcnt vmcnt(11)\n"
      "ds_read_b128 v[136:139], v252 offset:0\n"
      "s_waitcnt lgkmcnt(6)\n"
      "v_mfma_f32_16x16x32_bf16 v[196:199], v[116:119], a[232:235], v[196:199]\n"
      "ds_read_b128 v[140:143], v252 offset:64\n"
      "global_load_lds_dwordx4 v255, s[70:71] offset:3072\n"
      "s_waitcnt lgkmcnt(4)\n"
      "v_mfma_f32_16x16x32_bf16 v[192:195], v[120:123], a[244:247], v[192:195]\n"
      "ds_read_b128 v[144:147], v252 offset:256\n"
      "v_mfma_f32_16x16x32_bf16 v[196:199], v[124:127], a[244:247], v[196:199]\n"
      "v_mfma_f32_16x16x32_bf16 v[200:203], v[112:115], a[236:239], v[200:203]\n"
      "ds_read_b128 v[148:151], v252 offset:320\n"
      "v_mfma_f32_16x16x32_bf16 v[204:207], v[116:119], a[236:239], v[204:207]\n"
      "s_waitcnt lgkmcnt(5)\n"
      "v_mfma_f32_16x16x32_bf16 v[200:203], v[120:123], a[248:251], v[200:203]\n"
      "ds_read_b128 v[152:155], v252 offset:512\n"
      "v_cvt_pk_bf16_f32 v224, v192, v193\n"
      "v_cvt_pk_bf16_f32 v225, v194, v195\n"
      "v_mfma_f32_16x16x32_bf16 v[204:207], v[124:127], a[248:251], v[204:207]\n"
      "v_cvt_pk_bf16_f32 v226, v196, v197\n"
      "v_cvt_pk_bf16_f32 v227, v198, v199\n"
      "v_xor_b32 v224, s85, v224\n"
      "v_mfma_f32_16x16x32_bf16 v[208:211], v[112:115], a[240:243], v[208:211]\n"
      "ds_read_b128 v[156:159], v252 offset:576\n"
      "v_xor_b32 v225, s85, v225\n"
      "v_xor_b32 v226, s85, v226\n"
      "v_mfma_f32_16x16x32_bf16 v[212:215], v[116:119], a[240:243], v[212:215]\n"
      "v_xor_b32 v227, s85, v227\n"
      "s_waitcnt lgkmcnt(6)\n"
      "v_mfma_f32_16x16x32_bf16 v[208:211], v[120:123], a[252:255], v[208:211]\n"
      "ds_read_b128 v[160:163], v252 offset:768\n"
      "global_store_dwordx4 v242, v[224:227], s[62:63] offset:-4096 sc1\n"
      "v_cvt_pk_bf16_f32 v228, v200, v201\n"
      "v_mfma_f32_16x16x32_bf16 v[212:215], v[124:127], a[252:255], v[212:215]\n"
      "v_cvt_pk_bf16_f32 v229, v202, v203\n"
      "v_cvt_pk_bf16_f32 v230, v204, v205\n"
      "v_cvt_pk_bf16_f32 v231, v206, v207\n"
      "s_waitcnt lgkmcnt(6)\n"
      "v_mfma_f32_16x16x32_bf16 v[128:131], v[0:3], v[136:139], 0\n"
      "ds_read_b128 v[164:167], v252 offset:832\n"
      "v_xor_b32 v228, s85, v228\n"
      "v_xor_b32 v229, s85, v229\n"
      "v_mfma_f32_16x16x32_bf16 v[132:135], v[4:7], v[136:139], 0\n"
      "v_xor_b32 v230, s85, v230\n"
      "v_xor_b32 v231, s85, v231\n"
      "s_waitcnt lgkmcnt(6)\n"
      "v_mfma_f32_16x16x32_bf16 v[128:131], v[8:11], v[140:143], v[128:131]\n"
      "ds_read_b128 v[136:139], v252 offset:128\n"
      "global_store_dwordx4 v242, v[228:231], s[62:63] offset:-3072 sc1\n"
      "v_cvt_pk_bf16_f32 v232, v208, v209\n"
      "v_mfma_f32_16x16x32_bf16 v[132:135], v[12:15], v[140:143], v[132:135]\n"
      "v_cvt_pk_bf16_f32 v233, v210, v211\n"
      "v_cvt_pk_bf16_f32 v234, v212, v213\n"
      "v_cvt_pk_bf16_f32 v235, v214, v215\n"
      "s_waitcnt lgkmcnt(6)\n"
      "v_mfma_f32_16x16x32_bf16 v[128:131], v[16:19], v[144:147], v[128:131]\n"
      "ds_read_b128 v[140:143], v252 offset:192\n"
      "v_xor_b32 v232, s85, v232\n"
      "v_xor_b32 v233, s85, v233\n"
      "v_mfma_f32_16x16x32_bf16 v[132:135], v[20:23], v[144:147], v[132:135]\n"
      "v_xor_b32 v234, s85, v234\n"
      "v_xor_b32 v235, s85, v235\n"
      "s_waitcnt lgkmcnt(6)\n"
      "v_mfma_f32_16x16x32_bf16 v[128:131], v[24:27], v[148:151], v[128:131]\n"
      "ds_read_b128 v[144:147], v252 offset:384\n"
      "global_store_dwordx4 v242, v[232:235], s[62:63] offset:-2048 sc1\n"
      "v_mfma_f32_16x16x32_bf16 v[132:135], v[28:31], v[148:151], v[132:135]\n"
      "s_xor_b32 s70, s76, 0\n"
      "s_add_u32 s70, s8, s70\n"
      "s_addc_u32 s71, s9, 0\n"
      "s_add_u32 m0, s41, 4096\n"
      "s_waitcnt lgkmcnt(6)\n"
      "v_mfma_f32_16x16x32_bf16 v[128:131], v[32:35], v[152:155], v[128:131]\n"
      "s_add_u32 s48, s82, 32768\n"
      "ds_read_b128 v[148:151], v252 offset:448\n"
      "global_load_lds_dwordx4 v244, s[70:71] offset:0\n"
      "v_subrev_u32 v236, s41, v248\n"
      "v_mfma_f32_16x16x32_bf16 v[132:135], v[36:39], v[152:155], v[132:135]\n"
      "s_add_u32 s48, s48, 65536\n"
      "global_load_lds_dwordx4 v245, s[70:71] offset:1024\n"
      "v_add_u32 v236, s48, v236\n"
      "v_xor_b32 v237, 64, v236\n"
      "s_waitcnt lgkmcnt(6)\n"
      "v_mfma_f32_16x16x32_bf16 v[128:131], v[40:43], v[156:159], v[128:131]\n"
      "ds_read_b128 v[152:155], v252 offset:640\n"
      "global_load_lds_dwordx4 v246, s[70:71] offset:2048\n"
      "v_mfma_f32_16x16x32_bf16 v[132:135], v[44:47], v[156:159], v[132:135]\n"
      "global_load_lds_dwordx4 v247, s[70:71] offset:3072\n"
      "s_xor_b32 s70, s77, 0\n"
      "s_add_u32 s70, s8, s70\n"
      "s_addc_u32 s71, s9, 0\n"
      "s_add_u32 m0, s41, 8192\n"
      "s_waitcnt lgkmcnt(6)\n"
      "v_mfma_f32_16x16x32_bf16 v[128:131], v[48:51], v[160:163], v[128:131]\n"
      "ds_read_b128 v[156:159], v252 offset:704\n"
      "global_load_lds_dwordx4 v244, s[70:71] offset:0\n"
      "v_mfma_f32_16x16x32_bf16 v[132:135], v[52:55], v[160:163], v[132:135]\n"
      "global_load_lds_dwordx4 v245, s[70:71] offset:1024\n"
      "s_waitcnt lgkmcnt(6)\n"
      "v_mfma_f32_16x16x32_bf16 v[128:131], v[56:59], v[164:167], v[128:131]\n"
      "ds_read_b128 v[160:163], v252 offset:896\n"
      "global_load_lds_dwordx4 v246, s[70:71] offset:2048\n"
      "v_mfma_f32_16x16x32_bf16 v[132:135], v[60:63], v[164:167], v[132:135]\n"
      "global_load_lds_dwordx4 v247, s[70:71] offset:3072\n"
      "s_waitcnt vmcnt(11)\n"
      "s_barrier\n"
      "s_waitcnt lgkmcnt(6)\n"
      "v_mfma_f32_16x16x32_bf16 v[128:131], v[64:67], v[136:139], v[128:131]\n"
      "ds_read_b128 v[164:167], v252 offset:960\n"
      "s_add_u32 s70, s34, 4\n"
      "s_and_b32 s70, s70, 3\n"
      "s_lshl_b32 s70, s70, 15\n"
      "s_or_b32 s70, s70, s81\n"
      "s_add_u32 s70, s12, s70\n"
      "s_addc_u32 s71, s13, 0\n"
      "s_add_u32 m0, s82, 65536\n"
      "v_mfma_f32_16x16x32_bf16 v[132:135], v[68:71], v[136:139], v[132:135]\n"
      "global_load_lds_dwordx4 v240, s[70:71] offset:0\n"
      "ds_read_b128 a[192:195], v250 offset:34816\n"
      "s_waitcnt lgkmcnt(7)\n"
      "v_mfma_f32_16x16x32_bf16 v[128:131], v[72:75], v[140:143], v[128:131]\n"
      "ds_read_b128 a[196:199], v251 offset:34816\n"
      "ds_read_b128 a[200:203], v250 offset:38912\n"
      "v_mfma_f32_16x16x32_bf16 v[132:135], v[76:79], v[140:143], v[132:135]\n"
      "ds_read_b128 a[204:207], v251 offset:38912\n"
      "ds_read_b128 a[208:211], v250 offset:43008\n"
      "s_waitcnt lgkmcnt(10)\n"
      "v_mfma_f32_16x16x32_bf16 v[128:131], v[80:83], v[144:147], v[128:131]\n"
      "ds_read_b128 a[212:215], v251 offset:43008\n"
      "ds_read_b128 a[216:219], v250 offset:47104\n"
      "v_mfma_f32_16x16x32_bf16 v[132:135], v[84:87], v[144:147], v[132:135]\n"
      "global_load_lds_dwordx4 v241, s[70:71] offset:1024\n"
      "ds_read_b128 a[220:223], v251 offset:47104\n"
      "s_waitcnt lgkmcnt(12)\n"
      "v_mfma_f32_16x16x32_bf16 v[128:131], v[88:91], v[148:151], v[128:131]\n"
      "ds_read_b128 a[224:227], v250 offset:51200\n"
      "ds_read_b128 a[228:231], v251 offset:51200\n"
      "v_mfma_f32_16x16x32_bf16 v[132:135], v[92:95], v[148:151], v[132:135]\n"
      "ds_read_b128 a[232:235], v250 offset:55296\n"
      "s_waitcnt lgkmcnt(14)\n"
      "v_mfma_f32_16x16x32_bf16 v[128:131], v[96:99], v[152:155], v[128:131]\n"
      "v_mfma_f32_16x16x32_bf16 v[132:135], v[100:103], v[152:155], v[132:135]\n"
      "global_load_lds_dwordx4 v254, s[70:71] offset:2048\n"
      "ds_read_b128 a[236:239], v251 offset:55296\n"
      "s_waitcnt lgkmcnt(14)\n"
      "v_mfma_f32_16x16x32_bf16 v[128:131], v[104:107], v[156:159], v[128:131]\n"
      "v_mfma_f32_16x16x32_bf16 v[132:135], v[108:111], v[156:159], v[132:135]\n"
      "ds_read_b128 a[240:243], v250 offset:59392\n"
      "s_waitcnt lgkmcnt(14)\n"
      "v_mfma_f32_16x16x32_bf16 v[128:131], v[112:115], v[160:163], v[128:131]\n"
      "v_mfma_f32_16x16x32_bf16 v[132:135], v[116:119], v[160:163], v[132:135]\n"
      "global_load_lds_dwordx4 v255, s[70:71] offset:3072\n"
      "ds_read_b128 a[244:247], v251 offset:59392\n"
      "s_waitcnt lgkmcnt(14)\n"
      "v_mfma_f32_16x16x32_bf16 v[128:131], v[120:123], v[164:167], v[128:131]\n"
      "v_mfma_f32_16x16x32_bf16 v[132:135], v[124:127], v[164:167], v[132:135]\n"
      "ds_read_b128 a[248:251], v250 offset:63488\n"
      "s_waitcnt lgkmcnt(14)\n"
      "v_mfma_f32_16x16x32_bf16 v[216:219], v[0:3], a[192:195], 0\n"
      "v_mfma_f32_16x16x32_bf16 v[220:223], v[4:7], a[192:195], 0\n"
      "s_add_u32 s70, s34, 4\n"
      "s_and_b32 s70, s70, 3\n"
      "s_lshl_b32 s70, s70, 15\n"
      "s_or_b32 s70, s70, s81\n"
      "s_xor_b32 s70, s70, 128\n"
      "s_add_u32 s70, s12, s70\n"
      "s_addc_u32 s71, s13, 0\n"
      "s_add_u32 m0, s82, 81920\n"
      "s_waitcnt lgkmcnt(13)\n"
      "v_mfma_f32_16x16x32_bf16 v[216:219], v[8:11], a[196:199], v[216:219]\n"
      "s_mov_b32 s22, 0xf0f0f0f0\n"
      "global_load_lds_dwordx4 v240, s[70:71] offset:0\n"
      "ds_read_b128 a[252:255], v251 offset:63488\n"
      "s_mov_b32 s23, 0xf0f0f0f0\n"
      "v_mfma_f32_16x16x32_bf16 v[220:223], v[12:15], a[196:199], v[220:223]\n"
      "v_xor_b32 v250, 196608, v250\n"
      "v_xor_b32 v251, 196608, v251\n"
      "s_waitcnt lgkmcnt(13)\n"
      "v_mfma_f32_16x16x32_bf16 v[216:219], v[16:19], a[200:203], v[216:219]\n"
      "ds_read_b128 a[192:195], v250 offset:0\n"
      "v_cndmask_b32 v128, v128, v129, s[64:65]\n"
      "v_cndmask_b32 v130, v130, v131, s[64:65]\n"
      "v_mfma_f32_16x16x32_bf16 v[220:223], v[20:23], a[200:203], v[220:223]\n"
      "ds_read_b128 a[196:199], v250 offset:2048\n"
      "v_cndmask_b32 v132, v132, v133, s[64:65]\n"
      "v_cndmask_b32 v134, v134, v135, s[64:65]\n"
      "s_waitcnt lgkmcnt(14)\n"
      "v_mfma_f32_16x16x32_bf16 v[216:219], v[24:27], a[204:207], v[216:219]\n"
      "global_load_lds_dwordx4 v241, s[70:71] offset:1024\n"
      "v_cndmask_b32 v128, v128, v130, s[66:67]\n"
      "v_cndmask_b32 v132, v132, v134, s[66:67]\n"
      "v_mfma_f32_16x16x32_bf16 v[220:223], v[28:31], a[204:207], v[220:223]\n"
      "ds_read_b128 a[200:203], v251 offset:0\n"
      "v_lshrrev_b32 v133, 4, v243\n"
      "v_cndmask_b32 v128, v128, v132, s[22:23]\n"
      "s_waitcnt lgkmcnt(14)\n"
      "v_mfma_f32_16x16x32_bf16 v[216:219], v[32:35], a[208:211], v[216:219]\n"
      "v_and_b32 v134, 7, v243\n"
      "v_cvt_pk_bf16_f32 v128, v128, v128\n"
      "s_lshl_b32 s48, s40, 5\n"
      "v_mfma_f32_16x16x32_bf16 v[220:223], v[36:39], a[208:211], v[220:223]\n"
      "ds_read_b128 a[204:207], v251 offset:2048\n"
      "v_and_b32 v128, 0xffff0000, v128\n"
      "v_lshl_or_b32 v133, v133, 3, v134\n"
      "s_waitcnt lgkmcnt(14)\n"
      "v_mfma_f32_16x16x32_bf16 v[216:219], v[40:43], a[212:215], v[216:219]\n"
      "global_load_lds_dwordx4 v254, s[70:71] offset:2048\n"
      "v_add_u32 v133, s48, v133\n"
      "v_ashrrev_i32 v129, 31, v128\n"
      "v_mfma_f32_16x16x32_bf16 v[220:223], v[44:47], a[212:215], v[220:223]\n"
      "ds_read_b128 a[208:211], v250 offset:4096\n"
      "v_mul_f32 v130, 0xbfb8aa3b, v128\n"
      "v_or_b32 v129, 0x80000000, v129\n"
      "s_waitcnt lgkmcnt(14)\n"
      "v_mfma_f32_16x16x32_bf16 v[216:219], v[48:51], a[216:219], v[216:219]\n"
      "v_exp_f32 v130, v130\n"
      "v_xor_b32 v129, v129, v128\n"
      "v_sub_u32 v132, 0xfff, v133\n"
      "v_mfma_f32_16x16x32_bf16 v[220:223], v[52:55], a[216:219], v[220:223]\n"
      "ds_read_b128 a[212:215], v250 offset:6144\n"
      "v_add_f32 v130, 1.0, v130\n"
      "s_mov_b32 s49, 0xffff0000\n"
      "s_waitcnt lgkmcnt(14)\n"
      "v_mfma_f32_16x16x32_bf16 v[216:219], v[56:59], a[220:223], v[216:219]\n"
      "global_load_lds_dwordx4 v255, s[70:71] offset:3072\n"
      "v_and_or_b32 v129, v129, s49, v132\n"
      "v_rcp_f32 v130, v130\n"
      "v_mfma_f32_16x16x32_bf16 v[220:223], v[60:63], a[220:223], v[220:223]\n"
      "s_mov_b32 s64, 0x00ff00ff\n"
      "ds_read_b128 a[216:219], v251 offset:4096\n"
      "s_mov_b32 s65, 0x00ff00ff\n"
      "v_cndmask_b32 v129, v129, 0, s[64:65]\n"
      "s_waitcnt lgkmcnt(14)\n"
      "v_mfma_f32_16x16x32_bf16 v[216:219], v[64:67], a[224:227], v[216:219]\n"
      "v_add_u32 v238, s42, v242\n"
      "v_mov_b32 v224, s37\n"
      "v_mov_b32 v225, s37\n"
      "v_mov_b32 v226, s37\n"
      "v_mov_b32 v227, s37\n"
      "s_waitcnt vmcnt(16)\n"
      "s_mov_b64 exec, 0xff\n"
      "global_store_dwordx4 v238, v[224:227], s[58:59] offset:0 sc1\n"
      "s_mov_b64 exec, -1\n"
      "v_mfma_f32_16x16x32_bf16 v[220:223], v[68:71], a[224:227], v[220:223]\n"
      "ds_read_b128 a[220:223], v251 offset:6144\n"
      "v_cvt_pk_bf16_f32 v130, v130, v130\n"
      "s_lshr_b32 s48, s34, 1\n"
      "s_waitcnt lgkmcnt(14)\n"
      "v_mfma_f32_16x16x32_bf16 v[216:219], v[72:75], a[228:231], v[216:219]\n"
      "s_lshl2_add_u32 s48, s48, s35\n"
      "s_xor_b32 s70, s78, 0\n"
      "s_add_u32 s70, s8, s70\n"
      "s_addc_u32 s71, s9, 0\n"
      "s_add_u32 m0, s41, 12288\n"
      "v_mfma_f32_16x16x32_bf16 v[220:223], v[76:79], a[228:231], v[220:223]\n"
      "global_load_lds_dwordx4 v244, s[70:71] offset:0\n"
      "ds_read_b128 a[224:227], v250 offset:8192\n"
      "v_max_u32_dpp v129, v129, v129 quad_perm:[1,0,3,2] row_mask:0xf bank_mask:0xf\n"
      "s_waitcnt lgkmcnt(14)\n"
      "v_mfma_f32_16x16x32_bf16 v[216:219], v[80:83], a[232:235], v[216:219]\n"
      "v_and_b32 v130, 0xffff0000, v130\n"
      "s_lshl_b32 s49, s48, 15\n"
      "v_max_u32_dpp v129, v129, v129 quad_perm:[2,3,0,1] row_mask:0xf bank_mask:0xf\n"
      "v_mfma_f32_16x16x32_bf16 v[220:223], v[84:87], a[232:235], v[220:223]\n"
      "s_add_u32 s49, s49, 786432\n"
      "ds_read_b128 a[228:231], v250 offset:10240\n"
      "v_lshl_add_u32 v133, v133, 4, s49\n"
      "v_max_u32_dpp v129, v129, v129 row_half_mirror row_mask:0xf bank_mask:0xf\n"
      "s_waitcnt lgkmcnt(14)\n"
      "v_mfma_f32_16x16x32_bf16 v[216:219], v[88:91], a[236:239], v[216:219]\n"
      "s_lshl_b32 s48, s48, 6\n"
      "s_add_u32 s48, s48, s40\n"
      "v_max_u32_dpp v129, v129, v129 row_mirror row_mask:0xf bank_mask:0xf\n"
      "s_lshl_b32 s48, s48, 6\n"
      "v_mfma_f32_16x16x32_bf16 v[220:223], v[92:95], a[236:239], v[220:223]\n"
      "s_add_u32 s48, s48, 4096\n"
      "global_load_lds_dwordx4 v245, s[70:71] offset:1024\n"
      "ds_read_b128 a[232:235], v251 offset:8192\n"
      "v_mov_b32 v131, v129\n"
      "s_waitcnt lgkmcnt(14)\n"
      "v_mfma_f32_16x16x32_bf16 v[216:219], v[96:99], a[240:243], v[216:219]\n"
      "v_lshl_add_u32 v132, v134, 4, s48\n"
      "v_mov_b32 v137, s37\n"
      "v_permlane16_swap_b32 v129, v131\n"
      "v_mfma_f32_16x16x32_bf16 v[220:223], v[100:103], a[240:243], v[220:223]\n"
      "ds_read_b128 a[236:239], v251 offset:10240\n"
      "v_mov_b32 v138, s37\n"
      "v_mov_b32 v139, s37\n"
      "s_waitcnt lgkmcnt(14)\n"
      "v_mfma_f32_16x16x32_bf16 v[216:219], v[104:107], a[244:247], v[216:219]\n"
      "v_max_u32 v129, v129, v131\n"
      "v_cndmask_b32 v133, v132, v133, s[64:65]\n"
      "v_mov_b32 v131, v129\n"
      "v_mfma_f32_16x16x32_bf16 v[220:223], v[108:111], a[244:247], v[220:223]\n"
      "global_load_lds_dwordx4 v246, s[70:71] offset:2048\n"
      "ds_read_b128 a[240:243], v250 offset:12288\n"
      "v_permlane32_swap_b32 v129, v131\n"
      "s_waitcnt lgkmcnt(14)\n"
      "v_mfma_f32_16x16x32_bf16 v[216:219], v[112:115], a[248:251], v[216:219]\n"
      "s_waitcnt vmcnt(16)\n"
      "ds_read_b128 a[0:3], v248 offset:4096\n"
      "v_mfma_f32_16x16x32_bf16 v[220:223], v[116:119], a[248:251], v[220:223]\n"
      "ds_read_b128 a[244:247], v250 offset:14336\n"
      "ds_read_b128 a[4:7], v248 offset:6144\n"
      "v_max_u32 v129, v129, v131\n"
      "s_waitcnt lgkmcnt(15)\n"
      "v_mfma_f32_16x16x32_bf16 v[216:219], v[120:123], a[252:255], v[216:219]\n"
      "ds_read_b128 a[32:35], v249 offset:4096\n"
      "ds_read_b128 a[36:39], v249 offset:6144\n"
      "v_cndmask_b32 v136, v129, v130, s[64:65]\n"
      "v_mfma_f32_16x16x32_bf16 v[220:223], v[124:127], a[252:255], v[220:223]\n"
      "s_mov_b32 s64, 0x00ff0fff\n"
      "s_mov_b64 exec, s[64:65]\n"
      "global_store_dwordx4 v133, v[136:139], s[58:59] sc1\n"
      "s_mov_b64 exec, -1\n"
      "v_mfma_f32_16x16x32_bf16 v[192:195], v[0:3], a[192:195], 0\n"
      "global_load_lds_dwordx4 v247, s[70:71] offset:3072\n"
      "ds_read_b128 a[248:251], v251 offset:12288\n"
      "v_mfma_f32_16x16x32_bf16 v[196:199], v[4:7], a[192:195], 0\n"
      "ds_read_b128 a[252:255], v251 offset:14336\n"
      "s_waitcnt vmcnt(14)\n"
      "ds_read_b128 a[8:11], v248 offset:8192\n"
      "s_waitcnt lgkmcnt(15)\n"
      "v_mfma_f32_16x16x32_bf16 v[200:203], v[0:3], a[196:199], 0\n"
      "ds_read_b128 a[12:15], v248 offset:10240\n"
      "ds_read_b128 a[40:43], v249 offset:8192\n"
      "v_cvt_pk_bf16_f32 v224, v216, v217\n"
      "v_mfma_f32_16x16x32_bf16 v[204:207], v[4:7], a[196:199], 0\n"
      "s_xor_b32 s70, s79, 0\n"
      "s_add_u32 s70, s8, s70\n"
      "s_addc_u32 s71, s9, 0\n"
      "s_add_u32 m0, s41, 0\n"
      "v_mfma_f32_16x16x32_bf16 v[192:195], v[8:11], a[200:203], v[192:195]\n"
      "global_load_lds_dwordx4 v244, s[70:71] offset:0\n"
      "ds_read_b128 a[192:195], v250 offset:16384\n"
      "v_cvt_pk_bf16_f32 v225, v218, v219\n"
      "v_mfma_f32_16x16x32_bf16 v[196:199], v[12:15], a[200:203], v[196:199]\n"
      "ds_read_b128 a[196:199], v250 offset:18432\n"
      "ds_read_b128 a[44:47], v249 offset:10240\n"
      "v_cvt_pk_bf16_f32 v226, v220, v221\n"
      "v_mfma_f32_16x16x32_bf16 v[200:203], v[8:11], a[204:207], v[200:203]\n"
      "v_cvt_pk_bf16_f32 v227, v222, v223\n"
      "v_xor_b32 v224, s85, v224\n"
      "v_xor_b32 v225, s85, v225\n"
      "v_mfma_f32_16x16x32_bf16 v[204:207], v[12:15], a[204:207], v[204:207]\n"
      "ds_read_b128 a[200:203], v251 offset:16384\n"
      "v_xor_b32 v226, s85, v226\n"
      "v_xor_b32 v227, s85, v227\n"
      "v_mfma_f32_16x16x32_bf16 v[192:195], v[16:19], a[208:211], v[192:195]\n"
      "s_lshl2_add_u32 s54, s34, s35\n"
      "global_load_lds_dwordx4 v245, s[70:71] offset:1024\n"
      "s_lshr_b32 s56, s39, 1\n"
      "s_lshl1_add_u32 s56, s33, s56\n"
      "v_mfma_f32_16x16x32_bf16 v[196:199], v[20:23], a[208:211], v[196:199]\n"
      "s_lshl_b32 s56, s56, 15\n"
      "ds_read_b128 a[204:207], v251 offset:18432\n"
      "s_mul_i32 s55, s54, 266240\n"
      "s_add_u32 s55, s55, 4096\n"
      "v_mfma_f32_16x16x32_bf16 v[200:203], v[16:19], a[212:215], v[200:203]\n"
      "s_add_u32 s14, s18, s55\n"
      "s_addc_u32 s15, s19, 0\n"
      "s_add_u32 s10, s14, s56\n"
      "s_addc_u32 s11, s15, 0\n"
      "v_mfma_f32_16x16x32_bf16 v[204:207], v[20:23], a[212:215], v[204:207]\n"
      "s_xor_b32 s56, s56, 0x8000\n"
      "ds_read_b128 a[208:211], v250 offset:20480\n"
      "s_add_u32 s14, s14, s56\n"
      "s_addc_u32 s15, s15, 0\n"
      "s_waitcnt lgkmcnt(15)\n"
      "v_mfma_f32_16x16x32_bf16 v[192:195], v[24:27], a[216:219], v[192:195]\n"
      "s_and_b32 s55, s39, 1\n"
      "global_load_lds_dwordx4 v246, s[70:71] offset:2048\n"
      "s_lshl_b32 s56, s55, 14\n"
      "v_add_u32 v240, s56, v242\n"
      "v_mfma_f32_16x16x32_bf16 v[196:199], v[28:31], a[216:219], v[196:199]\n"
      "ds_read_b128 a[212:215], v250 offset:22528\n"
      "v_xor_b32 v241, 0x4000, v240\n"
      "s_add_u32 s72, s10, 0x2000\n"
      "s_addc_u32 s73, s11, 0\n"
      "v_mfma_f32_16x16x32_bf16 v[200:203], v[24:27], a[220:223], v[200:203]\n"
      "s_add_u32 s74, s14, 0x2000\n"
      "s_addc_u32 s75, s15, 0\n"
      "s_lshl2_add_u32 s55, s54, s33\n"
      "s_lshl_b32 s55, s55, 12\n"
      "v_mfma_f32_16x16x32_bf16 v[204:207], v[28:31], a[220:223], v[204:207]\n"
      "s_add_u32 s55, s55, 1048576\n"
      "ds_read_b128 a[216:219], v251 offset:20480\n"
      "v_and_b32 v253, 7, v243\n"
      "v_lshl_add_u32 v253, v253, 9, s55\n"
      "v_mfma_f32_16x16x32_bf16 v[192:195], v[32:35], a[224:227], v[192:195]\n"
      "global_load_lds_dwordx4 v247, s[70:71] offset:3072\n"
      "v_mfma_f32_16x16x32_bf16 v[196:199], v[36:39], a[224:227], v[196:199]\n"
      "ds_read_b128 a[220:223], v251 offset:22528\n"
      "s_waitcnt vmcnt(10)\n"
      "s_barrier\n"
      "v_mfma_f32_16x16x32_bf16 v[200:203], v[32:35], a[228:231], v[200:203]\n"
      "v_mfma_f32_16x16x32_bf16 v[204:207], v[36:39], a[228:231], v[204:207]\n"
      "s_xor_b32 s70, s76, 128\n"
      "s_add_u32 s70, s8, s70\n"
      "s_addc_u32 s71, s9, 0\n"
      "s_add_u32 m0, s82, 98304\n"
      "v_mfma_f32_16x16x32_bf16 v[192:195], v[40:43], a[232:235], v[192:195]\n"
      "global_load_lds_dwordx4 v244, s[70:71] offset:0\n"
      "ds_read_b128 a[224:227], v250 offset:24576\n"
      "v_mfma_f32_16x16x32_bf16 v[196:199], v[44:47], a[232:235], v[196:199]\n"
      "ds_read_b128 a[228:231], v250 offset:26624\n"
      "v_mfma_f32_16x16x32_bf16 v[200:203], v[40:43], a[236:239], v[200:203]\n"
      "v_mfma_f32_16x16x32_bf16 v[204:207], v[44:47], a[236:239], v[204:207]\n"
      "ds_read_b128 a[232:235], v251 offset:24576\n"
      "v_mfma_f32_16x16x32_bf16 v[192:195], v[48:51], a[240:243], v[192:195]\n"
      "global_load_lds_dwordx4 v245, s[70:71] offset:1024\n"
      "v_mfma_f32_16x16x32_bf16 v[196:199], v[52:55], a[240:243], v[196:199]\n"
      "ds_read_b128 a[236:239], v251 offset:26624\n"
      "s_waitcnt lgkmcnt(15)\n"
      "v_mfma_f32_16x16x32_bf16 v[200:203], v[48:51], a[244:247], v[200:203]\n"
      "v_mfma_f32_16x16x32_bf16 v[204:207], v[52:55], a[244:247], v[204:207]\n"
      "ds_read_b128 a[240:243], v250 offset:28672\n"
      "v_mfma_f32_16x16x32_bf16 v[192:195], v[56:59], a[248:251], v[192:195]\n"
      "global_load_lds_dwordx4 v246, s[70:71] offset:2048\n"
      "v_mfma_f32_16x16x32_bf16 v[196:199], v[60:63], a[248:251], v[196:199]\n"
      "ds_read_b128 a[244:247], v250 offset:30720\n"
      "v_mfma_f32_16x16x32_bf16 v[200:203], v[56:59], a[252:255], v[200:203]\n"
      "v_mfma_f32_16x16x32_bf16 v[204:207], v[60:63], a[252:255], v[204:207]\n"
      "ds_read_b128 a[248:251], v251 offset:28672\n"
      "s_waitcnt lgkmcnt(15)\n"
      "v_mfma_f32_16x16x32_bf16 v[192:195], v[64:67], a[192:195], v[192:195]\n"
      "global_load_lds_dwordx4 v247, s[70:71] offset:3072\n"
      "v_mfma_f32_16x16x32_bf16 v[196:199], v[68:71], a[192:195], v[196:199]\n"
      "ds_read_b128 a[252:255], v251 offset:30720\n"
      "v_xor_b32 v250, 196608, v250\n"
      "v_xor_b32 v251, 196608, v251\n"
      "s_waitcnt lgkmcnt(15)\n"
      "v_mfma_f32_16x16x32_bf16 v[200:203], v[64:67], a[196:199], v[200:203]\n"
      "v_mfma_f32_16x16x32_bf16 v[204:207], v[68:71], a[196:199], v[204:207]\n"
      "s_xor_b32 s70, s77, 128\n"
      "s_add_u32 s70, s8, s70\n"
      "s_addc_u32 s71, s9, 0\n"
      "s_add_u32 m0, s82, 114688\n"
      "s_waitcnt lgkmcnt(13)\n"
      "v_mfma_f32_16x16x32_bf16 v[192:195], v[72:75], a[200:203], v[192:195]\n"
      "global_load_lds_dwordx4 v244, s[70:71] offset:0\n"
      "ds_read_b128 a[192:195], v250 offset:0\n"
      "v_mfma_f32_16x16x32_bf16 v[196:199], v[76:79], a[200:203], v[196:199]\n"
      "ds_read_b128 a[196:199], v250 offset:2048\n"
      "s_waitcnt lgkmcnt(14)\n"
      "v_mfma_f32_16x16x32_bf16 v[200:203], v[72:75], a[204:207], v[200:203]\n"
      "v_mfma_f32_16x16x32_bf16 v[204:207], v[76:79], a[204:207], v[204:207]\n"
      "ds_read_b128 a[200:203], v251 offset:0\n"
      "s_waitcnt lgkmcnt(14)\n"
      "v_mfma_f32_16x16x32_bf16 v[192:195], v[80:83], a[208:211], v[192:195]\n"
      "global_load_lds_dwordx4 v245, s[70:71] offset:1024\n"
      "v_mfma_f32_16x16x32_bf16 v[196:199], v[84:87], a[208:211], v[196:199]\n"
      "ds_read_b128 a[204:207], v251 offset:2048\n"
      "s_waitcnt lgkmcnt(14)\n"
      "v_mfma_f32_16x16x32_bf16 v[200:203], v[80:83], a[212:215], v[200:203]\n"
      "v_mfma_f32_16x16x32_bf16 v[204:207], v[84:87], a[212:215], v[204:207]\n"
      "ds_read_b128 a[208:211], v250 offset:4096\n"
      "s_waitcnt lgkmcnt(14)\n"
      "v_mfma_f32_16x16x32_bf16 v[192:195], v[88:91], a[216:219], v[192:195]\n"
      "global_load_lds_dwordx4 v246, s[70:71] offset:2048\n"
      "v_mfma_f32_16x16x32_bf16 v[196:199], v[92:95], a[216:219], v[196:199]\n"
      "ds_read_b128 a[212:215], v250 offset:6144\n"
      "s_waitcnt vmcnt(11)\n"
      "ds_read_b128 a[16:19], v248 offset:12288\n"
      "s_waitcnt lgkmcnt(15)\n"
      "v_mfma_f32_16x16x32_bf16 v[200:203], v[88:91], a[220:223], v[200:203]\n"
      "ds_read_b128 a[20:23], v248 offset:14336\n"
      "ds_read_b128 a[48:51], v249 offset:12288\n"
      "v_mfma_f32_16x16x32_bf16 v[204:207], v[92:95], a[220:223], v[204:207]\n"
      "ds_read_b128 a[216:219], v251 offset:4096\n"
      "ds_read_b128 a[52:55], v249 offset:14336\n"
      "s_waitcnt lgkmcnt(15)\n"
      "v_mfma_f32_16x16x32_bf16 v[192:195], v[96:99], a[224:227], v[192:195]\n"
      "global_load_lds_dwordx4 v247, s[70:71] offset:3072\n"
      "v_mfma_f32_16x16x32_bf16 v[196:199], v[100:103], a[224:227], v[196:199]\n"
      "ds_read_b128 a[220:223], v251 offset:6144\n"
      "v_mfma_f32_16x16x32_bf16 v[200:203], v[96:99], a[228:231], v[200:203]\n"
      "v_mfma_f32_16x16x32_bf16 v[204:207], v[100:103], a[228:231], v[204:207]\n"
      "s_xor_b32 s70, s78, 128\n"
      "s_add_u32 s70, s8, s70\n"
      "s_addc_u32 s71, s9, 0\n"
      "s_add_u32 m0, s41, 4096\n"
      "v_mfma_f32_16x16x32_bf16 v[192:195], v[104:107], a[232:235], v[192:195]\n"
      "global_load_lds_dwordx4 v244, s[70:71] offset:0\n"
      "ds_read_b128 a[224:227], v250 offset:8192\n"
      "v_mfma_f32_16x16x32_bf16 v[196:199], v[108:111], a[232:235], v[196:199]\n"
      "ds_read_b128 a[228:231], v250 offset:10240\n"
      "v_mfma_f32_16x16x32_bf16 v[200:203], v[104:107], a[236:239], v[200:203]\n"
      "v_mfma_f32_16x16x32_bf16 v[204:207], v[108:111], a[236:239], v[204:207]\n"
      "ds_read_b128 a[232:235], v251 offset:8192\n"
      "s_waitcnt lgkmcnt(15)\n"
      "v_mfma_f32_16x16x32_bf16 v[192:195], v[112:115], a[240:243], v[192:195]\n"
      "global_load_lds_dwordx4 v245, s[70:71] offset:1024\n"
      "v_mfma_f32_16x16x32_bf16 v[196:199], v[116:119], a[240:243], v[196:199]\n"
      "ds_read_b128 a[236:239], v251 offset:10240\n"
      "s_barrier\n"
      "v_mfma_f32_16x16x32_bf16 v[200:203], v[112:115], a[244:247], v[200:203]\n"
      "v_mfma_f32_16x16x32_bf16 v[204:207], v[116:119], a[244:247], v[204:207]\n"
      "ds_read_b128 a[240:243], v250 offset:12288\n"
      "v_mfma_f32_16x16x32_bf16 v[192:195], v[120:123], a[248:251], v[192:195]\n"
      "global_load_lds_dwordx4 v246, s[70:71] offset:2048\n"
      "v_mfma_f32_16x16x32_bf16 v[196:199], v[124:127], a[248:251], v[196:199]\n"
      "ds_read_b128 a[244:247], v250 offset:14336\n"
      "s_waitcnt vmcnt(11)\n"
      "ds_read_b128 a[24:27], v248 offset:0\n"
      "v_mfma_f32_16x16x32_bf16 v[200:203], v[120:123], a[252:255], v[200:203]\n"
      "ds_read_b128 a[28:31], v248 offset:2048\n"
      "ds_read_b128 a[56:59], v249 offset:0\n"
      "v_mfma_f32_16x16x32_bf16 v[204:207], v[124:127], a[252:255], v[204:207]\n"
      "ds_read_b128 a[248:251], v251 offset:12288\n"
      "ds_read_b128 a[60:63], v249 offset:2048\n"
      "s_waitcnt lgkmcnt(15)\n"
      "v_mfma_f32_16x16x32_bf16 v[208:211], v[0:3], a[192:195], 0\n"
      "global_load_lds_dwordx4 v247, s[70:71] offset:3072\n"
      "v_mfma_f32_16x16x32_bf16 v[212:215], v[4:7], a[192:195], 0\n"
      "ds_read_b128 a[252:255], v251 offset:14336\n"
      "v_mfma_f32_16x16x32_bf16 v[216:219], v[0:3], a[196:199], 0\n"
      "v_cvt_pk_bf16_f32 v228, v192, v193\n"
      "v_cvt_pk_bf16_f32 v229, v194, v195\n"
      "v_cvt_pk_bf16_f32 v230, v196, v197\n"
      "v_mfma_f32_16x16x32_bf16 v[220:223], v[4:7], a[196:199], 0\n"
      "global_load_dword v252, v253, s[58:59] offset:0 sc1\n"
      "s_xor_b32 s70, s79, 128\n"
      "s_add_u32 s70, s8, s70\n"
      "s_addc_u32 s71, s9, 0\n"
      "s_add_u32 m0, s82, 131072\n"
      "v_mfma_f32_16x16x32_bf16 v[208:211], v[8:11], a[200:203], v[208:211]\n"
      "global_load_lds_dwordx4 v244, s[70:71] offset:0\n"
      "ds_read_b128 a[192:195], v250 offset:16384\n"
      "v_cvt_pk_bf16_f32 v231, v198, v199\n"
      "v_mfma_f32_16x16x32_bf16 v[212:215], v[12:15], a[200:203], v[212:215]\n"
      "ds_read_b128 a[196:199], v250 offset:18432\n"
      "v_xor_b32 v228, s85, v228\n"
      "v_xor_b32 v229, s85, v229\n"
      "v_mfma_f32_16x16x32_bf16 v[216:219], v[8:11], a[204:207], v[216:219]\n"
      "v_xor_b32 v230, s85, v230\n"
      "v_xor_b32 v231, s85, v231\n"
      "v_cvt_pk_bf16_f32 v232, v200, v201\n"
      "v_mfma_f32_16x16x32_bf16 v[220:223], v[12:15], a[204:207], v[220:223]\n"
      "ds_read_b128 a[200:203], v251 offset:16384\n"
      "v_cvt_pk_bf16_f32 v233, v202, v203\n"
      "v_cvt_pk_bf16_f32 v234, v204, v205\n"
      "v_mfma_f32_16x16x32_bf16 v[208:211], v[16:19], a[208:211], v[208:211]\n"
      "global_load_lds_dwordx4 v245, s[70:71] offset:1024\n"
      "v_cvt_pk_bf16_f32 v235, v206, v207\n"
      "v_xor_b32 v232, s85, v232\n"
      "v_mfma_f32_16x16x32_bf16 v[212:215], v[20:23], a[208:211], v[212:215]\n"
      "ds_read_b128 a[204:207], v251 offset:18432\n"
      "v_xor_b32 v233, s85, v233\n"
      "v_xor_b32 v234, s85, v234\n"
      "v_mfma_f32_16x16x32_bf16 v[216:219], v[16:19], a[212:215], v[216:219]\n"
      "v_xor_b32 v235, s85, v235\n"
      "v_mfma_f32_16x16x32_bf16 v[220:223], v[20:23], a[212:215], v[220:223]\n"
      "ds_read_b128 a[208:211], v250 offset:20480\n"
      "s_waitcnt lgkmcnt(15)\n"
      "v_mfma_f32_16x16x32_bf16 v[208:211], v[24:27], a[216:219], v[208:211]\n"
      "global_load_lds_dwordx4 v246, s[70:71] offset:2048\n"
      "v_mfma_f32_16x16x32_bf16 v[212:215], v[28:31], a[216:219], v[212:215]\n"
      "ds_read_b128 a[212:215], v250 offset:22528\n"
      "s_waitcnt vmcnt(12)\n"
      "ds_read_b128 a[64:67], v236 offset:0\n"
      "v_mfma_f32_16x16x32_bf16 v[216:219], v[24:27], a[220:223], v[216:219]\n"
      "ds_read_b128 a[68:71], v236 offset:2048\n"
      "ds_read_b128 a[96:99], v237 offset:0\n"
      "v_mfma_f32_16x16x32_bf16 v[220:223], v[28:31], a[220:223], v[220:223]\n"
      "ds_read_b128 a[216:219], v251 offset:20480\n"
      "ds_read_b128 a[100:103], v237 offset:2048\n"
      "v_mfma_f32_16x16x32_bf16 v[208:211], v[32:35], a[224:227], v[208:211]\n"
      "global_load_lds_dwordx4 v247, s[70:71] offset:3072\n"
      "v_mfma_f32_16x16x32_bf16 v[212:215], v[36:39], a[224:227], v[212:215]\n"
      "ds_read_b128 a[220:223], v251 offset:22528\n"
      "v_mfma_f32_16x16x32_bf16 v[216:219], v[32:35], a[228:231], v[216:219]\n"
      "v_mfma_f32_16x16x32_bf16 v[220:223], v[36:39], a[228:231], v[220:223]\n"
      "s_xor_b32 s70, s76, 256\n"
      "s_add_u32 s70, s8, s70\n"
      "s_addc_u32 s71, s9, 0\n"
      "s_add_u32 m0, s82, 147456\n"
      "s_waitcnt lgkmcnt(15)\n"
      "v_mfma_f32_16x16x32_bf16 v[208:211], v[40:43], a[232:235], v[208:211]\n"
      "global_load_lds_dwordx4 v244, s[70:71] offset:0\n"
      "ds_read_b128 a[224:227], v250 offset:24576\n"
      "v_mfma_f32_16x16x32_bf16 v[212:215], v[44:47], a[232:235], v[212:215]\n"
      "ds_read_b128 a[228:231], v250 offset:26624\n"
      "v_mfma_f32_16x16x32_bf16 v[216:219], v[40:43], a[236:239], v[216:219]\n"
      "v_mfma_f32_16x16x32_bf16 v[220:223], v[44:47], a[236:239], v[220:223]\n"
      "ds_read_b128 a[232:235], v251 offset:24576\n"
      "v_mfma_f32_16x16x32_bf16 v[208:211], v[48:51], a[240:243], v[208:211]\n"
      "global_load_lds_dwordx4 v245, s[70:71] offset:1024\n"
      "v_mfma_f32_16x16x32_bf16 v[212:215], v[52:55], a[240:243], v[212:215]\n"
      "ds_read_b128 a[236:239], v251 offset:26624\n"
      "v_mfma_f32_16x16x32_bf16 v[216:219], v[48:51], a[244:247], v[216:219]\n"
      "v_mfma_f32_16x16x32_bf16 v[220:223], v[52:55], a[244:247], v[220:223]\n"
      "ds_read_b128 a[240:243], v250 offset:28672\n"
      "s_waitcnt lgkmcnt(15)\n"
      "v_mfma_f32_16x16x32_bf16 v[208:211], v[56:59], a[248:251], v[208:211]\n"
      "global_load_lds_dwordx4 v246, s[70:71] offset:2048\n"
      "v_mfma_f32_16x16x32_bf16 v[212:215], v[60:63], a[248:251], v[212:215]\n"
      "ds_read_b128 a[244:247], v250 offset:30720\n"
      "s_waitcnt vmcnt(12)\n"
      "ds_read_b128 a[72:75], v236 offset:16384\n"
      "v_mfma_f32_16x16x32_bf16 v[216:219], v[56:59], a[252:255], v[216:219]\n"
      "ds_read_b128 a[76:79], v236 offset:18432\n"
      "ds_read_b128 a[104:107], v237 offset:16384\n"
      "v_mfma_f32_16x16x32_bf16 v[220:223], v[60:63], a[252:255], v[220:223]\n"
      "ds_read_b128 a[248:251], v251 offset:28672\n"
      "ds_read_b128 a[108:111], v237 offset:18432\n"
      "v_mfma_f32_16x16x32_bf16 v[208:211], v[64:67], a[192:195], v[208:211]\n"
      "global_load_lds_dwordx4 v247, s[70:71] offset:3072\n"
      "v_mfma_f32_16x16x32_bf16 v[212:215], v[68:71], a[192:195], v[212:215]\n"
      "ds_read_b128 a[252:255], v251 offset:30720\n"
      "v_mfma_f32_16x16x32_bf16 v[216:219], v[64:67], a[196:199], v[216:219]\n"
      "v_mfma_f32_16x16x32_bf16 v[220:223], v[68:71], a[196:199], v[220:223]\n"
      "s_waitcnt vmcnt(8)\n"
      "v_cmp_ne_u32 vcc, s37, v252\n"
      "s_nop 1\n"
      "s_cbranch_vccnz .Lp_s2_%=\n"
      ".Lp_o3_%=:\n"
      "s_waitcnt lgkmcnt(15)\n"
      "v_mfma_f32_16x16x32_bf16 v[208:211], v[72:75], a[200:203], v[208:211]\n"
      "global_load_dwordx4 v[128:131], v240, s[10:11] offset:-4096 sc1\n"
      "v_mfma_f32_16x16x32_bf16 v[212:215], v[76:79], a[200:203], v[212:215]\n"
      "v_mfma_f32_16x16x32_bf16 v[216:219], v[72:75], a[204:207], v[216:219]\n"
      "v_mfma_f32_16x16x32_bf16 v[220:223], v[76:79], a[204:207], v[220:223]\n"
      "v_mfma_f32_16x16x32_bf16 v[208:211], v[80:83], a[208:211], v[208:211]\n"
      "global_load_dwordx4 v[132:135], v240, s[10:11] offset:-3072 sc1\n"
      "v_mfma_f32_16x16x32_bf16 v[212:215], v[84:87], a[208:211], v[212:215]\n"
      "v_mfma_f32_16x16x32_bf16 v[216:219], v[80:83], a[212:215], v[216:219]\n"
      "v_mfma_f32_16x16x32_bf16 v[220:223], v[84:87], a[212:215], v[220:223]\n"
      "s_waitcnt lgkmcnt(14)\n"
      "v_mfma_f32_16x16x32_bf16 v[208:211], v[88:91], a[216:219], v[208:211]\n"
      "global_load_dwordx4 v[136:139], v240, s[10:11] offset:-2048 sc1\n"
      "v_mfma_f32_16x16x32_bf16 v[212:215], v[92:95], a[216:219], v[212:215]\n"
      "ds_read_b128 a[80:83], v248 offset:4096\n"
      "ds_read_b128 a[84:87], v248 offset:6144\n"
      "s_waitcnt lgkmcnt(14)\n"
      "v_mfma_f32_16x16x32_bf16 v[216:219], v[88:91], a[220:223], v[216:219]\n"
      "ds_read_b128 a[112:115], v249 offset:4096\n"
      "ds_read_b128 a[116:119], v249 offset:6144\n"
      "v_mfma_f32_16x16x32_bf16 v[220:223], v[92:95], a[220:223], v[220:223]\n"
      "s_waitcnt lgkmcnt(15)\n"
      "v_mfma_f32_16x16x32_bf16 v[208:211], v[96:99], a[224:227], v[208:211]\n"
      "global_load_dwordx4 v[140:143], v240, s[72:73] offset:-4096 sc1\n"
      "v_mfma_f32_16x16x32_bf16 v[212:215], v[100:103], a[224:227], v[212:215]\n"
      "s_waitcnt lgkmcnt(14)\n"
      "v_mfma_f32_16x16x32_bf16 v[216:219], v[96:99], a[228:231], v[216:219]\n"
      "v_mfma_f32_16x16x32_bf16 v[220:223], v[100:103], a[228:231], v[220:223]\n"
      "s_waitcnt lgkmcnt(13)\n"
      "v_mfma_f32_16x16x32_bf16 v[208:211], v[104:107], a[232:235], v[208:211]\n"
      "global_load_dwordx4 v[144:147], v240, s[72:73] offset:-3072 sc1\n"
      "v_mfma_f32_16x16x32_bf16 v[212:215], v[108:111], a[232:235], v[212:215]\n"
      "s_waitcnt lgkmcnt(12)\n"
      "v_mfma_f32_16x16x32_bf16 v[216:219], v[104:107], a[236:239], v[216:219]\n"
      "v_mfma_f32_16x16x32_bf16 v[220:223], v[108:111], a[236:239], v[220:223]\n"
      "s_waitcnt lgkmcnt(11)\n"
      "v_mfma_f32_16x16x32_bf16 v[208:211], v[112:115], a[240:243], v[208:211]\n"
      "global_store_dwordx4 v242, v[224:227], s[62:63] offset:-1024 sc1\n"
      "global_store_dwordx4 v242, v[228:231], s[62:63] offset:0 sc1\n"
      "v_mfma_f32_16x16x32_bf16 v[212:215], v[116:119], a[240:243], v[212:215]\n"
      "global_store_dwordx4 v242, v[232:235], s[62:63] offset:1024 sc1\n"
      "global_load_dwordx4 v[148:151], v240, s[72:73] offset:-2048 sc1\n"
      "s_waitcnt lgkmcnt(10)\n"
      "v_mfma_f32_16x16x32_bf16 v[216:219], v[112:115], a[244:247], v[216:219]\n"
      "v_mfma_f32_16x16x32_bf16 v[220:223], v[116:119], a[244:247], v[220:223]\n"
      "s_waitcnt lgkmcnt(6)\n"
      "v_mfma_f32_16x16x32_bf16 v[208:211], v[120:123], a[248:251], v[208:211]\n"
      "v_mfma_f32_16x16x32_bf16 v[212:215], v[124:127], a[248:251], v[212:215]\n"
      "s_waitcnt vmcnt(13)\n"
      "ds_read_b128 a[88:91], v236 offset:32768\n"
      "s_waitcnt lgkmcnt(5)\n"
      "v_mfma_f32_16x16x32_bf16 v[216:219], v[120:123], a[252:255], v[216:219]\n"
      "ds_read_b128 a[92:95], v236 offset:34816\n"
      "ds_read_b128 a[120:123], v237 offset:32768\n"
      "v_mfma_f32_16x16x32_bf16 v[220:223], v[124:127], a[252:255], v[220:223]\n"
      "ds_read_b128 a[124:127], v237 offset:34816\n"
      "s_waitcnt vmcnt(8)\n"
      "v_mfma_f32_16x16x32_bf16 v[0:3], v[128:131], a[0:3], 0\n"
      "v_mfma_f32_16x16x32_bf16 v[4:7], v[128:131], a[4:7], 0\n"
      "v_mfma_f32_16x16x32_bf16 v[8:11], v[128:131], a[8:11], 0\n"
      "v_cvt_pk_bf16_f32 v192, v208, v209\n"
      "v_cvt_pk_bf16_f32 v193, v210, v211\n"
      "v_mfma_f32_16x16x32_bf16 v[12:15], v[128:131], a[12:15], 0\n"
      "v_cvt_pk_bf16_f32 v194, v212, v213\n"
      "v_cvt_pk_bf16_f32 v195, v214, v215\n"
      "v_mfma_f32_16x16x32_bf16 v[16:19], v[128:131], a[16:19], 0\n"
      "v_xor_b32 v192, s85, v192\n"
      "v_xor_b32 v193, s85, v193\n"
      "v_mfma_f32_16x16x32_bf16 v[20:23], v[128:131], a[20:23], 0\n"
      "v_xor_b32 v194, s85, v194\n"
      "v_xor_b32 v195, s85, v195\n"
      "v_mfma_f32_16x16x32_bf16 v[24:27], v[128:131], a[24:27], 0\n"
      "v_cvt_pk_bf16_f32 v196, v216, v217\n"
      "v_cvt_pk_bf16_f32 v197, v218, v219\n"
      "v_mfma_f32_16x16x32_bf16 v[28:31], v[128:131], a[28:31], 0\n"
      "v_cvt_pk_bf16_f32 v198, v220, v221\n"
      "v_cvt_pk_bf16_f32 v199, v222, v223\n"
      "s_waitcnt vmcnt(7)\n"
      "v_mfma_f32_16x16x32_bf16 v[32:35], v[132:135], a[0:3], 0\n"
      "v_xor_b32 v196, s85, v196\n"
      "v_xor_b32 v197, s85, v197\n"
      "v_mfma_f32_16x16x32_bf16 v[36:39], v[132:135], a[4:7], 0\n"
      "v_xor_b32 v198, s85, v198\n"
      "v_xor_b32 v199, s85, v199\n"
      "v_mfma_f32_16x16x32_bf16 v[40:43], v[132:135], a[8:11], 0\n"
      "global_store_dwordx4 v242, v[192:195], s[62:63] offset:2048 sc1\n"
      "global_store_dwordx4 v242, v[196:199], s[62:63] offset:3072 sc1\n"
      "v_mfma_f32_16x16x32_bf16 v[44:47], v[132:135], a[12:15], 0\n"
      "v_mfma_f32_16x16x32_bf16 v[48:51], v[132:135], a[16:19], 0\n"
      "v_mfma_f32_16x16x32_bf16 v[52:55], v[132:135], a[20:23], 0\n"
      "s_xor_b32 s70, s77, 256\n"
      "s_add_u32 s70, s8, s70\n"
      "s_addc_u32 s71, s9, 0\n"
      "s_add_u32 m0, s41, 8192\n"
      "v_mfma_f32_16x16x32_bf16 v[56:59], v[132:135], a[24:27], 0\n"
      "global_load_lds_dwordx4 v244, s[70:71] offset:0\n"
      "ds_read_b128 a[128:131], v236 offset:49152\n"
      "v_mfma_f32_16x16x32_bf16 v[60:63], v[132:135], a[28:31], 0\n"
      "s_waitcnt vmcnt(8)\n"
      "ds_read_b128 a[132:135], v236 offset:51200\n"
      "ds_read_b128 a[160:163], v237 offset:49152\n"
      "v_mfma_f32_16x16x32_bf16 v[64:67], v[136:139], a[0:3], 0\n"
      "ds_read_b128 a[164:167], v237 offset:51200\n"
      "v_mfma_f32_16x16x32_bf16 v[68:71], v[136:139], a[4:7], 0\n"
      "global_load_lds_dwordx4 v245, s[70:71] offset:1024\n"
      "v_mfma_f32_16x16x32_bf16 v[72:75], v[136:139], a[8:11], 0\n"
      "v_mfma_f32_16x16x32_bf16 v[76:79], v[136:139], a[12:15], 0\n"
      "v_mfma_f32_16x16x32_bf16 v[80:83], v[136:139], a[16:19], 0\n"
      "global_load_lds_dwordx4 v246, s[70:71] offset:2048\n"
      "v_mfma_f32_16x16x32_bf16 v[84:87], v[136:139], a[20:23], 0\n"
      "v_mfma_f32_16x16x32_bf16 v[88:91], v[136:139], a[24:27], 0\n"
      "v_mfma_f32_16x16x32_bf16 v[92:95], v[136:139], a[28:31], 0\n"
      "global_load_lds_dwordx4 v247, s[70:71] offset:3072\n"
      "v_mfma_f32_16x16x32_bf16 v[0:3], v[140:143], a[32:35], v[0:3]\n"
      "v_mfma_f32_16x16x32_bf16 v[4:7], v[140:143], a[36:39], v[4:7]\n"
      "global_load_dwordx4 v[152:155], v241, s[10:11] offset:-4096 sc1\n"
      "v_mfma_f32_16x16x32_bf16 v[8:11], v[140:143], a[40:43], v[8:11]\n"
      "v_add_u32 v238, s42, v242\n"
      "v_mov_b32 v200, s37\n"
      "v_mov_b32 v201, s37\n"
      "v_mov_b32 v202, s37\n"
      "v_mov_b32 v203, s37\n"
      "s_waitcnt vmcnt(5)\n"
      "s_mov_b64 exec, 0xff\n"
      "global_store_dwordx4 v238, v[200:203], s[58:59] offset:128 sc1\n"
      "s_mov_b64 exec, -1\n"
      "v_mfma_f32_16x16x32_bf16 v[12:15], v[140:143], a[44:47], v[12:15]\n"
      "s_lshl2_add_u32 s48, s36, s35\n"
      "s_mul_i32 s48, s48, 147712\n"
      "s_mul_i32 s49, s33, 3072\n"
      "v_mfma_f32_16x16x32_bf16 v[16:19], v[140:143], a[48:51], v[16:19]\n"
      "s_add_u32 s48, s48, s49\n"
      "global_load_dwordx4 v[156:159], v241, s[10:11] offset:-3072 sc1\n"
      "s_add_u32 s48, s48, 6291456\n"
      "v_mfma_f32_16x16x32_bf16 v[20:23], v[140:143], a[52:55], v[20:23]\n"
      "s_add_u32 s60, s18, s48\n"
      "s_addc_u32 s61, s19, 0\n"
      "s_mul_i32 s49, s34, 36864\n"
      "v_mfma_f32_16x16x32_bf16 v[24:27], v[140:143], a[56:59], v[24:27]\n"
      "s_add_u32 s62, s60, s49\n"
      "s_addc_u32 s63, s61, 0\n"
      "v_mfma_f32_16x16x32_bf16 v[28:31], v[140:143], a[60:63], v[28:31]\n"
      "global_load_dwordx4 v[160:163], v241, s[10:11] offset:-2048 sc1\n"
      "v_mfma_f32_16x16x32_bf16 v[32:35], v[144:147], a[32:35], v[32:35]\n"
      "v_mfma_f32_16x16x32_bf16 v[36:39], v[144:147], a[36:39], v[36:39]\n"
      "v_mfma_f32_16x16x32_bf16 v[40:43], v[144:147], a[40:43], v[40:43]\n"
      "s_xor_b32 s70, s78, 256\n"
      "s_add_u32 s70, s8, s70\n"
      "s_addc_u32 s71, s9, 0\n"
      "s_add_u32 m0, s41, 12288\n"
      "v_mfma_f32_16x16x32_bf16 v[44:47], v[144:147], a[44:47], v[44:47]\n"
      "global_load_lds_dwordx4 v244, s[70:71] offset:0\n"
      "v_mfma_f32_16x16x32_bf16 v[48:51], v[144:147], a[48:51], v[48:51]\n"
      "v_mfma_f32_16x16x32_bf16 v[52:55], v[144:147], a[52:55], v[52:55]\n"
      "v_mfma_f32_16x16x32_bf16 v[56:59], v[144:147], a[56:59], v[56:59]\n"
      "global_load_lds_dwordx4 v245, s[70:71] offset:1024\n"
      "v_mfma_f32_16x16x32_bf16 v[60:63], v[144:147], a[60:63], v[60:63]\n"
      "v_mfma_f32_16x16x32_bf16 v[64:67], v[148:151], a[32:35], v[64:67]\n"
      "v_mfma_f32_16x16x32_bf16 v[68:71], v[148:151], a[36:39], v[68:71]\n"
      "global_load_lds_dwordx4 v246, s[70:71] offset:2048\n"
      "v_mfma_f32_16x16x32_bf16 v[72:75], v[148:151], a[40:43], v[72:75]\n"
      "v_mfma_f32_16x16x32_bf16 v[76:79], v[148:151], a[44:47], v[76:79]\n"
      "v_mfma_f32_16x16x32_bf16 v[80:83], v[148:151], a[48:51], v[80:83]\n"
      "global_load_lds_dwordx4 v247, s[70:71] offset:3072\n"
      "v_mfma_f32_16x16x32_bf16 v[84:87], v[148:151], a[52:55], v[84:87]\n"
      "v_mfma_f32_16x16x32_bf16 v[88:91], v[148:151], a[56:59], v[88:91]\n"
      "s_xor_b32 s70, s79, 256\n"
      "s_add_u32 s70, s8, s70\n"
      "s_addc_u32 s71, s9, 0\n"
      "s_add_u32 m0, s41, 0\n"
      "v_mfma_f32_16x16x32_bf16 v[92:95], v[148:151], a[60:63], v[92:95]\n"
      "s_waitcnt vmcnt(5)\n"
      "global_load_lds_dwordx4 v244, s[70:71] offset:0\n"
      "v_mfma_f32_16x16x32_bf16 v[0:3], v[152:155], a[64:67], v[0:3]\n"
      "v_mfma_f32_16x16x32_bf16 v[4:7], v[152:155], a[68:71], v[4:7]\n"
      "v_mfma_f32_16x16x32_bf16 v[8:11], v[152:155], a[72:75], v[8:11]\n"
      "global_load_lds_dwordx4 v245, s[70:71] offset:1024\n"
      "v_mfma_f32_16x16x32_bf16 v[12:15], v[152:155], a[76:79], v[12:15]\n"
      "s_waitcnt lgkmcnt(11)\n"
      "v_mfma_f32_16x16x32_bf16 v[16:19], v[152:155], a[80:83], v[16:19]\n"
      "s_waitcnt lgkmcnt(10)\n"
      "v_mfma_f32_16x16x32_bf16 v[20:23], v[152:155], a[84:87], v[20:23]\n"
      "global_load_lds_dwordx4 v246, s[70:71] offset:2048\n"
      "s_waitcnt lgkmcnt(7)\n"
      "v_mfma_f32_16x16x32_bf16 v[24:27], v[152:155], a[88:91], v[24:27]\n"
      "s_waitcnt lgkmcnt(6)\n"
      "v_mfma_f32_16x16x32_bf16 v[28:31], v[152:155], a[92:95], v[28:31]\n"
      "v_mfma_f32_16x16x32_bf16 v[32:35], v[156:159], a[64:67], v[32:35]\n"
      "global_load_lds_dwordx4 v247, s[70:71] offset:3072\n"
      "v_mfma_f32_16x16x32_bf16 v[36:39], v[156:159], a[68:71], v[36:39]\n"
      "v_mfma_f32_16x16x32_bf16 v[40:43], v[156:159], a[72:75], v[40:43]\n"
      "global_load_dwordx4 v[164:167], v241, s[72:73] offset:-4096 sc1\n"
      "v_mfma_f32_16x16x32_bf16 v[44:47], v[156:159], a[76:79], v[44:47]\n"
      "v_mfma_f32_16x16x32_bf16 v[48:51], v[156:159], a[80:83], v[48:51]\n"
      "v_mfma_f32_16x16x32_bf16 v[52:55], v[156:159], a[84:87], v[52:55]\n"
      "global_load_dwordx4 v[168:171], v241, s[72:73] offset:-3072 sc1\n"
      "v_mfma_f32_16x16x32_bf16 v[56:59], v[156:159], a[88:91], v[56:59]\n"
      "v_mfma_f32_16x16x32_bf16 v[60:63], v[156:159], a[92:95], v[60:63]\n"
      "s_waitcnt vmcnt(1)\n"
      "v_mfma_f32_16x16x32_bf16 v[64:67], v[160:163], a[64:67], v[64:67]\n"
      "global_load_dwordx4 v[172:175], v241, s[72:73] offset:-2048 sc1\n"
      "v_mfma_f32_16x16x32_bf16 v[68:71], v[160:163], a[68:71], v[68:71]\n"
      "v_mfma_f32_16x16x32_bf16 v[72:75], v[160:163], a[72:75], v[72:75]\n"
      "v_mfma_f32_16x16x32_bf16 v[76:79], v[160:163], a[76:79], v[76:79]\n"
      "global_load_dwordx4 v[176:179], v240, s[14:15] offset:-4096 sc1\n"
      "v_mfma_f32_16x16x32_bf16 v[80:83], v[160:163], a[80:83], v[80:83]\n"
      "ds_read_b128 a[136:139], v248 offset:8192\n"
      "ds_read_b128 a[140:143], v248 offset:10240\n"
      "v_mfma_f32_16x16x32_bf16 v[84:87], v[160:163], a[84:87], v[84:87]\n"
      "ds_read_b128 a[168:171], v249 offset:8192\n"
      "ds_read_b128 a[172:175], v249 offset:10240\n"
      "v_mfma_f32_16x16x32_bf16 v[88:91], v[160:163], a[88:91], v[88:91]\n"
      "global_load_dwordx4 v[180:183], v240, s[14:15] offset:-3072 sc1\n"
      "v_mfma_f32_16x16x32_bf16 v[92:95], v[160:163], a[92:95], v[92:95]\n"
      "v_mfma_f32_16x16x32_bf16 v[0:3], v[164:167], a[96:99], v[0:3]\n"
      "v_mfma_f32_16x16x32_bf16 v[4:7], v[164:167], a[100:103], v[4:7]\n"
      "global_load_dwordx4 v[184:187], v240, s[14:15] offset:-2048 sc1\n"
      "global_load_dwordx4 v[188:191], v240, s[74:75] offset:-4096 sc1\n"
      "v_mfma_f32_16x16x32_bf16 v[8:11], v[164:167], a[104:107], v[8:11]\n"
      "global_load_dwordx4 v[128:131], v240, s[74:75] offset:-3072 sc1\n"
      "global_load_dword v252, v253, s[58:59] offset:128 sc1\n"
      "v_mfma_f32_16x16x32_bf16 v[12:15], v[164:167], a[108:111], v[12:15]\n"
      "global_load_dwordx4 v[132:135], v240, s[74:75] offset:-2048 sc1\n"
      "v_mfma_f32_16x16x32_bf16 v[16:19], v[164:167], a[112:115], v[16:19]\n"
      "v_mfma_f32_16x16x32_bf16 v[20:23], v[164:167], a[116:119], v[20:23]\n"
      "s_waitcnt lgkmcnt(9)\n"
      "v_mfma_f32_16x16x32_bf16 v[24:27], v[164:167], a[120:123], v[24:27]\n"
      "s_waitcnt lgkmcnt(8)\n"
      "v_mfma_f32_16x16x32_bf16 v[28:31], v[164:167], a[124:127], v[28:31]\n"
      "s_waitcnt vmcnt(7)\n"
      "v_mfma_f32_16x16x32_bf16 v[32:35], v[168:171], a[96:99], v[32:35]\n"
      "v_mfma_f32_16x16x32_bf16 v[36:39], v[168:171], a[100:103], v[36:39]\n"
      "v_mfma_f32_16x16x32_bf16 v[40:43], v[168:171], a[104:107], v[40:43]\n"
      "v_mfma_f32_16x16x32_bf16 v[44:47], v[168:171], a[108:111], v[44:47]\n"
      "v_mfma_f32_16x16x32_bf16 v[48:51], v[168:171], a[112:115], v[48:51]\n"
      "v_mfma_f32_16x16x32_bf16 v[52:55], v[168:171], a[116:119], v[52:55]\n"
      "s_lshr_b32 s54, s34, 1\n"
      "s_lshl2_add_u32 s54, s54, s35\n"
      "s_lshl_b32 s54, s54, 12\n"
      "v_mfma_f32_16x16x32_bf16 v[56:59], v[168:171], a[120:123], v[56:59]\n"
      "s_add_u32 s54, s54, 4096\n"
      "v_lshl_add_u32 v232, v243, 6, s54\n"
      "global_load_dwordx2 v[234:235], v232, s[58:59] sc1\n"
      "v_mfma_f32_16x16x32_bf16 v[60:63], v[168:171], a[124:127], v[60:63]\n"
      "v_mfma_f32_16x16x32_bf16 v[64:67], v[172:175], a[96:99], v[64:67]\n"
      "v_mfma_f32_16x16x32_bf16 v[68:71], v[172:175], a[100:103], v[68:71]\n"
      "ds_read_b128 a[144:147], v248 offset:12288\n"
      "ds_read_b128 a[148:151], v248 offset:14336\n"
      "v_mfma_f32_16x16x32_bf16 v[72:75], v[172:175], a[104:107], v[72:75]\n"
      "ds_read_b128 a[176:179], v249 offset:12288\n"
      "ds_read_b128 a[180:183], v249 offset:14336\n"
      "v_mfma_f32_16x16x32_bf16 v[76:79], v[172:175], a[108:111], v[76:79]\n"
      "v_mfma_f32_16x16x32_bf16 v[80:83], v[172:175], a[112:115], v[80:83]\n"
      "v_mfma_f32_16x16x32_bf16 v[84:87], v[172:175], a[116:119], v[84:87]\n"
      "s_xor_b32 s70, s76, 384\n"
      "s_add_u32 s70, s8, s70\n"
      "s_addc_u32 s71, s9, 0\n"
      "s_add_u32 m0, s82, 98304\n"
      "v_mfma_f32_16x16x32_bf16 v[88:91], v[172:175], a[120:123], v[88:91]\n"
      "global_load_lds_dwordx4 v244, s[70:71] offset:0\n"
      "v_mfma_f32_16x16x32_bf16 v[92:95], v[172:175], a[124:127], v[92:95]\n"
      "s_waitcnt vmcnt(7)\n"
      "s_waitcnt lgkmcnt(11)\n"
      "v_mfma_f32_16x16x32_bf16 v[0:3], v[176:179], a[128:131], v[0:3]\n"
      "s_waitcnt lgkmcnt(10)\n"
      "v_mfma_f32_16x16x32_bf16 v[4:7], v[176:179], a[132:135], v[4:7]\n"
      "global_load_lds_dwordx4 v245, s[70:71] offset:1024\n"
      "s_waitcnt lgkmcnt(7)\n"
      "v_mfma_f32_16x16x32_bf16 v[8:11], v[176:179], a[136:139], v[8:11]\n"
      "s_waitcnt lgkmcnt(6)\n"
      "v_mfma_f32_16x16x32_bf16 v[12:15], v[176:179], a[140:143], v[12:15]\n"
      "s_waitcnt lgkmcnt(3)\n"
      "v_mfma_f32_16x16x32_bf16 v[16:19], v[176:179], a[144:147], v[16:19]\n"
      "global_load_lds_dwordx4 v246, s[70:71] offset:2048\n"
      "s_waitcnt lgkmcnt(2)\n"
      "v_mfma_f32_16x16x32_bf16 v[20:23], v[176:179], a[148:151], v[20:23]\n"
      "ds_read_b128 a[152:155], v248 offset:0\n"
      "ds_read_b128 a[156:159], v248 offset:2048\n"
      "ds_read_b128 a[184:187], v249 offset:0\n"
      "ds_read_b128 a[188:191], v249 offset:2048\n"
      "s_waitcnt lgkmcnt(3)\n"
      "v_mfma_f32_16x16x32_bf16 v[24:27], v[176:179], a[152:155], v[24:27]\n"
      "s_waitcnt lgkmcnt(2)\n"
      "v_mfma_f32_16x16x32_bf16 v[28:31], v[176:179], a[156:159], v[28:31]\n"
      "global_load_lds_dwordx4 v247, s[70:71] offset:3072\n"
      "v_mfma_f32_16x16x32_bf16 v[32:35], v[180:183], a[128:131], v[32:35]\n"
      "v_mfma_f32_16x16x32_bf16 v[36:39], v[180:183], a[132:135], v[36:39]\n"
      "s_xor_b32 s70, s77, 384\n"
      "s_add_u32 s70, s8, s70\n"
      "s_addc_u32 s71, s9, 0\n"
      "s_add_u32 m0, s82, 114688\n"
      "v_mfma_f32_16x16x32_bf16 v[40:43], v[180:183], a[136:139], v[40:43]\n"
      "global_load_lds_dwordx4 v244, s[70:71] offset:0\n"
      "v_mfma_f32_16x16x32_bf16 v[44:47], v[180:183], a[140:143], v[44:47]\n"
      "v_mfma_f32_16x16x32_bf16 v[48:51], v[180:183], a[144:147], v[48:51]\n"
      "v_mfma_f32_16x16x32_bf16 v[52:55], v[180:183], a[148:151], v[52:55]\n"
      "global_load_lds_dwordx4 v245, s[70:71] offset:1024\n"
      "v_mfma_f32_16x16x32_bf16 v[56:59], v[180:183], a[152:155], v[56:59]\n"
      "s_waitcnt vmcnt(8)\n"
      "v_cmp_ne_u32 vcc, s37, v252\n"
      "s_nop 1\n"
      "s_cbranch_vccnz .Lp_s5_%=\n"
      ".Lp_o6_%=:\n"
      "v_mfma_f32_16x16x32_bf16 v[60:63], v[180:183], a[156:159], v[60:63]\n"
      "v_mfma_f32_16x16x32_bf16 v[64:67], v[184:187], a[128:131], v[64:67]\n"
      "global_load_lds_dwordx4 v246, s[70:71] offset:2048\n"
      "global_load_dwordx4 v[136:139], v240, s[10:11] offset:-1024 sc1\n"
      "v_mfma_f32_16x16x32_bf16 v[68:71], v[184:187], a[132:135], v[68:71]\n"
      "v_mfma_f32_16x16x32_bf16 v[72:75], v[184:187], a[136:139], v[72:75]\n"
      "v_mfma_f32_16x16x32_bf16 v[76:79], v[184:187], a[140:143], v[76:79]\n"
      "global_load_lds_dwordx4 v247, s[70:71] offset:3072\n"
      "v_mfma_f32_16x16x32_bf16 v[80:83], v[184:187], a[144:147], v[80:83]\n"
      "global_load_dwordx4 v[140:143], v240, s[72:73] offset:-1024 sc1\n"
      "v_mfma_f32_16x16x32_bf16 v[84:87], v[184:187], a[148:151], v[84:87]\n"
      "s_xor_b32 s70, s78, 384\n"
      "s_add_u32 s70, s8, s70\n"
      "s_addc_u32 s71, s9, 0\n"
      "s_add_u32 m0, s41, 4096\n"
      "v_mfma_f32_16x16x32_bf16 v[88:91], v[184:187], a[152:155], v[88:91]\n"
      "global_load_lds_dwordx4 v244, s[70:71] offset:0\n"
      "v_mfma_f32_16x16x32_bf16 v[92:95], v[184:187], a[156:159], v[92:95]\n"
      "v_mfma_f32_16x16x32_bf16 v[0:3], v[188:191], a[160:163], v[0:3]\n"
      "global_load_dwordx4 v[144:147], v241, s[10:11] offset:-1024 sc1\n"
      "v_mfma_f32_16x16x32_bf16 v[4:7], v[188:191], a[164:167], v[4:7]\n"
      "global_load_lds_dwordx4 v245, s[70:71] offset:1024\n"
      "v_mfma_f32_16x16x32_bf16 v[8:11], v[188:191], a[168:171], v[8:11]\n"
      "v_mfma_f32_16x16x32_bf16 v[12:15], v[188:191], a[172:175], v[12:15]\n"
      "v_mfma_f32_16x16x32_bf16 v[16:19], v[188:191], a[176:179], v[16:19]\n"
      "global_load_lds_dwordx4 v246, s[70:71] offset:2048\n"
      "global_load_dwordx4 v[148:151], v241, s[14:15] offset:-4096 sc1\n"
      "v_mfma_f32_16x16x32_bf16 v[20:23], v[188:191], a[180:183], v[20:23]\n"
      "s_waitcnt lgkmcnt(1)\n"
      "v_mfma_f32_16x16x32_bf16 v[24:27], v[188:191], a[184:187], v[24:27]\n"
      "s_waitcnt lgkmcnt(0)\n"
      "v_mfma_f32_16x16x32_bf16 v[28:31], v[188:191], a[188:191], v[28:31]\n"
      "s_waitcnt vmcnt(16)\n"
      "global_load_lds_dwordx4 v247, s[70:71] offset:3072\n"
      "v_mfma_f32_16x16x32_bf16 v[32:35], v[128:131], a[160:163], v[32:35]\n"
      "global_load_dwordx4 v[152:155], v241, s[74:75] offset:-4096 sc1\n"
      "v_mfma_f32_16x16x32_bf16 v[36:39], v[128:131], a[164:167], v[36:39]\n"
      "s_xor_b32 s70, s79, 384\n"
      "s_add_u32 s70, s8, s70\n"
      "s_addc_u32 s71, s9, 0\n"
      "s_add_u32 m0, s82, 131072\n"
      "v_mfma_f32_16x16x32_bf16 v[40:43], v[128:131], a[168:171], v[40:43]\n"
      "global_load_lds_dwordx4 v244, s[70:71] offset:0\n"
      "v_mfma_f32_16x16x32_bf16 v[44:47], v[128:131], a[172:175], v[44:47]\n"
      "v_mfma_f32_16x16x32_bf16 v[48:51], v[128:131], a[176:179], v[48:51]\n"
      "global_load_dwordx4 v[156:159], v241, s[14:15] offset:-3072 sc1\n"
      "v_mfma_f32_16x16x32_bf16 v[52:55], v[128:131], a[180:183], v[52:55]\n"
      "global_load_lds_dwordx4 v245, s[70:71] offset:1024\n"
      "v_mfma_f32_16x16x32_bf16 v[56:59], v[128:131], a[184:187], v[56:59]\n"
      "v_mfma_f32_16x16x32_bf16 v[60:63], v[128:131], a[188:191], v[60:63]\n"
      "v_mfma_f32_16x16x32_bf16 v[64:67], v[132:135], a[160:163], v[64:67]\n"
      "global_load_lds_dwordx4 v246, s[70:71] offset:2048\n"
      "global_load_dwordx4 v[160:163], v241, s[74:75] offset:-3072 sc1\n"
      "v_mfma_f32_16x16x32_bf16 v[68:71], v[132:135], a[164:167], v[68:71]\n"
      "v_mfma_f32_16x16x32_bf16 v[72:75], v[132:135], a[168:171], v[72:75]\n"
      "v_mfma_f32_16x16x32_bf16 v[76:79], v[132:135], a[172:175], v[76:79]\n"
      "global_load_lds_dwordx4 v247, s[70:71] offset:3072\n"
      "v_mfma_f32_16x16x32_bf16 v[80:83], v[132:135], a[176:179], v[80:83]\n"
      "global_load_dwordx4 v[164:167], v241, s[14:15] offset:-2048 sc1\n"
      "v_mfma_f32_16x16x32_bf16 v[84:87], v[132:135], a[180:183], v[84:87]\n"
      "s_barrier\n"
      "s_lshr_b32 s80, s32, 4\n"
      "s_bfe_u32 s43, s32, 0x10002\n"
      "s_lshl1_add_u32 s80, s80, s43\n"
      "s_and_b32 s81, s32, 3\n"
      "s_cmp_lt_u32 s80, 28\n"
      "s_cselect_b32 s86, -1, 0\n"
      "s_lshr_b32 s83, s33, 1\n"
      "s_lshl1_add_u32 s83, s81, s83\n"
      "s_min_u32 s43, s80, 27\n"
      "v_lshrrev_b32 v226, 3, v243\n"
      "v_and_b32 v227, 0x70, v242\n"
      "v_lshl_or_b32 v224, v226, 10, v227\n"
      "v_add_u32 v225, 7168, v224\n"
      "v_add_u32 v226, 14336, v224\n"
      "v_add_u32 v227, 21504, v224\n"
      "s_lshr_b32 s44, s33, 1\n"
      "s_lshl1_add_u32 s44, s43, s44\n"
      "s_lshl_b32 s44, s44, 15\n"
      "s_and_b32 s45, s33, 1\n"
      "s_lshl1_add_u32 s45, s81, s45\n"
      "s_lshl_b32 s45, s45, 7\n"
      "s_add_u32 s44, s44, s45\n"
      "s_add_u32 s70, s30, s44\n"
      "s_addc_u32 s71, s31, 0\n"
      "s_lshl_b32 s43, s33, 12\n"
      "s_add_u32 m0, s43, 65536\n"
      "s_nop 0\n"
      "global_load_lds_dwordx4 v224, s[70:71] offset:0\n"
      "global_load_lds_dwordx4 v225, s[70:71] offset:1024\n"
      "global_load_lds_dwordx4 v226, s[70:71] offset:2048\n"
      "global_load_lds_dwordx4 v227, s[70:71] offset:3072\n"
      "v_mfma_f32_16x16x32_bf16 v[88:91], v[132:135], a[184:187], v[88:91]\n"
      "v_mfma_f32_16x16x32_bf16 v[92:95], v[132:135], a[188:191], v[92:95]\n"
      "s_waitcnt vmcnt(18)\n"
      "v_mfma_f32_16x16x32_bf16 v[96:99], v[136:139], a[0:3], 0\n"
      "global_load_dwordx4 v[168:171], v241, s[74:75] offset:-2048 sc1\n"
      "v_mfma_f32_16x16x32_bf16 v[100:103], v[136:139], a[4:7], 0\n"
      "v_mfma_f32_16x16x32_bf16 v[104:107], v[136:139], a[8:11], 0\n"
      "v_mfma_f32_16x16x32_bf16 v[108:111], v[136:139], a[12:15], 0\n"
      "v_mfma_f32_16x16x32_bf16 v[112:115], v[136:139], a[16:19], 0\n"
      "global_load_dwordx4 v[172:175], v241, s[72:73] offset:-1024 sc1\n"
      "ds_read_b128 a[192:195], v236 offset:0\n"
      "v_mfma_f32_16x16x32_bf16 v[116:119], v[136:139], a[20:23], 0\n"
      "ds_read_b128 a[196:199], v236 offset:2048\n"
      "ds_read_b128 a[224:227], v237 offset:0\n"
      "v_mfma_f32_16x16x32_bf16 v[120:123], v[136:139], a[24:27], 0\n"
      "ds_read_b128 a[228:231], v237 offset:2048\n"
      "v_mfma_f32_16x16x32_bf16 v[124:127], v[136:139], a[28:31], 0\n"
      "v_mfma_f32_16x16x32_bf16 v[96:99], v[140:143], a[32:35], v[96:99]\n"
      "v_mfma_f32_16x16x32_bf16 v[100:103], v[140:143], a[36:39], v[100:103]\n"
      "s_lshl2_add_u32 s54, s36, s33\n"
      "s_lshl_b32 s55, s54, 15\n"
      "s_lshl2_add_u32 s56, s35, s34\n"
      "v_mfma_f32_16x16x32_bf16 v[104:107], v[140:143], a[40:43], v[104:107]\n"
      "s_lshl_b32 s56, s56, 6\n"
      "global_load_dwordx4 v[176:179], v240, s[14:15] offset:-1024 sc1\n"
      "s_add_u32 s55, s55, s56\n"
      "v_mfma_f32_16x16x32_bf16 v[108:111], v[140:143], a[44:47], v[108:111]\n"
      "v_and_b32 v209, 15, v243\n"
      "v_and_b32 v208, 48, v243\n"
      "v_mfma_f32_16x16x32_bf16 v[112:115], v[140:143], a[48:51], v[112:115]\n"
      "v_lshl_or_b32 v208, v209, 10, v208\n"
      "v_add_u32 v208, s55, v208\n"
      "v_mfma_f32_16x16x32_bf16 v[116:119], v[140:143], a[52:55], v[116:119]\n"
      "v_add_u32 v210, 16384, v208\n"
      "global_load_dwordx4 v[216:219], v208, s[16:17]\n"
      "v_mfma_f32_16x16x32_bf16 v[120:123], v[140:143], a[56:59], v[120:123]\n"
      "global_load_dwordx4 v[220:223], v210, s[16:17]\n"
      "s_lshr_b32 s55, s34, 1\n"
      "v_mfma_f32_16x16x32_bf16 v[124:127], v[140:143], a[60:63], v[124:127]\n"
      "s_waitcnt vmcnt(18)\n"
      "s_lshl2_add_u32 s55, s55, s35\n"
      "s_lshl_b32 s55, s55, 15\n"
      "v_mfma_f32_16x16x32_bf16 v[96:99], v[144:147], a[64:67], v[96:99]\n"
      "s_lshl_b32 s54, s54, 9\n"
      "ds_read_b128 a[200:203], v236 offset:16384\n"
      "s_add_u32 s54, s54, s55\n"
      "v_mfma_f32_16x16x32_bf16 v[100:103], v[144:147], a[68:71], v[100:103]\n"
      "s_add_u32 s54, s54, 786432\n"
      "ds_read_b128 a[204:207], v236 offset:18432\n"
      "v_lshl_add_u32 v209, v209, 4, s54\n"
      "v_mfma_f32_16x16x32_bf16 v[104:107], v[144:147], a[72:75], v[104:107]\n"
      "global_load_dwordx4 v[180:183], v240, s[74:75] offset:-1024 sc1\n"
      "ds_read_b128 a[232:235], v237 offset:16384\n"
      "v_mfma_f32_16x16x32_bf16 v[108:111], v[144:147], a[76:79], v[108:111]\n"
      "ds_read_b128 a[236:239], v237 offset:18432\n"
      "global_load_dwordx2 v[204:205], v209, s[58:59] offset:0 sc1\n"
      "v_mfma_f32_16x16x32_bf16 v[112:115], v[144:147], a[80:83], v[112:115]\n"
      "global_load_dwordx2 v[206:207], v209, s[58:59] offset:256 sc1\n"
      "s_add_u32 s68, s20, 16384\n"
      "s_addc_u32 s69, s21, 0\n"
      "v_mfma_f32_16x16x32_bf16 v[116:119], v[144:147], a[84:87], v[116:119]\n"
      "v_cmp_ne_u32 vcc, s37, v235\n"
      "s_nop 1\n"
      "s_cbranch_vccnz .Lp_s8_%=\n"
      ".Lp_o9_%=:\n"
      "v_mfma_f32_16x16x32_bf16 v[120:123], v[144:147], a[88:91], v[120:123]\n"
      "v_max_u32_dpp v234, v234, v234 quad_perm:[1,0,3,2] row_mask:0xf bank_mask:0xf\n"
      "s_nop 1\n"
      "v_max_u32_dpp v234, v234, v234 quad_perm:[2,3,0,1] row_mask:0xf bank_mask:0xf\n"
      "v_mfma_f32_16x16x32_bf16 v[124:127], v[144:147], a[92:95], v[124:127]\n"
      "s_nop 0\n"
      "v_max_u32_dpp v234, v234, v234 row_half_mirror row_mask:0xf bank_mask:0xf\n"
      "s_waitcnt lgkmcnt(7)\n"
      "v_mfma_f32_16x16x32_bf16 v[0:3], v[148:151], a[192:195], v[0:3]\n"
      "v_max_u32_dpp v234, v234, v234 row_mirror row_mask:0xf bank_mask:0xf\n"
      "v_mov_b32 v233, v234\n"
      "s_waitcnt lgkmcnt(6)\n"
      "v_mfma_f32_16x16x32_bf16 v[4:7], v[148:151], a[196:199], v[4:7]\n"
      "v_permlane16_swap_b32 v234, v233\n"
      "v_max_u32 v234, v234, v233\n"
      "s_waitcnt lgkmcnt(3)\n"
      "v_mfma_f32_16x16x32_bf16 v[8:11], v[148:151], a[200:203], v[8:11]\n"
      "global_load_dwordx4 v[184:187], v241, s[14:15] offset:-1024 sc1\n"
      "v_mov_b32 v233, v234\n"
      "s_waitcnt lgkmcnt(2)\n"
      "v_mfma_f32_16x16x32_bf16 v[12:15], v[148:151], a[204:207], v[12:15]\n"
      "v_permlane32_swap_b32 v234, v233\n"
      "v_max_u32 v234, v234, v233\n"
      "s_waitcnt vmcnt(21)\n"
      "ds_read_b128 a[208:211], v248 offset:4096\n"
      "ds_read_b128 a[212:215], v248 offset:6144\n"
      "ds_read_b128 a[240:243], v249 offset:4096\n"
      "ds_read_b128 a[244:247], v249 offset:6144\n"
      "s_waitcnt lgkmcnt(3)\n"
      "v_mfma_f32_16x16x32_bf16 v[16:19], v[148:151], a[208:211], v[16:19]\n"
      "s_waitcnt vmcnt(14)\n"
      "ds_read_b128 a[216:219], v236 offset:32768\n"
      "s_waitcnt lgkmcnt(3)\n"
      "v_mfma_f32_16x16x32_bf16 v[20:23], v[148:151], a[212:215], v[20:23]\n"
      "ds_read_b128 a[220:223], v236 offset:34816\n"
      "v_readfirstlane_b32 s40, v234\n"
      "ds_read_b128 a[248:251], v237 offset:32768\n"
      "ds_read_b128 a[252:255], v237 offset:34816\n"
      "s_waitcnt lgkmcnt(3)\n"
      "v_mfma_f32_16x16x32_bf16 v[24:27], v[148:151], a[216:219], v[24:27]\n"
      "s_sub_u32 s48, s33, 1\n"
      "s_and_b32 s48, s48, 3\n"
      "s_lshl_b32 s48, s48, 14\n"
      "s_waitcnt lgkmcnt(2)\n"
      "v_mfma_f32_16x16x32_bf16 v[28:31], v[148:151], a[220:223], v[28:31]\n"
      "v_lshl_add_u32 v244, v243, 2, s48\n"
      "s_sub_u32 s48, s33, 2\n"
      "v_mfma_f32_16x16x32_bf16 v[0:3], v[152:155], a[224:227], v[0:3]\n"
      "s_and_b32 s48, s48, 3\n"
      "s_lshl_b32 s48, s48, 14\n"
      "v_lshl_add_u32 v245, v243, 2, s48\n"
      "v_mfma_f32_16x16x32_bf16 v[4:7], v[152:155], a[228:231], v[4:7]\n"
      "s_sub_u32 s48, s33, 3\n"
      "s_and_b32 s48, s48, 3\n"
      "s_lshl_b32 s48, s48, 14\n"
      "v_mfma_f32_16x16x32_bf16 v[8:11], v[152:155], a[232:235], v[8:11]\n"
      "s_mov_b32 m0, s41\n"
      "global_load_dwordx4 v[188:191], v241, s[74:75] offset:-1024 sc1\n"
      "v_lshl_add_u32 v246, v243, 2, s48\n"
      "v_mfma_f32_16x16x32_bf16 v[12:15], v[152:155], a[236:239], v[12:15]\n"
      "s_andn2_b32 s42, 0x7ff, s40\n"
      "v_bfe_u32 v248, v243, 1, 3\n"
      "v_lshrrev_b32 v249, 4, v243\n"
      "v_mfma_f32_16x16x32_bf16 v[16:19], v[152:155], a[240:243], v[16:19]\n"
      "v_lshlrev_b32 v249, 1, v249\n"
      "v_sub_u32 v248, v248, v249\n"
      "v_mfma_f32_16x16x32_bf16 v[20:23], v[152:155], a[244:247], v[20:23]\n"
      "v_and_b32 v249, 16, v242\n"
      "v_mov_b32 v254, 0x3f80\n"
      "s_waitcnt lgkmcnt(1)\n"
      "v_mfma_f32_16x16x32_bf16 v[24:27], v[152:155], a[248:251], v[24:27]\n"
      "v_lshlrev_b32 v249, v249, v254\n"
      "v_cvt_pk_bf16_f32 v8, v8, v9\n"
      "s_waitcnt lgkmcnt(0)\n"
      "v_mfma_f32_16x16x32_bf16 v[28:31], v[152:155], a[252:255], v[28:31]\n"
      "v_cmp_eq_u32 vcc, 0, v248\n"
      "s_nop 1\n"
      "v_cndmask_b32 v250, 0, v249, vcc\n"
      "v_mfma_f32_16x16x32_bf16 v[32:35], v[156:159], a[192:195], v[32:35]\n"
      "v_cmp_eq_u32 vcc, 1, v248\n"
      "s_nop 1\n"
      "v_cndmask_b32 v251, 0, v249, vcc\n"
      "v_mfma_f32_16x16x32_bf16 v[36:39], v[156:159], a[196:199], v[36:39]\n"
      "v_mov_b32 v252, v250\n"
      "v_mov_b32 v253, v251\n"
      "v_mfma_f32_16x16x32_bf16 v[40:43], v[156:159], a[200:203], v[40:43]\n"
      "global_load_dwordx4 v[128:131], v240, s[10:11] offset:0 sc1\n"
      "v_mov_b32 v248, 0\n"
      "v_mfma_f32_16x16x32_bf16 v[44:47], v[156:159], a[204:207], v[44:47]\n"
      "v_mov_b32 v249, 0\n"
      "v_mov_b32 v254, 0\n"
      "v_mfma_f32_16x16x32_bf16 v[48:51], v[156:159], a[208:211], v[48:51]\n"
      "v_mov_b32 v255, 0\n"
      "v_lshrrev_b32 v201, 4, v243\n"
      "v_mfma_f32_16x16x32_bf16 v[52:55], v[156:159], a[212:215], v[52:55]\n"
      "s_add_u32 s48, s34, 1\n"
      "v_bfe_u32 v224, v243, 2, 2\n"
      "v_lshl_or_b32 v201, v201, 3, v224\n"
      "v_mfma_f32_16x16x32_bf16 v[56:59], v[156:159], a[216:219], v[56:59]\n"
      "s_and_b32 s48, s48, 3\n"
      "v_and_b32 v224, 3, v243\n"
      "v_lshlrev_b32 v224, 3, v224\n"
      "v_mfma_f32_16x16x32_bf16 v[60:63], v[156:159], a[220:223], v[60:63]\n"
      "s_waitcnt vmcnt(15)\n"
      "v_lshl_or_b32 v201, v201, 7, v224\n"
      "s_mov_b32 s64, 0xffff0000\n"
      "v_mfma_f32_16x16x32_bf16 v[32:35], v[160:163], a[224:227], v[32:35]\n"
      "s_mov_b32 s65, 0xffff0000\n"
      "s_mov_b32 s66, -1\n"
      "s_mov_b32 s67, 0\n"
      "v_mfma_f32_16x16x32_bf16 v[36:39], v[160:163], a[228:231], v[36:39]\n"
      "s_mul_i32 s48, s48, 36864\n"
      "v_bfe_u32 v224, v243, 4, 1\n"
      "v_and_b32 v203, 0xf0, v242\n"
      "v_mfma_f32_16x16x32_bf16 v[40:43], v[160:163], a[232:235], v[40:43]\n"
      "s_add_u32 s48, s48, 0\n"
      "global_load_dwordx4 v[132:135], v240, s[72:73] offset:0 sc1\n"
      "v_lshl_or_b32 v203, v224, 14, v203\n"
      "v_mfma_f32_16x16x32_bf16 v[44:47], v[160:163], a[236:239], v[44:47]\n"
      "s_lshr_b32 s54, s33, 1\n"
      "s_lshl1_add_u32 s54, s80, s54\n"
      "s_lshl_b32 s54, s54, 12\n"
      "v_mfma_f32_16x16x32_bf16 v[48:51], v[160:163], a[240:243], v[48:51]\n"
      "s_add_u32 s54, s54, 262144\n"
      "v_add_u32 v200, s54, v242\n"
      "s_lshl_b32 s54, s33, 12\n"
      "v_mfma_f32_16x16x32_bf16 v[52:55], v[160:163], a[244:247], v[52:55]\n"
      "s_add_u32 s54, s54, 65536\n"
      "v_add_u32 v201, s54, v201\n"
      "v_and_b32 v202, 15, v243\n"
      "v_mfma_f32_16x16x32_bf16 v[56:59], v[160:163], a[248:251], v[56:59]\n"
      "s_add_u32 s52, s60, s48\n"
      "s_addc_u32 s53, s61, 0\n"
      "v_lshrrev_b32 v199, 5, v243\n"
      "v_mfma_f32_16x16x32_bf16 v[60:63], v[160:163], a[252:255], v[60:63]\n"
      "v_lshl_or_b32 v202, v199, 4, v202\n"
      "s_add_u32 s54, s41, 12288\n"
      "v_mfma_f32_16x16x32_bf16 v[64:67], v[164:167], a[192:195], v[64:67]\n"
      "v_lshl_add_u32 v202, v202, 1, s54\n"
      "v_and_b32 v199, 48, v243\n"
      "v_mfma_f32_16x16x32_bf16 v[68:71], v[164:167], a[196:199], v[68:71]\n"
      "v_add_u32 v199, s54, v199\n"
      "s_and_b32 s54, s33, 1\n"
      "v_mfma_f32_16x16x32_bf16 v[72:75], v[164:167], a[200:203], v[72:75]\n"
      "s_lshl1_add_u32 s54, s81, s54\n"
      "global_load_dwordx4 v[136:139], v241, s[10:11] offset:0 sc1\n"
      "s_lshl_b32 s54, s54, 15\n"
      "v_mfma_f32_16x16x32_bf16 v[76:79], v[164:167], a[204:207], v[76:79]\n"
      "s_lshr_b32 s55, s33, 1\n"
      "s_lshl1_add_u32 s55, s80, s55\n"
      "s_lshl_b32 s55, s55, 8\n"
      "v_mfma_f32_16x16x32_bf16 v[80:83], v[164:167], a[208:211], v[80:83]\n"
      "s_add_u32 s54, s54, s55\n"
      "s_add_u32 s54, s54, 524288\n"
      "v_add_u32 v203, s54, v203\n"
      "v_mfma_f32_16x16x32_bf16 v[84:87], v[164:167], a[212:215], v[84:87]\n"
      "v_cvt_pk_bf16_f32 v9, v10, v11\n"
      "ds_write_addtid_b32 v8 offset:0\n"
      "v_mfma_f32_16x16x32_bf16 v[88:91], v[164:167], a[216:219], v[88:91]\n"
      "global_load_dwordx4 v[140:143], v241, s[72:73] offset:0 sc1\n"
      "v_cvt_pk_bf16_f32 v10, v12, v13\n"
      "v_mfma_f32_16x16x32_bf16 v[92:95], v[164:167], a[220:223], v[92:95]\n"
      "s_waitcnt vmcnt(12)\n"
      "ds_write_addtid_b32 v9 offset:256\n"
      "v_cvt_pk_bf16_f32 v11, v14, v15\n"
      "v_mfma_f32_16x16x32_bf16 v[64:67], v[168:171], a[224:227], v[64:67]\n"
      "ds_write_addtid_b32 v10 offset:512\n"
      "v_cvt_pk_bf16_f32 v12, v16, v17\n"
      "v_mfma_f32_16x16x32_bf16 v[68:71], v[168:171], a[228:231], v[68:71]\n"
      "ds_write_addtid_b32 v11 offset:768\n"
      "v_cvt_pk_bf16_f32 v13, v18, v19\n"
      "v_mfma_f32_16x16x32_bf16 v[72:75], v[168:171], a[232:235], v[72:75]\n"
      "global_load_dwordx4 v[144:147], v240, s[14:15] offset:0 sc1\n"
      "ds_write_addtid_b32 v12 offset:1024\n"
      "v_mfma_f32_16x16x32_bf16 v[76:79], v[168:171], a[236:239], v[76:79]\n"
      "v_cvt_pk_bf16_f32 v14, v20, v21\n"
      "ds_write_addtid_b32 v13 offset:1280\n"
      "v_mfma_f32_16x16x32_bf16 v[80:83], v[168:171], a[240:243], v[80:83]\n"
      "v_cvt_pk_bf16_f32 v15, v22, v23\n"
      "ds_write_addtid_b32 v14 offset:1536\n"
      "v_mfma_f32_16x16x32_bf16 v[84:87], v[168:171], a[244:247], v[84:87]\n"
      "v_cvt_pk_bf16_f32 v16, v24, v25\n"
      "ds_write_addtid_b32 v15 offset:1792\n"
      "v_mfma_f32_16x16x32_bf16 v[88:91], v[168:171], a[248:251], v[88:91]\n"
      "global_load_dwordx4 v[148:151], v240, s[74:75] offset:0 sc1\n"
      "v_cvt_pk_bf16_f32 v17, v26, v27\n"
      "v_mfma_f32_16x16x32_bf16 v[92:95], v[168:171], a[252:255], v[92:95]\n"
      "ds_write_addtid_b32 v16 offset:2048\n"
      "v_cvt_pk_bf16_f32 v18, v28, v29\n"
      "v_mfma_f32_16x16x32_bf16 v[96:99], v[172:175], a[96:99], v[96:99]\n"
      "ds_write_addtid_b32 v17 offset:2304\n"
      "v_cvt_pk_bf16_f32 v19, v30, v31\n"
      "v_mfma_f32_16x16x32_bf16 v[100:103], v[172:175], a[100:103], v[100:103]\n"
      "ds_write_addtid_b32 v18 offset:2560\n"
      "ds_write_addtid_b32 v19 offset:2816\n"
      "v_mfma_f32_16x16x32_bf16 v[104:107], v[172:175], a[104:107], v[104:107]\n"
      "global_load_dwordx4 v[152:155], v241, s[14:15] offset:0 sc1\n"
      "v_cvt_pk_bf16_f32 v40, v40, v41\n"
      "v_mfma_f32_16x16x32_bf16 v[108:111], v[172:175], a[108:111], v[108:111]\n"
      "v_cvt_pk_bf16_f32 v41, v42, v43\n"
      "ds_write_addtid_b32 v40 offset:3072\n"
      "v_mfma_f32_16x16x32_bf16 v[112:115], v[172:175], a[112:115], v[112:115]\n"
      "v_cvt_pk_bf16_f32 v42, v44, v45\n"
      "ds_write_addtid_b32 v41 offset:3328\n"
      "v_mfma_f32_16x16x32_bf16 v[116:119], v[172:175], a[116:119], v[116:119]\n"
      "v_cvt_pk_bf16_f32 v43, v46, v47\n"
      "ds_write_addtid_b32 v42 offset:3584\n"
      "v_mfma_f32_16x16x32_bf16 v[120:123], v[172:175], a[120:123], v[120:123]\n"
      "global_load_dwordx4 v[156:159], v241, s[74:75] offset:0 sc1\n"
      "v_cvt_pk_bf16_f32 v44, v48, v49\n"
      "v_mfma_f32_16x16x32_bf16 v[124:127], v[172:175], a[124:127], v[124:127]\n"
      "s_waitcnt vmcnt(12)\n"
      "ds_write_addtid_b32 v43 offset:3840\n"
      "v_cvt_pk_bf16_f32 v45, v50, v51\n"
      "v_mfma_f32_16x16x32_bf16 v[96:99], v[176:179], a[128:131], v[96:99]\n"
      "ds_write_addtid_b32 v44 offset:4096\n"
      "v_cvt_pk_bf16_f32 v46, v52, v53\n"
      "v_mfma_f32_16x16x32_bf16 v[100:103], v[176:179], a[132:135], v[100:103]\n"
      "ds_write_addtid_b32 v45 offset:4352\n"
      "v_cvt_pk_bf16_f32 v47, v54, v55\n"
      "v_mfma_f32_16x16x32_bf16 v[104:107], v[176:179], a[136:139], v[104:107]\n"
      "global_load_dwordx4 v[160:163], v240, s[10:11] offset:1024 sc1\n"
      "ds_write_addtid_b32 v46 offset:4608\n"
      "v_mfma_f32_16x16x32_bf16 v[108:111], v[176:179], a[140:143], v[108:111]\n"
      "s_waitcnt vmcnt(11)\n"
      "v_bitop3_b32 v210, v205, v207, s37 bitop3:0x7e\n"
      "v_cmp_ne_u32 vcc, 0, v210\n"
      "s_nop 1\n"
      "s_cbranch_vccnz .Lp_s11_%=\n"
      ".Lp_o12_%=:\n"
      "v_mfma_f32_16x16x32_bf16 v[112:115], v[176:179], a[144:147], v[112:115]\n"
      "v_cvt_pk_bf16_f32 v48, v56, v57\n"
      "v_sub_f32 v205, 1.0, v204\n"
      "v_mfma_f32_16x16x32_bf16 v[116:119], v[176:179], a[148:151], v[116:119]\n"
      "ds_write_addtid_b32 v47 offset:4864\n"
      "v_cvt_pk_bf16_f32 v49, v58, v59\n"
      "v_mfma_f32_16x16x32_bf16 v[120:123], v[176:179], a[152:155], v[120:123]\n"
      "global_load_dwordx4 v[164:167], v240, s[72:73] offset:1024 sc1\n"
      "v_cvt_pk_bf16_f32 v205, v205, v205\n"
      "v_mfma_f32_16x16x32_bf16 v[124:127], v[176:179], a[156:159], v[124:127]\n"
      "ds_write_addtid_b32 v48 offset:5120\n"
      "v_cvt_pk_bf16_f32 v50, v60, v61\n"
      "v_mfma_f32_16x16x32_bf16 v[96:99], v[180:183], a[160:163], v[96:99]\n"
      "ds_write_addtid_b32 v49 offset:5376\n"
      "v_cvt_pk_bf16_f32 v51, v62, v63\n"
      "v_mfma_f32_16x16x32_bf16 v[100:103], v[180:183], a[164:167], v[100:103]\n"
      "ds_write_addtid_b32 v50 offset:5632\n"
      "v_and_b32 v205, 0xffff0000, v205\n"
      "v_mfma_f32_16x16x32_bf16 v[104:107], v[180:183], a[168:171], v[104:107]\n"
      "global_load_dwordx4 v[168:171], v241, s[10:11] offset:1024 sc1\n"
      "v_sub_f32 v207, 1.0, v206\n"
      "v_mfma_f32_16x16x32_bf16 v[108:111], v[180:183], a[172:175], v[108:111]\n"
      "ds_write_addtid_b32 v51 offset:5888\n"
      "v_cvt_pk_bf16_f32 v207, v207, v207\n"
      "v_mfma_f32_16x16x32_bf16 v[112:115], v[180:183], a[176:179], v[112:115]\n"
      "v_and_b32 v207, 0xffff0000, v207\n"
      "v_lshlrev_b32 v208, 16, v216\n"
      "v_mfma_f32_16x16x32_bf16 v[116:119], v[180:183], a[180:183], v[116:119]\n"
      "v_and_b32 v209, 0xffff0000, v216\n"
      "v_mul_f32 v208, v205, v208\n"
      "v_mfma_f32_16x16x32_bf16 v[120:123], v[180:183], a[184:187], v[120:123]\n"
      "global_load_dwordx4 v[172:175], v241, s[72:73] offset:1024 sc1\n"
      "v_mul_f32 v209, v205, v209\n"
      "v_mfma_f32_16x16x32_bf16 v[124:127], v[180:183], a[188:191], v[124:127]\n"
      "s_waitcnt vmcnt(12)\n"
      "s_waitcnt lgkmcnt(0)\n"
      "s_barrier\n"
      "v_mfma_f32_16x16x32_bf16 v[96:99], v[184:187], a[192:195], v[96:99]\n"
      "ds_read2st64_b32 v[20:21], v244 offset0:0 offset1:1\n"
      "v_lshlrev_b32 v212, 16, v220\n"
      "v_mfma_f32_16x16x32_bf16 v[100:103], v[184:187], a[196:199], v[100:103]\n"
      "ds_read2st64_b32 v[22:23], v245 offset0:4 offset1:5\n"
      "v_and_b32 v213, 0xffff0000, v220\n"
      "v_mfma_f32_16x16x32_bf16 v[104:107], v[184:187], a[200:203], v[104:107]\n"
      "global_load_dwordx4 v[176:179], v240, s[14:15] offset:1024 sc1\n"
      "v_mul_f32 v212, v207, v212\n"
      "v_mfma_f32_16x16x32_bf16 v[108:111], v[184:187], a[204:207], v[108:111]\n"
      "ds_read2st64_b32 v[24:25], v244 offset0:2 offset1:3\n"
      "v_mul_f32 v213, v207, v213\n"
      "v_mfma_f32_16x16x32_bf16 v[112:115], v[184:187], a[208:211], v[112:115]\n"
      "ds_read2st64_b32 v[26:27], v245 offset0:6 offset1:7\n"
      "v_lshlrev_b32 v210, 16, v217\n"
      "v_mfma_f32_16x16x32_bf16 v[116:119], v[184:187], a[212:215], v[116:119]\n"
      "ds_read2st64_b32 v[28:29], v246 offset0:8 offset1:9\n"
      "v_and_b32 v211, 0xffff0000, v217\n"
      "v_mfma_f32_16x16x32_bf16 v[120:123], v[184:187], a[216:219], v[120:123]\n"
      "global_load_dwordx4 v[180:183], v240, s[74:75] offset:1024 sc1\n"
      "v_mul_f32 v210, v205, v210\n"
      "v_mfma_f32_16x16x32_bf16 v[124:127], v[184:187], a[220:223], v[124:127]\n"
      "ds_read2st64_b32 v[30:31], v246 offset0:10 offset1:11\n"
      "v_mul_f32 v211, v205, v211\n"
      "v_mfma_f32_16x16x32_bf16 v[96:99], v[188:191], a[224:227], v[96:99]\n"
      "ds_read2st64_b32 v[52:53], v244 offset0:12 offset1:13\n"
      "v_lshlrev_b32 v214, 16, v221\n"
      "v_mfma_f32_16x16x32_bf16 v[100:103], v[188:191], a[228:231], v[100:103]\n"
      "ds_read2st64_b32 v[54:55], v245 offset0:16 offset1:17\n"
      "v_and_b32 v215, 0xffff0000, v221\n"
      "v_mfma_f32_16x16x32_bf16 v[104:107], v[188:191], a[232:235], v[104:107]\n"
      "global_load_dwordx4 v[184:187], v241, s[14:15] offset:1024 sc1\n"
      "v_mul_f32 v214, v207, v214\n"
      "v_mfma_f32_16x16x32_bf16 v[108:111], v[188:191], a[236:239], v[108:111]\n"
      "s_waitcnt lgkmcnt(2)\n"
      "v_mfma_f32_16x16x32_bf16 v[0:3], v[250:253], v[20:23], v[0:3]\n"
      "v_mul_f32 v215, v207, v215\n"
      "v_mfma_f32_16x16x32_bf16 v[112:115], v[188:191], a[240:243], v[112:115]\n"
      "v_mfma_f32_16x16x32_bf16 v[4:7], v[250:253], v[24:27], v[4:7]\n"
      "v_lshlrev_b32 v216, 16, v218\n"
      "v_mfma_f32_16x16x32_bf16 v[116:119], v[188:191], a[244:247], v[116:119]\n"
      "v_mfma_f32_16x16x32_bf16 v[0:3], v[252:255], v[28:31], v[0:3]\n"
      "v_and_b32 v217, 0xffff0000, v218\n"
      "v_mfma_f32_16x16x32_bf16 v[120:123], v[188:191], a[248:251], v[120:123]\n"
      "v_mfma_f32_16x16x32_bf16 v[4:7], v[248:251], v[28:31], v[4:7]\n"
      "v_mov_b32 v11, s37\n"
      "v_mfma_f32_16x16x32_bf16 v[124:127], v[188:191], a[252:255], v[124:127]\n"
      "s_waitcnt vmcnt(13)\n"
      "global_load_dwordx4 v[188:191], v241, s[74:75] offset:1024 sc1\n"
      "v_mul_f32 v216, v205, v216\n"
      "v_cvt_pk_bf16_f32 v8, v0, v1\n"
      "v_cvt_pk_bf16_f32 v9, v2, v3\n"
      "s_nop 0\n"
      "v_cvt_pk_bf16_f32 v10, v4, v5\n"
      "v_cvt_pk_bf16_f32 v247, v6, v7\n"
      "global_store_dwordx4 v242, v[8:11], s[52:53] offset:0 sc1\n"
      "v_mfma_f32_16x16x32_bf16 v[0:3], v[128:131], a[0:3], 0\n"
      "ds_read2st64_b32 v[56:57], v244 offset0:14 offset1:15\n"
      "v_mul_f32 v217, v205, v217\n"
      "v_mfma_f32_16x16x32_bf16 v[4:7], v[128:131], a[4:7], 0\n"
      "ds_read2st64_b32 v[58:59], v245 offset0:18 offset1:19\n"
      "v_lshlrev_b32 v220, 16, v222\n"
      "v_mfma_f32_16x16x32_bf16 v[8:11], v[128:131], a[8:11], 0\n"
      "ds_read2st64_b32 v[60:61], v246 offset0:20 offset1:21\n"
      "v_and_b32 v221, 0xffff0000, v222\n"
      "v_mfma_f32_16x16x32_bf16 v[12:15], v[128:131], a[12:15], 0\n"
      "ds_read2st64_b32 v[62:63], v246 offset0:22 offset1:23\n"
      "v_cvt_pk_bf16_f32 v72, v72, v73\n"
      "v_mfma_f32_16x16x32_bf16 v[16:19], v[128:131], a[16:19], 0\n"
      "v_cvt_pk_bf16_f32 v73, v74, v75\n"
      "v_mul_f32 v220, v207, v220\n"
      "v_mfma_f32_16x16x32_bf16 v[20:23], v[128:131], a[20:23], 0\n"
      "ds_write_addtid_b32 v72 offset:6144\n"
      "v_cvt_pk_bf16_f32 v74, v76, v77\n"
      "v_mfma_f32_16x16x32_bf16 v[24:27], v[128:131], a[24:27], 0\n"
      "ds_write_addtid_b32 v73 offset:6400\n"
      "v_cvt_pk_bf16_f32 v75, v78, v79\n"
      "v_mfma_f32_16x16x32_bf16 v[28:31], v[128:131], a[28:31], 0\n"
      "s_waitcnt lgkmcnt(2)\n"
      "global_load_dwordx4 v[128:131], v240, s[10:11] offset:2048 sc1\n"
      "v_mul_f32 v221, v207, v221\n"
      "v_mfma_f32_16x16x32_bf16 v[0:3], v[132:135], a[32:35], v[0:3]\n"
      "v_mfma_f32_16x16x32_bf16 v[32:35], v[250:253], v[52:55], v[32:35]\n"
      "v_lshlrev_b32 v218, 16, v219\n"
      "v_mfma_f32_16x16x32_bf16 v[4:7], v[132:135], a[36:39], v[4:7]\n"
      "v_mfma_f32_16x16x32_bf16 v[36:39], v[250:253], v[56:59], v[36:39]\n"
      "v_and_b32 v219, 0xffff0000, v219\n"
      "v_mfma_f32_16x16x32_bf16 v[8:11], v[132:135], a[40:43], v[8:11]\n"
      "v_mfma_f32_16x16x32_bf16 v[32:35], v[252:255], v[60:63], v[32:35]\n"
      "v_mul_f32 v218, v205, v218\n"
      "v_mfma_f32_16x16x32_bf16 v[12:15], v[132:135], a[44:47], v[12:15]\n"
      "v_mfma_f32_16x16x32_bf16 v[36:39], v[248:251], v[60:63], v[36:39]\n"
      "v_mov_b32 v43, s37\n"
      "v_mfma_f32_16x16x32_bf16 v[16:19], v[132:135], a[48:51], v[16:19]\n"
      "ds_write_addtid_b32 v74 offset:6656\n"
      "v_mov_b32 v44, v247\n"
      "v_mfma_f32_16x16x32_bf16 v[20:23], v[132:135], a[52:55], v[20:23]\n"
      "v_mov_b32 v47, s37\n"
      "v_cvt_pk_bf16_f32 v76, v80, v81\n"
      "v_mfma_f32_16x16x32_bf16 v[24:27], v[132:135], a[56:59], v[24:27]\n"
      "ds_write_addtid_b32 v75 offset:6912\n"
      "v_cvt_pk_bf16_f32 v77, v82, v83\n"
      "v_mfma_f32_16x16x32_bf16 v[28:31], v[132:135], a[60:63], v[28:31]\n"
      "s_waitcnt vmcnt(14)\n"
      "global_load_dwordx4 v[132:135], v240, s[72:73] offset:2048 sc1\n"
      "v_cvt_pk_bf16_f32 v40, v32, v33\n"
      "v_mfma_f32_16x16x32_bf16 v[0:3], v[136:139], a[64:67], v[0:3]\n"
      "ds_write_addtid_b32 v76 offset:7168\n"
      "v_cvt_pk_bf16_f32 v41, v34, v35\n"
      "v_mfma_f32_16x16x32_bf16 v[4:7], v[136:139], a[68:71], v[4:7]\n"
      "v_cvt_pk_bf16_f32 v42, v36, v37\n"
      "v_cvt_pk_bf16_f32 v45, v38, v39\n"
      "v_mfma_f32_16x16x32_bf16 v[8:11], v[136:139], a[72:75], v[8:11]\n"
      "global_store_dwordx4 v242, v[40:43], s[52:53] offset:1024 sc1\n"
      "v_cvt_pk_bf16_f32 v78, v84, v85\n"
      "v_mfma_f32_16x16x32_bf16 v[12:15], v[136:139], a[76:79], v[12:15]\n"
      "global_store_dwordx4 v242, v[44:47], s[52:53] offset:2048 sc1\n"
      "s_add_u32 s48, s34, 2\n"
      "v_mfma_f32_16x16x32_bf16 v[16:19], v[136:139], a[80:83], v[16:19]\n"
      "s_and_b32 s48, s48, 3\n"
      "ds_write_addtid_b32 v77 offset:7424\n"
      "s_mul_i32 s48, s48, 36864\n"
      "v_mfma_f32_16x16x32_bf16 v[20:23], v[136:139], a[84:87], v[20:23]\n"
      "s_add_u32 s48, s48, 12288\n"
      "s_add_u32 s52, s60, s48\n"
      "s_addc_u32 s53, s61, 0\n"
      "v_mfma_f32_16x16x32_bf16 v[24:27], v[136:139], a[88:91], v[24:27]\n"
      "v_cvt_pk_bf16_f32 v79, v86, v87\n"
      "v_mul_f32 v219, v205, v219\n"
      "v_mfma_f32_16x16x32_bf16 v[28:31], v[136:139], a[92:95], v[28:31]\n"
      "global_load_dwordx4 v[136:139], v241, s[10:11] offset:2048 sc1\n"
      "v_lshlrev_b32 v222, 16, v223\n"
      "v_mfma_f32_16x16x32_bf16 v[0:3], v[140:143], a[96:99], v[0:3]\n"
      "ds_write_addtid_b32 v78 offset:7680\n"
      "v_cvt_pk_bf16_f32 v80, v88, v89\n"
      "v_mfma_f32_16x16x32_bf16 v[4:7], v[140:143], a[100:103], v[4:7]\n"
      "ds_write_addtid_b32 v79 offset:7936\n"
      "v_cvt_pk_bf16_f32 v81, v90, v91\n"
      "v_mfma_f32_16x16x32_bf16 v[8:11], v[140:143], a[104:107], v[8:11]\n"
      "ds_write_addtid_b32 v80 offset:8192\n"
      "v_cvt_pk_bf16_f32 v82, v92, v93\n"
      "v_mfma_f32_16x16x32_bf16 v[12:15], v[140:143], a[108:111], v[12:15]\n"
      "ds_write_addtid_b32 v81 offset:8448\n"
      "v_cvt_pk_bf16_f32 v83, v94, v95\n"
      "v_mfma_f32_16x16x32_bf16 v[16:19], v[140:143], a[112:115], v[16:19]\n"
      "ds_write_addtid_b32 v82 offset:8704\n"
      "v_and_b32 v223, 0xffff0000, v223\n"
      "v_mfma_f32_16x16x32_bf16 v[20:23], v[140:143], a[116:119], v[20:23]\n"
      "ds_write_addtid_b32 v83 offset:8960\n"
      "v_cvt_pk_bf16_f32 v104, v104, v105\n"
      "v_mfma_f32_16x16x32_bf16 v[24:27], v[140:143], a[120:123], v[24:27]\n"
      "v_cvt_pk_bf16_f32 v105, v106, v107\n"
      "v_mul_f32 v222, v207, v222\n"
      "v_mfma_f32_16x16x32_bf16 v[28:31], v[140:143], a[124:127], v[28:31]\n"
      "s_waitcnt vmcnt(16)\n"
      "global_load_dwordx4 v[140:143], v241, s[72:73] offset:2048 sc1\n"
      "v_mul_f32 v223, v207, v223\n"
      "v_mfma_f32_16x16x32_bf16 v[0:3], v[144:147], a[128:131], v[0:3]\n"
      "ds_write_addtid_b32 v104 offset:9216\n"
      "v_cvt_pk_bf16_f32 v106, v108, v109\n"
      "v_mfma_f32_16x16x32_bf16 v[4:7], v[144:147], a[132:135], v[4:7]\n"
      "ds_write_addtid_b32 v105 offset:9472\n"
      "v_cvt_pk_bf16_f32 v107, v110, v111\n"
      "v_mfma_f32_16x16x32_bf16 v[8:11], v[144:147], a[136:139], v[8:11]\n"
      "global_load_dwordx4 v[224:227], v200, s[58:59] offset:0 sc1\n"
      "v_mfma_f32_16x16x32_bf16 v[12:15], v[144:147], a[140:143], v[12:15]\n"
      "global_load_dwordx4 v[228:231], v200, s[58:59] offset:1024 sc1\n"
      "v_mfma_f32_16x16x32_bf16 v[16:19], v[144:147], a[144:147], v[16:19]\n"
      "global_load_dwordx4 v[232:235], v200, s[58:59] offset:2048 sc1\n"
      "v_mfma_f32_16x16x32_bf16 v[20:23], v[144:147], a[148:151], v[20:23]\n"
      "global_load_dwordx4 v[236:239], v200, s[58:59] offset:3072 sc1\n"
      "v_mfma_f32_16x16x32_bf16 v[24:27], v[144:147], a[152:155], v[24:27]\n"
      "ds_write_addtid_b32 v106 offset:9728\n"
      "v_cvt_pk_bf16_f32 v108, v112, v113\n"
      "v_mfma_f32_16x16x32_bf16 v[28:31], v[144:147], a[156:159], v[28:31]\n"
      "global_load_dwordx4 v[144:147], v240, s[14:15] offset:2048 sc1\n"
      "v_mfma_f32_16x16x32_bf16 v[0:3], v[148:151], a[160:163], v[0:3]\n"
      "ds_write_addtid_b32 v107 offset:9984\n"
      "v_cvt_pk_bf16_f32 v109, v114, v115\n"
      "v_mfma_f32_16x16x32_bf16 v[4:7], v[148:151], a[164:167], v[4:7]\n"
      "ds_write_addtid_b32 v108 offset:10240\n"
      "v_cvt_pk_bf16_f32 v110, v116, v117\n"
      "v_mfma_f32_16x16x32_bf16 v[8:11], v[148:151], a[168:171], v[8:11]\n"
      "ds_write_addtid_b32 v109 offset:10496\n"
      "v_cvt_pk_bf16_f32 v111, v118, v119\n"
      "v_mfma_f32_16x16x32_bf16 v[12:15], v[148:151], a[172:175], v[12:15]\n"
      "ds_write_addtid_b32 v110 offset:10752\n"
      "v_cvt_pk_bf16_f32 v112, v120, v121\n"
      "v_mfma_f32_16x16x32_bf16 v[16:19], v[148:151], a[176:179], v[16:19]\n"
      "ds_write_addtid_b32 v111 offset:11008\n"
      "v_cvt_pk_bf16_f32 v113, v122, v123\n"
      "v_mfma_f32_16x16x32_bf16 v[20:23], v[148:151], a[180:183], v[20:23]\n"
      "ds_write_addtid_b32 v112 offset:11264\n"
      "v_cvt_pk_bf16_f32 v114, v124, v125\n"
      "v_mfma_f32_16x16x32_bf16 v[24:27], v[148:151], a[184:187], v[24:27]\n"
      "ds_write_addtid_b32 v113 offset:11520\n"
      "v_cvt_pk_bf16_f32 v115, v126, v127\n"
      "v_mfma_f32_16x16x32_bf16 v[28:31], v[148:151], a[188:191], v[28:31]\n"
      "s_waitcnt vmcnt(20)\n"
      "global_load_dwordx4 v[148:151], v240, s[74:75] offset:2048 sc1\n"
      "v_mfma_f32_16x16x32_bf16 v[0:3], v[152:155], a[192:195], v[0:3]\n"
      "ds_write_addtid_b32 v114 offset:11776\n"
      "v_mfma_f32_16x16x32_bf16 v[4:7], v[152:155], a[196:199], v[4:7]\n"
      "ds_write_addtid_b32 v115 offset:12032\n"
      "v_mfma_f32_16x16x32_bf16 v[8:11], v[152:155], a[200:203], v[8:11]\n"
      "v_mfma_f32_16x16x32_bf16 v[12:15], v[152:155], a[204:207], v[12:15]\n"
      "v_mfma_f32_16x16x32_bf16 v[16:19], v[152:155], a[208:211], v[16:19]\n"
      "s_waitcnt lgkmcnt(0)\n"
      "s_barrier\n"
      "v_mfma_f32_16x16x32_bf16 v[20:23], v[152:155], a[212:215], v[20:23]\n"
      "ds_read2st64_b32 v[84:85], v244 offset0:24 offset1:25\n"
      "v_mfma_f32_16x16x32_bf16 v[24:27], v[152:155], a[216:219], v[24:27]\n"
      "ds_read2st64_b32 v[86:87], v245 offset0:28 offset1:29\n"
      "v_mfma_f32_16x16x32_bf16 v[28:31], v[152:155], a[220:223], v[28:31]\n"
      "global_load_dwordx4 v[152:155], v241, s[14:15] offset:2048 sc1\n"
      "v_mfma_f32_16x16x32_bf16 v[0:3], v[156:159], a[224:227], v[0:3]\n"
      "ds_read2st64_b32 v[88:89], v244 offset0:26 offset1:27\n"
      "v_mfma_f32_16x16x32_bf16 v[4:7], v[156:159], a[228:231], v[4:7]\n"
      "ds_read2st64_b32 v[90:91], v245 offset0:30 offset1:31\n"
      "v_mfma_f32_16x16x32_bf16 v[8:11], v[156:159], a[232:235], v[8:11]\n"
      "ds_read2st64_b32 v[92:93], v246 offset0:32 offset1:33\n"
      "v_mfma_f32_16x16x32_bf16 v[12:15], v[156:159], a[236:239], v[12:15]\n"
      "ds_read2st64_b32 v[94:95], v246 offset0:34 offset1:35\n"
      "v_mfma_f32_16x16x32_bf16 v[16:19], v[156:159], a[240:243], v[16:19]\n"
      "ds_read2st64_b32 v[116:117], v244 offset0:36 offset1:37\n"
      "v_mfma_f32_16x16x32_bf16 v[20:23], v[156:159], a[244:247], v[20:23]\n"
      "ds_read2st64_b32 v[118:119], v245 offset0:40 offset1:41\n"
      "v_mfma_f32_16x16x32_bf16 v[24:27], v[156:159], a[248:251], v[24:27]\n"
      "ds_read2st64_b32 v[120:121], v244 offset0:38 offset1:39\n"
      "v_mfma_f32_16x16x32_bf16 v[28:31], v[156:159], a[252:255], v[28:31]\n"
      "s_waitcnt vmcnt(20)\n"
      "global_load_dwordx4 v[156:159], v241, s[74:75] offset:2048 sc1\n"
      "s_waitcnt lgkmcnt(3)\n"
      "v_mfma_f32_16x16x32_bf16 v[32:35], v[160:163], a[0:3], 0\n"
      "v_mfma_f32_16x16x32_bf16 v[64:67], v[250:253], v[84:87], v[64:67]\n"
      "v_mfma_f32_16x16x32_bf16 v[36:39], v[160:163], a[4:7], 0\n"
      "v_mfma_f32_16x16x32_bf16 v[68:71], v[250:253], v[88:91], v[68:71]\n"
      "v_mfma_f32_16x16x32_bf16 v[40:43], v[160:163], a[8:11], 0\n"
      "v_mfma_f32_16x16x32_bf16 v[64:67], v[252:255], v[92:95], v[64:67]\n"
      "v_mfma_f32_16x16x32_bf16 v[44:47], v[160:163], a[12:15], 0\n"
      "v_mfma_f32_16x16x32_bf16 v[68:71], v[248:251], v[92:95], v[68:71]\n"
      "v_mov_b32 v75, s37\n"
      "v_mfma_f32_16x16x32_bf16 v[48:51], v[160:163], a[16:19], 0\n"
      "ds_read2st64_b32 v[122:123], v245 offset0:42 offset1:43\n"
      "v_mfma_f32_16x16x32_bf16 v[52:55], v[160:163], a[20:23], 0\n"
      "ds_read2st64_b32 v[124:125], v246 offset0:44 offset1:45\n"
      "s_nop 0\n"
      "v_cvt_pk_bf16_f32 v72, v64, v65\n"
      "v_mfma_f32_16x16x32_bf16 v[56:59], v[160:163], a[24:27], 0\n"
      "ds_read2st64_b32 v[126:127], v246 offset0:46 offset1:47\n"
      "v_cvt_pk_bf16_f32 v73, v66, v67\n"
      "v_mfma_f32_16x16x32_bf16 v[60:63], v[160:163], a[28:31], 0\n"
      "global_load_dwordx4 v[160:163], v240, s[10:11] offset:3072 sc1\n"
      "v_cvt_pk_bf16_f32 v74, v68, v69\n"
      "v_mfma_f32_16x16x32_bf16 v[32:35], v[164:167], a[32:35], v[32:35]\n"
      "v_cvt_pk_bf16_f32 v247, v70, v71\n"
      "v_cvt_pk_bf16_f32 v8, v8, v9\n"
      "v_mfma_f32_16x16x32_bf16 v[36:39], v[164:167], a[36:39], v[36:39]\n"
      "global_store_dwordx4 v242, v[72:75], s[52:53] offset:0 sc1\n"
      "v_cvt_pk_bf16_f32 v9, v10, v11\n"
      "v_mfma_f32_16x16x32_bf16 v[40:43], v[164:167], a[40:43], v[40:43]\n"
      "s_waitcnt lgkmcnt(0)\n"
      "v_mfma_f32_16x16x32_bf16 v[96:99], v[250:253], v[116:119], v[96:99]\n"
      "v_mfma_f32_16x16x32_bf16 v[44:47], v[164:167], a[44:47], v[44:47]\n"
      "v_mfma_f32_16x16x32_bf16 v[100:103], v[250:253], v[120:123], v[100:103]\n"
      "v_mfma_f32_16x16x32_bf16 v[48:51], v[164:167], a[48:51], v[48:51]\n"
      "v_mfma_f32_16x16x32_bf16 v[96:99], v[252:255], v[124:127], v[96:99]\n"
      "v_mfma_f32_16x16x32_bf16 v[52:55], v[164:167], a[52:55], v[52:55]\n"
      "s_waitcnt vmcnt(6)\n"
      "v_bitop3_b32 v192, v225, v227, s37 bitop3:0x7e\n"
      "v_bitop3_b32 v192, v192, v229, s37 bitop3:0xf6\n"
      "v_bitop3_b32 v192, v192, v231, s37 bitop3:0xf6\n"
      "v_bitop3_b32 v192, v192, v233, s37 bitop3:0xf6\n"
      "v_bitop3_b32 v192, v192, v235, s37 bitop3:0xf6\n"
      "v_bitop3_b32 v192, v192, v237, s37 bitop3:0xf6\n"
      "v_bitop3_b32 v192, v192, v239, s37 bitop3:0xf6\n"
      "v_cmp_ne_u32 vcc, 0, v192\n"
      "s_nop 1\n"
      "s_cbranch_vccnz .Lp_s14_%=\n"
      ".Lp_o15_%=:\n"
      "v_mfma_f32_16x16x32_bf16 v[56:59], v[164:167], a[56:59], v[56:59]\n"
      "v_mfma_f32_16x16x32_bf16 v[100:103], v[248:251], v[124:127], v[100:103]\n"
      "v_add_f32 v192, v224, v228\n"
      "v_mfma_f32_16x16x32_bf16 v[60:63], v[164:167], a[60:63], v[60:63]\n"
      "global_load_dwordx4 v[164:167], v240, s[72:73] offset:3072 sc1\n"
      "v_add_f32 v193, v226, v230\n"
      "v_mfma_f32_16x16x32_bf16 v[32:35], v[168:171], a[64:67], v[32:35]\n"
      "ds_write_addtid_b32 v8 offset:0\n"
      "v_add_f32 v194, v232, v236\n"
      "v_mfma_f32_16x16x32_bf16 v[36:39], v[168:171], a[68:71], v[36:39]\n"
      "v_add_f32 v195, v234, v238\n"
      "v_mov_b32 v107, s37\n"
      "v_mfma_f32_16x16x32_bf16 v[40:43], v[168:171], a[72:75], v[40:43]\n"
      "ds_read_b64_tr_b16 v[224:225], v201 offset:0\n"
      "v_permlane32_swap_b32 v192, v193\n"
      "v_mfma_f32_16x16x32_bf16 v[44:47], v[168:171], a[76:79], v[44:47]\n"
      "ds_read_b64_tr_b16 v[226:227], v201 offset:512\n"
      "v_permlane32_swap_b32 v194, v195\n"
      "v_mfma_f32_16x16x32_bf16 v[48:51], v[168:171], a[80:83], v[48:51]\n"
      "ds_read_b64_tr_b16 v[228:229], v201 offset:32\n"
      "v_add_f32 v192, v192, v193\n"
      "v_mfma_f32_16x16x32_bf16 v[52:55], v[168:171], a[84:87], v[52:55]\n"
      "ds_read_b64_tr_b16 v[230:231], v201 offset:544\n"
      "v_add_f32 v194, v194, v195\n"
      "v_mfma_f32_16x16x32_bf16 v[56:59], v[168:171], a[88:91], v[56:59]\n"
      "ds_read_b64_tr_b16 v[232:233], v201 offset:64\n"
      "v_mov_b32 v193, v192\n"
      "v_mfma_f32_16x16x32_bf16 v[60:63], v[168:171], a[92:95], v[60:63]\n"
      "global_load_dwordx4 v[168:171], v241, s[10:11] offset:3072 sc1\n"
      "v_mov_b32 v108, v247\n"
      "v_mfma_f32_16x16x32_bf16 v[32:35], v[172:175], a[96:99], v[32:35]\n"
      "ds_read_b64_tr_b16 v[234:235], v201 offset:576\n"
      "v_mov_b32 v195, v194\n"
      "v_mfma_f32_16x16x32_bf16 v[36:39], v[172:175], a[100:103], v[36:39]\n"
      "ds_read_b64_tr_b16 v[236:237], v201 offset:96\n"
      "v_permlane16_swap_b32 v192, v193\n"
      "v_mfma_f32_16x16x32_bf16 v[40:43], v[172:175], a[104:107], v[40:43]\n"
      "ds_read_b64_tr_b16 v[238:239], v201 offset:608\n"
      "v_permlane16_swap_b32 v194, v195\n"
      "v_mfma_f32_16x16x32_bf16 v[44:47], v[172:175], a[108:111], v[44:47]\n"
      "v_add_f32 v192, v192, v193\n"
      "v_add_f32 v194, v194, v195\n"
      "v_mfma_f32_16x16x32_bf16 v[48:51], v[172:175], a[112:115], v[48:51]\n"
      "v_cvt_pk_bf16_f32 v192, v192, v194\n"
      "v_and_b32 v194, 0xffff0000, v192\n"
      "v_mfma_f32_16x16x32_bf16 v[52:55], v[172:175], a[116:119], v[52:55]\n"
      "v_lshlrev_b32 v192, 16, v192\n"
      "v_mul_f32 v193, 0xbfb8aa3b, v192\n"
      "v_mfma_f32_16x16x32_bf16 v[56:59], v[172:175], a[120:123], v[56:59]\n"
      "v_exp_f32 v193, v193\n"
      "s_nop 0\n"
      "v_add_f32 v193, 1.0, v193\n"
      "v_mfma_f32_16x16x32_bf16 v[60:63], v[172:175], a[124:127], v[60:63]\n"
      "global_load_dwordx4 v[172:175], v241, s[72:73] offset:3072 sc1\n"
      "v_rcp_f32 v193, v193\n"
      "v_mfma_f32_16x16x32_bf16 v[32:35], v[176:179], a[128:131], v[32:35]\n"
      "v_mul_f32 v192, v192, v193\n"
      "v_cvt_pk_bf16_f32 v192, v192, v192\n"
      "v_mfma_f32_16x16x32_bf16 v[36:39], v[176:179], a[132:135], v[36:39]\n"
      "v_and_b32 v192, 0xffff0000, v192\n"
      "v_mul_f32 v192, v192, v194\n"
      "v_mfma_f32_16x16x32_bf16 v[40:43], v[176:179], a[136:139], v[40:43]\n"
      "v_cvt_pk_bf16_f32 v192, v192, v192\n"
      "v_mov_b32 v111, s37\n"
      "v_mfma_f32_16x16x32_bf16 v[44:47], v[176:179], a[140:143], v[44:47]\n"
      "ds_write_b16 v202, v192\n"
      "v_cvt_pk_bf16_f32 v10, v12, v13\n"
      "v_mfma_f32_16x16x32_bf16 v[48:51], v[176:179], a[144:147], v[48:51]\n"
      "ds_read_b128 v[192:195], v199\n"
      "v_cvt_pk_bf16_f32 v104, v96, v97\n"
      "v_mfma_f32_16x16x32_bf16 v[52:55], v[176:179], a[148:151], v[52:55]\n"
      "ds_write_addtid_b32 v9 offset:256\n"
      "v_cvt_pk_bf16_f32 v105, v98, v99\n"
      "v_mfma_f32_16x16x32_bf16 v[56:59], v[176:179], a[152:155], v[56:59]\n"
      "v_cvt_pk_bf16_f32 v106, v100, v101\n"
      "v_cvt_pk_bf16_f32 v109, v102, v103\n"
      "v_mfma_f32_16x16x32_bf16 v[60:63], v[176:179], a[156:159], v[60:63]\n"
      "global_load_dwordx4 v[176:179], v240, s[14:15] offset:3072 sc1\n"
      "v_cvt_pk_bf16_f32 v11, v14, v15\n"
      "v_mfma_f32_16x16x32_bf16 v[32:35], v[180:183], a[160:163], v[32:35]\n"
      "global_store_dwordx4 v242, v[104:107], s[52:53] offset:1024 sc1\n"
      "v_mfma_f32_16x16x32_bf16 v[36:39], v[180:183], a[164:167], v[36:39]\n"
      "global_store_dwordx4 v242, v[108:111], s[52:53] offset:2048 sc1\n"
      "s_add_u32 s48, s34, 3\n"
      "v_mfma_f32_16x16x32_bf16 v[40:43], v[180:183], a[168:171], v[40:43]\n"
      "s_and_b32 s48, s48, 3\n"
      "s_waitcnt lgkmcnt(1)\n"
      "v_mfma_f32_16x16x32_bf16 v[196:199], v[192:195], v[224:227], 0\n"
      "v_mfma_f32_16x16x32_bf16 v[44:47], v[180:183], a[172:175], v[44:47]\n"
      "s_mul_i32 s48, s48, 36864\n"
      "v_mfma_f32_16x16x32_bf16 v[224:227], v[192:195], v[228:231], 0\n"
      "s_add_u32 s48, s48, 24576\n"
      "v_mfma_f32_16x16x32_bf16 v[48:51], v[180:183], a[176:179], v[48:51]\n"
      "s_add_u32 s52, s60, s48\n"
      "s_addc_u32 s53, s61, 0\n"
      "v_mfma_f32_16x16x32_bf16 v[228:231], v[192:195], v[232:235], 0\n"
      "v_mfma_f32_16x16x32_bf16 v[52:55], v[180:183], a[180:183], v[52:55]\n"
      "v_mfma_f32_16x16x32_bf16 v[232:235], v[192:195], v[236:239], 0\n"
      "ds_write_addtid_b32 v10 offset:512\n"
      "v_mov_b32 v193, s37\n"
      "v_mfma_f32_16x16x32_bf16 v[56:59], v[180:183], a[184:187], v[56:59]\n"
      "v_mov_b32 v195, s37\n"
      "v_cvt_pk_bf16_f32 v12, v16, v17\n"
      "v_mfma_f32_16x16x32_bf16 v[60:63], v[180:183], a[188:191], v[60:63]\n"
      "global_load_dwordx4 v[180:183], v240, s[74:75] offset:3072 sc1\n"
      "ds_write_addtid_b32 v11 offset:768\n"
      "v_cvt_pk_bf16_f32 v13, v18, v19\n"
      "v_mfma_f32_16x16x32_bf16 v[40:43], v[184:187], a[200:203], v[40:43]\n"
      "ds_write_addtid_b32 v12 offset:1024\n"
      "v_cndmask_b32 v192, v196, v228, s[64:65]\n"
      "v_cndmask_b32 v194, v224, v232, s[64:65]\n"
      "v_mfma_f32_16x16x32_bf16 v[44:47], v[184:187], a[204:207], v[44:47]\n"
      "v_and_b32 v192, s86, v192\n"
      "v_and_b32 v194, s86, v194\n"
      "v_cvt_pk_bf16_f32 v14, v20, v21\n"
      "v_mfma_f32_16x16x32_bf16 v[48:51], v[184:187], a[208:211], v[48:51]\n"
      "s_mov_b64 exec, s[66:67]\n"
      "global_store_dwordx4 v203, v[192:195], s[58:59] sc1\n"
      "s_mov_b64 exec, -1\n"
      "v_mfma_f32_16x16x32_bf16 v[52:55], v[184:187], a[212:215], v[52:55]\n"
      "ds_write_addtid_b32 v13 offset:1280\n"
      "v_cvt_pk_bf16_f32 v15, v22, v23\n"
      "v_mfma_f32_16x16x32_bf16 v[56:59], v[184:187], a[216:219], v[56:59]\n"
      "ds_write_addtid_b32 v14 offset:1536\n"
      "v_cvt_pk_bf16_f32 v16, v24, v25\n"
      "v_mfma_f32_16x16x32_bf16 v[60:63], v[184:187], a[220:223], v[60:63]\n"
      "ds_write_addtid_b32 v15 offset:1792\n"
      "v_cvt_pk_bf16_f32 v17, v26, v27\n"
      "v_mfma_f32_16x16x32_bf16 v[40:43], v[188:191], a[232:235], v[40:43]\n"
      "ds_write_addtid_b32 v16 offset:2048\n"
      "v_cvt_pk_bf16_f32 v18, v28, v29\n"
      "v_mfma_f32_16x16x32_bf16 v[44:47], v[188:191], a[236:239], v[44:47]\n"
      "ds_write_addtid_b32 v17 offset:2304\n"
      "v_cvt_pk_bf16_f32 v19, v30, v31\n"
      "v_mfma_f32_16x16x32_bf16 v[48:51], v[188:191], a[240:243], v[48:51]\n"
      "ds_write_addtid_b32 v18 offset:2560\n"
      "ds_write_addtid_b32 v19 offset:2816\n"
      "v_mfma_f32_16x16x32_bf16 v[52:55], v[188:191], a[244:247], v[52:55]\n"
      "v_mfma_f32_16x16x32_bf16 v[56:59], v[188:191], a[248:251], v[56:59]\n"
      "v_mfma_f32_16x16x32_bf16 v[60:63], v[188:191], a[252:255], v[60:63]\n"
      "s_waitcnt lgkmcnt(0)\n"
      "s_barrier\n"
      "v_mfma_f32_16x16x32_bf16 v[32:35], v[184:187], a[192:195], v[32:35]\n"
      "ds_read2st64_b32 v[20:21], v244 offset0:0 offset1:1\n"
      "ds_read2st64_b32 v[22:23], v245 offset0:4 offset1:5\n"
      "v_mfma_f32_16x16x32_bf16 v[36:39], v[184:187], a[196:199], v[36:39]\n"
      "global_load_dwordx4 v[184:187], v241, s[14:15] offset:3072 sc1\n"
      "ds_read2st64_b32 v[24:25], v244 offset0:2 offset1:3\n"
      "v_mfma_f32_16x16x32_bf16 v[32:35], v[188:191], a[224:227], v[32:35]\n"
      "ds_read2st64_b32 v[26:27], v245 offset0:6 offset1:7\n"
      "ds_read2st64_b32 v[28:29], v246 offset0:8 offset1:9\n"
      "v_mfma_f32_16x16x32_bf16 v[36:39], v[188:191], a[228:231], v[36:39]\n"
      "ds_read2st64_b32 v[30:31], v246 offset0:10 offset1:11\n"
      "v_cvt_pk_bf16_f32 v40, v40, v41\n"
      "v_cvt_pk_bf16_f32 v41, v42, v43\n"
      "v_mfma_f32_16x16x32_bf16 v[72:75], v[128:131], a[8:11], 0\n"
      "ds_write_addtid_b32 v40 offset:3072\n"
      "v_cvt_pk_bf16_f32 v42, v44, v45\n"
      "v_mfma_f32_16x16x32_bf16 v[76:79], v[128:131], a[12:15], 0\n"
      "global_load_dwordx4 v[188:191], v241, s[74:75] offset:3072 sc1\n"
      "ds_write_addtid_b32 v41 offset:3328\n"
      "v_cvt_pk_bf16_f32 v43, v46, v47\n"
      "v_mfma_f32_16x16x32_bf16 v[80:83], v[128:131], a[16:19], 0\n"
      "ds_write_addtid_b32 v42 offset:3584\n"
      "v_cvt_pk_bf16_f32 v44, v48, v49\n"
      "v_mfma_f32_16x16x32_bf16 v[84:87], v[128:131], a[20:23], 0\n"
      "s_waitcnt lgkmcnt(3)\n"
      "v_mfma_f32_16x16x32_bf16 v[0:3], v[250:253], v[20:23], v[0:3]\n"
      "v_mfma_f32_16x16x32_bf16 v[4:7], v[250:253], v[24:27], v[4:7]\n"
      "v_mfma_f32_16x16x32_bf16 v[88:91], v[128:131], a[24:27], 0\n"
      "v_mfma_f32_16x16x32_bf16 v[0:3], v[252:255], v[28:31], v[0:3]\n"
      "v_mfma_f32_16x16x32_bf16 v[4:7], v[248:251], v[28:31], v[4:7]\n"
      "v_mov_b32 v11, s37\n"
      "v_mfma_f32_16x16x32_bf16 v[92:95], v[128:131], a[28:31], 0\n"
      "ds_write_addtid_b32 v43 offset:3840\n"
      "v_cvt_pk_bf16_f32 v45, v50, v51\n"
      "v_mfma_f32_16x16x32_bf16 v[72:75], v[132:135], a[40:43], v[72:75]\n"
      "ds_write_addtid_b32 v44 offset:4096\n"
      "s_nop 0\n"
      "v_cvt_pk_bf16_f32 v8, v0, v1\n"
      "v_mfma_f32_16x16x32_bf16 v[76:79], v[132:135], a[44:47], v[76:79]\n"
      "v_cvt_pk_bf16_f32 v9, v2, v3\n"
      "v_cvt_pk_bf16_f32 v10, v4, v5\n"
      "v_cvt_pk_bf16_f32 v247, v6, v7\n"
      "v_mfma_f32_16x16x32_bf16 v[80:83], v[132:135], a[48:51], v[80:83]\n"
      "global_store_dwordx4 v242, v[8:11], s[52:53] offset:0 sc1\n"
      "v_cvt_pk_bf16_f32 v46, v52, v53\n"
      "s_lshl2_add_u32 s54, s36, s33\n"
      "v_mfma_f32_16x16x32_bf16 v[84:87], v[132:135], a[52:55], v[84:87]\n"
      "s_lshl_b32 s55, s54, 15\n"
      "ds_write_addtid_b32 v45 offset:4352\n"
      "v_cvt_pk_bf16_f32 v47, v54, v55\n"
      "s_lshl2_add_u32 s56, s35, s34\n"
      "v_mfma_f32_16x16x32_bf16 v[88:91], v[132:135], a[56:59], v[88:91]\n"
      "s_lshl_b32 s56, s56, 6\n"
      "ds_write_addtid_b32 v46 offset:4608\n"
      "v_cvt_pk_bf16_f32 v48, v56, v57\n"
      "s_add_u32 s55, s55, s56\n"
      "v_mfma_f32_16x16x32_bf16 v[92:95], v[132:135], a[60:63], v[92:95]\n"
      "ds_write_addtid_b32 v47 offset:4864\n"
      "v_cvt_pk_bf16_f32 v49, v58, v59\n"
      "v_and_b32 v8, 15, v243\n"
      "v_mfma_f32_16x16x32_bf16 v[72:75], v[136:139], a[72:75], v[72:75]\n"
      "ds_write_addtid_b32 v48 offset:5120\n"
      "v_cvt_pk_bf16_f32 v50, v60, v61\n"
      "v_and_b32 v7, 48, v243\n"
      "v_mfma_f32_16x16x32_bf16 v[76:79], v[136:139], a[76:79], v[76:79]\n"
      "ds_write_addtid_b32 v49 offset:5376\n"
      "v_cvt_pk_bf16_f32 v51, v62, v63\n"
      "v_lshl_or_b32 v7, v8, 10, v7\n"
      "v_mfma_f32_16x16x32_bf16 v[80:83], v[136:139], a[80:83], v[80:83]\n"
      "ds_write_addtid_b32 v50 offset:5632\n"
      "ds_write_addtid_b32 v51 offset:5888\n"
      "v_add_u32 v7, s55, v7\n"
      "v_mfma_f32_16x16x32_bf16 v[84:87], v[136:139], a[84:87], v[84:87]\n"
      "v_mfma_f32_16x16x32_bf16 v[88:91], v[136:139], a[88:91], v[88:91]\n"
      "v_add_u32 v4, 0, v242\n"
      "v_mfma_f32_16x16x32_bf16 v[92:95], v[136:139], a[92:95], v[92:95]\n"
      "global_load_dwordx4 v[12:15], v4, s[62:63] offset:0 sc1\n"
      "global_load_dwordx4 v[16:19], v4, s[62:63] offset:1024 sc1\n"
      "v_mfma_f32_16x16x32_bf16 v[72:75], v[140:143], a[104:107], v[72:75]\n"
      "global_load_dwordx4 v[20:23], v4, s[62:63] offset:2048 sc1\n"
      "s_waitcnt lgkmcnt(0)\n"
      "s_barrier\n"
      "v_mfma_f32_16x16x32_bf16 v[76:79], v[140:143], a[108:111], v[76:79]\n"
      "ds_read2st64_b32 v[52:53], v244 offset0:12 offset1:13\n"
      "ds_read2st64_b32 v[54:55], v245 offset0:16 offset1:17\n"
      "v_mfma_f32_16x16x32_bf16 v[80:83], v[140:143], a[112:115], v[80:83]\n"
      "ds_read2st64_b32 v[56:57], v244 offset0:14 offset1:15\n"
      "ds_read2st64_b32 v[58:59], v245 offset0:18 offset1:19\n"
      "v_mfma_f32_16x16x32_bf16 v[84:87], v[140:143], a[116:119], v[84:87]\n"
      "ds_read2st64_b32 v[60:61], v246 offset0:20 offset1:21\n"
      "ds_read2st64_b32 v[62:63], v246 offset0:22 offset1:23\n"
      "v_mfma_f32_16x16x32_bf16 v[88:91], v[140:143], a[120:123], v[88:91]\n"
      "v_mfma_f32_16x16x32_bf16 v[92:95], v[140:143], a[124:127], v[92:95]\n"
      "s_waitcnt vmcnt(18)\n"
      "v_mfma_f32_16x16x32_bf16 v[72:75], v[144:147], a[136:139], v[72:75]\n"
      "v_mfma_f32_16x16x32_bf16 v[76:79], v[144:147], a[140:143], v[76:79]\n"
      "s_waitcnt lgkmcnt(0)\n"
      "v_mfma_f32_16x16x32_bf16 v[32:35], v[250:253], v[52:55], v[32:35]\n"
      "v_mfma_f32_16x16x32_bf16 v[36:39], v[250:253], v[56:59], v[36:39]\n"
      "v_mfma_f32_16x16x32_bf16 v[80:83], v[144:147], a[144:147], v[80:83]\n"
      "v_mfma_f32_16x16x32_bf16 v[32:35], v[252:255], v[60:63], v[32:35]\n"
      "v_mfma_f32_16x16x32_bf16 v[84:87], v[144:147], a[148:151], v[84:87]\n"
      "v_mfma_f32_16x16x32_bf16 v[36:39], v[248:251], v[60:63], v[36:39]\n"
      "v_mov_b32 v43, s37\n"
      "v_mfma_f32_16x16x32_bf16 v[88:91], v[144:147], a[152:155], v[88:91]\n"
      "v_mov_b32 v44, v247\n"
      "v_mov_b32 v47, s37\n"
      "v_mfma_f32_16x16x32_bf16 v[92:95], v[144:147], a[156:159], v[92:95]\n"
      "v_mfma_f32_16x16x32_bf16 v[72:75], v[148:151], a[168:171], v[72:75]\n"
      "v_cvt_pk_bf16_f32 v40, v32, v33\n"
      "v_cvt_pk_bf16_f32 v41, v34, v35\n"
      "v_mfma_f32_16x16x32_bf16 v[76:79], v[148:151], a[172:175], v[76:79]\n"
      "v_cvt_pk_bf16_f32 v42, v36, v37\n"
      "v_cvt_pk_bf16_f32 v45, v38, v39\n"
      "v_mfma_f32_16x16x32_bf16 v[80:83], v[148:151], a[176:179], v[80:83]\n"
      "global_store_dwordx4 v242, v[40:43], s[52:53] offset:1024 sc1\n"
      "v_mfma_f32_16x16x32_bf16 v[84:87], v[148:151], a[180:183], v[84:87]\n"
      "global_store_dwordx4 v242, v[44:47], s[52:53] offset:2048 sc1\n"
      "v_mfma_f32_16x16x32_bf16 v[88:91], v[148:151], a[184:187], v[88:91]\n"
      "v_mfma_f32_16x16x32_bf16 v[92:95], v[148:151], a[188:191], v[92:95]\n"
      "s_waitcnt vmcnt(18)\n"
      "v_mfma_f32_16x16x32_bf16 v[72:75], v[152:155], a[200:203], v[72:75]\n"
      "v_mfma_f32_16x16x32_bf16 v[76:79], v[152:155], a[204:207], v[76:79]\n"
      "v_mfma_f32_16x16x32_bf16 v[80:83], v[152:155], a[208:211], v[80:83]\n"
      "v_mfma_f32_16x16x32_bf16 v[84:87], v[152:155], a[212:215], v[84:87]\n"
      "v_mfma_f32_16x16x32_bf16 v[88:91], v[152:155], a[216:219], v[88:91]\n"
      "v_mfma_f32_16x16x32_bf16 v[92:95], v[152:155], a[220:223], v[92:95]\n"
      "v_mfma_f32_16x16x32_bf16 v[72:75], v[156:159], a[232:235], v[72:75]\n"
      "v_mfma_f32_16x16x32_bf16 v[76:79], v[156:159], a[236:239], v[76:79]\n"
      "v_mfma_f32_16x16x32_bf16 v[80:83], v[156:159], a[240:243], v[80:83]\n"
      "v_mfma_f32_16x16x32_bf16 v[84:87], v[156:159], a[244:247], v[84:87]\n"
      "v_mfma_f32_16x16x32_bf16 v[88:91], v[156:159], a[248:251], v[88:91]\n"
      "s_nop 3\n"
      "v_cvt_pk_bf16_f32 v72, v72, v73\n"
      "v_mfma_f32_16x16x32_bf16 v[92:95], v[156:159], a[252:255], v[92:95]\n"
      "s_waitcnt vmcnt(15)\n"
      "v_cvt_pk_bf16_f32 v73, v74, v75\n"
      "v_mfma_f32_16x16x32_bf16 v[104:107], v[160:163], a[8:11], 0\n"
      "ds_write_addtid_b32 v72 offset:6144\n"
      "v_cvt_pk_bf16_f32 v74, v76, v77\n"
      "v_mfma_f32_16x16x32_bf16 v[108:111], v[160:163], a[12:15], 0\n"
      "ds_write_addtid_b32 v73 offset:6400\n"
      "v_cvt_pk_bf16_f32 v75, v78, v79\n"
      "v_mfma_f32_16x16x32_bf16 v[112:115], v[160:163], a[16:19], 0\n"
      "ds_write_addtid_b32 v74 offset:6656\n"
      "v_cvt_pk_bf16_f32 v76, v80, v81\n"
      "v_mfma_f32_16x16x32_bf16 v[116:119], v[160:163], a[20:23], 0\n"
      "ds_write_addtid_b32 v75 offset:6912\n"
      "v_cvt_pk_bf16_f32 v77, v82, v83\n"
      "v_mfma_f32_16x16x32_bf16 v[120:123], v[160:163], a[24:27], 0\n"
      "ds_write_addtid_b32 v76 offset:7168\n"
      "v_cvt_pk_bf16_f32 v78, v84, v85\n"
      "v_mfma_f32_16x16x32_bf16 v[124:127], v[160:163], a[28:31], 0\n"
      "ds_write_addtid_b32 v77 offset:7424\n"
      "v_cvt_pk_bf16_f32 v79, v86, v87\n"
      "v_mfma_f32_16x16x32_bf16 v[104:107], v[164:167], a[40:43], v[104:107]\n"
      "ds_write_addtid_b32 v78 offset:7680\n"
      "v_cvt_pk_bf16_f32 v80, v88, v89\n"
      "v_mfma_f32_16x16x32_bf16 v[108:111], v[164:167], a[44:47], v[108:111]\n"
      "ds_write_addtid_b32 v79 offset:7936\n"
      "v_cvt_pk_bf16_f32 v81, v90, v91\n"
      "v_mfma_f32_16x16x32_bf16 v[112:115], v[164:167], a[48:51], v[112:115]\n"
      "ds_write_addtid_b32 v80 offset:8192\n"
      "v_cvt_pk_bf16_f32 v82, v92, v93\n"
      "v_mfma_f32_16x16x32_bf16 v[116:119], v[164:167], a[52:55], v[116:119]\n"
      "ds_write_addtid_b32 v81 offset:8448\n"
      "v_cvt_pk_bf16_f32 v83, v94, v95\n"
      "v_mfma_f32_16x16x32_bf16 v[120:123], v[164:167], a[56:59], v[120:123]\n"
      "ds_write_addtid_b32 v82 offset:8704\n"
      "v_mfma_f32_16x16x32_bf16 v[124:127], v[164:167], a[60:63], v[124:127]\n"
      "s_waitcnt vmcnt(13)\n"
      "ds_write_addtid_b32 v83 offset:8960\n"
      "v_mfma_f32_16x16x32_bf16 v[104:107], v[168:171], a[72:75], v[104:107]\n"
      "v_mfma_f32_16x16x32_bf16 v[108:111], v[168:171], a[76:79], v[108:111]\n"
      "v_mfma_f32_16x16x32_bf16 v[112:115], v[168:171], a[80:83], v[112:115]\n"
      "v_mfma_f32_16x16x32_bf16 v[116:119], v[168:171], a[84:87], v[116:119]\n"
      "v_mfma_f32_16x16x32_bf16 v[120:123], v[168:171], a[88:91], v[120:123]\n"
      "v_mfma_f32_16x16x32_bf16 v[124:127], v[168:171], a[92:95], v[124:127]\n"
      "v_mfma_f32_16x16x32_bf16 v[104:107], v[172:175], a[104:107], v[104:107]\n"
      "s_lshr_b32 s43, s42, 7\n"
      "s_cmp_eq_u32 s43, s36\n"
      "s_cbranch_scc0 .Lp_e16_%=\n"
      "s_lshl2_add_u32 s44, s35, s34\n"
      "s_lshl2_add_u32 s43, s44, s33\n"
      "s_lshl_b32 s43, s43, 12\n"
      "s_add_u32 s43, s43, 524288\n"
      "v_add_u32 v60, s43, v242\n"
      "v_mov_b32 v62, 0\n"
      "global_load_dwordx4 v[24:27], v60, s[58:59] offset:0 sc1\n"
      "global_load_dwordx4 v[28:31], v60, s[58:59] offset:1024 sc1\n"
      "global_load_dwordx4 v[32:35], v60, s[58:59] offset:2048 sc1\n"
      "global_load_dwordx4 v[36:39], v60, s[58:59] offset:3072 sc1\n"
      "global_load_dwordx4 v[40:43], v62, s[58:59] offset:1024 sc1\n"
      "v_and_b32 v58, 15, v243\n"
      "v_lshrrev_b32 v59, 5, v243\n"
      "v_lshl_or_b32 v58, v59, 4, v58\n"
      "s_lshl4_add_u32 s44, s42, s44\n"
      "s_lshl_b32 s44, s44, 6\n"
      "v_lshl_add_u32 v59, v58, 1, s44\n"
      "global_load_ushort v57, v59, s[16:17]\n"
      "s_mov_b32 s44, 131072\n"
      "v_lshl_add_u32 v58, v58, 2, s44\n"
      "s_lshl_b32 s43, s33, 13\n"
      "v_add_u32 v59, s43, v58\n"
      ".Lp_e16_%=:\n"
      "v_mfma_f32_16x16x32_bf16 v[108:111], v[172:175], a[108:111], v[108:111]\n"
      "v_mfma_f32_16x16x32_bf16 v[112:115], v[172:175], a[112:115], v[112:115]\n"
      "v_mfma_f32_16x16x32_bf16 v[116:119], v[172:175], a[116:119], v[116:119]\n"
      "v_mfma_f32_16x16x32_bf16 v[120:123], v[172:175], a[120:123], v[120:123]\n"
      "v_mfma_f32_16x16x32_bf16 v[124:127], v[172:175], a[124:127], v[124:127]\n"
      "s_waitcnt vmcnt(9)\n"
      "v_mfma_f32_16x16x32_bf16 v[104:107], v[176:179], a[136:139], v[104:107]\n"
      "v_mfma_f32_16x16x32_bf16 v[108:111], v[176:179], a[140:143], v[108:111]\n"
      "v_mfma_f32_16x16x32_bf16 v[112:115], v[176:179], a[144:147], v[112:115]\n"
      "v_mfma_f32_16x16x32_bf16 v[116:119], v[176:179], a[148:151], v[116:119]\n"
      "v_mfma_f32_16x16x32_bf16 v[120:123], v[176:179], a[152:155], v[120:123]\n"
      "v_mfma_f32_16x16x32_bf16 v[124:127], v[176:179], a[156:159], v[124:127]\n"
      "v_mfma_f32_16x16x32_bf16 v[104:107], v[180:183], a[168:171], v[104:107]\n"
      "v_mfma_f32_16x16x32_bf16 v[108:111], v[180:183], a[172:175], v[108:111]\n"
      "v_mfma_f32_16x16x32_bf16 v[112:115], v[180:183], a[176:179], v[112:115]\n"
      "v_mfma_f32_16x16x32_bf16 v[116:119], v[180:183], a[180:183], v[116:119]\n"
      "v_add_u32 v5, 12288, v242\n"
      "v_mfma_f32_16x16x32_bf16 v[120:123], v[180:183], a[184:187], v[120:123]\n"
      "global_load_dwordx4 v[44:47], v5, s[62:63] offset:0 sc1\n"
      "v_mfma_f32_16x16x32_bf16 v[124:127], v[180:183], a[188:191], v[124:127]\n"
      "s_waitcnt vmcnt(7)\n"
      "global_load_dwordx4 v[48:51], v5, s[62:63] offset:1024 sc1\n"
      "v_mfma_f32_16x16x32_bf16 v[104:107], v[184:187], a[200:203], v[104:107]\n"
      "global_load_dwordx4 v[52:55], v5, s[62:63] offset:2048 sc1\n"
      "v_mfma_f32_16x16x32_bf16 v[108:111], v[184:187], a[204:207], v[108:111]\n"
      "v_mfma_f32_16x16x32_bf16 v[112:115], v[184:187], a[208:211], v[112:115]\n"
      "v_mfma_f32_16x16x32_bf16 v[116:119], v[184:187], a[212:215], v[116:119]\n"
      "v_mfma_f32_16x16x32_bf16 v[120:123], v[184:187], a[216:219], v[120:123]\n"
      "v_mfma_f32_16x16x32_bf16 v[124:127], v[184:187], a[220:223], v[124:127]\n"
      "v_mfma_f32_16x16x32_bf16 v[104:107], v[188:191], a[232:235], v[104:107]\n"
      "v_mfma_f32_16x16x32_bf16 v[108:111], v[188:191], a[236:239], v[108:111]\n"
      "v_mfma_f32_16x16x32_bf16 v[112:115], v[188:191], a[240:243], v[112:115]\n"
      "v_mfma_f32_16x16x32_bf16 v[116:119], v[188:191], a[244:247], v[116:119]\n"
      "s_waitcnt vmcnt(5)\n"
      "v_bitop3_b32 v22, v15, v19, s37 bitop3:0x7e\n"
      "v_bitop3_b32 v22, v22, v23, s37 bitop3:0xf6\n"
      "v_cmp_ne_u32 vcc, 0, v22\n"
      "s_nop 1\n"
      "s_cbranch_vccnz .Lp_s18_%=\n"
      ".Lp_o19_%=:\n"
      "v_mfma_f32_16x16x32_bf16 v[120:123], v[188:191], a[248:251], v[120:123]\n"
      "v_cvt_pk_bf16_f32 v104, v104, v105\n"
      "v_cvt_pk_bf16_f32 v105, v106, v107\n"
      "v_mfma_f32_16x16x32_bf16 v[124:127], v[188:191], a[252:255], v[124:127]\n"
      "ds_write_addtid_b32 v104 offset:9216\n"
      "v_cvt_pk_bf16_f32 v106, v108, v109\n"
      "v_mfma_f32_16x16x32_bf16 v[64:67], v[128:131], a[0:3], 0\n"
      "ds_write_addtid_b32 v105 offset:9472\n"
      "v_cvt_pk_bf16_f32 v107, v110, v111\n"
      "v_mfma_f32_16x16x32_bf16 v[68:71], v[128:131], a[4:7], 0\n"
      "ds_write_addtid_b32 v106 offset:9728\n"
      "v_cvt_pk_bf16_f32 v108, v112, v113\n"
      "v_mfma_f32_16x16x32_bf16 v[64:67], v[132:135], a[32:35], v[64:67]\n"
      "ds_write_addtid_b32 v107 offset:9984\n"
      "v_cvt_pk_bf16_f32 v109, v114, v115\n"
      "v_mfma_f32_16x16x32_bf16 v[68:71], v[132:135], a[36:39], v[68:71]\n"
      "ds_write_addtid_b32 v108 offset:10240\n"
      "v_cvt_pk_bf16_f32 v110, v116, v117\n"
      "v_mfma_f32_16x16x32_bf16 v[64:67], v[136:139], a[64:67], v[64:67]\n"
      "ds_write_addtid_b32 v109 offset:10496\n"
      "v_cvt_pk_bf16_f32 v111, v118, v119\n"
      "v_mfma_f32_16x16x32_bf16 v[68:71], v[136:139], a[68:71], v[68:71]\n"
      "ds_write_addtid_b32 v110 offset:10752\n"
      "v_cvt_pk_bf16_f32 v112, v120, v121\n"
      "v_mfma_f32_16x16x32_bf16 v[64:67], v[140:143], a[96:99], v[64:67]\n"
      "ds_write_addtid_b32 v111 offset:11008\n"
      "v_cvt_pk_bf16_f32 v113, v122, v123\n"
      "v_mfma_f32_16x16x32_bf16 v[68:71], v[140:143], a[100:103], v[68:71]\n"
      "ds_write_addtid_b32 v112 offset:11264\n"
      "v_cvt_pk_bf16_f32 v114, v124, v125\n"
      "v_mfma_f32_16x16x32_bf16 v[64:67], v[144:147], a[128:131], v[64:67]\n"
      "ds_write_addtid_b32 v113 offset:11520\n"
      "v_cvt_pk_bf16_f32 v115, v126, v127\n"
      "v_mfma_f32_16x16x32_bf16 v[68:71], v[144:147], a[132:135], v[68:71]\n"
      "ds_write_addtid_b32 v114 offset:11776\n"
      "v_mov_b32 v15, v20\n"
      "v_mfma_f32_16x16x32_bf16 v[64:67], v[148:151], a[160:163], v[64:67]\n"
      "ds_write_addtid_b32 v115 offset:12032\n"
      "v_add_u32 v6, 24576, v242\n"
      "v_mfma_f32_16x16x32_bf16 v[68:71], v[148:151], a[164:167], v[68:71]\n"
      "global_load_dwordx4 v[72:75], v6, s[62:63] offset:0 sc1\n"
      "v_mov_b32 v19, v21\n"
      "v_mfma_f32_16x16x32_bf16 v[64:67], v[152:155], a[192:195], v[64:67]\n"
      "global_load_dwordx4 v[76:79], v6, s[62:63] offset:1024 sc1\n"
      "v_mfma_f32_16x16x32_bf16 v[68:71], v[152:155], a[196:199], v[68:71]\n"
      "global_load_dwordx4 v[80:83], v6, s[62:63] offset:2048 sc1\n"
      "v_mfma_f32_16x16x32_bf16 v[64:67], v[156:159], a[224:227], v[64:67]\n"
      "s_lshr_b32 s43, s42, 7\n"
      "s_cmp_eq_u32 s43, s36\n"
      "s_cbranch_scc0 .Lp_e20_%=\n"
      "s_waitcnt vmcnt(6)\n"
      "v_bitop3_b32 v61, v25, v27, s37 bitop3:0x7e\n"
      "v_bitop3_b32 v61, v61, v29, s37 bitop3:0xf6\n"
      "v_bitop3_b32 v61, v61, v31, s37 bitop3:0xf6\n"
      "v_bitop3_b32 v61, v61, v33, s37 bitop3:0xf6\n"
      "v_bitop3_b32 v61, v61, v35, s37 bitop3:0xf6\n"
      "v_bitop3_b32 v61, v61, v37, s37 bitop3:0xf6\n"
      "v_bitop3_b32 v61, v61, v39, s37 bitop3:0xf6\n"
      "v_bitop3_b32 v61, v61, v43, s37 bitop3:0xf6\n"
      "v_cmp_ne_u32 vcc, 0, v61\n"
      "s_nop 1\n"
      "s_cbranch_vccnz .Lp_s22_%=\n"
      ".Lp_o23_%=:\n"
      "v_add_f32 v24, v24, v28\n"
      "v_add_f32 v26, v26, v30\n"
      "v_add_f32 v32, v32, v36\n"
      "v_add_f32 v34, v34, v38\n"
      "v_add_f32 v24, v24, v32\n"
      "v_add_f32 v26, v26, v34\n"
      "s_nop 1\n"
      "v_permlane32_swap_b32 v24, v26\n"
      "v_add_f32 v24, v24, v26\n"
      "v_mov_b32 v26, v24\n"
      "s_nop 1\n"
      "v_permlane16_swap_b32 v24, v26\n"
      "v_add_f32 v24, v24, v26\n"
      "ds_write_b32 v59, v24\n"
      "s_waitcnt lgkmcnt(0)\n"
      ".Lp_e20_%=:\n"
      "v_mfma_f32_16x16x32_bf16 v[68:71], v[156:159], a[228:231], v[68:71]\n"
      "s_waitcnt lgkmcnt(0)\n"
      "s_barrier\n"
      "v_mfma_f32_16x16x32_bf16 v[96:99], v[160:163], a[0:3], 0\n"
      "s_lshr_b32 s43, s42, 5\n"
      "s_lshl2_add_u32 s44, s36, s33\n"
      "s_cmp_eq_u32 s43, s44\n"
      "s_cbranch_scc0 .Lp_e24_%=\n"
      "ds_read_b32 v24, v58 offset:0\n"
      "ds_read_b32 v25, v58 offset:8192\n"
      "ds_read_b32 v26, v58 offset:16384\n"
      "ds_read_b32 v27, v58 offset:24576\n"
      "v_mov_b32 v63, s37\n"
      "s_or_b32 s43, s34, s35\n"
      "s_cmp_eq_u32 s43, 0\n"
      "s_cbranch_scc0 .Lp_z25_%=\n"
      "s_mov_b64 exec, 1\n"
      "global_store_dword v62, v63, s[58:59] sc1\n"
      ".Lp_z25_%=:\n"
      "s_mov_b64 exec, -1\n"
      "s_waitcnt vmcnt(6)\n"
      "v_lshlrev_b32 v57, 16, v57\n"
      "v_and_b32 v56, 48, v243\n"
      "s_mov_b32 s44, 131072\n"
      "v_lshl_add_u32 v56, v56, 1, s44\n"
      "s_waitcnt lgkmcnt(0)\n"
      "v_add_f32 v24, v24, v25\n"
      "s_waitcnt lgkmcnt(0)\n"
      "v_add_f32 v26, v26, v27\n"
      "v_add_f32 v24, v24, v26\n"
      "v_cvt_pk_bf16_f32 v24, v24, v24\n"
      "v_and_b32 v24, 0xffff0000, v24\n"
      "v_mul_f32 v24, v41, v24\n"
      "v_cvt_pk_bf16_f32 v24, v24, v24\n"
      "v_and_b32 v24, 0xffff0000, v24\n"
      "v_add_f32 v24, v24, v57\n"
      "v_cvt_pk_bf16_f32 v24, v24, v24\n"
      "v_and_b32 v24, 0xffff0000, v24\n"
      "v_sub_f32 v42, 1.0, v40\n"
      "v_cvt_pk_bf16_f32 v42, v42, v42\n"
      "v_and_b32 v42, 0xffff0000, v42\n"
      "v_mul_f32 v24, v42, v24\n"
      "ds_write_b32 v58, v24 offset:1024\n"
      "ds_read_b128 v[28:31], v56 offset:1024\n"
      "ds_read_b128 v[32:35], v56 offset:1040\n"
      ".Lp_e24_%=:\n"
      "v_mfma_f32_16x16x32_bf16 v[100:103], v[160:163], a[4:7], 0\n"
      "ds_read2st64_b32 v[84:85], v244 offset0:24 offset1:25\n"
      "v_mfma_f32_16x16x32_bf16 v[96:99], v[164:167], a[32:35], v[96:99]\n"
      "ds_read2st64_b32 v[86:87], v245 offset0:28 offset1:29\n"
      "v_mfma_f32_16x16x32_bf16 v[100:103], v[164:167], a[36:39], v[100:103]\n"
      "ds_read2st64_b32 v[88:89], v244 offset0:26 offset1:27\n"
      "v_mfma_f32_16x16x32_bf16 v[96:99], v[168:171], a[64:67], v[96:99]\n"
      "ds_read2st64_b32 v[90:91], v245 offset0:30 offset1:31\n"
      "v_mfma_f32_16x16x32_bf16 v[100:103], v[168:171], a[68:71], v[100:103]\n"
      "ds_read2st64_b32 v[92:93], v246 offset0:32 offset1:33\n"
      "v_mfma_f32_16x16x32_bf16 v[96:99], v[172:175], a[96:99], v[96:99]\n"
      "ds_read2st64_b32 v[94:95], v246 offset0:34 offset1:35\n"
      "v_mfma_f32_16x16x32_bf16 v[100:103], v[172:175], a[100:103], v[100:103]\n"
      "ds_read2st64_b32 v[116:117], v244 offset0:36 offset1:37\n"
      "v_mfma_f32_16x16x32_bf16 v[96:99], v[176:179], a[128:131], v[96:99]\n"
      "ds_read2st64_b32 v[118:119], v245 offset0:40 offset1:41\n"
      "v_mfma_f32_16x16x32_bf16 v[100:103], v[176:179], a[132:135], v[100:103]\n"
      "ds_read2st64_b32 v[120:121], v244 offset0:38 offset1:39\n"
      "v_mfma_f32_16x16x32_bf16 v[96:99], v[180:183], a[160:163], v[96:99]\n"
      "s_waitcnt lgkmcnt(3)\n"
      "v_mfma_f32_16x16x32_bf16 v[64:67], v[250:253], v[84:87], v[64:67]\n"
      "v_mfma_f32_16x16x32_bf16 v[100:103], v[180:183], a[164:167], v[100:103]\n"
      "v_mfma_f32_16x16x32_bf16 v[68:71], v[250:253], v[88:91], v[68:71]\n"
      "v_mfma_f32_16x16x32_bf16 v[96:99], v[184:187], a[192:195], v[96:99]\n"
      "v_mfma_f32_16x16x32_bf16 v[64:67], v[252:255], v[92:95], v[64:67]\n"
      "v_mfma_f32_16x16x32_bf16 v[100:103], v[184:187], a[196:199], v[100:103]\n"
      "v_mfma_f32_16x16x32_bf16 v[68:71], v[248:251], v[92:95], v[68:71]\n"
      "v_mfma_f32_16x16x32_bf16 v[96:99], v[188:191], a[224:227], v[96:99]\n"
      "ds_read2st64_b32 v[122:123], v245 offset0:42 offset1:43\n"
      "v_mfma_f32_16x16x32_bf16 v[100:103], v[188:191], a[228:231], v[100:103]\n"
      "ds_read2st64_b32 v[124:125], v246 offset0:44 offset1:45\n"
      "ds_read2st64_b32 v[126:127], v246 offset0:46 offset1:47\n"
      "v_mfma_f32_16x16x32_bf16 v[64:67], v[252:255], v[12:15], v[64:67]\n"
      "v_mfma_f32_16x16x32_bf16 v[68:71], v[248:251], v[12:15], v[68:71]\n"
      "v_mfma_f32_16x16x32_bf16 v[96:99], v[252:255], v[16:19], v[96:99]\n"
      "v_mfma_f32_16x16x32_bf16 v[100:103], v[248:251], v[16:19], v[100:103]\n"
      "s_waitcnt vmcnt(3)\n"
      "v_bitop3_b32 v54, v47, v51, s37 bitop3:0x7e\n"
      "v_bitop3_b32 v54, v54, v55, s37 bitop3:0xf6\n"
      "v_cmp_ne_u32 vcc, 0, v54\n"
      "s_nop 1\n"
      "s_cbranch_vccnz .Lp_s27_%=\n"
      ".Lp_o28_%=:\n"
      "v_mov_b32 v47, v52\n"
      "v_mov_b32 v51, v53\n"
      "s_nop 0\n"
      "v_mfma_f32_16x16x32_bf16 v[64:67], v[252:255], v[44:47], v[64:67]\n"
      "v_mfma_f32_16x16x32_bf16 v[68:71], v[248:251], v[44:47], v[68:71]\n"
      "v_mfma_f32_16x16x32_bf16 v[96:99], v[252:255], v[48:51], v[96:99]\n"
      "v_mfma_f32_16x16x32_bf16 v[100:103], v[248:251], v[48:51], v[100:103]\n"
      "s_waitcnt lgkmcnt(0)\n"
      "v_mfma_f32_16x16x32_bf16 v[96:99], v[250:253], v[116:119], v[96:99]\n"
      "v_mfma_f32_16x16x32_bf16 v[100:103], v[250:253], v[120:123], v[100:103]\n"
      "v_mfma_f32_16x16x32_bf16 v[96:99], v[252:255], v[124:127], v[96:99]\n"
      "v_mfma_f32_16x16x32_bf16 v[100:103], v[248:251], v[124:127], v[100:103]\n"
      "s_lshr_b32 s43, s42, 5\n"
      "s_lshl2_add_u32 s44, s36, s33\n"
      "s_cmp_eq_u32 s43, s44\n"
      "s_cbranch_scc0 .Lp_e29_%=\n"
      "v_lshrrev_b32 v24, 10, v7\n"
      "v_subrev_u32 v24, s42, v24\n"
      "v_cmp_eq_u32 vcc, 0, v24\n"
      "s_nop 1\n"
      "s_mov_b64 exec, vcc\n"
      "v_mov_b32 v204, v40\n"
      "v_mov_b32 v208, v28\n"
      "v_mov_b32 v209, v29\n"
      "v_mov_b32 v210, v30\n"
      "v_mov_b32 v211, v31\n"
      "v_mov_b32 v216, v32\n"
      "v_mov_b32 v217, v33\n"
      "v_mov_b32 v218, v34\n"
      "v_mov_b32 v219, v35\n"
      "s_mov_b64 exec, -1\n"
      "v_cmp_eq_u32 vcc, -16, v24\n"
      "s_nop 1\n"
      "s_mov_b64 exec, vcc\n"
      "v_mov_b32 v206, v40\n"
      "v_mov_b32 v212, v28\n"
      "v_mov_b32 v213, v29\n"
      "v_mov_b32 v214, v30\n"
      "v_mov_b32 v215, v31\n"
      "v_mov_b32 v220, v32\n"
      "v_mov_b32 v221, v33\n"
      "v_mov_b32 v222, v34\n"
      "v_mov_b32 v223, v35\n"
      "s_mov_b64 exec, -1\n"
      ".Lp_e29_%=:\n"
      "s_waitcnt vmcnt(0)\n"
      "v_bitop3_b32 v82, v75, v79, s37 bitop3:0x7e\n"
      "v_bitop3_b32 v82, v82, v83, s37 bitop3:0xf6\n"
      "v_cmp_ne_u32 vcc, 0, v82\n"
      "s_nop 1\n"
      "s_cbranch_vccnz .Lp_s31_%=\n"
      ".Lp_o32_%=:\n"
      "v_mov_b32 v75, v80\n"
      "v_mov_b32 v79, v81\n"
      "s_nop 0\n"
      "v_mfma_f32_16x16x32_bf16 v[64:67], v[252:255], v[72:75], v[64:67]\n"
      "v_mfma_f32_16x16x32_bf16 v[96:99], v[252:255], v[76:79], v[96:99]\n"
      "v_mfma_f32_16x16x32_bf16 v[68:71], v[248:251], v[72:75], v[68:71]\n"
      "v_mfma_f32_16x16x32_bf16 v[100:103], v[248:251], v[76:79], v[100:103]\n"
      "s_nop 4\n"
      "v_fma_f32 v208, v204, v64, v208\n"
      "v_fma_f32 v209, v204, v65, v209\n"
      "v_fma_f32 v210, v204, v66, v210\n"
      "v_fma_f32 v211, v204, v67, v211\n"
      "v_fma_f32 v216, v204, v96, v216\n"
      "v_fma_f32 v217, v204, v97, v217\n"
      "v_fma_f32 v218, v204, v98, v218\n"
      "v_fma_f32 v219, v204, v99, v219\n"
      "v_cvt_pk_bf16_f32 v104, v208, v209\n"
      "v_cvt_pk_bf16_f32 v105, v210, v211\n"
      "v_cvt_pk_bf16_f32 v106, v216, v217\n"
      "v_cvt_pk_bf16_f32 v107, v218, v219\n"
      "global_store_dwordx4 v7, v[104:107], s[20:21]\n"
      "v_fma_f32 v212, v206, v68, v212\n"
      "v_fma_f32 v213, v206, v69, v213\n"
      "v_fma_f32 v214, v206, v70, v214\n"
      "v_fma_f32 v215, v206, v71, v215\n"
      "v_fma_f32 v220, v206, v100, v220\n"
      "v_fma_f32 v221, v206, v101, v221\n"
      "v_fma_f32 v222, v206, v102, v222\n"
      "v_fma_f32 v223, v206, v103, v223\n"
      "v_cvt_pk_bf16_f32 v108, v212, v213\n"
      "v_cvt_pk_bf16_f32 v109, v214, v215\n"
      "v_cvt_pk_bf16_f32 v110, v220, v221\n"
      "v_cvt_pk_bf16_f32 v111, v222, v223\n"
      "global_store_dwordx4 v7, v[108:111], s[68:69]\n"
      "s_endpgm\n"
      ".Lp_s2_%=:\n"
      "s_cmp_eq_u32 s38, 0\n"
      "s_cbranch_scc1 .Lp_o3_%=\n"
      "s_sub_u32 s38, s38, 1\n"
      "s_sleep 4\n"
      "s_nop 7\n"
      "global_load_dword v252, v253, s[58:59] offset:0 sc1\n"
      "s_waitcnt vmcnt(0)\n"
      "v_cmp_ne_u32 vcc, s37, v252\n"
      "s_nop 1\n"
      "s_cbranch_vccnz .Lp_s2_%=\n"
      "s_nop 7\n"
      "s_branch .Lp_o3_%=\n"
      ".Lp_s5_%=:\n"
      "s_cmp_eq_u32 s38, 0\n"
      "s_cbranch_scc1 .Lp_o6_%=\n"
      "s_sub_u32 s38, s38, 1\n"
      "s_sleep 4\n"
      "s_nop 7\n"
      "global_load_dword v252, v253, s[58:59] offset:128 sc1\n"
      "s_waitcnt vmcnt(0)\n"
      "v_cmp_ne_u32 vcc, s37, v252\n"
      "s_nop 1\n"
      "s_cbranch_vccnz .Lp_s5_%=\n"
      "s_nop 7\n"
      "s_branch .Lp_o6_%=\n"
      ".Lp_s8_%=:\n"
      "s_cmp_eq_u32 s38, 0\n"
      "s_cbranch_scc1 .Lp_o9_%=\n"
      "s_sub_u32 s38, s38, 1\n"
      "s_sleep 2\n"
      "s_nop 7\n"
      "global_load_dwordx2 v[234:235], v232, s[58:59] sc1\n"
      "s_waitcnt vmcnt(0)\n"
      "v_cmp_ne_u32 vcc, s37, v235\n"
      "s_nop 1\n"
      "s_cbranch_vccnz .Lp_s8_%=\n"
      "s_nop 7\n"
      "s_branch .Lp_o9_%=\n"
      ".Lp_s11_%=:\n"
      "s_cmp_eq_u32 s38, 0\n"
      "s_cbranch_scc1 .Lp_o12_%=\n"
      "s_sub_u32 s38, s38, 1\n"
      "s_sleep 1\n"
      "s_nop 7\n"
      "global_load_dwordx2 v[204:205], v209, s[58:59] offset:0 sc1\n"
      "global_load_dwordx2 v[206:207], v209, s[58:59] offset:256 sc1\n"
      "s_waitcnt vmcnt(0)\n"
      "v_bitop3_b32 v210, v205, v207, s37 bitop3:0x7e\n"
      "v_cmp_ne_u32 vcc, 0, v210\n"
      "s_nop 1\n"
      "s_cbranch_vccnz .Lp_s11_%=\n"
      "s_nop 7\n"
      "s_branch .Lp_o12_%=\n"
      ".Lp_s14_%=:\n"
      "s_cmp_eq_u32 s38, 0\n"
      "s_cbranch_scc1 .Lp_o15_%=\n"
      "s_sub_u32 s38, s38, 1\n"
      "s_sleep 8\n"
      "s_nop 7\n"
      "global_load_dwordx4 v[224:227], v200, s[58:59] offset:0 sc1\n"
      "global_load_dwordx4 v[228:231], v200, s[58:59] offset:1024 sc1\n"
      "global_load_dwordx4 v[232:235], v200, s[58:59] offset:2048 sc1\n"
      "global_load_dwordx4 v[236:239], v200, s[58:59] offset:3072 sc1\n"
      "s_waitcnt vmcnt(0)\n"
      "v_bitop3_b32 v192, v225, v227, s37 bitop3:0x7e\n"
      "v_bitop3_b32 v192, v192, v229, s37 bitop3:0xf6\n"
      "v_bitop3_b32 v192, v192, v231, s37 bitop3:0xf6\n"
      "v_bitop3_b32 v192, v192, v233, s37 bitop3:0xf6\n"
      "v_bitop3_b32 v192, v192, v235, s37 bitop3:0xf6\n"
      "v_bitop3_b32 v192, v192, v237, s37 bitop3:0xf6\n"
      "v_bitop3_b32 v192, v192, v239, s37 bitop3:0xf6\n"
      "v_cmp_ne_u32 vcc, 0, v192\n"
      "s_nop 1\n"
      "s_cbranch_vccnz .Lp_s14_%=\n"
      "s_nop 7\n"
      "s_branch .Lp_o15_%=\n"
      ".Lp_s18_%=:\n"
      "s_cmp_eq_u32 s38, 0\n"
      "s_cbranch_scc1 .Lp_o19_%=\n"
      "s_sub_u32 s38, s38, 1\n"
      "s_nop 7\n"
      "global_load_dwordx4 v[12:15], v4, s[62:63] offset:0 sc1\n"
      "global_load_dwordx4 v[16:19], v4, s[62:63] offset:1024 sc1\n"
      "global_load_dwordx4 v[20:23], v4, s[62:63] offset:2048 sc1\n"
      "s_waitcnt vmcnt(0)\n"
      "v_bitop3_b32 v22, v15, v19, s37 bitop3:0x7e\n"
      "v_bitop3_b32 v22, v22, v23, s37 bitop3:0xf6\n"
      "v_cmp_ne_u32 vcc, 0, v22\n"
      "s_nop 1\n"
      "s_cbranch_vccnz .Lp_s18_%=\n"
      "s_nop 7\n"
      "s_branch .Lp_o19_%=\n"
      ".Lp_s22_%=:\n"
      "s_cmp_eq_u32 s38, 0\n"
      "s_cbranch_scc1 .Lp_o23_%=\n"
      "s_sub_u32 s38, s38, 1\n"
      "s_sleep 2\n"
      "s_nop 7\n"
      "global_load_dwordx4 v[24:27], v60, s[58:59] offset:0 sc1\n"
      "global_load_dwordx4 v[28:31], v60, s[58:59] offset:1024 sc1\n"
      "global_load_dwordx4 v[32:35], v60, s[58:59] offset:2048 sc1\n"
      "global_load_dwordx4 v[36:39], v60, s[58:59] offset:3072 sc1\n"
      "global_load_dwordx4 v[40:43], v62, s[58:59] offset:1024 sc1\n"
      "s_waitcnt vmcnt(0)\n"
      "v_bitop3_b32 v61, v25, v27, s37 bitop3:0x7e\n"
      "v_bitop3_b32 v61, v61, v29, s37 bitop3:0xf6\n"
      "v_bitop3_b32 v61, v61, v31, s37 bitop3:0xf6\n"
      "v_bitop3_b32 v61, v61, v33, s37 bitop3:0xf6\n"
      "v_bitop3_b32 v61, v61, v35, s37 bitop3:0xf6\n"
      "v_bitop3_b32 v61, v61, v37, s37 bitop3:0xf6\n"
      "v_bitop3_b32 v61, v61, v39, s37 bitop3:0xf6\n"
      "v_bitop3_b32 v61, v61, v43, s37 bitop3:0xf6\n"
      "v_cmp_ne_u32 vcc, 0, v61\n"
      "s_nop 1\n"
      "s_cbranch_vccnz .Lp_s22_%=\n"
      "s_nop 7\n"
      "s_branch .Lp_o23_%=\n"
      ".Lp_s27_%=:\n"
      "s_cmp_eq_u32 s38, 0\n"
      "s_cbranch_scc1 .Lp_o28_%=\n"
      "s_sub_u32 s38, s38, 1\n"
      "s_nop 7\n"
      "global_load_dwordx4 v[44:47], v5, s[62:63] offset:0 sc1\n"
      "global_load_dwordx4 v[48:51], v5, s[62:63] offset:1024 sc1\n"
      "global_load_dwordx4 v[52:55], v5, s[62:63] offset:2048 sc1\n"
      "s_waitcnt vmcnt(0)\n"
      "v_bitop3_b32 v54, v47, v51, s37 bitop3:0x7e\n"
      "v_bitop3_b32 v54, v54, v55, s37 bitop3:0xf6\n"
      "v_cmp_ne_u32 vcc, 0, v54\n"
      "s_nop 1\n"
      "s_cbranch_vccnz .Lp_s27_%=\n"
      "s_nop 7\n"
      "s_branch .Lp_o28_%=\n"
      ".Lp_s31_%=:\n"
      "s_cmp_eq_u32 s38, 0\n"
      "s_cbranch_scc1 .Lp_o32_%=\n"
      "s_sub_u32 s38, s38, 1\n"
      "s_nop 7\n"
      "global_load_dwordx4 v[72:75], v6, s[62:63] offset:0 sc1\n"
      "global_load_dwordx4 v[76:79], v6, s[62:63] offset:1024 sc1\n"
      "global_load_dwordx4 v[80:83], v6, s[62:63] offset:2048 sc1\n"
      "s_waitcnt vmcnt(0)\n"
      "v_bitop3_b32 v82, v75, v79, s37 bitop3:0x7e\n"
      "v_bitop3_b32 v82, v82, v83, s37 bitop3:0xf6\n"
      "v_cmp_ne_u32 vcc, 0, v82\n"
      "s_nop 1\n"
      "s_cbranch_vccnz .Lp_s31_%=\n"
      "s_nop 7\n"
      "s_branch .Lp_o32_%=\n"
      ".Ls_s3_%=:\n"
      "s_cmp_eq_u32 s38, 0\n"
      "s_cbranch_scc1 .Ls_o4_%=\n"
      "s_sub_u32 s38, s38, 1\n"
      "s_sleep 4\n"
      "s_nop 7\n"
      "global_load_dword v252, v253, s[58:59] offset:0 sc1\n"
      "s_waitcnt vmcnt(0)\n"
      "v_cmp_ne_u32 vcc, s37, v252\n"
      "s_nop 1\n"
      "s_cbranch_vccnz .Ls_s3_%=\n"
      "s_nop 7\n"
      "s_branch .Ls_o4_%=\n"
      ".Ls_s6_%=:\n"
      "s_cmp_eq_u32 s38, 0\n"
      "s_cbranch_scc1 .Ls_o7_%=\n"
      "s_sub_u32 s38, s38, 1\n"
      "s_sleep 2\n"
      "s_nop 7\n"
      "global_load_dwordx2 v[206:207], v204, s[58:59] sc1\n"
      "s_waitcnt vmcnt(0)\n"
      "v_cmp_ne_u32 vcc, s37, v207\n"
      "s_nop 1\n"
      "s_cbranch_vccnz .Ls_s6_%=\n"
      "s_nop 7\n"
      "s_branch .Ls_o7_%=\n"
      ".Ls_s11_%=:\n"
      "s_cmp_eq_u32 s38, 0\n"
      "s_cbranch_scc1 .Ls_o12_%=\n"
      "s_sub_u32 s38, s38, 1\n"
      "s_sleep 4\n"
      "s_nop 7\n"
      "global_load_dword v252, v253, s[58:59] offset:128 sc1\n"
      "s_waitcnt vmcnt(0)\n"
      "v_cmp_ne_u32 vcc, s37, v252\n"
      "s_nop 1\n"
      "s_cbranch_vccnz .Ls_s11_%=\n"
      "s_nop 7\n"
      "s_branch .Ls_o12_%=\n"
      ".Ls_s14_%=:\n"
      "s_cmp_eq_u32 s38, 0\n"
      "s_cbranch_scc1 .Ls_o15_%=\n"
      "s_sub_u32 s38, s38, 1\n"
      "s_sleep 1\n"
      "s_nop 7\n"
      "global_load_dwordx2 v[204:205], v209, s[58:59] offset:0 sc1\n"
      "global_load_dwordx2 v[206:207], v209, s[58:59] offset:256 sc1\n"
      "s_waitcnt vmcnt(0)\n"
      "v_bitop3_b32 v210, v205, v207, s37 bitop3:0x7e\n"
      "v_cmp_ne_u32 vcc, 0, v210\n"
      "s_nop 1\n"
      "s_cbranch_vccnz .Ls_s14_%=\n"
      "s_nop 7\n"
      "s_branch .Ls_o15_%=\n"
      ".Ls_s17_%=:\n"
      "s_cmp_eq_u32 s38, 0\n"
      "s_cbranch_scc1 .Ls_o18_%=\n"
      "s_sub_u32 s38, s38, 1\n"
      "s_nop 7\n"
      "global_load_dwordx4 v[12:15], v4, s[62:63] offset:0 sc1\n"
      "global_load_dwordx4 v[16:19], v4, s[62:63] offset:1024 sc1\n"
      "global_load_dwordx4 v[20:23], v4, s[62:63] offset:2048 sc1\n"
      "s_waitcnt vmcnt(0)\n"
      "v_bitop3_b32 v22, v15, v19, s37 bitop3:0x7e\n"
      "v_bitop3_b32 v22, v22, v23, s37 bitop3:0xf6\n"
      "v_cmp_ne_u32 vcc, 0, v22\n"
      "s_nop 1\n"
      "s_cbranch_vccnz .Ls_s17_%=\n"
      "s_nop 7\n"
      "s_branch .Ls_o18_%=\n"
      ".Ls_s22_%=:\n"
      "s_cmp_eq_u32 s38, 0\n"
      "s_cbranch_scc1 .Ls_o23_%=\n"
      "s_sub_u32 s38, s38, 1\n"
      "s_sleep 2\n"
      "s_nop 7\n"
      "global_load_dwordx4 v[24:27], v60, s[58:59] offset:0 sc1\n"
      "global_load_dwordx4 v[28:31], v60, s[58:59] offset:1024 sc1\n"
      "global_load_dwordx4 v[32:35], v60, s[58:59] offset:2048 sc1\n"
      "global_load_dwordx4 v[36:39], v60, s[58:59] offset:3072 sc1\n"
      "global_load_dwordx4 v[40:43], v62, s[58:59] offset:1024 sc1\n"
      "s_waitcnt vmcnt(0)\n"
      "v_bitop3_b32 v61, v25, v27, s37 bitop3:0x7e\n"
      "v_bitop3_b32 v61, v61, v29, s37 bitop3:0xf6\n"
      "v_bitop3_b32 v61, v61, v31, s37 bitop3:0xf6\n"
      "v_bitop3_b32 v61, v61, v33, s37 bitop3:0xf6\n"
      "v_bitop3_b32 v61, v61, v35, s37 bitop3:0xf6\n"
      "v_bitop3_b32 v61, v61, v37, s37 bitop3:0xf6\n"
      "v_bitop3_b32 v61, v61, v39, s37 bitop3:0xf6\n"
      "v_bitop3_b32 v61, v61, v43, s37 bitop3:0xf6\n"
      "v_cmp_ne_u32 vcc, 0, v61\n"
      "s_nop 1\n"
      "s_cbranch_vccnz .Ls_s22_%=\n"
      "s_nop 7\n"
      "s_branch .Ls_o23_%=\n"
      ".Ls_s27_%=:\n"
      "s_cmp_eq_u32 s38, 0\n"
      "s_cbranch_scc1 .Ls_o28_%=\n"
      "s_sub_u32 s38, s38, 1\n"
      "s_nop 7\n"
      "global_load_dwordx4 v[44:47], v5, s[62:63] offset:0 sc1\n"
      "global_load_dwordx4 v[48:51], v5, s[62:63] offset:1024 sc1\n"
      "global_load_dwordx4 v[52:55], v5, s[62:63] offset:2048 sc1\n"
      "s_waitcnt vmcnt(0)\n"
      "v_bitop3_b32 v54, v47, v51, s37 bitop3:0x7e\n"
      "v_bitop3_b32 v54, v54, v55, s37 bitop3:0xf6\n"
      "v_cmp_ne_u32 vcc, 0, v54\n"
      "s_nop 1\n"
      "s_cbranch_vccnz .Ls_s27_%=\n"
      "s_nop 7\n"
      "s_branch .Ls_o28_%=\n"
      ".Ls_s31_%=:\n"
      "s_cmp_eq_u32 s38, 0\n"
      "s_cbranch_scc1 .Ls_o32_%=\n"
      "s_sub_u32 s38, s38, 1\n"
      "s_nop 7\n"
      "global_load_dwordx4 v[72:75], v6, s[62:63] offset:0 sc1\n"
      "global_load_dwordx4 v[76:79], v6, s[62:63] offset:1024 sc1\n"
      "global_load_dwordx4 v[80:83], v6, s[62:63] offset:2048 sc1\n"
      "s_waitcnt vmcnt(0)\n"
      "v_bitop3_b32 v82, v75, v79, s37 bitop3:0x7e\n"
      "v_bitop3_b32 v82, v82, v83, s37 bitop3:0xf6\n"
      "v_cmp_ne_u32 vcc, 0, v82\n"
      "s_nop 1\n"
      "s_cbranch_vccnz .Ls_s31_%=\n"
      "s_nop 7\n"
      "s_branch .Ls_o32_%=\n"
      ".Lend_%=:\n"
      :
      : "s"(__builtin_amdgcn_kernarg_segment_ptr()), "s"(blockIdx.x), "s"(__builtin_amdgcn_readfirstlane(threadIdx.x >> 6)),
        "s"((unsigned)(size_t)smem)
      : "memory", "scc", "vcc",
        "v0", "v1", "v2", "v3", "v4", "v5", "v6", "v7", "v8", "v9", "v10", "v11", "v12", "v13", "v14", "v15", "v16", "v17", "v18", "v19", "v20", "v21", "v22", "v23", "v24", "v25", "v26", "v27", "v28", "v29", "v30", "v31", "v32", "v33", "v34", "v35", "v36", "v37", "v38", "v39", "v40", "v41", "v42", "v43", "v44", "v45", "v46", "v47", "v48", "v49", "v50", "v51", "v52", "v53", "v54", "v55", "v56", "v57", "v58", "v59", "v60", "v61", "v62", "v63", "v64", "v65", "v66", "v67", "v68", "v69", "v70", "v71", "v72", "v73", "v74", "v75", "v76", "v77", "v78", "v79", "v80", "v81", "v82", "v83", "v84", "v85", "v86", "v87", "v88", "v89", "v90", "v91", "v92", "v93", "v94", "v95", "v96", "v97", "v98", "v99", "v100", "v101", "v102", "v103", "v104", "v105", "v106", "v107", "v108", "v109", "v110", "v111", "v112", "v113", "v114", "v115", "v116", "v117", "v118", "v119", "v120", "v121", "v122", "v123", "v124", "v125", "v126", "v127", "v128", "v129", "v130", "v131", "v132", "v133", "v134", "v135", "v136", "v137", "v138", "v139", "v140", "v141", "v142", "v143", "v144", "v145", "v146", "v147", "v148", "v149", "v150", "v151", "v152", "v153", "v154", "v155", "v156", "v157", "v158", "v159", "v160", "v161", "v162", "v163", "v164", "v165", "v166", "v167", "v168", "v169", "v170", "v171", "v172", "v173", "v174", "v175", "v176", "v177", "v178", "v179", "v180", "v181", "v182", "v183", "v184", "v185", "v186", "v187", "v188", "v189", "v190", "v191", "v192", "v193", "v194", "v195", "v196", "v197", "v198", "v199", "v200", "v201", "v202", "v203", "v204", "v205", "v206", "v207", "v208", "v209", "v210", "v211", "v212", "v213", "v214", "v215", "v216", "v217", "v218", "v219", "v220", "v221", "v222", "v223", "v224", "v225", "v226", "v227", "v228", "v229", "v230", "v231", "v232", "v233", "v234", "v235", "v236", "v237", "v238", "v239", "v240", "v241", "v242", "v243", "v244", "v245", "v246", "v247", "v248", "v249", "v250", "v251", "v252", "v253", "v254", "v255",
        "a0", "a1", "a2", "a3", "a4", "a5", "a6", "a7", "a8", "a9", "a10", "a11", "a12", "a13", "a14", "a15", "a16", "a17", "a18", "a19", "a20", "a21", "a22", "a23", "a24", "a25", "a26", "a27", "a28", "a29", "a30", "a31", "a32", "a33", "a34", "a35", "a36", "a37", "a38", "a39", "a40", "a41", "a42", "a43", "a44", "a45", "a46", "a47", "a48", "a49", "a50", "a51", "a52", "a53", "a54", "a55", "a56", "a57", "a58", "a59", "a60", "a61", "a62", "a63", "a64", "a65", "a66", "a67", "a68", "a69", "a70", "a71", "a72", "a73", "a74", "a75", "a76", "a77", "a78", "a79", "a80", "a81", "a82", "a83", "a84", "a85", "a86", "a87", "a88", "a89", "a90", "a91", "a92", "a93", "a94", "a95", "a96", "a97", "a98", "a99", "a100", "a101", "a102", "a103", "a104", "a105", "a106", "a107", "a108", "a109", "a110", "a111", "a112", "a113", "a114", "a115", "a116", "a117", "a118", "a119", "a120", "a121", "a122", "a123", "a124", "a125", "a126", "a127", "a128", "a129", "a130", "a131", "a132", "a133", "a134", "a135", "a136", "a137", "a138", "a139", "a140", "a141", "a142", "a143", "a144", "a145", "a146", "a147", "a148", "a149", "a150", "a151", "a152", "a153", "a154", "a155", "a156", "a157", "a158", "a159", "a160", "a161", "a162", "a163", "a164", "a165", "a166", "a167", "a168", "a169", "a170", "a171", "a172", "a173", "a174", "a175", "a176", "a177", "a178", "a179", "a180", "a181", "a182", "a183", "a184", "a185", "a186", "a187", "a188", "a189", "a190", "a191", "a192", "a193", "a194", "a195", "a196", "a197", "a198", "a199", "a200", "a201", "a202", "a203", "a204", "a205", "a206", "a207", "a208", "a209", "a210", "a211", "a212", "a213", "a214", "a215", "a216", "a217", "a218", "a219", "a220", "a221", "a222", "a223", "a224", "a225", "a226", "a227", "a228", "a229", "a230", "a231", "a232", "a233", "a234", "a235", "a236", "a237", "a238", "a239", "a240", "a241", "a242", "a243", "a244", "a245", "a246", "a247", "a248", "a249", "a250", "a251", "a252", "a253", "a254", "a255",
        "s8", "s9", "s10", "s11", "s12", "s13", "s14", "s15", "s16", "s17", "s18", "s19", "s20", "s21", "s22", "s23", "s24", "s25", "s26", "s27", "s28", "s29", "s30", "s31", "s32", "s33", "s34", "s35", "s36", "s37", "s38", "s39", "s40", "s41", "s42", "s43", "s44", "s45", "s46", "s47", "s48", "s49", "s50", "s51", "s52", "s53", "s54", "s55", "s56", "s57", "s58", "s59", "s60", "s61", "s62", "s63", "s64", "s65", "s66", "s67", "s68", "s69", "s70", "s71", "s72", "s73", "s74", "s75", "s76", "s77", "s78", "s79", "s80", "s81", "s82", "s83", "s84", "s85", "s86", "s87");
}

extern "C" void kernel_launch(void* const* d_in, const int* in_sizes, int n_in, void* d_out, int out_size, void* d_ws, size_t ws_size,
                              hipStream_t stream) {
  fused_k<<<256, 256, 0, stream>>>((const u16*)d_in[1], (const u16*)d_in[2], (const u16*)d_in[3], (const u16*)d_in[4], (const u16*)d_in[0],
                                   (char*)d_ws, (u16*)d_out, (const u16*)d_in[6], (const u16*)d_in[10], (const u16*)d_in[7],
                                   (const u16*)d_in[8], (const u16*)d_in[9]);
}
```

```cpp
#include <hip/hip_runtime.h>

typedef unsigned short u16;

__global__ __launch_bounds__(256, 1) void fused_k(const u16* wsre, const u16* wsim, const u16* wdre, const u16* wdim, const u16* x, char* ws,
                                               u16* out, const u16* w_r, const u16* gate_w, const u16* w_gate, const u16* w_up,
                                               const u16* w_down) {
  __shared__ __attribute__((aligned(16))) char smem[163840];
  asm volatile(
      ".Lbegin_%=:\n"
      "s_load_dwordx16 s[8:23], %0, 0\n"
      "s_load_dwordx8 s[24:31], %0, 64\n"
      "v_mbcnt_lo_u32_b32 v243, -1, 0\n"
      "s_mov_b32 s32, %1\n"
      "v_mbcnt_hi_u32_b32 v243, -1, v243\n"
      "s_mov_b32 s33, %2\n"
      "s_bfe_u32 s34, s32, 0x20001\n"
      "s_bfe_u32 s35, s32, 0x20003\n"
      "v_lshrrev_b32 v236, 3, v243\n"
      "s_lshr_b32 s43, s32, 5\n"
      "v_xor_b32 v237, v243, v236\n"
      "s_and_b32 s36, s32, 1\n"
      "v_and_b32 v237, 7, v237\n"
      "s_lshl3_add_u32 s36, s36, s43\n"
      "v_lshlrev_b32 v237, 4, v237\n"
      "s_add_u32 s44, s43, s35\n"
      "v_lshlrev_b32 v242, 4, v243\n"
      "s_lshl_b32 s41, s33, 14\n"
      "s_bitcmp1_b32 s44, 0\n"
      "s_cbranch_scc1 .Lstream_%=\n"
      "v_and_b32 v238, 3, v236\n"
      "s_lshr_b32 s43, s43, 1\n"
      "v_and_b32 v239, 4, v236\n"
      "s_add_u32 s80, s43, s35\n"
      "v_lshl_or_b32 v238, v239, 1, v238\n"
      "s_lshl_b32 s80, s80, 8\n"
      "v_lshl_or_b32 v240, v238, 10, v237\n"
      "s_and_b32 s80, s80, 0x300\n"
      "v_add_u32 v241, 15360, v240\n"
      "s_lshr_b32 s83, s36, 1\n"
      "v_add_u32 v254, 2048, v240\n"
      "s_and_b32 s44, s34, 1\n"
      "v_add_u32 v255, 17408, v240\n"
      "s_lshl3_add_u32 s44, s44, s83\n"
      "s_lshl2_add_u32 s40, s44, s33\n"
      "s_lshl_b32 s44, s40, 15\n"
      "s_lshl_b32 s81, s33, 8\n"
      "s_add_u32 s81, s81, s80\n"
      "s_and_b32 s81, s81, 0x380\n"
      "s_lshl_b32 s45, s35, 17\n"
      "s_lshl_b32 s82, s33, 12\n"
      "s_add_u32 s76, s34, 1\n"
      "s_and_b32 s76, s76, 3\n"
      "s_lshl_b32 s76, s76, 15\n"
      "s_or_b32 s76, s76, s81\n"
      "s_add_u32 s76, s76, s45\n"
      "s_add_u32 s77, s34, 2\n"
      "s_and_b32 s77, s77, 3\n"
      "s_lshl_b32 s77, s77, 15\n"
      "s_or_b32 s77, s77, s81\n"
      "s_add_u32 s77, s77, s45\n"
      "s_add_u32 s78, s44, s80\n"
      "s_add_u32 s79, s80, 256\n"
      "s_and_b32 s79, s79, 0x300\n"
      "s_add_u32 s79, s79, s44\n"
      "s_add_u32 m0, s82, 65536\n"
      "s_bitcmp1_b32 s34, 1\n"
      "s_waitcnt lgkmcnt(0)\n"
      "s_cselect_b64 s[12:13], s[14:15], s[12:13]\n"
      "s_add_u32 s46, s12, s76\n"
      "s_addc_u32 s47, s13, 0\n"
      "global_load_lds_dwordx4 v240, s[46:47] offset:0\n"
      "global_load_lds_dwordx4 v241, s[46:47] offset:1024\n"
      "global_load_lds_dwordx4 v254, s[46:47] offset:2048\n"
      "global_load_lds_dwordx4 v255, s[46:47] offset:3072\n"
      "s_add_u32 s70, s16, s78\n"
      "s_addc_u32 s71, s17, 0\n"
      "s_mov_b32 m0, s41\n"
      "s_add_u32 s46, s12, s77\n"
      "s_addc_u32 s47, s13, 0\n"
      "global_load_lds_dwordx4 v240, s[70:71] offset:0\n"
      "global_load_lds_dwordx4 v241, s[70:71] offset:1024\n"
      "global_load_lds_dwordx4 v254, s[70:71] offset:2048\n"
      "global_load_lds_dwordx4 v255, s[70:71] offset:3072\n"
      "s_add_u32 m0, s82, 98304\n"
      "s_nop 0\n"
      "global_load_lds_dwordx4 v240, s[46:47] offset:0\n"
      "global_load_lds_dwordx4 v241, s[46:47] offset:1024\n"
      "s_add_u32 s70, s16, s79\n"
      "s_addc_u32 s71, s17, 0\n"
      "s_add_u32 m0, s41, 4096\n"
      "s_nop 0\n"
      "global_load_lds_dwordx4 v240, s[70:71] offset:0\n"
      "global_load_lds_dwordx4 v241, s[70:71] offset:1024\n"
      "global_load_lds_dwordx4 v254, s[70:71] offset:2048\n"
      "global_load_lds_dwordx4 v255, s[70:71] offset:3072\n"
      "s_branch .Lprod_%=\n"
      ".Lstream_%=:\n"
      "v_lshl_or_b32 v244, v236, 12, v237\n"
      "v_add_u32 v245, 31744, v244\n"
      "v_add_u32 v246, 63488, v244\n"
      "v_add_u32 v247, 95232, v244\n"
      "s_getpc_b64 s[46:47]\n"
      ".Lhere_%=:\n"
      "s_lshr_b32 s44, s32, 4\n"
      "s_lshl2_add_u32 s44, s44, s33\n"
      "s_lshl_b32 s44, s44, 10\n"
      "s_sub_u32 s46, s46, .Lhere_%=-.Lbegin_%=\n"
      "s_subb_u32 s47, s47, 0\n"
      "v_add_u32 v238, s44, v242\n"
      "v_min_u32 v238, .Lend_%=-.Lbegin_%=-16, v238\n"
      "global_load_dwordx4 v[192:195], v238, s[46:47]\n"
      "s_and_b32 s44, s34, 1\n"
      "s_lshr_b32 s45, s36, 1\n"
      "s_lshl3_add_u32 s44, s44, s45\n"
      "s_lshl2_add_u32 s44, s44, s35\n"
      "s_lshl2_add_u32 s44, s44, s33\n"
      "s_lshl_b32 s44, s44, 13\n"
      "s_add_u32 s44, s44, 128\n"
      "v_and_b32 v233, 31, v243\n"
      "v_lshl_add_u32 v233, v233, 8, s44\n"
      "s_lshr_b32 s44, s33, 1\n"
      "s_add_u32 s44, s44, s34\n"
      "s_add_u32 s44, s44, 1\n"
      "s_and_b32 s44, s44, 3\n"
      "s_lshl2_add_u32 s44, s35, s44\n"
      "s_lshr_b32 s45, s32, 6\n"
      "s_lshl2_add_u32 s44, s44, s45\n"
      "s_and_b32 s45, s33, 1\n"
      "s_lshl1_add_u32 s44, s44, s45\n"
      "s_lshl_b32 s44, s44, 12\n"
      "s_add_u32 s44, s44, 128\n"
      "v_and_b32 v234, 15, v243\n"
      "v_lshl_add_u32 v234, v234, 8, s44\n"
      "s_bitcmp1_b32 s34, 1\n"
      "s_waitcnt lgkmcnt(0)\n"
      "s_cselect_b64 s[68:69], s[14:15], s[12:13]\n"
      "s_bitcmp1_b32 s34, 1\n"
      "s_cselect_b64 s[8:9], s[10:11], s[8:9]\n"
      "s_and_b32 s45, s34, 1\n"
      "s_lshl2_add_u32 s45, s45, s33\n"
      "s_lshl_b32 s45, s45, 9\n"
      "s_lshl_b32 s76, s36, 19\n"
      "s_add_u32 s45, s45, s76\n"
      "s_add_u32 s8, s8, s45\n"
      "s_addc_u32 s9, s9, 0\n"
      "s_lshr_b32 s39, s32, 6\n"
      "s_and_b32 s45, s35, 2\n"
      "s_xor_b32 s39, s39, s45\n"
      "s_xor_b32 s39, s39, s33\n"
      "s_lshl_b32 s45, s39, 7\n"
      "s_add_u32 s76, s33, 0\n"
      "s_and_b32 s76, s76, 3\n"
      "s_lshl_b32 s76, s76, 17\n"
      "s_or_b32 s76, s76, s45\n"
      "s_add_u32 s77, s33, 1\n"
      "s_and_b32 s77, s77, 3\n"
      "s_lshl_b32 s77, s77, 17\n"
      "s_or_b32 s77, s77, s45\n"
      "s_add_u32 s78, s33, 2\n"
      "s_and_b32 s78, s78, 3\n"
      "s_lshl_b32 s78, s78, 17\n"
      "s_or_b32 s78, s78, s45\n"
      "s_add_u32 s79, s33, 3\n"
      "s_and_b32 s79, s79, 3\n"
      "s_lshl_b32 s79, s79, 17\n"
      "s_or_b32 s79, s79, s45\n"
      "v_mov_b32 v239, 0\n"
      "global_load_dword v199, v239, s[8:9]\n"
      "s_add_u32 s58, s18, 4718592\n"
      "s_addc_u32 s59, s19, 0\n"
      "global_load_dword v235, v239, s[58:59] sc1\n"
      "s_lshl2_add_u32 s43, s34, s35\n"
      "s_mul_i32 s43, s43, 266240\n"
      "s_add_u32 s43, s43, 262144\n"
      "s_lshl2_add_u32 s44, s36, s35\n"
      "s_mul_i32 s44, s44, 147712\n"
      "s_add_u32 s44, s44, 6438912\n"
      "s_bitcmp1_b32 s33, 0\n"
      "s_cselect_b32 s43, s43, s44\n"
      "s_cselect_b64 s[46:47], s[22:23], s[24:25]\n"
      "s_cselect_b64 s[70:71], s[28:29], s[26:27]\n"
      "s_lshl_b32 s44, s36, 17\n"
      "s_mul_i32 s45, s32, 7168\n"
      "s_bitcmp1_b32 s33, 1\n"
      "s_cselect_b64 s[46:47], s[46:47], s[18:19]\n"
      "s_cselect_b32 s43, 0, s43\n"
      "s_cselect_b32 s84, 0x380, 0\n"
      "v_lshlrev_b32 v238, 7, v243\n"
      "v_and_b32 v238, s84, v238\n"
      "v_add_u32 v238, s43, v238\n"
      "global_load_dword v197, v238, s[46:47] sc1\n"
      "s_cmp_eq_u32 s33, 2\n"
      "s_cselect_b64 s[70:71], s[30:31], s[70:71]\n"
      "s_cmp_eq_u32 s33, 3\n"
      "s_cselect_b64 s[70:71], s[20:21], s[70:71]\n"
      "s_cselect_b32 s44, s44, s45\n"
      "v_mov_b32 v239, s44\n"
      "global_load_dword v198, v239, s[70:71] sc1\n"
      "s_sleep 8\n"
      "global_load_dword v200, v234, s[68:69]\n"
      "global_load_dword v201, v233, s[16:17]\n"
      "s_sleep 4\n"
      "s_bitcmp1_b32 s34, 1\n"
      "s_cselect_b64 s[46:47], s[14:15], s[12:13]\n"
      "s_min_u32 s44, s33, 2\n"
      "s_add_u32 s44, s44, s34\n"
      "s_add_u32 s44, s44, 2\n"
      "s_and_b32 s44, s44, 3\n"
      "s_lshl2_add_u32 s44, s35, s44\n"
      "s_lshr_b32 s45, s32, 6\n"
      "s_lshl2_add_u32 s44, s44, s45\n"
      "s_lshl_b32 s44, s44, 13\n"
      "v_lshl_add_u32 v238, v243, 7, s44\n"
      "s_cmp_eq_u32 s33, 0\n"
      "s_cselect_b32 s44, 4096, 0\n"
      "v_or_b32 v238, s44, v238\n"
      "s_cmp_eq_u32 s33, 3\n"
      "s_cbranch_scc1 .Ls_e1_%=\n"
      "global_load_dword v196, v238, s[46:47]\n"
      ".Ls_e1_%=:\n"
      "s_xor_b32 s45, s76, 0\n"
      "s_add_u32 s46, s8, s45\n"
      "s_addc_u32 s47, s9, 0\n"
      "s_add_u32 m0, s41, 0\n"
      "s_nop 0\n"
      "global_load_lds_dwordx4 v244, s[46:47] offset:0\n"
      "global_load_lds_dwordx4 v245, s[46:47] offset:1024\n"
      "global_load_lds_dwordx4 v246, s[46:47] offset:2048\n"
      "global_load_lds_dwordx4 v247, s[46:47] offset:3072\n"
      "s_xor_b32 s45, s77, 0\n"
      "s_add_u32 s46, s8, s45\n"
      "s_addc_u32 s47, s9, 0\n"
      "s_add_u32 m0, s41, 4096\n"
      "s_nop 0\n"
      "global_load_lds_dwordx4 v244, s[46:47] offset:0\n"
      "global_load_lds_dwordx4 v245, s[46:47] offset:1024\n"
      "global_load_lds_dwordx4 v246, s[46:47] offset:2048\n"
      "global_load_lds_dwordx4 v247, s[46:47] offset:3072\n"
      "s_xor_b32 s45, s78, 0\n"
      "s_add_u32 s46, s8, s45\n"
      "s_addc_u32 s47, s9, 0\n"
      "s_add_u32 m0, s41, 8192\n"
      "s_nop 0\n"
      "global_load_lds_dwordx4 v244, s[46:47] offset:0\n"
      "global_load_lds_dwordx4 v245, s[46:47] offset:1024\n"
      "global_load_lds_dwordx4 v246, s[46:47] offset:2048\n"
      "global_load_lds_dwordx4 v247, s[46:47] offset:3072\n"
      "v_and_b32 v238, 15, v243\n"
      "v_lshrrev_b32 v239, 4, v243\n"
      "v_xor_b32 v239, v239, v238\n"
      "v_and_b32 v239, 7, v239\n"
      "v_lshlrev_b32 v239, 4, v239\n"
      "v_lshl_or_b32 v238, v238, 7, v239\n"
      "v_add_u32 v248, s41, v238\n"
      "v_xor_b32 v249, 64, v248\n"
      "s_lshl2_add_u32 s43, s34, s35\n"
      "s_lshr_b32 s45, s39, 1\n"
      "s_lshl1_add_u32 s45, s33, s45\n"
      "s_lshl_b32 s45, s45, 15\n"
      "s_mul_i32 s44, s43, 266240\n"
      "s_add_u32 s44, s44, 4096\n"
      "s_add_u32 s14, s18, s44\n"
      "s_addc_u32 s15, s19, 0\n"
      "s_add_u32 s10, s14, s45\n"
      "s_addc_u32 s11, s15, 0\n"
      "s_xor_b32 s45, s45, 0x8000\n"
      "s_add_u32 s14, s14, s45\n"
      "s_addc_u32 s15, s15, 0\n"
      "s_and_b32 s44, s39, 1\n"
      "s_lshl_b32 s45, s44, 14\n"
      "v_add_u32 v240, s45, v242\n"
      "v_xor_b32 v241, 0x4000, v240\n"
      "s_add_u32 s72, s10, 0x2000\n"
      "s_addc_u32 s73, s11, 0\n"
      "s_add_u32 s74, s14, 0x2000\n"
      "s_addc_u32 s75, s15, 0\n"
      "s_lshl2_add_u32 s44, s43, s33\n"
      "s_lshl_b32 s44, s44, 12\n"
      "s_add_u32 s44, s44, 1048576\n"
      "v_and_b32 v253, 7, v243\n"
      "v_lshl_add_u32 v253, v253, 9, s44\n"
      "s_lshl2_add_u32 s43, s36, s35\n"
      "s_mul_i32 s43, s43, 147712\n"
      "s_mul_i32 s44, s33, 3072\n"
      "s_add_u32 s43, s43, s44\n"
      "s_add_u32 s43, s43, 6291456\n"
      "s_add_u32 s60, s18, s43\n"
      "s_addc_u32 s61, s19, 0\n"
      "s_mul_i32 s44, s34, 36864\n"
      "s_add_u32 s62, s60, s44\n"
      "s_addc_u32 s63, s61, 0\n"
      "s_lshr_b32 s43, s34, 1\n"
      "s_lshl2_add_u32 s43, s43, s35\n"
      "s_lshl_b32 s43, s43, 12\n"
      "s_add_u32 s43, s43, 4096\n"
      "v_lshl_add_u32 v204, v243, 6, s43\n"
      "s_lshr_b32 s80, s32, 4\n"
      "s_bfe_u32 s43, s32, 0x10002\n"
      "s_lshl1_add_u32 s80, s80, s43\n"
      "s_and_b32 s81, s32, 3\n"
      "s_cmp_lt_u32 s80, 28\n"
      "s_cselect_b32 s86, -1, 0\n"
      "s_lshr_b32 s83, s33, 1\n"
      "s_lshl1_add_u32 s83, s81, s83\n"
      "v_add_u32 v250, 0x10000, v248\n"
      "v_add_u32 v251, 0x10000, v249\n"
      "s_xor_b32 s45, s79, 0\n"
      "s_add_u32 s46, s8, s45\n"
      "s_addc_u32 s47, s9, 0\n"
      "s_add_u32 m0, s41, 12288\n"
      "s_nop 0\n"
      "global_load_lds_dwordx4 v244, s[46:47] offset:0\n"
      "global_load_lds_dwordx4 v245, s[46:47] offset:1024\n"
      "global_load_lds_dwordx4 v246, s[46:47] offset:2048\n"
      "global_load_lds_dwordx4 v247, s[46:47] offset:3072\n"
      "s_xor_b32 s45, s76, 128\n"
      "s_add_u32 s46, s8, s45\n"
      "s_addc_u32 s47, s9, 0\n"
      "s_add_u32 m0, s41, 65536\n"
      "s_nop 0\n"
      "global_load_lds_dwordx4 v244, s[46:47] offset:0\n"
      "global_load_lds_dwordx4 v245, s[46:47] offset:1024\n"
      "global_load_lds_dwordx4 v246, s[46:47] offset:2048\n"
      "global_load_lds_dwordx4 v247, s[46:47] offset:3072\n"
      "s_waitcnt vmcnt(16)\n"
      "ds_read_b128 a[0:3], v248 offset:0\n"
      "ds_read_b128 a[4:7], v248 offset:2048\n"
      "ds_read_b128 a[32:35], v249 offset:0\n"
      "ds_read_b128 a[36:39], v249 offset:2048\n"
      "s_xor_b32 s45, s77, 128\n"
      "s_add_u32 s46, s8, s45\n"
      "s_addc_u32 s47, s9, 0\n"
      "s_add_u32 m0, s41, 69632\n"
      "s_nop 0\n"
      "global_load_lds_dwordx4 v244, s[46:47] offset:0\n"
      "global_load_lds_dwordx4 v245, s[46:47] offset:1024\n"
      "global_load_lds_dwordx4 v246, s[46:47] offset:2048\n"
      "global_load_lds_dwordx4 v247, s[46:47] offset:3072\n"
      "s_waitcnt vmcnt(16)\n"
      "ds_read_b128 a[8:11], v248 offset:4096\n"
      "ds_read_b128 a[12:15], v248 offset:6144\n"
      "ds_read_b128 a[40:43], v249 offset:4096\n"
      "ds_read_b128 a[44:47], v249 offset:6144\n"
      "s_xor_b32 s45, s78, 128\n"
      "s_add_u32 s46, s8, s45\n"
      "s_addc_u32 s47, s9, 0\n"
      "s_add_u32 m0, s41, 73728\n"
      "s_nop 0\n"
      "global_load_lds_dwordx4 v244, s[46:47] offset:0\n"
      "global_load_lds_dwordx4 v245, s[46:47] offset:1024\n"
      "global_load_lds_dwordx4 v246, s[46:47] offset:2048\n"
      "global_load_lds_dwordx4 v247, s[46:47] offset:3072\n"
      "s_waitcnt vmcnt(16)\n"
      "ds_read_b128 a[16:19], v248 offset:8192\n"
      "ds_read_b128 a[20:23], v248 offset:10240\n"
      "ds_read_b128 a[48:51], v249 offset:8192\n"
      "ds_read_b128 a[52:55], v249 offset:10240\n"
      "v_readfirstlane_b32 s37, v235\n"
      "s_nop 1\n"
      "s_mul_i32 s37, s37, 0x9E3779B1\n"
      "s_add_u32 s37, s37, 0x7F4A7C15\n"
      "s_mov_b32 s38, 0x8000\n"
      "s_waitcnt lgkmcnt(8)\n"
      "s_xor_b32 s45, s79, 128\n"
      "s_add_u32 s46, s8, s45\n"
      "s_addc_u32 s47, s9, 0\n"
      "s_add_u32 m0, s41, 0\n"
      "s_nop 0\n"
      "global_load_lds_dwordx4 v244, s[46:47] offset:0\n"
      "global_load_lds_dwordx4 v245, s[46:47] offset:1024\n"
      "global_load_lds_dwordx4 v246, s[46:47] offset:2048\n"
      "global_load_lds_dwordx4 v247, s[46:47] offset:3072\n"
      "s_barrier\n"
      "s_waitcnt vmcnt(16)\n"
      "ds_read_b128 a[24:27], v248 offset:12288\n"
      "ds_read_b128 a[28:31], v248 offset:14336\n"
      "ds_read_b128 a[56:59], v249 offset:12288\n"
      "ds_read_b128 a[60:63], v249 offset:14336\n"
      "s_waitcnt lgkmcnt(8)\n"
      "s_xor_b32 s45, s76, 256\n"
      "s_add_u32 s46, s8, s45\n"
      "s_addc_u32 s47, s9, 0\n"
      "s_add_u32 m0, s41, 4096\n"
      "s_nop 0\n"
      "global_load_lds_dwordx4 v244, s[46:47] offset:0\n"
      "global_load_lds_dwordx4 v245, s[46:47] offset:1024\n"
      "global_load_lds_dwordx4 v246, s[46:47] offset:2048\n"
      "global_load_lds_dwordx4 v247, s[46:47] offset:3072\n"
      "s_waitcnt vmcnt(16)\n"
      "ds_read_b128 a[64:67], v250 offset:0\n"
      "ds_read_b128 a[68:71], v250 offset:2048\n"
      "ds_read_b128 a[96:99], v251 offset:0\n"
      "ds_read_b128 a[100:103], v251 offset:2048\n"
      "s_waitcnt lgkmcnt(8)\n"
      "s_xor_b32 s45, s77, 256\n"
      "s_add_u32 s46, s8, s45\n"
      "s_addc_u32 s47, s9, 0\n"
      "s_add_u32 m0, s41, 8192\n"
      "s_nop 0\n"
      "global_load_lds_dwordx4 v244, s[46:47] offset:0\n"
      "global_load_lds_dwordx4 v245, s[46:47] offset:1024\n"
      "global_load_lds_dwordx4 v246, s[46:47] offset:2048\n"
      "global_load_lds_dwordx4 v247, s[46:47] offset:3072\n"
      "s_waitcnt vmcnt(16)\n"
      "ds_read_b128 a[72:75], v250 offset:4096\n"
      "ds_read_b128 a[76:79], v250 offset:6144\n"
      "ds_read_b128 a[104:107], v251 offset:4096\n"
      "ds_read_b128 a[108:111], v251 offset:6144\n"
      "s_waitcnt lgkmcnt(8)\n"
      "s_xor_b32 s45, s78, 256\n"
      "s_add_u32 s46, s8, s45\n"
      "s_addc_u32 s47, s9, 0\n"
      "s_add_u32 m0, s41, 12288\n"
      "s_nop 0\n"
      "global_load_lds_dwordx4 v244, s[46:47] offset:0\n"
      "global_load_lds_dwordx4 v245, s[46:47] offset:1024\n"
      "global_load_lds_dwordx4 v246, s[46:47] offset:2048\n"
      "global_load_lds_dwordx4 v247, s[46:47] offset:3072\n"
      "s_waitcnt vmcnt(16)\n"
      "ds_read_b128 a[80:83], v250 offset:8192\n"
      "ds_read_b128 a[84:87], v250 offset:10240\n"
      "ds_read_b128 a[112:115], v251 offset:8192\n"
      "ds_read_b128 a[116:119], v251 offset:10240\n"
      "s_waitcnt vmcnt(12)\n"
      "ds_read_b128 a[88:91], v248 offset:0\n"
      "ds_read_b128 a[92:95], v248 offset:2048\n"
      "ds_read_b128 a[120:123], v249 offset:0\n"
      "ds_read_b128 a[124:127], v249 offset:2048\n"
      "global_load_dword v252, v253, s[58:59] offset:0 sc1\n"
      "s_waitcnt lgkmcnt(12)\n"
      "s_xor_b32 s45, s79, 256\n"
      "s_add_u32 s46, s8, s45\n"
      "s_addc_u32 s47, s9, 0\n"
      "s_add_u32 m0, s41, 65536\n"
      "s_nop 0\n"
      "global_load_lds_dwordx4 v244, s[46:47] offset:0\n"
      "global_load_lds_dwordx4 v245, s[46:47] offset:1024\n"
      "global_load_lds_dwordx4 v246, s[46:47] offset:2048\n"
      "global_load_lds_dwordx4 v247, s[46:47] offset:3072\n"
      "s_waitcnt lgkmcnt(8)\n"
      "s_xor_b32 s45, s76, 384\n"
      "s_add_u32 s46, s8, s45\n"
      "s_addc_u32 s47, s9, 0\n"
      "s_add_u32 m0, s41, 69632\n"
      "s_nop 0\n"
      "global_load_lds_dwordx4 v244, s[46:47] offset:0\n"
      "global_load_lds_dwordx4 v245, s[46:47] offset:1024\n"
      "global_load_lds_dwordx4 v246, s[46:47] offset:2048\n"
      "global_load_lds_dwordx4 v247, s[46:47] offset:3072\n"
      "s_waitcnt vmcnt(17)\n"
      "ds_read_b128 a[128:131], v248 offset:4096\n"
      "ds_read_b128 a[132:135], v248 offset:6144\n"
      "ds_read_b128 a[160:163], v249 offset:4096\n"
      "ds_read_b128 a[164:167], v249 offset:6144\n"
      "s_waitcnt vmcnt(13)\n"
      "ds_read_b128 a[136:139], v248 offset:8192\n"
      "ds_read_b128 a[140:143], v248 offset:10240\n"
      "ds_read_b128 a[168:171], v249 offset:8192\n"
      "ds_read_b128 a[172:175], v249 offset:10240\n"
      "s_barrier\n"
      "s_waitcnt vmcnt(8)\n"
      "v_cmp_ne_u32 vcc, s37, v252\n"
      "s_nop 1\n"
      "s_cbranch_vccnz .Ls_s3_%=\n"
      ".Ls_o4_%=:\n"
      "global_load_dwordx2 v[206:207], v204, s[58:59] sc1\n"
      "global_load_dwordx4 v[128:131], v240, s[10:11] offset:-4096 sc1\n"
      "global_load_dwordx4 v[132:135], v240, s[10:11] offset:-3072 sc1\n"
      "global_load_dwordx4 v[136:139], v240, s[10:11] offset:-2048 sc1\n"
      "global_load_dwordx4 v[140:143], v240, s[72:73] offset:-4096 sc1\n"
      "global_load_dwordx4 v[144:147], v240, s[72:73] offset:-3072 sc1\n"
      "global_load_dwordx4 v[148:151], v240, s[72:73] offset:-2048 sc1\n"
      "s_waitcnt lgkmcnt(12)\n"
      "s_xor_b32 s45, s77, 384\n"
      "s_add_u32 s46, s8, s45\n"
      "s_addc_u32 s47, s9, 0\n"
      "s_add_u32 m0, s41, 73728\n"
      "s_nop 0\n"
      "global_load_lds_dwordx4 v244, s[46:47] offset:0\n"
      "global_load_lds_dwordx4 v245, s[46:47] offset:1024\n"
      "global_load_lds_dwordx4 v246, s[46:47] offset:2048\n"
      "global_load_lds_dwordx4 v247, s[46:47] offset:3072\n"
      "s_waitcnt vmcnt(9)\n"
      "v_mfma_f32_16x16x32_bf16 v[0:3], v[128:131], a[0:3], 0\n"
      "global_load_dwordx4 v[152:155], v241, s[10:11] offset:-4096 sc1\n"
      "v_cmp_ne_u32 vcc, s37, v207\n"
      "s_nop 1\n"
      "s_cbranch_vccnz .Ls_s6_%=\n"
      ".Ls_o7_%=:\n"
      "v_mfma_f32_16x16x32_bf16 v[4:7], v[128:131], a[4:7], 0\n"
      "v_max_u32_dpp v206, v206, v206 quad_perm:[1,0,3,2] row_mask:0xf bank_mask:0xf\n"
      "s_nop 1\n"
      "v_max_u32_dpp v206, v206, v206 quad_perm:[2,3,0,1] row_mask:0xf bank_mask:0xf\n"
      "v_mfma_f32_16x16x32_bf16 v[8:11], v[128:131], a[8:11], 0\n"
      "global_load_dwordx4 v[156:159], v241, s[10:11] offset:-3072 sc1\n"
      "v_max_u32_dpp v206, v206, v206 row_half_mirror row_mask:0xf bank_mask:0xf\n"
      "v_mfma_f32_16x16x32_bf16 v[12:15], v[128:131], a[12:15], 0\n"
      "s_nop 0\n"
      "v_max_u32_dpp v206, v206, v206 row_mirror row_mask:0xf bank_mask:0xf\n"
      "v_mfma_f32_16x16x32_bf16 v[16:19], v[128:131], a[16:19], 0\n"
      "global_load_dwordx4 v[160:163], v241, s[10:11] offset:-2048 sc1\n"
      "v_mov_b32 v205, v206\n"
      "v_mfma_f32_16x16x32_bf16 v[20:23], v[128:131], a[20:23], 0\n"
      "s_nop 0\n"
      "v_permlane16_swap_b32 v206, v205\n"
      "v_mfma_f32_16x16x32_bf16 v[24:27], v[128:131], a[24:27], 0\n"
      "global_load_dwordx4 v[164:167], v241, s[72:73] offset:-4096 sc1\n"
      "v_max_u32 v206, v206, v205\n"
      "v_mfma_f32_16x16x32_bf16 v[28:31], v[128:131], a[28:31], 0\n"
      "v_mov_b32 v205, v206\n"
      "s_nop 1\n"
      "v_permlane32_swap_b32 v206, v205\n"
      "s_waitcnt vmcnt(12)\n"
      "v_mfma_f32_16x16x32_bf16 v[32:35], v[132:135], a[0:3], 0\n"
      "global_load_dwordx4 v[168:171], v241, s[72:73] offset:-3072 sc1\n"
      "v_max_u32 v206, v206, v205\n"
      "v_mfma_f32_16x16x32_bf16 v[36:39], v[132:135], a[4:7], 0\n"
      "v_readfirstlane_b32 s40, v206\n"
      "s_nop 1\n"
      "s_andn2_b32 s42, 0x7ff, s40\n"
      "v_mfma_f32_16x16x32_bf16 v[40:43], v[132:135], a[8:11], 0\n"
      "global_load_dwordx4 v[172:175], v241, s[72:73] offset:-2048 sc1\n"
      "s_lshl2_add_u32 s43, s32, s33\n"
      "s_cmp_eq_u32 s43, 960\n"
      "s_cbranch_scc0 .Ls_e8_%=\n"
      "s_lshl_b32 s43, s42, 10\n"
      "v_add_u32 v208, s43, v242\n"
      "global_load_dwordx4 v[216:219], v208, s[16:17]\n"
      "global_load_dwordx4 v[220:223], v242, s[24:25]\n"
      ".Ls_e8_%=:\n"
      "v_mfma_f32_16x16x32_bf16 v[44:47], v[132:135], a[12:15], 0\n"
      "v_mfma_f32_16x16x32_bf16 v[48:51], v[132:135], a[16:19], 0\n"
      "global_load_dwordx4 v[176:179], v240, s[14:15] offset:-4096 sc1\n"
      "v_mfma_f32_16x16x32_bf16 v[52:55], v[132:135], a[20:23], 0\n"
      "v_mfma_f32_16x16x32_bf16 v[56:59], v[132:135], a[24:27], 0\n"
      "global_load_dwordx4 v[180:183], v240, s[14:15] offset:-3072 sc1\n"
      "v_mfma_f32_16x16x32_bf16 v[60:63], v[132:135], a[28:31], 0\n"
      "s_waitcnt vmcnt(14)\n"
      "v_mfma_f32_16x16x32_bf16 v[64:67], v[136:139], a[0:3], 0\n"
      "global_load_dwordx4 v[184:187], v240, s[14:15] offset:-2048 sc1\n"
      "global_load_dwordx4 v[188:191], v240, s[74:75] offset:-4096 sc1\n"
      "v_mfma_f32_16x16x32_bf16 v[68:71], v[136:139], a[4:7], 0\n"
      "global_load_dwordx4 v[128:131], v240, s[74:75] offset:-3072 sc1\n"
      "global_load_dwordx4 v[132:135], v240, s[74:75] offset:-2048 sc1\n"
      "v_mfma_f32_16x16x32_bf16 v[72:75], v[136:139], a[8:11], 0\n"
      "s_waitcnt lgkmcnt(8)\n"
      "s_xor_b32 s70, s78, 384\n"
      "s_add_u32 s70, s8, s70\n"
      "s_addc_u32 s71, s9, 0\n"
      "s_add_u32 m0, s41, 0\n"
      "v_mfma_f32_16x16x32_bf16 v[76:79], v[136:139], a[12:15], 0\n"
      "global_load_lds_dwordx4 v244, s[70:71] offset:0\n"
      "v_mfma_f32_16x16x32_bf16 v[80:83], v[136:139], a[16:19], 0\n"
      "v_mfma_f32_16x16x32_bf16 v[84:87], v[136:139], a[20:23], 0\n"
      "global_load_lds_dwordx4 v245, s[70:71] offset:1024\n"
      "ds_read_b128 a[144:147], v248 offset:12288\n"
      "v_mfma_f32_16x16x32_bf16 v[88:91], v[136:139], a[24:27], 0\n"
      "ds_read_b128 a[148:151], v248 offset:14336\n"
      "ds_read_b128 a[176:179], v249 offset:12288\n"
      "v_mfma_f32_16x16x32_bf16 v[92:95], v[136:139], a[28:31], 0\n"
      "global_load_lds_dwordx4 v246, s[70:71] offset:2048\n"
      "global_load_dwordx4 v[136:139], v241, s[14:15] offset:-4096 sc1\n"
      "v_mfma_f32_16x16x32_bf16 v[0:3], v[140:143], a[32:35], v[0:3]\n"
      "ds_read_b128 a[180:183], v249 offset:14336\n"
      "ds_read_b128 a[152:155], v250 offset:0\n"
      "v_mfma_f32_16x16x32_bf16 v[4:7], v[140:143], a[36:39], v[4:7]\n"
      "global_load_lds_dwordx4 v247, s[70:71] offset:3072\n"
      "ds_read_b128 a[156:159], v250 offset:2048\n"
      "v_mfma_f32_16x16x32_bf16 v[8:11], v[140:143], a[40:43], v[8:11]\n"
      "s_waitcnt lgkmcnt(10)\n"
      "s_xor_b32 s70, s79, 384\n"
      "s_add_u32 s70, s8, s70\n"
      "s_addc_u32 s71, s9, 0\n"
      "s_add_u32 m0, s41, 4096\n"
      "v_mfma_f32_16x16x32_bf16 v[12:15], v[140:143], a[44:47], v[12:15]\n"
      "global_load_lds_dwordx4 v244, s[70:71] offset:0\n"
      "ds_read_b128 a[184:187], v251 offset:0\n"
      "v_mfma_f32_16x16x32_bf16 v[16:19], v[140:143], a[48:51], v[16:19]\n"
      "ds_read_b128 a[188:191], v251 offset:2048\n"
      "ds_read_b128 a[192:195], v250 offset:4096\n"
      "v_mfma_f32_16x16x32_bf16 v[20:23], v[140:143], a[52:55], v[20:23]\n"
      "global_load_lds_dwordx4 v245, s[70:71] offset:1024\n"
      "ds_read_b128 a[196:199], v250 offset:6144\n"
      "v_mfma_f32_16x16x32_bf16 v[24:27], v[140:143], a[56:59], v[24:27]\n"
      "ds_read_b128 a[224:227], v251 offset:4096\n"
      "ds_read_b128 a[228:231], v251 offset:6144\n"
      "v_mfma_f32_16x16x32_bf16 v[28:31], v[140:143], a[60:63], v[28:31]\n"
      "s_waitcnt vmcnt(23)\n"
      "global_load_lds_dwordx4 v246, s[70:71] offset:2048\n"
      "global_load_dwordx4 v[140:143], v241, s[74:75] offset:-4096 sc1\n"
      "v_mfma_f32_16x16x32_bf16 v[32:35], v[144:147], a[32:35], v[32:35]\n"
      "s_waitcnt vmcnt(21)\n"
      "ds_read_b128 a[200:203], v250 offset:8192\n"
      "v_mfma_f32_16x16x32_bf16 v[36:39], v[144:147], a[36:39], v[36:39]\n"
      "global_load_lds_dwordx4 v247, s[70:71] offset:3072\n"
      "ds_read_b128 a[204:207], v250 offset:10240\n"
      "v_mfma_f32_16x16x32_bf16 v[40:43], v[144:147], a[40:43], v[40:43]\n"
      "ds_read_b128 a[232:235], v251 offset:8192\n"
      "ds_read_b128 a[236:239], v251 offset:10240\n"
      "v_mfma_f32_16x16x32_bf16 v[44:47], v[144:147], a[44:47], v[44:47]\n"
      "v_mfma_f32_16x16x32_bf16 v[48:51], v[144:147], a[48:51], v[48:51]\n"
      "v_mfma_f32_16x16x32_bf16 v[52:55], v[144:147], a[52:55], v[52:55]\n"
      "v_mfma_f32_16x16x32_bf16 v[56:59], v[144:147], a[56:59], v[56:59]\n"
      "v_mfma_f32_16x16x32_bf16 v[60:63], v[144:147], a[60:63], v[60:63]\n"
      "global_load_dwordx4 v[144:147], v241, s[14:15] offset:-3072 sc1\n"
      "v_mfma_f32_16x16x32_bf16 v[64:67], v[148:151], a[32:35], v[64:67]\n"
      "v_mfma_f32_16x16x32_bf16 v[68:71], v[148:151], a[36:39], v[68:71]\n"
      "v_mfma_f32_16x16x32_bf16 v[72:75], v[148:151], a[40:43], v[72:75]\n"
      "v_mfma_f32_16x16x32_bf16 v[76:79], v[148:151], a[44:47], v[76:79]\n"
      "v_mfma_f32_16x16x32_bf16 v[80:83], v[148:151], a[48:51], v[80:83]\n"
      "v_mfma_f32_16x16x32_bf16 v[84:87], v[148:151], a[52:55], v[84:87]\n"
      "v_mfma_f32_16x16x32_bf16 v[88:91], v[148:151], a[56:59], v[88:91]\n"
      "v_mfma_f32_16x16x32_bf16 v[92:95], v[148:151], a[60:63], v[92:95]\n"
      "s_waitcnt vmcnt(21)\n"
      "global_load_dwordx4 v[148:151], v241, s[74:75] offset:-3072 sc1\n"
      "s_waitcnt vmcnt(7)\n"
      "ds_read_b128 a[208:211], v248 offset:0\n"
      "v_mfma_f32_16x16x32_bf16 v[0:3], v[152:155], a[64:67], v[0:3]\n"
      "ds_read_b128 a[212:215], v248 offset:2048\n"
      "ds_read_b128 a[240:243], v249 offset:0\n"
      "v_mfma_f32_16x16x32_bf16 v[4:7], v[152:155], a[68:71], v[4:7]\n"
      "ds_read_b128 a[244:247], v249 offset:2048\n"
      "v_mfma_f32_16x16x32_bf16 v[8:11], v[152:155], a[72:75], v[8:11]\n"
      "v_mfma_f32_16x16x32_bf16 v[12:15], v[152:155], a[76:79], v[12:15]\n"
      "v_mfma_f32_16x16x32_bf16 v[16:19], v[152:155], a[80:83], v[16:19]\n"
      "v_mfma_f32_16x16x32_bf16 v[20:23], v[152:155], a[84:87], v[20:23]\n"
      "v_mfma_f32_16x16x32_bf16 v[24:27], v[152:155], a[88:91], v[24:27]\n"
      "v_mfma_f32_16x16x32_bf16 v[28:31], v[152:155], a[92:95], v[28:31]\n"
      "global_load_dwordx4 v[152:155], v241, s[14:15] offset:-2048 sc1\n"
      "s_waitcnt vmcnt(3)\n"
      "ds_read_b128 a[216:219], v248 offset:4096\n"
      "v_mfma_f32_16x16x32_bf16 v[32:35], v[156:159], a[64:67], v[32:35]\n"
      "ds_read_b128 a[220:223], v248 offset:6144\n"
      "ds_read_b128 a[248:251], v249 offset:4096\n"
      "v_mfma_f32_16x16x32_bf16 v[36:39], v[156:159], a[68:71], v[36:39]\n"
      "ds_read_b128 a[252:255], v249 offset:6144\n"
      "v_mfma_f32_16x16x32_bf16 v[40:43], v[156:159], a[72:75], v[40:43]\n"
      "v_mfma_f32_16x16x32_bf16 v[44:47], v[156:159], a[76:79], v[44:47]\n"
      "v_mfma_f32_16x16x32_bf16 v[48:51], v[156:159], a[80:83], v[48:51]\n"
      "s_lshl_b32 s54, s42, 10\n"
      "s_lshl_b32 s55, s83, 7\n"
      "s_add_u32 s54, s54, s55\n"
      "v_mfma_f32_16x16x32_bf16 v[52:55], v[156:159], a[84:87], v[52:55]\n"
      "v_and_b32 v224, 48, v243\n"
      "v_add_u32 v224, s54, v224\n"
      "v_mfma_f32_16x16x32_bf16 v[56:59], v[156:159], a[88:91], v[56:59]\n"
      "global_load_dwordx4 v[192:195], v224, s[16:17]\n"
      "global_load_dwordx4 v[196:199], v224, s[16:17] offset:64\n"
      "v_mfma_f32_16x16x32_bf16 v[60:63], v[156:159], a[92:95], v[60:63]\n"
      "global_load_dwordx4 v[156:159], v241, s[74:75] offset:-2048 sc1\n"
      "v_mfma_f32_16x16x32_bf16 v[64:67], v[160:163], a[64:67], v[64:67]\n"
      "v_mfma_f32_16x16x32_bf16 v[68:71], v[160:163], a[68:71], v[68:71]\n"
      "v_mfma_f32_16x16x32_bf16 v[72:75], v[160:163], a[72:75], v[72:75]\n"
      "v_mfma_f32_16x16x32_bf16 v[76:79], v[160:163], a[76:79], v[76:79]\n"
      "v_mfma_f32_16x16x32_bf16 v[80:83], v[160:163], a[80:83], v[80:83]\n"
      "v_mfma_f32_16x16x32_bf16 v[84:87], v[160:163], a[84:87], v[84:87]\n"
      "v_mfma_f32_16x16x32_bf16 v[88:91], v[160:163], a[88:91], v[88:91]\n"
      "s_getpc_b64 s[46:47]\n"
      ".Ls_c9_%=:\n"
      "s_lshr_b32 s44, s32, 4\n"
      "s_lshl2_add_u32 s44, s44, s33\n"
      "s_lshl_b32 s44, s44, 10\n"
      "s_sub_u32 s46, s46, .Ls_c9_%=-.Lbegin_%=\n"
      "s_subb_u32 s47, s47, 0\n"
      "v_add_u32 v238, s44, v242\n"
      "v_min_u32 v238, .Lend_%=-.Lbegin_%=-16, v238\n"
      "global_load_dword v205, v238, s[46:47]\n"
      "v_mfma_f32_16x16x32_bf16 v[92:95], v[160:163], a[92:95], v[92:95]\n"
      "s_min_u32 s43, s80, 27\n"
      "v_lshrrev_b32 v202, 3, v243\n"
      "v_and_b32 v203, 0x70, v242\n"
      "s_movk_i32 s44, 3584\n"
      "v_mad_u32_u24 v200, v202, s44, v203\n"
      "v_add_u32 v201, 27648, v200\n"
      "v_add_u32 v202, 55296, v200\n"
      "v_add_u32 v203, 82944, v200\n"
      "s_bitcmp1_b32 s33, 0\n"
      "s_cselect_b64 s[70:71], s[28:29], s[26:27]\n"
      "s_mul_i32 s44, s83, 229376\n"
      "s_lshl_b32 s45, s43, 7\n"
      "s_add_u32 s44, s44, s45\n"
      "s_add_u32 s70, s70, s44\n"
      "s_addc_u32 s71, s71, 0\n"
      "s_lshl_b32 s45, s33, 13\n"
      "s_add_u32 m0, s45, 131072\n"
      "v_mfma_f32_16x16x32_bf16 v[0:3], v[164:167], a[96:99], v[0:3]\n"
      "global_load_lds_dwordx4 v200, s[70:71] offset:0\n"
      "s_lshl2_add_u32 s43, s32, s33\n"
      "s_cmp_eq_u32 s43, 960\n"
      "s_cbranch_scc0 .Ls_e10_%=\n"
      "s_ashr_i32 s43, s40, 31\n"
      "s_not_b32 s43, s43\n"
      "s_or_b32 s43, s43, 0x80000000\n"
      "s_xor_b32 s43, s43, s40\n"
      "s_and_b32 s43, s43, 0xffff0000\n"
      "v_lshlrev_b32 v208, 16, v216\n"
      "v_lshlrev_b32 v209, 16, v220\n"
      "v_and_b32 v216, 0xffff0000, v216\n"
      "v_and_b32 v220, 0xffff0000, v220\n"
      "v_mul_f32 v208, v208, v209\n"
      "v_mul_f32 v210, v216, v220\n"
      "v_cvt_pk_bf16_f32 v208, v208, v210\n"
      "v_lshlrev_b32 v209, 16, v208\n"
      "v_and_b32 v208, 0xffff0000, v208\n"
      "v_add_f32 v211, v208, v209\n"
      "v_lshlrev_b32 v208, 16, v217\n"
      "v_lshlrev_b32 v209, 16, v221\n"
      "v_and_b32 v217, 0xffff0000, v217\n"
      "v_and_b32 v221, 0xffff0000, v221\n"
      "v_mul_f32 v208, v208, v209\n"
      "v_mul_f32 v210, v217, v221\n"
      "v_cvt_pk_bf16_f32 v208, v208, v210\n"
      "v_lshlrev_b32 v209, 16, v208\n"
      "v_and_b32 v208, 0xffff0000, v208\n"
      "v_add_f32 v208, v208, v209\n"
      "v_add_f32 v211, v211, v208\n"
      "v_lshlrev_b32 v208, 16, v218\n"
      "v_lshlrev_b32 v209, 16, v222\n"
      "v_and_b32 v218, 0xffff0000, v218\n"
      "v_and_b32 v222, 0xffff0000, v222\n"
      "v_mul_f32 v208, v208, v209\n"
      "v_mul_f32 v210, v218, v222\n"
      "v_cvt_pk_bf16_f32 v208, v208, v210\n"
      "v_lshlrev_b32 v209, 16, v208\n"
      "v_and_b32 v208, 0xffff0000, v208\n"
      "v_add_f32 v208, v208, v209\n"
      "v_add_f32 v211, v211, v208\n"
      "v_lshlrev_b32 v208, 16, v219\n"
      "v_lshlrev_b32 v209, 16, v223\n"
      "v_and_b32 v219, 0xffff0000, v219\n"
      "v_and_b32 v223, 0xffff0000, v223\n"
      "v_mul_f32 v208, v208, v209\n"
      "v_mul_f32 v210, v219, v223\n"
      "v_cvt_pk_bf16_f32 v208, v208, v210\n"
      "v_lshlrev_b32 v209, 16, v208\n"
      "v_and_b32 v208, 0xffff0000, v208\n"
      "v_add_f32 v208, v208, v209\n"
      "v_add_f32 v211, v211, v208\n"
      "s_nop 1\n"
      "v_add_f32_dpp v211, v211, v211 quad_perm:[1,0,3,2] row_mask:0xf bank_mask:0xf\n"
      "s_nop 1\n"
      "v_add_f32_dpp v211, v211, v211 quad_perm:[2,3,0,1] row_mask:0xf bank_mask:0xf\n"
      "s_nop 1\n"
      "v_add_f32_dpp v211, v211, v211 row_half_mirror row_mask:0xf bank_mask:0xf\n"
      "s_nop 1\n"
      "v_add_f32_dpp v211, v211, v211 row_mirror row_mask:0xf bank_mask:0xf\n"
      "v_mov_b32 v208, v211\n"
      "s_nop 1\n"
      "v_permlane16_swap_b32 v211, v208\n"
      "v_add_f32 v211, v211, v208\n"
      "v_mov_b32 v208, v211\n"
      "s_nop 1\n"
      "v_permlane32_swap_b32 v211, v208\n"
      "v_add_f32 v211, v211, v208\n"
      "v_cvt_pk_bf16_f32 v211, v211, v211\n"
      "v_and_b32 v211, 0xffff0000, v211\n"
      "v_writelane_b32 v211, s43, 1\n"
      "v_mul_f32 v211, 0xbfb8aa3b, v211\n"
      "v_exp_f32 v211, v211\n"
      "s_nop 0\n"
      "v_add_f32 v211, 1.0, v211\n"
      "v_rcp_f32 v211, v211\n"
      "s_nop 0\n"
      "v_cvt_pk_bf16_f32 v211, v211, v211\n"
      "v_and_b32 v211, 0xffff0000, v211\n"
      "s_nop 0\n"
      "v_readfirstlane_b32 s39, v211\n"
      "v_readlane_b32 s84, v211, 1\n"
      "s_nop 0\n"
      "v_mov_b32 v216, s39\n"
      "v_mov_b32 v217, s84\n"
      "v_mov_b32 v218, s37\n"
      "v_mov_b32 v219, s37\n"
      "s_mov_b64 exec, 0xff\n"
      "global_store_dwordx4 v242, v[216:219], s[58:59] offset:1024 sc1\n"
      "s_mov_b64 exec, -1\n"
      ".Ls_e10_%=:\n"
      "v_mfma_f32_16x16x32_bf16 v[4:7], v[164:167], a[100:103], v[4:7]\n"
      "global_load_lds_dwordx4 v201, s[70:71] offset:1024\n"
      "v_mfma_f32_16x16x32_bf16 v[8:11], v[164:167], a[104:107], v[8:11]\n"
      "global_load_lds_dwordx4 v202, s[70:71] offset:2048\n"
      "v_mfma_f32_16x16x32_bf16 v[12:15], v[164:167], a[108:111], v[12:15]\n"
      "global_load_lds_dwordx4 v203, s[70:71] offset:3072\n"
      "v_mfma_f32_16x16x32_bf16 v[16:19], v[164:167], a[112:115], v[16:19]\n"
      "s_add_u32 s70, s70, 114688\n"
      "s_addc_u32 s71, s71, 0\n"
      "s_add_u32 m0, m0, 4096\n"
      "v_mfma_f32_16x16x32_bf16 v[20:23], v[164:167], a[116:119], v[20:23]\n"
      "global_load_lds_dwordx4 v200, s[70:71] offset:0\n"
      "v_mfma_f32_16x16x32_bf16 v[24:27], v[164:167], a[120:123], v[24:27]\n"
      "global_load_lds_dwordx4 v201, s[70:71] offset:1024\n"
      "v_mfma_f32_16x16x32_bf16 v[28:31], v[164:167], a[124:127], v[28:31]\n"
      "global_load_lds_dwordx4 v202, s[70:71] offset:2048\n"
      "v_mfma_f32_16x16x32_bf16 v[32:35], v[168:171], a[96:99], v[32:35]\n"
      "global_load_lds_dwordx4 v203, s[70:71] offset:3072\n"
      "v_mfma_f32_16x16x32_bf16 v[36:39], v[168:171], a[100:103], v[36:39]\n"
      "s_min_u32 s43, s80, 27\n"
      "s_lshr_b32 s44, s33, 1\n"
      "s_lshl1_add_u32 s44, s43, s44\n"
      "s_lshl_b32 s44, s44, 15\n"
      "s_and_b32 s45, s33, 1\n"
      "s_lshl1_add_u32 s45, s81, s45\n"
      "s_lshl_b32 s45, s45, 7\n"
      "s_add_u32 s44, s44, s45\n"
      "v_and_b32 v239, 31, v243\n"
      "v_lshl_add_u32 v239, v239, 10, s44\n"
      "global_load_dword v207, v239, s[30:31]\n"
      "v_mfma_f32_16x16x32_bf16 v[40:43], v[168:171], a[104:107], v[40:43]\n"
      "s_sub_u32 s48, s33, 1\n"
      "s_and_b32 s48, s48, 3\n"
      "s_lshl_b32 s48, s48, 14\n"
      "v_mfma_f32_16x16x32_bf16 v[44:47], v[168:171], a[108:111], v[44:47]\n"
      "s_mov_b32 m0, s41\n"
      "v_lshl_add_u32 v244, v243, 2, s48\n"
      "s_sub_u32 s48, s33, 2\n"
      "v_mfma_f32_16x16x32_bf16 v[48:51], v[168:171], a[112:115], v[48:51]\n"
      "s_and_b32 s48, s48, 3\n"
      "s_lshl_b32 s48, s48, 14\n"
      "v_lshl_add_u32 v245, v243, 2, s48\n"
      "v_mfma_f32_16x16x32_bf16 v[52:55], v[168:171], a[116:119], v[52:55]\n"
      "s_sub_u32 s48, s33, 3\n"
      "s_and_b32 s48, s48, 3\n"
      "s_lshl_b32 s48, s48, 14\n"
      "v_mfma_f32_16x16x32_bf16 v[56:59], v[168:171], a[120:123], v[56:59]\n"
      "v_lshl_add_u32 v246, v243, 2, s48\n"
      "v_lshrrev_b32 v201, 4, v243\n"
      "v_mfma_f32_16x16x32_bf16 v[60:63], v[168:171], a[124:127], v[60:63]\n"
      "v_bfe_u32 v224, v243, 2, 2\n"
      "v_lshl_or_b32 v201, v201, 3, v224\n"
      "v_mfma_f32_16x16x32_bf16 v[64:67], v[172:175], a[96:99], v[64:67]\n"
      "v_and_b32 v224, 3, v243\n"
      "v_lshlrev_b32 v224, 3, v224\n"
      "v_mfma_f32_16x16x32_bf16 v[68:71], v[172:175], a[100:103], v[68:71]\n"
      "v_lshl_or_b32 v201, v201, 7, v224\n"
      "s_mov_b32 s64, 0xffff0000\n"
      "v_mfma_f32_16x16x32_bf16 v[72:75], v[172:175], a[104:107], v[72:75]\n"
      "s_mov_b32 s65, 0xffff0000\n"
      "s_mov_b32 s66, -1\n"
      "s_mov_b32 s67, 0\n"
      "v_mfma_f32_16x16x32_bf16 v[76:79], v[172:175], a[108:111], v[76:79]\n"
      "s_lshl_b32 s54, s33, 13\n"
      "s_add_u32 s54, s54, 131072\n"
      "v_add_u32 v202, s54, v201\n"
      "v_mfma_f32_16x16x32_bf16 v[80:83], v[172:175], a[112:115], v[80:83]\n"
      "s_and_b32 s54, s33, 1\n"
      "s_lshl3_add_u32 s54, s54, s83\n"
      "s_lshl_b32 s54, s54, 8\n"
      "v_mfma_f32_16x16x32_bf16 v[84:87], v[172:175], a[116:119], v[84:87]\n"
      "s_lshl_b32 s55, s80, 13\n"
      "s_add_u32 s55, s55, 262144\n"
      "s_add_u32 s54, s54, s55\n"
      "v_mfma_f32_16x16x32_bf16 v[88:91], v[172:175], a[120:123], v[88:91]\n"
      "v_bfe_u32 v224, v243, 4, 1\n"
      "v_and_b32 v203, 0xf0, v242\n"
      "v_mfma_f32_16x16x32_bf16 v[92:95], v[172:175], a[124:127], v[92:95]\n"
      "global_load_dword v252, v253, s[58:59] offset:128 sc1\n"
      "v_lshl_or_b32 v200, v224, 12, v203\n"
      "v_mfma_f32_16x16x32_bf16 v[0:3], v[176:179], a[128:131], v[0:3]\n"
      "v_add_u32 v200, s54, v200\n"
      "v_lshl_or_b32 v203, v224, 14, v203\n"
      "v_mfma_f32_16x16x32_bf16 v[4:7], v[176:179], a[132:135], v[4:7]\n"
      "s_waitcnt lgkmcnt(15)\n"
      "v_mfma_f32_16x16x32_bf16 v[8:11], v[176:179], a[136:139], v[8:11]\n"
      "v_mfma_f32_16x16x32_bf16 v[12:15], v[176:179], a[140:143], v[12:15]\n"
      "v_mfma_f32_16x16x32_bf16 v[16:19], v[176:179], a[144:147], v[16:19]\n"
      "v_mfma_f32_16x16x32_bf16 v[20:23], v[176:179], a[148:151], v[20:23]\n"
      "v_mfma_f32_16x16x32_bf16 v[24:27], v[176:179], a[152:155], v[24:27]\n"
      "v_mfma_f32_16x16x32_bf16 v[28:31], v[176:179], a[156:159], v[28:31]\n"
      "v_mfma_f32_16x16x32_bf16 v[32:35], v[180:183], a[128:131], v[32:35]\n"
      "v_mfma_f32_16x16x32_bf16 v[36:39], v[180:183], a[132:135], v[36:39]\n"
      "v_mfma_f32_16x16x32_bf16 v[40:43], v[180:183], a[136:139], v[40:43]\n"
      "v_mfma_f32_16x16x32_bf16 v[44:47], v[180:183], a[140:143], v[44:47]\n"
      "v_mfma_f32_16x16x32_bf16 v[48:51], v[180:183], a[144:147], v[48:51]\n"
      "v_mfma_f32_16x16x32_bf16 v[52:55], v[180:183], a[148:151], v[52:55]\n"
      "v_mfma_f32_16x16x32_bf16 v[56:59], v[180:183], a[152:155], v[56:59]\n"
      "v_mfma_f32_16x16x32_bf16 v[60:63], v[180:183], a[156:159], v[60:63]\n"
      "v_mfma_f32_16x16x32_bf16 v[64:67], v[184:187], a[128:131], v[64:67]\n"
      "v_mfma_f32_16x16x32_bf16 v[68:71], v[184:187], a[132:135], v[68:71]\n"
      "v_mfma_f32_16x16x32_bf16 v[72:75], v[184:187], a[136:139], v[72:75]\n"
      "v_mfma_f32_16x16x32_bf16 v[76:79], v[184:187], a[140:143], v[76:79]\n"
      "v_mfma_f32_16x16x32_bf16 v[80:83], v[184:187], a[144:147], v[80:83]\n"
      "v_mfma_f32_16x16x32_bf16 v[84:87], v[184:187], a[148:151], v[84:87]\n"
      "v_mfma_f32_16x16x32_bf16 v[88:91], v[184:187], a[152:155], v[88:91]\n"
      "v_mfma_f32_16x16x32_bf16 v[92:95], v[184:187], a[156:159], v[92:95]\n"
      "v_mfma_f32_16x16x32_bf16 v[0:3], v[188:191], a[160:163], v[0:3]\n"
      "v_mfma_f32_16x16x32_bf16 v[4:7], v[188:191], a[164:167], v[4:7]\n"
      "v_mfma_f32_16x16x32_bf16 v[8:11], v[188:191], a[168:171], v[8:11]\n"
      "v_mfma_f32_16x16x32_bf16 v[12:15], v[188:191], a[172:175], v[12:15]\n"
      "v_mfma_f32_16x16x32_bf16 v[16:19], v[188:191], a[176:179], v[16:19]\n"
      "v_mfma_f32_16x16x32_bf16 v[20:23], v[188:191], a[180:183], v[20:23]\n"
      "v_mfma_f32_16x16x32_bf16 v[24:27], v[188:191], a[184:187], v[24:27]\n"
      "v_mfma_f32_16x16x32_bf16 v[28:31], v[188:191], a[188:191], v[28:31]\n"
      "v_mfma_f32_16x16x32_bf16 v[32:35], v[128:131], a[160:163], v[32:35]\n"
      "v_mfma_f32_16x16x32_bf16 v[36:39], v[128:131], a[164:167], v[36:39]\n"
      "v_mfma_f32_16x16x32_bf16 v[40:43], v[128:131], a[168:171], v[40:43]\n"
      "v_mfma_f32_16x16x32_bf16 v[44:47], v[128:131], a[172:175], v[44:47]\n"
      "v_mfma_f32_16x16x32_bf16 v[48:51], v[128:131], a[176:179], v[48:51]\n"
      "v_mfma_f32_16x16x32_bf16 v[52:55], v[128:131], a[180:183], v[52:55]\n"
      "v_mfma_f32_16x16x32_bf16 v[56:59], v[128:131], a[184:187], v[56:59]\n"
      "v_mfma_f32_16x16x32_bf16 v[60:63], v[128:131], a[188:191], v[60:63]\n"
      "v_mfma_f32_16x16x32_bf16 v[64:67], v[132:135], a[160:163], v[64:67]\n"
      "v_mfma_f32_16x16x32_bf16 v[68:71], v[132:135], a[164:167], v[68:71]\n"
      "v_mfma_f32_16x16x32_bf16 v[72:75], v[132:135], a[168:171], v[72:75]\n"
      "v_mfma_f32_16x16x32_bf16 v[76:79], v[132:135], a[172:175], v[76:79]\n"
      "v_mfma_f32_16x16x32_bf16 v[80:83], v[132:135], a[176:179], v[80:83]\n"
      "v_mfma_f32_16x16x32_bf16 v[84:87], v[132:135], a[180:183], v[84:87]\n"
      "v_mfma_f32_16x16x32_bf16 v[88:91], v[132:135], a[184:187], v[88:91]\n"
      "v_mfma_f32_16x16x32_bf16 v[92:95], v[132:135], a[188:191], v[92:95]\n"
      "v_mfma_f32_16x16x32_bf16 v[0:3], v[136:139], a[192:195], v[0:3]\n"
      "s_waitcnt lgkmcnt(14)\n"
      "v_mfma_f32_16x16x32_bf16 v[4:7], v[136:139], a[196:199], v[4:7]\n"
      "s_waitcnt lgkmcnt(11)\n"
      "v_mfma_f32_16x16x32_bf16 v[8:11], v[136:139], a[200:203], v[8:11]\n"
      "s_waitcnt lgkmcnt(10)\n"
      "v_mfma_f32_16x16x32_bf16 v[12:15], v[136:139], a[204:207], v[12:15]\n"
      "s_waitcnt lgkmcnt(7)\n"
      "v_mfma_f32_16x16x32_bf16 v[16:19], v[136:139], a[208:211], v[16:19]\n"
      "s_waitcnt lgkmcnt(6)\n"
      "v_mfma_f32_16x16x32_bf16 v[20:23], v[136:139], a[212:215], v[20:23]\n"
      "s_waitcnt lgkmcnt(3)\n"
      "v_mfma_f32_16x16x32_bf16 v[24:27], v[136:139], a[216:219], v[24:27]\n"
      "s_waitcnt vmcnt(2)\n"
      "ds_read_b64_tr_b16 v[224:225], v202 offset:0\n"
      "s_waitcnt lgkmcnt(3)\n"
      "v_mfma_f32_16x16x32_bf16 v[28:31], v[136:139], a[220:223], v[28:31]\n"
      "ds_read_b64_tr_b16 v[226:227], v202 offset:512\n"
      "ds_read_b64_tr_b16 v[228:229], v202 offset:4096\n"
      "v_mfma_f32_16x16x32_bf16 v[0:3], v[140:143], a[224:227], v[0:3]\n"
      "ds_read_b64_tr_b16 v[230:231], v202 offset:4608\n"
      "ds_read_b64_tr_b16 v[232:233], v202 offset:64\n"
      "v_mfma_f32_16x16x32_bf16 v[4:7], v[140:143], a[228:231], v[4:7]\n"
      "s_waitcnt vmcnt(0)\n"
      "v_cmp_ne_u32 vcc, s37, v252\n"
      "s_nop 1\n"
      "s_cbranch_vccnz .Ls_s12_%=\n"
      ".Ls_o13_%=:\n"
      "v_mfma_f32_16x16x32_bf16 v[8:11], v[140:143], a[232:235], v[8:11]\n"
      "ds_read_b64_tr_b16 v[234:235], v202 offset:576\n"
      "ds_read_b64_tr_b16 v[236:237], v202 offset:4160\n"
      "v_mfma_f32_16x16x32_bf16 v[12:15], v[140:143], a[236:239], v[12:15]\n"
      "global_load_dwordx4 v[160:163], v240, s[10:11] offset:-1024 sc1\n"
      "global_load_dwordx4 v[164:167], v240, s[72:73] offset:-1024 sc1\n"
      "v_mfma_f32_16x16x32_bf16 v[16:19], v[140:143], a[240:243], v[16:19]\n"
      "global_load_dwordx4 v[168:171], v241, s[10:11] offset:-1024 sc1\n"
      "global_load_dwordx4 v[172:175], v241, s[72:73] offset:-1024 sc1\n"
      "v_mfma_f32_16x16x32_bf16 v[20:23], v[140:143], a[244:247], v[20:23]\n"
      "global_load_dwordx4 v[176:179], v240, s[14:15] offset:-1024 sc1\n"
      "global_load_dwordx4 v[180:183], v240, s[74:75] offset:-1024 sc1\n"
      "s_waitcnt lgkmcnt(8)\n"
      "v_mfma_f32_16x16x32_bf16 v[24:27], v[140:143], a[248:251], v[24:27]\n"
      "global_load_dwordx4 v[184:187], v241, s[14:15] offset:-1024 sc1\n"
      "v_cvt_pk_bf16_f32 v8, v8, v9\n"
      "s_waitcnt lgkmcnt(7)\n"
      "v_mfma_f32_16x16x32_bf16 v[28:31], v[140:143], a[252:255], v[28:31]\n"
      "global_load_dwordx4 v[188:191], v241, s[74:75] offset:-1024 sc1\n"
      "v_cvt_pk_bf16_f32 v9, v10, v11\n"
      "v_mfma_f32_16x16x32_bf16 v[32:35], v[144:147], a[192:195], v[32:35]\n"
      "global_load_dwordx4 v[128:131], v240, s[10:11] offset:0 sc1\n"
      "global_load_dwordx4 v[132:135], v240, s[72:73] offset:0 sc1\n"
      "v_mfma_f32_16x16x32_bf16 v[36:39], v[144:147], a[196:199], v[36:39]\n"
      "global_load_dwordx4 v[136:139], v241, s[10:11] offset:0 sc1\n"
      "global_load_dwordx4 v[140:143], v241, s[72:73] offset:0 sc1\n"
      "v_mfma_f32_16x16x32_bf16 v[40:43], v[144:147], a[200:203], v[40:43]\n"
      "ds_read_b64_tr_b16 v[238:239], v202 offset:4672\n"
      "ds_write_addtid_b32 v8 offset:0\n"
      "v_mfma_f32_16x16x32_bf16 v[44:47], v[144:147], a[204:207], v[44:47]\n"
      "v_cvt_pk_bf16_f32 v10, v12, v13\n"
      "ds_write_addtid_b32 v9 offset:256\n"
      "v_mfma_f32_16x16x32_bf16 v[48:51], v[144:147], a[208:211], v[48:51]\n"
      "v_cvt_pk_bf16_f32 v11, v14, v15\n"
      "ds_write_addtid_b32 v10 offset:512\n"
      "v_mfma_f32_16x16x32_bf16 v[52:55], v[144:147], a[212:215], v[52:55]\n"
      "s_waitcnt lgkmcnt(9)\n"
      "v_mfma_f32_16x16x32_bf16 v[248:251], v[192:195], v[224:227], 0\n"
      "v_mfma_f32_16x16x32_bf16 v[56:59], v[144:147], a[216:219], v[56:59]\n"
      "s_waitcnt lgkmcnt(7)\n"
      "v_mfma_f32_16x16x32_bf16 v[248:251], v[196:199], v[228:231], v[248:251]\n"
      "v_mfma_f32_16x16x32_bf16 v[60:63], v[144:147], a[220:223], v[60:63]\n"
      "global_load_dwordx4 v[144:147], v240, s[14:15] offset:0 sc1\n"
      "v_cvt_pk_bf16_f32 v12, v16, v17\n"
      "v_mfma_f32_16x16x32_bf16 v[32:35], v[148:151], a[224:227], v[32:35]\n"
      "s_waitcnt lgkmcnt(5)\n"
      "v_mfma_f32_16x16x32_bf16 v[224:227], v[192:195], v[232:235], 0\n"
      "v_mfma_f32_16x16x32_bf16 v[36:39], v[148:151], a[228:231], v[36:39]\n"
      "s_waitcnt lgkmcnt(3)\n"
      "v_mfma_f32_16x16x32_bf16 v[224:227], v[196:199], v[236:239], v[224:227]\n"
      "v_mfma_f32_16x16x32_bf16 v[40:43], v[148:151], a[232:235], v[40:43]\n"
      "ds_write_addtid_b32 v11 offset:768\n"
      "v_cvt_pk_bf16_f32 v13, v18, v19\n"
      "v_mfma_f32_16x16x32_bf16 v[44:47], v[148:151], a[236:239], v[44:47]\n"
      "ds_write_addtid_b32 v12 offset:1024\n"
      "v_cvt_pk_bf16_f32 v14, v20, v21\n"
      "v_mfma_f32_16x16x32_bf16 v[48:51], v[148:151], a[240:243], v[48:51]\n"
      "ds_write_addtid_b32 v13 offset:1280\n"
      "v_cndmask_b32 v247, v248, v224, s[64:65]\n"
      "v_mfma_f32_16x16x32_bf16 v[52:55], v[148:151], a[244:247], v[52:55]\n"
      "ds_read_b64_tr_b16 v[224:225], v202 offset:32\n"
      "v_cvt_pk_bf16_f32 v15, v22, v23\n"
      "v_mfma_f32_16x16x32_bf16 v[56:59], v[148:151], a[248:251], v[56:59]\n"
      "ds_read_b64_tr_b16 v[226:227], v202 offset:544\n"
      "ds_read_b64_tr_b16 v[228:229], v202 offset:4128\n"
      "v_mfma_f32_16x16x32_bf16 v[60:63], v[148:151], a[252:255], v[60:63]\n"
      "global_load_dwordx4 v[148:151], v240, s[74:75] offset:0 sc1\n"
      "ds_read_b64_tr_b16 v[230:231], v202 offset:4640\n"
      "v_mfma_f32_16x16x32_bf16 v[64:67], v[152:155], a[192:195], v[64:67]\n"
      "ds_read_b64_tr_b16 v[232:233], v202 offset:96\n"
      "ds_read_b64_tr_b16 v[234:235], v202 offset:608\n"
      "v_mfma_f32_16x16x32_bf16 v[68:71], v[152:155], a[196:199], v[68:71]\n"
      "ds_read_b64_tr_b16 v[236:237], v202 offset:4192\n"
      "ds_read_b64_tr_b16 v[238:239], v202 offset:4704\n"
      "v_mfma_f32_16x16x32_bf16 v[72:75], v[152:155], a[200:203], v[72:75]\n"
      "ds_write_addtid_b32 v14 offset:1536\n"
      "v_cvt_pk_bf16_f32 v16, v24, v25\n"
      "v_mfma_f32_16x16x32_bf16 v[76:79], v[152:155], a[204:207], v[76:79]\n"
      "ds_write_addtid_b32 v15 offset:1792\n"
      "v_cvt_pk_bf16_f32 v17, v26, v27\n"
      "v_mfma_f32_16x16x32_bf16 v[80:83], v[152:155], a[208:211], v[80:83]\n"
      "s_waitcnt lgkmcnt(8)\n"
      "v_mfma_f32_16x16x32_bf16 v[248:251], v[192:195], v[224:227], 0\n"
      "v_mfma_f32_16x16x32_bf16 v[84:87], v[152:155], a[212:215], v[84:87]\n"
      "s_waitcnt lgkmcnt(6)\n"
      "v_mfma_f32_16x16x32_bf16 v[248:251], v[196:199], v[228:231], v[248:251]\n"
      "v_mfma_f32_16x16x32_bf16 v[88:91], v[152:155], a[216:219], v[88:91]\n"
      "s_waitcnt lgkmcnt(4)\n"
      "v_mfma_f32_16x16x32_bf16 v[224:227], v[192:195], v[232:235], 0\n"
      "v_mfma_f32_16x16x32_bf16 v[92:95], v[152:155], a[220:223], v[92:95]\n"
      "global_load_dwordx4 v[152:155], v241, s[14:15] offset:0 sc1\n"
      "s_waitcnt lgkmcnt(2)\n"
      "v_mfma_f32_16x16x32_bf16 v[224:227], v[196:199], v[236:239], v[224:227]\n"
      "v_mfma_f32_16x16x32_bf16 v[64:67], v[156:159], a[224:227], v[64:67]\n"
      "ds_write_addtid_b32 v16 offset:2048\n"
      "v_cvt_pk_bf16_f32 v18, v28, v29\n"
      "v_mfma_f32_16x16x32_bf16 v[68:71], v[156:159], a[228:231], v[68:71]\n"
      "ds_write_addtid_b32 v17 offset:2304\n"
      "v_cvt_pk_bf16_f32 v19, v30, v31\n"
      "v_mfma_f32_16x16x32_bf16 v[72:75], v[156:159], a[232:235], v[72:75]\n"
      "ds_write_addtid_b32 v18 offset:2560\n"
      "v_cndmask_b32 v250, v248, v224, s[64:65]\n"
      "v_mfma_f32_16x16x32_bf16 v[76:79], v[156:159], a[236:239], v[76:79]\n"
      "ds_write_addtid_b32 v19 offset:2816\n"
      "v_and_b32 v248, s86, v247\n"
      "v_mfma_f32_16x16x32_bf16 v[80:83], v[156:159], a[240:243], v[80:83]\n"
      "v_mov_b32 v249, s37\n"
      "v_and_b32 v250, s86, v250\n"
      "v_mfma_f32_16x16x32_bf16 v[84:87], v[156:159], a[244:247], v[84:87]\n"
      "v_mov_b32 v251, s37\n"
      "v_cvt_pk_bf16_f32 v40, v40, v41\n"
      "v_mfma_f32_16x16x32_bf16 v[88:91], v[156:159], a[248:251], v[88:91]\n"
      "s_mov_b64 exec, s[66:67]\n"
      "global_store_dwordx4 v200, v[248:251], s[58:59] sc1\n"
      "s_mov_b64 exec, -1\n"
      "v_mfma_f32_16x16x32_bf16 v[92:95], v[156:159], a[252:255], v[92:95]\n"
      "s_waitcnt vmcnt(14)\n"
      "global_load_dwordx4 v[156:159], v241, s[74:75] offset:0 sc1\n"
      "v_bfe_u32 v248, v243, 1, 3\n"
      "v_mfma_f32_16x16x32_bf16 v[96:99], v[160:163], a[0:3], 0\n"
      "v_lshrrev_b32 v249, 4, v243\n"
      "v_lshlrev_b32 v249, 1, v249\n"
      "v_mfma_f32_16x16x32_bf16 v[100:103], v[160:163], a[4:7], 0\n"
      "v_sub_u32 v248, v248, v249\n"
      "v_and_b32 v249, 16, v242\n"
      "v_mfma_f32_16x16x32_bf16 v[104:107], v[160:163], a[8:11], 0\n"
      "v_mov_b32 v254, 0x3f80\n"
      "v_lshlrev_b32 v249, v249, v254\n"
      "v_mfma_f32_16x16x32_bf16 v[108:111], v[160:163], a[12:15], 0\n"
      "v_cmp_eq_u32 vcc, 0, v248\n"
      "s_nop 1\n"
      "v_cndmask_b32 v250, 0, v249, vcc\n"
      "v_mfma_f32_16x16x32_bf16 v[112:115], v[160:163], a[16:19], 0\n"
      "v_cmp_eq_u32 vcc, 1, v248\n"
      "s_nop 1\n"
      "v_cndmask_b32 v251, 0, v249, vcc\n"
      "v_mfma_f32_16x16x32_bf16 v[116:119], v[160:163], a[20:23], 0\n"
      "v_mov_b32 v252, v250\n"
      "v_mov_b32 v253, v251\n"
      "v_mfma_f32_16x16x32_bf16 v[120:123], v[160:163], a[24:27], 0\n"
      "v_mov_b32 v248, 0\n"
      "v_mov_b32 v249, 0\n"
      "v_mfma_f32_16x16x32_bf16 v[124:127], v[160:163], a[28:31], 0\n"
      "global_load_dwordx4 v[160:163], v240, s[10:11] offset:1024 sc1\n"
      "v_mov_b32 v254, 0\n"
      "v_mfma_f32_16x16x32_bf16 v[96:99], v[164:167], a[32:35], v[96:99]\n"
      "v_mov_b32 v255, 0\n"
      "s_add_u32 s48, s34, 1\n"
      "v_mfma_f32_16x16x32_bf16 v[100:103], v[164:167], a[36:39], v[100:103]\n"
      "s_and_b32 s48, s48, 3\n"
      "s_mul_i32 s48, s48, 36864\n"
      "s_add_u32 s48, s48, 0\n"
      "v_mfma_f32_16x16x32_bf16 v[104:107], v[164:167], a[40:43], v[104:107]\n"
      "s_add_u32 s52, s60, s48\n"
      "s_addc_u32 s53, s61, 0\n"
      "v_cvt_pk_bf16_f32 v41, v42, v43\n"
      "v_mfma_f32_16x16x32_bf16 v[108:111], v[164:167], a[44:47], v[108:111]\n"
      "s_lshl2_add_u32 s54, s36, s33\n"
      "ds_write_addtid_b32 v40 offset:3072\n"
      "v_cvt_pk_bf16_f32 v42, v44, v45\n"
      "v_mfma_f32_16x16x32_bf16 v[112:115], v[164:167], a[48:51], v[112:115]\n"
      "s_lshl_b32 s55, s54, 15\n"
      "ds_write_addtid_b32 v41 offset:3328\n"
      "v_cvt_pk_bf16_f32 v43, v46, v47\n"
      "v_mfma_f32_16x16x32_bf16 v[116:119], v[164:167], a[52:55], v[116:119]\n"
      "s_lshl2_add_u32 s56, s35, s34\n"
      "ds_write_addtid_b32 v42 offset:3584\n"
      "v_cvt_pk_bf16_f32 v44, v48, v49\n"
      "v_mfma_f32_16x16x32_bf16 v[120:123], v[164:167], a[56:59], v[120:123]\n"
      "s_lshl_b32 s56, s56, 6\n"
      "ds_write_addtid_b32 v43 offset:3840\n"
      "v_cvt_pk_bf16_f32 v45, v50, v51\n"
      "v_mfma_f32_16x16x32_bf16 v[124:127], v[164:167], a[60:63], v[124:127]\n"
      "s_waitcnt vmcnt(14)\n"
      "global_load_dwordx4 v[164:167], v240, s[72:73] offset:1024 sc1\n"
      "s_add_u32 s55, s55, s56\n"
      "v_mfma_f32_16x16x32_bf16 v[96:99], v[168:171], a[64:67], v[96:99]\n"
      "ds_write_addtid_b32 v44 offset:4096\n"
      "v_cvt_pk_bf16_f32 v46, v52, v53\n"
      "v_mfma_f32_16x16x32_bf16 v[100:103], v[168:171], a[68:71], v[100:103]\n"
      "ds_write_addtid_b32 v45 offset:4352\n"
      "v_cvt_pk_bf16_f32 v47, v54, v55\n"
      "v_mfma_f32_16x16x32_bf16 v[104:107], v[168:171], a[72:75], v[104:107]\n"
      "ds_write_addtid_b32 v46 offset:4608\n"
      "v_cvt_pk_bf16_f32 v48, v56, v57\n"
      "v_mfma_f32_16x16x32_bf16 v[108:111], v[168:171], a[76:79], v[108:111]\n"
      "ds_write_addtid_b32 v47 offset:4864\n"
      "v_cvt_pk_bf16_f32 v49, v58, v59\n"
      "v_mfma_f32_16x16x32_bf16 v[112:115], v[168:171], a[80:83], v[112:115]\n"
      "ds_write_addtid_b32 v48 offset:5120\n"
      "v_cvt_pk_bf16_f32 v50, v60, v61\n"
      "v_mfma_f32_16x16x32_bf16 v[116:119], v[168:171], a[84:87], v[116:119]\n"
      "ds_write_addtid_b32 v49 offset:5376\n"
      "v_cvt_pk_bf16_f32 v51, v62, v63\n"
      "v_mfma_f32_16x16x32_bf16 v[120:123], v[168:171], a[88:91], v[120:123]\n"
      "ds_write_addtid_b32 v50 offset:5632\n"
      "v_and_b32 v209, 15, v243\n"
      "v_mfma_f32_16x16x32_bf16 v[124:127], v[168:171], a[92:95], v[124:127]\n"
      "global_load_dwordx4 v[168:171], v241, s[10:11] offset:1024 sc1\n"
      "v_and_b32 v208, 48, v243\n"
      "v_mfma_f32_16x16x32_bf16 v[96:99], v[172:175], a[96:99], v[96:99]\n"
      "ds_write_addtid_b32 v51 offset:5888\n"
      "v_lshl_or_b32 v208, v209, 10, v208\n"
      "v_mfma_f32_16x16x32_bf16 v[100:103], v[172:175], a[100:103], v[100:103]\n"
      "v_add_u32 v208, s55, v208\n"
      "v_add_u32 v210, 16384, v208\n"
      "v_mfma_f32_16x16x32_bf16 v[104:107], v[172:175], a[104:107], v[104:107]\n"
      "global_load_dwordx4 v[216:219], v208, s[16:17]\n"
      "global_load_dwordx4 v[220:223], v210, s[16:17]\n"
      "v_mfma_f32_16x16x32_bf16 v[108:111], v[172:175], a[108:111], v[108:111]\n"
      "s_lshr_b32 s55, s34, 1\n"
      "s_waitcnt lgkmcnt(0)\n"
      "s_barrier\n"
      "v_mfma_f32_16x16x32_bf16 v[112:115], v[172:175], a[112:115], v[112:115]\n"
      "s_lshl2_add_u32 s55, s55, s35\n"
      "ds_read2st64_b32 v[20:21], v244 offset0:0 offset1:1\n"
      "s_lshl_b32 s55, s55, 15\n"
      "v_mfma_f32_16x16x32_bf16 v[116:119], v[172:175], a[116:119], v[116:119]\n"
      "s_lshl_b32 s54, s54, 9\n"
      "ds_read2st64_b32 v[22:23], v245 offset0:4 offset1:5\n"
      "s_add_u32 s54, s54, s55\n"
      "v_mfma_f32_16x16x32_bf16 v[120:123], v[172:175], a[120:123], v[120:123]\n"
      "s_add_u32 s54, s54, 786432\n"
      "ds_read2st64_b32 v[24:25], v244 offset0:2 offset1:3\n"
      "v_lshl_add_u32 v209, v209, 4, s54\n"
      "v_mfma_f32_16x16x32_bf16 v[124:127], v[172:175], a[124:127], v[124:127]\n"
      "s_waitcnt vmcnt(16)\n"
      "global_load_dwordx4 v[172:175], v241, s[72:73] offset:1024 sc1\n"
      "ds_read2st64_b32 v[26:27], v245 offset0:6 offset1:7\n"
      "v_mfma_f32_16x16x32_bf16 v[96:99], v[176:179], a[128:131], v[96:99]\n"
      "ds_read2st64_b32 v[28:29], v246 offset0:8 offset1:9\n"
      "ds_read2st64_b32 v[30:31], v246 offset0:10 offset1:11\n"
      "v_mfma_f32_16x16x32_bf16 v[100:103], v[176:179], a[132:135], v[100:103]\n"
      "ds_read2st64_b32 v[52:53], v244 offset0:12 offset1:13\n"
      "ds_read2st64_b32 v[54:55], v245 offset0:16 offset1:17\n"
      "v_mfma_f32_16x16x32_bf16 v[104:107], v[176:179], a[136:139], v[104:107]\n"
      "ds_read2st64_b32 v[56:57], v244 offset0:14 offset1:15\n"
      "ds_read2st64_b32 v[58:59], v245 offset0:18 offset1:19\n"
      "v_mfma_f32_16x16x32_bf16 v[108:111], v[176:179], a[140:143], v[108:111]\n"
      "ds_read2st64_b32 v[60:61], v246 offset0:20 offset1:21\n"
      "ds_read2st64_b32 v[62:63], v246 offset0:22 offset1:23\n"
      "v_mfma_f32_16x16x32_bf16 v[112:115], v[176:179], a[144:147], v[112:115]\n"
      "s_waitcnt lgkmcnt(6)\n"
      "v_mfma_f32_16x16x32_bf16 v[0:3], v[250:253], v[20:23], v[0:3]\n"
      "v_cvt_pk_bf16_f32 v72, v72, v73\n"
      "v_mfma_f32_16x16x32_bf16 v[116:119], v[176:179], a[148:151], v[116:119]\n"
      "v_mfma_f32_16x16x32_bf16 v[4:7], v[250:253], v[24:27], v[4:7]\n"
      "v_cvt_pk_bf16_f32 v73, v74, v75\n"
      "v_mfma_f32_16x16x32_bf16 v[120:123], v[176:179], a[152:155], v[120:123]\n"
      "v_mfma_f32_16x16x32_bf16 v[0:3], v[252:255], v[28:31], v[0:3]\n"
      "v_mfma_f32_16x16x32_bf16 v[124:127], v[176:179], a[156:159], v[124:127]\n"
      "global_load_dwordx4 v[176:179], v240, s[14:15] offset:1024 sc1\n"
      "v_mfma_f32_16x16x32_bf16 v[96:99], v[180:183], a[160:163], v[96:99]\n"
      "v_mfma_f32_16x16x32_bf16 v[4:7], v[248:251], v[28:31], v[4:7]\n"
      "v_mov_b32 v11, s37\n"
      "v_mfma_f32_16x16x32_bf16 v[100:103], v[180:183], a[164:167], v[100:103]\n"
      "ds_write_addtid_b32 v72 offset:6144\n"
      "v_cvt_pk_bf16_f32 v74, v76, v77\n"
      "v_mfma_f32_16x16x32_bf16 v[104:107], v[180:183], a[168:171], v[104:107]\n"
      "ds_write_addtid_b32 v73 offset:6400\n"
      "v_cvt_pk_bf16_f32 v8, v0, v1\n"
      "v_mfma_f32_16x16x32_bf16 v[108:111], v[180:183], a[172:175], v[108:111]\n"
      "global_load_dwordx2 v[204:205], v209, s[58:59] offset:0 sc1\n"
      "v_cvt_pk_bf16_f32 v9, v2, v3\n"
      "v_mfma_f32_16x16x32_bf16 v[112:115], v[180:183], a[176:179], v[112:115]\n"
      "global_load_dwordx2 v[206:207], v209, s[58:59] offset:256 sc1\n"
      "v_cvt_pk_bf16_f32 v10, v4, v5\n"
      "v_mfma_f32_16x16x32_bf16 v[116:119], v[180:183], a[180:183], v[116:119]\n"
      "s_add_u32 s68, s20, 16384\n"
      "s_addc_u32 s69, s21, 0\n"
      "v_cvt_pk_bf16_f32 v247, v6, v7\n"
      "v_mfma_f32_16x16x32_bf16 v[120:123], v[180:183], a[184:187], v[120:123]\n"
      "global_store_dwordx4 v242, v[8:11], s[52:53] offset:0 sc1\n"
      "s_waitcnt lgkmcnt(2)\n"
      "v_mfma_f32_16x16x32_bf16 v[124:127], v[180:183], a[188:191], v[124:127]\n"
      "s_waitcnt vmcnt(19)\n"
      "global_load_dwordx4 v[180:183], v240, s[74:75] offset:1024 sc1\n"
      "v_cvt_pk_bf16_f32 v75, v78, v79\n"
      "v_mfma_f32_16x16x32_bf16 v[96:99], v[184:187], a[192:195], v[96:99]\n"
      "v_mfma_f32_16x16x32_bf16 v[32:35], v[250:253], v[52:55], v[32:35]\n"
      "v_mfma_f32_16x16x32_bf16 v[100:103], v[184:187], a[196:199], v[100:103]\n"
      "v_mfma_f32_16x16x32_bf16 v[36:39], v[250:253], v[56:59], v[36:39]\n"
      "v_mfma_f32_16x16x32_bf16 v[104:107], v[184:187], a[200:203], v[104:107]\n"
      "v_mfma_f32_16x16x32_bf16 v[32:35], v[252:255], v[60:63], v[32:35]\n"
      "v_mfma_f32_16x16x32_bf16 v[108:111], v[184:187], a[204:207], v[108:111]\n"
      "v_mfma_f32_16x16x32_bf16 v[36:39], v[248:251], v[60:63], v[36:39]\n"
      "v_mov_b32 v43, s37\n"
      "v_mfma_f32_16x16x32_bf16 v[112:115], v[184:187], a[208:211], v[112:115]\n"
      "ds_write_addtid_b32 v74 offset:6656\n"
      "v_mov_b32 v44, v247\n"
      "v_mfma_f32_16x16x32_bf16 v[116:119], v[184:187], a[212:215], v[116:119]\n"
      "v_mov_b32 v47, s37\n"
      "v_cvt_pk_bf16_f32 v76, v80, v81\n"
      "v_mfma_f32_16x16x32_bf16 v[120:123], v[184:187], a[216:219], v[120:123]\n"
      "ds_write_addtid_b32 v75 offset:6912\n"
      "v_cvt_pk_bf16_f32 v77, v82, v83\n"
      "v_mfma_f32_16x16x32_bf16 v[124:127], v[184:187], a[220:223], v[124:127]\n"
      "global_load_dwordx4 v[184:187], v241, s[14:15] offset:1024 sc1\n"
      "v_cvt_pk_bf16_f32 v40, v32, v33\n"
      "v_mfma_f32_16x16x32_bf16 v[96:99], v[188:191], a[224:227], v[96:99]\n"
      "ds_write_addtid_b32 v76 offset:7168\n"
      "v_cvt_pk_bf16_f32 v41, v34, v35\n"
      "v_mfma_f32_16x16x32_bf16 v[100:103], v[188:191], a[228:231], v[100:103]\n"
      "v_cvt_pk_bf16_f32 v42, v36, v37\n"
      "v_cvt_pk_bf16_f32 v45, v38, v39\n"
      "v_mfma_f32_16x16x32_bf16 v[104:107], v[188:191], a[232:235], v[104:107]\n"
      "global_store_dwordx4 v242, v[40:43], s[52:53] offset:1024 sc1\n"
      "v_cvt_pk_bf16_f32 v78, v84, v85\n"
      "v_mfma_f32_16x16x32_bf16 v[108:111], v[188:191], a[236:239], v[108:111]\n"
      "global_store_dwordx4 v242, v[44:47], s[52:53] offset:2048 sc1\n"
      "s_add_u32 s48, s34, 2\n"
      "v_mfma_f32_16x16x32_bf16 v[112:115], v[188:191], a[240:243], v[112:115]\n"
      "s_and_b32 s48, s48, 3\n"
      "ds_write_addtid_b32 v77 offset:7424\n"
      "s_mul_i32 s48, s48, 36864\n"
      "v_mfma_f32_16x16x32_bf16 v[116:119], v[188:191], a[244:247], v[116:119]\n"
      "s_add_u32 s48, s48, 12288\n"
      "s_add_u32 s52, s60, s48\n"
      "s_addc_u32 s53, s61, 0\n"
      "v_mfma_f32_16x16x32_bf16 v[120:123], v[188:191], a[248:251], v[120:123]\n"
      "v_cvt_pk_bf16_f32 v79, v86, v87\n"
      "v_mfma_f32_16x16x32_bf16 v[124:127], v[188:191], a[252:255], v[124:127]\n"
      "s_waitcnt vmcnt(21)\n"
      "global_load_dwordx4 v[188:191], v241, s[74:75] offset:1024 sc1\n"
      "v_mfma_f32_16x16x32_bf16 v[0:3], v[128:131], a[0:3], 0\n"
      "ds_write_addtid_b32 v78 offset:7680\n"
      "v_cvt_pk_bf16_f32 v80, v88, v89\n"
      "v_mfma_f32_16x16x32_bf16 v[4:7], v[128:131], a[4:7], 0\n"
      "ds_write_addtid_b32 v79 offset:7936\n"
      "v_cvt_pk_bf16_f32 v81, v90, v91\n"
      "v_mfma_f32_16x16x32_bf16 v[8:11], v[128:131], a[8:11], 0\n"
      "ds_write_addtid_b32 v80 offset:8192\n"
      "v_cvt_pk_bf16_f32 v82, v92, v93\n"
      "v_mfma_f32_16x16x32_bf16 v[12:15], v[128:131], a[12:15], 0\n"
      "ds_write_addtid_b32 v81 offset:8448\n"
      "v_cvt_pk_bf16_f32 v83, v94, v95\n"
      "v_mfma_f32_16x16x32_bf16 v[16:19], v[128:131], a[16:19], 0\n"
      "ds_write_addtid_b32 v82 offset:8704\n"
      "v_mfma_f32_16x16x32_bf16 v[20:23], v[128:131], a[20:23], 0\n"
      "ds_write_addtid_b32 v83 offset:8960\n"
      "v_cvt_pk_bf16_f32 v104, v104, v105\n"
      "v_mfma_f32_16x16x32_bf16 v[24:27], v[128:131], a[24:27], 0\n"
      "v_cvt_pk_bf16_f32 v105, v106, v107\n"
      "v_mfma_f32_16x16x32_bf16 v[28:31], v[128:131], a[28:31], 0\n"
      "global_load_dwordx4 v[128:131], v240, s[10:11] offset:2048 sc1\n"
      "v_mfma_f32_16x16x32_bf16 v[0:3], v[132:135], a[32:35], v[0:3]\n"
      "ds_write_addtid_b32 v104 offset:9216\n"
      "v_cvt_pk_bf16_f32 v106, v108, v109\n"
      "v_mfma_f32_16x16x32_bf16 v[4:7], v[132:135], a[36:39], v[4:7]\n"
      "ds_write_addtid_b32 v105 offset:9472\n"
      "v_cvt_pk_bf16_f32 v107, v110, v111\n"
      "v_mfma_f32_16x16x32_bf16 v[8:11], v[132:135], a[40:43], v[8:11]\n"
      "ds_write_addtid_b32 v106 offset:9728\n"
      "v_cvt_pk_bf16_f32 v108, v112, v113\n"
      "v_mfma_f32_16x16x32_bf16 v[12:15], v[132:135], a[44:47], v[12:15]\n"
      "ds_write_addtid_b32 v107 offset:9984\n"
      "v_cvt_pk_bf16_f32 v109, v114, v115\n"
      "v_mfma_f32_16x16x32_bf16 v[16:19], v[132:135], a[48:51], v[16:19]\n"
      "ds_write_addtid_b32 v108 offset:10240\n"
      "v_cvt_pk_bf16_f32 v110, v116, v117\n"
      "v_mfma_f32_16x16x32_bf16 v[20:23], v[132:135], a[52:55], v[20:23]\n"
      "ds_write_addtid_b32 v109 offset:10496\n"
      "v_cvt_pk_bf16_f32 v111, v118, v119\n"
      "v_mfma_f32_16x16x32_bf16 v[24:27], v[132:135], a[56:59], v[24:27]\n"
      "ds_write_addtid_b32 v110 offset:10752\n"
      "v_cvt_pk_bf16_f32 v112, v120, v121\n"
      "v_mfma_f32_16x16x32_bf16 v[28:31], v[132:135], a[60:63], v[28:31]\n"
      "s_waitcnt vmcnt(21)\n"
      "global_load_dwordx4 v[132:135], v240, s[72:73] offset:2048 sc1\n"
      "v_mfma_f32_16x16x32_bf16 v[0:3], v[136:139], a[64:67], v[0:3]\n"
      "ds_write_addtid_b32 v111 offset:11008\n"
      "v_cvt_pk_bf16_f32 v113, v122, v123\n"
      "v_mfma_f32_16x16x32_bf16 v[4:7], v[136:139], a[68:71], v[4:7]\n"
      "ds_write_addtid_b32 v112 offset:11264\n"
      "v_cvt_pk_bf16_f32 v114, v124, v125\n"
      "v_mfma_f32_16x16x32_bf16 v[8:11], v[136:139], a[72:75], v[8:11]\n"
      "ds_write_addtid_b32 v113 offset:11520\n"
      "v_cvt_pk_bf16_f32 v115, v126, v127\n"
      "v_mfma_f32_16x16x32_bf16 v[12:15], v[136:139], a[76:79], v[12:15]\n"
      "ds_write_addtid_b32 v114 offset:11776\n"
      "v_mfma_f32_16x16x32_bf16 v[16:19], v[136:139], a[80:83], v[16:19]\n"
      "ds_write_addtid_b32 v115 offset:12032\n"
      "v_mfma_f32_16x16x32_bf16 v[20:23], v[136:139], a[84:87], v[20:23]\n"
      "v_mfma_f32_16x16x32_bf16 v[24:27], v[136:139], a[88:91], v[24:27]\n"
      "v_mfma_f32_16x16x32_bf16 v[28:31], v[136:139], a[92:95], v[28:31]\n"
      "global_load_dwordx4 v[136:139], v241, s[10:11] offset:2048 sc1\n"
      "v_mfma_f32_16x16x32_bf16 v[0:3], v[140:143], a[96:99], v[0:3]\n"
      "s_waitcnt lgkmcnt(0)\n"
      "s_barrier\n"
      "v_mfma_f32_16x16x32_bf16 v[4:7], v[140:143], a[100:103], v[4:7]\n"
      "ds_read2st64_b32 v[84:85], v244 offset0:24 offset1:25\n"
      "v_mfma_f32_16x16x32_bf16 v[8:11], v[140:143], a[104:107], v[8:11]\n"
      "ds_read2st64_b32 v[86:87], v245 offset0:28 offset1:29\n"
      "v_mfma_f32_16x16x32_bf16 v[12:15], v[140:143], a[108:111], v[12:15]\n"
      "ds_read2st64_b32 v[88:89], v244 offset0:26 offset1:27\n"
      "v_mfma_f32_16x16x32_bf16 v[16:19], v[140:143], a[112:115], v[16:19]\n"
      "ds_read2st64_b32 v[90:91], v245 offset0:30 offset1:31\n"
      "v_mfma_f32_16x16x32_bf16 v[20:23], v[140:143], a[116:119], v[20:23]\n"
      "ds_read2st64_b32 v[92:93], v246 offset0:32 offset1:33\n"
      "v_mfma_f32_16x16x32_bf16 v[24:27], v[140:143], a[120:123], v[24:27]\n"
      "ds_read2st64_b32 v[94:95], v246 offset0:34 offset1:35\n"
      "v_mfma_f32_16x16x32_bf16 v[28:31], v[140:143], a[124:127], v[28:31]\n"
      "s_waitcnt vmcnt(21)\n"
      "global_load_dwordx4 v[140:143], v241, s[72:73] offset:2048 sc1\n"
      "v_mfma_f32_16x16x32_bf16 v[0:3], v[144:147], a[128:131], v[0:3]\n"
      "ds_read2st64_b32 v[116:117], v244 offset0:36 offset1:37\n"
      "v_mfma_f32_16x16x32_bf16 v[4:7], v[144:147], a[132:135], v[4:7]\n"
      "ds_read2st64_b32 v[118:119], v245 offset0:40 offset1:41\n"
      "v_mfma_f32_16x16x32_bf16 v[8:11], v[144:147], a[136:139], v[8:11]\n"
      "s_waitcnt lgkmcnt(2)\n"
      "v_mfma_f32_16x16x32_bf16 v[64:67], v[250:253], v[84:87], v[64:67]\n"
      "v_mfma_f32_16x16x32_bf16 v[12:15], v[144:147], a[140:143], v[12:15]\n"
      "v_mfma_f32_16x16x32_bf16 v[68:71], v[250:253], v[88:91], v[68:71]\n"
      "v_mfma_f32_16x16x32_bf16 v[16:19], v[144:147], a[144:147], v[16:19]\n"
      "v_mfma_f32_16x16x32_bf16 v[64:67], v[252:255], v[92:95], v[64:67]\n"
      "v_mfma_f32_16x16x32_bf16 v[20:23], v[144:147], a[148:151], v[20:23]\n"
      "v_mfma_f32_16x16x32_bf16 v[68:71], v[248:251], v[92:95], v[68:71]\n"
      "v_mov_b32 v75, s37\n"
      "v_mfma_f32_16x16x32_bf16 v[24:27], v[144:147], a[152:155], v[24:27]\n"
      "ds_read2st64_b32 v[120:121], v244 offset0:38 offset1:39\n"
      "v_mfma_f32_16x16x32_bf16 v[28:31], v[144:147], a[156:159], v[28:31]\n"
      "global_load_dwordx4 v[144:147], v240, s[14:15] offset:2048 sc1\n"
      "s_nop 0\n"
      "v_cvt_pk_bf16_f32 v72, v64, v65\n"
      "v_mfma_f32_16x16x32_bf16 v[0:3], v[148:151], a[160:163], v[0:3]\n"
      "ds_read2st64_b32 v[122:123], v245 offset0:42 offset1:43\n"
      "v_cvt_pk_bf16_f32 v73, v66, v67\n"
      "v_mfma_f32_16x16x32_bf16 v[4:7], v[148:151], a[164:167], v[4:7]\n"
      "ds_read2st64_b32 v[124:125], v246 offset0:44 offset1:45\n"
      "v_cvt_pk_bf16_f32 v74, v68, v69\n"
      "v_mfma_f32_16x16x32_bf16 v[8:11], v[148:151], a[168:171], v[8:11]\n"
      "ds_read2st64_b32 v[126:127], v246 offset0:46 offset1:47\n"
      "v_cvt_pk_bf16_f32 v247, v70, v71\n"
      "v_mfma_f32_16x16x32_bf16 v[12:15], v[148:151], a[172:175], v[12:15]\n"
      "global_store_dwordx4 v242, v[72:75], s[52:53] offset:0 sc1\n"
      "v_mfma_f32_16x16x32_bf16 v[16:19], v[148:151], a[176:179], v[16:19]\n"
      "s_waitcnt vmcnt(12)\n"
      "v_bitop3_b32 v210, v205, v207, s37 bitop3:0x7e\n"
      "v_cmp_ne_u32 vcc, 0, v210\n"
      "s_nop 1\n"
      "s_cbranch_vccnz .Ls_s15_%=\n"
      ".Ls_o16_%=:\n"
      "v_mfma_f32_16x16x32_bf16 v[20:23], v[148:151], a[180:183], v[20:23]\n"
      "v_sub_f32 v205, 1.0, v204\n"
      "v_cvt_pk_bf16_f32 v205, v205, v205\n"
      "v_mfma_f32_16x16x32_bf16 v[24:27], v[148:151], a[184:187], v[24:27]\n"
      "s_waitcnt lgkmcnt(0)\n"
      "v_mfma_f32_16x16x32_bf16 v[96:99], v[250:253], v[116:119], v[96:99]\n"
      "v_and_b32 v205, 0xffff0000, v205\n"
      "v_mfma_f32_16x16x32_bf16 v[28:31], v[148:151], a[188:191], v[28:31]\n"
      "global_load_dwordx4 v[148:151], v240, s[74:75] offset:2048 sc1\n"
      "v_sub_f32 v207, 1.0, v206\n"
      "v_mfma_f32_16x16x32_bf16 v[0:3], v[152:155], a[192:195], v[0:3]\n"
      "v_mfma_f32_16x16x32_bf16 v[100:103], v[250:253], v[120:123], v[100:103]\n"
      "v_cvt_pk_bf16_f32 v207, v207, v207\n"
      "v_mfma_f32_16x16x32_bf16 v[4:7], v[152:155], a[196:199], v[4:7]\n"
      "v_mfma_f32_16x16x32_bf16 v[96:99], v[252:255], v[124:127], v[96:99]\n"
      "v_and_b32 v207, 0xffff0000, v207\n"
      "v_mfma_f32_16x16x32_bf16 v[8:11], v[152:155], a[200:203], v[8:11]\n"
      "v_mfma_f32_16x16x32_bf16 v[100:103], v[248:251], v[124:127], v[100:103]\n"
      "v_mov_b32 v107, s37\n"
      "v_mfma_f32_16x16x32_bf16 v[12:15], v[152:155], a[204:207], v[12:15]\n"
      "v_mov_b32 v108, v247\n"
      "v_mov_b32 v111, s37\n"
      "v_mfma_f32_16x16x32_bf16 v[16:19], v[152:155], a[208:211], v[16:19]\n"
      "v_lshlrev_b32 v208, 16, v216\n"
      "v_and_b32 v209, 0xffff0000, v216\n"
      "v_mfma_f32_16x16x32_bf16 v[20:23], v[152:155], a[212:215], v[20:23]\n"
      "v_cvt_pk_bf16_f32 v104, v96, v97\n"
      "v_cvt_pk_bf16_f32 v105, v98, v99\n"
      "v_mfma_f32_16x16x32_bf16 v[24:27], v[152:155], a[216:219], v[24:27]\n"
      "v_cvt_pk_bf16_f32 v106, v100, v101\n"
      "v_cvt_pk_bf16_f32 v109, v102, v103\n"
      "v_mfma_f32_16x16x32_bf16 v[28:31], v[152:155], a[220:223], v[28:31]\n"
      "global_load_dwordx4 v[152:155], v241, s[14:15] offset:2048 sc1\n"
      "v_mul_f32 v208, v205, v208\n"
      "v_mfma_f32_16x16x32_bf16 v[0:3], v[156:159], a[224:227], v[0:3]\n"
      "global_store_dwordx4 v242, v[104:107], s[52:53] offset:1024 sc1\n"
      "v_mul_f32 v209, v205, v209\n"
      "v_mfma_f32_16x16x32_bf16 v[4:7], v[156:159], a[228:231], v[4:7]\n"
      "global_store_dwordx4 v242, v[108:111], s[52:53] offset:2048 sc1\n"
      "s_add_u32 s48, s34, 3\n"
      "v_mfma_f32_16x16x32_bf16 v[8:11], v[156:159], a[232:235], v[8:11]\n"
      "s_and_b32 s48, s48, 3\n"
      "s_mul_i32 s48, s48, 36864\n"
      "s_add_u32 s48, s48, 24576\n"
      "v_mfma_f32_16x16x32_bf16 v[12:15], v[156:159], a[236:239], v[12:15]\n"
      "s_add_u32 s52, s60, s48\n"
      "s_addc_u32 s53, s61, 0\n"
      "v_lshlrev_b32 v212, 16, v220\n"
      "v_mfma_f32_16x16x32_bf16 v[16:19], v[156:159], a[240:243], v[16:19]\n"
      "v_and_b32 v213, 0xffff0000, v220\n"
      "v_mul_f32 v212, v207, v212\n"
      "v_mfma_f32_16x16x32_bf16 v[20:23], v[156:159], a[244:247], v[20:23]\n"
      "v_mul_f32 v213, v207, v213\n"
      "v_lshlrev_b32 v210, 16, v217\n"
      "v_mfma_f32_16x16x32_bf16 v[24:27], v[156:159], a[248:251], v[24:27]\n"
      "v_cvt_pk_bf16_f32 v8, v8, v9\n"
      "v_cvt_pk_bf16_f32 v9, v10, v11\n"
      "v_mfma_f32_16x16x32_bf16 v[28:31], v[156:159], a[252:255], v[28:31]\n"
      "global_load_dwordx4 v[156:159], v241, s[74:75] offset:2048 sc1\n"
      "v_and_b32 v211, 0xffff0000, v217\n"
      "v_mfma_f32_16x16x32_bf16 v[32:35], v[160:163], a[0:3], 0\n"
      "ds_write_addtid_b32 v8 offset:0\n"
      "v_cvt_pk_bf16_f32 v10, v12, v13\n"
      "v_mfma_f32_16x16x32_bf16 v[36:39], v[160:163], a[4:7], 0\n"
      "ds_write_addtid_b32 v9 offset:256\n"
      "v_cvt_pk_bf16_f32 v11, v14, v15\n"
      "v_mfma_f32_16x16x32_bf16 v[40:43], v[160:163], a[8:11], 0\n"
      "ds_write_addtid_b32 v10 offset:512\n"
      "v_cvt_pk_bf16_f32 v12, v16, v17\n"
      "v_mfma_f32_16x16x32_bf16 v[44:47], v[160:163], a[12:15], 0\n"
      "ds_write_addtid_b32 v11 offset:768\n"
      "v_cvt_pk_bf16_f32 v13, v18, v19\n"
      "v_mfma_f32_16x16x32_bf16 v[48:51], v[160:163], a[16:19], 0\n"
      "ds_write_addtid_b32 v12 offset:1024\n"
      "v_cvt_pk_bf16_f32 v14, v20, v21\n"
      "v_mfma_f32_16x16x32_bf16 v[52:55], v[160:163], a[20:23], 0\n"
      "ds_write_addtid_b32 v13 offset:1280\n"
      "v_cvt_pk_bf16_f32 v15, v22, v23\n"
      "v_mfma_f32_16x16x32_bf16 v[56:59], v[160:163], a[24:27], 0\n"
      "ds_write_addtid_b32 v14 offset:1536\n"
      "v_cvt_pk_bf16_f32 v16, v24, v25\n"
      "v_mfma_f32_16x16x32_bf16 v[60:63], v[160:163], a[28:31], 0\n"
      "global_load_dwordx4 v[160:163], v240, s[10:11] offset:3072 sc1\n"
      "v_mul_f32 v210, v205, v210\n"
      "v_mfma_f32_16x16x32_bf16 v[32:35], v[164:167], a[32:35], v[32:35]\n"
      "ds_write_addtid_b32 v15 offset:1792\n"
      "v_cvt_pk_bf16_f32 v17, v26, v27\n"
      "v_mfma_f32_16x16x32_bf16 v[36:39], v[164:167], a[36:39], v[36:39]\n"
      "ds_write_addtid_b32 v16 offset:2048\n"
      "v_cvt_pk_bf16_f32 v18, v28, v29\n"
      "v_mfma_f32_16x16x32_bf16 v[40:43], v[164:167], a[40:43], v[40:43]\n"
      "ds_write_addtid_b32 v17 offset:2304\n"
      "v_cvt_pk_bf16_f32 v19, v30, v31\n"
      "v_mfma_f32_16x16x32_bf16 v[44:47], v[164:167], a[44:47], v[44:47]\n"
      "ds_write_addtid_b32 v18 offset:2560\n"
      "v_mul_f32 v211, v205, v211\n"
      "v_mfma_f32_16x16x32_bf16 v[48:51], v[164:167], a[48:51], v[48:51]\n"
      "ds_write_addtid_b32 v19 offset:2816\n"
      "v_lshlrev_b32 v214, 16, v221\n"
      "v_mfma_f32_16x16x32_bf16 v[52:55], v[164:167], a[52:55], v[52:55]\n"
      "v_and_b32 v215, 0xffff0000, v221\n"
      "v_mul_f32 v214, v207, v214\n"
      "v_mfma_f32_16x16x32_bf16 v[56:59], v[164:167], a[56:59], v[56:59]\n"
      "v_mul_f32 v215, v207, v215\n"
      "v_lshlrev_b32 v216, 16, v218\n"
      "v_mfma_f32_16x16x32_bf16 v[60:63], v[164:167], a[60:63], v[60:63]\n"
      "global_load_dwordx4 v[164:167], v240, s[72:73] offset:3072 sc1\n"
      "v_and_b32 v217, 0xffff0000, v218\n"
      "v_mfma_f32_16x16x32_bf16 v[32:35], v[168:171], a[64:67], v[32:35]\n"
      "s_waitcnt lgkmcnt(0)\n"
      "s_barrier\n"
      "v_mfma_f32_16x16x32_bf16 v[36:39], v[168:171], a[68:71], v[36:39]\n"
      "ds_read2st64_b32 v[20:21], v244 offset0:0 offset1:1\n"
      "v_mul_f32 v216, v205, v216\n"
      "v_mfma_f32_16x16x32_bf16 v[40:43], v[168:171], a[72:75], v[40:43]\n"
      "ds_read2st64_b32 v[22:23], v245 offset0:4 offset1:5\n"
      "v_mul_f32 v217, v205, v217\n"
      "v_mfma_f32_16x16x32_bf16 v[44:47], v[168:171], a[76:79], v[44:47]\n"
      "ds_read2st64_b32 v[24:25], v244 offset0:2 offset1:3\n"
      "v_lshlrev_b32 v220, 16, v222\n"
      "v_mfma_f32_16x16x32_bf16 v[48:51], v[168:171], a[80:83], v[48:51]\n"
      "ds_read2st64_b32 v[26:27], v245 offset0:6 offset1:7\n"
      "v_and_b32 v221, 0xffff0000, v222\n"
      "v_mfma_f32_16x16x32_bf16 v[52:55], v[168:171], a[84:87], v[52:55]\n"
      "ds_read2st64_b32 v[28:29], v246 offset0:8 offset1:9\n"
      "v_mul_f32 v220, v207, v220\n"
      "v_mfma_f32_16x16x32_bf16 v[56:59], v[168:171], a[88:91], v[56:59]\n"
      "ds_read2st64_b32 v[30:31], v246 offset0:10 offset1:11\n"
      "v_mul_f32 v221, v207, v221\n"
      "v_mfma_f32_16x16x32_bf16 v[60:63], v[168:171], a[92:95], v[60:63]\n"
      "global_load_dwordx4 v[168:171], v241, s[10:11] offset:3072 sc1\n"
      "v_lshlrev_b32 v218, 16, v219\n"
      "v_mfma_f32_16x16x32_bf16 v[32:35], v[172:175], a[96:99], v[32:35]\n"
      "v_and_b32 v219, 0xffff0000, v219\n"
      "v_mul_f32 v218, v205, v218\n"
      "v_mfma_f32_16x16x32_bf16 v[36:39], v[172:175], a[100:103], v[36:39]\n"
      "v_mul_f32 v219, v205, v219\n"
      "v_lshlrev_b32 v222, 16, v223\n"
      "v_mfma_f32_16x16x32_bf16 v[40:43], v[172:175], a[104:107], v[40:43]\n"
      "s_waitcnt lgkmcnt(0)\n"
      "v_mfma_f32_16x16x32_bf16 v[0:3], v[250:253], v[20:23], v[0:3]\n"
      "v_and_b32 v223, 0xffff0000, v223\n"
      "v_mfma_f32_16x16x32_bf16 v[44:47], v[172:175], a[108:111], v[44:47]\n"
      "v_mfma_f32_16x16x32_bf16 v[4:7], v[250:253], v[24:27], v[4:7]\n"
      "v_mul_f32 v222, v207, v222\n"
      "v_mfma_f32_16x16x32_bf16 v[48:51], v[172:175], a[112:115], v[48:51]\n"
      "v_mfma_f32_16x16x32_bf16 v[0:3], v[252:255], v[28:31], v[0:3]\n"
      "v_mul_f32 v223, v207, v223\n"
      "v_mfma_f32_16x16x32_bf16 v[52:55], v[172:175], a[116:119], v[52:55]\n"
      "v_mfma_f32_16x16x32_bf16 v[4:7], v[248:251], v[28:31], v[4:7]\n"
      "v_mov_b32 v11, s37\n"
      "v_mfma_f32_16x16x32_bf16 v[56:59], v[172:175], a[120:123], v[56:59]\n"
      "v_mfma_f32_16x16x32_bf16 v[60:63], v[172:175], a[124:127], v[60:63]\n"
      "s_waitcnt vmcnt(18)\n"
      "global_load_dwordx4 v[172:175], v241, s[72:73] offset:3072 sc1\n"
      "v_cvt_pk_bf16_f32 v8, v0, v1\n"
      "v_mfma_f32_16x16x32_bf16 v[32:35], v[176:179], a[128:131], v[32:35]\n"
      "v_cvt_pk_bf16_f32 v9, v2, v3\n"
      "v_cvt_pk_bf16_f32 v10, v4, v5\n"
      "v_mfma_f32_16x16x32_bf16 v[36:39], v[176:179], a[132:135], v[36:39]\n"
      "v_cvt_pk_bf16_f32 v247, v6, v7\n"
      "v_mfma_f32_16x16x32_bf16 v[40:43], v[176:179], a[136:139], v[40:43]\n"
      "global_store_dwordx4 v242, v[8:11], s[52:53] offset:0 sc1\n"
      "s_lshl2_add_u32 s54, s36, s33\n"
      "v_mfma_f32_16x16x32_bf16 v[44:47], v[176:179], a[140:143], v[44:47]\n"
      "s_lshl_b32 s55, s54, 15\n"
      "s_lshl2_add_u32 s56, s35, s34\n"
      "s_lshl_b32 s56, s56, 6\n"
      "v_mfma_f32_16x16x32_bf16 v[48:51], v[176:179], a[144:147], v[48:51]\n"
      "s_add_u32 s55, s55, s56\n"
      "v_and_b32 v8, 15, v243\n"
      "v_and_b32 v7, 48, v243\n"
      "v_mfma_f32_16x16x32_bf16 v[52:55], v[176:179], a[148:151], v[52:55]\n"
      "v_lshl_or_b32 v7, v8, 10, v7\n"
      "v_add_u32 v7, s55, v7\n"
      "v_mfma_f32_16x16x32_bf16 v[56:59], v[176:179], a[152:155], v[56:59]\n"
      "v_mfma_f32_16x16x32_bf16 v[60:63], v[176:179], a[156:159], v[60:63]\n"
      "global_load_dwordx4 v[176:179], v240, s[14:15] offset:3072 sc1\n"
      "v_mfma_f32_16x16x32_bf16 v[32:35], v[180:183], a[160:163], v[32:35]\n"
      "v_mfma_f32_16x16x32_bf16 v[36:39], v[180:183], a[164:167], v[36:39]\n"
      "v_mfma_f32_16x16x32_bf16 v[40:43], v[180:183], a[168:171], v[40:43]\n"
      "v_mfma_f32_16x16x32_bf16 v[44:47], v[180:183], a[172:175], v[44:47]\n"
      "v_mfma_f32_16x16x32_bf16 v[48:51], v[180:183], a[176:179], v[48:51]\n"
      "v_mfma_f32_16x16x32_bf16 v[52:55], v[180:183], a[180:183], v[52:55]\n"
      "v_mfma_f32_16x16x32_bf16 v[56:59], v[180:183], a[184:187], v[56:59]\n"
      "v_mfma_f32_16x16x32_bf16 v[60:63], v[180:183], a[188:191], v[60:63]\n"
      "s_waitcnt vmcnt(17)\n"
      "global_load_dwordx4 v[180:183], v240, s[74:75] offset:3072 sc1\n"
      "v_mfma_f32_16x16x32_bf16 v[40:43], v[184:187], a[200:203], v[40:43]\n"
      "v_mfma_f32_16x16x32_bf16 v[44:47], v[184:187], a[204:207], v[44:47]\n"
      "v_mfma_f32_16x16x32_bf16 v[48:51], v[184:187], a[208:211], v[48:51]\n"
      "v_mfma_f32_16x16x32_bf16 v[52:55], v[184:187], a[212:215], v[52:55]\n"
      "v_mfma_f32_16x16x32_bf16 v[56:59], v[184:187], a[216:219], v[56:59]\n"
      "v_mfma_f32_16x16x32_bf16 v[60:63], v[184:187], a[220:223], v[60:63]\n"
      "v_mfma_f32_16x16x32_bf16 v[40:43], v[188:191], a[232:235], v[40:43]\n"
      "v_mfma_f32_16x16x32_bf16 v[44:47], v[188:191], a[236:239], v[44:47]\n"
      "v_mfma_f32_16x16x32_bf16 v[48:51], v[188:191], a[240:243], v[48:51]\n"
      "v_mfma_f32_16x16x32_bf16 v[52:55], v[188:191], a[244:247], v[52:55]\n"
      "v_mfma_f32_16x16x32_bf16 v[56:59], v[188:191], a[248:251], v[56:59]\n"
      "s_nop 3\n"
      "v_cvt_pk_bf16_f32 v40, v40, v41\n"
      "v_cvt_pk_bf16_f32 v41, v42, v43\n"
      "v_mfma_f32_16x16x32_bf16 v[60:63], v[188:191], a[252:255], v[60:63]\n"
      "ds_write_addtid_b32 v40 offset:3072\n"
      "v_cvt_pk_bf16_f32 v42, v44, v45\n"
      "v_mfma_f32_16x16x32_bf16 v[32:35], v[184:187], a[192:195], v[32:35]\n"
      "ds_write_addtid_b32 v41 offset:3328\n"
      "v_cvt_pk_bf16_f32 v43, v46, v47\n"
      "v_mfma_f32_16x16x32_bf16 v[36:39], v[184:187], a[196:199], v[36:39]\n"
      "global_load_dwordx4 v[184:187], v241, s[14:15] offset:3072 sc1\n"
      "ds_write_addtid_b32 v42 offset:3584\n"
      "v_cvt_pk_bf16_f32 v44, v48, v49\n"
      "v_mfma_f32_16x16x32_bf16 v[32:35], v[188:191], a[224:227], v[32:35]\n"
      "ds_write_addtid_b32 v43 offset:3840\n"
      "v_cvt_pk_bf16_f32 v45, v50, v51\n"
      "v_mfma_f32_16x16x32_bf16 v[36:39], v[188:191], a[228:231], v[36:39]\n"
      "s_waitcnt vmcnt(17)\n"
      "global_load_dwordx4 v[188:191], v241, s[74:75] offset:3072 sc1\n"
      "ds_write_addtid_b32 v44 offset:4096\n"
      "v_cvt_pk_bf16_f32 v46, v52, v53\n"
      "v_mfma_f32_16x16x32_bf16 v[72:75], v[128:131], a[8:11], 0\n"
      "ds_write_addtid_b32 v45 offset:4352\n"
      "v_cvt_pk_bf16_f32 v47, v54, v55\n"
      "v_mfma_f32_16x16x32_bf16 v[76:79], v[128:131], a[12:15], 0\n"
      "ds_write_addtid_b32 v46 offset:4608\n"
      "v_cvt_pk_bf16_f32 v48, v56, v57\n"
      "v_mfma_f32_16x16x32_bf16 v[80:83], v[128:131], a[16:19], 0\n"
      "ds_write_addtid_b32 v47 offset:4864\n"
      "v_cvt_pk_bf16_f32 v49, v58, v59\n"
      "v_mfma_f32_16x16x32_bf16 v[84:87], v[128:131], a[20:23], 0\n"
      "ds_write_addtid_b32 v48 offset:5120\n"
      "v_cvt_pk_bf16_f32 v50, v60, v61\n"
      "v_mfma_f32_16x16x32_bf16 v[88:91], v[128:131], a[24:27], 0\n"
      "ds_write_addtid_b32 v49 offset:5376\n"
      "v_cvt_pk_bf16_f32 v51, v62, v63\n"
      "v_mfma_f32_16x16x32_bf16 v[92:95], v[128:131], a[28:31], 0\n"
      "ds_write_addtid_b32 v50 offset:5632\n"
      "ds_write_addtid_b32 v51 offset:5888\n"
      "v_mfma_f32_16x16x32_bf16 v[72:75], v[132:135], a[40:43], v[72:75]\n"
      "v_mfma_f32_16x16x32_bf16 v[76:79], v[132:135], a[44:47], v[76:79]\n"
      "v_mfma_f32_16x16x32_bf16 v[80:83], v[132:135], a[48:51], v[80:83]\n"
      "s_waitcnt lgkmcnt(0)\n"
      "s_barrier\n"
      "v_mfma_f32_16x16x32_bf16 v[84:87], v[132:135], a[52:55], v[84:87]\n"
      "ds_read2st64_b32 v[52:53], v244 offset0:12 offset1:13\n"
      "ds_read2st64_b32 v[54:55], v245 offset0:16 offset1:17\n"
      "v_mfma_f32_16x16x32_bf16 v[88:91], v[132:135], a[56:59], v[88:91]\n"
      "ds_read2st64_b32 v[56:57], v244 offset0:14 offset1:15\n"
      "ds_read2st64_b32 v[58:59], v245 offset0:18 offset1:19\n"
      "v_mfma_f32_16x16x32_bf16 v[92:95], v[132:135], a[60:63], v[92:95]\n"
      "s_waitcnt vmcnt(16)\n"
      "ds_read2st64_b32 v[60:61], v246 offset0:20 offset1:21\n"
      "ds_read2st64_b32 v[62:63], v246 offset0:22 offset1:23\n"
      "v_mfma_f32_16x16x32_bf16 v[72:75], v[136:139], a[72:75], v[72:75]\n"
      "v_mfma_f32_16x16x32_bf16 v[76:79], v[136:139], a[76:79], v[76:79]\n"
      "v_mfma_f32_16x16x32_bf16 v[80:83], v[136:139], a[80:83], v[80:83]\n"
      "v_mfma_f32_16x16x32_bf16 v[84:87], v[136:139], a[84:87], v[84:87]\n"
      "s_waitcnt lgkmcnt(0)\n"
      "v_mfma_f32_16x16x32_bf16 v[32:35], v[250:253], v[52:55], v[32:35]\n"
      "v_mfma_f32_16x16x32_bf16 v[36:39], v[250:253], v[56:59], v[36:39]\n"
      "v_mfma_f32_16x16x32_bf16 v[88:91], v[136:139], a[88:91], v[88:91]\n"
      "v_mfma_f32_16x16x32_bf16 v[32:35], v[252:255], v[60:63], v[32:35]\n"
      "v_mfma_f32_16x16x32_bf16 v[36:39], v[248:251], v[60:63], v[36:39]\n"
      "v_add_u32 v4, 0, v242\n"
      "v_mfma_f32_16x16x32_bf16 v[92:95], v[136:139], a[92:95], v[92:95]\n"
      "global_load_dwordx4 v[12:15], v4, s[62:63] offset:0 sc1\n"
      "global_load_dwordx4 v[16:19], v4, s[62:63] offset:1024 sc1\n"
      "v_mov_b32 v43, s37\n"
      "v_mfma_f32_16x16x32_bf16 v[72:75], v[140:143], a[104:107], v[72:75]\n"
      "global_load_dwordx4 v[20:23], v4, s[62:63] offset:2048 sc1\n"
      "v_mov_b32 v44, v247\n"
      "v_mov_b32 v47, s37\n"
      "v_mfma_f32_16x16x32_bf16 v[76:79], v[140:143], a[108:111], v[76:79]\n"
      "v_mfma_f32_16x16x32_bf16 v[80:83], v[140:143], a[112:115], v[80:83]\n"
      "v_cvt_pk_bf16_f32 v40, v32, v33\n"
      "v_cvt_pk_bf16_f32 v41, v34, v35\n"
      "v_cvt_pk_bf16_f32 v42, v36, v37\n"
      "v_mfma_f32_16x16x32_bf16 v[84:87], v[140:143], a[116:119], v[84:87]\n"
      "v_cvt_pk_bf16_f32 v45, v38, v39\n"
      "v_mfma_f32_16x16x32_bf16 v[88:91], v[140:143], a[120:123], v[88:91]\n"
      "global_store_dwordx4 v242, v[40:43], s[52:53] offset:1024 sc1\n"
      "global_store_dwordx4 v242, v[44:47], s[52:53] offset:2048 sc1\n"
      "v_mfma_f32_16x16x32_bf16 v[92:95], v[140:143], a[124:127], v[92:95]\n"
      "s_waitcnt vmcnt(18)\n"
      "v_mfma_f32_16x16x32_bf16 v[72:75], v[144:147], a[136:139], v[72:75]\n"
      "v_mfma_f32_16x16x32_bf16 v[76:79], v[144:147], a[140:143], v[76:79]\n"
      "v_mfma_f32_16x16x32_bf16 v[80:83], v[144:147], a[144:147], v[80:83]\n"
      "v_mfma_f32_16x16x32_bf16 v[84:87], v[144:147], a[148:151], v[84:87]\n"
      "v_mfma_f32_16x16x32_bf16 v[88:91], v[144:147], a[152:155], v[88:91]\n"
      "v_mfma_f32_16x16x32_bf16 v[92:95], v[144:147], a[156:159], v[92:95]\n"
      "v_mfma_f32_16x16x32_bf16 v[72:75], v[148:151], a[168:171], v[72:75]\n"
      "v_mfma_f32_16x16x32_bf16 v[76:79], v[148:151], a[172:175], v[76:79]\n"
      "v_mfma_f32_16x16x32_bf16 v[80:83], v[148:151], a[176:179], v[80:83]\n"
      "v_mfma_f32_16x16x32_bf16 v[84:87], v[148:151], a[180:183], v[84:87]\n"
      "v_mfma_f32_16x16x32_bf16 v[88:91], v[148:151], a[184:187], v[88:91]\n"
      "v_mfma_f32_16x16x32_bf16 v[92:95], v[148:151], a[188:191], v[92:95]\n"
      "s_waitcnt vmcnt(14)\n"
      "v_mfma_f32_16x16x32_bf16 v[72:75], v[152:155], a[200:203], v[72:75]\n"
      "v_mfma_f32_16x16x32_bf16 v[76:79], v[152:155], a[204:207], v[76:79]\n"
      "v_mfma_f32_16x16x32_bf16 v[80:83], v[152:155], a[208:211], v[80:83]\n"
      "v_mfma_f32_16x16x32_bf16 v[84:87], v[152:155], a[212:215], v[84:87]\n"
      "v_mfma_f32_16x16x32_bf16 v[88:91], v[152:155], a[216:219], v[88:91]\n"
      "v_mfma_f32_16x16x32_bf16 v[92:95], v[152:155], a[220:223], v[92:95]\n"
      "v_mfma_f32_16x16x32_bf16 v[72:75], v[156:159], a[232:235], v[72:75]\n"
      "v_mfma_f32_16x16x32_bf16 v[76:79], v[156:159], a[236:239], v[76:79]\n"
      "v_mfma_f32_16x16x32_bf16 v[80:83], v[156:159], a[240:243], v[80:83]\n"
      "v_mfma_f32_16x16x32_bf16 v[84:87], v[156:159], a[244:247], v[84:87]\n"
      "v_mfma_f32_16x16x32_bf16 v[88:91], v[156:159], a[248:251], v[88:91]\n"
      "s_nop 3\n"
      "v_cvt_pk_bf16_f32 v72, v72, v73\n"
      "v_mfma_f32_16x16x32_bf16 v[92:95], v[156:159], a[252:255], v[92:95]\n"
      "s_waitcnt vmcnt(12)\n"
      "v_cvt_pk_bf16_f32 v73, v74, v75\n"
      "v_mfma_f32_16x16x32_bf16 v[104:107], v[160:163], a[8:11], 0\n"
      "ds_write_addtid_b32 v72 offset:6144\n"
      "v_cvt_pk_bf16_f32 v74, v76, v77\n"
      "v_mfma_f32_16x16x32_bf16 v[108:111], v[160:163], a[12:15], 0\n"
      "ds_write_addtid_b32 v73 offset:6400\n"
      "v_cvt_pk_bf16_f32 v75, v78, v79\n"
      "v_mfma_f32_16x16x32_bf16 v[112:115], v[160:163], a[16:19], 0\n"
      "ds_write_addtid_b32 v74 offset:6656\n"
      "v_cvt_pk_bf16_f32 v76, v80, v81\n"
      "v_mfma_f32_16x16x32_bf16 v[116:119], v[160:163], a[20:23], 0\n"
      "ds_write_addtid_b32 v75 offset:6912\n"
      "v_cvt_pk_bf16_f32 v77, v82, v83\n"
      "v_mfma_f32_16x16x32_bf16 v[120:123], v[160:163], a[24:27], 0\n"
      "ds_write_addtid_b32 v76 offset:7168\n"
      "v_cvt_pk_bf16_f32 v78, v84, v85\n"
      "v_mfma_f32_16x16x32_bf16 v[124:127], v[160:163], a[28:31], 0\n"
      "ds_write_addtid_b32 v77 offset:7424\n"
      "v_cvt_pk_bf16_f32 v79, v86, v87\n"
      "v_mfma_f32_16x16x32_bf16 v[104:107], v[164:167], a[40:43], v[104:107]\n"
      "ds_write_addtid_b32 v78 offset:7680\n"
      "v_cvt_pk_bf16_f32 v80, v88, v89\n"
      "v_mfma_f32_16x16x32_bf16 v[108:111], v[164:167], a[44:47], v[108:111]\n"
      "ds_write_addtid_b32 v79 offset:7936\n"
      "v_cvt_pk_bf16_f32 v81, v90, v91\n"
      "v_mfma_f32_16x16x32_bf16 v[112:115], v[164:167], a[48:51], v[112:115]\n"
      "ds_write_addtid_b32 v80 offset:8192\n"
      "v_cvt_pk_bf16_f32 v82, v92, v93\n"
      "v_mfma_f32_16x16x32_bf16 v[116:119], v[164:167], a[52:55], v[116:119]\n"
      "ds_write_addtid_b32 v81 offset:8448\n"
      "v_cvt_pk_bf16_f32 v83, v94, v95\n"
      "v_mfma_f32_16x16x32_bf16 v[120:123], v[164:167], a[56:59], v[120:123]\n"
      "ds_write_addtid_b32 v82 offset:8704\n"
      "v_mfma_f32_16x16x32_bf16 v[124:127], v[164:167], a[60:63], v[124:127]\n"
      "s_waitcnt vmcnt(10)\n"
      "ds_write_addtid_b32 v83 offset:8960\n"
      "v_mfma_f32_16x16x32_bf16 v[104:107], v[168:171], a[72:75], v[104:107]\n"
      "s_waitcnt vmcnt(2)\n"
      "v_bitop3_b32 v22, v15, v19, s37 bitop3:0x7e\n"
      "v_bitop3_b32 v22, v22, v23, s37 bitop3:0xf6\n"
      "v_cmp_ne_u32 vcc, 0, v22\n"
      "s_nop 1\n"
      "s_cbranch_vccnz .Ls_s18_%=\n"
      ".Ls_o19_%=:\n"
      "v_mfma_f32_16x16x32_bf16 v[108:111], v[168:171], a[76:79], v[108:111]\n"
      "v_mov_b32 v15, v20\n"
      "v_mov_b32 v19, v21\n"
      "v_mfma_f32_16x16x32_bf16 v[112:115], v[168:171], a[80:83], v[112:115]\n"
      "s_waitcnt lgkmcnt(0)\n"
      "s_barrier\n"
      "v_mfma_f32_16x16x32_bf16 v[116:119], v[168:171], a[84:87], v[116:119]\n"
      "ds_read2st64_b32 v[84:85], v244 offset0:24 offset1:25\n"
      "v_mfma_f32_16x16x32_bf16 v[120:123], v[168:171], a[88:91], v[120:123]\n"
      "ds_read2st64_b32 v[86:87], v245 offset0:28 offset1:29\n"
      "v_mfma_f32_16x16x32_bf16 v[124:127], v[168:171], a[92:95], v[124:127]\n"
      "ds_read2st64_b32 v[88:89], v244 offset0:26 offset1:27\n"
      "v_mfma_f32_16x16x32_bf16 v[104:107], v[172:175], a[104:107], v[104:107]\n"
      "ds_read2st64_b32 v[90:91], v245 offset0:30 offset1:31\n"
      "v_mfma_f32_16x16x32_bf16 v[108:111], v[172:175], a[108:111], v[108:111]\n"
      "ds_read2st64_b32 v[92:93], v246 offset0:32 offset1:33\n"
      "v_mfma_f32_16x16x32_bf16 v[112:115], v[172:175], a[112:115], v[112:115]\n"
      "ds_read2st64_b32 v[94:95], v246 offset0:34 offset1:35\n"
      "v_mfma_f32_16x16x32_bf16 v[116:119], v[172:175], a[116:119], v[116:119]\n"
      "s_lshr_b32 s43, s42, 7\n"
      "s_cmp_eq_u32 s43, s36\n"
      "s_cbranch_scc0 .Ls_e20_%=\n"
      "s_lshl2_add_u32 s44, s35, s34\n"
      "s_lshl2_add_u32 s43, s44, s33\n"
      "s_lshl_b32 s43, s43, 12\n"
      "s_add_u32 s43, s43, 524288\n"
      "v_add_u32 v60, s43, v242\n"
      "v_mov_b32 v62, 0\n"
      "global_load_dwordx4 v[24:27], v60, s[58:59] offset:0 sc1\n"
      "global_load_dwordx4 v[28:31], v60, s[58:59] offset:1024 sc1\n"
      "global_load_dwordx4 v[32:35], v60, s[58:59] offset:2048 sc1\n"
      "global_load_dwordx4 v[36:39], v60, s[58:59] offset:3072 sc1\n"
      "global_load_dwordx4 v[40:43], v62, s[58:59] offset:1024 sc1\n"
      "v_and_b32 v58, 15, v243\n"
      "v_lshrrev_b32 v59, 5, v243\n"
      "v_lshl_or_b32 v58, v59, 4, v58\n"
      "s_lshl4_add_u32 s44, s42, s44\n"
      "s_lshl_b32 s44, s44, 6\n"
      "v_lshl_add_u32 v59, v58, 1, s44\n"
      "global_load_ushort v57, v59, s[16:17]\n"
      "s_mov_b32 s44, 131072\n"
      "v_lshl_add_u32 v58, v58, 2, s44\n"
      "s_lshl_b32 s43, s33, 13\n"
      "v_add_u32 v59, s43, v58\n"
      ".Ls_e20_%=:\n"
      "v_mfma_f32_16x16x32_bf16 v[120:123], v[172:175], a[120:123], v[120:123]\n"
      "v_mfma_f32_16x16x32_bf16 v[64:67], v[252:255], v[12:15], 0\n"
      "v_mfma_f32_16x16x32_bf16 v[124:127], v[172:175], a[124:127], v[124:127]\n"
      "v_mfma_f32_16x16x32_bf16 v[68:71], v[248:251], v[12:15], 0\n"
      "v_mfma_f32_16x16x32_bf16 v[104:107], v[176:179], a[136:139], v[104:107]\n"
      "s_waitcnt lgkmcnt(0)\n"
      "v_mfma_f32_16x16x32_bf16 v[64:67], v[250:253], v[84:87], v[64:67]\n"
      "v_mfma_f32_16x16x32_bf16 v[108:111], v[176:179], a[140:143], v[108:111]\n"
      "v_mfma_f32_16x16x32_bf16 v[68:71], v[250:253], v[88:91], v[68:71]\n"
      "v_mfma_f32_16x16x32_bf16 v[112:115], v[176:179], a[144:147], v[112:115]\n"
      "v_mfma_f32_16x16x32_bf16 v[64:67], v[252:255], v[92:95], v[64:67]\n"
      "v_mfma_f32_16x16x32_bf16 v[116:119], v[176:179], a[148:151], v[116:119]\n"
      "v_mfma_f32_16x16x32_bf16 v[68:71], v[248:251], v[92:95], v[68:71]\n"
      "v_mfma_f32_16x16x32_bf16 v[120:123], v[176:179], a[152:155], v[120:123]\n"
      "v_mfma_f32_16x16x32_bf16 v[96:99], v[252:255], v[16:19], 0\n"
      "v_mfma_f32_16x16x32_bf16 v[124:127], v[176:179], a[156:159], v[124:127]\n"
      "v_mfma_f32_16x16x32_bf16 v[100:103], v[248:251], v[16:19], 0\n"
      "v_mfma_f32_16x16x32_bf16 v[104:107], v[180:183], a[168:171], v[104:107]\n"
      "v_mfma_f32_16x16x32_bf16 v[108:111], v[180:183], a[172:175], v[108:111]\n"
      "v_mfma_f32_16x16x32_bf16 v[112:115], v[180:183], a[176:179], v[112:115]\n"
      "v_mfma_f32_16x16x32_bf16 v[116:119], v[180:183], a[180:183], v[116:119]\n"
      "v_add_u32 v5, 12288, v242\n"
      "v_mfma_f32_16x16x32_bf16 v[120:123], v[180:183], a[184:187], v[120:123]\n"
      "global_load_dwordx4 v[44:47], v5, s[62:63] offset:0 sc1\n"
      "v_mfma_f32_16x16x32_bf16 v[124:127], v[180:183], a[188:191], v[124:127]\n"
      "global_load_dwordx4 v[48:51], v5, s[62:63] offset:1024 sc1\n"
      "v_mfma_f32_16x16x32_bf16 v[104:107], v[184:187], a[200:203], v[104:107]\n"
      "global_load_dwordx4 v[52:55], v5, s[62:63] offset:2048 sc1\n"
      "v_mfma_f32_16x16x32_bf16 v[108:111], v[184:187], a[204:207], v[108:111]\n"
      "v_mfma_f32_16x16x32_bf16 v[112:115], v[184:187], a[208:211], v[112:115]\n"
      "v_mfma_f32_16x16x32_bf16 v[116:119], v[184:187], a[212:215], v[116:119]\n"
      "v_mfma_f32_16x16x32_bf16 v[120:123], v[184:187], a[216:219], v[120:123]\n"
      "v_mfma_f32_16x16x32_bf16 v[124:127], v[184:187], a[220:223], v[124:127]\n"
      "v_mfma_f32_16x16x32_bf16 v[104:107], v[188:191], a[232:235], v[104:107]\n"
      "v_mfma_f32_16x16x32_bf16 v[108:111], v[188:191], a[236:239], v[108:111]\n"
      "v_mfma_f32_16x16x32_bf16 v[112:115], v[188:191], a[240:243], v[112:115]\n"
      "v_mfma_f32_16x16x32_bf16 v[116:119], v[188:191], a[244:247], v[116:119]\n"
      "v_mfma_f32_16x16x32_bf16 v[120:123], v[188:191], a[248:251], v[120:123]\n"
      "s_nop 3\n"
      "v_cvt_pk_bf16_f32 v104, v104, v105\n"
      "v_mfma_f32_16x16x32_bf16 v[124:127], v[188:191], a[252:255], v[124:127]\n"
      "v_cvt_pk_bf16_f32 v105, v106, v107\n"
      "v_mfma_f32_16x16x32_bf16 v[64:67], v[128:131], a[0:3], v[64:67]\n"
      "ds_write_addtid_b32 v104 offset:9216\n"
      "v_cvt_pk_bf16_f32 v106, v108, v109\n"
      "v_mfma_f32_16x16x32_bf16 v[68:71], v[128:131], a[4:7], v[68:71]\n"
      "ds_write_addtid_b32 v105 offset:9472\n"
      "v_cvt_pk_bf16_f32 v107, v110, v111\n"
      "v_mfma_f32_16x16x32_bf16 v[64:67], v[132:135], a[32:35], v[64:67]\n"
      "ds_write_addtid_b32 v106 offset:9728\n"
      "v_cvt_pk_bf16_f32 v108, v112, v113\n"
      "v_mfma_f32_16x16x32_bf16 v[68:71], v[132:135], a[36:39], v[68:71]\n"
      "ds_write_addtid_b32 v107 offset:9984\n"
      "v_cvt_pk_bf16_f32 v109, v114, v115\n"
      "v_mfma_f32_16x16x32_bf16 v[64:67], v[136:139], a[64:67], v[64:67]\n"
      "ds_write_addtid_b32 v108 offset:10240\n"
      "v_cvt_pk_bf16_f32 v110, v116, v117\n"
      "v_mfma_f32_16x16x32_bf16 v[68:71], v[136:139], a[68:71], v[68:71]\n"
      "ds_write_addtid_b32 v109 offset:10496\n"
      "v_cvt_pk_bf16_f32 v111, v118, v119\n"
      "v_mfma_f32_16x16x32_bf16 v[64:67], v[140:143], a[96:99], v[64:67]\n"
      "ds_write_addtid_b32 v110 offset:10752\n"
      "v_cvt_pk_bf16_f32 v112, v120, v121\n"
      "v_mfma_f32_16x16x32_bf16 v[68:71], v[140:143], a[100:103], v[68:71]\n"
      "ds_write_addtid_b32 v111 offset:11008\n"
      "v_cvt_pk_bf16_f32 v113, v122, v123\n"
      "v_mfma_f32_16x16x32_bf16 v[64:67], v[144:147], a[128:131], v[64:67]\n"
      "ds_write_addtid_b32 v112 offset:11264\n"
      "v_cvt_pk_bf16_f32 v114, v124, v125\n"
      "v_mfma_f32_16x16x32_bf16 v[68:71], v[144:147], a[132:135], v[68:71]\n"
      "ds_write_addtid_b32 v113 offset:11520\n"
      "v_cvt_pk_bf16_f32 v115, v126, v127\n"
      "v_mfma_f32_16x16x32_bf16 v[64:67], v[148:151], a[160:163], v[64:67]\n"
      "ds_write_addtid_b32 v114 offset:11776\n"
      "v_add_u32 v6, 24576, v242\n"
      "v_mfma_f32_16x16x32_bf16 v[68:71], v[148:151], a[164:167], v[68:71]\n"
      "global_load_dwordx4 v[72:75], v6, s[62:63] offset:0 sc1\n"
      "v_mfma_f32_16x16x32_bf16 v[64:67], v[152:155], a[192:195], v[64:67]\n"
      "global_load_dwordx4 v[76:79], v6, s[62:63] offset:1024 sc1\n"
      "v_mfma_f32_16x16x32_bf16 v[68:71], v[152:155], a[196:199], v[68:71]\n"
      "global_load_dwordx4 v[80:83], v6, s[62:63] offset:2048 sc1\n"
      "v_mfma_f32_16x16x32_bf16 v[64:67], v[156:159], a[224:227], v[64:67]\n"
      "ds_write_addtid_b32 v115 offset:12032\n"
      "v_mfma_f32_16x16x32_bf16 v[68:71], v[156:159], a[228:231], v[68:71]\n"
      "v_mfma_f32_16x16x32_bf16 v[96:99], v[160:163], a[0:3], v[96:99]\n"
      "v_mfma_f32_16x16x32_bf16 v[100:103], v[160:163], a[4:7], v[100:103]\n"
      "s_lshr_b32 s43, s42, 7\n"
      "s_cmp_eq_u32 s43, s36\n"
      "s_cbranch_scc0 .Ls_e21_%=\n"
      "s_waitcnt vmcnt(6)\n"
      "v_bitop3_b32 v61, v25, v27, s37 bitop3:0x7e\n"
      "v_bitop3_b32 v61, v61, v29, s37 bitop3:0xf6\n"
      "v_bitop3_b32 v61, v61, v31, s37 bitop3:0xf6\n"
      "v_bitop3_b32 v61, v61, v33, s37 bitop3:0xf6\n"
      "v_bitop3_b32 v61, v61, v35, s37 bitop3:0xf6\n"
      "v_bitop3_b32 v61, v61, v37, s37 bitop3:0xf6\n"
      "v_bitop3_b32 v61, v61, v39, s37 bitop3:0xf6\n"
      "v_bitop3_b32 v61, v61, v43, s37 bitop3:0xf6\n"
      "v_cmp_ne_u32 vcc, 0, v61\n"
      "s_nop 1\n"
      "s_cbranch_vccnz .Ls_s23_%=\n"
      ".Ls_o24_%=:\n"
      "v_add_f32 v24, v24, v28\n"
      "v_add_f32 v26, v26, v30\n"
      "v_add_f32 v32, v32, v36\n"
      "v_add_f32 v34, v34, v38\n"
      "v_add_f32 v24, v24, v32\n"
      "v_add_f32 v26, v26, v34\n"
      "s_nop 1\n"
      "v_permlane32_swap_b32 v24, v26\n"
      "v_add_f32 v24, v24, v26\n"
      "v_mov_b32 v26, v24\n"
      "s_nop 1\n"
      "v_permlane16_swap_b32 v24, v26\n"
      "v_add_f32 v24, v24, v26\n"
      "ds_write_b32 v59, v24\n"
      "s_waitcnt lgkmcnt(0)\n"
      ".Ls_e21_%=:\n"
      "v_mfma_f32_16x16x32_bf16 v[96:99], v[164:167], a[32:35], v[96:99]\n"
      "s_waitcnt lgkmcnt(0)\n"
      "s_barrier\n"
      "v_mfma_f32_16x16x32_bf16 v[100:103], v[164:167], a[36:39], v[100:103]\n"
      "s_lshr_b32 s43, s42, 5\n"
      "s_lshl2_add_u32 s44, s36, s33\n"
      "s_cmp_eq_u32 s43, s44\n"
      "s_cbranch_scc0 .Ls_e25_%=\n"
      "ds_read_b32 v24, v58 offset:0\n"
      "ds_read_b32 v25, v58 offset:8192\n"
      "ds_read_b32 v26, v58 offset:16384\n"
      "ds_read_b32 v27, v58 offset:24576\n"
      "v_mov_b32 v63, s37\n"
      "s_or_b32 s43, s34, s35\n"
      "s_cmp_eq_u32 s43, 0\n"
      "s_cbranch_scc0 .Ls_z26_%=\n"
      "s_mov_b64 exec, 1\n"
      "global_store_dword v62, v63, s[58:59] sc1\n"
      ".Ls_z26_%=:\n"
      "s_mov_b64 exec, -1\n"
      "s_waitcnt vmcnt(6)\n"
      "v_lshlrev_b32 v57, 16, v57\n"
      "v_and_b32 v56, 48, v243\n"
      "s_mov_b32 s44, 131072\n"
      "v_lshl_add_u32 v56, v56, 1, s44\n"
      "s_waitcnt lgkmcnt(0)\n"
      "v_add_f32 v24, v24, v25\n"
      "s_waitcnt lgkmcnt(0)\n"
      "v_add_f32 v26, v26, v27\n"
      "v_add_f32 v24, v24, v26\n"
      "v_cvt_pk_bf16_f32 v24, v24, v24\n"
      "v_and_b32 v24, 0xffff0000, v24\n"
      "v_mul_f32 v24, v41, v24\n"
      "v_cvt_pk_bf16_f32 v24, v24, v24\n"
      "v_and_b32 v24, 0xffff0000, v24\n"
      "v_add_f32 v24, v24, v57\n"
      "v_cvt_pk_bf16_f32 v24, v24, v24\n"
      "v_and_b32 v24, 0xffff0000, v24\n"
      "v_sub_f32 v42, 1.0, v40\n"
      "v_cvt_pk_bf16_f32 v42, v42, v42\n"
      "v_and_b32 v42, 0xffff0000, v42\n"
      "v_mul_f32 v24, v42, v24\n"
      "ds_write_b32 v58, v24 offset:1024\n"
      "ds_read_b128 v[28:31], v56 offset:1024\n"
      "ds_read_b128 v[32:35], v56 offset:1040\n"
      ".Ls_e25_%=:\n"
      "v_mfma_f32_16x16x32_bf16 v[96:99], v[168:171], a[64:67], v[96:99]\n"
      "ds_read2st64_b32 v[116:117], v244 offset0:36 offset1:37\n"
      "v_mfma_f32_16x16x32_bf16 v[100:103], v[168:171], a[68:71], v[100:103]\n"
      "ds_read2st64_b32 v[118:119], v245 offset0:40 offset1:41\n"
      "v_mfma_f32_16x16x32_bf16 v[96:99], v[172:175], a[96:99], v[96:99]\n"
      "ds_read2st64_b32 v[120:121], v244 offset0:38 offset1:39\n"
      "v_mfma_f32_16x16x32_bf16 v[100:103], v[172:175], a[100:103], v[100:103]\n"
      "ds_read2st64_b32 v[122:123], v245 offset0:42 offset1:43\n"
      "v_mfma_f32_16x16x32_bf16 v[96:99], v[176:179], a[128:131], v[96:99]\n"
      "ds_read2st64_b32 v[124:125], v246 offset0:44 offset1:45\n"
      "v_mfma_f32_16x16x32_bf16 v[100:103], v[176:179], a[132:135], v[100:103]\n"
      "ds_read2st64_b32 v[126:127], v246 offset0:46 offset1:47\n"
      "v_mfma_f32_16x16x32_bf16 v[96:99], v[180:183], a[160:163], v[96:99]\n"
      "v_mfma_f32_16x16x32_bf16 v[100:103], v[180:183], a[164:167], v[100:103]\n"
      "v_mfma_f32_16x16x32_bf16 v[96:99], v[184:187], a[192:195], v[96:99]\n"
      "s_waitcnt vmcnt(3)\n"
      "v_bitop3_b32 v54, v47, v51, s37 bitop3:0x7e\n"
      "v_bitop3_b32 v54, v54, v55, s37 bitop3:0xf6\n"
      "v_cmp_ne_u32 vcc, 0, v54\n"
      "s_nop 1\n"
      "s_cbranch_vccnz .Ls_s28_%=\n"
      ".Ls_o29_%=:\n"
      "v_mfma_f32_16x16x32_bf16 v[100:103], v[184:187], a[196:199], v[100:103]\n"
      "s_waitcnt lgkmcnt(0)\n"
      "v_mfma_f32_16x16x32_bf16 v[96:99], v[250:253], v[116:119], v[96:99]\n"
      "v_mov_b32 v47, v52\n"
      "v_mfma_f32_16x16x32_bf16 v[96:99], v[188:191], a[224:227], v[96:99]\n"
      "v_mfma_f32_16x16x32_bf16 v[100:103], v[250:253], v[120:123], v[100:103]\n"
      "v_mov_b32 v51, v53\n"
      "v_mfma_f32_16x16x32_bf16 v[100:103], v[188:191], a[228:231], v[100:103]\n"
      "v_mfma_f32_16x16x32_bf16 v[96:99], v[252:255], v[124:127], v[96:99]\n"
      "v_mfma_f32_16x16x32_bf16 v[100:103], v[248:251], v[124:127], v[100:103]\n"
      "v_mfma_f32_16x16x32_bf16 v[64:67], v[252:255], v[44:47], v[64:67]\n"
      "v_mfma_f32_16x16x32_bf16 v[68:71], v[248:251], v[44:47], v[68:71]\n"
      "v_mfma_f32_16x16x32_bf16 v[96:99], v[252:255], v[48:51], v[96:99]\n"
      "v_mfma_f32_16x16x32_bf16 v[100:103], v[248:251], v[48:51], v[100:103]\n"
      "s_lshr_b32 s43, s42, 5\n"
      "s_lshl2_add_u32 s44, s36, s33\n"
      "s_cmp_eq_u32 s43, s44\n"
      "s_cbranch_scc0 .Ls_e30_%=\n"
      "v_lshrrev_b32 v24, 10, v7\n"
      "v_subrev_u32 v24, s42, v24\n"
      "v_cmp_eq_u32 vcc, 0, v24\n"
      "s_nop 1\n"
      "s_mov_b64 exec, vcc\n"
      "v_mov_b32 v204, v40\n"
      "v_mov_b32 v208, v28\n"
      "v_mov_b32 v209, v29\n"
      "v_mov_b32 v210, v30\n"
      "v_mov_b32 v211, v31\n"
      "v_mov_b32 v216, v32\n"
      "v_mov_b32 v217, v33\n"
      "v_mov_b32 v218, v34\n"
      "v_mov_b32 v219, v35\n"
      "s_mov_b64 exec, -1\n"
      "v_cmp_eq_u32 vcc, -16, v24\n"
      "s_nop 1\n"
      "s_mov_b64 exec, vcc\n"
      "v_mov_b32 v206, v40\n"
      "v_mov_b32 v212, v28\n"
      "v_mov_b32 v213, v29\n"
      "v_mov_b32 v214, v30\n"
      "v_mov_b32 v215, v31\n"
      "v_mov_b32 v220, v32\n"
      "v_mov_b32 v221, v33\n"
      "v_mov_b32 v222, v34\n"
      "v_mov_b32 v223, v35\n"
      "s_mov_b64 exec, -1\n"
      ".Ls_e30_%=:\n"
      "s_waitcnt vmcnt(0)\n"
      "v_bitop3_b32 v82, v75, v79, s37 bitop3:0x7e\n"
      "v_bitop3_b32 v82, v82, v83, s37 bitop3:0xf6\n"
      "v_cmp_ne_u32 vcc, 0, v82\n"
      "s_nop 1\n"
      "s_cbranch_vccnz .Ls_s32_%=\n"
      ".Ls_o33_%=:\n"
      "v_mov_b32 v75, v80\n"
      "v_mov_b32 v79, v81\n"
      "s_nop 0\n"
      "v_mfma_f32_16x16x32_bf16 v[64:67], v[252:255], v[72:75], v[64:67]\n"
      "v_mfma_f32_16x16x32_bf16 v[96:99], v[252:255], v[76:79], v[96:99]\n"
      "v_mfma_f32_16x16x32_bf16 v[68:71], v[248:251], v[72:75], v[68:71]\n"
      "v_mfma_f32_16x16x32_bf16 v[100:103], v[248:251], v[76:79], v[100:103]\n"
      "s_nop 4\n"
      "v_fma_f32 v208, v204, v64, v208\n"
      "v_fma_f32 v209, v204, v65, v209\n"
      "v_fma_f32 v210, v204, v66, v210\n"
      "v_fma_f32 v211, v204, v67, v211\n"
      "v_fma_f32 v216, v204, v96, v216\n"
      "v_fma_f32 v217, v204, v97, v217\n"
      "v_fma_f32 v218, v204, v98, v218\n"
      "v_fma_f32 v219, v204, v99, v219\n"
      "v_cvt_pk_bf16_f32 v104, v208, v209\n"
      "v_cvt_pk_bf16_f32 v105, v210, v211\n"
      "v_cvt_pk_bf16_f32 v106, v216, v217\n"
      "v_cvt_pk_bf16_f32 v107, v218, v219\n"
      "global_store_dwordx4 v7, v[104:107], s[20:21]\n"
      "v_fma_f32 v212, v206, v68, v212\n"
      "v_fma_f32 v213, v206, v69, v213\n"
      "v_fma_f32 v214, v206, v70, v214\n"
      "v_fma_f32 v215, v206, v71, v215\n"
      "v_fma_f32 v220, v206, v100, v220\n"
      "v_fma_f32 v221, v206, v101, v221\n"
      "v_fma_f32 v222, v206, v102, v222\n"
      "v_fma_f32 v223, v206, v103, v223\n"
      "v_cvt_pk_bf16_f32 v108, v212, v213\n"
      "v_cvt_pk_bf16_f32 v109, v214, v215\n"
      "v_cvt_pk_bf16_f32 v110, v220, v221\n"
      "v_cvt_pk_bf16_f32 v111, v222, v223\n"
      "global_store_dwordx4 v7, v[108:111], s[68:69]\n"
      "s_endpgm\n"
      ".Lprod_%=:\n"
      "s_waitcnt lgkmcnt(0)\n"
      "s_add_u32 s68, s16, s44\n"
      "s_addc_u32 s69, s17, 0\n"
      "s_add_u32 s12, s12, s45\n"
      "s_addc_u32 s13, s13, 0\n"
      "s_add_u32 s58, s18, 4718592\n"
      "s_addc_u32 s59, s19, 0\n"
      "v_mov_b32 v235, 0\n"
      "global_load_dword v235, v235, s[58:59] sc1\n"
      "v_and_b32 v238, 15, v243\n"
      "v_lshrrev_b32 v239, 4, v243\n"
      "v_xor_b32 v239, v239, v238\n"
      "v_and_b32 v239, 7, v239\n"
      "v_lshlrev_b32 v239, 4, v239\n"
      "v_lshl_or_b32 v238, v238, 7, v239\n"
      "v_add_u32 v248, s41, v238\n"
      "v_xor_b32 v249, 64, v248\n"
      "v_add_u32 v250, 65536, v238\n"
      "v_xor_b32 v251, 64, v250\n"
      "v_and_b32 v252, 8, v243\n"
      "v_lshlrev_b32 v252, 7, v252\n"
      "v_and_or_b32 v252, v243, 48, v252\n"
      "v_add_u32 v252, s41, v252\n"
      "v_add_u32 v238, s80, v242\n"
      "v_and_b32 v238, 0x3ff, v238\n"
      "v_lshl_or_b32 v244, v236, 12, v237\n"
      "v_add_u32 v245, 31744, v244\n"
      "v_add_u32 v246, 63488, v244\n"
      "v_add_u32 v247, 95232, v244\n"
      "s_bitcmp1_b32 s34, 1\n"
      "s_cselect_b64 s[8:9], s[10:11], s[8:9]\n"
      "s_and_b32 s45, s34, 1\n"
      "s_lshl2_add_u32 s45, s45, s33\n"
      "s_lshl_b32 s45, s45, 9\n"
      "s_lshl_b32 s76, s36, 19\n"
      "s_add_u32 s45, s45, s76\n"
      "s_add_u32 s8, s8, s45\n"
      "s_addc_u32 s9, s9, 0\n"
      "s_lshr_b32 s39, s32, 6\n"
      "s_and_b32 s45, s35, 2\n"
      "s_xor_b32 s39, s39, s45\n"
      "s_xor_b32 s39, s39, s33\n"
      "s_lshl_b32 s45, s39, 7\n"
      "s_add_u32 s76, s33, 0\n"
      "s_and_b32 s76, s76, 3\n"
      "s_lshl_b32 s76, s76, 17\n"
      "s_or_b32 s76, s76, s45\n"
      "s_add_u32 s77, s33, 1\n"
      "s_and_b32 s77, s77, 3\n"
      "s_lshl_b32 s77, s77, 17\n"
      "s_or_b32 s77, s77, s45\n"
      "s_add_u32 s78, s33, 2\n"
      "s_and_b32 s78, s78, 3\n"
      "s_lshl_b32 s78, s78, 17\n"
      "s_or_b32 s78, s78, s45\n"
      "s_add_u32 s79, s33, 3\n"
      "s_and_b32 s79, s79, 3\n"
      "s_lshl_b32 s79, s79, 17\n"
      "s_or_b32 s79, s79, s45\n"
      "s_lshl2_add_u32 s43, s34, s35\n"
      "s_and_b32 s44, s40, 31\n"
      "s_mul_i32 s45, s43, 266240\n"
      "s_lshl_b32 s42, s44, 13\n"
      "s_add_u32 s45, s45, s42\n"
      "s_add_u32 s45, s45, 4096\n"
      "s_add_u32 s62, s18, s45\n"
      "s_addc_u32 s63, s19, 0\n"
      "s_bitcmp1_b32 s34, 1\n"
      "s_cselect_b32 s85, 0x80008000, 0\n"
      "s_lshl3_add_u32 s42, s43, s83\n"
      "s_lshl2_add_u32 s42, s42, s33\n"
      "s_lshl_b32 s42, s42, 9\n"
      "s_add_u32 s42, s42, 1048576\n"
      "s_mov_b32 s64, 0xaaaaaaaa\n"
      "s_mov_b32 s65, 0xaaaaaaaa\n"
      "s_mov_b32 s66, 0xcccccccc\n"
      "s_mov_b32 s67, 0xcccccccc\n"
      "s_add_u32 s45, s80, 512\n"
      "s_and_b32 s45, s45, 0x380\n"
      "s_add_u32 s46, s68, s45\n"
      "s_addc_u32 s47, s69, 0\n"
      "s_add_u32 m0, s41, 8192\n"
      "s_nop 0\n"
      "global_load_lds_dwordx4 v240, s[46:47] offset:0\n"
      "global_load_lds_dwordx4 v241, s[46:47] offset:1024\n"
      "global_load_lds_dwordx4 v254, s[46:47] offset:2048\n"
      "global_load_lds_dwordx4 v255, s[46:47] offset:3072\n"
      "s_add_u32 s45, s80, 768\n"
      "s_and_b32 s45, s45, 0x380\n"
      "s_add_u32 s46, s68, s45\n"
      "s_addc_u32 s47, s69, 0\n"
      "s_add_u32 m0, s41, 12288\n"
      "s_nop 0\n"
      "global_load_lds_dwordx4 v240, s[46:47] offset:0\n"
      "global_load_lds_dwordx4 v241, s[46:47] offset:1024\n"
      "global_load_lds_dwordx4 v254, s[46:47] offset:2048\n"
      "global_load_lds_dwordx4 v255, s[46:47] offset:3072\n"
      "s_waitcnt vmcnt(13)\n"
      "s_barrier\n"
      "ds_read_b128 v[0:3], v248 offset:0\n"
      "ds_read_b128 v[4:7], v248 offset:2048\n"
      "ds_read_b128 v[8:11], v249 offset:0\n"
      "ds_read_b128 v[12:15], v249 offset:2048\n"
      "ds_read_b128 a[192:195], v250 offset:0\n"
      "ds_read_b128 a[196:199], v250 offset:2048\n"
      "ds_read_b128 a[200:203], v250 offset:32768\n"
      "ds_read_b128 a[204:207], v251 offset:0\n"
      "ds_read_b128 a[208:211], v251 offset:2048\n"
      "ds_read_b128 a[212:215], v251 offset:32768\n"
      "s_waitcnt lgkmcnt(5)\n"
      "v_mfma_f32_16x16x32_bf16 v[192:195], v[0:3], a[192:195], 0\n"
      "s_add_u32 s70, s34, 1\n"
      "s_and_b32 s70, s70, 3\n"
      "s_lshl_b32 s70, s70, 15\n"
      "s_or_b32 s70, s70, s81\n"
      "s_xor_b32 s70, s70, 128\n"
      "s_add_u32 s70, s12, s70\n"
      "s_addc_u32 s71, s13, 0\n"
      "s_add_u32 m0, s82, 81920\n"
      "v_mfma_f32_16x16x32_bf16 v[196:199], v[4:7], a[192:195], 0\n"
      "global_load_lds_dwordx4 v240, s[70:71] offset:0\n"
      "ds_read_b128 a[216:219], v250 offset:4096\n"
      "s_waitcnt lgkmcnt(5)\n"
      "v_mfma_f32_16x16x32_bf16 v[200:203], v[0:3], a[196:199], 0\n"
      "ds_read_b128 a[220:223], v250 offset:6144\n"
      "ds_read_b128 a[224:227], v250 offset:36864\n"
      "v_mfma_f32_16x16x32_bf16 v[204:207], v[4:7], a[196:199], 0\n"
      "global_load_lds_dwordx4 v241, s[70:71] offset:1024\n"
      "s_waitcnt vmcnt(11)\n"
      "ds_read_b128 v[16:19], v248 offset:4096\n"
      "s_waitcnt lgkmcnt(7)\n"
      "v_mfma_f32_16x16x32_bf16 v[208:211], v[0:3], a[200:203], 0\n"
      "ds_read_b128 v[20:23], v248 offset:6144\n"
      "ds_read_b128 v[24:27], v249 offset:4096\n"
      "v_mfma_f32_16x16x32_bf16 v[212:215], v[4:7], a[200:203], 0\n"
      "global_load_lds_dwordx4 v254, s[70:71] offset:2048\n"
      "ds_read_b128 v[28:31], v249 offset:6144\n"
      "s_waitcnt lgkmcnt(9)\n"
      "v_mfma_f32_16x16x32_bf16 v[192:195], v[8:11], a[204:207], v[192:195]\n"
      "ds_read_b128 a[228:231], v251 offset:4096\n"
      "ds_read_b128 a[232:235], v251 offset:6144\n"
      "v_mfma_f32_16x16x32_bf16 v[196:199], v[12:15], a[204:207], v[196:199]\n"
      "global_load_lds_dwordx4 v255, s[70:71] offset:3072\n"
      "ds_read_b128 a[236:239], v251 offset:36864\n"
      "s_waitcnt lgkmcnt(11)\n"
      "v_mfma_f32_16x16x32_bf16 v[200:203], v[8:11], a[208:211], v[200:203]\n"
      "s_add_u32 s70, s34, 2\n"
      "s_and_b32 s70, s70, 3\n"
      "s_lshl_b32 s70, s70, 15\n"
      "s_or_b32 s70, s70, s81\n"
      "s_xor_b32 s70, s70, 128\n"
      "s_add_u32 s70, s12, s70\n"
      "s_addc_u32 s71, s13, 0\n"
      "s_add_u32 m0, s82, 114688\n"
      "v_mfma_f32_16x16x32_bf16 v[204:207], v[12:15], a[208:211], v[204:207]\n"
      "global_load_lds_dwordx4 v240, s[70:71] offset:0\n"
      "s_waitcnt lgkmcnt(10)\n"
      "v_mfma_f32_16x16x32_bf16 v[208:211], v[8:11], a[212:215], v[208:211]\n"
      "v_mfma_f32_16x16x32_bf16 v[212:215], v[12:15], a[212:215], v[212:215]\n"
      "global_load_lds_dwordx4 v241, s[70:71] offset:1024\n"
      "s_waitcnt lgkmcnt(6)\n"
      "v_mfma_f32_16x16x32_bf16 v[192:195], v[16:19], a[216:219], v[192:195]\n"
      "s_add_u32 s70, s80, 128\n"
      "s_and_b32 s70, s70, 0x380\n"
      "s_add_u32 s70, s68, s70\n"
      "s_addc_u32 s71, s69, 0\n"
      "s_add_u32 m0, s41, 0\n"
      "s_waitcnt lgkmcnt(5)\n"
      "v_mfma_f32_16x16x32_bf16 v[196:199], v[20:23], a[216:219], v[196:199]\n"
      "global_load_lds_dwordx4 v240, s[70:71] offset:0\n"
      "ds_read_b128 a[240:243], v250 offset:8192\n"
      "v_mfma_f32_16x16x32_bf16 v[200:203], v[16:19], a[220:223], v[200:203]\n"
      "ds_read_b128 a[244:247], v250 offset:10240\n"
      "ds_read_b128 a[248:251], v250 offset:40960\n"
      "v_mfma_f32_16x16x32_bf16 v[204:207], v[20:23], a[220:223], v[204:207]\n"
      "global_load_lds_dwordx4 v241, s[70:71] offset:1024\n"
      "s_waitcnt vmcnt(12)\n"
      "ds_read_b128 v[32:35], v248 offset:8192\n"
      "v_mfma_f32_16x16x32_bf16 v[208:211], v[16:19], a[224:227], v[208:211]\n"
      "ds_read_b128 v[36:39], v248 offset:10240\n"
      "ds_read_b128 v[40:43], v249 offset:8192\n"
      "v_mfma_f32_16x16x32_bf16 v[212:215], v[20:23], a[224:227], v[212:215]\n"
      "global_load_lds_dwordx4 v254, s[70:71] offset:2048\n"
      "ds_read_b128 v[44:47], v249 offset:10240\n"
      "s_waitcnt lgkmcnt(9)\n"
      "v_mfma_f32_16x16x32_bf16 v[192:195], v[24:27], a[228:231], v[192:195]\n"
      "ds_read_b128 a[252:255], v251 offset:8192\n"
      "ds_read_b128 a[192:195], v251 offset:10240\n"
      "v_mfma_f32_16x16x32_bf16 v[196:199], v[28:31], a[228:231], v[196:199]\n"
      "global_load_lds_dwordx4 v255, s[70:71] offset:3072\n"
      "ds_read_b128 a[196:199], v251 offset:40960\n"
      "s_waitcnt lgkmcnt(11)\n"
      "v_mfma_f32_16x16x32_bf16 v[200:203], v[24:27], a[232:235], v[200:203]\n"
      "s_add_u32 s70, s80, 384\n"
      "s_and_b32 s70, s70, 0x380\n"
      "s_add_u32 s70, s68, s70\n"
      "s_addc_u32 s71, s69, 0\n"
      "s_add_u32 m0, s41, 4096\n"
      "v_mfma_f32_16x16x32_bf16 v[204:207], v[28:31], a[232:235], v[204:207]\n"
      "global_load_lds_dwordx4 v240, s[70:71] offset:0\n"
      "s_waitcnt lgkmcnt(10)\n"
      "v_mfma_f32_16x16x32_bf16 v[208:211], v[24:27], a[236:239], v[208:211]\n"
      "v_mfma_f32_16x16x32_bf16 v[212:215], v[28:31], a[236:239], v[212:215]\n"
      "global_load_lds_dwordx4 v241, s[70:71] offset:1024\n"
      "s_waitcnt lgkmcnt(6)\n"
      "v_mfma_f32_16x16x32_bf16 v[192:195], v[32:35], a[240:243], v[192:195]\n"
      "ds_read_b128 a[200:203], v250 offset:12288\n"
      "ds_read_b128 a[204:207], v250 offset:14336\n"
      "s_waitcnt lgkmcnt(7)\n"
      "v_mfma_f32_16x16x32_bf16 v[196:199], v[36:39], a[240:243], v[196:199]\n"
      "global_load_lds_dwordx4 v254, s[70:71] offset:2048\n"
      "ds_read_b128 a[208:211], v250 offset:45056\n"
      "v_mfma_f32_16x16x32_bf16 v[200:203], v[32:35], a[244:247], v[200:203]\n"
      "s_waitcnt vmcnt(13)\n"
      "ds_read_b128 v[48:51], v248 offset:12288\n"
      "v_mfma_f32_16x16x32_bf16 v[204:207], v[36:39], a[244:247], v[204:207]\n"
      "global_load_lds_dwordx4 v255, s[70:71] offset:3072\n"
      "ds_read_b128 v[52:55], v248 offset:14336\n"
      "v_mfma_f32_16x16x32_bf16 v[208:211], v[32:35], a[248:251], v[208:211]\n"
      "s_add_u32 s70, s80, 640\n"
      "s_and_b32 s70, s70, 0x380\n"
      "s_add_u32 s70, s68, s70\n"
      "s_addc_u32 s71, s69, 0\n"
      "s_waitcnt lgkmcnt(8)\n"
      "s_add_u32 m0, s41, 8192\n"
      "v_mfma_f32_16x16x32_bf16 v[212:215], v[36:39], a[248:251], v[212:215]\n"
      "global_load_lds_dwordx4 v240, s[70:71] offset:0\n"
      "ds_read_b128 v[56:59], v249 offset:12288\n"
      "s_waitcnt lgkmcnt(8)\n"
      "v_mfma_f32_16x16x32_bf16 v[192:195], v[40:43], a[252:255], v[192:195]\n"
      "ds_read_b128 v[60:63], v249 offset:14336\n"
      "ds_read_b128 a[212:215], v251 offset:12288\n"
      "v_readfirstlane_b32 s37, v235\n"
      "v_mfma_f32_16x16x32_bf16 v[196:199], v[44:47], a[252:255], v[196:199]\n"
      "s_nop 0\n"
      "s_mul_i32 s37, s37, 0x9E3779B1\n"
      "global_load_lds_dwordx4 v241, s[70:71] offset:1024\n"
      "s_waitcnt lgkmcnt(9)\n"
      "v_mfma_f32_16x16x32_bf16 v[200:203], v[40:43], a[192:195], v[200:203]\n"
      "s_add_u32 s37, s37, 0x7F4A7C15\n"
      "ds_read_b128 a[216:219], v251 offset:14336\n"
      "ds_read_b128 a[220:223], v251 offset:45056\n"
      "s_mov_b32 s38, 0x8000\n"
      "v_mfma_f32_16x16x32_bf16 v[204:207], v[44:47], a[192:195], v[204:207]\n"
      "global_load_lds_dwordx4 v254, s[70:71] offset:2048\n"
      "s_waitcnt lgkmcnt(10)\n"
      "v_mfma_f32_16x16x32_bf16 v[208:211], v[40:43], a[196:199], v[208:211]\n"
      "v_mfma_f32_16x16x32_bf16 v[212:215], v[44:47], a[196:199], v[212:215]\n"
      "global_load_lds_dwordx4 v255, s[70:71] offset:3072\n"
      "s_waitcnt lgkmcnt(6)\n"
      "v_mfma_f32_16x16x32_bf16 v[192:195], v[48:51], a[200:203], v[192:195]\n"
      "s_add_u32 s70, s80, 896\n"
      "s_and_b32 s70, s70, 0x380\n"
      "s_add_u32 s70, s68, s70\n"
      "s_addc_u32 s71, s69, 0\n"
      "s_waitcnt lgkmcnt(3)\n"
      "s_add_u32 m0, s41, 12288\n"
      "v_mfma_f32_16x16x32_bf16 v[196:199], v[52:55], a[200:203], v[196:199]\n"
      "global_load_lds_dwordx4 v240, s[70:71] offset:0\n"
      "s_waitcnt vmcnt(13)\n"
      "s_barrier\n"
      "v_mfma_f32_16x16x32_bf16 v[200:203], v[48:51], a[204:207], v[200:203]\n"
      "ds_read_b128 a[224:227], v250 offset:16384\n"
      "ds_read_b128 a[228:231], v250 offset:18432\n"
      "v_mfma_f32_16x16x32_bf16 v[204:207], v[52:55], a[204:207], v[204:207]\n"
      "global_load_lds_dwordx4 v241, s[70:71] offset:1024\n"
      "ds_read_b128 a[232:235], v250 offset:49152\n"
      "v_mfma_f32_16x16x32_bf16 v[208:211], v[48:51], a[208:211], v[208:211]\n"
      "s_waitcnt vmcnt(10)\n"
      "ds_read_b128 v[64:67], v248 offset:0\n"
      "v_mfma_f32_16x16x32_bf16 v[212:215], v[52:55], a[208:211], v[212:215]\n"
      "global_load_lds_dwordx4 v254, s[70:71] offset:2048\n"
      "ds_read_b128 v[68:71], v248 offset:2048\n"
      "s_waitcnt lgkmcnt(7)\n"
      "v_mfma_f32_16x16x32_bf16 v[192:195], v[56:59], a[212:215], v[192:195]\n"
      "ds_read_b128 v[72:75], v249 offset:0\n"
      "ds_read_b128 v[76:79], v249 offset:2048\n"
      "v_mfma_f32_16x16x32_bf16 v[196:199], v[60:63], a[212:215], v[196:199]\n"
      "global_load_lds_dwordx4 v255, s[70:71] offset:3072\n"
      "ds_read_b128 a[236:239], v251 offset:16384\n"
      "s_waitcnt lgkmcnt(9)\n"
      "v_mfma_f32_16x16x32_bf16 v[200:203], v[56:59], a[216:219], v[200:203]\n"
      "ds_read_b128 a[240:243], v251 offset:18432\n"
      "ds_read_b128 a[244:247], v251 offset:49152\n"
      "v_mfma_f32_16x16x32_bf16 v[204:207], v[60:63], a[216:219], v[204:207]\n"
      "s_waitcnt lgkmcnt(10)\n"
      "v_mfma_f32_16x16x32_bf16 v[208:211], v[56:59], a[220:223], v[208:211]\n"
      "v_mfma_f32_16x16x32_bf16 v[212:215], v[60:63], a[220:223], v[212:215]\n"
      "s_waitcnt lgkmcnt(6)\n"
      "v_mfma_f32_16x16x32_bf16 v[192:195], v[64:67], a[224:227], v[192:195]\n"
      "s_waitcnt lgkmcnt(3)\n"
      "s_add_u32 m0, s41, 0\n"
      "s_nop 0\n"
      "global_load_lds_dwordx4 v238, s[24:25]\n"
      "s_add_u32 m0, s41, 1024\n"
      "s_nop 0\n"
      "global_load_lds_dwordx4 v238, s[22:23]\n"
      "v_mfma_f32_16x16x32_bf16 v[196:199], v[68:71], a[224:227], v[196:199]\n"
      "ds_read_b128 a[248:251], v250 offset:20480\n"
      "ds_read_b128 a[252:255], v250 offset:22528\n"
      "v_mfma_f32_16x16x32_bf16 v[200:203], v[64:67], a[228:231], v[200:203]\n"
      "s_add_u32 s70, s34, 2\n"
      "s_and_b32 s70, s70, 3\n"
      "s_lshl_b32 s70, s70, 15\n"
      "s_or_b32 s70, s70, s81\n"
      "s_add_u32 s70, s12, s70\n"
      "s_addc_u32 s71, s13, 0\n"
      "s_add_u32 m0, s82, 98304\n"
      "v_mfma_f32_16x16x32_bf16 v[204:207], v[68:71], a[228:231], v[204:207]\n"
      "global_load_lds_dwordx4 v254, s[70:71] offset:2048\n"
      "ds_read_b128 a[192:195], v250 offset:53248\n"
      "v_mfma_f32_16x16x32_bf16 v[208:211], v[64:67], a[232:235], v[208:211]\n"
      "s_waitcnt vmcnt(11)\n"
      "ds_read_b128 v[80:83], v248 offset:4096\n"
      "v_mfma_f32_16x16x32_bf16 v[212:215], v[68:71], a[232:235], v[212:215]\n"
      "ds_read_b128 v[84:87], v248 offset:6144\n"
      "ds_read_b128 v[88:91], v249 offset:4096\n"
      "s_waitcnt lgkmcnt(8)\n"
      "v_mfma_f32_16x16x32_bf16 v[192:195], v[72:75], a[236:239], v[192:195]\n"
      "global_load_lds_dwordx4 v255, s[70:71] offset:3072\n"
      "ds_read_b128 v[92:95], v249 offset:6144\n"
      "v_mfma_f32_16x16x32_bf16 v[196:199], v[76:79], a[236:239], v[196:199]\n"
      "ds_read_b128 a[196:199], v251 offset:20480\n"
      "ds_read_b128 a[200:203], v251 offset:22528\n"
      "s_waitcnt lgkmcnt(10)\n"
      "v_mfma_f32_16x16x32_bf16 v[200:203], v[72:75], a[240:243], v[200:203]\n"
      "s_add_u32 s70, s34, 2\n"
      "s_and_b32 s70, s70, 3\n"
      "s_lshl_b32 s70, s70, 15\n"
      "s_or_b32 s70, s70, s81\n"
      "s_xor_b32 s70, s70, 128\n"
      "s_add_u32 s70, s12, s70\n"
      "s_addc_u32 s71, s13, 0\n"
      "s_add_u32 m0, s82, 114688\n"
      "v_mfma_f32_16x16x32_bf16 v[204:207], v[76:79], a[240:243], v[204:207]\n"
      "global_load_lds_dwordx4 v254, s[70:71] offset:2048\n"
      "ds_read_b128 a[204:207], v251 offset:53248\n"
      "s_waitcnt lgkmcnt(10)\n"
      "v_mfma_f32_16x16x32_bf16 v[208:211], v[72:75], a[244:247], v[208:211]\n"
      "v_mfma_f32_16x16x32_bf16 v[212:215], v[76:79], a[244:247], v[212:215]\n"
      "s_waitcnt lgkmcnt(6)\n"
      "v_mfma_f32_16x16x32_bf16 v[192:195], v[80:83], a[248:251], v[192:195]\n"
      "global_load_lds_dwordx4 v255, s[70:71] offset:3072\n"
      "ds_read_b128 a[208:211], v250 offset:24576\n"
      "s_waitcnt lgkmcnt(6)\n"
      "v_mfma_f32_16x16x32_bf16 v[196:199], v[84:87], a[248:251], v[196:199]\n"
      "ds_read_b128 a[212:215], v250 offset:26624\n"
      "ds_read_b128 a[216:219], v250 offset:57344\n"
      "v_mfma_f32_16x16x32_bf16 v[200:203], v[80:83], a[252:255], v[200:203]\n"
      "s_add_u32 s70, s34, 3\n"
      "s_and_b32 s70, s70, 3\n"
      "s_lshl_b32 s70, s70, 15\n"
      "s_or_b32 s70, s70, s81\n"
      "s_add_u32 s70, s12, s70\n"
      "s_addc_u32 s71, s13, 0\n"
      "s_add_u32 m0, s82, 131072\n"
      "v_mfma_f32_16x16x32_bf16 v[204:207], v[84:87], a[252:255], v[204:207]\n"
      "global_load_lds_dwordx4 v240, s[70:71] offset:0\n"
      "s_waitcnt vmcnt(11)\n"
      "ds_read_b128 v[96:99], v248 offset:8192\n"
      "v_mfma_f32_16x16x32_bf16 v[208:211], v[80:83], a[192:195], v[208:211]\n"
      "ds_read_b128 v[100:103], v248 offset:10240\n"
      "ds_read_b128 v[104:107], v249 offset:8192\n"
      "v_mfma_f32_16x16x32_bf16 v[212:215], v[84:87], a[192:195], v[212:215]\n"
      "ds_read_b128 v[108:111], v249 offset:10240\n"
      "ds_read_b128 a[220:223], v251 offset:24576\n"
      "s_waitcnt lgkmcnt(10)\n"
      "v_mfma_f32_16x16x32_bf16 v[192:195], v[88:91], a[196:199], v[192:195]\n"
      "global_load_lds_dwordx4 v241, s[70:71] offset:1024\n"
      "ds_read_b128 a[224:227], v251 offset:26624\n"
      "v_mfma_f32_16x16x32_bf16 v[196:199], v[92:95], a[196:199], v[196:199]\n"
      "ds_read_b128 a[228:231], v251 offset:57344\n"
      "s_waitcnt lgkmcnt(11)\n"
      "v_mfma_f32_16x16x32_bf16 v[200:203], v[88:91], a[200:203], v[200:203]\n"
      "v_mfma_f32_16x16x32_bf16 v[204:207], v[92:95], a[200:203], v[204:207]\n"
      "global_load_lds_dwordx4 v254, s[70:71] offset:2048\n"
      "s_waitcnt lgkmcnt(10)\n"
      "v_mfma_f32_16x16x32_bf16 v[208:211], v[88:91], a[204:207], v[208:211]\n"
      "v_mfma_f32_16x16x32_bf16 v[212:215], v[92:95], a[204:207], v[212:215]\n"
      "s_waitcnt lgkmcnt(6)\n"
      "v_mfma_f32_16x16x32_bf16 v[192:195], v[96:99], a[208:211], v[192:195]\n"
      "global_load_lds_dwordx4 v255, s[70:71] offset:3072\n"
      "ds_read_b128 a[232:235], v250 offset:28672\n"
      "s_waitcnt lgkmcnt(6)\n"
      "v_mfma_f32_16x16x32_bf16 v[196:199], v[100:103], a[208:211], v[196:199]\n"
      "ds_read_b128 a[236:239], v250 offset:30720\n"
      "ds_read_b128 a[240:243], v250 offset:61440\n"
      "v_mfma_f32_16x16x32_bf16 v[200:203], v[96:99], a[212:215], v[200:203]\n"
      "s_add_u32 s70, s34, 3\n"
      "s_and_b32 s70, s70, 3\n"
      "s_lshl_b32 s70, s70, 15\n"
      "s_or_b32 s70, s70, s81\n"
      "s_xor_b32 s70, s70, 128\n"
      "s_add_u32 s70, s12, s70\n"
      "s_addc_u32 s71, s13, 0\n"
      "s_add_u32 m0, s82, 147456\n"
      "v_mfma_f32_16x16x32_bf16 v[204:207], v[100:103], a[212:215], v[204:207]\n"
      "global_load_lds_dwordx4 v240, s[70:71] offset:0\n"
      "s_waitcnt vmcnt(11)\n"
      "ds_read_b128 v[112:115], v248 offset:12288\n"
      "v_mfma_f32_16x16x32_bf16 v[208:211], v[96:99], a[216:219], v[208:211]\n"
      "ds_read_b128 v[116:119], v248 offset:14336\n"
      "ds_read_b128 v[120:123], v249 offset:12288\n"
      "v_mfma_f32_16x16x32_bf16 v[212:215], v[100:103], a[216:219], v[212:215]\n"
      "ds_read_b128 v[124:127], v249 offset:14336\n"
      "ds_read_b128 a[244:247], v251 offset:28672\n"
      "s_waitcnt lgkmcnt(10)\n"
      "v_mfma_f32_16x16x32_bf16 v[192:195], v[104:107], a[220:223], v[192:195]\n"
      "global_load_lds_dwordx4 v241, s[70:71] offset:1024\n"
      "ds_read_b128 a[248:251], v251 offset:30720\n"
      "v_mfma_f32_16x16x32_bf16 v[196:199], v[108:111], a[220:223], v[196:199]\n"
      "ds_read_b128 a[252:255], v251 offset:61440\n"
      "s_waitcnt lgkmcnt(11)\n"
      "v_mfma_f32_16x16x32_bf16 v[200:203], v[104:107], a[224:227], v[200:203]\n"
      "v_mfma_f32_16x16x32_bf16 v[204:207], v[108:111], a[224:227], v[204:207]\n"
      "global_load_lds_dwordx4 v254, s[70:71] offset:2048\n"
      "s_waitcnt lgkmcnt(10)\n"
      "v_mfma_f32_16x16x32_bf16 v[208:211], v[104:107], a[228:231], v[208:211]\n"
      "v_mfma_f32_16x16x32_bf16 v[212:215], v[108:111], a[228:231], v[212:215]\n"
      "s_waitcnt lgkmcnt(6)\n"
      "v_mfma_f32_16x16x32_bf16 v[192:195], v[112:115], a[232:235], v[192:195]\n"
      "s_waitcnt vmcnt(11)\n"
      "ds_read_b128 v[136:139], v252 offset:0\n"
      "s_waitcnt lgkmcnt(6)\n"
      "v_mfma_f32_16x16x32_bf16 v[196:199], v[116:119], a[232:235], v[196:199]\n"
      "ds_read_b128 v[140:143], v252 offset:64\n"
      "global_load_lds_dwordx4 v255, s[70:71] offset:3072\n"
      "s_waitcnt lgkmcnt(4)\n"
      "v_mfma_f32_16x16x32_bf16 v[192:195], v[120:123], a[244:247], v[192:195]\n"
      "ds_read_b128 v[144:147], v252 offset:256\n"
      "v_mfma_f32_16x16x32_bf16 v[196:199], v[124:127], a[244:247], v[196:199]\n"
      "v_mfma_f32_16x16x32_bf16 v[200:203], v[112:115], a[236:239], v[200:203]\n"
      "ds_read_b128 v[148:151], v252 offset:320\n"
      "v_mfma_f32_16x16x32_bf16 v[204:207], v[116:119], a[236:239], v[204:207]\n"
      "s_waitcnt lgkmcnt(5)\n"
      "v_mfma_f32_16x16x32_bf16 v[200:203], v[120:123], a[248:251], v[200:203]\n"
      "ds_read_b128 v[152:155], v252 offset:512\n"
      "v_cvt_pk_bf16_f32 v224, v192, v193\n"
      "v_cvt_pk_bf16_f32 v225, v194, v195\n"
      "v_mfma_f32_16x16x32_bf16 v[204:207], v[124:127], a[248:251], v[204:207]\n"
      "v_cvt_pk_bf16_f32 v226, v196, v197\n"
      "v_cvt_pk_bf16_f32 v227, v198, v199\n"
      "v_xor_b32 v224, s85, v224\n"
      "v_mfma_f32_16x16x32_bf16 v[208:211], v[112:115], a[240:243], v[208:211]\n"
      "ds_read_b128 v[156:159], v252 offset:576\n"
      "v_xor_b32 v225, s85, v225\n"
      "v_xor_b32 v226, s85, v226\n"
      "v_mfma_f32_16x16x32_bf16 v[212:215], v[116:119], a[240:243], v[212:215]\n"
      "v_xor_b32 v227, s85, v227\n"
      "s_waitcnt lgkmcnt(6)\n"
      "v_mfma_f32_16x16x32_bf16 v[208:211], v[120:123], a[252:255], v[208:211]\n"
      "ds_read_b128 v[160:163], v252 offset:768\n"
      "global_store_dwordx4 v242, v[224:227], s[62:63] offset:-4096 sc1\n"
      "v_cvt_pk_bf16_f32 v228, v200, v201\n"
      "v_mfma_f32_16x16x32_bf16 v[212:215], v[124:127], a[252:255], v[212:215]\n"
      "v_cvt_pk_bf16_f32 v229, v202, v203\n"
      "v_cvt_pk_bf16_f32 v230, v204, v205\n"
      "v_cvt_pk_bf16_f32 v231, v206, v207\n"
      "s_waitcnt lgkmcnt(6)\n"
      "v_mfma_f32_16x16x32_bf16 v[128:131], v[0:3], v[136:139], 0\n"
      "ds_read_b128 v[164:167], v252 offset:832\n"
      "v_xor_b32 v228, s85, v228\n"
      "v_xor_b32 v229, s85, v229\n"
      "v_mfma_f32_16x16x32_bf16 v[132:135], v[4:7], v[136:139], 0\n"
      "v_xor_b32 v230, s85, v230\n"
      "v_xor_b32 v231, s85, v231\n"
      "s_waitcnt lgkmcnt(6)\n"
      "v_mfma_f32_16x16x32_bf16 v[128:131], v[8:11], v[140:143], v[128:131]\n"
      "ds_read_b128 v[136:139], v252 offset:128\n"
      "global_store_dwordx4 v242, v[228:231], s[62:63] offset:-3072 sc1\n"
      "v_cvt_pk_bf16_f32 v232, v208, v209\n"
      "v_mfma_f32_16x16x32_bf16 v[132:135], v[12:15], v[140:143], v[132:135]\n"
      "v_cvt_pk_bf16_f32 v233, v210, v211\n"
      "v_cvt_pk_bf16_f32 v234, v212, v213\n"
      "v_cvt_pk_bf16_f32 v235, v214, v215\n"
      "s_waitcnt lgkmcnt(6)\n"
      "v_mfma_f32_16x16x32_bf16 v[128:131], v[16:19], v[144:147], v[128:131]\n"
      "ds_read_b128 v[140:143], v252 offset:192\n"
      "v_xor_b32 v232, s85, v232\n"
      "v_xor_b32 v233, s85, v233\n"
      "v_mfma_f32_16x16x32_bf16 v[132:135], v[20:23], v[144:147], v[132:135]\n"
      "v_xor_b32 v234, s85, v234\n"
      "v_xor_b32 v235, s85, v235\n"
      "s_waitcnt lgkmcnt(6)\n"
      "v_mfma_f32_16x16x32_bf16 v[128:131], v[24:27], v[148:151], v[128:131]\n"
      "ds_read_b128 v[144:147], v252 offset:384\n"
      "global_store_dwordx4 v242, v[232:235], s[62:63] offset:-2048 sc1\n"
      "v_mfma_f32_16x16x32_bf16 v[132:135], v[28:31], v[148:151], v[132:135]\n"
      "s_xor_b32 s70, s76, 0\n"
      "s_add_u32 s70, s8, s70\n"
      "s_addc_u32 s71, s9, 0\n"
      "s_add_u32 m0, s41, 4096\n"
      "s_waitcnt lgkmcnt(6)\n"
      "v_mfma_f32_16x16x32_bf16 v[128:131], v[32:35], v[152:155], v[128:131]\n"
      "s_add_u32 s48, s82, 32768\n"
      "ds_read_b128 v[148:151], v252 offset:448\n"
      "global_load_lds_dwordx4 v244, s[70:71] offset:0\n"
      "v_subrev_u32 v236, s41, v248\n"
      "v_mfma_f32_16x16x32_bf16 v[132:135], v[36:39], v[152:155], v[132:135]\n"
      "s_add_u32 s48, s48, 65536\n"
      "global_load_lds_dwordx4 v245, s[70:71] offset:1024\n"
      "v_add_u32 v236, s48, v236\n"
      "v_xor_b32 v237, 64, v236\n"
      "s_waitcnt lgkmcnt(6)\n"
      "v_mfma_f32_16x16x32_bf16 v[128:131], v[40:43], v[156:159], v[128:131]\n"
      "ds_read_b128 v[152:155], v252 offset:640\n"
      "global_load_lds_dwordx4 v246, s[70:71] offset:2048\n"
      "v_mfma_f32_16x16x32_bf16 v[132:135], v[44:47], v[156:159], v[132:135]\n"
      "global_load_lds_dwordx4 v247, s[70:71] offset:3072\n"
      "s_xor_b32 s70, s77, 0\n"
      "s_add_u32 s70, s8, s70\n"
      "s_addc_u32 s71, s9, 0\n"
      "s_add_u32 m0, s41, 8192\n"
      "s_waitcnt lgkmcnt(6)\n"
      "v_mfma_f32_16x16x32_bf16 v[128:131], v[48:51], v[160:163], v[128:131]\n"
      "ds_read_b128 v[156:159], v252 offset:704\n"
      "global_load_lds_dwordx4 v244, s[70:71] offset:0\n"
      "v_mfma_f32_16x16x32_bf16 v[132:135], v[52:55], v[160:163], v[132:135]\n"
      "global_load_lds_dwordx4 v245, s[70:71] offset:1024\n"
      "s_waitcnt lgkmcnt(6)\n"
      "v_mfma_f32_16x16x32_bf16 v[128:131], v[56:59], v[164:167], v[128:131]\n"
      "ds_read_b128 v[160:163], v252 offset:896\n"
      "global_load_lds_dwordx4 v246, s[70:71] offset:2048\n"
      "v_mfma_f32_16x16x32_bf16 v[132:135], v[60:63], v[164:167], v[132:135]\n"
      "global_load_lds_dwordx4 v247, s[70:71] offset:3072\n"
      "s_waitcnt vmcnt(11)\n"
      "s_barrier\n"
      "s_waitcnt lgkmcnt(6)\n"
      "v_mfma_f32_16x16x32_bf16 v[128:131], v[64:67], v[136:139], v[128:131]\n"
      "ds_read_b128 v[164:167], v252 offset:960\n"
      "s_add_u32 s70, s34, 4\n"
      "s_and_b32 s70, s70, 3\n"
      "s_lshl_b32 s70, s70, 15\n"
      "s_or_b32 s70, s70, s81\n"
      "s_add_u32 s70, s12, s70\n"
      "s_addc_u32 s71, s13, 0\n"
      "s_add_u32 m0, s82, 65536\n"
      "v_mfma_f32_16x16x32_bf16 v[132:135], v[68:71], v[136:139], v[132:135]\n"
      "global_load_lds_dwordx4 v240, s[70:71] offset:0\n"
      "ds_read_b128 a[192:195], v250 offset:34816\n"
      "s_waitcnt lgkmcnt(7)\n"
      "v_mfma_f32_16x16x32_bf16 v[128:131], v[72:75], v[140:143], v[128:131]\n"
      "ds_read_b128 a[196:199], v251 offset:34816\n"
      "ds_read_b128 a[200:203], v250 offset:38912\n"
      "v_mfma_f32_16x16x32_bf16 v[132:135], v[76:79], v[140:143], v[132:135]\n"
      "ds_read_b128 a[204:207], v251 offset:38912\n"
      "ds_read_b128 a[208:211], v250 offset:43008\n"
      "s_waitcnt lgkmcnt(10)\n"
      "v_mfma_f32_16x16x32_bf16 v[128:131], v[80:83], v[144:147], v[128:131]\n"
      "ds_read_b128 a[212:215], v251 offset:43008\n"
      "ds_read_b128 a[216:219], v250 offset:47104\n"
      "v_mfma_f32_16x16x32_bf16 v[132:135], v[84:87], v[144:147], v[132:135]\n"
      "global_load_lds_dwordx4 v241, s[70:71] offset:1024\n"
      "ds_read_b128 a[220:223], v251 offset:47104\n"
      "s_waitcnt lgkmcnt(12)\n"
      "v_mfma_f32_16x16x32_bf16 v[128:131], v[88:91], v[148:151], v[128:131]\n"
      "ds_read_b128 a[224:227], v250 offset:51200\n"
      "ds_read_b128 a[228:231], v251 offset:51200\n"
      "v_mfma_f32_16x16x32_bf16 v[132:135], v[92:95], v[148:151], v[132:135]\n"
      "ds_read_b128 a[232:235], v250 offset:55296\n"
      "s_waitcnt lgkmcnt(14)\n"
      "v_mfma_f32_16x16x32_bf16 v[128:131], v[96:99], v[152:155], v[128:131]\n"
      "v_mfma_f32_16x16x32_bf16 v[132:135], v[100:103], v[152:155], v[132:135]\n"
      "global_load_lds_dwordx4 v254, s[70:71] offset:2048\n"
      "ds_read_b128 a[236:239], v251 offset:55296\n"
      "s_waitcnt lgkmcnt(14)\n"
      "v_mfma_f32_16x16x32_bf16 v[128:131], v[104:107], v[156:159], v[128:131]\n"
      "v_mfma_f32_16x16x32_bf16 v[132:135], v[108:111], v[156:159], v[132:135]\n"
      "ds_read_b128 a[240:243], v250 offset:59392\n"
      "s_waitcnt lgkmcnt(14)\n"
      "v_mfma_f32_16x16x32_bf16 v[128:131], v[112:115], v[160:163], v[128:131]\n"
      "v_mfma_f32_16x16x32_bf16 v[132:135], v[116:119], v[160:163], v[132:135]\n"
      "global_load_lds_dwordx4 v255, s[70:71] offset:3072\n"
      "ds_read_b128 a[244:247], v251 offset:59392\n"
      "s_waitcnt lgkmcnt(14)\n"
      "v_mfma_f32_16x16x32_bf16 v[128:131], v[120:123], v[164:167], v[128:131]\n"
      "v_mfma_f32_16x16x32_bf16 v[132:135], v[124:127], v[164:167], v[132:135]\n"
      "ds_read_b128 a[248:251], v250 offset:63488\n"
      "s_waitcnt lgkmcnt(14)\n"
      "v_mfma_f32_16x16x32_bf16 v[216:219], v[0:3], a[192:195], 0\n"
      "v_mfma_f32_16x16x32_bf16 v[220:223], v[4:7], a[192:195], 0\n"
      "s_add_u32 s70, s34, 4\n"
      "s_and_b32 s70, s70, 3\n"
      "s_lshl_b32 s70, s70, 15\n"
      "s_or_b32 s70, s70, s81\n"
      "s_xor_b32 s70, s70, 128\n"
      "s_add_u32 s70, s12, s70\n"
      "s_addc_u32 s71, s13, 0\n"
      "s_add_u32 m0, s82, 81920\n"
      "s_waitcnt lgkmcnt(13)\n"
      "v_mfma_f32_16x16x32_bf16 v[216:219], v[8:11], a[196:199], v[216:219]\n"
      "s_mov_b32 s22, 0xf0f0f0f0\n"
      "global_load_lds_dwordx4 v240, s[70:71] offset:0\n"
      "ds_read_b128 a[252:255], v251 offset:63488\n"
      "s_mov_b32 s23, 0xf0f0f0f0\n"
      "v_mfma_f32_16x16x32_bf16 v[220:223], v[12:15], a[196:199], v[220:223]\n"
      "v_xor_b32 v250, 196608, v250\n"
      "v_xor_b32 v251, 196608, v251\n"
      "s_waitcnt lgkmcnt(13)\n"
      "v_mfma_f32_16x16x32_bf16 v[216:219], v[16:19], a[200:203], v[216:219]\n"
      "ds_read_b128 a[192:195], v250 offset:0\n"
      "v_cndmask_b32 v128, v128, v129, s[64:65]\n"
      "v_cndmask_b32 v130, v130, v131, s[64:65]\n"
      "v_mfma_f32_16x16x32_bf16 v[220:223], v[20:23], a[200:203], v[220:223]\n"
      "ds_read_b128 a[196:199], v250 offset:2048\n"
      "v_cndmask_b32 v132, v132, v133, s[64:65]\n"
      "v_cndmask_b32 v134, v134, v135, s[64:65]\n"
      "s_waitcnt lgkmcnt(14)\n"
      "v_mfma_f32_16x16x32_bf16 v[216:219], v[24:27], a[204:207], v[216:219]\n"
      "global_load_lds_dwordx4 v241, s[70:71] offset:1024\n"
      "v_cndmask_b32 v128, v128, v130, s[66:67]\n"
      "v_cndmask_b32 v132, v132, v134, s[66:67]\n"
      "v_mfma_f32_16x16x32_bf16 v[220:223], v[28:31], a[204:207], v[220:223]\n"
      "ds_read_b128 a[200:203], v251 offset:0\n"
      "v_lshrrev_b32 v133, 4, v243\n"
      "v_cndmask_b32 v128, v128, v132, s[22:23]\n"
      "s_waitcnt lgkmcnt(14)\n"
      "v_mfma_f32_16x16x32_bf16 v[216:219], v[32:35], a[208:211], v[216:219]\n"
      "v_and_b32 v134, 7, v243\n"
      "v_cvt_pk_bf16_f32 v128, v128, v128\n"
      "s_lshl_b32 s48, s40, 5\n"
      "v_mfma_f32_16x16x32_bf16 v[220:223], v[36:39], a[208:211], v[220:223]\n"
      "ds_read_b128 a[204:207], v251 offset:2048\n"
      "v_and_b32 v128, 0xffff0000, v128\n"
      "v_lshl_or_b32 v133, v133, 3, v134\n"
      "s_waitcnt lgkmcnt(14)\n"
      "v_mfma_f32_16x16x32_bf16 v[216:219], v[40:43], a[212:215], v[216:219]\n"
      "global_load_lds_dwordx4 v254, s[70:71] offset:2048\n"
      "v_add_u32 v133, s48, v133\n"
      "v_ashrrev_i32 v129, 31, v128\n"
      "v_mfma_f32_16x16x32_bf16 v[220:223], v[44:47], a[212:215], v[220:223]\n"
      "ds_read_b128 a[208:211], v250 offset:4096\n"
      "v_mul_f32 v130, 0xbfb8aa3b, v128\n"
      "v_or_b32 v129, 0x80000000, v129\n"
      "s_waitcnt lgkmcnt(14)\n"
      "v_mfma_f32_16x16x32_bf16 v[216:219], v[48:51], a[216:219], v[216:219]\n"
      "v_exp_f32 v130, v130\n"
      "v_xor_b32 v129, v129, v128\n"
      "v_sub_u32 v132, 0xfff, v133\n"
      "v_mfma_f32_16x16x32_bf16 v[220:223], v[52:55], a[216:219], v[220:223]\n"
      "ds_read_b128 a[212:215], v250 offset:6144\n"
      "v_add_f32 v130, 1.0, v130\n"
      "s_mov_b32 s49, 0xffff0000\n"
      "s_waitcnt lgkmcnt(14)\n"
      "v_mfma_f32_16x16x32_bf16 v[216:219], v[56:59], a[220:223], v[216:219]\n"
      "global_load_lds_dwordx4 v255, s[70:71] offset:3072\n"
      "v_and_or_b32 v129, v129, s49, v132\n"
      "v_rcp_f32 v130, v130\n"
      "v_mfma_f32_16x16x32_bf16 v[220:223], v[60:63], a[220:223], v[220:223]\n"
      "s_mov_b32 s64, 0x00ff00ff\n"
      "ds_read_b128 a[216:219], v251 offset:4096\n"
      "s_mov_b32 s65, 0x00ff00ff\n"
      "v_cndmask_b32 v129, v129, 0, s[64:65]\n"
      "s_waitcnt lgkmcnt(14)\n"
      "v_mfma_f32_16x16x32_bf16 v[216:219], v[64:67], a[224:227], v[216:219]\n"
      "v_add_u32 v238, s42, v242\n"
      "v_mov_b32 v224, s37\n"
      "v_mov_b32 v225, s37\n"
      "v_mov_b32 v226, s37\n"
      "v_mov_b32 v227, s37\n"
      "s_waitcnt vmcnt(16)\n"
      "s_mov_b64 exec, 0xff\n"
      "global_store_dwordx4 v238, v[224:227], s[58:59] offset:0 sc1\n"
      "s_mov_b64 exec, -1\n"
      "v_mfma_f32_16x16x32_bf16 v[220:223], v[68:71], a[224:227], v[220:223]\n"
      "ds_read_b128 a[220:223], v251 offset:6144\n"
      "v_cvt_pk_bf16_f32 v130, v130, v130\n"
      "s_lshr_b32 s48, s34, 1\n"
      "s_waitcnt lgkmcnt(14)\n"
      "v_mfma_f32_16x16x32_bf16 v[216:219], v[72:75], a[228:231], v[216:219]\n"
      "s_lshl2_add_u32 s48, s48, s35\n"
      "s_xor_b32 s70, s78, 0\n"
      "s_add_u32 s70, s8, s70\n"
      "s_addc_u32 s71, s9, 0\n"
      "s_add_u32 m0, s41, 12288\n"
      "v_mfma_f32_16x16x32_bf16 v[220:223], v[76:79], a[228:231], v[220:223]\n"
      "global_load_lds_dwordx4 v244, s[70:71] offset:0\n"
      "ds_read_b128 a[224:227], v250 offset:8192\n"
      "v_max_u32_dpp v129, v129, v129 quad_perm:[1,0,3,2] row_mask:0xf bank_mask:0xf\n"
      "s_waitcnt lgkmcnt(14)\n"
      "v_mfma_f32_16x16x32_bf16 v[216:219], v[80:83], a[232:235], v[216:219]\n"
      "v_and_b32 v130, 0xffff0000, v130\n"
      "s_lshl_b32 s49, s48, 15\n"
      "v_max_u32_dpp v129, v129, v129 quad_perm:[2,3,0,1] row_mask:0xf bank_mask:0xf\n"
      "v_mfma_f32_16x16x32_bf16 v[220:223], v[84:87], a[232:235], v[220:223]\n"
      "s_add_u32 s49, s49, 786432\n"
      "ds_read_b128 a[228:231], v250 offset:10240\n"
      "v_lshl_add_u32 v133, v133, 4, s49\n"
      "v_max_u32_dpp v129, v129, v129 row_half_mirror row_mask:0xf bank_mask:0xf\n"
      "s_waitcnt lgkmcnt(14)\n"
      "v_mfma_f32_16x16x32_bf16 v[216:219], v[88:91], a[236:239], v[216:219]\n"
      "s_lshl_b32 s48, s48, 6\n"
      "s_add_u32 s48, s48, s40\n"
      "v_max_u32_dpp v129, v129, v129 row_mirror row_mask:0xf bank_mask:0xf\n"
      "s_lshl_b32 s48, s48, 6\n"
      "v_mfma_f32_16x16x32_bf16 v[220:223], v[92:95], a[236:239], v[220:223]\n"
      "s_add_u32 s48, s48, 4096\n"
      "global_load_lds_dwordx4 v245, s[70:71] offset:1024\n"
      "ds_read_b128 a[232:235], v251 offset:8192\n"
      "v_mov_b32 v131, v129\n"
      "s_waitcnt lgkmcnt(14)\n"
      "v_mfma_f32_16x16x32_bf16 v[216:219], v[96:99], a[240:243], v[216:219]\n"
      "v_lshl_add_u32 v132, v134, 4, s48\n"
      "v_mov_b32 v137, s37\n"
      "v_permlane16_swap_b32 v129, v131\n"
      "v_mfma_f32_16x16x32_bf16 v[220:223], v[100:103], a[240:243], v[220:223]\n"
      "ds_read_b128 a[236:239], v251 offset:10240\n"
      "v_mov_b32 v138, s37\n"
      "v_mov_b32 v139, s37\n"
      "s_waitcnt lgkmcnt(14)\n"
      "v_mfma_f32_16x16x32_bf16 v[216:219], v[104:107], a[244:247], v[216:219]\n"
      "v_max_u32 v129, v129, v131\n"
      "v_cndmask_b32 v133, v132, v133, s[64:65]\n"
      "v_mov_b32 v131, v129\n"
      "v_mfma_f32_16x16x32_bf16 v[220:223], v[108:111], a[244:247], v[220:223]\n"
      "global_load_lds_dwordx4 v246, s[70:71] offset:2048\n"
      "ds_read_b128 a[240:243], v250 offset:12288\n"
      "v_permlane32_swap_b32 v129, v131\n"
      "s_waitcnt lgkmcnt(14)\n"
      "v_mfma_f32_16x16x32_bf16 v[216:219], v[112:115], a[248:251], v[216:219]\n"
      "s_waitcnt vmcnt(16)\n"
      "ds_read_b128 a[0:3], v248 offset:4096\n"
      "v_mfma_f32_16x16x32_bf16 v[220:223], v[116:119], a[248:251], v[220:223]\n"
      "ds_read_b128 a[244:247], v250 offset:14336\n"
      "ds_read_b128 a[4:7], v248 offset:6144\n"
      "v_max_u32 v129, v129, v131\n"
      "s_waitcnt lgkmcnt(15)\n"
      "v_mfma_f32_16x16x32_bf16 v[216:219], v[120:123], a[252:255], v[216:219]\n"
      "ds_read_b128 a[32:35], v249 offset:4096\n"
      "ds_read_b128 a[36:39], v249 offset:6144\n"
      "v_cndmask_b32 v136, v129, v130, s[64:65]\n"
      "v_mfma_f32_16x16x32_bf16 v[220:223], v[124:127], a[252:255], v[220:223]\n"
      "s_mov_b32 s64, 0x00ff0fff\n"
      "s_mov_b64 exec, s[64:65]\n"
      "global_store_dwordx4 v133, v[136:139], s[58:59] sc1\n"
      "s_mov_b64 exec, -1\n"
      "v_mfma_f32_16x16x32_bf16 v[192:195], v[0:3], a[192:195], 0\n"
      "global_load_lds_dwordx4 v247, s[70:71] offset:3072\n"
      "ds_read_b128 a[248:251], v251 offset:12288\n"
      "v_mfma_f32_16x16x32_bf16 v[196:199], v[4:7], a[192:195], 0\n"
      "ds_read_b128 a[252:255], v251 offset:14336\n"
      "s_waitcnt vmcnt(14)\n"
      "ds_read_b128 a[8:11], v248 offset:8192\n"
      "s_waitcnt lgkmcnt(15)\n"
      "v_mfma_f32_16x16x32_bf16 v[200:203], v[0:3], a[196:199], 0\n"
      "ds_read_b128 a[12:15], v248 offset:10240\n"
      "ds_read_b128 a[40:43], v249 offset:8192\n"
      "v_cvt_pk_bf16_f32 v224, v216, v217\n"
      "v_mfma_f32_16x16x32_bf16 v[204:207], v[4:7], a[196:199], 0\n"
      "s_xor_b32 s70, s79, 0\n"
      "s_add_u32 s70, s8, s70\n"
      "s_addc_u32 s71, s9, 0\n"
      "s_add_u32 m0, s41, 0\n"
      "v_mfma_f32_16x16x32_bf16 v[192:195], v[8:11], a[200:203], v[192:195]\n"
      "global_load_lds_dwordx4 v244, s[70:71] offset:0\n"
      "ds_read_b128 a[192:195], v250 offset:16384\n"
      "v_cvt_pk_bf16_f32 v225, v218, v219\n"
      "v_mfma_f32_16x16x32_bf16 v[196:199], v[12:15], a[200:203], v[196:199]\n"
      "ds_read_b128 a[196:199], v250 offset:18432\n"
      "ds_read_b128 a[44:47], v249 offset:10240\n"
      "v_cvt_pk_bf16_f32 v226, v220, v221\n"
      "v_mfma_f32_16x16x32_bf16 v[200:203], v[8:11], a[204:207], v[200:203]\n"
      "v_cvt_pk_bf16_f32 v227, v222, v223\n"
      "v_xor_b32 v224, s85, v224\n"
      "v_xor_b32 v225, s85, v225\n"
      "v_mfma_f32_16x16x32_bf16 v[204:207], v[12:15], a[204:207], v[204:207]\n"
      "ds_read_b128 a[200:203], v251 offset:16384\n"
      "v_xor_b32 v226, s85, v226\n"
      "v_xor_b32 v227, s85, v227\n"
      "v_mfma_f32_16x16x32_bf16 v[192:195], v[16:19], a[208:211], v[192:195]\n"
      "s_lshl2_add_u32 s54, s34, s35\n"
      "global_load_lds_dwordx4 v245, s[70:71] offset:1024\n"
      "s_lshr_b32 s56, s39, 1\n"
      "s_lshl1_add_u32 s56, s33, s56\n"
      "v_mfma_f32_16x16x32_bf16 v[196:199], v[20:23], a[208:211], v[196:199]\n"
      "s_lshl_b32 s56, s56, 15\n"
      "ds_read_b128 a[204:207], v251 offset:18432\n"
      "s_mul_i32 s55, s54, 266240\n"
      "s_add_u32 s55, s55, 4096\n"
      "v_mfma_f32_16x16x32_bf16 v[200:203], v[16:19], a[212:215], v[200:203]\n"
      "s_add_u32 s14, s18, s55\n"
      "s_addc_u32 s15, s19, 0\n"
      "s_add_u32 s10, s14, s56\n"
      "s_addc_u32 s11, s15, 0\n"
      "v_mfma_f32_16x16x32_bf16 v[204:207], v[20:23], a[212:215], v[204:207]\n"
      "s_xor_b32 s56, s56, 0x8000\n"
      "ds_read_b128 a[208:211], v250 offset:20480\n"
      "s_add_u32 s14, s14, s56\n"
      "s_addc_u32 s15, s15, 0\n"
      "s_waitcnt lgkmcnt(15)\n"
      "v_mfma_f32_16x16x32_bf16 v[192:195], v[24:27], a[216:219], v[192:195]\n"
      "s_and_b32 s55, s39, 1\n"
      "global_load_lds_dwordx4 v246, s[70:71] offset:2048\n"
      "s_lshl_b32 s56, s55, 14\n"
      "v_add_u32 v240, s56, v242\n"
      "v_mfma_f32_16x16x32_bf16 v[196:199], v[28:31], a[216:219], v[196:199]\n"
      "ds_read_b128 a[212:215], v250 offset:22528\n"
      "v_xor_b32 v241, 0x4000, v240\n"
      "s_add_u32 s72, s10, 0x2000\n"
      "s_addc_u32 s73, s11, 0\n"
      "v_mfma_f32_16x16x32_bf16 v[200:203], v[24:27], a[220:223], v[200:203]\n"
      "s_add_u32 s74, s14, 0x2000\n"
      "s_addc_u32 s75, s15, 0\n"
      "s_lshl2_add_u32 s55, s54, s33\n"
      "s_lshl_b32 s55, s55, 12\n"
      "v_mfma_f32_16x16x32_bf16 v[204:207], v[28:31], a[220:223], v[204:207]\n"
      "s_add_u32 s55, s55, 1048576\n"
      "ds_read_b128 a[216:219], v251 offset:20480\n"
      "v_and_b32 v253, 7, v243\n"
      "v_lshl_add_u32 v253, v253, 9, s55\n"
      "v_mfma_f32_16x16x32_bf16 v[192:195], v[32:35], a[224:227], v[192:195]\n"
      "global_load_lds_dwordx4 v247, s[70:71] offset:3072\n"
      "v_mfma_f32_16x16x32_bf16 v[196:199], v[36:39], a[224:227], v[196:199]\n"
      "ds_read_b128 a[220:223], v251 offset:22528\n"
      "s_waitcnt vmcnt(10)\n"
      "s_barrier\n"
      "v_mfma_f32_16x16x32_bf16 v[200:203], v[32:35], a[228:231], v[200:203]\n"
      "v_mfma_f32_16x16x32_bf16 v[204:207], v[36:39], a[228:231], v[204:207]\n"
      "s_xor_b32 s70, s76, 128\n"
      "s_add_u32 s70, s8, s70\n"
      "s_addc_u32 s71, s9, 0\n"
      "s_add_u32 m0, s82, 98304\n"
      "v_mfma_f32_16x16x32_bf16 v[192:195], v[40:43], a[232:235], v[192:195]\n"
      "global_load_lds_dwordx4 v244, s[70:71] offset:0\n"
      "ds_read_b128 a[224:227], v250 offset:24576\n"
      "v_mfma_f32_16x16x32_bf16 v[196:199], v[44:47], a[232:235], v[196:199]\n"
      "ds_read_b128 a[228:231], v250 offset:26624\n"
      "v_mfma_f32_16x16x32_bf16 v[200:203], v[40:43], a[236:239], v[200:203]\n"
      "v_mfma_f32_16x16x32_bf16 v[204:207], v[44:47], a[236:239], v[204:207]\n"
      "ds_read_b128 a[232:235], v251 offset:24576\n"
      "v_mfma_f32_16x16x32_bf16 v[192:195], v[48:51], a[240:243], v[192:195]\n"
      "global_load_lds_dwordx4 v245, s[70:71] offset:1024\n"
      "v_mfma_f32_16x16x32_bf16 v[196:199], v[52:55], a[240:243], v[196:199]\n"
      "ds_read_b128 a[236:239], v251 offset:26624\n"
      "s_waitcnt lgkmcnt(15)\n"
      "v_mfma_f32_16x16x32_bf16 v[200:203], v[48:51], a[244:247], v[200:203]\n"
      "v_mfma_f32_16x16x32_bf16 v[204:207], v[52:55], a[244:247], v[204:207]\n"
      "ds_read_b128 a[240:243], v250 offset:28672\n"
      "v_mfma_f32_16x16x32_bf16 v[192:195], v[56:59], a[248:251], v[192:195]\n"
      "global_load_lds_dwordx4 v246, s[70:71] offset:2048\n"
      "v_mfma_f32_16x16x32_bf16 v[196:199], v[60:63], a[248:251], v[196:199]\n"
      "ds_read_b128 a[244:247], v250 offset:30720\n"
      "v_mfma_f32_16x16x32_bf16 v[200:203], v[56:59], a[252:255], v[200:203]\n"
      "v_mfma_f32_16x16x32_bf16 v[204:207], v[60:63], a[252:255], v[204:207]\n"
      "ds_read_b128 a[248:251], v251 offset:28672\n"
      "s_waitcnt lgkmcnt(15)\n"
      "v_mfma_f32_16x16x32_bf16 v[192:195], v[64:67], a[192:195], v[192:195]\n"
      "global_load_lds_dwordx4 v247, s[70:71] offset:3072\n"
      "v_mfma_f32_16x16x32_bf16 v[196:199], v[68:71], a[192:195], v[196:199]\n"
      "ds_read_b128 a[252:255], v251 offset:30720\n"
      "v_xor_b32 v250, 196608, v250\n"
      "v_xor_b32 v251, 196608, v251\n"
      "s_waitcnt lgkmcnt(15)\n"
      "v_mfma_f32_16x16x32_bf16 v[200:203], v[64:67], a[196:199], v[200:203]\n"
      "v_mfma_f32_16x16x32_bf16 v[204:207], v[68:71], a[196:199], v[204:207]\n"
      "s_xor_b32 s70, s77, 128\n"
      "s_add_u32 s70, s8, s70\n"
      "s_addc_u32 s71, s9, 0\n"
      "s_add_u32 m0, s82, 114688\n"
      "s_waitcnt lgkmcnt(13)\n"
      "v_mfma_f32_16x16x32_bf16 v[192:195], v[72:75], a[200:203], v[192:195]\n"
      "global_load_lds_dwordx4 v244, s[70:71] offset:0\n"
      "ds_read_b128 a[192:195], v250 offset:0\n"
      "v_mfma_f32_16x16x32_bf16 v[196:199], v[76:79], a[200:203], v[196:199]\n"
      "ds_read_b128 a[196:199], v250 offset:2048\n"
      "s_waitcnt lgkmcnt(14)\n"
      "v_mfma_f32_16x16x32_bf16 v[200:203], v[72:75], a[204:207], v[200:203]\n"
      "v_mfma_f32_16x16x32_bf16 v[204:207], v[76:79], a[204:207], v[204:207]\n"
      "ds_read_b128 a[200:203], v251 offset:0\n"
      "s_waitcnt lgkmcnt(14)\n"
      "v_mfma_f32_16x16x32_bf16 v[192:195], v[80:83], a[208:211], v[192:195]\n"
      "global_load_lds_dwordx4 v245, s[70:71] offset:1024\n"
      "v_mfma_f32_16x16x32_bf16 v[196:199], v[84:87], a[208:211], v[196:199]\n"
      "ds_read_b128 a[204:207], v251 offset:2048\n"
      "s_waitcnt lgkmcnt(14)\n"
      "v_mfma_f32_16x16x32_bf16 v[200:203], v[80:83], a[212:215], v[200:203]\n"
      "v_mfma_f32_16x16x32_bf16 v[204:207], v[84:87], a[212:215], v[204:207]\n"
      "ds_read_b128 a[208:211], v250 offset:4096\n"
      "s_waitcnt lgkmcnt(14)\n"
      "v_mfma_f32_16x16x32_bf16 v[192:195], v[88:91], a[216:219], v[192:195]\n"
      "global_load_lds_dwordx4 v246, s[70:71] offset:2048\n"
      "v_mfma_f32_16x16x32_bf16 v[196:199], v[92:95], a[216:219], v[196:199]\n"
      "ds_read_b128 a[212:215], v250 offset:6144\n"
      "s_waitcnt vmcnt(11)\n"
      "ds_read_b128 a[16:19], v248 offset:12288\n"
      "s_waitcnt lgkmcnt(15)\n"
      "v_mfma_f32_16x16x32_bf16 v[200:203], v[88:91], a[220:223], v[200:203]\n"
      "ds_read_b128 a[20:23], v248 offset:14336\n"
      "ds_read_b128 a[48:51], v249 offset:12288\n"
      "v_mfma_f32_16x16x32_bf16 v[204:207], v[92:95], a[220:223], v[204:207]\n"
      "ds_read_b128 a[216:219], v251 offset:4096\n"
      "ds_read_b128 a[52:55], v249 offset:14336\n"
      "s_waitcnt lgkmcnt(15)\n"
      "v_mfma_f32_16x16x32_bf16 v[192:195], v[96:99], a[224:227], v[192:195]\n"
      "global_load_lds_dwordx4 v247, s[70:71] offset:3072\n"
      "v_mfma_f32_16x16x32_bf16 v[196:199], v[100:103], a[224:227], v[196:199]\n"
      "ds_read_b128 a[220:223], v251 offset:6144\n"
      "v_mfma_f32_16x16x32_bf16 v[200:203], v[96:99], a[228:231], v[200:203]\n"
      "v_mfma_f32_16x16x32_bf16 v[204:207], v[100:103], a[228:231], v[204:207]\n"
      "s_xor_b32 s70, s78, 128\n"
      "s_add_u32 s70, s8, s70\n"
      "s_addc_u32 s71, s9, 0\n"
      "s_add_u32 m0, s41, 4096\n"
      "v_mfma_f32_16x16x32_bf16 v[192:195], v[104:107], a[232:235], v[192:195]\n"
      "global_load_lds_dwordx4 v244, s[70:71] offset:0\n"
      "ds_read_b128 a[224:227], v250 offset:8192\n"
      "v_mfma_f32_16x16x32_bf16 v[196:199], v[108:111], a[232:235], v[196:199]\n"
      "ds_read_b128 a[228:231], v250 offset:10240\n"
      "v_mfma_f32_16x16x32_bf16 v[200:203], v[104:107], a[236:239], v[200:203]\n"
      "v_mfma_f32_16x16x32_bf16 v[204:207], v[108:111], a[236:239], v[204:207]\n"
      "ds_read_b128 a[232:235], v251 offset:8192\n"
      "s_waitcnt lgkmcnt(15)\n"
      "v_mfma_f32_16x16x32_bf16 v[192:195], v[112:115], a[240:243], v[192:195]\n"
      "global_load_lds_dwordx4 v245, s[70:71] offset:1024\n"
      "v_mfma_f32_16x16x32_bf16 v[196:199], v[116:119], a[240:243], v[196:199]\n"
      "ds_read_b128 a[236:239], v251 offset:10240\n"
      "s_barrier\n"
      "v_mfma_f32_16x16x32_bf16 v[200:203], v[112:115], a[244:247], v[200:203]\n"
      "v_mfma_f32_16x16x32_bf16 v[204:207], v[116:119], a[244:247], v[204:207]\n"
      "ds_read_b128 a[240:243], v250 offset:12288\n"
      "v_mfma_f32_16x16x32_bf16 v[192:195], v[120:123], a[248:251], v[192:195]\n"
      "global_load_lds_dwordx4 v246, s[70:71] offset:2048\n"
      "v_mfma_f32_16x16x32_bf16 v[196:199], v[124:127], a[248:251], v[196:199]\n"
      "ds_read_b128 a[244:247], v250 offset:14336\n"
      "s_waitcnt vmcnt(11)\n"
      "ds_read_b128 a[24:27], v248 offset:0\n"
      "v_mfma_f32_16x16x32_bf16 v[200:203], v[120:123], a[252:255], v[200:203]\n"
      "ds_read_b128 a[28:31], v248 offset:2048\n"
      "ds_read_b128 a[56:59], v249 offset:0\n"
      "v_mfma_f32_16x16x32_bf16 v[204:207], v[124:127], a[252:255], v[204:207]\n"
      "ds_read_b128 a[248:251], v251 offset:12288\n"
      "ds_read_b128 a[60:63], v249 offset:2048\n"
      "s_waitcnt lgkmcnt(15)\n"
      "v_mfma_f32_16x16x32_bf16 v[208:211], v[0:3], a[192:195], 0\n"
      "global_load_lds_dwordx4 v247, s[70:71] offset:3072\n"
      "v_mfma_f32_16x16x32_bf16 v[212:215], v[4:7], a[192:195], 0\n"
      "ds_read_b128 a[252:255], v251 offset:14336\n"
      "v_mfma_f32_16x16x32_bf16 v[216:219], v[0:3], a[196:199], 0\n"
      "v_cvt_pk_bf16_f32 v228, v192, v193\n"
      "v_cvt_pk_bf16_f32 v229, v194, v195\n"
      "v_cvt_pk_bf16_f32 v230, v196, v197\n"
      "v_mfma_f32_16x16x32_bf16 v[220:223], v[4:7], a[196:199], 0\n"
      "global_load_dword v252, v253, s[58:59] offset:0 sc1\n"
      "s_xor_b32 s70, s79, 128\n"
      "s_add_u32 s70, s8, s70\n"
      "s_addc_u32 s71, s9, 0\n"
      "s_add_u32 m0, s82, 131072\n"
      "v_mfma_f32_16x16x32_bf16 v[208:211], v[8:11], a[200:203], v[208:211]\n"
      "global_load_lds_dwordx4 v244, s[70:71] offset:0\n"
      "ds_read_b128 a[192:195], v250 offset:16384\n"
      "v_cvt_pk_bf16_f32 v231, v198, v199\n"
      "v_mfma_f32_16x16x32_bf16 v[212:215], v[12:15], a[200:203], v[212:215]\n"
      "ds_read_b128 a[196:199], v250 offset:18432\n"
      "v_xor_b32 v228, s85, v228\n"
      "v_xor_b32 v229, s85, v229\n"
      "v_mfma_f32_16x16x32_bf16 v[216:219], v[8:11], a[204:207], v[216:219]\n"
      "v_xor_b32 v230, s85, v230\n"
      "v_xor_b32 v231, s85, v231\n"
      "v_cvt_pk_bf16_f32 v232, v200, v201\n"
      "v_mfma_f32_16x16x32_bf16 v[220:223], v[12:15], a[204:207], v[220:223]\n"
      "ds_read_b128 a[200:203], v251 offset:16384\n"
      "v_cvt_pk_bf16_f32 v233, v202, v203\n"
      "v_cvt_pk_bf16_f32 v234, v204, v205\n"
      "v_mfma_f32_16x16x32_bf16 v[208:211], v[16:19], a[208:211], v[208:211]\n"
      "global_load_lds_dwordx4 v245, s[70:71] offset:1024\n"
      "v_cvt_pk_bf16_f32 v235, v206, v207\n"
      "v_xor_b32 v232, s85, v232\n"
      "v_mfma_f32_16x16x32_bf16 v[212:215], v[20:23], a[208:211], v[212:215]\n"
      "ds_read_b128 a[204:207], v251 offset:18432\n"
      "v_xor_b32 v233, s85, v233\n"
      "v_xor_b32 v234, s85, v234\n"
      "v_mfma_f32_16x16x32_bf16 v[216:219], v[16:19], a[212:215], v[216:219]\n"
      "v_xor_b32 v235, s85, v235\n"
      "v_mfma_f32_16x16x32_bf16 v[220:223], v[20:23], a[212:215], v[220:223]\n"
      "ds_read_b128 a[208:211], v250 offset:20480\n"
      "s_waitcnt lgkmcnt(15)\n"
      "v_mfma_f32_16x16x32_bf16 v[208:211], v[24:27], a[216:219], v[208:211]\n"
      "global_load_lds_dwordx4 v246, s[70:71] offset:2048\n"
      "v_mfma_f32_16x16x32_bf16 v[212:215], v[28:31], a[216:219], v[212:215]\n"
      "ds_read_b128 a[212:215], v250 offset:22528\n"
      "s_waitcnt vmcnt(12)\n"
      "ds_read_b128 a[64:67], v236 offset:0\n"
      "v_mfma_f32_16x16x32_bf16 v[216:219], v[24:27], a[220:223], v[216:219]\n"
      "ds_read_b128 a[68:71], v236 offset:2048\n"
      "ds_read_b128 a[96:99], v237 offset:0\n"
      "v_mfma_f32_16x16x32_bf16 v[220:223], v[28:31], a[220:223], v[220:223]\n"
      "ds_read_b128 a[216:219], v251 offset:20480\n"
      "ds_read_b128 a[100:103], v237 offset:2048\n"
      "v_mfma_f32_16x16x32_bf16 v[208:211], v[32:35], a[224:227], v[208:211]\n"
      "global_load_lds_dwordx4 v247, s[70:71] offset:3072\n"
      "v_mfma_f32_16x16x32_bf16 v[212:215], v[36:39], a[224:227], v[212:215]\n"
      "ds_read_b128 a[220:223], v251 offset:22528\n"
      "v_mfma_f32_16x16x32_bf16 v[216:219], v[32:35], a[228:231], v[216:219]\n"
      "v_mfma_f32_16x16x32_bf16 v[220:223], v[36:39], a[228:231], v[220:223]\n"
      "s_xor_b32 s70, s76, 256\n"
      "s_add_u32 s70, s8, s70\n"
      "s_addc_u32 s71, s9, 0\n"
      "s_add_u32 m0, s82, 147456\n"
      "s_waitcnt lgkmcnt(15)\n"
      "v_mfma_f32_16x16x32_bf16 v[208:211], v[40:43], a[232:235], v[208:211]\n"
      "global_load_lds_dwordx4 v244, s[70:71] offset:0\n"
      "ds_read_b128 a[224:227], v250 offset:24576\n"
      "v_mfma_f32_16x16x32_bf16 v[212:215], v[44:47], a[232:235], v[212:215]\n"
      "ds_read_b128 a[228:231], v250 offset:26624\n"
      "v_mfma_f32_16x16x32_bf16 v[216:219], v[40:43], a[236:239], v[216:219]\n"
      "v_mfma_f32_16x16x32_bf16 v[220:223], v[44:47], a[236:239], v[220:223]\n"
      "ds_read_b128 a[232:235], v251 offset:24576\n"
      "v_mfma_f32_16x16x32_bf16 v[208:211], v[48:51], a[240:243], v[208:211]\n"
      "global_load_lds_dwordx4 v245, s[70:71] offset:1024\n"
      "v_mfma_f32_16x16x32_bf16 v[212:215], v[52:55], a[240:243], v[212:215]\n"
      "ds_read_b128 a[236:239], v251 offset:26624\n"
      "v_mfma_f32_16x16x32_bf16 v[216:219], v[48:51], a[244:247], v[216:219]\n"
      "v_mfma_f32_16x16x32_bf16 v[220:223], v[52:55], a[244:247], v[220:223]\n"
      "ds_read_b128 a[240:243], v250 offset:28672\n"
      "s_waitcnt lgkmcnt(15)\n"
      "v_mfma_f32_16x16x32_bf16 v[208:211], v[56:59], a[248:251], v[208:211]\n"
      "global_load_lds_dwordx4 v246, s[70:71] offset:2048\n"
      "v_mfma_f32_16x16x32_bf16 v[212:215], v[60:63], a[248:251], v[212:215]\n"
      "ds_read_b128 a[244:247], v250 offset:30720\n"
      "s_waitcnt vmcnt(12)\n"
      "ds_read_b128 a[72:75], v236 offset:16384\n"
      "v_mfma_f32_16x16x32_bf16 v[216:219], v[56:59], a[252:255], v[216:219]\n"
      "ds_read_b128 a[76:79], v236 offset:18432\n"
      "ds_read_b128 a[104:107], v237 offset:16384\n"
      "v_mfma_f32_16x16x32_bf16 v[220:223], v[60:63], a[252:255], v[220:223]\n"
      "ds_read_b128 a[248:251], v251 offset:28672\n"
      "ds_read_b128 a[108:111], v237 offset:18432\n"
      "v_mfma_f32_16x16x32_bf16 v[208:211], v[64:67], a[192:195], v[208:211]\n"
      "global_load_lds_dwordx4 v247, s[70:71] offset:3072\n"
      "v_mfma_f32_16x16x32_bf16 v[212:215], v[68:71], a[192:195], v[212:215]\n"
      "ds_read_b128 a[252:255], v251 offset:30720\n"
      "v_mfma_f32_16x16x32_bf16 v[216:219], v[64:67], a[196:199], v[216:219]\n"
      "v_mfma_f32_16x16x32_bf16 v[220:223], v[68:71], a[196:199], v[220:223]\n"
      "s_waitcnt vmcnt(8)\n"
      "v_cmp_ne_u32 vcc, s37, v252\n"
      "s_nop 1\n"
      "s_cbranch_vccnz .Lp_s2_%=\n"
      ".Lp_o3_%=:\n"
      "s_waitcnt lgkmcnt(15)\n"
      "v_mfma_f32_16x16x32_bf16 v[208:211], v[72:75], a[200:203], v[208:211]\n"
      "global_load_dwordx4 v[128:131], v240, s[10:11] offset:-4096 sc1\n"
      "v_mfma_f32_16x16x32_bf16 v[212:215], v[76:79], a[200:203], v[212:215]\n"
      "v_mfma_f32_16x16x32_bf16 v[216:219], v[72:75], a[204:207], v[216:219]\n"
      "v_mfma_f32_16x16x32_bf16 v[220:223], v[76:79], a[204:207], v[220:223]\n"
      "v_mfma_f32_16x16x32_bf16 v[208:211], v[80:83], a[208:211], v[208:211]\n"
      "global_load_dwordx4 v[132:135], v240, s[10:11] offset:-3072 sc1\n"
      "v_mfma_f32_16x16x32_bf16 v[212:215], v[84:87], a[208:211], v[212:215]\n"
      "v_mfma_f32_16x16x32_bf16 v[216:219], v[80:83], a[212:215], v[216:219]\n"
      "v_mfma_f32_16x16x32_bf16 v[220:223], v[84:87], a[212:215], v[220:223]\n"
      "s_waitcnt lgkmcnt(14)\n"
      "v_mfma_f32_16x16x32_bf16 v[208:211], v[88:91], a[216:219], v[208:211]\n"
      "global_load_dwordx4 v[136:139], v240, s[10:11] offset:-2048 sc1\n"
      "v_mfma_f32_16x16x32_bf16 v[212:215], v[92:95], a[216:219], v[212:215]\n"
      "ds_read_b128 a[80:83], v248 offset:4096\n"
      "ds_read_b128 a[84:87], v248 offset:6144\n"
      "s_waitcnt lgkmcnt(14)\n"
      "v_mfma_f32_16x16x32_bf16 v[216:219], v[88:91], a[220:223], v[216:219]\n"
      "ds_read_b128 a[112:115], v249 offset:4096\n"
      "ds_read_b128 a[116:119], v249 offset:6144\n"
      "v_mfma_f32_16x16x32_bf16 v[220:223], v[92:95], a[220:223], v[220:223]\n"
      "s_waitcnt lgkmcnt(15)\n"
      "v_mfma_f32_16x16x32_bf16 v[208:211], v[96:99], a[224:227], v[208:211]\n"
      "global_load_dwordx4 v[140:143], v240, s[72:73] offset:-4096 sc1\n"
      "v_mfma_f32_16x16x32_bf16 v[212:215], v[100:103], a[224:227], v[212:215]\n"
      "s_waitcnt lgkmcnt(14)\n"
      "v_mfma_f32_16x16x32_bf16 v[216:219], v[96:99], a[228:231], v[216:219]\n"
      "v_mfma_f32_16x16x32_bf16 v[220:223], v[100:103], a[228:231], v[220:223]\n"
      "s_waitcnt lgkmcnt(13)\n"
      "v_mfma_f32_16x16x32_bf16 v[208:211], v[104:107], a[232:235], v[208:211]\n"
      "global_load_dwordx4 v[144:147], v240, s[72:73] offset:-3072 sc1\n"
      "v_mfma_f32_16x16x32_bf16 v[212:215], v[108:111], a[232:235], v[212:215]\n"
      "s_waitcnt lgkmcnt(12)\n"
      "v_mfma_f32_16x16x32_bf16 v[216:219], v[104:107], a[236:239], v[216:219]\n"
      "v_mfma_f32_16x16x32_bf16 v[220:223], v[108:111], a[236:239], v[220:223]\n"
      "s_waitcnt lgkmcnt(11)\n"
      "v_mfma_f32_16x16x32_bf16 v[208:211], v[112:115], a[240:243], v[208:211]\n"
      "global_store_dwordx4 v242, v[224:227], s[62:63] offset:-1024 sc1\n"
      "global_store_dwordx4 v242, v[228:231], s[62:63] offset:0 sc1\n"
      "v_mfma_f32_16x16x32_bf16 v[212:215], v[116:119], a[240:243], v[212:215]\n"
      "global_store_dwordx4 v242, v[232:235], s[62:63] offset:1024 sc1\n"
      "global_load_dwordx4 v[148:151], v240, s[72:73] offset:-2048 sc1\n"
      "s_waitcnt lgkmcnt(10)\n"
      "v_mfma_f32_16x16x32_bf16 v[216:219], v[112:115], a[244:247], v[216:219]\n"
      "v_mfma_f32_16x16x32_bf16 v[220:223], v[116:119], a[244:247], v[220:223]\n"
      "s_waitcnt lgkmcnt(6)\n"
      "v_mfma_f32_16x16x32_bf16 v[208:211], v[120:123], a[248:251], v[208:211]\n"
      "v_mfma_f32_16x16x32_bf16 v[212:215], v[124:127], a[248:251], v[212:215]\n"
      "s_waitcnt vmcnt(13)\n"
      "ds_read_b128 a[88:91], v236 offset:32768\n"
      "s_waitcnt lgkmcnt(5)\n"
      "v_mfma_f32_16x16x32_bf16 v[216:219], v[120:123], a[252:255], v[216:219]\n"
      "ds_read_b128 a[92:95], v236 offset:34816\n"
      "ds_read_b128 a[120:123], v237 offset:32768\n"
      "v_mfma_f32_16x16x32_bf16 v[220:223], v[124:127], a[252:255], v[220:223]\n"
      "ds_read_b128 a[124:127], v237 offset:34816\n"
      "s_waitcnt vmcnt(8)\n"
      "v_mfma_f32_16x16x32_bf16 v[0:3], v[128:131], a[0:3], 0\n"
      "v_mfma_f32_16x16x32_bf16 v[4:7], v[128:131], a[4:7], 0\n"
      "v_mfma_f32_16x16x32_bf16 v[8:11], v[128:131], a[8:11], 0\n"
      "v_cvt_pk_bf16_f32 v192, v208, v209\n"
      "v_cvt_pk_bf16_f32 v193, v210, v211\n"
      "v_mfma_f32_16x16x32_bf16 v[12:15], v[128:131], a[12:15], 0\n"
      "v_cvt_pk_bf16_f32 v194, v212, v213\n"
      "v_cvt_pk_bf16_f32 v195, v214, v215\n"
      "v_mfma_f32_16x16x32_bf16 v[16:19], v[128:131], a[16:19], 0\n"
      "v_xor_b32 v192, s85, v192\n"
      "v_xor_b32 v193, s85, v193\n"
      "v_mfma_f32_16x16x32_bf16 v[20:23], v[128:131], a[20:23], 0\n"
      "v_xor_b32 v194, s85, v194\n"
      "v_xor_b32 v195, s85, v195\n"
      "v_mfma_f32_16x16x32_bf16 v[24:27], v[128:131], a[24:27], 0\n"
      "v_cvt_pk_bf16_f32 v196, v216, v217\n"
      "v_cvt_pk_bf16_f32 v197, v218, v219\n"
      "v_mfma_f32_16x16x32_bf16 v[28:31], v[128:131], a[28:31], 0\n"
      "v_cvt_pk_bf16_f32 v198, v220, v221\n"
      "v_cvt_pk_bf16_f32 v199, v222, v223\n"
      "s_waitcnt vmcnt(7)\n"
      "v_mfma_f32_16x16x32_bf16 v[32:35], v[132:135], a[0:3], 0\n"
      "v_xor_b32 v196, s85, v196\n"
      "v_xor_b32 v197, s85, v197\n"
      "v_mfma_f32_16x16x32_bf16 v[36:39], v[132:135], a[4:7], 0\n"
      "v_xor_b32 v198, s85, v198\n"
      "v_xor_b32 v199, s85, v199\n"
      "v_mfma_f32_16x16x32_bf16 v[40:43], v[132:135], a[8:11], 0\n"
      "global_store_dwordx4 v242, v[192:195], s[62:63] offset:2048 sc1\n"
      "global_store_dwordx4 v242, v[196:199], s[62:63] offset:3072 sc1\n"
      "v_mfma_f32_16x16x32_bf16 v[44:47], v[132:135], a[12:15], 0\n"
      "v_mfma_f32_16x16x32_bf16 v[48:51], v[132:135], a[16:19], 0\n"
      "v_mfma_f32_16x16x32_bf16 v[52:55], v[132:135], a[20:23], 0\n"
      "s_xor_b32 s70, s77, 256\n"
      "s_add_u32 s70, s8, s70\n"
      "s_addc_u32 s71, s9, 0\n"
      "s_add_u32 m0, s41, 8192\n"
      "v_mfma_f32_16x16x32_bf16 v[56:59], v[132:135], a[24:27], 0\n"
      "global_load_lds_dwordx4 v244, s[70:71] offset:0\n"
      "ds_read_b128 a[128:131], v236 offset:49152\n"
      "v_mfma_f32_16x16x32_bf16 v[60:63], v[132:135], a[28:31], 0\n"
      "s_waitcnt vmcnt(8)\n"
      "ds_read_b128 a[132:135], v236 offset:51200\n"
      "ds_read_b128 a[160:163], v237 offset:49152\n"
      "v_mfma_f32_16x16x32_bf16 v[64:67], v[136:139], a[0:3], 0\n"
      "ds_read_b128 a[164:167], v237 offset:51200\n"
      "v_mfma_f32_16x16x32_bf16 v[68:71], v[136:139], a[4:7], 0\n"
      "global_load_lds_dwordx4 v245, s[70:71] offset:1024\n"
      "v_mfma_f32_16x16x32_bf16 v[72:75], v[136:139], a[8:11], 0\n"
      "v_mfma_f32_16x16x32_bf16 v[76:79], v[136:139], a[12:15], 0\n"
      "v_mfma_f32_16x16x32_bf16 v[80:83], v[136:139], a[16:19], 0\n"
      "global_load_lds_dwordx4 v246, s[70:71] offset:2048\n"
      "v_mfma_f32_16x16x32_bf16 v[84:87], v[136:139], a[20:23], 0\n"
      "v_mfma_f32_16x16x32_bf16 v[88:91], v[136:139], a[24:27], 0\n"
      "v_mfma_f32_16x16x32_bf16 v[92:95], v[136:139], a[28:31], 0\n"
      "global_load_lds_dwordx4 v247, s[70:71] offset:3072\n"
      "v_mfma_f32_16x16x32_bf16 v[0:3], v[140:143], a[32:35], v[0:3]\n"
      "v_mfma_f32_16x16x32_bf16 v[4:7], v[140:143], a[36:39], v[4:7]\n"
      "global_load_dwordx4 v[152:155], v241, s[10:11] offset:-4096 sc1\n"
      "v_mfma_f32_16x16x32_bf16 v[8:11], v[140:143], a[40:43], v[8:11]\n"
      "v_add_u32 v238, s42, v242\n"
      "v_mov_b32 v200, s37\n"
      "v_mov_b32 v201, s37\n"
      "v_mov_b32 v202, s37\n"
      "v_mov_b32 v203, s37\n"
      "s_waitcnt vmcnt(5)\n"
      "s_mov_b64 exec, 0xff\n"
      "global_store_dwordx4 v238, v[200:203], s[58:59] offset:128 sc1\n"
      "s_mov_b64 exec, -1\n"
      "v_mfma_f32_16x16x32_bf16 v[12:15], v[140:143], a[44:47], v[12:15]\n"
      "s_lshl2_add_u32 s48, s36, s35\n"
      "s_mul_i32 s48, s48, 147712\n"
      "s_mul_i32 s49, s33, 3072\n"
      "v_mfma_f32_16x16x32_bf16 v[16:19], v[140:143], a[48:51], v[16:19]\n"
      "s_add_u32 s48, s48, s49\n"
      "global_load_dwordx4 v[156:159], v241, s[10:11] offset:-3072 sc1\n"
      "s_add_u32 s48, s48, 6291456\n"
      "v_mfma_f32_16x16x32_bf16 v[20:23], v[140:143], a[52:55], v[20:23]\n"
      "s_add_u32 s60, s18, s48\n"
      "s_addc_u32 s61, s19, 0\n"
      "s_mul_i32 s49, s34, 36864\n"
      "v_mfma_f32_16x16x32_bf16 v[24:27], v[140:143], a[56:59], v[24:27]\n"
      "s_add_u32 s62, s60, s49\n"
      "s_addc_u32 s63, s61, 0\n"
      "v_mfma_f32_16x16x32_bf16 v[28:31], v[140:143], a[60:63], v[28:31]\n"
      "global_load_dwordx4 v[160:163], v241, s[10:11] offset:-2048 sc1\n"
      "v_mfma_f32_16x16x32_bf16 v[32:35], v[144:147], a[32:35], v[32:35]\n"
      "v_mfma_f32_16x16x32_bf16 v[36:39], v[144:147], a[36:39], v[36:39]\n"
      "v_mfma_f32_16x16x32_bf16 v[40:43], v[144:147], a[40:43], v[40:43]\n"
      "s_xor_b32 s70, s78, 256\n"
      "s_add_u32 s70, s8, s70\n"
      "s_addc_u32 s71, s9, 0\n"
      "s_add_u32 m0, s41, 12288\n"
      "v_mfma_f32_16x16x32_bf16 v[44:47], v[144:147], a[44:47], v[44:47]\n"
      "global_load_lds_dwordx4 v244, s[70:71] offset:0\n"
      "v_mfma_f32_16x16x32_bf16 v[48:51], v[144:147], a[48:51], v[48:51]\n"
      "v_mfma_f32_16x16x32_bf16 v[52:55], v[144:147], a[52:55], v[52:55]\n"
      "v_mfma_f32_16x16x32_bf16 v[56:59], v[144:147], a[56:59], v[56:59]\n"
      "global_load_lds_dwordx4 v245, s[70:71] offset:1024\n"
      "v_mfma_f32_16x16x32_bf16 v[60:63], v[144:147], a[60:63], v[60:63]\n"
      "v_mfma_f32_16x16x32_bf16 v[64:67], v[148:151], a[32:35], v[64:67]\n"
      "v_mfma_f32_16x16x32_bf16 v[68:71], v[148:151], a[36:39], v[68:71]\n"
      "global_load_lds_dwordx4 v246, s[70:71] offset:2048\n"
      "v_mfma_f32_16x16x32_bf16 v[72:75], v[148:151], a[40:43], v[72:75]\n"
      "v_mfma_f32_16x16x32_bf16 v[76:79], v[148:151], a[44:47], v[76:79]\n"
      "v_mfma_f32_16x16x32_bf16 v[80:83], v[148:151], a[48:51], v[80:83]\n"
      "global_load_lds_dwordx4 v247, s[70:71] offset:3072\n"
      "v_mfma_f32_16x16x32_bf16 v[84:87], v[148:151], a[52:55], v[84:87]\n"
      "v_mfma_f32_16x16x32_bf16 v[88:91], v[148:151], a[56:59], v[88:91]\n"
      "s_xor_b32 s70, s79, 256\n"
      "s_add_u32 s70, s8, s70\n"
      "s_addc_u32 s71, s9, 0\n"
      "s_add_u32 m0, s41, 0\n"
      "v_mfma_f32_16x16x32_bf16 v[92:95], v[148:151], a[60:63], v[92:95]\n"
      "s_waitcnt vmcnt(5)\n"
      "global_load_lds_dwordx4 v244, s[70:71] offset:0\n"
      "v_mfma_f32_16x16x32_bf16 v[0:3], v[152:155], a[64:67], v[0:3]\n"
      "v_mfma_f32_16x16x32_bf16 v[4:7], v[152:155], a[68:71], v[4:7]\n"
      "v_mfma_f32_16x16x32_bf16 v[8:11], v[152:155], a[72:75], v[8:11]\n"
      "global_load_lds_dwordx4 v245, s[70:71] offset:1024\n"
      "v_mfma_f32_16x16x32_bf16 v[12:15], v[152:155], a[76:79], v[12:15]\n"
      "s_waitcnt lgkmcnt(11)\n"
      "v_mfma_f32_16x16x32_bf16 v[16:19], v[152:155], a[80:83], v[16:19]\n"
      "s_waitcnt lgkmcnt(10)\n"
      "v_mfma_f32_16x16x32_bf16 v[20:23], v[152:155], a[84:87], v[20:23]\n"
      "global_load_lds_dwordx4 v246, s[70:71] offset:2048\n"
      "s_waitcnt lgkmcnt(7)\n"
      "v_mfma_f32_16x16x32_bf16 v[24:27], v[152:155], a[88:91], v[24:27]\n"
      "s_waitcnt lgkmcnt(6)\n"
      "v_mfma_f32_16x16x32_bf16 v[28:31], v[152:155], a[92:95], v[28:31]\n"
      "v_mfma_f32_16x16x32_bf16 v[32:35], v[156:159], a[64:67], v[32:35]\n"
      "global_load_lds_dwordx4 v247, s[70:71] offset:3072\n"
      "v_mfma_f32_16x16x32_bf16 v[36:39], v[156:159], a[68:71], v[36:39]\n"
      "v_mfma_f32_16x16x32_bf16 v[40:43], v[156:159], a[72:75], v[40:43]\n"
      "global_load_dwordx4 v[164:167], v241, s[72:73] offset:-4096 sc1\n"
      "v_mfma_f32_16x16x32_bf16 v[44:47], v[156:159], a[76:79], v[44:47]\n"
      "v_mfma_f32_16x16x32_bf16 v[48:51], v[156:159], a[80:83], v[48:51]\n"
      "v_mfma_f32_16x16x32_bf16 v[52:55], v[156:159], a[84:87], v[52:55]\n"
      "global_load_dwordx4 v[168:171], v241, s[72:73] offset:-3072 sc1\n"
      "v_mfma_f32_16x16x32_bf16 v[56:59], v[156:159], a[88:91], v[56:59]\n"
      "v_mfma_f32_16x16x32_bf16 v[60:63], v[156:159], a[92:95], v[60:63]\n"
      "s_waitcnt vmcnt(1)\n"
      "v_mfma_f32_16x16x32_bf16 v[64:67], v[160:163], a[64:67], v[64:67]\n"
      "global_load_dwordx4 v[172:175], v241, s[72:73] offset:-2048 sc1\n"
      "v_mfma_f32_16x16x32_bf16 v[68:71], v[160:163], a[68:71], v[68:71]\n"
      "v_mfma_f32_16x16x32_bf16 v[72:75], v[160:163], a[72:75], v[72:75]\n"
      "v_mfma_f32_16x16x32_bf16 v[76:79], v[160:163], a[76:79], v[76:79]\n"
      "global_load_dwordx4 v[176:179], v240, s[14:15] offset:-4096 sc1\n"
      "v_mfma_f32_16x16x32_bf16 v[80:83], v[160:163], a[80:83], v[80:83]\n"
      "ds_read_b128 a[136:139], v248 offset:8192\n"
      "ds_read_b128 a[140:143], v248 offset:10240\n"
      "v_mfma_f32_16x16x32_bf16 v[84:87], v[160:163], a[84:87], v[84:87]\n"
      "ds_read_b128 a[168:171], v249 offset:8192\n"
      "ds_read_b128 a[172:175], v249 offset:10240\n"
      "v_mfma_f32_16x16x32_bf16 v[88:91], v[160:163], a[88:91], v[88:91]\n"
      "global_load_dwordx4 v[180:183], v240, s[14:15] offset:-3072 sc1\n"
      "v_mfma_f32_16x16x32_bf16 v[92:95], v[160:163], a[92:95], v[92:95]\n"
      "v_mfma_f32_16x16x32_bf16 v[0:3], v[164:167], a[96:99], v[0:3]\n"
      "v_mfma_f32_16x16x32_bf16 v[4:7], v[164:167], a[100:103], v[4:7]\n"
      "global_load_dwordx4 v[184:187], v240, s[14:15] offset:-2048 sc1\n"
      "global_load_dwordx4 v[188:191], v240, s[74:75] offset:-4096 sc1\n"
      "v_mfma_f32_16x16x32_bf16 v[8:11], v[164:167], a[104:107], v[8:11]\n"
      "global_load_dwordx4 v[128:131], v240, s[74:75] offset:-3072 sc1\n"
      "global_load_dword v252, v253, s[58:59] offset:128 sc1\n"
      "v_mfma_f32_16x16x32_bf16 v[12:15], v[164:167], a[108:111], v[12:15]\n"
      "global_load_dwordx4 v[132:135], v240, s[74:75] offset:-2048 sc1\n"
      "v_mfma_f32_16x16x32_bf16 v[16:19], v[164:167], a[112:115], v[16:19]\n"
      "v_mfma_f32_16x16x32_bf16 v[20:23], v[164:167], a[116:119], v[20:23]\n"
      "s_waitcnt lgkmcnt(9)\n"
      "v_mfma_f32_16x16x32_bf16 v[24:27], v[164:167], a[120:123], v[24:27]\n"
      "s_waitcnt lgkmcnt(8)\n"
      "v_mfma_f32_16x16x32_bf16 v[28:31], v[164:167], a[124:127], v[28:31]\n"
      "s_waitcnt vmcnt(7)\n"
      "v_mfma_f32_16x16x32_bf16 v[32:35], v[168:171], a[96:99], v[32:35]\n"
      "v_mfma_f32_16x16x32_bf16 v[36:39], v[168:171], a[100:103], v[36:39]\n"
      "v_mfma_f32_16x16x32_bf16 v[40:43], v[168:171], a[104:107], v[40:43]\n"
      "v_mfma_f32_16x16x32_bf16 v[44:47], v[168:171], a[108:111], v[44:47]\n"
      "v_mfma_f32_16x16x32_bf16 v[48:51], v[168:171], a[112:115], v[48:51]\n"
      "v_mfma_f32_16x16x32_bf16 v[52:55], v[168:171], a[116:119], v[52:55]\n"
      "s_lshr_b32 s54, s34, 1\n"
      "s_lshl2_add_u32 s54, s54, s35\n"
      "s_lshl_b32 s54, s54, 12\n"
      "v_mfma_f32_16x16x32_bf16 v[56:59], v[168:171], a[120:123], v[56:59]\n"
      "s_add_u32 s54, s54, 4096\n"
      "v_lshl_add_u32 v232, v243, 6, s54\n"
      "global_load_dwordx2 v[234:235], v232, s[58:59] sc1\n"
      "v_mfma_f32_16x16x32_bf16 v[60:63], v[168:171], a[124:127], v[60:63]\n"
      "v_mfma_f32_16x16x32_bf16 v[64:67], v[172:175], a[96:99], v[64:67]\n"
      "v_mfma_f32_16x16x32_bf16 v[68:71], v[172:175], a[100:103], v[68:71]\n"
      "ds_read_b128 a[144:147], v248 offset:12288\n"
      "ds_read_b128 a[148:151], v248 offset:14336\n"
      "v_mfma_f32_16x16x32_bf16 v[72:75], v[172:175], a[104:107], v[72:75]\n"
      "ds_read_b128 a[176:179], v249 offset:12288\n"
      "ds_read_b128 a[180:183], v249 offset:14336\n"
      "v_mfma_f32_16x16x32_bf16 v[76:79], v[172:175], a[108:111], v[76:79]\n"
      "v_mfma_f32_16x16x32_bf16 v[80:83], v[172:175], a[112:115], v[80:83]\n"
      "v_mfma_f32_16x16x32_bf16 v[84:87], v[172:175], a[116:119], v[84:87]\n"
      "s_xor_b32 s70, s76, 384\n"
      "s_add_u32 s70, s8, s70\n"
      "s_addc_u32 s71, s9, 0\n"
      "s_add_u32 m0, s82, 98304\n"
      "v_mfma_f32_16x16x32_bf16 v[88:91], v[172:175], a[120:123], v[88:91]\n"
      "global_load_lds_dwordx4 v244, s[70:71] offset:0\n"
      "v_mfma_f32_16x16x32_bf16 v[92:95], v[172:175], a[124:127], v[92:95]\n"
      "s_waitcnt vmcnt(7)\n"
      "s_waitcnt lgkmcnt(11)\n"
      "v_mfma_f32_16x16x32_bf16 v[0:3], v[176:179], a[128:131], v[0:3]\n"
      "s_waitcnt lgkmcnt(10)\n"
      "v_mfma_f32_16x16x32_bf16 v[4:7], v[176:179], a[132:135], v[4:7]\n"
      "global_load_lds_dwordx4 v245, s[70:71] offset:1024\n"
      "s_waitcnt lgkmcnt(7)\n"
      "v_mfma_f32_16x16x32_bf16 v[8:11], v[176:179], a[136:139], v[8:11]\n"
      "s_waitcnt lgkmcnt(6)\n"
      "v_mfma_f32_16x16x32_bf16 v[12:15], v[176:179], a[140:143], v[12:15]\n"
      "s_waitcnt lgkmcnt(3)\n"
      "v_mfma_f32_16x16x32_bf16 v[16:19], v[176:179], a[144:147], v[16:19]\n"
      "global_load_lds_dwordx4 v246, s[70:71] offset:2048\n"
      "s_waitcnt lgkmcnt(2)\n"
      "v_mfma_f32_16x16x32_bf16 v[20:23], v[176:179], a[148:151], v[20:23]\n"
      "ds_read_b128 a[152:155], v248 offset:0\n"
      "ds_read_b128 a[156:159], v248 offset:2048\n"
      "ds_read_b128 a[184:187], v249 offset:0\n"
      "ds_read_b128 a[188:191], v249 offset:2048\n"
      "s_waitcnt lgkmcnt(3)\n"
      "v_mfma_f32_16x16x32_bf16 v[24:27], v[176:179], a[152:155], v[24:27]\n"
      "s_waitcnt lgkmcnt(2)\n"
      "v_mfma_f32_16x16x32_bf16 v[28:31], v[176:179], a[156:159], v[28:31]\n"
      "global_load_lds_dwordx4 v247, s[70:71] offset:3072\n"
      "v_mfma_f32_16x16x32_bf16 v[32:35], v[180:183], a[128:131], v[32:35]\n"
      "v_mfma_f32_16x16x32_bf16 v[36:39], v[180:183], a[132:135], v[36:39]\n"
      "s_xor_b32 s70, s77, 384\n"
      "s_add_u32 s70, s8, s70\n"
      "s_addc_u32 s71, s9, 0\n"
      "s_add_u32 m0, s82, 114688\n"
      "v_mfma_f32_16x16x32_bf16 v[40:43], v[180:183], a[136:139], v[40:43]\n"
      "global_load_lds_dwordx4 v244, s[70:71] offset:0\n"
      "v_mfma_f32_16x16x32_bf16 v[44:47], v[180:183], a[140:143], v[44:47]\n"
      "v_mfma_f32_16x16x32_bf16 v[48:51], v[180:183], a[144:147], v[48:51]\n"
      "v_mfma_f32_16x16x32_bf16 v[52:55], v[180:183], a[148:151], v[52:55]\n"
      "global_load_lds_dwordx4 v245, s[70:71] offset:1024\n"
      "v_mfma_f32_16x16x32_bf16 v[56:59], v[180:183], a[152:155], v[56:59]\n"
      "s_waitcnt vmcnt(8)\n"
      "v_cmp_ne_u32 vcc, s37, v252\n"
      "s_nop 1\n"
      "s_cbranch_vccnz .Lp_s5_%=\n"
      ".Lp_o6_%=:\n"
      "v_mfma_f32_16x16x32_bf16 v[60:63], v[180:183], a[156:159], v[60:63]\n"
      "v_mfma_f32_16x16x32_bf16 v[64:67], v[184:187], a[128:131], v[64:67]\n"
      "global_load_lds_dwordx4 v246, s[70:71] offset:2048\n"
      "global_load_dwordx4 v[136:139], v240, s[10:11] offset:-1024 sc1\n"
      "v_mfma_f32_16x16x32_bf16 v[68:71], v[184:187], a[132:135], v[68:71]\n"
      "v_mfma_f32_16x16x32_bf16 v[72:75], v[184:187], a[136:139], v[72:75]\n"
      "v_mfma_f32_16x16x32_bf16 v[76:79], v[184:187], a[140:143], v[76:79]\n"
      "global_load_lds_dwordx4 v247, s[70:71] offset:3072\n"
      "v_mfma_f32_16x16x32_bf16 v[80:83], v[184:187], a[144:147], v[80:83]\n"
      "global_load_dwordx4 v[140:143], v240, s[72:73] offset:-1024 sc1\n"
      "v_mfma_f32_16x16x32_bf16 v[84:87], v[184:187], a[148:151], v[84:87]\n"
      "s_xor_b32 s70, s78, 384\n"
      "s_add_u32 s70, s8, s70\n"
      "s_addc_u32 s71, s9, 0\n"
      "s_add_u32 m0, s41, 4096\n"
      "v_mfma_f32_16x16x32_bf16 v[88:91], v[184:187], a[152:155], v[88:91]\n"
      "global_load_lds_dwordx4 v244, s[70:71] offset:0\n"
      "v_mfma_f32_16x16x32_bf16 v[92:95], v[184:187], a[156:159], v[92:95]\n"
      "v_mfma_f32_16x16x32_bf16 v[0:3], v[188:191], a[160:163], v[0:3]\n"
      "global_load_dwordx4 v[144:147], v241, s[10:11] offset:-1024 sc1\n"
      "v_mfma_f32_16x16x32_bf16 v[4:7], v[188:191], a[164:167], v[4:7]\n"
      "global_load_lds_dwordx4 v245, s[70:71] offset:1024\n"
      "v_mfma_f32_16x16x32_bf16 v[8:11], v[188:191], a[168:171], v[8:11]\n"
      "v_mfma_f32_16x16x32_bf16 v[12:15], v[188:191], a[172:175], v[12:15]\n"
      "v_mfma_f32_16x16x32_bf16 v[16:19], v[188:191], a[176:179], v[16:19]\n"
      "global_load_lds_dwordx4 v246, s[70:71] offset:2048\n"
      "global_load_dwordx4 v[148:151], v241, s[14:15] offset:-4096 sc1\n"
      "v_mfma_f32_16x16x32_bf16 v[20:23], v[188:191], a[180:183], v[20:23]\n"
      "s_waitcnt lgkmcnt(1)\n"
      "v_mfma_f32_16x16x32_bf16 v[24:27], v[188:191], a[184:187], v[24:27]\n"
      "s_waitcnt lgkmcnt(0)\n"
      "v_mfma_f32_16x16x32_bf16 v[28:31], v[188:191], a[188:191], v[28:31]\n"
      "s_waitcnt vmcnt(16)\n"
      "global_load_lds_dwordx4 v247, s[70:71] offset:3072\n"
      "v_mfma_f32_16x16x32_bf16 v[32:35], v[128:131], a[160:163], v[32:35]\n"
      "global_load_dwordx4 v[152:155], v241, s[74:75] offset:-4096 sc1\n"
      "v_mfma_f32_16x16x32_bf16 v[36:39], v[128:131], a[164:167], v[36:39]\n"
      "s_xor_b32 s70, s79, 384\n"
      "s_add_u32 s70, s8, s70\n"
      "s_addc_u32 s71, s9, 0\n"
      "s_add_u32 m0, s82, 131072\n"
      "v_mfma_f32_16x16x32_bf16 v[40:43], v[128:131], a[168:171], v[40:43]\n"
      "global_load_lds_dwordx4 v244, s[70:71] offset:0\n"
      "v_mfma_f32_16x16x32_bf16 v[44:47], v[128:131], a[172:175], v[44:47]\n"
      "v_mfma_f32_16x16x32_bf16 v[48:51], v[128:131], a[176:179], v[48:51]\n"
      "global_load_dwordx4 v[156:159], v241, s[14:15] offset:-3072 sc1\n"
      "v_mfma_f32_16x16x32_bf16 v[52:55], v[128:131], a[180:183], v[52:55]\n"
      "global_load_lds_dwordx4 v245, s[70:71] offset:1024\n"
      "v_mfma_f32_16x16x32_bf16 v[56:59], v[128:131], a[184:187], v[56:59]\n"
      "v_mfma_f32_16x16x32_bf16 v[60:63], v[128:131], a[188:191], v[60:63]\n"
      "v_mfma_f32_16x16x32_bf16 v[64:67], v[132:135], a[160:163], v[64:67]\n"
      "global_load_lds_dwordx4 v246, s[70:71] offset:2048\n"
      "global_load_dwordx4 v[160:163], v241, s[74:75] offset:-3072 sc1\n"
      "v_mfma_f32_16x16x32_bf16 v[68:71], v[132:135], a[164:167], v[68:71]\n"
      "v_mfma_f32_16x16x32_bf16 v[72:75], v[132:135], a[168:171], v[72:75]\n"
      "v_mfma_f32_16x16x32_bf16 v[76:79], v[132:135], a[172:175], v[76:79]\n"
      "global_load_lds_dwordx4 v247, s[70:71] offset:3072\n"
      "v_mfma_f32_16x16x32_bf16 v[80:83], v[132:135], a[176:179], v[80:83]\n"
      "global_load_dwordx4 v[164:167], v241, s[14:15] offset:-2048 sc1\n"
      "v_mfma_f32_16x16x32_bf16 v[84:87], v[132:135], a[180:183], v[84:87]\n"
      "s_barrier\n"
      "s_lshr_b32 s80, s32, 4\n"
      "s_bfe_u32 s43, s32, 0x10002\n"
      "s_lshl1_add_u32 s80, s80, s43\n"
      "s_and_b32 s81, s32, 3\n"
      "s_cmp_lt_u32 s80, 28\n"
      "s_cselect_b32 s86, -1, 0\n"
      "s_lshr_b32 s83, s33, 1\n"
      "s_lshl1_add_u32 s83, s81, s83\n"
      "s_min_u32 s43, s80, 27\n"
      "v_lshrrev_b32 v226, 3, v243\n"
      "v_and_b32 v227, 0x70, v242\n"
      "v_lshl_or_b32 v224, v226, 10, v227\n"
      "v_add_u32 v225, 7168, v224\n"
      "v_add_u32 v226, 14336, v224\n"
      "v_add_u32 v227, 21504, v224\n"
      "s_lshr_b32 s44, s33, 1\n"
      "s_lshl1_add_u32 s44, s43, s44\n"
      "s_lshl_b32 s44, s44, 15\n"
      "s_and_b32 s45, s33, 1\n"
      "s_lshl1_add_u32 s45, s81, s45\n"
      "s_lshl_b32 s45, s45, 7\n"
      "s_add_u32 s44, s44, s45\n"
      "s_add_u32 s70, s30, s44\n"
      "s_addc_u32 s71, s31, 0\n"
      "s_lshl_b32 s43, s33, 12\n"
      "s_add_u32 m0, s43, 65536\n"
      "s_nop 0\n"
      "global_load_lds_dwordx4 v224, s[70:71] offset:0\n"
      "global_load_lds_dwordx4 v225, s[70:71] offset:1024\n"
      "global_load_lds_dwordx4 v226, s[70:71] offset:2048\n"
      "global_load_lds_dwordx4 v227, s[70:71] offset:3072\n"
      "v_mfma_f32_16x16x32_bf16 v[88:91], v[132:135], a[184:187], v[88:91]\n"
      "v_mfma_f32_16x16x32_bf16 v[92:95], v[132:135], a[188:191], v[92:95]\n"
      "s_waitcnt vmcnt(18)\n"
      "v_mfma_f32_16x16x32_bf16 v[96:99], v[136:139], a[0:3], 0\n"
      "global_load_dwordx4 v[168:171], v241, s[74:75] offset:-2048 sc1\n"
      "v_mfma_f32_16x16x32_bf16 v[100:103], v[136:139], a[4:7], 0\n"
      "v_mfma_f32_16x16x32_bf16 v[104:107], v[136:139], a[8:11], 0\n"
      "v_mfma_f32_16x16x32_bf16 v[108:111], v[136:139], a[12:15], 0\n"
      "v_mfma_f32_16x16x32_bf16 v[112:115], v[136:139], a[16:19], 0\n"
      "global_load_dwordx4 v[172:175], v241, s[72:73] offset:-1024 sc1\n"
      "ds_read_b128 a[192:195], v236 offset:0\n"
      "v_mfma_f32_16x16x32_bf16 v[116:119], v[136:139], a[20:23], 0\n"
      "ds_read_b128 a[196:199], v236 offset:2048\n"
      "ds_read_b128 a[224:227], v237 offset:0\n"
      "v_mfma_f32_16x16x32_bf16 v[120:123], v[136:139], a[24:27], 0\n"
      "ds_read_b128 a[228:231], v237 offset:2048\n"
      "v_mfma_f32_16x16x32_bf16 v[124:127], v[136:139], a[28:31], 0\n"
      "v_mfma_f32_16x16x32_bf16 v[96:99], v[140:143], a[32:35], v[96:99]\n"
      "v_mfma_f32_16x16x32_bf16 v[100:103], v[140:143], a[36:39], v[100:103]\n"
      "s_lshl2_add_u32 s54, s36, s33\n"
      "s_lshl_b32 s55, s54, 15\n"
      "s_lshl2_add_u32 s56, s35, s34\n"
      "v_mfma_f32_16x16x32_bf16 v[104:107], v[140:143], a[40:43], v[104:107]\n"
      "s_lshl_b32 s56, s56, 6\n"
      "global_load_dwordx4 v[176:179], v240, s[14:15] offset:-1024 sc1\n"
      "s_add_u32 s55, s55, s56\n"
      "v_mfma_f32_16x16x32_bf16 v[108:111], v[140:143], a[44:47], v[108:111]\n"
      "v_and_b32 v209, 15, v243\n"
      "v_and_b32 v208, 48, v243\n"
      "v_mfma_f32_16x16x32_bf16 v[112:115], v[140:143], a[48:51], v[112:115]\n"
      "v_lshl_or_b32 v208, v209, 10, v208\n"
      "v_add_u32 v208, s55, v208\n"
      "v_mfma_f32_16x16x32_bf16 v[116:119], v[140:143], a[52:55], v[116:119]\n"
      "v_add_u32 v210, 16384, v208\n"
      "global_load_dwordx4 v[216:219], v208, s[16:17]\n"
      "v_mfma_f32_16x16x32_bf16 v[120:123], v[140:143], a[56:59], v[120:123]\n"
      "global_load_dwordx4 v[220:223], v210, s[16:17]\n"
      "s_lshr_b32 s55, s34, 1\n"
      "v_mfma_f32_16x16x32_bf16 v[124:127], v[140:143], a[60:63], v[124:127]\n"
      "s_waitcnt vmcnt(18)\n"
      "s_lshl2_add_u32 s55, s55, s35\n"
      "s_lshl_b32 s55, s55, 15\n"
      "v_mfma_f32_16x16x32_bf16 v[96:99], v[144:147], a[64:67], v[96:99]\n"
      "s_lshl_b32 s54, s54, 9\n"
      "ds_read_b128 a[200:203], v236 offset:16384\n"
      "s_add_u32 s54, s54, s55\n"
      "v_mfma_f32_16x16x32_bf16 v[100:103], v[144:147], a[68:71], v[100:103]\n"
      "s_add_u32 s54, s54, 786432\n"
      "ds_read_b128 a[204:207], v236 offset:18432\n"
      "v_lshl_add_u32 v209, v209, 4, s54\n"
      "v_mfma_f32_16x16x32_bf16 v[104:107], v[144:147], a[72:75], v[104:107]\n"
      "global_load_dwordx4 v[180:183], v240, s[74:75] offset:-1024 sc1\n"
      "ds_read_b128 a[232:235], v237 offset:16384\n"
      "v_mfma_f32_16x16x32_bf16 v[108:111], v[144:147], a[76:79], v[108:111]\n"
      "ds_read_b128 a[236:239], v237 offset:18432\n"
      "global_load_dwordx2 v[204:205], v209, s[58:59] offset:0 sc1\n"
      "v_mfma_f32_16x16x32_bf16 v[112:115], v[144:147], a[80:83], v[112:115]\n"
      "global_load_dwordx2 v[206:207], v209, s[58:59] offset:256 sc1\n"
      "s_add_u32 s68, s20, 16384\n"
      "s_addc_u32 s69, s21, 0\n"
      "v_mfma_f32_16x16x32_bf16 v[116:119], v[144:147], a[84:87], v[116:119]\n"
      "v_cmp_ne_u32 vcc, s37, v235\n"
      "s_nop 1\n"
      "s_cbranch_vccnz .Lp_s8_%=\n"
      ".Lp_o9_%=:\n"
      "v_mfma_f32_16x16x32_bf16 v[120:123], v[144:147], a[88:91], v[120:123]\n"
      "v_max_u32_dpp v234, v234, v234 quad_perm:[1,0,3,2] row_mask:0xf bank_mask:0xf\n"
      "s_nop 1\n"
      "v_max_u32_dpp v234, v234, v234 quad_perm:[2,3,0,1] row_mask:0xf bank_mask:0xf\n"
      "v_mfma_f32_16x16x32_bf16 v[124:127], v[144:147], a[92:95], v[124:127]\n"
      "s_nop 0\n"
      "v_max_u32_dpp v234, v234, v234 row_half_mirror row_mask:0xf bank_mask:0xf\n"
      "s_waitcnt lgkmcnt(7)\n"
      "v_mfma_f32_16x16x32_bf16 v[0:3], v[148:151], a[192:195], v[0:3]\n"
      "v_max_u32_dpp v234, v234, v234 row_mirror row_mask:0xf bank_mask:0xf\n"
      "v_mov_b32 v233, v234\n"
      "s_waitcnt lgkmcnt(6)\n"
      "v_mfma_f32_16x16x32_bf16 v[4:7], v[148:151], a[196:199], v[4:7]\n"
      "v_permlane16_swap_b32 v234, v233\n"
      "v_max_u32 v234, v234, v233\n"
      "s_waitcnt lgkmcnt(3)\n"
      "v_mfma_f32_16x16x32_bf16 v[8:11], v[148:151], a[200:203], v[8:11]\n"
      "global_load_dwordx4 v[184:187], v241, s[14:15] offset:-1024 sc1\n"
      "v_mov_b32 v233, v234\n"
      "s_waitcnt lgkmcnt(2)\n"
      "v_mfma_f32_16x16x32_bf16 v[12:15], v[148:151], a[204:207], v[12:15]\n"
      "v_permlane32_swap_b32 v234, v233\n"
      "v_max_u32 v234, v234, v233\n"
      "s_waitcnt vmcnt(21)\n"
      "ds_read_b128 a[208:211], v248 offset:4096\n"
      "ds_read_b128 a[212:215], v248 offset:6144\n"
      "ds_read_b128 a[240:243], v249 offset:4096\n"
      "ds_read_b128 a[244:247], v249 offset:6144\n"
      "s_waitcnt lgkmcnt(3)\n"
      "v_mfma_f32_16x16x32_bf16 v[16:19], v[148:151], a[208:211], v[16:19]\n"
      "s_waitcnt vmcnt(14)\n"
      "ds_read_b128 a[216:219], v236 offset:32768\n"
      "s_waitcnt lgkmcnt(3)\n"
      "v_mfma_f32_16x16x32_bf16 v[20:23], v[148:151], a[212:215], v[20:23]\n"
      "ds_read_b128 a[220:223], v236 offset:34816\n"
      "v_readfirstlane_b32 s40, v234\n"
      "ds_read_b128 a[248:251], v237 offset:32768\n"
      "ds_read_b128 a[252:255], v237 offset:34816\n"
      "s_waitcnt lgkmcnt(3)\n"
      "v_mfma_f32_16x16x32_bf16 v[24:27], v[148:151], a[216:219], v[24:27]\n"
      "s_sub_u32 s48, s33, 1\n"
      "s_and_b32 s48, s48, 3\n"
      "s_lshl_b32 s48, s48, 14\n"
      "s_waitcnt lgkmcnt(2)\n"
      "v_mfma_f32_16x16x32_bf16 v[28:31], v[148:151], a[220:223], v[28:31]\n"
      "v_lshl_add_u32 v244, v243, 2, s48\n"
      "s_sub_u32 s48, s33, 2\n"
      "v_mfma_f32_16x16x32_bf16 v[0:3], v[152:155], a[224:227], v[0:3]\n"
      "s_and_b32 s48, s48, 3\n"
      "s_lshl_b32 s48, s48, 14\n"
      "v_lshl_add_u32 v245, v243, 2, s48\n"
      "v_mfma_f32_16x16x32_bf16 v[4:7], v[152:155], a[228:231], v[4:7]\n"
      "s_sub_u32 s48, s33, 3\n"
      "s_and_b32 s48, s48, 3\n"
      "s_lshl_b32 s48, s48, 14\n"
      "v_mfma_f32_16x16x32_bf16 v[8:11], v[152:155], a[232:235], v[8:11]\n"
      "s_mov_b32 m0, s41\n"
      "global_load_dwordx4 v[188:191], v241, s[74:75] offset:-1024 sc1\n"
      "v_lshl_add_u32 v246, v243, 2, s48\n"
      "v_mfma_f32_16x16x32_bf16 v[12:15], v[152:155], a[236:239], v[12:15]\n"
      "s_andn2_b32 s42, 0x7ff, s40\n"
      "v_bfe_u32 v248, v243, 1, 3\n"
      "v_lshrrev_b32 v249, 4, v243\n"
      "v_mfma_f32_16x16x32_bf16 v[16:19], v[152:155], a[240:243], v[16:19]\n"
      "v_lshlrev_b32 v249, 1, v249\n"
      "v_sub_u32 v248, v248, v249\n"
      "v_mfma_f32_16x16x32_bf16 v[20:23], v[152:155], a[244:247], v[20:23]\n"
      "v_and_b32 v249, 16, v242\n"
      "v_mov_b32 v254, 0x3f80\n"
      "s_waitcnt lgkmcnt(1)\n"
      "v_mfma_f32_16x16x32_bf16 v[24:27], v[152:155], a[248:251], v[24:27]\n"
      "v_lshlrev_b32 v249, v249, v254\n"
      "v_cvt_pk_bf16_f32 v8, v8, v9\n"
      "s_waitcnt lgkmcnt(0)\n"
      "v_mfma_f32_16x16x32_bf16 v[28:31], v[152:155], a[252:255], v[28:31]\n"
      "v_cmp_eq_u32 vcc, 0, v248\n"
      "s_nop 1\n"
      "v_cndmask_b32 v250, 0, v249, vcc\n"
      "v_mfma_f32_16x16x32_bf16 v[32:35], v[156:159], a[192:195], v[32:35]\n"
      "v_cmp_eq_u32 vcc, 1, v248\n"
      "s_nop 1\n"
      "v_cndmask_b32 v251, 0, v249, vcc\n"
      "v_mfma_f32_16x16x32_bf16 v[36:39], v[156:159], a[196:199], v[36:39]\n"
      "v_mov_b32 v252, v250\n"
      "v_mov_b32 v253, v251\n"
      "v_mfma_f32_16x16x32_bf16 v[40:43], v[156:159], a[200:203], v[40:43]\n"
      "global_load_dwordx4 v[128:131], v240, s[10:11] offset:0 sc1\n"
      "v_mov_b32 v248, 0\n"
      "v_mfma_f32_16x16x32_bf16 v[44:47], v[156:159], a[204:207], v[44:47]\n"
      "v_mov_b32 v249, 0\n"
      "v_mov_b32 v254, 0\n"
      "v_mfma_f32_16x16x32_bf16 v[48:51], v[156:159], a[208:211], v[48:51]\n"
      "v_mov_b32 v255, 0\n"
      "v_lshrrev_b32 v201, 4, v243\n"
      "v_mfma_f32_16x16x32_bf16 v[52:55], v[156:159], a[212:215], v[52:55]\n"
      "s_add_u32 s48, s34, 1\n"
      "v_bfe_u32 v224, v243, 2, 2\n"
      "v_lshl_or_b32 v201, v201, 3, v224\n"
      "v_mfma_f32_16x16x32_bf16 v[56:59], v[156:159], a[216:219], v[56:59]\n"
      "s_and_b32 s48, s48, 3\n"
      "v_and_b32 v224, 3, v243\n"
      "v_lshlrev_b32 v224, 3, v224\n"
      "v_mfma_f32_16x16x32_bf16 v[60:63], v[156:159], a[220:223], v[60:63]\n"
      "s_waitcnt vmcnt(15)\n"
      "v_lshl_or_b32 v201, v201, 7, v224\n"
      "s_mov_b32 s64, 0xffff0000\n"
      "v_mfma_f32_16x16x32_bf16 v[32:35], v[160:163], a[224:227], v[32:35]\n"
      "s_mov_b32 s65, 0xffff0000\n"
      "s_mov_b32 s66, -1\n"
      "s_mov_b32 s67, 0\n"
      "v_mfma_f32_16x16x32_bf16 v[36:39], v[160:163], a[228:231], v[36:39]\n"
      "s_mul_i32 s48, s48, 36864\n"
      "v_bfe_u32 v224, v243, 4, 1\n"
      "v_and_b32 v203, 0xf0, v242\n"
      "v_mfma_f32_16x16x32_bf16 v[40:43], v[160:163], a[232:235], v[40:43]\n"
      "s_add_u32 s48, s48, 0\n"
      "global_load_dwordx4 v[132:135], v240, s[72:73] offset:0 sc1\n"
      "v_lshl_or_b32 v203, v224, 14, v203\n"
      "v_mfma_f32_16x16x32_bf16 v[44:47], v[160:163], a[236:239], v[44:47]\n"
      "s_lshr_b32 s54, s33, 1\n"
      "s_lshl1_add_u32 s54, s80, s54\n"
      "s_lshl_b32 s54, s54, 12\n"
      "v_mfma_f32_16x16x32_bf16 v[48:51], v[160:163], a[240:243], v[48:51]\n"
      "s_add_u32 s54, s54, 262144\n"
      "v_add_u32 v200, s54, v242\n"
      "s_lshl_b32 s54, s33, 12\n"
      "v_mfma_f32_16x16x32_bf16 v[52:55], v[160:163], a[244:247], v[52:55]\n"
      "s_add_u32 s54, s54, 65536\n"
      "v_add_u32 v201, s54, v201\n"
      "v_and_b32 v202, 15, v243\n"
      "v_mfma_f32_16x16x32_bf16 v[56:59], v[160:163], a[248:251], v[56:59]\n"
      "s_add_u32 s52, s60, s48\n"
      "s_addc_u32 s53, s61, 0\n"
      "v_lshrrev_b32 v199, 5, v243\n"
      "v_mfma_f32_16x16x32_bf16 v[60:63], v[160:163], a[252:255], v[60:63]\n"
      "v_lshl_or_b32 v202, v199, 4, v202\n"
      "s_add_u32 s54, s41, 12288\n"
      "v_mfma_f32_16x16x32_bf16 v[64:67], v[164:167], a[192:195], v[64:67]\n"
      "v_lshl_add_u32 v202, v202, 1, s54\n"
      "v_and_b32 v199, 48, v243\n"
      "v_mfma_f32_16x16x32_bf16 v[68:71], v[164:167], a[196:199], v[68:71]\n"
      "v_add_u32 v199, s54, v199\n"
      "s_and_b32 s54, s33, 1\n"
      "v_mfma_f32_16x16x32_bf16 v[72:75], v[164:167], a[200:203], v[72:75]\n"
      "s_lshl1_add_u32 s54, s81, s54\n"
      "global_load_dwordx4 v[136:139], v241, s[10:11] offset:0 sc1\n"
      "s_lshl_b32 s54, s54, 15\n"
      "v_mfma_f32_16x16x32_bf16 v[76:79], v[164:167], a[204:207], v[76:79]\n"
      "s_lshr_b32 s55, s33, 1\n"
      "s_lshl1_add_u32 s55, s80, s55\n"
      "s_lshl_b32 s55, s55, 8\n"
      "v_mfma_f32_16x16x32_bf16 v[80:83], v[164:167], a[208:211], v[80:83]\n"
      "s_add_u32 s54, s54, s55\n"
      "s_add_u32 s54, s54, 524288\n"
      "v_add_u32 v203, s54, v203\n"
      "v_mfma_f32_16x16x32_bf16 v[84:87], v[164:167], a[212:215], v[84:87]\n"
      "v_cvt_pk_bf16_f32 v9, v10, v11\n"
      "ds_write_addtid_b32 v8 offset:0\n"
      "v_mfma_f32_16x16x32_bf16 v[88:91], v[164:167], a[216:219], v[88:91]\n"
      "global_load_dwordx4 v[140:143], v241, s[72:73] offset:0 sc1\n"
      "v_cvt_pk_bf16_f32 v10, v12, v13\n"
      "v_mfma_f32_16x16x32_bf16 v[92:95], v[164:167], a[220:223], v[92:95]\n"
      "s_waitcnt vmcnt(12)\n"
      "ds_write_addtid_b32 v9 offset:256\n"
      "v_cvt_pk_bf16_f32 v11, v14, v15\n"
      "v_mfma_f32_16x16x32_bf16 v[64:67], v[168:171], a[224:227], v[64:67]\n"
      "ds_write_addtid_b32 v10 offset:512\n"
      "v_cvt_pk_bf16_f32 v12, v16, v17\n"
      "v_mfma_f32_16x16x32_bf16 v[68:71], v[168:171], a[228:231], v[68:71]\n"
      "ds_write_addtid_b32 v11 offset:768\n"
      "v_cvt_pk_bf16_f32 v13, v18, v19\n"
      "v_mfma_f32_16x16x32_bf16 v[72:75], v[168:171], a[232:235], v[72:75]\n"
      "global_load_dwordx4 v[144:147], v240, s[14:15] offset:0 sc1\n"
      "ds_write_addtid_b32 v12 offset:1024\n"
      "v_mfma_f32_16x16x32_bf16 v[76:79], v[168:171], a[236:239], v[76:79]\n"
      "v_cvt_pk_bf16_f32 v14, v20, v21\n"
      "ds_write_addtid_b32 v13 offset:1280\n"
      "v_mfma_f32_16x16x32_bf16 v[80:83], v[168:171], a[240:243], v[80:83]\n"
      "v_cvt_pk_bf16_f32 v15, v22, v23\n"
      "ds_write_addtid_b32 v14 offset:1536\n"
      "v_mfma_f32_16x16x32_bf16 v[84:87], v[168:171], a[244:247], v[84:87]\n"
      "v_cvt_pk_bf16_f32 v16, v24, v25\n"
      "ds_write_addtid_b32 v15 offset:1792\n"
      "v_mfma_f32_16x16x32_bf16 v[88:91], v[168:171], a[248:251], v[88:91]\n"
      "global_load_dwordx4 v[148:151], v240, s[74:75] offset:0 sc1\n"
      "v_cvt_pk_bf16_f32 v17, v26, v27\n"
      "v_mfma_f32_16x16x32_bf16 v[92:95], v[168:171], a[252:255], v[92:95]\n"
      "ds_write_addtid_b32 v16 offset:2048\n"
      "v_cvt_pk_bf16_f32 v18, v28, v29\n"
      "v_mfma_f32_16x16x32_bf16 v[96:99], v[172:175], a[96:99], v[96:99]\n"
      "ds_write_addtid_b32 v17 offset:2304\n"
      "v_cvt_pk_bf16_f32 v19, v30, v31\n"
      "v_mfma_f32_16x16x32_bf16 v[100:103], v[172:175], a[100:103], v[100:103]\n"
      "ds_write_addtid_b32 v18 offset:2560\n"
      "ds_write_addtid_b32 v19 offset:2816\n"
      "v_mfma_f32_16x16x32_bf16 v[104:107], v[172:175], a[104:107], v[104:107]\n"
      "global_load_dwordx4 v[152:155], v241, s[14:15] offset:0 sc1\n"
      "v_cvt_pk_bf16_f32 v40, v40, v41\n"
      "v_mfma_f32_16x16x32_bf16 v[108:111], v[172:175], a[108:111], v[108:111]\n"
      "v_cvt_pk_bf16_f32 v41, v42, v43\n"
      "ds_write_addtid_b32 v40 offset:3072\n"
      "v_mfma_f32_16x16x32_bf16 v[112:115], v[172:175], a[112:115], v[112:115]\n"
      "v_cvt_pk_bf16_f32 v42, v44, v45\n"
      "ds_write_addtid_b32 v41 offset:3328\n"
      "v_mfma_f32_16x16x32_bf16 v[116:119], v[172:175], a[116:119], v[116:119]\n"
      "v_cvt_pk_bf16_f32 v43, v46, v47\n"
      "ds_write_addtid_b32 v42 offset:3584\n"
      "v_mfma_f32_16x16x32_bf16 v[120:123], v[172:175], a[120:123], v[120:123]\n"
      "global_load_dwordx4 v[156:159], v241, s[74:75] offset:0 sc1\n"
      "v_cvt_pk_bf16_f32 v44, v48, v49\n"
      "v_mfma_f32_16x16x32_bf16 v[124:127], v[172:175], a[124:127], v[124:127]\n"
      "s_waitcnt vmcnt(12)\n"
      "ds_write_addtid_b32 v43 offset:3840\n"
      "v_cvt_pk_bf16_f32 v45, v50, v51\n"
      "v_mfma_f32_16x16x32_bf16 v[96:99], v[176:179], a[128:131], v[96:99]\n"
      "ds_write_addtid_b32 v44 offset:4096\n"
      "v_cvt_pk_bf16_f32 v46, v52, v53\n"
      "v_mfma_f32_16x16x32_bf16 v[100:103], v[176:179], a[132:135], v[100:103]\n"
      "ds_write_addtid_b32 v45 offset:4352\n"
      "v_cvt_pk_bf16_f32 v47, v54, v55\n"
      "v_mfma_f32_16x16x32_bf16 v[104:107], v[176:179], a[136:139], v[104:107]\n"
      "global_load_dwordx4 v[160:163], v240, s[10:11] offset:1024 sc1\n"
      "ds_write_addtid_b32 v46 offset:4608\n"
      "v_mfma_f32_16x16x32_bf16 v[108:111], v[176:179], a[140:143], v[108:111]\n"
      "s_waitcnt vmcnt(11)\n"
      "v_bitop3_b32 v210, v205, v207, s37 bitop3:0x7e\n"
      "v_cmp_ne_u32 vcc, 0, v210\n"
      "s_nop 1\n"
      "s_cbranch_vccnz .Lp_s11_%=\n"
      ".Lp_o12_%=:\n"
      "v_mfma_f32_16x16x32_bf16 v[112:115], v[176:179], a[144:147], v[112:115]\n"
      "v_cvt_pk_bf16_f32 v48, v56, v57\n"
      "v_sub_f32 v205, 1.0, v204\n"
      "v_mfma_f32_16x16x32_bf16 v[116:119], v[176:179], a[148:151], v[116:119]\n"
      "ds_write_addtid_b32 v47 offset:4864\n"
      "v_cvt_pk_bf16_f32 v49, v58, v59\n"
      "v_mfma_f32_16x16x32_bf16 v[120:123], v[176:179], a[152:155], v[120:123]\n"
      "global_load_dwordx4 v[164:167], v240, s[72:73] offset:1024 sc1\n"
      "v_cvt_pk_bf16_f32 v205, v205, v205\n"
      "v_mfma_f32_16x16x32_bf16 v[124:127], v[176:179], a[156:159], v[124:127]\n"
      "ds_write_addtid_b32 v48 offset:5120\n"
      "v_cvt_pk_bf16_f32 v50, v60, v61\n"
      "v_mfma_f32_16x16x32_bf16 v[96:99], v[180:183], a[160:163], v[96:99]\n"
      "ds_write_addtid_b32 v49 offset:5376\n"
      "v_cvt_pk_bf16_f32 v51, v62, v63\n"
      "v_mfma_f32_16x16x32_bf16 v[100:103], v[180:183], a[164:167], v[100:103]\n"
      "ds_write_addtid_b32 v50 offset:5632\n"
      "v_and_b32 v205, 0xffff0000, v205\n"
      "v_mfma_f32_16x16x32_bf16 v[104:107], v[180:183], a[168:171], v[104:107]\n"
      "global_load_dwordx4 v[168:171], v241, s[10:11] offset:1024 sc1\n"
      "v_sub_f32 v207, 1.0, v206\n"
      "v_mfma_f32_16x16x32_bf16 v[108:111], v[180:183], a[172:175], v[108:111]\n"
      "ds_write_addtid_b32 v51 offset:5888\n"
      "v_cvt_pk_bf16_f32 v207, v207, v207\n"
      "v_mfma_f32_16x16x32_bf16 v[112:115], v[180:183], a[176:179], v[112:115]\n"
      "v_and_b32 v207, 0xffff0000, v207\n"
      "v_lshlrev_b32 v208, 16, v216\n"
      "v_mfma_f32_16x16x32_bf16 v[116:119], v[180:183], a[180:183], v[116:119]\n"
      "v_and_b32 v209, 0xffff0000, v216\n"
      "v_mul_f32 v208, v205, v208\n"
      "v_mfma_f32_16x16x32_bf16 v[120:123], v[180:183], a[184:187], v[120:123]\n"
      "global_load_dwordx4 v[172:175], v241, s[72:73] offset:1024 sc1\n"
      "v_mul_f32 v209, v205, v209\n"
      "v_mfma_f32_16x16x32_bf16 v[124:127], v[180:183], a[188:191], v[124:127]\n"
      "s_waitcnt vmcnt(12)\n"
      "s_waitcnt lgkmcnt(0)\n"
      "s_barrier\n"
      "v_mfma_f32_16x16x32_bf16 v[96:99], v[184:187], a[192:195], v[96:99]\n"
      "ds_read2st64_b32 v[20:21], v244 offset0:0 offset1:1\n"
      "v_lshlrev_b32 v212, 16, v220\n"
      "v_mfma_f32_16x16x32_bf16 v[100:103], v[184:187], a[196:199], v[100:103]\n"
      "ds_read2st64_b32 v[22:23], v245 offset0:4 offset1:5\n"
      "v_and_b32 v213, 0xffff0000, v220\n"
      "v_mfma_f32_16x16x32_bf16 v[104:107], v[184:187], a[200:203], v[104:107]\n"
      "global_load_dwordx4 v[176:179], v240, s[14:15] offset:1024 sc1\n"
      "v_mul_f32 v212, v207, v212\n"
      "v_mfma_f32_16x16x32_bf16 v[108:111], v[184:187], a[204:207], v[108:111]\n"
      "ds_read2st64_b32 v[24:25], v244 offset0:2 offset1:3\n"
      "v_mul_f32 v213, v207, v213\n"
      "v_mfma_f32_16x16x32_bf16 v[112:115], v[184:187], a[208:211], v[112:115]\n"
      "ds_read2st64_b32 v[26:27], v245 offset0:6 offset1:7\n"
      "v_lshlrev_b32 v210, 16, v217\n"
      "v_mfma_f32_16x16x32_bf16 v[116:119], v[184:187], a[212:215], v[116:119]\n"
      "ds_read2st64_b32 v[28:29], v246 offset0:8 offset1:9\n"
      "v_and_b32 v211, 0xffff0000, v217\n"
      "v_mfma_f32_16x16x32_bf16 v[120:123], v[184:187], a[216:219], v[120:123]\n"
      "global_load_dwordx4 v[180:183], v240, s[74:75] offset:1024 sc1\n"
      "v_mul_f32 v210, v205, v210\n"
      "v_mfma_f32_16x16x32_bf16 v[124:127], v[184:187], a[220:223], v[124:127]\n"
      "ds_read2st64_b32 v[30:31], v246 offset0:10 offset1:11\n"
      "v_mul_f32 v211, v205, v211\n"
      "v_mfma_f32_16x16x32_bf16 v[96:99], v[188:191], a[224:227], v[96:99]\n"
      "ds_read2st64_b32 v[52:53], v244 offset0:12 offset1:13\n"
      "v_lshlrev_b32 v214, 16, v221\n"
      "v_mfma_f32_16x16x32_bf16 v[100:103], v[188:191], a[228:231], v[100:103]\n"
      "ds_read2st64_b32 v[54:55], v245 offset0:16 offset1:17\n"
      "v_and_b32 v215, 0xffff0000, v221\n"
      "v_mfma_f32_16x16x32_bf16 v[104:107], v[188:191], a[232:235], v[104:107]\n"
      "global_load_dwordx4 v[184:187], v241, s[14:15] offset:1024 sc1\n"
      "v_mul_f32 v214, v207, v214\n"
      "v_mfma_f32_16x16x32_bf16 v[108:111], v[188:191], a[236:239], v[108:111]\n"
      "s_waitcnt lgkmcnt(2)\n"
      "v_mfma_f32_16x16x32_bf16 v[0:3], v[250:253], v[20:23], v[0:3]\n"
      "v_mul_f32 v215, v207, v215\n"
      "v_mfma_f32_16x16x32_bf16 v[112:115], v[188:191], a[240:243], v[112:115]\n"
      "v_mfma_f32_16x16x32_bf16 v[4:7], v[250:253], v[24:27], v[4:7]\n"
      "v_lshlrev_b32 v216, 16, v218\n"
      "v_mfma_f32_16x16x32_bf16 v[116:119], v[188:191], a[244:247], v[116:119]\n"
      "v_mfma_f32_16x16x32_bf16 v[0:3], v[252:255], v[28:31], v[0:3]\n"
      "v_and_b32 v217, 0xffff0000, v218\n"
      "v_mfma_f32_16x16x32_bf16 v[120:123], v[188:191], a[248:251], v[120:123]\n"
      "v_mfma_f32_16x16x32_bf16 v[4:7], v[248:251], v[28:31], v[4:7]\n"
      "v_mov_b32 v11, s37\n"
      "v_mfma_f32_16x16x32_bf16 v[124:127], v[188:191], a[252:255], v[124:127]\n"
      "s_waitcnt vmcnt(13)\n"
      "global_load_dwordx4 v[188:191], v241, s[74:75] offset:1024 sc1\n"
      "v_mul_f32 v216, v205, v216\n"
      "v_cvt_pk_bf16_f32 v8, v0, v1\n"
      "v_cvt_pk_bf16_f32 v9, v2, v3\n"
      "s_nop 0\n"
      "v_cvt_pk_bf16_f32 v10, v4, v5\n"
      "v_cvt_pk_bf16_f32 v247, v6, v7\n"
      "global_store_dwordx4 v242, v[8:11], s[52:53] offset:0 sc1\n"
      "v_mfma_f32_16x16x32_bf16 v[0:3], v[128:131], a[0:3], 0\n"
      "ds_read2st64_b32 v[56:57], v244 offset0:14 offset1:15\n"
      "v_mul_f32 v217, v205, v217\n"
      "v_mfma_f32_16x16x32_bf16 v[4:7], v[128:131], a[4:7], 0\n"
      "ds_read2st64_b32 v[58:59], v245 offset0:18 offset1:19\n"
      "v_lshlrev_b32 v220, 16, v222\n"
      "v_mfma_f32_16x16x32_bf16 v[8:11], v[128:131], a[8:11], 0\n"
      "ds_read2st64_b32 v[60:61], v246 offset0:20 offset1:21\n"
      "v_and_b32 v221, 0xffff0000, v222\n"
      "v_mfma_f32_16x16x32_bf16 v[12:15], v[128:131], a[12:15], 0\n"
      "ds_read2st64_b32 v[62:63], v246 offset0:22 offset1:23\n"
      "v_cvt_pk_bf16_f32 v72, v72, v73\n"
      "v_mfma_f32_16x16x32_bf16 v[16:19], v[128:131], a[16:19], 0\n"
      "v_cvt_pk_bf16_f32 v73, v74, v75\n"
      "v_mul_f32 v220, v207, v220\n"
      "v_mfma_f32_16x16x32_bf16 v[20:23], v[128:131], a[20:23], 0\n"
      "ds_write_addtid_b32 v72 offset:6144\n"
      "v_cvt_pk_bf16_f32 v74, v76, v77\n"
      "v_mfma_f32_16x16x32_bf16 v[24:27], v[128:131], a[24:27], 0\n"
      "ds_write_addtid_b32 v73 offset:6400\n"
      "v_cvt_pk_bf16_f32 v75, v78, v79\n"
      "v_mfma_f32_16x16x32_bf16 v[28:31], v[128:131], a[28:31], 0\n"
      "s_waitcnt lgkmcnt(2)\n"
      "global_load_dwordx4 v[128:131], v240, s[10:11] offset:2048 sc1\n"
      "v_mul_f32 v221, v207, v221\n"
      "v_mfma_f32_16x16x32_bf16 v[0:3], v[132:135], a[32:35], v[0:3]\n"
      "v_mfma_f32_16x16x32_bf16 v[32:35], v[250:253], v[52:55], v[32:35]\n"
      "v_lshlrev_b32 v218, 16, v219\n"
      "v_mfma_f32_16x16x32_bf16 v[4:7], v[132:135], a[36:39], v[4:7]\n"
      "v_mfma_f32_16x16x32_bf16 v[36:39], v[250:253], v[56:59], v[36:39]\n"
      "v_and_b32 v219, 0xffff0000, v219\n"
      "v_mfma_f32_16x16x32_bf16 v[8:11], v[132:135], a[40:43], v[8:11]\n"
      "v_mfma_f32_16x16x32_bf16 v[32:35], v[252:255], v[60:63], v[32:35]\n"
      "v_mul_f32 v218, v205, v218\n"
      "v_mfma_f32_16x16x32_bf16 v[12:15], v[132:135], a[44:47], v[12:15]\n"
      "v_mfma_f32_16x16x32_bf16 v[36:39], v[248:251], v[60:63], v[36:39]\n"
      "v_mov_b32 v43, s37\n"
      "v_mfma_f32_16x16x32_bf16 v[16:19], v[132:135], a[48:51], v[16:19]\n"
      "ds_write_addtid_b32 v74 offset:6656\n"
      "v_mov_b32 v44, v247\n"
      "v_mfma_f32_16x16x32_bf16 v[20:23], v[132:135], a[52:55], v[20:23]\n"
      "v_mov_b32 v47, s37\n"
      "v_cvt_pk_bf16_f32 v76, v80, v81\n"
      "v_mfma_f32_16x16x32_bf16 v[24:27], v[132:135], a[56:59], v[24:27]\n"
      "ds_write_addtid_b32 v75 offset:6912\n"
      "v_cvt_pk_bf16_f32 v77, v82, v83\n"
      "v_mfma_f32_16x16x32_bf16 v[28:31], v[132:135], a[60:63], v[28:31]\n"
      "s_waitcnt vmcnt(14)\n"
      "global_load_dwordx4 v[132:135], v240, s[72:73] offset:2048 sc1\n"
      "v_cvt_pk_bf16_f32 v40, v32, v33\n"
      "v_mfma_f32_16x16x32_bf16 v[0:3], v[136:139], a[64:67], v[0:3]\n"
      "ds_write_addtid_b32 v76 offset:7168\n"
      "v_cvt_pk_bf16_f32 v41, v34, v35\n"
      "v_mfma_f32_16x16x32_bf16 v[4:7], v[136:139], a[68:71], v[4:7]\n"
      "v_cvt_pk_bf16_f32 v42, v36, v37\n"
      "v_cvt_pk_bf16_f32 v45, v38, v39\n"
      "v_mfma_f32_16x16x32_bf16 v[8:11], v[136:139], a[72:75], v[8:11]\n"
      "global_store_dwordx4 v242, v[40:43], s[52:53] offset:1024 sc1\n"
      "v_cvt_pk_bf16_f32 v78, v84, v85\n"
      "v_mfma_f32_16x16x32_bf16 v[12:15], v[136:139], a[76:79], v[12:15]\n"
      "global_store_dwordx4 v242, v[44:47], s[52:53] offset:2048 sc1\n"
      "s_add_u32 s48, s34, 2\n"
      "v_mfma_f32_16x16x32_bf16 v[16:19], v[136:139], a[80:83], v[16:19]\n"
      "s_and_b32 s48, s48, 3\n"
      "ds_write_addtid_b32 v77 offset:7424\n"
      "s_mul_i32 s48, s48, 36864\n"
      "v_mfma_f32_16x16x32_bf16 v[20:23], v[136:139], a[84:87], v[20:23]\n"
      "s_add_u32 s48, s48, 12288\n"
      "s_add_u32 s52, s60, s48\n"
      "s_addc_u32 s53, s61, 0\n"
      "v_mfma_f32_16x16x32_bf16 v[24:27], v[136:139], a[88:91], v[24:27]\n"
      "v_cvt_pk_bf16_f32 v79, v86, v87\n"
      "v_mul_f32 v219, v205, v219\n"
      "v_mfma_f32_16x16x32_bf16 v[28:31], v[136:139], a[92:95], v[28:31]\n"
      "global_load_dwordx4 v[136:139], v241, s[10:11] offset:2048 sc1\n"
      "v_lshlrev_b32 v222, 16, v223\n"
      "v_mfma_f32_16x16x32_bf16 v[0:3], v[140:143], a[96:99], v[0:3]\n"
      "ds_write_addtid_b32 v78 offset:7680\n"
      "v_cvt_pk_bf16_f32 v80, v88, v89\n"
      "v_mfma_f32_16x16x32_bf16 v[4:7], v[140:143], a[100:103], v[4:7]\n"
      "ds_write_addtid_b32 v79 offset:7936\n"
      "v_cvt_pk_bf16_f32 v81, v90, v91\n"
      "v_mfma_f32_16x16x32_bf16 v[8:11], v[140:143], a[104:107], v[8:11]\n"
      "ds_write_addtid_b32 v80 offset:8192\n"
      "v_cvt_pk_bf16_f32 v82, v92, v93\n"
      "v_mfma_f32_16x16x32_bf16 v[12:15], v[140:143], a[108:111], v[12:15]\n"
      "ds_write_addtid_b32 v81 offset:8448\n"
      "v_cvt_pk_bf16_f32 v83, v94, v95\n"
      "v_mfma_f32_16x16x32_bf16 v[16:19], v[140:143], a[112:115], v[16:19]\n"
      "ds_write_addtid_b32 v82 offset:8704\n"
      "v_and_b32 v223, 0xffff0000, v223\n"
      "v_mfma_f32_16x16x32_bf16 v[20:23], v[140:143], a[116:119], v[20:23]\n"
      "ds_write_addtid_b32 v83 offset:8960\n"
      "v_cvt_pk_bf16_f32 v104, v104, v105\n"
      "v_mfma_f32_16x16x32_bf16 v[24:27], v[140:143], a[120:123], v[24:27]\n"
      "v_cvt_pk_bf16_f32 v105, v106, v107\n"
      "v_mul_f32 v222, v207, v222\n"
      "v_mfma_f32_16x16x32_bf16 v[28:31], v[140:143], a[124:127], v[28:31]\n"
      "s_waitcnt vmcnt(16)\n"
      "global_load_dwordx4 v[140:143], v241, s[72:73] offset:2048 sc1\n"
      "v_mul_f32 v223, v207, v223\n"
      "v_mfma_f32_16x16x32_bf16 v[0:3], v[144:147], a[128:131], v[0:3]\n"
      "ds_write_addtid_b32 v104 offset:9216\n"
      "v_cvt_pk_bf16_f32 v106, v108, v109\n"
      "v_mfma_f32_16x16x32_bf16 v[4:7], v[144:147], a[132:135], v[4:7]\n"
      "ds_write_addtid_b32 v105 offset:9472\n"
      "v_cvt_pk_bf16_f32 v107, v110, v111\n"
      "v_mfma_f32_16x16x32_bf16 v[8:11], v[144:147], a[136:139], v[8:11]\n"
      "global_load_dwordx4 v[224:227], v200, s[58:59] offset:0 sc1\n"
      "v_mfma_f32_16x16x32_bf16 v[12:15], v[144:147], a[140:143], v[12:15]\n"
      "global_load_dwordx4 v[228:231], v200, s[58:59] offset:1024 sc1\n"
      "v_mfma_f32_16x16x32_bf16 v[16:19], v[144:147], a[144:147], v[16:19]\n"
      "global_load_dwordx4 v[232:235], v200, s[58:59] offset:2048 sc1\n"
      "v_mfma_f32_16x16x32_bf16 v[20:23], v[144:147], a[148:151], v[20:23]\n"
      "global_load_dwordx4 v[236:239], v200, s[58:59] offset:3072 sc1\n"
      "v_mfma_f32_16x16x32_bf16 v[24:27], v[144:147], a[152:155], v[24:27]\n"
      "ds_write_addtid_b32 v106 offset:9728\n"
      "v_cvt_pk_bf16_f32 v108, v112, v113\n"
      "v_mfma_f32_16x16x32_bf16 v[28:31], v[144:147], a[156:159], v[28:31]\n"
      "global_load_dwordx4 v[144:147], v240, s[14:15] offset:2048 sc1\n"
      "v_mfma_f32_16x16x32_bf16 v[0:3], v[148:151], a[160:163], v[0:3]\n"
      "ds_write_addtid_b32 v107 offset:9984\n"
      "v_cvt_pk_bf16_f32 v109, v114, v115\n"
      "v_mfma_f32_16x16x32_bf16 v[4:7], v[148:151], a[164:167], v[4:7]\n"
      "ds_write_addtid_b32 v108 offset:10240\n"
      "v_cvt_pk_bf16_f32 v110, v116, v117\n"
      "v_mfma_f32_16x16x32_bf16 v[8:11], v[148:151], a[168:171], v[8:11]\n"
      "ds_write_addtid_b32 v109 offset:10496\n"
      "v_cvt_pk_bf16_f32 v111, v118, v119\n"
      "v_mfma_f32_16x16x32_bf16 v[12:15], v[148:151], a[172:175], v[12:15]\n"
      "ds_write_addtid_b32 v110 offset:10752\n"
      "v_cvt_pk_bf16_f32 v112, v120, v121\n"
      "v_mfma_f32_16x16x32_bf16 v[16:19], v[148:151], a[176:179], v[16:19]\n"
      "ds_write_addtid_b32 v111 offset:11008\n"
      "v_cvt_pk_bf16_f32 v113, v122, v123\n"
      "v_mfma_f32_16x16x32_bf16 v[20:23], v[148:151], a[180:183], v[20:23]\n"
      "ds_write_addtid_b32 v112 offset:11264\n"
      "v_cvt_pk_bf16_f32 v114, v124, v125\n"
      "v_mfma_f32_16x16x32_bf16 v[24:27], v[148:151], a[184:187], v[24:27]\n"
      "ds_write_addtid_b32 v113 offset:11520\n"
      "v_cvt_pk_bf16_f32 v115, v126, v127\n"
      "v_mfma_f32_16x16x32_bf16 v[28:31], v[148:151], a[188:191], v[28:31]\n"
      "s_waitcnt vmcnt(20)\n"
      "global_load_dwordx4 v[148:151], v240, s[74:75] offset:2048 sc1\n"
      "v_mfma_f32_16x16x32_bf16 v[0:3], v[152:155], a[192:195], v[0:3]\n"
      "ds_write_addtid_b32 v114 offset:11776\n"
      "v_mfma_f32_16x16x32_bf16 v[4:7], v[152:155], a[196:199], v[4:7]\n"
      "ds_write_addtid_b32 v115 offset:12032\n"
      "v_mfma_f32_16x16x32_bf16 v[8:11], v[152:155], a[200:203], v[8:11]\n"
      "v_mfma_f32_16x16x32_bf16 v[12:15], v[152:155], a[204:207], v[12:15]\n"
      "v_mfma_f32_16x16x32_bf16 v[16:19], v[152:155], a[208:211], v[16:19]\n"
      "s_waitcnt lgkmcnt(0)\n"
      "s_barrier\n"
      "v_mfma_f32_16x16x32_bf16 v[20:23], v[152:155], a[212:215], v[20:23]\n"
      "ds_read2st64_b32 v[84:85], v244 offset0:24 offset1:25\n"
      "v_mfma_f32_16x16x32_bf16 v[24:27], v[152:155], a[216:219], v[24:27]\n"
      "ds_read2st64_b32 v[86:87], v245 offset0:28 offset1:29\n"
      "v_mfma_f32_16x16x32_bf16 v[28:31], v[152:155], a[220:223], v[28:31]\n"
      "global_load_dwordx4 v[152:155], v241, s[14:15] offset:2048 sc1\n"
      "v_mfma_f32_16x16x32_bf16 v[0:3], v[156:159], a[224:227], v[0:3]\n"
      "ds_read2st64_b32 v[88:89], v244 offset0:26 offset1:27\n"
      "v_mfma_f32_16x16x32_bf16 v[4:7], v[156:159], a[228:231], v[4:7]\n"
      "ds_read2st64_b32 v[90:91], v245 offset0:30 offset1:31\n"
      "v_mfma_f32_16x16x32_bf16 v[8:11], v[156:159], a[232:235], v[8:11]\n"
      "ds_read2st64_b32 v[92:93], v246 offset0:32 offset1:33\n"
      "v_mfma_f32_16x16x32_bf16 v[12:15], v[156:159], a[236:239], v[12:15]\n"
      "ds_read2st64_b32 v[94:95], v246 offset0:34 offset1:35\n"
      "v_mfma_f32_16x16x32_bf16 v[16:19], v[156:159], a[240:243], v[16:19]\n"
      "ds_read2st64_b32 v[116:117], v244 offset0:36 offset1:37\n"
      "v_mfma_f32_16x16x32_bf16 v[20:23], v[156:159], a[244:247], v[20:23]\n"
      "ds_read2st64_b32 v[118:119], v245 offset0:40 offset1:41\n"
      "v_mfma_f32_16x16x32_bf16 v[24:27], v[156:159], a[248:251], v[24:27]\n"
      "ds_read2st64_b32 v[120:121], v244 offset0:38 offset1:39\n"
      "v_mfma_f32_16x16x32_bf16 v[28:31], v[156:159], a[252:255], v[28:31]\n"
      "s_waitcnt vmcnt(20)\n"
      "global_load_dwordx4 v[156:159], v241, s[74:75] offset:2048 sc1\n"
      "s_waitcnt lgkmcnt(3)\n"
      "v_mfma_f32_16x16x32_bf16 v[32:35], v[160:163], a[0:3], 0\n"
      "v_mfma_f32_16x16x32_bf16 v[64:67], v[250:253], v[84:87], v[64:67]\n"
      "v_mfma_f32_16x16x32_bf16 v[36:39], v[160:163], a[4:7], 0\n"
      "v_mfma_f32_16x16x32_bf16 v[68:71], v[250:253], v[88:91], v[68:71]\n"
      "v_mfma_f32_16x16x32_bf16 v[40:43], v[160:163], a[8:11], 0\n"
      "v_mfma_f32_16x16x32_bf16 v[64:67], v[252:255], v[92:95], v[64:67]\n"
      "v_mfma_f32_16x16x32_bf16 v[44:47], v[160:163], a[12:15], 0\n"
      "v_mfma_f32_16x16x32_bf16 v[68:71], v[248:251], v[92:95], v[68:71]\n"
      "v_mov_b32 v75, s37\n"
      "v_mfma_f32_16x16x32_bf16 v[48:51], v[160:163], a[16:19], 0\n"
      "ds_read2st64_b32 v[122:123], v245 offset0:42 offset1:43\n"
      "v_mfma_f32_16x16x32_bf16 v[52:55], v[160:163], a[20:23], 0\n"
      "ds_read2st64_b32 v[124:125], v246 offset0:44 offset1:45\n"
      "s_nop 0\n"
      "v_cvt_pk_bf16_f32 v72, v64, v65\n"
      "v_mfma_f32_16x16x32_bf16 v[56:59], v[160:163], a[24:27], 0\n"
      "ds_read2st64_b32 v[126:127], v246 offset0:46 offset1:47\n"
      "v_cvt_pk_bf16_f32 v73, v66, v67\n"
      "v_mfma_f32_16x16x32_bf16 v[60:63], v[160:163], a[28:31], 0\n"
      "global_load_dwordx4 v[160:163], v240, s[10:11] offset:3072 sc1\n"
      "v_cvt_pk_bf16_f32 v74, v68, v69\n"
      "v_mfma_f32_16x16x32_bf16 v[32:35], v[164:167], a[32:35], v[32:35]\n"
      "v_cvt_pk_bf16_f32 v247, v70, v71\n"
      "v_cvt_pk_bf16_f32 v8, v8, v9\n"
      "v_mfma_f32_16x16x32_bf16 v[36:39], v[164:167], a[36:39], v[36:39]\n"
      "global_store_dwordx4 v242, v[72:75], s[52:53] offset:0 sc1\n"
      "v_cvt_pk_bf16_f32 v9, v10, v11\n"
      "v_mfma_f32_16x16x32_bf16 v[40:43], v[164:167], a[40:43], v[40:43]\n"
      "s_waitcnt lgkmcnt(0)\n"
      "v_mfma_f32_16x16x32_bf16 v[96:99], v[250:253], v[116:119], v[96:99]\n"
      "v_mfma_f32_16x16x32_bf16 v[44:47], v[164:167], a[44:47], v[44:47]\n"
      "v_mfma_f32_16x16x32_bf16 v[100:103], v[250:253], v[120:123], v[100:103]\n"
      "v_mfma_f32_16x16x32_bf16 v[48:51], v[164:167], a[48:51], v[48:51]\n"
      "v_mfma_f32_16x16x32_bf16 v[96:99], v[252:255], v[124:127], v[96:99]\n"
      "v_mfma_f32_16x16x32_bf16 v[52:55], v[164:167], a[52:55], v[52:55]\n"
      "s_waitcnt vmcnt(6)\n"
      "v_bitop3_b32 v192, v225, v227, s37 bitop3:0x7e\n"
      "v_bitop3_b32 v192, v192, v229, s37 bitop3:0xf6\n"
      "v_bitop3_b32 v192, v192, v231, s37 bitop3:0xf6\n"
      "v_bitop3_b32 v192, v192, v233, s37 bitop3:0xf6\n"
      "v_bitop3_b32 v192, v192, v235, s37 bitop3:0xf6\n"
      "v_bitop3_b32 v192, v192, v237, s37 bitop3:0xf6\n"
      "v_bitop3_b32 v192, v192, v239, s37 bitop3:0xf6\n"
      "v_cmp_ne_u32 vcc, 0, v192\n"
      "s_nop 1\n"
      "s_cbranch_vccnz .Lp_s14_%=\n"
      ".Lp_o15_%=:\n"
      "v_mfma_f32_16x16x32_bf16 v[56:59], v[164:167], a[56:59], v[56:59]\n"
      "v_mfma_f32_16x16x32_bf16 v[100:103], v[248:251], v[124:127], v[100:103]\n"
      "v_add_f32 v192, v224, v228\n"
      "v_mfma_f32_16x16x32_bf16 v[60:63], v[164:167], a[60:63], v[60:63]\n"
      "global_load_dwordx4 v[164:167], v240, s[72:73] offset:3072 sc1\n"
      "v_add_f32 v193, v226, v230\n"
      "v_mfma_f32_16x16x32_bf16 v[32:35], v[168:171], a[64:67], v[32:35]\n"
      "ds_write_addtid_b32 v8 offset:0\n"
      "v_add_f32 v194, v232, v236\n"
      "v_mfma_f32_16x16x32_bf16 v[36:39], v[168:171], a[68:71], v[36:39]\n"
      "v_add_f32 v195, v234, v238\n"
      "v_mov_b32 v107, s37\n"
      "v_mfma_f32_16x16x32_bf16 v[40:43], v[168:171], a[72:75], v[40:43]\n"
      "ds_read_b64_tr_b16 v[224:225], v201 offset:0\n"
      "v_permlane32_swap_b32 v192, v193\n"
      "v_mfma_f32_16x16x32_bf16 v[44:47], v[168:171], a[76:79], v[44:47]\n"
      "ds_read_b64_tr_b16 v[226:227], v201 offset:512\n"
      "v_permlane32_swap_b32 v194, v195\n"
      "v_mfma_f32_16x16x32_bf16 v[48:51], v[168:171], a[80:83], v[48:51]\n"
      "ds_read_b64_tr_b16 v[228:229], v201 offset:32\n"
      "v_add_f32 v192, v192, v193\n"
      "v_mfma_f32_16x16x32_bf16 v[52:55], v[168:171], a[84:87], v[52:55]\n"
      "ds_read_b64_tr_b16 v[230:231], v201 offset:544\n"
      "v_add_f32 v194, v194, v195\n"
      "v_mfma_f32_16x16x32_bf16 v[56:59], v[168:171], a[88:91], v[56:59]\n"
      "ds_read_b64_tr_b16 v[232:233], v201 offset:64\n"
      "v_mov_b32 v193, v192\n"
      "v_mfma_f32_16x16x32_bf16 v[60:63], v[168:171], a[92:95], v[60:63]\n"
      "global_load_dwordx4 v[168:171], v241, s[10:11] offset:3072 sc1\n"
      "v_mov_b32 v108, v247\n"
      "v_mfma_f32_16x16x32_bf16 v[32:35], v[172:175], a[96:99], v[32:35]\n"
      "ds_read_b64_tr_b16 v[234:235], v201 offset:576\n"
      "v_mov_b32 v195, v194\n"
      "v_mfma_f32_16x16x32_bf16 v[36:39], v[172:175], a[100:103], v[36:39]\n"
      "ds_read_b64_tr_b16 v[236:237], v201 offset:96\n"
      "v_permlane16_swap_b32 v192, v193\n"
      "v_mfma_f32_16x16x32_bf16 v[40:43], v[172:175], a[104:107], v[40:43]\n"
      "ds_read_b64_tr_b16 v[238:239], v201 offset:608\n"
      "v_permlane16_swap_b32 v194, v195\n"
      "v_mfma_f32_16x16x32_bf16 v[44:47], v[172:175], a[108:111], v[44:47]\n"
      "v_add_f32 v192, v192, v193\n"
      "v_add_f32 v194, v194, v195\n"
      "v_mfma_f32_16x16x32_bf16 v[48:51], v[172:175], a[112:115], v[48:51]\n"
      "v_cvt_pk_bf16_f32 v192, v192, v194\n"
      "v_and_b32 v194, 0xffff0000, v192\n"
      "v_mfma_f32_16x16x32_bf16 v[52:55], v[172:175], a[116:119], v[52:55]\n"
      "v_lshlrev_b32 v192, 16, v192\n"
      "v_mul_f32 v193, 0xbfb8aa3b, v192\n"
      "v_mfma_f32_16x16x32_bf16 v[56:59], v[172:175], a[120:123], v[56:59]\n"
      "v_exp_f32 v193, v193\n"
      "s_nop 0\n"
      "v_add_f32 v193, 1.0, v193\n"
      "v_mfma_f32_16x16x32_bf16 v[60:63], v[172:175], a[124:127], v[60:63]\n"
      "global_load_dwordx4 v[172:175], v241, s[72:73] offset:3072 sc1\n"
      "v_rcp_f32 v193, v193\n"
      "v_mfma_f32_16x16x32_bf16 v[32:35], v[176:179], a[128:131], v[32:35]\n"
      "v_mul_f32 v192, v192, v193\n"
      "v_cvt_pk_bf16_f32 v192, v192, v192\n"
      "v_mfma_f32_16x16x32_bf16 v[36:39], v[176:179], a[132:135], v[36:39]\n"
      "v_and_b32 v192, 0xffff0000, v192\n"
      "v_mul_f32 v192, v192, v194\n"
      "v_mfma_f32_16x16x32_bf16 v[40:43], v[176:179], a[136:139], v[40:43]\n"
      "v_cvt_pk_bf16_f32 v192, v192, v192\n"
      "v_mov_b32 v111, s37\n"
      "v_mfma_f32_16x16x32_bf16 v[44:47], v[176:179], a[140:143], v[44:47]\n"
      "ds_write_b16 v202, v192\n"
      "v_cvt_pk_bf16_f32 v10, v12, v13\n"
      "v_mfma_f32_16x16x32_bf16 v[48:51], v[176:179], a[144:147], v[48:51]\n"
      "ds_read_b128 v[192:195], v199\n"
      "v_cvt_pk_bf16_f32 v104, v96, v97\n"
      "v_mfma_f32_16x16x32_bf16 v[52:55], v[176:179], a[148:151], v[52:55]\n"
      "ds_write_addtid_b32 v9 offset:256\n"
      "v_cvt_pk_bf16_f32 v105, v98, v99\n"
      "v_mfma_f32_16x16x32_bf16 v[56:59], v[176:179], a[152:155], v[56:59]\n"
      "v_cvt_pk_bf16_f32 v106, v100, v101\n"
      "v_cvt_pk_bf16_f32 v109, v102, v103\n"
      "v_mfma_f32_16x16x32_bf16 v[60:63], v[176:179], a[156:159], v[60:63]\n"
      "global_load_dwordx4 v[176:179], v240, s[14:15] offset:3072 sc1\n"
      "v_cvt_pk_bf16_f32 v11, v14, v15\n"
      "v_mfma_f32_16x16x32_bf16 v[32:35], v[180:183], a[160:163], v[32:35]\n"
      "global_store_dwordx4 v242, v[104:107], s[52:53] offset:1024 sc1\n"
      "v_mfma_f32_16x16x32_bf16 v[36:39], v[180:183], a[164:167], v[36:39]\n"
      "global_store_dwordx4 v242, v[108:111], s[52:53] offset:2048 sc1\n"
      "s_add_u32 s48, s34, 3\n"
      "v_mfma_f32_16x16x32_bf16 v[40:43], v[180:183], a[168:171], v[40:43]\n"
      "s_and_b32 s48, s48, 3\n"
      "s_waitcnt lgkmcnt(1)\n"
      "v_mfma_f32_16x16x32_bf16 v[196:199], v[192:195], v[224:227], 0\n"
      "v_mfma_f32_16x16x32_bf16 v[44:47], v[180:183], a[172:175], v[44:47]\n"
      "s_mul_i32 s48, s48, 36864\n"
      "v_mfma_f32_16x16x32_bf16 v[224:227], v[192:195], v[228:231], 0\n"
      "s_add_u32 s48, s48, 24576\n"
      "v_mfma_f32_16x16x32_bf16 v[48:51], v[180:183], a[176:179], v[48:51]\n"
      "s_add_u32 s52, s60, s48\n"
      "s_addc_u32 s53, s61, 0\n"
      "v_mfma_f32_16x16x32_bf16 v[228:231], v[192:195], v[232:235], 0\n"
      "v_mfma_f32_16x16x32_bf16 v[52:55], v[180:183], a[180:183], v[52:55]\n"
      "v_mfma_f32_16x16x32_bf16 v[232:235], v[192:195], v[236:239], 0\n"
      "ds_write_addtid_b32 v10 offset:512\n"
      "v_mov_b32 v193, s37\n"
      "v_mfma_f32_16x16x32_bf16 v[56:59], v[180:183], a[184:187], v[56:59]\n"
      "v_mov_b32 v195, s37\n"
      "v_cvt_pk_bf16_f32 v12, v16, v17\n"
      "v_mfma_f32_16x16x32_bf16 v[60:63], v[180:183], a[188:191], v[60:63]\n"
      "global_load_dwordx4 v[180:183], v240, s[74:75] offset:3072 sc1\n"
      "ds_write_addtid_b32 v11 offset:768\n"
      "v_cvt_pk_bf16_f32 v13, v18, v19\n"
      "v_mfma_f32_16x16x32_bf16 v[40:43], v[184:187], a[200:203], v[40:43]\n"
      "ds_write_addtid_b32 v12 offset:1024\n"
      "v_cndmask_b32 v192, v196, v228, s[64:65]\n"
      "v_cndmask_b32 v194, v224, v232, s[64:65]\n"
      "v_mfma_f32_16x16x32_bf16 v[44:47], v[184:187], a[204:207], v[44:47]\n"
      "v_and_b32 v192, s86, v192\n"
      "v_and_b32 v194, s86, v194\n"
      "v_cvt_pk_bf16_f32 v14, v20, v21\n"
      "v_mfma_f32_16x16x32_bf16 v[48:51], v[184:187], a[208:211], v[48:51]\n"
      "s_mov_b64 exec, s[66:67]\n"
      "global_store_dwordx4 v203, v[192:195], s[58:59] sc1\n"
      "s_mov_b64 exec, -1\n"
      "v_mfma_f32_16x16x32_bf16 v[52:55], v[184:187], a[212:215], v[52:55]\n"
      "ds_write_addtid_b32 v13 offset:1280\n"
      "v_cvt_pk_bf16_f32 v15, v22, v23\n"
      "v_mfma_f32_16x16x32_bf16 v[56:59], v[184:187], a[216:219], v[56:59]\n"
      "ds_write_addtid_b32 v14 offset:1536\n"
      "v_cvt_pk_bf16_f32 v16, v24, v25\n"
      "v_mfma_f32_16x16x32_bf16 v[60:63], v[184:187], a[220:223], v[60:63]\n"
      "ds_write_addtid_b32 v15 offset:1792\n"
      "v_cvt_pk_bf16_f32 v17, v26, v27\n"
      "v_mfma_f32_16x16x32_bf16 v[40:43], v[188:191], a[232:235], v[40:43]\n"
      "ds_write_addtid_b32 v16 offset:2048\n"
      "v_cvt_pk_bf16_f32 v18, v28, v29\n"
      "v_mfma_f32_16x16x32_bf16 v[44:47], v[188:191], a[236:239], v[44:47]\n"
      "ds_write_addtid_b32 v17 offset:2304\n"
      "v_cvt_pk_bf16_f32 v19, v30, v31\n"
      "v_mfma_f32_16x16x32_bf16 v[48:51], v[188:191], a[240:243], v[48:51]\n"
      "ds_write_addtid_b32 v18 offset:2560\n"
      "ds_write_addtid_b32 v19 offset:2816\n"
      "v_mfma_f32_16x16x32_bf16 v[52:55], v[188:191], a[244:247], v[52:55]\n"
      "v_mfma_f32_16x16x32_bf16 v[56:59], v[188:191], a[248:251], v[56:59]\n"
      "v_mfma_f32_16x16x32_bf16 v[60:63], v[188:191], a[252:255], v[60:63]\n"
      "s_waitcnt lgkmcnt(0)\n"
      "s_barrier\n"
      "v_mfma_f32_16x16x32_bf16 v[32:35], v[184:187], a[192:195], v[32:35]\n"
      "ds_read2st64_b32 v[20:21], v244 offset0:0 offset1:1\n"
      "ds_read2st64_b32 v[22:23], v245 offset0:4 offset1:5\n"
      "v_mfma_f32_16x16x32_bf16 v[36:39], v[184:187], a[196:199], v[36:39]\n"
      "global_load_dwordx4 v[184:187], v241, s[14:15] offset:3072 sc1\n"
      "ds_read2st64_b32 v[24:25], v244 offset0:2 offset1:3\n"
      "v_mfma_f32_16x16x32_bf16 v[32:35], v[188:191], a[224:227], v[32:35]\n"
      "ds_read2st64_b32 v[26:27], v245 offset0:6 offset1:7\n"
      "ds_read2st64_b32 v[28:29], v246 offset0:8 offset1:9\n"
      "v_mfma_f32_16x16x32_bf16 v[36:39], v[188:191], a[228:231], v[36:39]\n"
      "ds_read2st64_b32 v[30:31], v246 offset0:10 offset1:11\n"
      "v_cvt_pk_bf16_f32 v40, v40, v41\n"
      "v_cvt_pk_bf16_f32 v41, v42, v43\n"
      "v_mfma_f32_16x16x32_bf16 v[72:75], v[128:131], a[8:11], 0\n"
      "ds_write_addtid_b32 v40 offset:3072\n"
      "v_cvt_pk_bf16_f32 v42, v44, v45\n"
      "v_mfma_f32_16x16x32_bf16 v[76:79], v[128:131], a[12:15], 0\n"
      "global_load_dwordx4 v[188:191], v241, s[74:75] offset:3072 sc1\n"
      "ds_write_addtid_b32 v41 offset:3328\n"
      "v_cvt_pk_bf16_f32 v43, v46, v47\n"
      "v_mfma_f32_16x16x32_bf16 v[80:83], v[128:131], a[16:19], 0\n"
      "ds_write_addtid_b32 v42 offset:3584\n"
      "v_cvt_pk_bf16_f32 v44, v48, v49\n"
      "v_mfma_f32_16x16x32_bf16 v[84:87], v[128:131], a[20:23], 0\n"
      "s_waitcnt lgkmcnt(3)\n"
      "v_mfma_f32_16x16x32_bf16 v[0:3], v[250:253], v[20:23], v[0:3]\n"
      "v_mfma_f32_16x16x32_bf16 v[4:7], v[250:253], v[24:27], v[4:7]\n"
      "v_mfma_f32_16x16x32_bf16 v[88:91], v[128:131], a[24:27], 0\n"
      "v_mfma_f32_16x16x32_bf16 v[0:3], v[252:255], v[28:31], v[0:3]\n"
      "v_mfma_f32_16x16x32_bf16 v[4:7], v[248:251], v[28:31], v[4:7]\n"
      "v_mov_b32 v11, s37\n"
      "v_mfma_f32_16x16x32_bf16 v[92:95], v[128:131], a[28:31], 0\n"
      "ds_write_addtid_b32 v43 offset:3840\n"
      "v_cvt_pk_bf16_f32 v45, v50, v51\n"
      "v_mfma_f32_16x16x32_bf16 v[72:75], v[132:135], a[40:43], v[72:75]\n"
      "ds_write_addtid_b32 v44 offset:4096\n"
      "s_nop 0\n"
      "v_cvt_pk_bf16_f32 v8, v0, v1\n"
      "v_mfma_f32_16x16x32_bf16 v[76:79], v[132:135], a[44:47], v[76:79]\n"
      "v_cvt_pk_bf16_f32 v9, v2, v3\n"
      "v_cvt_pk_bf16_f32 v10, v4, v5\n"
      "v_cvt_pk_bf16_f32 v247, v6, v7\n"
      "v_mfma_f32_16x16x32_bf16 v[80:83], v[132:135], a[48:51], v[80:83]\n"
      "global_store_dwordx4 v242, v[8:11], s[52:53] offset:0 sc1\n"
      "v_cvt_pk_bf16_f32 v46, v52, v53\n"
      "s_lshl2_add_u32 s54, s36, s33\n"
      "v_mfma_f32_16x16x32_bf16 v[84:87], v[132:135], a[52:55], v[84:87]\n"
      "s_lshl_b32 s55, s54, 15\n"
      "ds_write_addtid_b32 v45 offset:4352\n"
      "v_cvt_pk_bf16_f32 v47, v54, v55\n"
      "s_lshl2_add_u32 s56, s35, s34\n"
      "v_mfma_f32_16x16x32_bf16 v[88:91], v[132:135], a[56:59], v[88:91]\n"
      "s_lshl_b32 s56, s56, 6\n"
      "ds_write_addtid_b32 v46 offset:4608\n"
      "v_cvt_pk_bf16_f32 v48, v56, v57\n"
      "s_add_u32 s55, s55, s56\n"
      "v_mfma_f32_16x16x32_bf16 v[92:95], v[132:135], a[60:63], v[92:95]\n"
      "ds_write_addtid_b32 v47 offset:4864\n"
      "v_cvt_pk_bf16_f32 v49, v58, v59\n"
      "v_and_b32 v8, 15, v243\n"
      "v_mfma_f32_16x16x32_bf16 v[72:75], v[136:139], a[72:75], v[72:75]\n"
      "ds_write_addtid_b32 v48 offset:5120\n"
      "v_cvt_pk_bf16_f32 v50, v60, v61\n"
      "v_and_b32 v7, 48, v243\n"
      "v_mfma_f32_16x16x32_bf16 v[76:79], v[136:139], a[76:79], v[76:79]\n"
      "ds_write_addtid_b32 v49 offset:5376\n"
      "v_cvt_pk_bf16_f32 v51, v62, v63\n"
      "v_lshl_or_b32 v7, v8, 10, v7\n"
      "v_mfma_f32_16x16x32_bf16 v[80:83], v[136:139], a[80:83], v[80:83]\n"
      "ds_write_addtid_b32 v50 offset:5632\n"
      "ds_write_addtid_b32 v51 offset:5888\n"
      "v_add_u32 v7, s55, v7\n"
      "v_mfma_f32_16x16x32_bf16 v[84:87], v[136:139], a[84:87], v[84:87]\n"
      "v_mfma_f32_16x16x32_bf16 v[88:91], v[136:139], a[88:91], v[88:91]\n"
      "v_add_u32 v4, 0, v242\n"
      "v_mfma_f32_16x16x32_bf16 v[92:95], v[136:139], a[92:95], v[92:95]\n"
      "global_load_dwordx4 v[12:15], v4, s[62:63] offset:0 sc1\n"
      "global_load_dwordx4 v[16:19], v4, s[62:63] offset:1024 sc1\n"
      "v_mfma_f32_16x16x32_bf16 v[72:75], v[140:143], a[104:107], v[72:75]\n"
      "global_load_dwordx4 v[20:23], v4, s[62:63] offset:2048 sc1\n"
      "s_waitcnt lgkmcnt(0)\n"
      "s_barrier\n"
      "v_mfma_f32_16x16x32_bf16 v[76:79], v[140:143], a[108:111], v[76:79]\n"
      "ds_read2st64_b32 v[52:53], v244 offset0:12 offset1:13\n"
      "ds_read2st64_b32 v[54:55], v245 offset0:16 offset1:17\n"
      "v_mfma_f32_16x16x32_bf16 v[80:83], v[140:143], a[112:115], v[80:83]\n"
      "ds_read2st64_b32 v[56:57], v244 offset0:14 offset1:15\n"
      "ds_read2st64_b32 v[58:59], v245 offset0:18 offset1:19\n"
      "v_mfma_f32_16x16x32_bf16 v[84:87], v[140:143], a[116:119], v[84:87]\n"
      "ds_read2st64_b32 v[60:61], v246 offset0:20 offset1:21\n"
      "ds_read2st64_b32 v[62:63], v246 offset0:22 offset1:23\n"
      "v_mfma_f32_16x16x32_bf16 v[88:91], v[140:143], a[120:123], v[88:91]\n"
      "v_mfma_f32_16x16x32_bf16 v[92:95], v[140:143], a[124:127], v[92:95]\n"
      "s_waitcnt vmcnt(18)\n"
      "v_mfma_f32_16x16x32_bf16 v[72:75], v[144:147], a[136:139], v[72:75]\n"
      "v_mfma_f32_16x16x32_bf16 v[76:79], v[144:147], a[140:143], v[76:79]\n"
      "s_waitcnt lgkmcnt(0)\n"
      "v_mfma_f32_16x16x32_bf16 v[32:35], v[250:253], v[52:55], v[32:35]\n"
      "v_mfma_f32_16x16x32_bf16 v[36:39], v[250:253], v[56:59], v[36:39]\n"
      "v_mfma_f32_16x16x32_bf16 v[80:83], v[144:147], a[144:147], v[80:83]\n"
      "v_mfma_f32_16x16x32_bf16 v[32:35], v[252:255], v[60:63], v[32:35]\n"
      "v_mfma_f32_16x16x32_bf16 v[84:87], v[144:147], a[148:151], v[84:87]\n"
      "v_mfma_f32_16x16x32_bf16 v[36:39], v[248:251], v[60:63], v[36:39]\n"
      "v_mov_b32 v43, s37\n"
      "v_mfma_f32_16x16x32_bf16 v[88:91], v[144:147], a[152:155], v[88:91]\n"
      "v_mov_b32 v44, v247\n"
      "v_mov_b32 v47, s37\n"
      "v_mfma_f32_16x16x32_bf16 v[92:95], v[144:147], a[156:159], v[92:95]\n"
      "v_mfma_f32_16x16x32_bf16 v[72:75], v[148:151], a[168:171], v[72:75]\n"
      "v_cvt_pk_bf16_f32 v40, v32, v33\n"
      "v_cvt_pk_bf16_f32 v41, v34, v35\n"
      "v_mfma_f32_16x16x32_bf16 v[76:79], v[148:151], a[172:175], v[76:79]\n"
      "v_cvt_pk_bf16_f32 v42, v36, v37\n"
      "v_cvt_pk_bf16_f32 v45, v38, v39\n"
      "v_mfma_f32_16x16x32_bf16 v[80:83], v[148:151], a[176:179], v[80:83]\n"
      "global_store_dwordx4 v242, v[40:43], s[52:53] offset:1024 sc1\n"
      "v_mfma_f32_16x16x32_bf16 v[84:87], v[148:151], a[180:183], v[84:87]\n"
      "global_store_dwordx4 v242, v[44:47], s[52:53] offset:2048 sc1\n"
      "v_mfma_f32_16x16x32_bf16 v[88:91], v[148:151], a[184:187], v[88:91]\n"
      "v_mfma_f32_16x16x32_bf16 v[92:95], v[148:151], a[188:191], v[92:95]\n"
      "s_waitcnt vmcnt(18)\n"
      "v_mfma_f32_16x16x32_bf16 v[72:75], v[152:155], a[200:203], v[72:75]\n"
      "v_mfma_f32_16x16x32_bf16 v[76:79], v[152:155], a[204:207], v[76:79]\n"
      "v_mfma_f32_16x16x32_bf16 v[80:83], v[152:155], a[208:211], v[80:83]\n"
      "v_mfma_f32_16x16x32_bf16 v[84:87], v[152:155], a[212:215], v[84:87]\n"
      "v_mfma_f32_16x16x32_bf16 v[88:91], v[152:155], a[216:219], v[88:91]\n"
      "v_mfma_f32_16x16x32_bf16 v[92:95], v[152:155], a[220:223], v[92:95]\n"
      "v_mfma_f32_16x16x32_bf16 v[72:75], v[156:159], a[232:235], v[72:75]\n"
      "v_mfma_f32_16x16x32_bf16 v[76:79], v[156:159], a[236:239], v[76:79]\n"
      "v_mfma_f32_16x16x32_bf16 v[80:83], v[156:159], a[240:243], v[80:83]\n"
      "v_mfma_f32_16x16x32_bf16 v[84:87], v[156:159], a[244:247], v[84:87]\n"
      "v_mfma_f32_16x16x32_bf16 v[88:91], v[156:159], a[248:251], v[88:91]\n"
      "s_nop 3\n"
      "v_cvt_pk_bf16_f32 v72, v72, v73\n"
      "v_mfma_f32_16x16x32_bf16 v[92:95], v[156:159], a[252:255], v[92:95]\n"
      "s_waitcnt vmcnt(15)\n"
      "v_cvt_pk_bf16_f32 v73, v74, v75\n"
      "v_mfma_f32_16x16x32_bf16 v[104:107], v[160:163], a[8:11], 0\n"
      "ds_write_addtid_b32 v72 offset:6144\n"
      "v_cvt_pk_bf16_f32 v74, v76, v77\n"
      "v_mfma_f32_16x16x32_bf16 v[108:111], v[160:163], a[12:15], 0\n"
      "ds_write_addtid_b32 v73 offset:6400\n"
      "v_cvt_pk_bf16_f32 v75, v78, v79\n"
      "v_mfma_f32_16x16x32_bf16 v[112:115], v[160:163], a[16:19], 0\n"
      "ds_write_addtid_b32 v74 offset:6656\n"
      "v_cvt_pk_bf16_f32 v76, v80, v81\n"
      "v_mfma_f32_16x16x32_bf16 v[116:119], v[160:163], a[20:23], 0\n"
      "ds_write_addtid_b32 v75 offset:6912\n"
      "v_cvt_pk_bf16_f32 v77, v82, v83\n"
      "v_mfma_f32_16x16x32_bf16 v[120:123], v[160:163], a[24:27], 0\n"
      "ds_write_addtid_b32 v76 offset:7168\n"
      "v_cvt_pk_bf16_f32 v78, v84, v85\n"
      "v_mfma_f32_16x16x32_bf16 v[124:127], v[160:163], a[28:31], 0\n"
      "ds_write_addtid_b32 v77 offset:7424\n"
      "v_cvt_pk_bf16_f32 v79, v86, v87\n"
      "v_mfma_f32_16x16x32_bf16 v[104:107], v[164:167], a[40:43], v[104:107]\n"
      "ds_write_addtid_b32 v78 offset:7680\n"
      "v_cvt_pk_bf16_f32 v80, v88, v89\n"
      "v_mfma_f32_16x16x32_bf16 v[108:111], v[164:167], a[44:47], v[108:111]\n"
      "ds_write_addtid_b32 v79 offset:7936\n"
      "v_cvt_pk_bf16_f32 v81, v90, v91\n"
      "v_mfma_f32_16x16x32_bf16 v[112:115], v[164:167], a[48:51], v[112:115]\n"
      "ds_write_addtid_b32 v80 offset:8192\n"
      "v_cvt_pk_bf16_f32 v82, v92, v93\n"
      "v_mfma_f32_16x16x32_bf16 v[116:119], v[164:167], a[52:55], v[116:119]\n"
      "ds_write_addtid_b32 v81 offset:8448\n"
      "v_cvt_pk_bf16_f32 v83, v94, v95\n"
      "v_mfma_f32_16x16x32_bf16 v[120:123], v[164:167], a[56:59], v[120:123]\n"
      "ds_write_addtid_b32 v82 offset:8704\n"
      "v_mfma_f32_16x16x32_bf16 v[124:127], v[164:167], a[60:63], v[124:127]\n"
      "s_waitcnt vmcnt(13)\n"
      "ds_write_addtid_b32 v83 offset:8960\n"
      "v_mfma_f32_16x16x32_bf16 v[104:107], v[168:171], a[72:75], v[104:107]\n"
      "v_mfma_f32_16x16x32_bf16 v[108:111], v[168:171], a[76:79], v[108:111]\n"
      "v_mfma_f32_16x16x32_bf16 v[112:115], v[168:171], a[80:83], v[112:115]\n"
      "v_mfma_f32_16x16x32_bf16 v[116:119], v[168:171], a[84:87], v[116:119]\n"
      "v_mfma_f32_16x16x32_bf16 v[120:123], v[168:171], a[88:91], v[120:123]\n"
      "v_mfma_f32_16x16x32_bf16 v[124:127], v[168:171], a[92:95], v[124:127]\n"
      "v_mfma_f32_16x16x32_bf16 v[104:107], v[172:175], a[104:107], v[104:107]\n"
      "s_lshr_b32 s43, s42, 7\n"
      "s_cmp_eq_u32 s43, s36\n"
      "s_cbranch_scc0 .Lp_e16_%=\n"
      "s_lshl2_add_u32 s44, s35, s34\n"
      "s_lshl2_add_u32 s43, s44, s33\n"
      "s_lshl_b32 s43, s43, 12\n"
      "s_add_u32 s43, s43, 524288\n"
      "v_add_u32 v60, s43, v242\n"
      "v_mov_b32 v62, 0\n"
      "global_load_dwordx4 v[24:27], v60, s[58:59] offset:0 sc1\n"
      "global_load_dwordx4 v[28:31], v60, s[58:59] offset:1024 sc1\n"
      "global_load_dwordx4 v[32:35], v60, s[58:59] offset:2048 sc1\n"
      "global_load_dwordx4 v[36:39], v60, s[58:59] offset:3072 sc1\n"
      "global_load_dwordx4 v[40:43], v62, s[58:59] offset:1024 sc1\n"
      "v_and_b32 v58, 15, v243\n"
      "v_lshrrev_b32 v59, 5, v243\n"
      "v_lshl_or_b32 v58, v59, 4, v58\n"
      "s_lshl4_add_u32 s44, s42, s44\n"
      "s_lshl_b32 s44, s44, 6\n"
      "v_lshl_add_u32 v59, v58, 1, s44\n"
      "global_load_ushort v57, v59, s[16:17]\n"
      "s_mov_b32 s44, 131072\n"
      "v_lshl_add_u32 v58, v58, 2, s44\n"
      "s_lshl_b32 s43, s33, 13\n"
      "v_add_u32 v59, s43, v58\n"
      ".Lp_e16_%=:\n"
      "v_mfma_f32_16x16x32_bf16 v[108:111], v[172:175], a[108:111], v[108:111]\n"
      "v_mfma_f32_16x16x32_bf16 v[112:115], v[172:175], a[112:115], v[112:115]\n"
      "v_mfma_f32_16x16x32_bf16 v[116:119], v[172:175], a[116:119], v[116:119]\n"
      "v_mfma_f32_16x16x32_bf16 v[120:123], v[172:175], a[120:123], v[120:123]\n"
      "v_mfma_f32_16x16x32_bf16 v[124:127], v[172:175], a[124:127], v[124:127]\n"
      "s_waitcnt vmcnt(9)\n"
      "v_mfma_f32_16x16x32_bf16 v[104:107], v[176:179], a[136:139], v[104:107]\n"
      "v_mfma_f32_16x16x32_bf16 v[108:111], v[176:179], a[140:143], v[108:111]\n"
      "v_mfma_f32_16x16x32_bf16 v[112:115], v[176:179], a[144:147], v[112:115]\n"
      "v_mfma_f32_16x16x32_bf16 v[116:119], v[176:179], a[148:151], v[116:119]\n"
      "v_mfma_f32_16x16x32_bf16 v[120:123], v[176:179], a[152:155], v[120:123]\n"
      "v_mfma_f32_16x16x32_bf16 v[124:127], v[176:179], a[156:159], v[124:127]\n"
      "v_mfma_f32_16x16x32_bf16 v[104:107], v[180:183], a[168:171], v[104:107]\n"
      "v_mfma_f32_16x16x32_bf16 v[108:111], v[180:183], a[172:175], v[108:111]\n"
      "v_mfma_f32_16x16x32_bf16 v[112:115], v[180:183], a[176:179], v[112:115]\n"
      "v_mfma_f32_16x16x32_bf16 v[116:119], v[180:183], a[180:183], v[116:119]\n"
      "v_add_u32 v5, 12288, v242\n"
      "v_mfma_f32_16x16x32_bf16 v[120:123], v[180:183], a[184:187], v[120:123]\n"
      "global_load_dwordx4 v[44:47], v5, s[62:63] offset:0 sc1\n"
      "v_mfma_f32_16x16x32_bf16 v[124:127], v[180:183], a[188:191], v[124:127]\n"
      "s_waitcnt vmcnt(7)\n"
      "global_load_dwordx4 v[48:51], v5, s[62:63] offset:1024 sc1\n"
      "v_mfma_f32_16x16x32_bf16 v[104:107], v[184:187], a[200:203], v[104:107]\n"
      "global_load_dwordx4 v[52:55], v5, s[62:63] offset:2048 sc1\n"
      "v_mfma_f32_16x16x32_bf16 v[108:111], v[184:187], a[204:207], v[108:111]\n"
      "v_mfma_f32_16x16x32_bf16 v[112:115], v[184:187], a[208:211], v[112:115]\n"
      "v_mfma_f32_16x16x32_bf16 v[116:119], v[184:187], a[212:215], v[116:119]\n"
      "v_mfma_f32_16x16x32_bf16 v[120:123], v[184:187], a[216:219], v[120:123]\n"
      "v_mfma_f32_16x16x32_bf16 v[124:127], v[184:187], a[220:223], v[124:127]\n"
      "v_mfma_f32_16x16x32_bf16 v[104:107], v[188:191], a[232:235], v[104:107]\n"
      "v_mfma_f32_16x16x32_bf16 v[108:111], v[188:191], a[236:239], v[108:111]\n"
      "v_mfma_f32_16x16x32_bf16 v[112:115], v[188:191], a[240:243], v[112:115]\n"
      "v_mfma_f32_16x16x32_bf16 v[116:119], v[188:191], a[244:247], v[116:119]\n"
      "s_waitcnt vmcnt(5)\n"
      "v_bitop3_b32 v22, v15, v19, s37 bitop3:0x7e\n"
      "v_bitop3_b32 v22, v22, v23, s37 bitop3:0xf6\n"
      "v_cmp_ne_u32 vcc, 0, v22\n"
      "s_nop 1\n"
      "s_cbranch_vccnz .Lp_s18_%=\n"
      ".Lp_o19_%=:\n"
      "v_mfma_f32_16x16x32_bf16 v[120:123], v[188:191], a[248:251], v[120:123]\n"
      "v_cvt_pk_bf16_f32 v104, v104, v105\n"
      "v_cvt_pk_bf16_f32 v105, v106, v107\n"
      "v_mfma_f32_16x16x32_bf16 v[124:127], v[188:191], a[252:255], v[124:127]\n"
      "ds_write_addtid_b32 v104 offset:9216\n"
      "v_cvt_pk_bf16_f32 v106, v108, v109\n"
      "v_mfma_f32_16x16x32_bf16 v[64:67], v[128:131], a[0:3], 0\n"
      "ds_write_addtid_b32 v105 offset:9472\n"
      "v_cvt_pk_bf16_f32 v107, v110, v111\n"
      "v_mfma_f32_16x16x32_bf16 v[68:71], v[128:131], a[4:7], 0\n"
      "ds_write_addtid_b32 v106 offset:9728\n"
      "v_cvt_pk_bf16_f32 v108, v112, v113\n"
      "v_mfma_f32_16x16x32_bf16 v[64:67], v[132:135], a[32:35], v[64:67]\n"
      "ds_write_addtid_b32 v107 offset:9984\n"
      "v_cvt_pk_bf16_f32 v109, v114, v115\n"
      "v_mfma_f32_16x16x32_bf16 v[68:71], v[132:135], a[36:39], v[68:71]\n"
      "ds_write_addtid_b32 v108 offset:10240\n"
      "v_cvt_pk_bf16_f32 v110, v116, v117\n"
      "v_mfma_f32_16x16x32_bf16 v[64:67], v[136:139], a[64:67], v[64:67]\n"
      "ds_write_addtid_b32 v109 offset:10496\n"
      "v_cvt_pk_bf16_f32 v111, v118, v119\n"
      "v_mfma_f32_16x16x32_bf16 v[68:71], v[136:139], a[68:71], v[68:71]\n"
      "ds_write_addtid_b32 v110 offset:10752\n"
      "v_cvt_pk_bf16_f32 v112, v120, v121\n"
      "v_mfma_f32_16x16x32_bf16 v[64:67], v[140:143], a[96:99], v[64:67]\n"
      "ds_write_addtid_b32 v111 offset:11008\n"
      "v_cvt_pk_bf16_f32 v113, v122, v123\n"
      "v_mfma_f32_16x16x32_bf16 v[68:71], v[140:143], a[100:103], v[68:71]\n"
      "ds_write_addtid_b32 v112 offset:11264\n"
      "v_cvt_pk_bf16_f32 v114, v124, v125\n"
      "v_mfma_f32_16x16x32_bf16 v[64:67], v[144:147], a[128:131], v[64:67]\n"
      "ds_write_addtid_b32 v113 offset:11520\n"
      "v_cvt_pk_bf16_f32 v115, v126, v127\n"
      "v_mfma_f32_16x16x32_bf16 v[68:71], v[144:147], a[132:135], v[68:71]\n"
      "ds_write_addtid_b32 v114 offset:11776\n"
      "v_mov_b32 v15, v20\n"
      "v_mfma_f32_16x16x32_bf16 v[64:67], v[148:151], a[160:163], v[64:67]\n"
      "ds_write_addtid_b32 v115 offset:12032\n"
      "v_add_u32 v6, 24576, v242\n"
      "v_mfma_f32_16x16x32_bf16 v[68:71], v[148:151], a[164:167], v[68:71]\n"
      "global_load_dwordx4 v[72:75], v6, s[62:63] offset:0 sc1\n"
      "v_mov_b32 v19, v21\n"
      "v_mfma_f32_16x16x32_bf16 v[64:67], v[152:155], a[192:195], v[64:67]\n"
      "global_load_dwordx4 v[76:79], v6, s[62:63] offset:1024 sc1\n"
      "v_mfma_f32_16x16x32_bf16 v[68:71], v[152:155], a[196:199], v[68:71]\n"
      "global_load_dwordx4 v[80:83], v6, s[62:63] offset:2048 sc1\n"
      "v_mfma_f32_16x16x32_bf16 v[64:67], v[156:159], a[224:227], v[64:67]\n"
      "s_lshr_b32 s43, s42, 7\n"
      "s_cmp_eq_u32 s43, s36\n"
      "s_cbranch_scc0 .Lp_e20_%=\n"
      "s_waitcnt vmcnt(6)\n"
      "v_bitop3_b32 v61, v25, v27, s37 bitop3:0x7e\n"
      "v_bitop3_b32 v61, v61, v29, s37 bitop3:0xf6\n"
      "v_bitop3_b32 v61, v61, v31, s37 bitop3:0xf6\n"
      "v_bitop3_b32 v61, v61, v33, s37 bitop3:0xf6\n"
      "v_bitop3_b32 v61, v61, v35, s37 bitop3:0xf6\n"
      "v_bitop3_b32 v61, v61, v37, s37 bitop3:0xf6\n"
      "v_bitop3_b32 v61, v61, v39, s37 bitop3:0xf6\n"
      "v_bitop3_b32 v61, v61, v43, s37 bitop3:0xf6\n"
      "v_cmp_ne_u32 vcc, 0, v61\n"
      "s_nop 1\n"
      "s_cbranch_vccnz .Lp_s22_%=\n"
      ".Lp_o23_%=:\n"
      "v_add_f32 v24, v24, v28\n"
      "v_add_f32 v26, v26, v30\n"
      "v_add_f32 v32, v32, v36\n"
      "v_add_f32 v34, v34, v38\n"
      "v_add_f32 v24, v24, v32\n"
      "v_add_f32 v26, v26, v34\n"
      "s_nop 1\n"
      "v_permlane32_swap_b32 v24, v26\n"
      "v_add_f32 v24, v24, v26\n"
      "v_mov_b32 v26, v24\n"
      "s_nop 1\n"
      "v_permlane16_swap_b32 v24, v26\n"
      "v_add_f32 v24, v24, v26\n"
      "ds_write_b32 v59, v24\n"
      "s_waitcnt lgkmcnt(0)\n"
      ".Lp_e20_%=:\n"
      "v_mfma_f32_16x16x32_bf16 v[68:71], v[156:159], a[228:231], v[68:71]\n"
      "s_waitcnt lgkmcnt(0)\n"
      "s_barrier\n"
      "v_mfma_f32_16x16x32_bf16 v[96:99], v[160:163], a[0:3], 0\n"
      "s_lshr_b32 s43, s42, 5\n"
      "s_lshl2_add_u32 s44, s36, s33\n"
      "s_cmp_eq_u32 s43, s44\n"
      "s_cbranch_scc0 .Lp_e24_%=\n"
      "ds_read_b32 v24, v58 offset:0\n"
      "ds_read_b32 v25, v58 offset:8192\n"
      "ds_read_b32 v26, v58 offset:16384\n"
      "ds_read_b32 v27, v58 offset:24576\n"
      "v_mov_b32 v63, s37\n"
      "s_or_b32 s43, s34, s35\n"
      "s_cmp_eq_u32 s43, 0\n"
      "s_cbranch_scc0 .Lp_z25_%=\n"
      "s_mov_b64 exec, 1\n"
      "global_store_dword v62, v63, s[58:59] sc1\n"
      ".Lp_z25_%=:\n"
      "s_mov_b64 exec, -1\n"
      "s_waitcnt vmcnt(6)\n"
      "v_lshlrev_b32 v57, 16, v57\n"
      "v_and_b32 v56, 48, v243\n"
      "s_mov_b32 s44, 131072\n"
      "v_lshl_add_u32 v56, v56, 1, s44\n"
      "s_waitcnt lgkmcnt(0)\n"
      "v_add_f32 v24, v24, v25\n"
      "s_waitcnt lgkmcnt(0)\n"
      "v_add_f32 v26, v26, v27\n"
      "v_add_f32 v24, v24, v26\n"
      "v_cvt_pk_bf16_f32 v24, v24, v24\n"
      "v_and_b32 v24, 0xffff0000, v24\n"
      "v_mul_f32 v24, v41, v24\n"
      "v_cvt_pk_bf16_f32 v24, v24, v24\n"
      "v_and_b32 v24, 0xffff0000, v24\n"
      "v_add_f32 v24, v24, v57\n"
      "v_cvt_pk_bf16_f32 v24, v24, v24\n"
      "v_and_b32 v24, 0xffff0000, v24\n"
      "v_sub_f32 v42, 1.0, v40\n"
      "v_cvt_pk_bf16_f32 v42, v42, v42\n"
      "v_and_b32 v42, 0xffff0000, v42\n"
      "v_mul_f32 v24, v42, v24\n"
      "ds_write_b32 v58, v24 offset:1024\n"
      "ds_read_b128 v[28:31], v56 offset:1024\n"
      "ds_read_b128 v[32:35], v56 offset:1040\n"
      ".Lp_e24_%=:\n"
      "v_mfma_f32_16x16x32_bf16 v[100:103], v[160:163], a[4:7], 0\n"
      "ds_read2st64_b32 v[84:85], v244 offset0:24 offset1:25\n"
      "v_mfma_f32_16x16x32_bf16 v[96:99], v[164:167], a[32:35], v[96:99]\n"
      "ds_read2st64_b32 v[86:87], v245 offset0:28 offset1:29\n"
      "v_mfma_f32_16x16x32_bf16 v[100:103], v[164:167], a[36:39], v[100:103]\n"
      "ds_read2st64_b32 v[88:89], v244 offset0:26 offset1:27\n"
      "v_mfma_f32_16x16x32_bf16 v[96:99], v[168:171], a[64:67], v[96:99]\n"
      "ds_read2st64_b32 v[90:91], v245 offset0:30 offset1:31\n"
      "v_mfma_f32_16x16x32_bf16 v[100:103], v[168:171], a[68:71], v[100:103]\n"
      "ds_read2st64_b32 v[92:93], v246 offset0:32 offset1:33\n"
      "v_mfma_f32_16x16x32_bf16 v[96:99], v[172:175], a[96:99], v[96:99]\n"
      "ds_read2st64_b32 v[94:95], v246 offset0:34 offset1:35\n"
      "v_mfma_f32_16x16x32_bf16 v[100:103], v[172:175], a[100:103], v[100:103]\n"
      "ds_read2st64_b32 v[116:117], v244 offset0:36 offset1:37\n"
      "v_mfma_f32_16x16x32_bf16 v[96:99], v[176:179], a[128:131], v[96:99]\n"
      "ds_read2st64_b32 v[118:119], v245 offset0:40 offset1:41\n"
      "v_mfma_f32_16x16x32_bf16 v[100:103], v[176:179], a[132:135], v[100:103]\n"
      "ds_read2st64_b32 v[120:121], v244 offset0:38 offset1:39\n"
      "v_mfma_f32_16x16x32_bf16 v[96:99], v[180:183], a[160:163], v[96:99]\n"
      "s_waitcnt lgkmcnt(3)\n"
      "v_mfma_f32_16x16x32_bf16 v[64:67], v[250:253], v[84:87], v[64:67]\n"
      "v_mfma_f32_16x16x32_bf16 v[100:103], v[180:183], a[164:167], v[100:103]\n"
      "v_mfma_f32_16x16x32_bf16 v[68:71], v[250:253], v[88:91], v[68:71]\n"
      "v_mfma_f32_16x16x32_bf16 v[96:99], v[184:187], a[192:195], v[96:99]\n"
      "v_mfma_f32_16x16x32_bf16 v[64:67], v[252:255], v[92:95], v[64:67]\n"
      "v_mfma_f32_16x16x32_bf16 v[100:103], v[184:187], a[196:199], v[100:103]\n"
      "v_mfma_f32_16x16x32_bf16 v[68:71], v[248:251], v[92:95], v[68:71]\n"
      "v_mfma_f32_16x16x32_bf16 v[96:99], v[188:191], a[224:227], v[96:99]\n"
      "ds_read2st64_b32 v[122:123], v245 offset0:42 offset1:43\n"
      "v_mfma_f32_16x16x32_bf16 v[100:103], v[188:191], a[228:231], v[100:103]\n"
      "ds_read2st64_b32 v[124:125], v246 offset0:44 offset1:45\n"
      "ds_read2st64_b32 v[126:127], v246 offset0:46 offset1:47\n"
      "v_mfma_f32_16x16x32_bf16 v[64:67], v[252:255], v[12:15], v[64:67]\n"
      "v_mfma_f32_16x16x32_bf16 v[68:71], v[248:251], v[12:15], v[68:71]\n"
      "v_mfma_f32_16x16x32_bf16 v[96:99], v[252:255], v[16:19], v[96:99]\n"
      "v_mfma_f32_16x16x32_bf16 v[100:103], v[248:251], v[16:19], v[100:103]\n"
      "s_waitcnt vmcnt(3)\n"
      "v_bitop3_b32 v54, v47, v51, s37 bitop3:0x7e\n"
      "v_bitop3_b32 v54, v54, v55, s37 bitop3:0xf6\n"
      "v_cmp_ne_u32 vcc, 0, v54\n"
      "s_nop 1\n"
      "s_cbranch_vccnz .Lp_s27_%=\n"
      ".Lp_o28_%=:\n"
      "v_mov_b32 v47, v52\n"
      "v_mov_b32 v51, v53\n"
      "s_nop 0\n"
      "v_mfma_f32_16x16x32_bf16 v[64:67], v[252:255], v[44:47], v[64:67]\n"
      "v_mfma_f32_16x16x32_bf16 v[68:71], v[248:251], v[44:47], v[68:71]\n"
      "v_mfma_f32_16x16x32_bf16 v[96:99], v[252:255], v[48:51], v[96:99]\n"
      "v_mfma_f32_16x16x32_bf16 v[100:103], v[248:251], v[48:51], v[100:103]\n"
      "s_waitcnt lgkmcnt(0)\n"
      "v_mfma_f32_16x16x32_bf16 v[96:99], v[250:253], v[116:119], v[96:99]\n"
      "v_mfma_f32_16x16x32_bf16 v[100:103], v[250:253], v[120:123], v[100:103]\n"
      "v_mfma_f32_16x16x32_bf16 v[96:99], v[252:255], v[124:127], v[96:99]\n"
      "v_mfma_f32_16x16x32_bf16 v[100:103], v[248:251], v[124:127], v[100:103]\n"
      "s_lshr_b32 s43, s42, 5\n"
      "s_lshl2_add_u32 s44, s36, s33\n"
      "s_cmp_eq_u32 s43, s44\n"
      "s_cbranch_scc0 .Lp_e29_%=\n"
      "v_lshrrev_b32 v24, 10, v7\n"
      "v_subrev_u32 v24, s42, v24\n"
      "v_cmp_eq_u32 vcc, 0, v24\n"
      "s_nop 1\n"
      "s_mov_b64 exec, vcc\n"
      "v_mov_b32 v204, v40\n"
      "v_mov_b32 v208, v28\n"
      "v_mov_b32 v209, v29\n"
      "v_mov_b32 v210, v30\n"
      "v_mov_b32 v211, v31\n"
      "v_mov_b32 v216, v32\n"
      "v_mov_b32 v217, v33\n"
      "v_mov_b32 v218, v34\n"
      "v_mov_b32 v219, v35\n"
      "s_mov_b64 exec, -1\n"
      "v_cmp_eq_u32 vcc, -16, v24\n"
      "s_nop 1\n"
      "s_mov_b64 exec, vcc\n"
      "v_mov_b32 v206, v40\n"
      "v_mov_b32 v212, v28\n"
      "v_mov_b32 v213, v29\n"
      "v_mov_b32 v214, v30\n"
      "v_mov_b32 v215, v31\n"
      "v_mov_b32 v220, v32\n"
      "v_mov_b32 v221, v33\n"
      "v_mov_b32 v222, v34\n"
      "v_mov_b32 v223, v35\n"
      "s_mov_b64 exec, -1\n"
      ".Lp_e29_%=:\n"
      "s_waitcnt vmcnt(0)\n"
      "v_bitop3_b32 v82, v75, v79, s37 bitop3:0x7e\n"
      "v_bitop3_b32 v82, v82, v83, s37 bitop3:0xf6\n"
      "v_cmp_ne_u32 vcc, 0, v82\n"
      "s_nop 1\n"
      "s_cbranch_vccnz .Lp_s31_%=\n"
      ".Lp_o32_%=:\n"
      "v_mov_b32 v75, v80\n"
      "v_mov_b32 v79, v81\n"
      "s_nop 0\n"
      "v_mfma_f32_16x16x32_bf16 v[64:67], v[252:255], v[72:75], v[64:67]\n"
      "v_mfma_f32_16x16x32_bf16 v[96:99], v[252:255], v[76:79], v[96:99]\n"
      "v_mfma_f32_16x16x32_bf16 v[68:71], v[248:251], v[72:75], v[68:71]\n"
      "v_mfma_f32_16x16x32_bf16 v[100:103], v[248:251], v[76:79], v[100:103]\n"
      "s_nop 4\n"
      "v_fma_f32 v208, v204, v64, v208\n"
      "v_fma_f32 v209, v204, v65, v209\n"
      "v_fma_f32 v210, v204, v66, v210\n"
      "v_fma_f32 v211, v204, v67, v211\n"
      "v_fma_f32 v216, v204, v96, v216\n"
      "v_fma_f32 v217, v204, v97, v217\n"
      "v_fma_f32 v218, v204, v98, v218\n"
      "v_fma_f32 v219, v204, v99, v219\n"
      "v_cvt_pk_bf16_f32 v104, v208, v209\n"
      "v_cvt_pk_bf16_f32 v105, v210, v211\n"
      "v_cvt_pk_bf16_f32 v106, v216, v217\n"
      "v_cvt_pk_bf16_f32 v107, v218, v219\n"
      "global_store_dwordx4 v7, v[104:107], s[20:21]\n"
      "v_fma_f32 v212, v206, v68, v212\n"
      "v_fma_f32 v213, v206, v69, v213\n"
      "v_fma_f32 v214, v206, v70, v214\n"
      "v_fma_f32 v215, v206, v71, v215\n"
      "v_fma_f32 v220, v206, v100, v220\n"
      "v_fma_f32 v221, v206, v101, v221\n"
      "v_fma_f32 v222, v206, v102, v222\n"
      "v_fma_f32 v223, v206, v103, v223\n"
      "v_cvt_pk_bf16_f32 v108, v212, v213\n"
      "v_cvt_pk_bf16_f32 v109, v214, v215\n"
      "v_cvt_pk_bf16_f32 v110, v220, v221\n"
      "v_cvt_pk_bf16_f32 v111, v222, v223\n"
      "global_store_dwordx4 v7, v[108:111], s[68:69]\n"
      "s_endpgm\n"
      ".Lp_s2_%=:\n"
      "s_cmp_eq_u32 s38, 0\n"
      "s_cbranch_scc1 .Lp_o3_%=\n"
      "s_sub_u32 s38, s38, 1\n"
      "s_sleep 4\n"
      "s_nop 7\n"
      "global_load_dword v252, v253, s[58:59] offset:0 sc1\n"
      "s_waitcnt vmcnt(0)\n"
      "v_cmp_ne_u32 vcc, s37, v252\n"
      "s_nop 1\n"
      "s_cbranch_vccnz .Lp_s2_%=\n"
      "s_nop 7\n"
      "s_branch .Lp_o3_%=\n"
      ".Lp_s5_%=:\n"
      "s_cmp_eq_u32 s38, 0\n"
      "s_cbranch_scc1 .Lp_o6_%=\n"
      "s_sub_u32 s38, s38, 1\n"
      "s_sleep 4\n"
      "s_nop 7\n"
      "global_load_dword v252, v253, s[58:59] offset:128 sc1\n"
      "s_waitcnt vmcnt(0)\n"
      "v_cmp_ne_u32 vcc, s37, v252\n"
      "s_nop 1\n"
      "s_cbranch_vccnz .Lp_s5_%=\n"
      "s_nop 7\n"
      "s_branch .Lp_o6_%=\n"
      ".Lp_s8_%=:\n"
      "s_cmp_eq_u32 s38, 0\n"
      "s_cbranch_scc1 .Lp_o9_%=\n"
      "s_sub_u32 s38, s38, 1\n"
      "s_sleep 2\n"
      "s_nop 7\n"
      "global_load_dwordx2 v[234:235], v232, s[58:59] sc1\n"
      "s_waitcnt vmcnt(0)\n"
      "v_cmp_ne_u32 vcc, s37, v235\n"
      "s_nop 1\n"
      "s_cbranch_vccnz .Lp_s8_%=\n"
      "s_nop 7\n"
      "s_branch .Lp_o9_%=\n"
      ".Lp_s11_%=:\n"
      "s_cmp_eq_u32 s38, 0\n"
      "s_cbranch_scc1 .Lp_o12_%=\n"
      "s_sub_u32 s38, s38, 1\n"
      "s_sleep 1\n"
      "s_nop 7\n"
      "global_load_dwordx2 v[204:205], v209, s[58:59] offset:0 sc1\n"
      "global_load_dwordx2 v[206:207], v209, s[58:59] offset:256 sc1\n"
      "s_waitcnt vmcnt(0)\n"
      "v_bitop3_b32 v210, v205, v207, s37 bitop3:0x7e\n"
      "v_cmp_ne_u32 vcc, 0, v210\n"
      "s_nop 1\n"
      "s_cbranch_vccnz .Lp_s11_%=\n"
      "s_nop 7\n"
      "s_branch .Lp_o12_%=\n"
      ".Lp_s14_%=:\n"
      "s_cmp_eq_u32 s38, 0\n"
      "s_cbranch_scc1 .Lp_o15_%=\n"
      "s_sub_u32 s38, s38, 1\n"
      "s_sleep 8\n"
      "s_nop 7\n"
      "global_load_dwordx4 v[224:227], v200, s[58:59] offset:0 sc1\n"
      "global_load_dwordx4 v[228:231], v200, s[58:59] offset:1024 sc1\n"
      "global_load_dwordx4 v[232:235], v200, s[58:59] offset:2048 sc1\n"
      "global_load_dwordx4 v[236:239], v200, s[58:59] offset:3072 sc1\n"
      "s_waitcnt vmcnt(0)\n"
      "v_bitop3_b32 v192, v225, v227, s37 bitop3:0x7e\n"
      "v_bitop3_b32 v192, v192, v229, s37 bitop3:0xf6\n"
      "v_bitop3_b32 v192, v192, v231, s37 bitop3:0xf6\n"
      "v_bitop3_b32 v192, v192, v233, s37 bitop3:0xf6\n"
      "v_bitop3_b32 v192, v192, v235, s37 bitop3:0xf6\n"
      "v_bitop3_b32 v192, v192, v237, s37 bitop3:0xf6\n"
      "v_bitop3_b32 v192, v192, v239, s37 bitop3:0xf6\n"
      "v_cmp_ne_u32 vcc, 0, v192\n"
      "s_nop 1\n"
      "s_cbranch_vccnz .Lp_s14_%=\n"
      "s_nop 7\n"
      "s_branch .Lp_o15_%=\n"
      ".Lp_s18_%=:\n"
      "s_cmp_eq_u32 s38, 0\n"
      "s_cbranch_scc1 .Lp_o19_%=\n"
      "s_sub_u32 s38, s38, 1\n"
      "s_nop 7\n"
      "global_load_dwordx4 v[12:15], v4, s[62:63] offset:0 sc1\n"
      "global_load_dwordx4 v[16:19], v4, s[62:63] offset:1024 sc1\n"
      "global_load_dwordx4 v[20:23], v4, s[62:63] offset:2048 sc1\n"
      "s_waitcnt vmcnt(0)\n"
      "v_bitop3_b32 v22, v15, v19, s37 bitop3:0x7e\n"
      "v_bitop3_b32 v22, v22, v23, s37 bitop3:0xf6\n"
      "v_cmp_ne_u32 vcc, 0, v22\n"
      "s_nop 1\n"
      "s_cbranch_vccnz .Lp_s18_%=\n"
      "s_nop 7\n"
      "s_branch .Lp_o19_%=\n"
      ".Lp_s22_%=:\n"
      "s_cmp_eq_u32 s38, 0\n"
      "s_cbranch_scc1 .Lp_o23_%=\n"
      "s_sub_u32 s38, s38, 1\n"
      "s_sleep 2\n"
      "s_nop 7\n"
      "global_load_dwordx4 v[24:27], v60, s[58:59] offset:0 sc1\n"
      "global_load_dwordx4 v[28:31], v60, s[58:59] offset:1024 sc1\n"
      "global_load_dwordx4 v[32:35], v60, s[58:59] offset:2048 sc1\n"
      "global_load_dwordx4 v[36:39], v60, s[58:59] offset:3072 sc1\n"
      "global_load_dwordx4 v[40:43], v62, s[58:59] offset:1024 sc1\n"
      "s_waitcnt vmcnt(0)\n"
      "v_bitop3_b32 v61, v25, v27, s37 bitop3:0x7e\n"
      "v_bitop3_b32 v61, v61, v29, s37 bitop3:0xf6\n"
      "v_bitop3_b32 v61, v61, v31, s37 bitop3:0xf6\n"
      "v_bitop3_b32 v61, v61, v33, s37 bitop3:0xf6\n"
      "v_bitop3_b32 v61, v61, v35, s37 bitop3:0xf6\n"
      "v_bitop3_b32 v61, v61, v37, s37 bitop3:0xf6\n"
      "v_bitop3_b32 v61, v61, v39, s37 bitop3:0xf6\n"
      "v_bitop3_b32 v61, v61, v43, s37 bitop3:0xf6\n"
      "v_cmp_ne_u32 vcc, 0, v61\n"
      "s_nop 1\n"
      "s_cbranch_vccnz .Lp_s22_%=\n"
      "s_nop 7\n"
      "s_branch .Lp_o23_%=\n"
      ".Lp_s27_%=:\n"
      "s_cmp_eq_u32 s38, 0\n"
      "s_cbranch_scc1 .Lp_o28_%=\n"
      "s_sub_u32 s38, s38, 1\n"
      "s_nop 7\n"
      "global_load_dwordx4 v[44:47], v5, s[62:63] offset:0 sc1\n"
      "global_load_dwordx4 v[48:51], v5, s[62:63] offset:1024 sc1\n"
      "global_load_dwordx4 v[52:55], v5, s[62:63] offset:2048 sc1\n"
      "s_waitcnt vmcnt(0)\n"
      "v_bitop3_b32 v54, v47, v51, s37 bitop3:0x7e\n"
      "v_bitop3_b32 v54, v54, v55, s37 bitop3:0xf6\n"
      "v_cmp_ne_u32 vcc, 0, v54\n"
      "s_nop 1\n"
      "s_cbranch_vccnz .Lp_s27_%=\n"
      "s_nop 7\n"
      "s_branch .Lp_o28_%=\n"
      ".Lp_s31_%=:\n"
      "s_cmp_eq_u32 s38, 0\n"
      "s_cbranch_scc1 .Lp_o32_%=\n"
      "s_sub_u32 s38, s38, 1\n"
      "s_nop 7\n"
      "global_load_dwordx4 v[72:75], v6, s[62:63] offset:0 sc1\n"
      "global_load_dwordx4 v[76:79], v6, s[62:63] offset:1024 sc1\n"
      "global_load_dwordx4 v[80:83], v6, s[62:63] offset:2048 sc1\n"
      "s_waitcnt vmcnt(0)\n"
      "v_bitop3_b32 v82, v75, v79, s37 bitop3:0x7e\n"
      "v_bitop3_b32 v82, v82, v83, s37 bitop3:0xf6\n"
      "v_cmp_ne_u32 vcc, 0, v82\n"
      "s_nop 1\n"
      "s_cbranch_vccnz .Lp_s31_%=\n"
      "s_nop 7\n"
      "s_branch .Lp_o32_%=\n"
      ".Ls_s3_%=:\n"
      "s_cmp_eq_u32 s38, 0\n"
      "s_cbranch_scc1 .Ls_o4_%=\n"
      "s_sub_u32 s38, s38, 1\n"
      "s_sleep 4\n"
      "s_nop 7\n"
      "global_load_dword v252, v253, s[58:59] offset:0 sc1\n"
      "s_waitcnt vmcnt(0)\n"
      "v_cmp_ne_u32 vcc, s37, v252\n"
      "s_nop 1\n"
      "s_cbranch_vccnz .Ls_s3_%=\n"
      "s_nop 7\n"
      "s_branch .Ls_o4_%=\n"
      ".Ls_s6_%=:\n"
      "s_cmp_eq_u32 s38, 0\n"
      "s_cbranch_scc1 .Ls_o7_%=\n"
      "s_sub_u32 s38, s38, 1\n"
      "s_sleep 2\n"
      "s_nop 7\n"
      "global_load_dwordx2 v[206:207], v204, s[58:59] sc1\n"
      "s_waitcnt vmcnt(0)\n"
      "v_cmp_ne_u32 vcc, s37, v207\n"
      "s_nop 1\n"
      "s_cbranch_vccnz .Ls_s6_%=\n"
      "s_nop 7\n"
      "s_branch .Ls_o7_%=\n"
      ".Ls_s12_%=:\n"
      "s_cmp_eq_u32 s38, 0\n"
      "s_cbranch_scc1 .Ls_o13_%=\n"
      "s_sub_u32 s38, s38, 1\n"
      "s_sleep 4\n"
      "s_nop 7\n"
      "global_load_dword v252, v253, s[58:59] offset:128 sc1\n"
      "s_waitcnt vmcnt(0)\n"
      "v_cmp_ne_u32 vcc, s37, v252\n"
      "s_nop 1\n"
      "s_cbranch_vccnz .Ls_s12_%=\n"
      "s_nop 7\n"
      "s_branch .Ls_o13_%=\n"
      ".Ls_s15_%=:\n"
      "s_cmp_eq_u32 s38, 0\n"
      "s_cbranch_scc1 .Ls_o16_%=\n"
      "s_sub_u32 s38, s38, 1\n"
      "s_sleep 1\n"
      "s_nop 7\n"
      "global_load_dwordx2 v[204:205], v209, s[58:59] offset:0 sc1\n"
      "global_load_dwordx2 v[206:207], v209, s[58:59] offset:256 sc1\n"
      "s_waitcnt vmcnt(0)\n"
      "v_bitop3_b32 v210, v205, v207, s37 bitop3:0x7e\n"
      "v_cmp_ne_u32 vcc, 0, v210\n"
      "s_nop 1\n"
      "s_cbranch_vccnz .Ls_s15_%=\n"
      "s_nop 7\n"
      "s_branch .Ls_o16_%=\n"
      ".Ls_s18_%=:\n"
      "s_cmp_eq_u32 s38, 0\n"
      "s_cbranch_scc1 .Ls_o19_%=\n"
      "s_sub_u32 s38, s38, 1\n"
      "s_nop 7\n"
      "global_load_dwordx4 v[12:15], v4, s[62:63] offset:0 sc1\n"
      "global_load_dwordx4 v[16:19], v4, s[62:63] offset:1024 sc1\n"
      "global_load_dwordx4 v[20:23], v4, s[62:63] offset:2048 sc1\n"
      "s_waitcnt vmcnt(0)\n"
      "v_bitop3_b32 v22, v15, v19, s37 bitop3:0x7e\n"
      "v_bitop3_b32 v22, v22, v23, s37 bitop3:0xf6\n"
      "v_cmp_ne_u32 vcc, 0, v22\n"
      "s_nop 1\n"
      "s_cbranch_vccnz .Ls_s18_%=\n"
      "s_nop 7\n"
      "s_branch .Ls_o19_%=\n"
      ".Ls_s23_%=:\n"
      "s_cmp_eq_u32 s38, 0\n"
      "s_cbranch_scc1 .Ls_o24_%=\n"
      "s_sub_u32 s38, s38, 1\n"
      "s_sleep 2\n"
      "s_nop 7\n"
      "global_load_dwordx4 v[24:27], v60, s[58:59] offset:0 sc1\n"
      "global_load_dwordx4 v[28:31], v60, s[58:59] offset:1024 sc1\n"
      "global_load_dwordx4 v[32:35], v60, s[58:59] offset:2048 sc1\n"
      "global_load_dwordx4 v[36:39], v60, s[58:59] offset:3072 sc1\n"
      "global_load_dwordx4 v[40:43], v62, s[58:59] offset:1024 sc1\n"
      "s_waitcnt vmcnt(0)\n"
      "v_bitop3_b32 v61, v25, v27, s37 bitop3:0x7e\n"
      "v_bitop3_b32 v61, v61, v29, s37 bitop3:0xf6\n"
      "v_bitop3_b32 v61, v61, v31, s37 bitop3:0xf6\n"
      "v_bitop3_b32 v61, v61, v33, s37 bitop3:0xf6\n"
      "v_bitop3_b32 v61, v61, v35, s37 bitop3:0xf6\n"
      "v_bitop3_b32 v61, v61, v37, s37 bitop3:0xf6\n"
      "v_bitop3_b32 v61, v61, v39, s37 bitop3:0xf6\n"
      "v_bitop3_b32 v61, v61, v43, s37 bitop3:0xf6\n"
      "v_cmp_ne_u32 vcc, 0, v61\n"
      "s_nop 1\n"
      "s_cbranch_vccnz .Ls_s23_%=\n"
      "s_nop 7\n"
      "s_branch .Ls_o24_%=\n"
      ".Ls_s28_%=:\n"
      "s_cmp_eq_u32 s38, 0\n"
      "s_cbranch_scc1 .Ls_o29_%=\n"
      "s_sub_u32 s38, s38, 1\n"
      "s_nop 7\n"
      "global_load_dwordx4 v[44:47], v5, s[62:63] offset:0 sc1\n"
      "global_load_dwordx4 v[48:51], v5, s[62:63] offset:1024 sc1\n"
      "global_load_dwordx4 v[52:55], v5, s[62:63] offset:2048 sc1\n"
      "s_waitcnt vmcnt(0)\n"
      "v_bitop3_b32 v54, v47, v51, s37 bitop3:0x7e\n"
      "v_bitop3_b32 v54, v54, v55, s37 bitop3:0xf6\n"
      "v_cmp_ne_u32 vcc, 0, v54\n"
      "s_nop 1\n"
      "s_cbranch_vccnz .Ls_s28_%=\n"
      "s_nop 7\n"
      "s_branch .Ls_o29_%=\n"
      ".Ls_s32_%=:\n"
      "s_cmp_eq_u32 s38, 0\n"
      "s_cbranch_scc1 .Ls_o33_%=\n"
      "s_sub_u32 s38, s38, 1\n"
      "s_nop 7\n"
      "global_load_dwordx4 v[72:75], v6, s[62:63] offset:0 sc1\n"
      "global_load_dwordx4 v[76:79], v6, s[62:63] offset:1024 sc1\n"
      "global_load_dwordx4 v[80:83], v6, s[62:63] offset:2048 sc1\n"
      "s_waitcnt vmcnt(0)\n"
      "v_bitop3_b32 v82, v75, v79, s37 bitop3:0x7e\n"
      "v_bitop3_b32 v82, v82, v83, s37 bitop3:0xf6\n"
      "v_cmp_ne_u32 vcc, 0, v82\n"
      "s_nop 1\n"
      "s_cbranch_vccnz .Ls_s32_%=\n"
      "s_nop 7\n"
      "s_branch .Ls_o33_%=\n"
      ".Lend_%=:\n"
      :
      : "s"(__builtin_amdgcn_kernarg_segment_ptr()), "s"(blockIdx.x), "s"(__builtin_amdgcn_readfirstlane(threadIdx.x >> 6)),
        "s"((unsigned)(size_t)smem)
      : "memory", "scc", "vcc",
        "v0", "v1", "v2", "v3", "v4", "v5", "v6", "v7", "v8", "v9", "v10", "v11", "v12", "v13", "v14", "v15", "v16", "v17", "v18", "v19", "v20", "v21", "v22", "v23", "v24", "v25", "v26", "v27", "v28", "v29", "v30", "v31", "v32", "v33", "v34", "v35", "v36", "v37", "v38", "v39", "v40", "v41", "v42", "v43", "v44", "v45", "v46", "v47", "v48", "v49", "v50", "v51", "v52", "v53", "v54", "v55", "v56", "v57", "v58", "v59", "v60", "v61", "v62", "v63", "v64", "v65", "v66", "v67", "v68", "v69", "v70", "v71", "v72", "v73", "v74", "v75", "v76", "v77", "v78", "v79", "v80", "v81", "v82", "v83", "v84", "v85", "v86", "v87", "v88", "v89", "v90", "v91", "v92", "v93", "v94", "v95", "v96", "v97", "v98", "v99", "v100", "v101", "v102", "v103", "v104", "v105", "v106", "v107", "v108", "v109", "v110", "v111", "v112", "v113", "v114", "v115", "v116", "v117", "v118", "v119", "v120", "v121", "v122", "v123", "v124", "v125", "v126", "v127", "v128", "v129", "v130", "v131", "v132", "v133", "v134", "v135", "v136", "v137", "v138", "v139", "v140", "v141", "v142", "v143", "v144", "v145", "v146", "v147", "v148", "v149", "v150", "v151", "v152", "v153", "v154", "v155", "v156", "v157", "v158", "v159", "v160", "v161", "v162", "v163", "v164", "v165", "v166", "v167", "v168", "v169", "v170", "v171", "v172", "v173", "v174", "v175", "v176", "v177", "v178", "v179", "v180", "v181", "v182", "v183", "v184", "v185", "v186", "v187", "v188", "v189", "v190", "v191", "v192", "v193", "v194", "v195", "v196", "v197", "v198", "v199", "v200", "v201", "v202", "v203", "v204", "v205", "v206", "v207", "v208", "v209", "v210", "v211", "v212", "v213", "v214", "v215", "v216", "v217", "v218", "v219", "v220", "v221", "v222", "v223", "v224", "v225", "v226", "v227", "v228", "v229", "v230", "v231", "v232", "v233", "v234", "v235", "v236", "v237", "v238", "v239", "v240", "v241", "v242", "v243", "v244", "v245", "v246", "v247", "v248", "v249", "v250", "v251", "v252", "v253", "v254", "v255",
        "a0", "a1", "a2", "a3", "a4", "a5", "a6", "a7", "a8", "a9", "a10", "a11", "a12", "a13", "a14", "a15", "a16", "a17", "a18", "a19", "a20", "a21", "a22", "a23", "a24", "a25", "a26", "a27", "a28", "a29", "a30", "a31", "a32", "a33", "a34", "a35", "a36", "a37", "a38", "a39", "a40", "a41", "a42", "a43", "a44", "a45", "a46", "a47", "a48", "a49", "a50", "a51", "a52", "a53", "a54", "a55", "a56", "a57", "a58", "a59", "a60", "a61", "a62", "a63", "a64", "a65", "a66", "a67", "a68", "a69", "a70", "a71", "a72", "a73", "a74", "a75", "a76", "a77", "a78", "a79", "a80", "a81", "a82", "a83", "a84", "a85", "a86", "a87", "a88", "a89", "a90", "a91", "a92", "a93", "a94", "a95", "a96", "a97", "a98", "a99", "a100", "a101", "a102", "a103", "a104", "a105", "a106", "a107", "a108", "a109", "a110", "a111", "a112", "a113", "a114", "a115", "a116", "a117", "a118", "a119", "a120", "a121", "a122", "a123", "a124", "a125", "a126", "a127", "a128", "a129", "a130", "a131", "a132", "a133", "a134", "a135", "a136", "a137", "a138", "a139", "a140", "a141", "a142", "a143", "a144", "a145", "a146", "a147", "a148", "a149", "a150", "a151", "a152", "a153", "a154", "a155", "a156", "a157", "a158", "a159", "a160", "a161", "a162", "a163", "a164", "a165", "a166", "a167", "a168", "a169", "a170", "a171", "a172", "a173", "a174", "a175", "a176", "a177", "a178", "a179", "a180", "a181", "a182", "a183", "a184", "a185", "a186", "a187", "a188", "a189", "a190", "a191", "a192", "a193", "a194", "a195", "a196", "a197", "a198", "a199", "a200", "a201", "a202", "a203", "a204", "a205", "a206", "a207", "a208", "a209", "a210", "a211", "a212", "a213", "a214", "a215", "a216", "a217", "a218", "a219", "a220", "a221", "a222", "a223", "a224", "a225", "a226", "a227", "a228", "a229", "a230", "a231", "a232", "a233", "a234", "a235", "a236", "a237", "a238", "a239", "a240", "a241", "a242", "a243", "a244", "a245", "a246", "a247", "a248", "a249", "a250", "a251", "a252", "a253", "a254", "a255",
        "s8", "s9", "s10", "s11", "s12", "s13", "s14", "s15", "s16", "s17", "s18", "s19", "s20", "s21", "s22", "s23", "s24", "s25", "s26", "s27", "s28", "s29", "s30", "s31", "s32", "s33", "s34", "s35", "s36", "s37", "s38", "s39", "s40", "s41", "s42", "s43", "s44", "s45", "s46", "s47", "s48", "s49", "s50", "s51", "s52", "s53", "s54", "s55", "s56", "s57", "s58", "s59", "s60", "s61", "s62", "s63", "s64", "s65", "s66", "s67", "s68", "s69", "s70", "s71", "s72", "s73", "s74", "s75", "s76", "s77", "s78", "s79", "s80", "s81", "s82", "s83", "s84", "s85", "s86", "s87");
}

extern "C" void kernel_launch(void* const* d_in, const int* in_sizes, int n_in, void* d_out, int out_size, void* d_ws, size_t ws_size,
                              hipStream_t stream) {
  fused_k<<<256, 256, 0, stream>>>((const u16*)d_in[1], (const u16*)d_in[2], (const u16*)d_in[3], (const u16*)d_in[4], (const u16*)d_in[0],
                                   (char*)d_ws, (u16*)d_out, (const u16*)d_in[6], (const u16*)d_in[10], (const u16*)d_in[7],
                                   (const u16*)d_in[8], (const u16*)d_in[9]);
}
```

```cpp
#include <hip/hip_runtime.h>

typedef unsigned short u16;

__global__ __launch_bounds__(256, 1) void fused_k(const u16* wsre, const u16* wsim, const u16* wdre, const u16* wdim, const u16* x, char* ws,
                                               u16* out, const u16* w_r, const u16* gate_w, const u16* w_gate, const u16* w_up,
                                               const u16* w_down) {
  __shared__ __attribute__((aligned(16))) char smem[163840];
  asm volatile(
      ".Lbegin_%=:\n"
      "s_load_dwordx16 s[8:23], %0, 0\n"
      "s_load_dwordx8 s[24:31], %0, 64\n"
      "v_mbcnt_lo_u32_b32 v243, -1, 0\n"
      "s_mov_b32 s32, %1\n"
      "v_mbcnt_hi_u32_b32 v243, -1, v243\n"
      "s_mov_b32 s33, %2\n"
      "s_bfe_u32 s34, s32, 0x20001\n"
      "s_bfe_u32 s35, s32, 0x20003\n"
      "v_lshrrev_b32 v236, 3, v243\n"
      "s_lshr_b32 s43, s32, 5\n"
      "v_xor_b32 v237, v243, v236\n"
      "s_and_b32 s36, s32, 1\n"
      "v_and_b32 v237, 7, v237\n"
      "s_lshl3_add_u32 s36, s36, s43\n"
      "v_lshlrev_b32 v237, 4, v237\n"
      "s_add_u32 s44, s43, s35\n"
      "v_lshlrev_b32 v242, 4, v243\n"
      "s_lshl_b32 s41, s33, 14\n"
      "s_bitcmp1_b32 s44, 0\n"
      "s_cbranch_scc1 .Lstream_%=\n"
      "v_and_b32 v238, 3, v236\n"
      "s_lshr_b32 s43, s43, 1\n"
      "v_and_b32 v239, 4, v236\n"
      "s_add_u32 s80, s43, s35\n"
      "v_lshl_or_b32 v238, v239, 1, v238\n"
      "s_lshl_b32 s80, s80, 8\n"
      "v_lshl_or_b32 v240, v238, 10, v237\n"
      "s_and_b32 s80, s80, 0x300\n"
      "v_add_u32 v241, 15360, v240\n"
      "s_lshr_b32 s83, s36, 1\n"
      "v_add_u32 v254, 2048, v240\n"
      "s_and_b32 s44, s34, 1\n"
      "v_add_u32 v255, 17408, v240\n"
      "s_lshl3_add_u32 s44, s44, s83\n"
      "s_lshl2_add_u32 s40, s44, s33\n"
      "s_lshl_b32 s44, s40, 15\n"
      "s_lshl_b32 s81, s33, 8\n"
      "s_add_u32 s81, s81, s80\n"
      "s_and_b32 s81, s81, 0x380\n"
      "s_lshl_b32 s45, s35, 17\n"
      "s_lshl_b32 s82, s33, 12\n"
      "s_add_u32 s76, s34, 1\n"
      "s_and_b32 s76, s76, 3\n"
      "s_lshl_b32 s76, s76, 15\n"
      "s_or_b32 s76, s76, s81\n"
      "s_add_u32 s76, s76, s45\n"
      "s_add_u32 s77, s34, 2\n"
      "s_and_b32 s77, s77, 3\n"
      "s_lshl_b32 s77, s77, 15\n"
      "s_or_b32 s77, s77, s81\n"
      "s_add_u32 s77, s77, s45\n"
      "s_add_u32 s78, s44, s80\n"
      "s_add_u32 s79, s80, 256\n"
      "s_and_b32 s79, s79, 0x300\n"
      "s_add_u32 s79, s79, s44\n"
      "s_add_u32 m0, s82, 65536\n"
      "s_bitcmp1_b32 s34, 1\n"
      "s_waitcnt lgkmcnt(0)\n"
      "s_cselect_b64 s[12:13], s[14:15], s[12:13]\n"
      "s_add_u32 s46, s12, s76\n"
      "s_addc_u32 s47, s13, 0\n"
      "global_load_lds_dwordx4 v240, s[46:47] offset:0\n"
      "global_load_lds_dwordx4 v241, s[46:47] offset:1024\n"
      "global_load_lds_dwordx4 v254, s[46:47] offset:2048\n"
      "global_load_lds_dwordx4 v255, s[46:47] offset:3072\n"
      "s_add_u32 s70, s16, s78\n"
      "s_addc_u32 s71, s17, 0\n"
      "s_mov_b32 m0, s41\n"
      "s_add_u32 s46, s12, s77\n"
      "s_addc_u32 s47, s13, 0\n"
      "global_load_lds_dwordx4 v240, s[70:71] offset:0\n"
      "global_load_lds_dwordx4 v241, s[70:71] offset:1024\n"
      "global_load_lds_dwordx4 v254, s[70:71] offset:2048\n"
      "global_load_lds_dwordx4 v255, s[70:71] offset:3072\n"
      "s_add_u32 m0, s82, 98304\n"
      "s_nop 0\n"
      "global_load_lds_dwordx4 v240, s[46:47] offset:0\n"
      "global_load_lds_dwordx4 v241, s[46:47] offset:1024\n"
      "s_add_u32 s70, s16, s79\n"
      "s_addc_u32 s71, s17, 0\n"
      "s_add_u32 m0, s41, 4096\n"
      "s_nop 0\n"
      "global_load_lds_dwordx4 v240, s[70:71] offset:0\n"
      "global_load_lds_dwordx4 v241, s[70:71] offset:1024\n"
      "global_load_lds_dwordx4 v254, s[70:71] offset:2048\n"
      "global_load_lds_dwordx4 v255, s[70:71] offset:3072\n"
      "s_branch .Lprod_%=\n"
      ".Lstream_%=:\n"
      "v_lshl_or_b32 v244, v236, 12, v237\n"
      "v_add_u32 v245, 31744, v244\n"
      "v_add_u32 v246, 63488, v244\n"
      "v_add_u32 v247, 95232, v244\n"
      "s_getpc_b64 s[46:47]\n"
      ".Lhere_%=:\n"
      "s_lshr_b32 s44, s32, 4\n"
      "s_lshl2_add_u32 s44, s44, s33\n"
      "s_lshl_b32 s44, s44, 10\n"
      "s_sub_u32 s46, s46, .Lhere_%=-.Lbegin_%=\n"
      "s_subb_u32 s47, s47, 0\n"
      "v_add_u32 v238, s44, v242\n"
      "v_min_u32 v238, .Lend_%=-.Lbegin_%=-16, v238\n"
      "global_load_dwordx4 v[192:195], v238, s[46:47]\n"
      "s_and_b32 s44, s34, 1\n"
      "s_lshr_b32 s45, s36, 1\n"
      "s_lshl3_add_u32 s44, s44, s45\n"
      "s_lshl2_add_u32 s44, s44, s35\n"
      "s_lshl2_add_u32 s44, s44, s33\n"
      "s_lshl_b32 s44, s44, 13\n"
      "s_add_u32 s44, s44, 128\n"
      "v_and_b32 v233, 31, v243\n"
      "v_lshl_add_u32 v233, v233, 8, s44\n"
      "s_lshr_b32 s44, s33, 1\n"
      "s_add_u32 s44, s44, s34\n"
      "s_add_u32 s44, s44, 1\n"
      "s_and_b32 s44, s44, 3\n"
      "s_lshl2_add_u32 s44, s35, s44\n"
      "s_lshr_b32 s45, s32, 6\n"
      "s_lshl2_add_u32 s44, s44, s45\n"
      "s_and_b32 s45, s33, 1\n"
      "s_lshl1_add_u32 s44, s44, s45\n"
      "s_lshl_b32 s44, s44, 12\n"
      "s_add_u32 s44, s44, 128\n"
      "v_and_b32 v234, 15, v243\n"
      "v_lshl_add_u32 v234, v234, 8, s44\n"
      "s_bitcmp1_b32 s34, 1\n"
      "s_waitcnt lgkmcnt(0)\n"
      "s_cselect_b64 s[68:69], s[14:15], s[12:13]\n"
      "s_bitcmp1_b32 s34, 1\n"
      "s_cselect_b64 s[8:9], s[10:11], s[8:9]\n"
      "s_and_b32 s45, s34, 1\n"
      "s_lshl2_add_u32 s45, s45, s33\n"
      "s_lshl_b32 s45, s45, 9\n"
      "s_lshl_b32 s76, s36, 19\n"
      "s_add_u32 s45, s45, s76\n"
      "s_add_u32 s8, s8, s45\n"
      "s_addc_u32 s9, s9, 0\n"
      "s_lshr_b32 s39, s32, 6\n"
      "s_and_b32 s45, s35, 2\n"
      "s_xor_b32 s39, s39, s45\n"
      "s_xor_b32 s39, s39, s33\n"
      "s_lshl_b32 s45, s39, 7\n"
      "s_add_u32 s76, s33, 0\n"
      "s_and_b32 s76, s76, 3\n"
      "s_lshl_b32 s76, s76, 17\n"
      "s_or_b32 s76, s76, s45\n"
      "s_add_u32 s77, s33, 1\n"
      "s_and_b32 s77, s77, 3\n"
      "s_lshl_b32 s77, s77, 17\n"
      "s_or_b32 s77, s77, s45\n"
      "s_add_u32 s78, s33, 2\n"
      "s_and_b32 s78, s78, 3\n"
      "s_lshl_b32 s78, s78, 17\n"
      "s_or_b32 s78, s78, s45\n"
      "s_add_u32 s79, s33, 3\n"
      "s_and_b32 s79, s79, 3\n"
      "s_lshl_b32 s79, s79, 17\n"
      "s_or_b32 s79, s79, s45\n"
      "v_mov_b32 v239, 0\n"
      "global_load_dword v199, v239, s[8:9]\n"
      "s_add_u32 s58, s18, 4718592\n"
      "s_addc_u32 s59, s19, 0\n"
      "global_load_dword v235, v239, s[58:59] sc1\n"
      "s_lshl2_add_u32 s43, s34, s35\n"
      "s_mul_i32 s43, s43, 266240\n"
      "s_add_u32 s43, s43, 262144\n"
      "s_lshl2_add_u32 s44, s36, s35\n"
      "s_mul_i32 s44, s44, 147712\n"
      "s_add_u32 s44, s44, 6438912\n"
      "s_bitcmp1_b32 s33, 0\n"
      "s_cselect_b32 s43, s43, s44\n"
      "s_cselect_b64 s[46:47], s[22:23], s[24:25]\n"
      "s_cselect_b64 s[70:71], s[28:29], s[26:27]\n"
      "s_lshl_b32 s44, s36, 17\n"
      "s_mul_i32 s45, s32, 7168\n"
      "s_bitcmp1_b32 s33, 1\n"
      "s_cselect_b64 s[46:47], s[46:47], s[18:19]\n"
      "s_cselect_b32 s43, 0, s43\n"
      "s_cselect_b32 s84, 0x380, 0\n"
      "v_lshlrev_b32 v238, 7, v243\n"
      "v_and_b32 v238, s84, v238\n"
      "v_add_u32 v238, s43, v238\n"
      "global_load_dword v197, v238, s[46:47] sc1\n"
      "s_cmp_eq_u32 s33, 2\n"
      "s_cselect_b64 s[70:71], s[30:31], s[70:71]\n"
      "s_cmp_eq_u32 s33, 3\n"
      "s_cselect_b64 s[70:71], s[20:21], s[70:71]\n"
      "s_cselect_b32 s44, s44, s45\n"
      "v_mov_b32 v239, s44\n"
      "global_load_dword v198, v239, s[70:71] sc1\n"
      "s_sleep 8\n"
      "global_load_dword v200, v234, s[68:69]\n"
      "global_load_dword v201, v233, s[16:17]\n"
      "s_sleep 4\n"
      "s_bitcmp1_b32 s34, 1\n"
      "s_cselect_b64 s[46:47], s[14:15], s[12:13]\n"
      "s_min_u32 s44, s33, 2\n"
      "s_add_u32 s44, s44, s34\n"
      "s_add_u32 s44, s44, 2\n"
      "s_and_b32 s44, s44, 3\n"
      "s_lshl2_add_u32 s44, s35, s44\n"
      "s_lshr_b32 s45, s32, 6\n"
      "s_lshl2_add_u32 s44, s44, s45\n"
      "s_lshl_b32 s44, s44, 13\n"
      "v_lshl_add_u32 v238, v243, 7, s44\n"
      "s_cmp_eq_u32 s33, 0\n"
      "s_cselect_b32 s44, 4096, 0\n"
      "v_or_b32 v238, s44, v238\n"
      "s_cmp_eq_u32 s33, 3\n"
      "s_cbranch_scc1 .Ls_e1_%=\n"
      "global_load_dword v196, v238, s[46:47]\n"
      ".Ls_e1_%=:\n"
      "s_xor_b32 s45, s76, 0\n"
      "s_add_u32 s46, s8, s45\n"
      "s_addc_u32 s47, s9, 0\n"
      "s_add_u32 m0, s41, 0\n"
      "s_nop 0\n"
      "global_load_lds_dwordx4 v244, s[46:47] offset:0\n"
      "global_load_lds_dwordx4 v245, s[46:47] offset:1024\n"
      "global_load_lds_dwordx4 v246, s[46:47] offset:2048\n"
      "global_load_lds_dwordx4 v247, s[46:47] offset:3072\n"
      "s_xor_b32 s45, s77, 0\n"
      "s_add_u32 s46, s8, s45\n"
      "s_addc_u32 s47, s9, 0\n"
      "s_add_u32 m0, s41, 4096\n"
      "s_nop 0\n"
      "global_load_lds_dwordx4 v244, s[46:47] offset:0\n"
      "global_load_lds_dwordx4 v245, s[46:47] offset:1024\n"
      "global_load_lds_dwordx4 v246, s[46:47] offset:2048\n"
      "global_load_lds_dwordx4 v247, s[46:47] offset:3072\n"
      "s_xor_b32 s45, s78, 0\n"
      "s_add_u32 s46, s8, s45\n"
      "s_addc_u32 s47, s9, 0\n"
      "s_add_u32 m0, s41, 8192\n"
      "s_nop 0\n"
      "global_load_lds_dwordx4 v244, s[46:47] offset:0\n"
      "global_load_lds_dwordx4 v245, s[46:47] offset:1024\n"
      "global_load_lds_dwordx4 v246, s[46:47] offset:2048\n"
      "global_load_lds_dwordx4 v247, s[46:47] offset:3072\n"
      "v_and_b32 v238, 15, v243\n"
      "v_lshrrev_b32 v239, 4, v243\n"
      "v_xor_b32 v239, v239, v238\n"
      "v_and_b32 v239, 7, v239\n"
      "v_lshlrev_b32 v239, 4, v239\n"
      "v_lshl_or_b32 v238, v238, 7, v239\n"
      "v_add_u32 v248, s41, v238\n"
      "v_xor_b32 v249, 64, v248\n"
      "s_lshl2_add_u32 s43, s34, s35\n"
      "s_lshr_b32 s45, s39, 1\n"
      "s_lshl1_add_u32 s45, s33, s45\n"
      "s_lshl_b32 s45, s45, 15\n"
      "s_mul_i32 s44, s43, 266240\n"
      "s_add_u32 s44, s44, 4096\n"
      "s_add_u32 s14, s18, s44\n"
      "s_addc_u32 s15, s19, 0\n"
      "s_add_u32 s10, s14, s45\n"
      "s_addc_u32 s11, s15, 0\n"
      "s_xor_b32 s45, s45, 0x8000\n"
      "s_add_u32 s14, s14, s45\n"
      "s_addc_u32 s15, s15, 0\n"
      "s_and_b32 s44, s39, 1\n"
      "s_lshl_b32 s45, s44, 14\n"
      "v_add_u32 v240, s45, v242\n"
      "v_xor_b32 v241, 0x4000, v240\n"
      "s_add_u32 s72, s10, 0x2000\n"
      "s_addc_u32 s73, s11, 0\n"
      "s_add_u32 s74, s14, 0x2000\n"
      "s_addc_u32 s75, s15, 0\n"
      "s_lshl2_add_u32 s44, s43, s33\n"
      "s_lshl_b32 s44, s44, 12\n"
      "s_add_u32 s44, s44, 1048576\n"
      "v_and_b32 v253, 7, v243\n"
      "v_lshl_add_u32 v253, v253, 9, s44\n"
      "s_lshl2_add_u32 s43, s36, s35\n"
      "s_mul_i32 s43, s43, 147712\n"
      "s_mul_i32 s44, s33, 3072\n"
      "s_add_u32 s43, s43, s44\n"
      "s_add_u32 s43, s43, 6291456\n"
      "s_add_u32 s60, s18, s43\n"
      "s_addc_u32 s61, s19, 0\n"
      "s_mul_i32 s44, s34, 36864\n"
      "s_add_u32 s62, s60, s44\n"
      "s_addc_u32 s63, s61, 0\n"
      "s_lshr_b32 s43, s34, 1\n"
      "s_lshl2_add_u32 s43, s43, s35\n"
      "s_lshl_b32 s43, s43, 12\n"
      "s_add_u32 s43, s43, 4096\n"
      "v_lshl_add_u32 v204, v243, 6, s43\n"
      "s_lshr_b32 s80, s32, 4\n"
      "s_bfe_u32 s43, s32, 0x10002\n"
      "s_lshl1_add_u32 s80, s80, s43\n"
      "s_and_b32 s81, s32, 3\n"
      "s_cmp_lt_u32 s80, 28\n"
      "s_cselect_b32 s86, -1, 0\n"
      "s_lshr_b32 s83, s33, 1\n"
      "s_lshl1_add_u32 s83, s81, s83\n"
      "v_add_u32 v250, 0x10000, v248\n"
      "v_add_u32 v251, 0x10000, v249\n"
      "s_xor_b32 s45, s79, 0\n"
      "s_add_u32 s46, s8, s45\n"
      "s_addc_u32 s47, s9, 0\n"
      "s_add_u32 m0, s41, 12288\n"
      "s_nop 0\n"
      "global_load_lds_dwordx4 v244, s[46:47] offset:0\n"
      "global_load_lds_dwordx4 v245, s[46:47] offset:1024\n"
      "global_load_lds_dwordx4 v246, s[46:47] offset:2048\n"
      "global_load_lds_dwordx4 v247, s[46:47] offset:3072\n"
      "s_xor_b32 s45, s76, 128\n"
      "s_add_u32 s46, s8, s45\n"
      "s_addc_u32 s47, s9, 0\n"
      "s_add_u32 m0, s41, 65536\n"
      "s_nop 0\n"
      "global_load_lds_dwordx4 v244, s[46:47] offset:0\n"
      "global_load_lds_dwordx4 v245, s[46:47] offset:1024\n"
      "global_load_lds_dwordx4 v246, s[46:47] offset:2048\n"
      "global_load_lds_dwordx4 v247, s[46:47] offset:3072\n"
      "s_waitcnt vmcnt(16)\n"
      "ds_read_b128 a[0:3], v248 offset:0\n"
      "ds_read_b128 a[4:7], v248 offset:2048\n"
      "ds_read_b128 a[32:35], v249 offset:0\n"
      "ds_read_b128 a[36:39], v249 offset:2048\n"
      "s_xor_b32 s45, s77, 128\n"
      "s_add_u32 s46, s8, s45\n"
      "s_addc_u32 s47, s9, 0\n"
      "s_add_u32 m0, s41, 69632\n"
      "s_nop 0\n"
      "global_load_lds_dwordx4 v244, s[46:47] offset:0\n"
      "global_load_lds_dwordx4 v245, s[46:47] offset:1024\n"
      "global_load_lds_dwordx4 v246, s[46:47] offset:2048\n"
      "global_load_lds_dwordx4 v247, s[46:47] offset:3072\n"
      "s_waitcnt vmcnt(16)\n"
      "ds_read_b128 a[8:11], v248 offset:4096\n"
      "ds_read_b128 a[12:15], v248 offset:6144\n"
      "ds_read_b128 a[40:43], v249 offset:4096\n"
      "ds_read_b128 a[44:47], v249 offset:6144\n"
      "s_xor_b32 s45, s78, 128\n"
      "s_add_u32 s46, s8, s45\n"
      "s_addc_u32 s47, s9, 0\n"
      "s_add_u32 m0, s41, 73728\n"
      "s_nop 0\n"
      "global_load_lds_dwordx4 v244, s[46:47] offset:0\n"
      "global_load_lds_dwordx4 v245, s[46:47] offset:1024\n"
      "global_load_lds_dwordx4 v246, s[46:47] offset:2048\n"
      "global_load_lds_dwordx4 v247, s[46:47] offset:3072\n"
      "s_waitcnt vmcnt(16)\n"
      "ds_read_b128 a[16:19], v248 offset:8192\n"
      "ds_read_b128 a[20:23], v248 offset:10240\n"
      "ds_read_b128 a[48:51], v249 offset:8192\n"
      "ds_read_b128 a[52:55], v249 offset:10240\n"
      "v_readfirstlane_b32 s37, v235\n"
      "s_nop 1\n"
      "s_mul_i32 s37, s37, 0x9E3779B1\n"
      "s_add_u32 s37, s37, 0x7F4A7C15\n"
      "s_mov_b32 s38, 0x8000\n"
      "s_waitcnt lgkmcnt(8)\n"
      "s_xor_b32 s45, s79, 128\n"
      "s_add_u32 s46, s8, s45\n"
      "s_addc_u32 s47, s9, 0\n"
      "s_add_u32 m0, s41, 0\n"
      "s_nop 0\n"
      "global_load_lds_dwordx4 v244, s[46:47] offset:0\n"
      "global_load_lds_dwordx4 v245, s[46:47] offset:1024\n"
      "global_load_lds_dwordx4 v246, s[46:47] offset:2048\n"
      "global_load_lds_dwordx4 v247, s[46:47] offset:3072\n"
      "s_barrier\n"
      "s_waitcnt vmcnt(16)\n"
      "ds_read_b128 a[24:27], v248 offset:12288\n"
      "ds_read_b128 a[28:31], v248 offset:14336\n"
      "ds_read_b128 a[56:59], v249 offset:12288\n"
      "ds_read_b128 a[60:63], v249 offset:14336\n"
      "s_waitcnt lgkmcnt(8)\n"
      "s_xor_b32 s45, s76, 256\n"
      "s_add_u32 s46, s8, s45\n"
      "s_addc_u32 s47, s9, 0\n"
      "s_add_u32 m0, s41, 4096\n"
      "s_nop 0\n"
      "global_load_lds_dwordx4 v244, s[46:47] offset:0\n"
      "global_load_lds_dwordx4 v245, s[46:47] offset:1024\n"
      "global_load_lds_dwordx4 v246, s[46:47] offset:2048\n"
      "global_load_lds_dwordx4 v247, s[46:47] offset:3072\n"
      "s_waitcnt vmcnt(16)\n"
      "ds_read_b128 a[64:67], v250 offset:0\n"
      "ds_read_b128 a[68:71], v250 offset:2048\n"
      "ds_read_b128 a[96:99], v251 offset:0\n"
      "ds_read_b128 a[100:103], v251 offset:2048\n"
      "s_waitcnt lgkmcnt(8)\n"
      "s_xor_b32 s45, s77, 256\n"
      "s_add_u32 s46, s8, s45\n"
      "s_addc_u32 s47, s9, 0\n"
      "s_add_u32 m0, s41, 8192\n"
      "s_nop 0\n"
      "global_load_lds_dwordx4 v244, s[46:47] offset:0\n"
      "global_load_lds_dwordx4 v245, s[46:47] offset:1024\n"
      "global_load_lds_dwordx4 v246, s[46:47] offset:2048\n"
      "global_load_lds_dwordx4 v247, s[46:47] offset:3072\n"
      "s_waitcnt vmcnt(16)\n"
      "ds_read_b128 a[72:75], v250 offset:4096\n"
      "ds_read_b128 a[76:79], v250 offset:6144\n"
      "ds_read_b128 a[104:107], v251 offset:4096\n"
      "ds_read_b128 a[108:111], v251 offset:6144\n"
      "s_waitcnt lgkmcnt(8)\n"
      "s_xor_b32 s45, s78, 256\n"
      "s_add_u32 s46, s8, s45\n"
      "s_addc_u32 s47, s9, 0\n"
      "s_add_u32 m0, s41, 12288\n"
      "s_nop 0\n"
      "global_load_lds_dwordx4 v244, s[46:47] offset:0\n"
      "global_load_lds_dwordx4 v245, s[46:47] offset:1024\n"
      "global_load_lds_dwordx4 v246, s[46:47] offset:2048\n"
      "global_load_lds_dwordx4 v247, s[46:47] offset:3072\n"
      "s_waitcnt vmcnt(16)\n"
      "ds_read_b128 a[80:83], v250 offset:8192\n"
      "ds_read_b128 a[84:87], v250 offset:10240\n"
      "ds_read_b128 a[112:115], v251 offset:8192\n"
      "ds_read_b128 a[116:119], v251 offset:10240\n"
      "s_waitcnt vmcnt(12)\n"
      "ds_read_b128 a[88:91], v248 offset:0\n"
      "ds_read_b128 a[92:95], v248 offset:2048\n"
      "ds_read_b128 a[120:123], v249 offset:0\n"
      "ds_read_b128 a[124:127], v249 offset:2048\n"
      "global_load_dword v252, v253, s[58:59] offset:0 sc1\n"
      "s_waitcnt lgkmcnt(12)\n"
      "s_xor_b32 s45, s79, 256\n"
      "s_add_u32 s46, s8, s45\n"
      "s_addc_u32 s47, s9, 0\n"
      "s_add_u32 m0, s41, 65536\n"
      "s_nop 0\n"
      "global_load_lds_dwordx4 v244, s[46:47] offset:0\n"
      "global_load_lds_dwordx4 v245, s[46:47] offset:1024\n"
      "global_load_lds_dwordx4 v246, s[46:47] offset:2048\n"
      "global_load_lds_dwordx4 v247, s[46:47] offset:3072\n"
      "s_waitcnt lgkmcnt(8)\n"
      "s_xor_b32 s45, s76, 384\n"
      "s_add_u32 s46, s8, s45\n"
      "s_addc_u32 s47, s9, 0\n"
      "s_add_u32 m0, s41, 69632\n"
      "s_nop 0\n"
      "global_load_lds_dwordx4 v244, s[46:47] offset:0\n"
      "global_load_lds_dwordx4 v245, s[46:47] offset:1024\n"
      "global_load_lds_dwordx4 v246, s[46:47] offset:2048\n"
      "global_load_lds_dwordx4 v247, s[46:47] offset:3072\n"
      "s_waitcnt vmcnt(17)\n"
      "ds_read_b128 a[128:131], v248 offset:4096\n"
      "ds_read_b128 a[132:135], v248 offset:6144\n"
      "ds_read_b128 a[160:163], v249 offset:4096\n"
      "ds_read_b128 a[164:167], v249 offset:6144\n"
      "s_waitcnt vmcnt(13)\n"
      "ds_read_b128 a[136:139], v248 offset:8192\n"
      "ds_read_b128 a[140:143], v248 offset:10240\n"
      "ds_read_b128 a[168:171], v249 offset:8192\n"
      "ds_read_b128 a[172:175], v249 offset:10240\n"
      "s_barrier\n"
      "s_waitcnt vmcnt(8)\n"
      "v_cmp_ne_u32 vcc, s37, v252\n"
      "s_nop 1\n"
      "s_cbranch_vccnz .Ls_s3_%=\n"
      ".Ls_o4_%=:\n"
      "global_load_dwordx2 v[206:207], v204, s[58:59] sc1\n"
      "global_load_dwordx4 v[128:131], v240, s[10:11] offset:-4096 sc1\n"
      "global_load_dwordx4 v[132:135], v240, s[10:11] offset:-3072 sc1\n"
      "global_load_dwordx4 v[136:139], v240, s[10:11] offset:-2048 sc1\n"
      "global_load_dwordx4 v[140:143], v240, s[72:73] offset:-4096 sc1\n"
      "global_load_dwordx4 v[144:147], v240, s[72:73] offset:-3072 sc1\n"
      "global_load_dwordx4 v[148:151], v240, s[72:73] offset:-2048 sc1\n"
      "s_waitcnt lgkmcnt(12)\n"
      "s_xor_b32 s45, s77, 384\n"
      "s_add_u32 s46, s8, s45\n"
      "s_addc_u32 s47, s9, 0\n"
      "s_add_u32 m0, s41, 73728\n"
      "s_nop 0\n"
      "global_load_lds_dwordx4 v244, s[46:47] offset:0\n"
      "global_load_lds_dwordx4 v245, s[46:47] offset:1024\n"
      "global_load_lds_dwordx4 v246, s[46:47] offset:2048\n"
      "global_load_lds_dwordx4 v247, s[46:47] offset:3072\n"
      "s_waitcnt vmcnt(9)\n"
      "v_mfma_f32_16x16x32_bf16 v[0:3], v[128:131], a[0:3], 0\n"
      "global_load_dwordx4 v[152:155], v241, s[10:11] offset:-4096 sc1\n"
      "v_cmp_ne_u32 vcc, s37, v207\n"
      "s_nop 1\n"
      "s_cbranch_vccnz .Ls_s6_%=\n"
      ".Ls_o7_%=:\n"
      "v_mfma_f32_16x16x32_bf16 v[4:7], v[128:131], a[4:7], 0\n"
      "v_max_u32_dpp v206, v206, v206 quad_perm:[1,0,3,2] row_mask:0xf bank_mask:0xf\n"
      "s_nop 1\n"
      "v_max_u32_dpp v206, v206, v206 quad_perm:[2,3,0,1] row_mask:0xf bank_mask:0xf\n"
      "v_mfma_f32_16x16x32_bf16 v[8:11], v[128:131], a[8:11], 0\n"
      "global_load_dwordx4 v[156:159], v241, s[10:11] offset:-3072 sc1\n"
      "v_max_u32_dpp v206, v206, v206 row_half_mirror row_mask:0xf bank_mask:0xf\n"
      "v_mfma_f32_16x16x32_bf16 v[12:15], v[128:131], a[12:15], 0\n"
      "s_nop 0\n"
      "v_max_u32_dpp v206, v206, v206 row_mirror row_mask:0xf bank_mask:0xf\n"
      "v_mfma_f32_16x16x32_bf16 v[16:19], v[128:131], a[16:19], 0\n"
      "global_load_dwordx4 v[160:163], v241, s[10:11] offset:-2048 sc1\n"
      "v_mov_b32 v205, v206\n"
      "v_mfma_f32_16x16x32_bf16 v[20:23], v[128:131], a[20:23], 0\n"
      "s_nop 0\n"
      "v_permlane16_swap_b32 v206, v205\n"
      "v_mfma_f32_16x16x32_bf16 v[24:27], v[128:131], a[24:27], 0\n"
      "global_load_dwordx4 v[164:167], v241, s[72:73] offset:-4096 sc1\n"
      "v_max_u32 v206, v206, v205\n"
      "v_mfma_f32_16x16x32_bf16 v[28:31], v[128:131], a[28:31], 0\n"
      "v_mov_b32 v205, v206\n"
      "s_nop 1\n"
      "v_permlane32_swap_b32 v206, v205\n"
      "s_waitcnt vmcnt(12)\n"
      "v_mfma_f32_16x16x32_bf16 v[32:35], v[132:135], a[0:3], 0\n"
      "global_load_dwordx4 v[168:171], v241, s[72:73] offset:-3072 sc1\n"
      "v_max_u32 v206, v206, v205\n"
      "v_mfma_f32_16x16x32_bf16 v[36:39], v[132:135], a[4:7], 0\n"
      "v_readfirstlane_b32 s40, v206\n"
      "s_nop 1\n"
      "s_andn2_b32 s42, 0x7ff, s40\n"
      "v_mfma_f32_16x16x32_bf16 v[40:43], v[132:135], a[8:11], 0\n"
      "global_load_dwordx4 v[172:175], v241, s[72:73] offset:-2048 sc1\n"
      "s_lshl2_add_u32 s43, s32, s33\n"
      "s_cmp_eq_u32 s43, 960\n"
      "s_cbranch_scc0 .Ls_e8_%=\n"
      "s_lshl_b32 s43, s42, 10\n"
      "v_add_u32 v208, s43, v242\n"
      "global_load_dwordx4 v[216:219], v208, s[16:17]\n"
      "global_load_dwordx4 v[220:223], v242, s[24:25]\n"
      ".Ls_e8_%=:\n"
      "v_mfma_f32_16x16x32_bf16 v[44:47], v[132:135], a[12:15], 0\n"
      "v_mfma_f32_16x16x32_bf16 v[48:51], v[132:135], a[16:19], 0\n"
      "global_load_dwordx4 v[176:179], v240, s[14:15] offset:-4096 sc1\n"
      "v_mfma_f32_16x16x32_bf16 v[52:55], v[132:135], a[20:23], 0\n"
      "v_mfma_f32_16x16x32_bf16 v[56:59], v[132:135], a[24:27], 0\n"
      "global_load_dwordx4 v[180:183], v240, s[14:15] offset:-3072 sc1\n"
      "v_mfma_f32_16x16x32_bf16 v[60:63], v[132:135], a[28:31], 0\n"
      "s_waitcnt vmcnt(14)\n"
      "v_mfma_f32_16x16x32_bf16 v[64:67], v[136:139], a[0:3], 0\n"
      "global_load_dwordx4 v[184:187], v240, s[14:15] offset:-2048 sc1\n"
      "global_load_dwordx4 v[188:191], v240, s[74:75] offset:-4096 sc1\n"
      "v_mfma_f32_16x16x32_bf16 v[68:71], v[136:139], a[4:7], 0\n"
      "global_load_dwordx4 v[128:131], v240, s[74:75] offset:-3072 sc1\n"
      "global_load_dwordx4 v[132:135], v240, s[74:75] offset:-2048 sc1\n"
      "v_mfma_f32_16x16x32_bf16 v[72:75], v[136:139], a[8:11], 0\n"
      "s_waitcnt lgkmcnt(8)\n"
      "s_xor_b32 s70, s78, 384\n"
      "s_add_u32 s70, s8, s70\n"
      "s_addc_u32 s71, s9, 0\n"
      "s_add_u32 m0, s41, 0\n"
      "v_mfma_f32_16x16x32_bf16 v[76:79], v[136:139], a[12:15], 0\n"
      "global_load_lds_dwordx4 v244, s[70:71] offset:0\n"
      "v_mfma_f32_16x16x32_bf16 v[80:83], v[136:139], a[16:19], 0\n"
      "v_mfma_f32_16x16x32_bf16 v[84:87], v[136:139], a[20:23], 0\n"
      "global_load_lds_dwordx4 v245, s[70:71] offset:1024\n"
      "ds_read_b128 a[144:147], v248 offset:12288\n"
      "v_mfma_f32_16x16x32_bf16 v[88:91], v[136:139], a[24:27], 0\n"
      "ds_read_b128 a[148:151], v248 offset:14336\n"
      "ds_read_b128 a[176:179], v249 offset:12288\n"
      "v_mfma_f32_16x16x32_bf16 v[92:95], v[136:139], a[28:31], 0\n"
      "global_load_lds_dwordx4 v246, s[70:71] offset:2048\n"
      "global_load_dwordx4 v[136:139], v241, s[14:15] offset:-4096 sc1\n"
      "v_mfma_f32_16x16x32_bf16 v[0:3], v[140:143], a[32:35], v[0:3]\n"
      "ds_read_b128 a[180:183], v249 offset:14336\n"
      "ds_read_b128 a[152:155], v250 offset:0\n"
      "v_mfma_f32_16x16x32_bf16 v[4:7], v[140:143], a[36:39], v[4:7]\n"
      "global_load_lds_dwordx4 v247, s[70:71] offset:3072\n"
      "ds_read_b128 a[156:159], v250 offset:2048\n"
      "v_mfma_f32_16x16x32_bf16 v[8:11], v[140:143], a[40:43], v[8:11]\n"
      "s_waitcnt lgkmcnt(10)\n"
      "s_xor_b32 s70, s79, 384\n"
      "s_add_u32 s70, s8, s70\n"
      "s_addc_u32 s71, s9, 0\n"
      "s_add_u32 m0, s41, 4096\n"
      "v_mfma_f32_16x16x32_bf16 v[12:15], v[140:143], a[44:47], v[12:15]\n"
      "global_load_lds_dwordx4 v244, s[70:71] offset:0\n"
      "ds_read_b128 a[184:187], v251 offset:0\n"
      "v_mfma_f32_16x16x32_bf16 v[16:19], v[140:143], a[48:51], v[16:19]\n"
      "ds_read_b128 a[188:191], v251 offset:2048\n"
      "ds_read_b128 a[192:195], v250 offset:4096\n"
      "v_mfma_f32_16x16x32_bf16 v[20:23], v[140:143], a[52:55], v[20:23]\n"
      "global_load_lds_dwordx4 v245, s[70:71] offset:1024\n"
      "ds_read_b128 a[196:199], v250 offset:6144\n"
      "v_mfma_f32_16x16x32_bf16 v[24:27], v[140:143], a[56:59], v[24:27]\n"
      "ds_read_b128 a[224:227], v251 offset:4096\n"
      "ds_read_b128 a[228:231], v251 offset:6144\n"
      "v_mfma_f32_16x16x32_bf16 v[28:31], v[140:143], a[60:63], v[28:31]\n"
      "s_waitcnt vmcnt(23)\n"
      "global_load_lds_dwordx4 v246, s[70:71] offset:2048\n"
      "global_load_dwordx4 v[140:143], v241, s[74:75] offset:-4096 sc1\n"
      "v_mfma_f32_16x16x32_bf16 v[32:35], v[144:147], a[32:35], v[32:35]\n"
      "s_waitcnt vmcnt(21)\n"
      "ds_read_b128 a[200:203], v250 offset:8192\n"
      "v_mfma_f32_16x16x32_bf16 v[36:39], v[144:147], a[36:39], v[36:39]\n"
      "global_load_lds_dwordx4 v247, s[70:71] offset:3072\n"
      "ds_read_b128 a[204:207], v250 offset:10240\n"
      "v_mfma_f32_16x16x32_bf16 v[40:43], v[144:147], a[40:43], v[40:43]\n"
      "ds_read_b128 a[232:235], v251 offset:8192\n"
      "ds_read_b128 a[236:239], v251 offset:10240\n"
      "v_mfma_f32_16x16x32_bf16 v[44:47], v[144:147], a[44:47], v[44:47]\n"
      "v_mfma_f32_16x16x32_bf16 v[48:51], v[144:147], a[48:51], v[48:51]\n"
      "v_mfma_f32_16x16x32_bf16 v[52:55], v[144:147], a[52:55], v[52:55]\n"
      "v_mfma_f32_16x16x32_bf16 v[56:59], v[144:147], a[56:59], v[56:59]\n"
      "v_mfma_f32_16x16x32_bf16 v[60:63], v[144:147], a[60:63], v[60:63]\n"
      "global_load_dwordx4 v[144:147], v241, s[14:15] offset:-3072 sc1\n"
      "v_mfma_f32_16x16x32_bf16 v[64:67], v[148:151], a[32:35], v[64:67]\n"
      "v_mfma_f32_16x16x32_bf16 v[68:71], v[148:151], a[36:39], v[68:71]\n"
      "v_mfma_f32_16x16x32_bf16 v[72:75], v[148:151], a[40:43], v[72:75]\n"
      "v_mfma_f32_16x16x32_bf16 v[76:79], v[148:151], a[44:47], v[76:79]\n"
      "v_mfma_f32_16x16x32_bf16 v[80:83], v[148:151], a[48:51], v[80:83]\n"
      "v_mfma_f32_16x16x32_bf16 v[84:87], v[148:151], a[52:55], v[84:87]\n"
      "v_mfma_f32_16x16x32_bf16 v[88:91], v[148:151], a[56:59], v[88:91]\n"
      "v_mfma_f32_16x16x32_bf16 v[92:95], v[148:151], a[60:63], v[92:95]\n"
      "s_waitcnt vmcnt(21)\n"
      "global_load_dwordx4 v[148:151], v241, s[74:75] offset:-3072 sc1\n"
      "s_waitcnt vmcnt(7)\n"
      "ds_read_b128 a[208:211], v248 offset:0\n"
      "v_mfma_f32_16x16x32_bf16 v[0:3], v[152:155], a[64:67], v[0:3]\n"
      "ds_read_b128 a[212:215], v248 offset:2048\n"
      "ds_read_b128 a[240:243], v249 offset:0\n"
      "v_mfma_f32_16x16x32_bf16 v[4:7], v[152:155], a[68:71], v[4:7]\n"
      "ds_read_b128 a[244:247], v249 offset:2048\n"
      "v_mfma_f32_16x16x32_bf16 v[8:11], v[152:155], a[72:75], v[8:11]\n"
      "v_mfma_f32_16x16x32_bf16 v[12:15], v[152:155], a[76:79], v[12:15]\n"
      "v_mfma_f32_16x16x32_bf16 v[16:19], v[152:155], a[80:83], v[16:19]\n"
      "s_lshl_b32 s54, s42, 10\n"
      "s_lshl_b32 s55, s83, 7\n"
      "s_add_u32 s54, s54, s55\n"
      "v_mfma_f32_16x16x32_bf16 v[20:23], v[152:155], a[84:87], v[20:23]\n"
      "v_and_b32 v224, 48, v243\n"
      "v_add_u32 v224, s54, v224\n"
      "v_mfma_f32_16x16x32_bf16 v[24:27], v[152:155], a[88:91], v[24:27]\n"
      "global_load_dwordx4 v[192:195], v224, s[16:17]\n"
      "global_load_dwordx4 v[196:199], v224, s[16:17] offset:64\n"
      "v_mfma_f32_16x16x32_bf16 v[28:31], v[152:155], a[92:95], v[28:31]\n"
      "global_load_dwordx4 v[152:155], v241, s[14:15] offset:-2048 sc1\n"
      "s_waitcnt vmcnt(5)\n"
      "ds_read_b128 a[216:219], v248 offset:4096\n"
      "v_mfma_f32_16x16x32_bf16 v[32:35], v[156:159], a[64:67], v[32:35]\n"
      "ds_read_b128 a[220:223], v248 offset:6144\n"
      "ds_read_b128 a[248:251], v249 offset:4096\n"
      "v_mfma_f32_16x16x32_bf16 v[36:39], v[156:159], a[68:71], v[36:39]\n"
      "ds_read_b128 a[252:255], v249 offset:6144\n"
      "v_mfma_f32_16x16x32_bf16 v[40:43], v[156:159], a[72:75], v[40:43]\n"
      "v_mfma_f32_16x16x32_bf16 v[44:47], v[156:159], a[76:79], v[44:47]\n"
      "v_mfma_f32_16x16x32_bf16 v[48:51], v[156:159], a[80:83], v[48:51]\n"
      "v_mfma_f32_16x16x32_bf16 v[52:55], v[156:159], a[84:87], v[52:55]\n"
      "v_mfma_f32_16x16x32_bf16 v[56:59], v[156:159], a[88:91], v[56:59]\n"
      "v_mfma_f32_16x16x32_bf16 v[60:63], v[156:159], a[92:95], v[60:63]\n"
      "global_load_dwordx4 v[156:159], v241, s[74:75] offset:-2048 sc1\n"
      "v_mfma_f32_16x16x32_bf16 v[64:67], v[160:163], a[64:67], v[64:67]\n"
      "v_mfma_f32_16x16x32_bf16 v[68:71], v[160:163], a[68:71], v[68:71]\n"
      "v_mfma_f32_16x16x32_bf16 v[72:75], v[160:163], a[72:75], v[72:75]\n"
      "v_mfma_f32_16x16x32_bf16 v[76:79], v[160:163], a[76:79], v[76:79]\n"
      "v_mfma_f32_16x16x32_bf16 v[80:83], v[160:163], a[80:83], v[80:83]\n"
      "v_mfma_f32_16x16x32_bf16 v[84:87], v[160:163], a[84:87], v[84:87]\n"
      "v_mfma_f32_16x16x32_bf16 v[88:91], v[160:163], a[88:91], v[88:91]\n"
      "s_getpc_b64 s[46:47]\n"
      ".Ls_c9_%=:\n"
      "s_lshr_b32 s44, s32, 4\n"
      "s_lshl2_add_u32 s44, s44, s33\n"
      "s_lshl_b32 s44, s44, 10\n"
      "s_sub_u32 s46, s46, .Ls_c9_%=-.Lbegin_%=\n"
      "s_subb_u32 s47, s47, 0\n"
      "v_add_u32 v238, s44, v242\n"
      "v_min_u32 v238, .Lend_%=-.Lbegin_%=-16, v238\n"
      "global_load_dword v205, v238, s[46:47]\n"
      "v_mfma_f32_16x16x32_bf16 v[92:95], v[160:163], a[92:95], v[92:95]\n"
      "s_min_u32 s43, s80, 27\n"
      "v_lshrrev_b32 v202, 3, v243\n"
      "v_and_b32 v203, 0x70, v242\n"
      "s_movk_i32 s44, 3584\n"
      "v_mad_u32_u24 v200, v202, s44, v203\n"
      "v_add_u32 v201, 27648, v200\n"
      "v_add_u32 v202, 55296, v200\n"
      "v_add_u32 v203, 82944, v200\n"
      "s_bitcmp1_b32 s33, 0\n"
      "s_cselect_b64 s[70:71], s[28:29], s[26:27]\n"
      "s_mul_i32 s44, s83, 229376\n"
      "s_lshl_b32 s45, s43, 7\n"
      "s_add_u32 s44, s44, s45\n"
      "s_add_u32 s70, s70, s44\n"
      "s_addc_u32 s71, s71, 0\n"
      "s_lshl_b32 s45, s33, 13\n"
      "s_add_u32 m0, s45, 131072\n"
      "v_mfma_f32_16x16x32_bf16 v[0:3], v[164:167], a[96:99], v[0:3]\n"
      "global_load_lds_dwordx4 v200, s[70:71] offset:0\n"
      "v_mfma_f32_16x16x32_bf16 v[4:7], v[164:167], a[100:103], v[4:7]\n"
      "global_load_lds_dwordx4 v201, s[70:71] offset:1024\n"
      "v_mfma_f32_16x16x32_bf16 v[8:11], v[164:167], a[104:107], v[8:11]\n"
      "global_load_lds_dwordx4 v202, s[70:71] offset:2048\n"
      "v_mfma_f32_16x16x32_bf16 v[12:15], v[164:167], a[108:111], v[12:15]\n"
      "global_load_lds_dwordx4 v203, s[70:71] offset:3072\n"
      "v_mfma_f32_16x16x32_bf16 v[16:19], v[164:167], a[112:115], v[16:19]\n"
      "s_add_u32 s70, s70, 114688\n"
      "s_addc_u32 s71, s71, 0\n"
      "s_add_u32 m0, m0, 4096\n"
      "v_mfma_f32_16x16x32_bf16 v[20:23], v[164:167], a[116:119], v[20:23]\n"
      "global_load_lds_dwordx4 v200, s[70:71] offset:0\n"
      "v_mfma_f32_16x16x32_bf16 v[24:27], v[164:167], a[120:123], v[24:27]\n"
      "global_load_lds_dwordx4 v201, s[70:71] offset:1024\n"
      "v_mfma_f32_16x16x32_bf16 v[28:31], v[164:167], a[124:127], v[28:31]\n"
      "global_load_lds_dwordx4 v202, s[70:71] offset:2048\n"
      "v_mfma_f32_16x16x32_bf16 v[32:35], v[168:171], a[96:99], v[32:35]\n"
      "global_load_lds_dwordx4 v203, s[70:71] offset:3072\n"
      "v_mfma_f32_16x16x32_bf16 v[36:39], v[168:171], a[100:103], v[36:39]\n"
      "s_min_u32 s43, s80, 27\n"
      "s_lshr_b32 s44, s33, 1\n"
      "s_lshl1_add_u32 s44, s43, s44\n"
      "s_lshl_b32 s44, s44, 15\n"
      "s_and_b32 s45, s33, 1\n"
      "s_lshl1_add_u32 s45, s81, s45\n"
      "s_lshl_b32 s45, s45, 7\n"
      "s_add_u32 s44, s44, s45\n"
      "v_and_b32 v239, 31, v243\n"
      "v_lshl_add_u32 v239, v239, 10, s44\n"
      "global_load_dword v207, v239, s[30:31]\n"
      "v_mfma_f32_16x16x32_bf16 v[40:43], v[168:171], a[104:107], v[40:43]\n"
      "s_sub_u32 s48, s33, 1\n"
      "s_lshl2_add_u32 s43, s32, s33\n"
      "s_cmp_eq_u32 s43, 960\n"
      "s_cbranch_scc0 .Ls_e10_%=\n"
      "s_ashr_i32 s43, s40, 31\n"
      "s_not_b32 s43, s43\n"
      "s_or_b32 s43, s43, 0x80000000\n"
      "s_xor_b32 s43, s43, s40\n"
      "s_and_b32 s43, s43, 0xffff0000\n"
      "v_lshlrev_b32 v208, 16, v216\n"
      "v_lshlrev_b32 v209, 16, v220\n"
      "v_and_b32 v216, 0xffff0000, v216\n"
      "v_and_b32 v220, 0xffff0000, v220\n"
      "v_mul_f32 v208, v208, v209\n"
      "v_mul_f32 v210, v216, v220\n"
      "v_cvt_pk_bf16_f32 v208, v208, v210\n"
      "v_lshlrev_b32 v209, 16, v208\n"
      "v_and_b32 v208, 0xffff0000, v208\n"
      "v_add_f32 v211, v208, v209\n"
      "v_lshlrev_b32 v208, 16, v217\n"
      "v_lshlrev_b32 v209, 16, v221\n"
      "v_and_b32 v217, 0xffff0000, v217\n"
      "v_and_b32 v221, 0xffff0000, v221\n"
      "v_mul_f32 v208, v208, v209\n"
      "v_mul_f32 v210, v217, v221\n"
      "v_cvt_pk_bf16_f32 v208, v208, v210\n"
      "v_lshlrev_b32 v209, 16, v208\n"
      "v_and_b32 v208, 0xffff0000, v208\n"
      "v_add_f32 v208, v208, v209\n"
      "v_add_f32 v211, v211, v208\n"
      "v_lshlrev_b32 v208, 16, v218\n"
      "v_lshlrev_b32 v209, 16, v222\n"
      "v_and_b32 v218, 0xffff0000, v218\n"
      "v_and_b32 v222, 0xffff0000, v222\n"
      "v_mul_f32 v208, v208, v209\n"
      "v_mul_f32 v210, v218, v222\n"
      "v_cvt_pk_bf16_f32 v208, v208, v210\n"
      "v_lshlrev_b32 v209, 16, v208\n"
      "v_and_b32 v208, 0xffff0000, v208\n"
      "v_add_f32 v208, v208, v209\n"
      "v_add_f32 v211, v211, v208\n"
      "v_lshlrev_b32 v208, 16, v219\n"
      "v_lshlrev_b32 v209, 16, v223\n"
      "v_and_b32 v219, 0xffff0000, v219\n"
      "v_and_b32 v223, 0xffff0000, v223\n"
      "v_mul_f32 v208, v208, v209\n"
      "v_mul_f32 v210, v219, v223\n"
      "v_cvt_pk_bf16_f32 v208, v208, v210\n"
      "v_lshlrev_b32 v209, 16, v208\n"
      "v_and_b32 v208, 0xffff0000, v208\n"
      "v_add_f32 v208, v208, v209\n"
      "v_add_f32 v211, v211, v208\n"
      "s_nop 1\n"
      "v_add_f32_dpp v211, v211, v211 quad_perm:[1,0,3,2] row_mask:0xf bank_mask:0xf\n"
      "s_nop 1\n"
      "v_add_f32_dpp v211, v211, v211 quad_perm:[2,3,0,1] row_mask:0xf bank_mask:0xf\n"
      "s_nop 1\n"
      "v_add_f32_dpp v211, v211, v211 row_half_mirror row_mask:0xf bank_mask:0xf\n"
      "s_nop 1\n"
      "v_add_f32_dpp v211, v211, v211 row_mirror row_mask:0xf bank_mask:0xf\n"
      "v_mov_b32 v208, v211\n"
      "s_nop 1\n"
      "v_permlane16_swap_b32 v211, v208\n"
      "v_add_f32 v211, v211, v208\n"
      "v_mov_b32 v208, v211\n"
      "s_nop 1\n"
      "v_permlane32_swap_b32 v211, v208\n"
      "v_add_f32 v211, v211, v208\n"
      "v_cvt_pk_bf16_f32 v211, v211, v211\n"
      "v_and_b32 v211, 0xffff0000, v211\n"
      "v_writelane_b32 v211, s43, 1\n"
      "v_mul_f32 v211, 0xbfb8aa3b, v211\n"
      "v_exp_f32 v211, v211\n"
      "s_nop 0\n"
      "v_add_f32 v211, 1.0, v211\n"
      "v_rcp_f32 v211, v211\n"
      "s_nop 0\n"
      "v_cvt_pk_bf16_f32 v211, v211, v211\n"
      "v_and_b32 v211, 0xffff0000, v211\n"
      "s_nop 0\n"
      "v_readfirstlane_b32 s39, v211\n"
      "v_readlane_b32 s84, v211, 1\n"
      "s_nop 0\n"
      "v_mov_b32 v216, s39\n"
      "v_mov_b32 v217, s84\n"
      "v_mov_b32 v218, s40\n"
      "v_mov_b32 v219, s37\n"
      "s_mov_b64 exec, 0xff\n"
      "global_store_dwordx4 v242, v[216:219], s[58:59] offset:1024 sc1\n"
      "s_mov_b64 exec, -1\n"
      ".Ls_e10_%=:\n"
      "v_mfma_f32_16x16x32_bf16 v[44:47], v[168:171], a[108:111], v[44:47]\n"
      "s_and_b32 s48, s48, 3\n"
      "s_lshl_b32 s48, s48, 14\n"
      "v_lshl_add_u32 v244, v243, 2, s48\n"
      "v_mfma_f32_16x16x32_bf16 v[48:51], v[168:171], a[112:115], v[48:51]\n"
      "s_sub_u32 s48, s33, 2\n"
      "s_and_b32 s48, s48, 3\n"
      "s_lshl_b32 s48, s48, 14\n"
      "v_mfma_f32_16x16x32_bf16 v[52:55], v[168:171], a[116:119], v[52:55]\n"
      "s_mov_b32 m0, s41\n"
      "v_lshl_add_u32 v245, v243, 2, s48\n"
      "s_sub_u32 s48, s33, 3\n"
      "v_mfma_f32_16x16x32_bf16 v[56:59], v[168:171], a[120:123], v[56:59]\n"
      "s_and_b32 s48, s48, 3\n"
      "s_lshl_b32 s48, s48, 14\n"
      "v_lshl_add_u32 v246, v243, 2, s48\n"
      "v_mfma_f32_16x16x32_bf16 v[60:63], v[168:171], a[124:127], v[60:63]\n"
      "v_lshrrev_b32 v201, 4, v243\n"
      "v_bfe_u32 v224, v243, 2, 2\n"
      "v_mfma_f32_16x16x32_bf16 v[64:67], v[172:175], a[96:99], v[64:67]\n"
      "v_lshl_or_b32 v201, v201, 3, v224\n"
      "v_and_b32 v224, 3, v243\n"
      "v_mfma_f32_16x16x32_bf16 v[68:71], v[172:175], a[100:103], v[68:71]\n"
      "v_lshlrev_b32 v224, 3, v224\n"
      "v_lshl_or_b32 v201, v201, 7, v224\n"
      "v_mfma_f32_16x16x32_bf16 v[72:75], v[172:175], a[104:107], v[72:75]\n"
      "s_mov_b32 s64, 0xffff0000\n"
      "s_mov_b32 s65, 0xffff0000\n"
      "s_mov_b32 s66, -1\n"
      "v_mfma_f32_16x16x32_bf16 v[76:79], v[172:175], a[108:111], v[76:79]\n"
      "s_mov_b32 s67, 0\n"
      "s_lshl_b32 s54, s33, 13\n"
      "s_add_u32 s54, s54, 131072\n"
      "v_mfma_f32_16x16x32_bf16 v[80:83], v[172:175], a[112:115], v[80:83]\n"
      "v_add_u32 v202, s54, v201\n"
      "s_and_b32 s54, s33, 1\n"
      "v_mfma_f32_16x16x32_bf16 v[84:87], v[172:175], a[116:119], v[84:87]\n"
      "s_lshl3_add_u32 s54, s54, s83\n"
      "s_lshl_b32 s54, s54, 8\n"
      "s_lshl_b32 s55, s80, 13\n"
      "v_mfma_f32_16x16x32_bf16 v[88:91], v[172:175], a[120:123], v[88:91]\n"
      "s_add_u32 s55, s55, 262144\n"
      "s_add_u32 s54, s54, s55\n"
      "v_bfe_u32 v224, v243, 4, 1\n"
      "v_mfma_f32_16x16x32_bf16 v[92:95], v[172:175], a[124:127], v[92:95]\n"
      "global_load_dword v252, v253, s[58:59] offset:128 sc1\n"
      "v_and_b32 v203, 0xf0, v242\n"
      "v_mfma_f32_16x16x32_bf16 v[0:3], v[176:179], a[128:131], v[0:3]\n"
      "v_lshl_or_b32 v200, v224, 12, v203\n"
      "v_add_u32 v200, s54, v200\n"
      "v_mfma_f32_16x16x32_bf16 v[4:7], v[176:179], a[132:135], v[4:7]\n"
      "v_lshl_or_b32 v203, v224, 14, v203\n"
      "s_waitcnt lgkmcnt(15)\n"
      "v_mfma_f32_16x16x32_bf16 v[8:11], v[176:179], a[136:139], v[8:11]\n"
      "v_mfma_f32_16x16x32_bf16 v[12:15], v[176:179], a[140:143], v[12:15]\n"
      "v_mfma_f32_16x16x32_bf16 v[16:19], v[176:179], a[144:147], v[16:19]\n"
      "v_mfma_f32_16x16x32_bf16 v[20:23], v[176:179], a[148:151], v[20:23]\n"
      "v_mfma_f32_16x16x32_bf16 v[24:27], v[176:179], a[152:155], v[24:27]\n"
      "v_mfma_f32_16x16x32_bf16 v[28:31], v[176:179], a[156:159], v[28:31]\n"
      "v_mfma_f32_16x16x32_bf16 v[32:35], v[180:183], a[128:131], v[32:35]\n"
      "v_mfma_f32_16x16x32_bf16 v[36:39], v[180:183], a[132:135], v[36:39]\n"
      "v_mfma_f32_16x16x32_bf16 v[40:43], v[180:183], a[136:139], v[40:43]\n"
      "v_mfma_f32_16x16x32_bf16 v[44:47], v[180:183], a[140:143], v[44:47]\n"
      "v_mfma_f32_16x16x32_bf16 v[48:51], v[180:183], a[144:147], v[48:51]\n"
      "v_mfma_f32_16x16x32_bf16 v[52:55], v[180:183], a[148:151], v[52:55]\n"
      "v_mfma_f32_16x16x32_bf16 v[56:59], v[180:183], a[152:155], v[56:59]\n"
      "v_mfma_f32_16x16x32_bf16 v[60:63], v[180:183], a[156:159], v[60:63]\n"
      "v_mfma_f32_16x16x32_bf16 v[64:67], v[184:187], a[128:131], v[64:67]\n"
      "v_mfma_f32_16x16x32_bf16 v[68:71], v[184:187], a[132:135], v[68:71]\n"
      "v_mfma_f32_16x16x32_bf16 v[72:75], v[184:187], a[136:139], v[72:75]\n"
      "v_mfma_f32_16x16x32_bf16 v[76:79], v[184:187], a[140:143], v[76:79]\n"
      "v_mfma_f32_16x16x32_bf16 v[80:83], v[184:187], a[144:147], v[80:83]\n"
      "v_mfma_f32_16x16x32_bf16 v[84:87], v[184:187], a[148:151], v[84:87]\n"
      "v_mfma_f32_16x16x32_bf16 v[88:91], v[184:187], a[152:155], v[88:91]\n"
      "v_mfma_f32_16x16x32_bf16 v[92:95], v[184:187], a[156:159], v[92:95]\n"
      "v_mfma_f32_16x16x32_bf16 v[0:3], v[188:191], a[160:163], v[0:3]\n"
      "v_mfma_f32_16x16x32_bf16 v[4:7], v[188:191], a[164:167], v[4:7]\n"
      "v_mfma_f32_16x16x32_bf16 v[8:11], v[188:191], a[168:171], v[8:11]\n"
      "v_mfma_f32_16x16x32_bf16 v[12:15], v[188:191], a[172:175], v[12:15]\n"
      "v_mfma_f32_16x16x32_bf16 v[16:19], v[188:191], a[176:179], v[16:19]\n"
      "v_mfma_f32_16x16x32_bf16 v[20:23], v[188:191], a[180:183], v[20:23]\n"
      "v_mfma_f32_16x16x32_bf16 v[24:27], v[188:191], a[184:187], v[24:27]\n"
      "v_mfma_f32_16x16x32_bf16 v[28:31], v[188:191], a[188:191], v[28:31]\n"
      "v_mfma_f32_16x16x32_bf16 v[32:35], v[128:131], a[160:163], v[32:35]\n"
      "v_mfma_f32_16x16x32_bf16 v[36:39], v[128:131], a[164:167], v[36:39]\n"
      "v_mfma_f32_16x16x32_bf16 v[40:43], v[128:131], a[168:171], v[40:43]\n"
      "v_mfma_f32_16x16x32_bf16 v[44:47], v[128:131], a[172:175], v[44:47]\n"
      "v_mfma_f32_16x16x32_bf16 v[48:51], v[128:131], a[176:179], v[48:51]\n"
      "v_mfma_f32_16x16x32_bf16 v[52:55], v[128:131], a[180:183], v[52:55]\n"
      "v_mfma_f32_16x16x32_bf16 v[56:59], v[128:131], a[184:187], v[56:59]\n"
      "v_mfma_f32_16x16x32_bf16 v[60:63], v[128:131], a[188:191], v[60:63]\n"
      "v_mfma_f32_16x16x32_bf16 v[64:67], v[132:135], a[160:163], v[64:67]\n"
      "v_mfma_f32_16x16x32_bf16 v[68:71], v[132:135], a[164:167], v[68:71]\n"
      "v_mfma_f32_16x16x32_bf16 v[72:75], v[132:135], a[168:171], v[72:75]\n"
      "v_mfma_f32_16x16x32_bf16 v[76:79], v[132:135], a[172:175], v[76:79]\n"
      "v_mfma_f32_16x16x32_bf16 v[80:83], v[132:135], a[176:179], v[80:83]\n"
      "v_mfma_f32_16x16x32_bf16 v[84:87], v[132:135], a[180:183], v[84:87]\n"
      "v_mfma_f32_16x16x32_bf16 v[88:91], v[132:135], a[184:187], v[88:91]\n"
      "v_mfma_f32_16x16x32_bf16 v[92:95], v[132:135], a[188:191], v[92:95]\n"
      "v_mfma_f32_16x16x32_bf16 v[0:3], v[136:139], a[192:195], v[0:3]\n"
      "s_waitcnt lgkmcnt(14)\n"
      "v_mfma_f32_16x16x32_bf16 v[4:7], v[136:139], a[196:199], v[4:7]\n"
      "s_waitcnt lgkmcnt(11)\n"
      "v_mfma_f32_16x16x32_bf16 v[8:11], v[136:139], a[200:203], v[8:11]\n"
      "s_waitcnt lgkmcnt(10)\n"
      "v_mfma_f32_16x16x32_bf16 v[12:15], v[136:139], a[204:207], v[12:15]\n"
      "s_waitcnt lgkmcnt(7)\n"
      "v_mfma_f32_16x16x32_bf16 v[16:19], v[136:139], a[208:211], v[16:19]\n"
      "s_waitcnt lgkmcnt(6)\n"
      "v_mfma_f32_16x16x32_bf16 v[20:23], v[136:139], a[212:215], v[20:23]\n"
      "s_waitcnt lgkmcnt(3)\n"
      "v_mfma_f32_16x16x32_bf16 v[24:27], v[136:139], a[216:219], v[24:27]\n"
      "s_waitcnt lgkmcnt(2)\n"
      "v_mfma_f32_16x16x32_bf16 v[28:31], v[136:139], a[220:223], v[28:31]\n"
      "v_mfma_f32_16x16x32_bf16 v[0:3], v[140:143], a[224:227], v[0:3]\n"
      "v_mfma_f32_16x16x32_bf16 v[4:7], v[140:143], a[228:231], v[4:7]\n"
      "s_waitcnt vmcnt(0)\n"
      "v_cmp_ne_u32 vcc, s37, v252\n"
      "s_nop 1\n"
      "s_cbranch_vccnz .Ls_s12_%=\n"
      ".Ls_o13_%=:\n"
      "v_mfma_f32_16x16x32_bf16 v[8:11], v[140:143], a[232:235], v[8:11]\n"
      "v_mfma_f32_16x16x32_bf16 v[12:15], v[140:143], a[236:239], v[12:15]\n"
      "global_load_dwordx4 v[160:163], v240, s[10:11] offset:-1024 sc1\n"
      "global_load_dwordx4 v[164:167], v240, s[72:73] offset:-1024 sc1\n"
      "v_mfma_f32_16x16x32_bf16 v[16:19], v[140:143], a[240:243], v[16:19]\n"
      "global_load_dwordx4 v[168:171], v241, s[10:11] offset:-1024 sc1\n"
      "global_load_dwordx4 v[172:175], v241, s[72:73] offset:-1024 sc1\n"
      "v_mfma_f32_16x16x32_bf16 v[20:23], v[140:143], a[244:247], v[20:23]\n"
      "global_load_dwordx4 v[176:179], v240, s[14:15] offset:-1024 sc1\n"
      "global_load_dwordx4 v[180:183], v240, s[74:75] offset:-1024 sc1\n"
      "s_waitcnt lgkmcnt(1)\n"
      "v_mfma_f32_16x16x32_bf16 v[24:27], v[140:143], a[248:251], v[24:27]\n"
      "global_load_dwordx4 v[184:187], v241, s[14:15] offset:-1024 sc1\n"
      "v_cvt_pk_bf16_f32 v8, v8, v9\n"
      "s_waitcnt lgkmcnt(0)\n"
      "v_mfma_f32_16x16x32_bf16 v[28:31], v[140:143], a[252:255], v[28:31]\n"
      "global_load_dwordx4 v[188:191], v241, s[74:75] offset:-1024 sc1\n"
      "v_cvt_pk_bf16_f32 v9, v10, v11\n"
      "v_mfma_f32_16x16x32_bf16 v[32:35], v[144:147], a[192:195], v[32:35]\n"
      "global_load_dwordx4 v[128:131], v240, s[10:11] offset:0 sc1\n"
      "global_load_dwordx4 v[132:135], v240, s[72:73] offset:0 sc1\n"
      "v_mfma_f32_16x16x32_bf16 v[36:39], v[144:147], a[196:199], v[36:39]\n"
      "global_load_dwordx4 v[136:139], v241, s[10:11] offset:0 sc1\n"
      "global_load_dwordx4 v[140:143], v241, s[72:73] offset:0 sc1\n"
      "v_mfma_f32_16x16x32_bf16 v[40:43], v[144:147], a[200:203], v[40:43]\n"
      "ds_read_b64_tr_b16 v[224:225], v202 offset:0\n"
      "ds_read_b64_tr_b16 v[226:227], v202 offset:512\n"
      "v_mfma_f32_16x16x32_bf16 v[44:47], v[144:147], a[204:207], v[44:47]\n"
      "ds_read_b64_tr_b16 v[228:229], v202 offset:4096\n"
      "ds_read_b64_tr_b16 v[230:231], v202 offset:4608\n"
      "v_mfma_f32_16x16x32_bf16 v[48:51], v[144:147], a[208:211], v[48:51]\n"
      "ds_read_b64_tr_b16 v[232:233], v202 offset:64\n"
      "ds_read_b64_tr_b16 v[234:235], v202 offset:576\n"
      "v_mfma_f32_16x16x32_bf16 v[52:55], v[144:147], a[212:215], v[52:55]\n"
      "ds_read_b64_tr_b16 v[236:237], v202 offset:4160\n"
      "ds_read_b64_tr_b16 v[238:239], v202 offset:4672\n"
      "v_mfma_f32_16x16x32_bf16 v[56:59], v[144:147], a[216:219], v[56:59]\n"
      "ds_write_addtid_b32 v8 offset:0\n"
      "v_cvt_pk_bf16_f32 v10, v12, v13\n"
      "v_mfma_f32_16x16x32_bf16 v[60:63], v[144:147], a[220:223], v[60:63]\n"
      "global_load_dwordx4 v[144:147], v240, s[14:15] offset:0 sc1\n"
      "ds_write_addtid_b32 v9 offset:256\n"
      "v_mfma_f32_16x16x32_bf16 v[32:35], v[148:151], a[224:227], v[32:35]\n"
      "s_waitcnt lgkmcnt(8)\n"
      "v_mfma_f32_16x16x32_bf16 v[248:251], v[192:195], v[224:227], 0\n"
      "v_mfma_f32_16x16x32_bf16 v[36:39], v[148:151], a[228:231], v[36:39]\n"
      "s_waitcnt lgkmcnt(6)\n"
      "v_mfma_f32_16x16x32_bf16 v[248:251], v[196:199], v[228:231], v[248:251]\n"
      "v_mfma_f32_16x16x32_bf16 v[40:43], v[148:151], a[232:235], v[40:43]\n"
      "s_waitcnt lgkmcnt(4)\n"
      "v_mfma_f32_16x16x32_bf16 v[224:227], v[192:195], v[232:235], 0\n"
      "v_mfma_f32_16x16x32_bf16 v[44:47], v[148:151], a[236:239], v[44:47]\n"
      "s_waitcnt lgkmcnt(2)\n"
      "v_mfma_f32_16x16x32_bf16 v[224:227], v[196:199], v[236:239], v[224:227]\n"
      "v_mfma_f32_16x16x32_bf16 v[48:51], v[148:151], a[240:243], v[48:51]\n"
      "v_cvt_pk_bf16_f32 v11, v14, v15\n"
      "ds_write_addtid_b32 v10 offset:512\n"
      "v_mfma_f32_16x16x32_bf16 v[52:55], v[148:151], a[244:247], v[52:55]\n"
      "v_cvt_pk_bf16_f32 v12, v16, v17\n"
      "ds_write_addtid_b32 v11 offset:768\n"
      "v_mfma_f32_16x16x32_bf16 v[56:59], v[148:151], a[248:251], v[56:59]\n"
      "s_nop 0\n"
      "v_cndmask_b32 v247, v248, v224, s[64:65]\n"
      "v_mfma_f32_16x16x32_bf16 v[60:63], v[148:151], a[252:255], v[60:63]\n"
      "global_load_dwordx4 v[148:151], v240, s[74:75] offset:0 sc1\n"
      "v_cvt_pk_bf16_f32 v13, v18, v19\n"
      "v_mfma_f32_16x16x32_bf16 v[64:67], v[152:155], a[192:195], v[64:67]\n"
      "ds_read_b64_tr_b16 v[224:225], v202 offset:32\n"
      "ds_read_b64_tr_b16 v[226:227], v202 offset:544\n"
      "v_mfma_f32_16x16x32_bf16 v[68:71], v[152:155], a[196:199], v[68:71]\n"
      "ds_read_b64_tr_b16 v[228:229], v202 offset:4128\n"
      "ds_read_b64_tr_b16 v[230:231], v202 offset:4640\n"
      "v_mfma_f32_16x16x32_bf16 v[72:75], v[152:155], a[200:203], v[72:75]\n"
      "ds_read_b64_tr_b16 v[232:233], v202 offset:96\n"
      "ds_read_b64_tr_b16 v[234:235], v202 offset:608\n"
      "v_mfma_f32_16x16x32_bf16 v[76:79], v[152:155], a[204:207], v[76:79]\n"
      "ds_read_b64_tr_b16 v[236:237], v202 offset:4192\n"
      "ds_read_b64_tr_b16 v[238:239], v202 offset:4704\n"
      "v_mfma_f32_16x16x32_bf16 v[80:83], v[152:155], a[208:211], v[80:83]\n"
      "ds_write_addtid_b32 v12 offset:1024\n"
      "v_cvt_pk_bf16_f32 v14, v20, v21\n"
      "v_mfma_f32_16x16x32_bf16 v[84:87], v[152:155], a[212:215], v[84:87]\n"
      "ds_write_addtid_b32 v13 offset:1280\n"
      "v_cvt_pk_bf16_f32 v15, v22, v23\n"
      "v_mfma_f32_16x16x32_bf16 v[88:91], v[152:155], a[216:219], v[88:91]\n"
      "s_waitcnt lgkmcnt(8)\n"
      "v_mfma_f32_16x16x32_bf16 v[248:251], v[192:195], v[224:227], 0\n"
      "v_mfma_f32_16x16x32_bf16 v[92:95], v[152:155], a[220:223], v[92:95]\n"
      "global_load_dwordx4 v[152:155], v241, s[14:15] offset:0 sc1\n"
      "s_waitcnt lgkmcnt(6)\n"
      "v_mfma_f32_16x16x32_bf16 v[248:251], v[196:199], v[228:231], v[248:251]\n"
      "v_mfma_f32_16x16x32_bf16 v[64:67], v[156:159], a[224:227], v[64:67]\n"
      "s_waitcnt lgkmcnt(4)\n"
      "v_mfma_f32_16x16x32_bf16 v[224:227], v[192:195], v[232:235], 0\n"
      "v_mfma_f32_16x16x32_bf16 v[68:71], v[156:159], a[228:231], v[68:71]\n"
      "s_waitcnt lgkmcnt(2)\n"
      "v_mfma_f32_16x16x32_bf16 v[224:227], v[196:199], v[236:239], v[224:227]\n"
      "v_mfma_f32_16x16x32_bf16 v[72:75], v[156:159], a[232:235], v[72:75]\n"
      "ds_write_addtid_b32 v14 offset:1536\n"
      "v_cvt_pk_bf16_f32 v16, v24, v25\n"
      "v_mfma_f32_16x16x32_bf16 v[76:79], v[156:159], a[236:239], v[76:79]\n"
      "ds_write_addtid_b32 v15 offset:1792\n"
      "v_cvt_pk_bf16_f32 v17, v26, v27\n"
      "v_mfma_f32_16x16x32_bf16 v[80:83], v[156:159], a[240:243], v[80:83]\n"
      "ds_write_addtid_b32 v16 offset:2048\n"
      "v_cndmask_b32 v250, v248, v224, s[64:65]\n"
      "v_mfma_f32_16x16x32_bf16 v[84:87], v[156:159], a[244:247], v[84:87]\n"
      "v_and_b32 v248, s86, v247\n"
      "v_mov_b32 v249, s37\n"
      "v_mfma_f32_16x16x32_bf16 v[88:91], v[156:159], a[248:251], v[88:91]\n"
      "v_and_b32 v250, s86, v250\n"
      "v_mov_b32 v251, s37\n"
      "v_mfma_f32_16x16x32_bf16 v[92:95], v[156:159], a[252:255], v[92:95]\n"
      "s_waitcnt vmcnt(13)\n"
      "global_load_dwordx4 v[156:159], v241, s[74:75] offset:0 sc1\n"
      "v_cvt_pk_bf16_f32 v18, v28, v29\n"
      "v_mfma_f32_16x16x32_bf16 v[96:99], v[160:163], a[0:3], 0\n"
      "s_mov_b64 exec, s[66:67]\n"
      "global_store_dwordx4 v200, v[248:251], s[58:59] sc1\n"
      "s_mov_b64 exec, -1\n"
      "v_mfma_f32_16x16x32_bf16 v[100:103], v[160:163], a[4:7], 0\n"
      "ds_write_addtid_b32 v17 offset:2304\n"
      "v_bfe_u32 v248, v243, 1, 3\n"
      "v_mfma_f32_16x16x32_bf16 v[104:107], v[160:163], a[8:11], 0\n"
      "v_lshrrev_b32 v249, 4, v243\n"
      "v_lshlrev_b32 v249, 1, v249\n"
      "v_mfma_f32_16x16x32_bf16 v[108:111], v[160:163], a[12:15], 0\n"
      "v_sub_u32 v248, v248, v249\n"
      "v_and_b32 v249, 16, v242\n"
      "v_mfma_f32_16x16x32_bf16 v[112:115], v[160:163], a[16:19], 0\n"
      "v_mov_b32 v254, 0x3f80\n"
      "v_lshlrev_b32 v249, v249, v254\n"
      "v_mfma_f32_16x16x32_bf16 v[116:119], v[160:163], a[20:23], 0\n"
      "v_cmp_eq_u32 vcc, 0, v248\n"
      "s_nop 1\n"
      "v_cndmask_b32 v250, 0, v249, vcc\n"
      "v_mfma_f32_16x16x32_bf16 v[120:123], v[160:163], a[24:27], 0\n"
      "v_cmp_eq_u32 vcc, 1, v248\n"
      "s_nop 1\n"
      "v_cndmask_b32 v251, 0, v249, vcc\n"
      "v_mfma_f32_16x16x32_bf16 v[124:127], v[160:163], a[28:31], 0\n"
      "global_load_dwordx4 v[160:163], v240, s[10:11] offset:1024 sc1\n"
      "v_mov_b32 v252, v250\n"
      "v_mfma_f32_16x16x32_bf16 v[96:99], v[164:167], a[32:35], v[96:99]\n"
      "v_mov_b32 v253, v251\n"
      "v_mov_b32 v248, 0\n"
      "v_mfma_f32_16x16x32_bf16 v[100:103], v[164:167], a[36:39], v[100:103]\n"
      "v_mov_b32 v249, 0\n"
      "v_mov_b32 v254, 0\n"
      "v_mfma_f32_16x16x32_bf16 v[104:107], v[164:167], a[40:43], v[104:107]\n"
      "v_mov_b32 v255, 0\n"
      "s_add_u32 s48, s34, 1\n"
      "v_mfma_f32_16x16x32_bf16 v[108:111], v[164:167], a[44:47], v[108:111]\n"
      "s_and_b32 s48, s48, 3\n"
      "s_mul_i32 s48, s48, 36864\n"
      "s_add_u32 s48, s48, 0\n"
      "v_mfma_f32_16x16x32_bf16 v[112:115], v[164:167], a[48:51], v[112:115]\n"
      "s_add_u32 s52, s60, s48\n"
      "s_addc_u32 s53, s61, 0\n"
      "v_cvt_pk_bf16_f32 v19, v30, v31\n"
      "v_mfma_f32_16x16x32_bf16 v[116:119], v[164:167], a[52:55], v[116:119]\n"
      "s_lshl2_add_u32 s54, s36, s33\n"
      "ds_write_addtid_b32 v18 offset:2560\n"
      "s_lshl_b32 s55, s54, 15\n"
      "v_mfma_f32_16x16x32_bf16 v[120:123], v[164:167], a[56:59], v[120:123]\n"
      "s_lshl2_add_u32 s56, s35, s34\n"
      "ds_write_addtid_b32 v19 offset:2816\n"
      "v_cvt_pk_bf16_f32 v40, v40, v41\n"
      "v_mfma_f32_16x16x32_bf16 v[124:127], v[164:167], a[60:63], v[124:127]\n"
      "s_waitcnt vmcnt(14)\n"
      "global_load_dwordx4 v[164:167], v240, s[72:73] offset:1024 sc1\n"
      "v_cvt_pk_bf16_f32 v41, v42, v43\n"
      "v_mfma_f32_16x16x32_bf16 v[96:99], v[168:171], a[64:67], v[96:99]\n"
      "s_lshl_b32 s56, s56, 6\n"
      "ds_write_addtid_b32 v40 offset:3072\n"
      "v_cvt_pk_bf16_f32 v42, v44, v45\n"
      "v_mfma_f32_16x16x32_bf16 v[100:103], v[168:171], a[68:71], v[100:103]\n"
      "s_add_u32 s55, s55, s56\n"
      "ds_write_addtid_b32 v41 offset:3328\n"
      "v_cvt_pk_bf16_f32 v43, v46, v47\n"
      "v_mfma_f32_16x16x32_bf16 v[104:107], v[168:171], a[72:75], v[104:107]\n"
      "ds_write_addtid_b32 v42 offset:3584\n"
      "v_cvt_pk_bf16_f32 v44, v48, v49\n"
      "v_mfma_f32_16x16x32_bf16 v[108:111], v[168:171], a[76:79], v[108:111]\n"
      "ds_write_addtid_b32 v43 offset:3840\n"
      "v_cvt_pk_bf16_f32 v45, v50, v51\n"
      "v_mfma_f32_16x16x32_bf16 v[112:115], v[168:171], a[80:83], v[112:115]\n"
      "ds_write_addtid_b32 v44 offset:4096\n"
      "v_cvt_pk_bf16_f32 v46, v52, v53\n"
      "v_mfma_f32_16x16x32_bf16 v[116:119], v[168:171], a[84:87], v[116:119]\n"
      "ds_write_addtid_b32 v45 offset:4352\n"
      "v_cvt_pk_bf16_f32 v47, v54, v55\n"
      "v_mfma_f32_16x16x32_bf16 v[120:123], v[168:171], a[88:91], v[120:123]\n"
      "ds_write_addtid_b32 v46 offset:4608\n"
      "v_cvt_pk_bf16_f32 v48, v56, v57\n"
      "v_mfma_f32_16x16x32_bf16 v[124:127], v[168:171], a[92:95], v[124:127]\n"
      "global_load_dwordx4 v[168:171], v241, s[10:11] offset:1024 sc1\n"
      "v_and_b32 v209, 15, v243\n"
      "v_mfma_f32_16x16x32_bf16 v[96:99], v[172:175], a[96:99], v[96:99]\n"
      "ds_write_addtid_b32 v47 offset:4864\n"
      "v_cvt_pk_bf16_f32 v49, v58, v59\n"
      "v_mfma_f32_16x16x32_bf16 v[100:103], v[172:175], a[100:103], v[100:103]\n"
      "ds_write_addtid_b32 v48 offset:5120\n"
      "v_cvt_pk_bf16_f32 v50, v60, v61\n"
      "v_mfma_f32_16x16x32_bf16 v[104:107], v[172:175], a[104:107], v[104:107]\n"
      "ds_write_addtid_b32 v49 offset:5376\n"
      "v_cvt_pk_bf16_f32 v51, v62, v63\n"
      "v_mfma_f32_16x16x32_bf16 v[108:111], v[172:175], a[108:111], v[108:111]\n"
      "ds_write_addtid_b32 v50 offset:5632\n"
      "v_and_b32 v208, 48, v243\n"
      "v_mfma_f32_16x16x32_bf16 v[112:115], v[172:175], a[112:115], v[112:115]\n"
      "ds_write_addtid_b32 v51 offset:5888\n"
      "v_lshl_or_b32 v208, v209, 10, v208\n"
      "v_mfma_f32_16x16x32_bf16 v[116:119], v[172:175], a[116:119], v[116:119]\n"
      "v_add_u32 v208, s55, v208\n"
      "v_add_u32 v210, 16384, v208\n"
      "v_mfma_f32_16x16x32_bf16 v[120:123], v[172:175], a[120:123], v[120:123]\n"
      "global_load_dwordx4 v[216:219], v208, s[16:17]\n"
      "global_load_dwordx4 v[220:223], v210, s[16:17]\n"
      "v_mfma_f32_16x16x32_bf16 v[124:127], v[172:175], a[124:127], v[124:127]\n"
      "s_waitcnt vmcnt(16)\n"
      "global_load_dwordx4 v[172:175], v241, s[72:73] offset:1024 sc1\n"
      "s_lshr_b32 s55, s34, 1\n"
      "v_mfma_f32_16x16x32_bf16 v[96:99], v[176:179], a[128:131], v[96:99]\n"
      "s_lshl2_add_u32 s55, s55, s35\n"
      "s_waitcnt lgkmcnt(0)\n"
      "s_barrier\n"
      "v_mfma_f32_16x16x32_bf16 v[100:103], v[176:179], a[132:135], v[100:103]\n"
      "s_lshl_b32 s55, s55, 15\n"
      "ds_read2st64_b32 v[20:21], v244 offset0:0 offset1:1\n"
      "s_lshl_b32 s54, s54, 9\n"
      "v_mfma_f32_16x16x32_bf16 v[104:107], v[176:179], a[136:139], v[104:107]\n"
      "s_add_u32 s54, s54, s55\n"
      "ds_read2st64_b32 v[22:23], v245 offset0:4 offset1:5\n"
      "s_add_u32 s54, s54, 786432\n"
      "v_mfma_f32_16x16x32_bf16 v[108:111], v[176:179], a[140:143], v[108:111]\n"
      "ds_read2st64_b32 v[24:25], v244 offset0:2 offset1:3\n"
      "v_lshl_add_u32 v209, v209, 4, s54\n"
      "v_mfma_f32_16x16x32_bf16 v[112:115], v[176:179], a[144:147], v[112:115]\n"
      "ds_read2st64_b32 v[26:27], v245 offset0:6 offset1:7\n"
      "ds_read2st64_b32 v[28:29], v246 offset0:8 offset1:9\n"
      "v_mfma_f32_16x16x32_bf16 v[116:119], v[176:179], a[148:151], v[116:119]\n"
      "ds_read2st64_b32 v[30:31], v246 offset0:10 offset1:11\n"
      "ds_read2st64_b32 v[52:53], v244 offset0:12 offset1:13\n"
      "v_mfma_f32_16x16x32_bf16 v[120:123], v[176:179], a[152:155], v[120:123]\n"
      "ds_read2st64_b32 v[54:55], v245 offset0:16 offset1:17\n"
      "v_mfma_f32_16x16x32_bf16 v[124:127], v[176:179], a[156:159], v[124:127]\n"
      "global_load_dwordx4 v[176:179], v240, s[14:15] offset:1024 sc1\n"
      "v_mfma_f32_16x16x32_bf16 v[96:99], v[180:183], a[160:163], v[96:99]\n"
      "ds_read2st64_b32 v[56:57], v244 offset0:14 offset1:15\n"
      "v_mfma_f32_16x16x32_bf16 v[100:103], v[180:183], a[164:167], v[100:103]\n"
      "s_waitcnt lgkmcnt(3)\n"
      "v_mfma_f32_16x16x32_bf16 v[0:3], v[250:253], v[20:23], v[0:3]\n"
      "v_mfma_f32_16x16x32_bf16 v[104:107], v[180:183], a[168:171], v[104:107]\n"
      "v_mfma_f32_16x16x32_bf16 v[4:7], v[250:253], v[24:27], v[4:7]\n"
      "v_mfma_f32_16x16x32_bf16 v[108:111], v[180:183], a[172:175], v[108:111]\n"
      "v_mfma_f32_16x16x32_bf16 v[0:3], v[252:255], v[28:31], v[0:3]\n"
      "v_mfma_f32_16x16x32_bf16 v[112:115], v[180:183], a[176:179], v[112:115]\n"
      "v_mfma_f32_16x16x32_bf16 v[4:7], v[248:251], v[28:31], v[4:7]\n"
      "v_mov_b32 v11, s37\n"
      "v_mfma_f32_16x16x32_bf16 v[116:119], v[180:183], a[180:183], v[116:119]\n"
      "ds_read2st64_b32 v[58:59], v245 offset0:18 offset1:19\n"
      "v_mfma_f32_16x16x32_bf16 v[120:123], v[180:183], a[184:187], v[120:123]\n"
      "ds_read2st64_b32 v[60:61], v246 offset0:20 offset1:21\n"
      "s_nop 0\n"
      "v_cvt_pk_bf16_f32 v8, v0, v1\n"
      "v_mfma_f32_16x16x32_bf16 v[124:127], v[180:183], a[188:191], v[124:127]\n"
      "s_waitcnt vmcnt(16)\n"
      "global_load_dwordx4 v[180:183], v240, s[74:75] offset:1024 sc1\n"
      "v_cvt_pk_bf16_f32 v9, v2, v3\n"
      "v_mfma_f32_16x16x32_bf16 v[96:99], v[184:187], a[192:195], v[96:99]\n"
      "ds_read2st64_b32 v[62:63], v246 offset0:22 offset1:23\n"
      "v_cvt_pk_bf16_f32 v10, v4, v5\n"
      "v_mfma_f32_16x16x32_bf16 v[100:103], v[184:187], a[196:199], v[100:103]\n"
      "global_load_dwordx2 v[204:205], v209, s[58:59] offset:0 sc1\n"
      "v_cvt_pk_bf16_f32 v247, v6, v7\n"
      "v_mfma_f32_16x16x32_bf16 v[104:107], v[184:187], a[200:203], v[104:107]\n"
      "global_store_dwordx4 v242, v[8:11], s[52:53] offset:0 sc1\n"
      "v_cvt_pk_bf16_f32 v72, v72, v73\n"
      "v_mfma_f32_16x16x32_bf16 v[108:111], v[184:187], a[204:207], v[108:111]\n"
      "global_load_dwordx2 v[206:207], v209, s[58:59] offset:256 sc1\n"
      "v_cvt_pk_bf16_f32 v73, v74, v75\n"
      "v_mfma_f32_16x16x32_bf16 v[112:115], v[184:187], a[208:211], v[112:115]\n"
      "s_waitcnt lgkmcnt(0)\n"
      "v_mfma_f32_16x16x32_bf16 v[32:35], v[250:253], v[52:55], v[32:35]\n"
      "s_add_u32 s68, s20, 16384\n"
      "s_addc_u32 s69, s21, 0\n"
      "v_mfma_f32_16x16x32_bf16 v[116:119], v[184:187], a[212:215], v[116:119]\n"
      "v_mfma_f32_16x16x32_bf16 v[36:39], v[250:253], v[56:59], v[36:39]\n"
      "v_mfma_f32_16x16x32_bf16 v[120:123], v[184:187], a[216:219], v[120:123]\n"
      "v_mfma_f32_16x16x32_bf16 v[32:35], v[252:255], v[60:63], v[32:35]\n"
      "v_mfma_f32_16x16x32_bf16 v[124:127], v[184:187], a[220:223], v[124:127]\n"
      "global_load_dwordx4 v[184:187], v241, s[14:15] offset:1024 sc1\n"
      "v_mfma_f32_16x16x32_bf16 v[96:99], v[188:191], a[224:227], v[96:99]\n"
      "v_mfma_f32_16x16x32_bf16 v[36:39], v[248:251], v[60:63], v[36:39]\n"
      "v_mov_b32 v43, s37\n"
      "v_mfma_f32_16x16x32_bf16 v[100:103], v[188:191], a[228:231], v[100:103]\n"
      "ds_write_addtid_b32 v72 offset:6144\n"
      "v_mov_b32 v44, v247\n"
      "v_mfma_f32_16x16x32_bf16 v[104:107], v[188:191], a[232:235], v[104:107]\n"
      "v_mov_b32 v47, s37\n"
      "v_cvt_pk_bf16_f32 v74, v76, v77\n"
      "v_mfma_f32_16x16x32_bf16 v[108:111], v[188:191], a[236:239], v[108:111]\n"
      "ds_write_addtid_b32 v73 offset:6400\n"
      "v_cvt_pk_bf16_f32 v75, v78, v79\n"
      "v_mfma_f32_16x16x32_bf16 v[112:115], v[188:191], a[240:243], v[112:115]\n"
      "ds_write_addtid_b32 v74 offset:6656\n"
      "v_cvt_pk_bf16_f32 v40, v32, v33\n"
      "v_mfma_f32_16x16x32_bf16 v[116:119], v[188:191], a[244:247], v[116:119]\n"
      "v_cvt_pk_bf16_f32 v41, v34, v35\n"
      "v_cvt_pk_bf16_f32 v42, v36, v37\n"
      "v_mfma_f32_16x16x32_bf16 v[120:123], v[188:191], a[248:251], v[120:123]\n"
      "v_cvt_pk_bf16_f32 v45, v38, v39\n"
      "v_cvt_pk_bf16_f32 v76, v80, v81\n"
      "v_mfma_f32_16x16x32_bf16 v[124:127], v[188:191], a[252:255], v[124:127]\n"
      "s_waitcnt vmcnt(19)\n"
      "global_load_dwordx4 v[188:191], v241, s[74:75] offset:1024 sc1\n"
      "v_mfma_f32_16x16x32_bf16 v[0:3], v[128:131], a[0:3], 0\n"
      "global_store_dwordx4 v242, v[40:43], s[52:53] offset:1024 sc1\n"
      "v_mfma_f32_16x16x32_bf16 v[4:7], v[128:131], a[4:7], 0\n"
      "global_store_dwordx4 v242, v[44:47], s[52:53] offset:2048 sc1\n"
      "s_add_u32 s48, s34, 2\n"
      "v_mfma_f32_16x16x32_bf16 v[8:11], v[128:131], a[8:11], 0\n"
      "s_and_b32 s48, s48, 3\n"
      "ds_write_addtid_b32 v75 offset:6912\n"
      "s_mul_i32 s48, s48, 36864\n"
      "v_mfma_f32_16x16x32_bf16 v[12:15], v[128:131], a[12:15], 0\n"
      "s_add_u32 s48, s48, 12288\n"
      "s_add_u32 s52, s60, s48\n"
      "s_addc_u32 s53, s61, 0\n"
      "v_mfma_f32_16x16x32_bf16 v[16:19], v[128:131], a[16:19], 0\n"
      "v_cvt_pk_bf16_f32 v77, v82, v83\n"
      "v_mfma_f32_16x16x32_bf16 v[20:23], v[128:131], a[20:23], 0\n"
      "ds_write_addtid_b32 v76 offset:7168\n"
      "v_cvt_pk_bf16_f32 v78, v84, v85\n"
      "v_mfma_f32_16x16x32_bf16 v[24:27], v[128:131], a[24:27], 0\n"
      "ds_write_addtid_b32 v77 offset:7424\n"
      "v_cvt_pk_bf16_f32 v79, v86, v87\n"
      "v_mfma_f32_16x16x32_bf16 v[28:31], v[128:131], a[28:31], 0\n"
      "global_load_dwordx4 v[128:131], v240, s[10:11] offset:2048 sc1\n"
      "v_mfma_f32_16x16x32_bf16 v[0:3], v[132:135], a[32:35], v[0:3]\n"
      "ds_write_addtid_b32 v78 offset:7680\n"
      "v_cvt_pk_bf16_f32 v80, v88, v89\n"
      "v_mfma_f32_16x16x32_bf16 v[4:7], v[132:135], a[36:39], v[4:7]\n"
      "ds_write_addtid_b32 v79 offset:7936\n"
      "v_cvt_pk_bf16_f32 v81, v90, v91\n"
      "v_mfma_f32_16x16x32_bf16 v[8:11], v[132:135], a[40:43], v[8:11]\n"
      "ds_write_addtid_b32 v80 offset:8192\n"
      "v_cvt_pk_bf16_f32 v82, v92, v93\n"
      "v_mfma_f32_16x16x32_bf16 v[12:15], v[132:135], a[44:47], v[12:15]\n"
      "ds_write_addtid_b32 v81 offset:8448\n"
      "v_cvt_pk_bf16_f32 v83, v94, v95\n"
      "v_mfma_f32_16x16x32_bf16 v[16:19], v[132:135], a[48:51], v[16:19]\n"
      "ds_write_addtid_b32 v82 offset:8704\n"
      "v_mfma_f32_16x16x32_bf16 v[20:23], v[132:135], a[52:55], v[20:23]\n"
      "ds_write_addtid_b32 v83 offset:8960\n"
      "v_cvt_pk_bf16_f32 v104, v104, v105\n"
      "v_mfma_f32_16x16x32_bf16 v[24:27], v[132:135], a[56:59], v[24:27]\n"
      "v_cvt_pk_bf16_f32 v105, v106, v107\n"
      "v_mfma_f32_16x16x32_bf16 v[28:31], v[132:135], a[60:63], v[28:31]\n"
      "s_waitcnt vmcnt(21)\n"
      "global_load_dwordx4 v[132:135], v240, s[72:73] offset:2048 sc1\n"
      "v_mfma_f32_16x16x32_bf16 v[0:3], v[136:139], a[64:67], v[0:3]\n"
      "ds_write_addtid_b32 v104 offset:9216\n"
      "v_cvt_pk_bf16_f32 v106, v108, v109\n"
      "v_mfma_f32_16x16x32_bf16 v[4:7], v[136:139], a[68:71], v[4:7]\n"
      "ds_write_addtid_b32 v105 offset:9472\n"
      "v_cvt_pk_bf16_f32 v107, v110, v111\n"
      "v_mfma_f32_16x16x32_bf16 v[8:11], v[136:139], a[72:75], v[8:11]\n"
      "ds_write_addtid_b32 v106 offset:9728\n"
      "v_cvt_pk_bf16_f32 v108, v112, v113\n"
      "v_mfma_f32_16x16x32_bf16 v[12:15], v[136:139], a[76:79], v[12:15]\n"
      "ds_write_addtid_b32 v107 offset:9984\n"
      "v_cvt_pk_bf16_f32 v109, v114, v115\n"
      "v_mfma_f32_16x16x32_bf16 v[16:19], v[136:139], a[80:83], v[16:19]\n"
      "ds_write_addtid_b32 v108 offset:10240\n"
      "v_cvt_pk_bf16_f32 v110, v116, v117\n"
      "v_mfma_f32_16x16x32_bf16 v[20:23], v[136:139], a[84:87], v[20:23]\n"
      "ds_write_addtid_b32 v109 offset:10496\n"
      "v_cvt_pk_bf16_f32 v111, v118, v119\n"
      "v_mfma_f32_16x16x32_bf16 v[24:27], v[136:139], a[88:91], v[24:27]\n"
      "ds_write_addtid_b32 v110 offset:10752\n"
      "v_cvt_pk_bf16_f32 v112, v120, v121\n"
      "v_mfma_f32_16x16x32_bf16 v[28:31], v[136:139], a[92:95], v[28:31]\n"
      "global_load_dwordx4 v[136:139], v241, s[10:11] offset:2048 sc1\n"
      "v_mfma_f32_16x16x32_bf16 v[0:3], v[140:143], a[96:99], v[0:3]\n"
      "ds_write_addtid_b32 v111 offset:11008\n"
      "v_cvt_pk_bf16_f32 v113, v122, v123\n"
      "v_mfma_f32_16x16x32_bf16 v[4:7], v[140:143], a[100:103], v[4:7]\n"
      "ds_write_addtid_b32 v112 offset:11264\n"
      "v_cvt_pk_bf16_f32 v114, v124, v125\n"
      "v_mfma_f32_16x16x32_bf16 v[8:11], v[140:143], a[104:107], v[8:11]\n"
      "ds_write_addtid_b32 v113 offset:11520\n"
      "v_cvt_pk_bf16_f32 v115, v126, v127\n"
      "v_mfma_f32_16x16x32_bf16 v[12:15], v[140:143], a[108:111], v[12:15]\n"
      "ds_write_addtid_b32 v114 offset:11776\n"
      "v_mfma_f32_16x16x32_bf16 v[16:19], v[140:143], a[112:115], v[16:19]\n"
      "ds_write_addtid_b32 v115 offset:12032\n"
      "v_mfma_f32_16x16x32_bf16 v[20:23], v[140:143], a[116:119], v[20:23]\n"
      "v_mfma_f32_16x16x32_bf16 v[24:27], v[140:143], a[120:123], v[24:27]\n"
      "v_mfma_f32_16x16x32_bf16 v[28:31], v[140:143], a[124:127], v[28:31]\n"
      "s_waitcnt vmcnt(21)\n"
      "global_load_dwordx4 v[140:143], v241, s[72:73] offset:2048 sc1\n"
      "v_mfma_f32_16x16x32_bf16 v[0:3], v[144:147], a[128:131], v[0:3]\n"
      "s_waitcnt lgkmcnt(0)\n"
      "s_barrier\n"
      "v_mfma_f32_16x16x32_bf16 v[4:7], v[144:147], a[132:135], v[4:7]\n"
      "ds_read2st64_b32 v[84:85], v244 offset0:24 offset1:25\n"
      "v_mfma_f32_16x16x32_bf16 v[8:11], v[144:147], a[136:139], v[8:11]\n"
      "ds_read2st64_b32 v[86:87], v245 offset0:28 offset1:29\n"
      "v_mfma_f32_16x16x32_bf16 v[12:15], v[144:147], a[140:143], v[12:15]\n"
      "ds_read2st64_b32 v[88:89], v244 offset0:26 offset1:27\n"
      "v_mfma_f32_16x16x32_bf16 v[16:19], v[144:147], a[144:147], v[16:19]\n"
      "ds_read2st64_b32 v[90:91], v245 offset0:30 offset1:31\n"
      "v_mfma_f32_16x16x32_bf16 v[20:23], v[144:147], a[148:151], v[20:23]\n"
      "ds_read2st64_b32 v[92:93], v246 offset0:32 offset1:33\n"
      "v_mfma_f32_16x16x32_bf16 v[24:27], v[144:147], a[152:155], v[24:27]\n"
      "ds_read2st64_b32 v[94:95], v246 offset0:34 offset1:35\n"
      "v_mfma_f32_16x16x32_bf16 v[28:31], v[144:147], a[156:159], v[28:31]\n"
      "global_load_dwordx4 v[144:147], v240, s[14:15] offset:2048 sc1\n"
      "v_mfma_f32_16x16x32_bf16 v[0:3], v[148:151], a[160:163], v[0:3]\n"
      "ds_read2st64_b32 v[116:117], v244 offset0:36 offset1:37\n"
      "v_mfma_f32_16x16x32_bf16 v[4:7], v[148:151], a[164:167], v[4:7]\n"
      "ds_read2st64_b32 v[118:119], v245 offset0:40 offset1:41\n"
      "v_mfma_f32_16x16x32_bf16 v[8:11], v[148:151], a[168:171], v[8:11]\n"
      "s_waitcnt lgkmcnt(2)\n"
      "v_mfma_f32_16x16x32_bf16 v[64:67], v[250:253], v[84:87], v[64:67]\n"
      "v_mfma_f32_16x16x32_bf16 v[12:15], v[148:151], a[172:175], v[12:15]\n"
      "v_mfma_f32_16x16x32_bf16 v[68:71], v[250:253], v[88:91], v[68:71]\n"
      "v_mfma_f32_16x16x32_bf16 v[16:19], v[148:151], a[176:179], v[16:19]\n"
      "v_mfma_f32_16x16x32_bf16 v[64:67], v[252:255], v[92:95], v[64:67]\n"
      "v_mfma_f32_16x16x32_bf16 v[20:23], v[148:151], a[180:183], v[20:23]\n"
      "v_mfma_f32_16x16x32_bf16 v[68:71], v[248:251], v[92:95], v[68:71]\n"
      "v_mov_b32 v75, s37\n"
      "v_mfma_f32_16x16x32_bf16 v[24:27], v[148:151], a[184:187], v[24:27]\n"
      "ds_read2st64_b32 v[120:121], v244 offset0:38 offset1:39\n"
      "v_mfma_f32_16x16x32_bf16 v[28:31], v[148:151], a[188:191], v[28:31]\n"
      "s_waitcnt vmcnt(21)\n"
      "global_load_dwordx4 v[148:151], v240, s[74:75] offset:2048 sc1\n"
      "v_cvt_pk_bf16_f32 v72, v64, v65\n"
      "v_mfma_f32_16x16x32_bf16 v[0:3], v[152:155], a[192:195], v[0:3]\n"
      "ds_read2st64_b32 v[122:123], v245 offset0:42 offset1:43\n"
      "v_cvt_pk_bf16_f32 v73, v66, v67\n"
      "v_mfma_f32_16x16x32_bf16 v[4:7], v[152:155], a[196:199], v[4:7]\n"
      "ds_read2st64_b32 v[124:125], v246 offset0:44 offset1:45\n"
      "v_cvt_pk_bf16_f32 v74, v68, v69\n"
      "v_mfma_f32_16x16x32_bf16 v[8:11], v[152:155], a[200:203], v[8:11]\n"
      "ds_read2st64_b32 v[126:127], v246 offset0:46 offset1:47\n"
      "v_cvt_pk_bf16_f32 v247, v70, v71\n"
      "v_mfma_f32_16x16x32_bf16 v[12:15], v[152:155], a[204:207], v[12:15]\n"
      "global_store_dwordx4 v242, v[72:75], s[52:53] offset:0 sc1\n"
      "v_mfma_f32_16x16x32_bf16 v[16:19], v[152:155], a[208:211], v[16:19]\n"
      "s_waitcnt vmcnt(11)\n"
      "v_bitop3_b32 v210, v205, v207, s37 bitop3:0x7e\n"
      "v_cmp_ne_u32 vcc, 0, v210\n"
      "s_nop 1\n"
      "s_cbranch_vccnz .Ls_s15_%=\n"
      ".Ls_o16_%=:\n"
      "v_mfma_f32_16x16x32_bf16 v[20:23], v[152:155], a[212:215], v[20:23]\n"
      "v_sub_f32 v205, 1.0, v204\n"
      "v_cvt_pk_bf16_f32 v205, v205, v205\n"
      "v_mfma_f32_16x16x32_bf16 v[24:27], v[152:155], a[216:219], v[24:27]\n"
      "s_waitcnt lgkmcnt(0)\n"
      "v_mfma_f32_16x16x32_bf16 v[96:99], v[250:253], v[116:119], v[96:99]\n"
      "v_and_b32 v205, 0xffff0000, v205\n"
      "v_mfma_f32_16x16x32_bf16 v[28:31], v[152:155], a[220:223], v[28:31]\n"
      "global_load_dwordx4 v[152:155], v241, s[14:15] offset:2048 sc1\n"
      "v_sub_f32 v207, 1.0, v206\n"
      "v_mfma_f32_16x16x32_bf16 v[0:3], v[156:159], a[224:227], v[0:3]\n"
      "v_mfma_f32_16x16x32_bf16 v[100:103], v[250:253], v[120:123], v[100:103]\n"
      "v_cvt_pk_bf16_f32 v207, v207, v207\n"
      "v_mfma_f32_16x16x32_bf16 v[4:7], v[156:159], a[228:231], v[4:7]\n"
      "v_mfma_f32_16x16x32_bf16 v[96:99], v[252:255], v[124:127], v[96:99]\n"
      "v_and_b32 v207, 0xffff0000, v207\n"
      "v_mfma_f32_16x16x32_bf16 v[8:11], v[156:159], a[232:235], v[8:11]\n"
      "v_mfma_f32_16x16x32_bf16 v[100:103], v[248:251], v[124:127], v[100:103]\n"
      "v_mov_b32 v107, s37\n"
      "v_mfma_f32_16x16x32_bf16 v[12:15], v[156:159], a[236:239], v[12:15]\n"
      "v_mov_b32 v108, v247\n"
      "v_mov_b32 v111, s37\n"
      "v_mfma_f32_16x16x32_bf16 v[16:19], v[156:159], a[240:243], v[16:19]\n"
      "v_lshlrev_b32 v208, 16, v216\n"
      "v_and_b32 v209, 0xffff0000, v216\n"
      "v_mfma_f32_16x16x32_bf16 v[20:23], v[156:159], a[244:247], v[20:23]\n"
      "v_cvt_pk_bf16_f32 v104, v96, v97\n"
      "v_cvt_pk_bf16_f32 v105, v98, v99\n"
      "v_mfma_f32_16x16x32_bf16 v[24:27], v[156:159], a[248:251], v[24:27]\n"
      "v_cvt_pk_bf16_f32 v106, v100, v101\n"
      "v_cvt_pk_bf16_f32 v109, v102, v103\n"
      "v_mfma_f32_16x16x32_bf16 v[28:31], v[156:159], a[252:255], v[28:31]\n"
      "global_load_dwordx4 v[156:159], v241, s[74:75] offset:2048 sc1\n"
      "v_cvt_pk_bf16_f32 v8, v8, v9\n"
      "v_mfma_f32_16x16x32_bf16 v[32:35], v[160:163], a[0:3], 0\n"
      "global_store_dwordx4 v242, v[104:107], s[52:53] offset:1024 sc1\n"
      "v_cvt_pk_bf16_f32 v9, v10, v11\n"
      "v_mfma_f32_16x16x32_bf16 v[36:39], v[160:163], a[4:7], 0\n"
      "global_store_dwordx4 v242, v[108:111], s[52:53] offset:2048 sc1\n"
      "s_add_u32 s48, s34, 3\n"
      "v_mfma_f32_16x16x32_bf16 v[40:43], v[160:163], a[8:11], 0\n"
      "s_and_b32 s48, s48, 3\n"
      "ds_write_addtid_b32 v8 offset:0\n"
      "s_mul_i32 s48, s48, 36864\n"
      "v_mfma_f32_16x16x32_bf16 v[44:47], v[160:163], a[12:15], 0\n"
      "s_add_u32 s48, s48, 24576\n"
      "s_add_u32 s52, s60, s48\n"
      "s_addc_u32 s53, s61, 0\n"
      "v_mfma_f32_16x16x32_bf16 v[48:51], v[160:163], a[16:19], 0\n"
      "v_cvt_pk_bf16_f32 v10, v12, v13\n"
      "v_mul_f32 v208, v205, v208\n"
      "v_mfma_f32_16x16x32_bf16 v[52:55], v[160:163], a[20:23], 0\n"
      "ds_write_addtid_b32 v9 offset:256\n"
      "v_cvt_pk_bf16_f32 v11, v14, v15\n"
      "v_mfma_f32_16x16x32_bf16 v[56:59], v[160:163], a[24:27], 0\n"
      "ds_write_addtid_b32 v10 offset:512\n"
      "v_cvt_pk_bf16_f32 v12, v16, v17\n"
      "v_mfma_f32_16x16x32_bf16 v[60:63], v[160:163], a[28:31], 0\n"
      "global_load_dwordx4 v[160:163], v240, s[10:11] offset:3072 sc1\n"
      "v_mul_f32 v209, v205, v209\n"
      "v_mfma_f32_16x16x32_bf16 v[32:35], v[164:167], a[32:35], v[32:35]\n"
      "ds_write_addtid_b32 v11 offset:768\n"
      "v_cvt_pk_bf16_f32 v13, v18, v19\n"
      "v_mfma_f32_16x16x32_bf16 v[36:39], v[164:167], a[36:39], v[36:39]\n"
      "ds_write_addtid_b32 v12 offset:1024\n"
      "v_cvt_pk_bf16_f32 v14, v20, v21\n"
      "v_mfma_f32_16x16x32_bf16 v[40:43], v[164:167], a[40:43], v[40:43]\n"
      "ds_write_addtid_b32 v13 offset:1280\n"
      "v_cvt_pk_bf16_f32 v15, v22, v23\n"
      "v_mfma_f32_16x16x32_bf16 v[44:47], v[164:167], a[44:47], v[44:47]\n"
      "ds_write_addtid_b32 v14 offset:1536\n"
      "v_cvt_pk_bf16_f32 v16, v24, v25\n"
      "v_mfma_f32_16x16x32_bf16 v[48:51], v[164:167], a[48:51], v[48:51]\n"
      "ds_write_addtid_b32 v15 offset:1792\n"
      "v_cvt_pk_bf16_f32 v17, v26, v27\n"
      "v_mfma_f32_16x16x32_bf16 v[52:55], v[164:167], a[52:55], v[52:55]\n"
      "ds_write_addtid_b32 v16 offset:2048\n"
      "v_cvt_pk_bf16_f32 v18, v28, v29\n"
      "v_mfma_f32_16x16x32_bf16 v[56:59], v[164:167], a[56:59], v[56:59]\n"
      "ds_write_addtid_b32 v17 offset:2304\n"
      "v_cvt_pk_bf16_f32 v19, v30, v31\n"
      "v_mfma_f32_16x16x32_bf16 v[60:63], v[164:167], a[60:63], v[60:63]\n"
      "global_load_dwordx4 v[164:167], v240, s[72:73] offset:3072 sc1\n"
      "v_lshlrev_b32 v212, 16, v220\n"
      "v_mfma_f32_16x16x32_bf16 v[32:35], v[168:171], a[64:67], v[32:35]\n"
      "ds_write_addtid_b32 v18 offset:2560\n"
      "v_and_b32 v213, 0xffff0000, v220\n"
      "v_mfma_f32_16x16x32_bf16 v[36:39], v[168:171], a[68:71], v[36:39]\n"
      "ds_write_addtid_b32 v19 offset:2816\n"
      "v_mul_f32 v212, v207, v212\n"
      "v_mfma_f32_16x16x32_bf16 v[40:43], v[168:171], a[72:75], v[40:43]\n"
      "v_mul_f32 v213, v207, v213\n"
      "v_lshlrev_b32 v210, 16, v217\n"
      "v_mfma_f32_16x16x32_bf16 v[44:47], v[168:171], a[76:79], v[44:47]\n"
      "v_and_b32 v211, 0xffff0000, v217\n"
      "v_mul_f32 v210, v205, v210\n"
      "v_mfma_f32_16x16x32_bf16 v[48:51], v[168:171], a[80:83], v[48:51]\n"
      "s_waitcnt lgkmcnt(0)\n"
      "s_barrier\n"
      "v_mfma_f32_16x16x32_bf16 v[52:55], v[168:171], a[84:87], v[52:55]\n"
      "ds_read2st64_b32 v[20:21], v244 offset0:0 offset1:1\n"
      "v_mul_f32 v211, v205, v211\n"
      "v_mfma_f32_16x16x32_bf16 v[56:59], v[168:171], a[88:91], v[56:59]\n"
      "ds_read2st64_b32 v[22:23], v245 offset0:4 offset1:5\n"
      "v_lshlrev_b32 v214, 16, v221\n"
      "v_mfma_f32_16x16x32_bf16 v[60:63], v[168:171], a[92:95], v[60:63]\n"
      "global_load_dwordx4 v[168:171], v241, s[10:11] offset:3072 sc1\n"
      "v_and_b32 v215, 0xffff0000, v221\n"
      "v_mfma_f32_16x16x32_bf16 v[32:35], v[172:175], a[96:99], v[32:35]\n"
      "ds_read2st64_b32 v[24:25], v244 offset0:2 offset1:3\n"
      "v_mul_f32 v214, v207, v214\n"
      "v_mfma_f32_16x16x32_bf16 v[36:39], v[172:175], a[100:103], v[36:39]\n"
      "ds_read2st64_b32 v[26:27], v245 offset0:6 offset1:7\n"
      "v_mul_f32 v215, v207, v215\n"
      "v_mfma_f32_16x16x32_bf16 v[40:43], v[172:175], a[104:107], v[40:43]\n"
      "ds_read2st64_b32 v[28:29], v246 offset0:8 offset1:9\n"
      "v_lshlrev_b32 v216, 16, v218\n"
      "v_mfma_f32_16x16x32_bf16 v[44:47], v[172:175], a[108:111], v[44:47]\n"
      "ds_read2st64_b32 v[30:31], v246 offset0:10 offset1:11\n"
      "v_and_b32 v217, 0xffff0000, v218\n"
      "v_mfma_f32_16x16x32_bf16 v[48:51], v[172:175], a[112:115], v[48:51]\n"
      "v_mul_f32 v216, v205, v216\n"
      "v_mul_f32 v217, v205, v217\n"
      "v_mfma_f32_16x16x32_bf16 v[52:55], v[172:175], a[116:119], v[52:55]\n"
      "v_lshlrev_b32 v220, 16, v222\n"
      "v_and_b32 v221, 0xffff0000, v222\n"
      "v_mfma_f32_16x16x32_bf16 v[56:59], v[172:175], a[120:123], v[56:59]\n"
      "v_mul_f32 v220, v207, v220\n"
      "v_mul_f32 v221, v207, v221\n"
      "v_mfma_f32_16x16x32_bf16 v[60:63], v[172:175], a[124:127], v[60:63]\n"
      "global_load_dwordx4 v[172:175], v241, s[72:73] offset:3072 sc1\n"
      "s_waitcnt lgkmcnt(0)\n"
      "v_mfma_f32_16x16x32_bf16 v[32:35], v[176:179], a[128:131], v[32:35]\n"
      "v_mfma_f32_16x16x32_bf16 v[0:3], v[250:253], v[20:23], v[0:3]\n"
      "v_lshlrev_b32 v218, 16, v219\n"
      "v_mfma_f32_16x16x32_bf16 v[36:39], v[176:179], a[132:135], v[36:39]\n"
      "v_mfma_f32_16x16x32_bf16 v[4:7], v[250:253], v[24:27], v[4:7]\n"
      "v_and_b32 v219, 0xffff0000, v219\n"
      "v_mfma_f32_16x16x32_bf16 v[40:43], v[176:179], a[136:139], v[40:43]\n"
      "v_mfma_f32_16x16x32_bf16 v[0:3], v[252:255], v[28:31], v[0:3]\n"
      "v_mul_f32 v218, v205, v218\n"
      "v_mfma_f32_16x16x32_bf16 v[44:47], v[176:179], a[140:143], v[44:47]\n"
      "v_mfma_f32_16x16x32_bf16 v[4:7], v[248:251], v[28:31], v[4:7]\n"
      "v_mov_b32 v11, s37\n"
      "v_mfma_f32_16x16x32_bf16 v[48:51], v[176:179], a[144:147], v[48:51]\n"
      "v_mul_f32 v219, v205, v219\n"
      "v_lshlrev_b32 v222, 16, v223\n"
      "v_mfma_f32_16x16x32_bf16 v[52:55], v[176:179], a[148:151], v[52:55]\n"
      "v_cvt_pk_bf16_f32 v8, v0, v1\n"
      "v_cvt_pk_bf16_f32 v9, v2, v3\n"
      "v_mfma_f32_16x16x32_bf16 v[56:59], v[176:179], a[152:155], v[56:59]\n"
      "v_cvt_pk_bf16_f32 v10, v4, v5\n"
      "v_cvt_pk_bf16_f32 v247, v6, v7\n"
      "v_mfma_f32_16x16x32_bf16 v[60:63], v[176:179], a[156:159], v[60:63]\n"
      "global_load_dwordx4 v[176:179], v240, s[14:15] offset:3072 sc1\n"
      "v_and_b32 v223, 0xffff0000, v223\n"
      "v_mfma_f32_16x16x32_bf16 v[32:35], v[180:183], a[160:163], v[32:35]\n"
      "global_store_dwordx4 v242, v[8:11], s[52:53] offset:0 sc1\n"
      "v_mul_f32 v222, v207, v222\n"
      "v_mfma_f32_16x16x32_bf16 v[36:39], v[180:183], a[164:167], v[36:39]\n"
      "v_mul_f32 v223, v207, v223\n"
      "s_lshl2_add_u32 s54, s36, s33\n"
      "v_mfma_f32_16x16x32_bf16 v[40:43], v[180:183], a[168:171], v[40:43]\n"
      "s_lshl_b32 s55, s54, 15\n"
      "s_lshl2_add_u32 s56, s35, s34\n"
      "s_lshl_b32 s56, s56, 6\n"
      "v_mfma_f32_16x16x32_bf16 v[44:47], v[180:183], a[172:175], v[44:47]\n"
      "s_add_u32 s55, s55, s56\n"
      "v_and_b32 v8, 15, v243\n"
      "v_and_b32 v7, 48, v243\n"
      "v_mfma_f32_16x16x32_bf16 v[48:51], v[180:183], a[176:179], v[48:51]\n"
      "v_lshl_or_b32 v7, v8, 10, v7\n"
      "v_add_u32 v7, s55, v7\n"
      "v_mfma_f32_16x16x32_bf16 v[52:55], v[180:183], a[180:183], v[52:55]\n"
      "v_mfma_f32_16x16x32_bf16 v[56:59], v[180:183], a[184:187], v[56:59]\n"
      "v_mfma_f32_16x16x32_bf16 v[60:63], v[180:183], a[188:191], v[60:63]\n"
      "s_waitcnt vmcnt(19)\n"
      "global_load_dwordx4 v[180:183], v240, s[74:75] offset:3072 sc1\n"
      "v_mfma_f32_16x16x32_bf16 v[40:43], v[184:187], a[200:203], v[40:43]\n"
      "v_mfma_f32_16x16x32_bf16 v[44:47], v[184:187], a[204:207], v[44:47]\n"
      "v_mfma_f32_16x16x32_bf16 v[48:51], v[184:187], a[208:211], v[48:51]\n"
      "v_mfma_f32_16x16x32_bf16 v[52:55], v[184:187], a[212:215], v[52:55]\n"
      "v_mfma_f32_16x16x32_bf16 v[56:59], v[184:187], a[216:219], v[56:59]\n"
      "v_mfma_f32_16x16x32_bf16 v[60:63], v[184:187], a[220:223], v[60:63]\n"
      "v_mfma_f32_16x16x32_bf16 v[40:43], v[188:191], a[232:235], v[40:43]\n"
      "v_mfma_f32_16x16x32_bf16 v[44:47], v[188:191], a[236:239], v[44:47]\n"
      "v_mfma_f32_16x16x32_bf16 v[48:51], v[188:191], a[240:243], v[48:51]\n"
      "v_mfma_f32_16x16x32_bf16 v[52:55], v[188:191], a[244:247], v[52:55]\n"
      "v_mfma_f32_16x16x32_bf16 v[56:59], v[188:191], a[248:251], v[56:59]\n"
      "s_nop 3\n"
      "v_cvt_pk_bf16_f32 v40, v40, v41\n"
      "v_cvt_pk_bf16_f32 v41, v42, v43\n"
      "v_mfma_f32_16x16x32_bf16 v[60:63], v[188:191], a[252:255], v[60:63]\n"
      "ds_write_addtid_b32 v40 offset:3072\n"
      "v_cvt_pk_bf16_f32 v42, v44, v45\n"
      "v_mfma_f32_16x16x32_bf16 v[32:35], v[184:187], a[192:195], v[32:35]\n"
      "ds_write_addtid_b32 v41 offset:3328\n"
      "v_cvt_pk_bf16_f32 v43, v46, v47\n"
      "v_mfma_f32_16x16x32_bf16 v[36:39], v[184:187], a[196:199], v[36:39]\n"
      "global_load_dwordx4 v[184:187], v241, s[14:15] offset:3072 sc1\n"
      "ds_write_addtid_b32 v42 offset:3584\n"
      "v_cvt_pk_bf16_f32 v44, v48, v49\n"
      "v_mfma_f32_16x16x32_bf16 v[32:35], v[188:191], a[224:227], v[32:35]\n"
      "ds_write_addtid_b32 v43 offset:3840\n"
      "v_cvt_pk_bf16_f32 v45, v50, v51\n"
      "v_mfma_f32_16x16x32_bf16 v[36:39], v[188:191], a[228:231], v[36:39]\n"
      "s_waitcnt vmcnt(17)\n"
      "global_load_dwordx4 v[188:191], v241, s[74:75] offset:3072 sc1\n"
      "ds_write_addtid_b32 v44 offset:4096\n"
      "v_cvt_pk_bf16_f32 v46, v52, v53\n"
      "v_mfma_f32_16x16x32_bf16 v[72:75], v[128:131], a[8:11], 0\n"
      "ds_write_addtid_b32 v45 offset:4352\n"
      "v_cvt_pk_bf16_f32 v47, v54, v55\n"
      "v_mfma_f32_16x16x32_bf16 v[76:79], v[128:131], a[12:15], 0\n"
      "ds_write_addtid_b32 v46 offset:4608\n"
      "v_cvt_pk_bf16_f32 v48, v56, v57\n"
      "v_mfma_f32_16x16x32_bf16 v[80:83], v[128:131], a[16:19], 0\n"
      "ds_write_addtid_b32 v47 offset:4864\n"
      "v_cvt_pk_bf16_f32 v49, v58, v59\n"
      "v_mfma_f32_16x16x32_bf16 v[84:87], v[128:131], a[20:23], 0\n"
      "ds_write_addtid_b32 v48 offset:5120\n"
      "v_cvt_pk_bf16_f32 v50, v60, v61\n"
      "v_mfma_f32_16x16x32_bf16 v[88:91], v[128:131], a[24:27], 0\n"
      "ds_write_addtid_b32 v49 offset:5376\n"
      "v_cvt_pk_bf16_f32 v51, v62, v63\n"
      "v_mfma_f32_16x16x32_bf16 v[92:95], v[128:131], a[28:31], 0\n"
      "ds_write_addtid_b32 v50 offset:5632\n"
      "ds_write_addtid_b32 v51 offset:5888\n"
      "v_mfma_f32_16x16x32_bf16 v[72:75], v[132:135], a[40:43], v[72:75]\n"
      "v_mfma_f32_16x16x32_bf16 v[76:79], v[132:135], a[44:47], v[76:79]\n"
      "v_mfma_f32_16x16x32_bf16 v[80:83], v[132:135], a[48:51], v[80:83]\n"
      "s_waitcnt lgkmcnt(0)\n"
      "s_barrier\n"
      "v_mfma_f32_16x16x32_bf16 v[84:87], v[132:135], a[52:55], v[84:87]\n"
      "ds_read2st64_b32 v[52:53], v244 offset0:12 offset1:13\n"
      "ds_read2st64_b32 v[54:55], v245 offset0:16 offset1:17\n"
      "v_mfma_f32_16x16x32_bf16 v[88:91], v[132:135], a[56:59], v[88:91]\n"
      "ds_read2st64_b32 v[56:57], v244 offset0:14 offset1:15\n"
      "ds_read2st64_b32 v[58:59], v245 offset0:18 offset1:19\n"
      "v_mfma_f32_16x16x32_bf16 v[92:95], v[132:135], a[60:63], v[92:95]\n"
      "s_waitcnt vmcnt(16)\n"
      "ds_read2st64_b32 v[60:61], v246 offset0:20 offset1:21\n"
      "ds_read2st64_b32 v[62:63], v246 offset0:22 offset1:23\n"
      "v_mfma_f32_16x16x32_bf16 v[72:75], v[136:139], a[72:75], v[72:75]\n"
      "v_mfma_f32_16x16x32_bf16 v[76:79], v[136:139], a[76:79], v[76:79]\n"
      "v_mfma_f32_16x16x32_bf16 v[80:83], v[136:139], a[80:83], v[80:83]\n"
      "v_mfma_f32_16x16x32_bf16 v[84:87], v[136:139], a[84:87], v[84:87]\n"
      "s_waitcnt lgkmcnt(0)\n"
      "v_mfma_f32_16x16x32_bf16 v[32:35], v[250:253], v[52:55], v[32:35]\n"
      "v_mfma_f32_16x16x32_bf16 v[36:39], v[250:253], v[56:59], v[36:39]\n"
      "v_mfma_f32_16x16x32_bf16 v[88:91], v[136:139], a[88:91], v[88:91]\n"
      "v_mfma_f32_16x16x32_bf16 v[32:35], v[252:255], v[60:63], v[32:35]\n"
      "v_mfma_f32_16x16x32_bf16 v[36:39], v[248:251], v[60:63], v[36:39]\n"
      "v_add_u32 v4, 0, v242\n"
      "v_mfma_f32_16x16x32_bf16 v[92:95], v[136:139], a[92:95], v[92:95]\n"
      "global_load_dwordx4 v[12:15], v4, s[62:63] offset:0 sc1\n"
      "global_load_dwordx4 v[16:19], v4, s[62:63] offset:1024 sc1\n"
      "v_mov_b32 v43, s37\n"
      "v_mfma_f32_16x16x32_bf16 v[72:75], v[140:143], a[104:107], v[72:75]\n"
      "global_load_dwordx4 v[20:23], v4, s[62:63] offset:2048 sc1\n"
      "v_mov_b32 v44, v247\n"
      "v_mov_b32 v47, s37\n"
      "v_mfma_f32_16x16x32_bf16 v[76:79], v[140:143], a[108:111], v[76:79]\n"
      "v_mfma_f32_16x16x32_bf16 v[80:83], v[140:143], a[112:115], v[80:83]\n"
      "v_cvt_pk_bf16_f32 v40, v32, v33\n"
      "v_cvt_pk_bf16_f32 v41, v34, v35\n"
      "v_cvt_pk_bf16_f32 v42, v36, v37\n"
      "v_mfma_f32_16x16x32_bf16 v[84:87], v[140:143], a[116:119], v[84:87]\n"
      "v_cvt_pk_bf16_f32 v45, v38, v39\n"
      "v_mfma_f32_16x16x32_bf16 v[88:91], v[140:143], a[120:123], v[88:91]\n"
      "global_store_dwordx4 v242, v[40:43], s[52:53] offset:1024 sc1\n"
      "global_store_dwordx4 v242, v[44:47], s[52:53] offset:2048 sc1\n"
      "v_mfma_f32_16x16x32_bf16 v[92:95], v[140:143], a[124:127], v[92:95]\n"
      "s_waitcnt vmcnt(19)\n"
      "v_mfma_f32_16x16x32_bf16 v[72:75], v[144:147], a[136:139], v[72:75]\n"
      "v_mfma_f32_16x16x32_bf16 v[76:79], v[144:147], a[140:143], v[76:79]\n"
      "v_mfma_f32_16x16x32_bf16 v[80:83], v[144:147], a[144:147], v[80:83]\n"
      "v_mfma_f32_16x16x32_bf16 v[84:87], v[144:147], a[148:151], v[84:87]\n"
      "v_mfma_f32_16x16x32_bf16 v[88:91], v[144:147], a[152:155], v[88:91]\n"
      "v_mfma_f32_16x16x32_bf16 v[92:95], v[144:147], a[156:159], v[92:95]\n"
      "v_mfma_f32_16x16x32_bf16 v[72:75], v[148:151], a[168:171], v[72:75]\n"
      "v_mfma_f32_16x16x32_bf16 v[76:79], v[148:151], a[172:175], v[76:79]\n"
      "v_mfma_f32_16x16x32_bf16 v[80:83], v[148:151], a[176:179], v[80:83]\n"
      "v_mfma_f32_16x16x32_bf16 v[84:87], v[148:151], a[180:183], v[84:87]\n"
      "v_mfma_f32_16x16x32_bf16 v[88:91], v[148:151], a[184:187], v[88:91]\n"
      "v_mfma_f32_16x16x32_bf16 v[92:95], v[148:151], a[188:191], v[92:95]\n"
      "s_waitcnt vmcnt(16)\n"
      "v_mfma_f32_16x16x32_bf16 v[72:75], v[152:155], a[200:203], v[72:75]\n"
      "v_mfma_f32_16x16x32_bf16 v[76:79], v[152:155], a[204:207], v[76:79]\n"
      "v_mfma_f32_16x16x32_bf16 v[80:83], v[152:155], a[208:211], v[80:83]\n"
      "v_mfma_f32_16x16x32_bf16 v[84:87], v[152:155], a[212:215], v[84:87]\n"
      "v_mfma_f32_16x16x32_bf16 v[88:91], v[152:155], a[216:219], v[88:91]\n"
      "v_mfma_f32_16x16x32_bf16 v[92:95], v[152:155], a[220:223], v[92:95]\n"
      "v_mfma_f32_16x16x32_bf16 v[72:75], v[156:159], a[232:235], v[72:75]\n"
      "v_mfma_f32_16x16x32_bf16 v[76:79], v[156:159], a[236:239], v[76:79]\n"
      "v_mfma_f32_16x16x32_bf16 v[80:83], v[156:159], a[240:243], v[80:83]\n"
      "v_mfma_f32_16x16x32_bf16 v[84:87], v[156:159], a[244:247], v[84:87]\n"
      "v_mfma_f32_16x16x32_bf16 v[88:91], v[156:159], a[248:251], v[88:91]\n"
      "s_nop 3\n"
      "v_cvt_pk_bf16_f32 v72, v72, v73\n"
      "v_mfma_f32_16x16x32_bf16 v[92:95], v[156:159], a[252:255], v[92:95]\n"
      "s_waitcnt vmcnt(12)\n"
      "v_cvt_pk_bf16_f32 v73, v74, v75\n"
      "v_mfma_f32_16x16x32_bf16 v[104:107], v[160:163], a[8:11], 0\n"
      "ds_write_addtid_b32 v72 offset:6144\n"
      "v_cvt_pk_bf16_f32 v74, v76, v77\n"
      "v_mfma_f32_16x16x32_bf16 v[108:111], v[160:163], a[12:15], 0\n"
      "ds_write_addtid_b32 v73 offset:6400\n"
      "v_cvt_pk_bf16_f32 v75, v78, v79\n"
      "v_mfma_f32_16x16x32_bf16 v[112:115], v[160:163], a[16:19], 0\n"
      "ds_write_addtid_b32 v74 offset:6656\n"
      "v_cvt_pk_bf16_f32 v76, v80, v81\n"
      "v_mfma_f32_16x16x32_bf16 v[116:119], v[160:163], a[20:23], 0\n"
      "ds_write_addtid_b32 v75 offset:6912\n"
      "v_cvt_pk_bf16_f32 v77, v82, v83\n"
      "v_mfma_f32_16x16x32_bf16 v[120:123], v[160:163], a[24:27], 0\n"
      "ds_write_addtid_b32 v76 offset:7168\n"
      "v_cvt_pk_bf16_f32 v78, v84, v85\n"
      "v_mfma_f32_16x16x32_bf16 v[124:127], v[160:163], a[28:31], 0\n"
      "ds_write_addtid_b32 v77 offset:7424\n"
      "v_cvt_pk_bf16_f32 v79, v86, v87\n"
      "v_mfma_f32_16x16x32_bf16 v[104:107], v[164:167], a[40:43], v[104:107]\n"
      "ds_write_addtid_b32 v78 offset:7680\n"
      "v_cvt_pk_bf16_f32 v80, v88, v89\n"
      "v_mfma_f32_16x16x32_bf16 v[108:111], v[164:167], a[44:47], v[108:111]\n"
      "ds_write_addtid_b32 v79 offset:7936\n"
      "v_cvt_pk_bf16_f32 v81, v90, v91\n"
      "v_mfma_f32_16x16x32_bf16 v[112:115], v[164:167], a[48:51], v[112:115]\n"
      "ds_write_addtid_b32 v80 offset:8192\n"
      "v_cvt_pk_bf16_f32 v82, v92, v93\n"
      "v_mfma_f32_16x16x32_bf16 v[116:119], v[164:167], a[52:55], v[116:119]\n"
      "ds_write_addtid_b32 v81 offset:8448\n"
      "v_cvt_pk_bf16_f32 v83, v94, v95\n"
      "v_mfma_f32_16x16x32_bf16 v[120:123], v[164:167], a[56:59], v[120:123]\n"
      "ds_write_addtid_b32 v82 offset:8704\n"
      "v_mfma_f32_16x16x32_bf16 v[124:127], v[164:167], a[60:63], v[124:127]\n"
      "s_waitcnt vmcnt(10)\n"
      "ds_write_addtid_b32 v83 offset:8960\n"
      "v_mfma_f32_16x16x32_bf16 v[104:107], v[168:171], a[72:75], v[104:107]\n"
      "s_waitcnt vmcnt(2)\n"
      "v_bitop3_b32 v22, v15, v19, s37 bitop3:0x7e\n"
      "v_bitop3_b32 v22, v22, v23, s37 bitop3:0xf6\n"
      "v_cmp_ne_u32 vcc, 0, v22\n"
      "s_nop 1\n"
      "s_cbranch_vccnz .Ls_s18_%=\n"
      ".Ls_o19_%=:\n"
      "v_mfma_f32_16x16x32_bf16 v[108:111], v[168:171], a[76:79], v[108:111]\n"
      "v_mov_b32 v15, v20\n"
      "v_mov_b32 v19, v21\n"
      "v_mfma_f32_16x16x32_bf16 v[112:115], v[168:171], a[80:83], v[112:115]\n"
      "s_waitcnt lgkmcnt(0)\n"
      "s_barrier\n"
      "v_mfma_f32_16x16x32_bf16 v[116:119], v[168:171], a[84:87], v[116:119]\n"
      "ds_read2st64_b32 v[84:85], v244 offset0:24 offset1:25\n"
      "v_mfma_f32_16x16x32_bf16 v[120:123], v[168:171], a[88:91], v[120:123]\n"
      "ds_read2st64_b32 v[86:87], v245 offset0:28 offset1:29\n"
      "v_mfma_f32_16x16x32_bf16 v[124:127], v[168:171], a[92:95], v[124:127]\n"
      "ds_read2st64_b32 v[88:89], v244 offset0:26 offset1:27\n"
      "v_mfma_f32_16x16x32_bf16 v[104:107], v[172:175], a[104:107], v[104:107]\n"
      "ds_read2st64_b32 v[90:91], v245 offset0:30 offset1:31\n"
      "v_mfma_f32_16x16x32_bf16 v[108:111], v[172:175], a[108:111], v[108:111]\n"
      "ds_read2st64_b32 v[92:93], v246 offset0:32 offset1:33\n"
      "v_mfma_f32_16x16x32_bf16 v[112:115], v[172:175], a[112:115], v[112:115]\n"
      "ds_read2st64_b32 v[94:95], v246 offset0:34 offset1:35\n"
      "v_mfma_f32_16x16x32_bf16 v[116:119], v[172:175], a[116:119], v[116:119]\n"
      "v_mfma_f32_16x16x32_bf16 v[64:67], v[252:255], v[12:15], 0\n"
      "v_mfma_f32_16x16x32_bf16 v[120:123], v[172:175], a[120:123], v[120:123]\n"
      "v_mfma_f32_16x16x32_bf16 v[68:71], v[248:251], v[12:15], 0\n"
      "v_mfma_f32_16x16x32_bf16 v[124:127], v[172:175], a[124:127], v[124:127]\n"
      "v_mfma_f32_16x16x32_bf16 v[96:99], v[252:255], v[16:19], 0\n"
      "v_mfma_f32_16x16x32_bf16 v[104:107], v[176:179], a[136:139], v[104:107]\n"
      "s_waitcnt lgkmcnt(0)\n"
      "v_mfma_f32_16x16x32_bf16 v[64:67], v[250:253], v[84:87], v[64:67]\n"
      "v_mfma_f32_16x16x32_bf16 v[108:111], v[176:179], a[140:143], v[108:111]\n"
      "v_mfma_f32_16x16x32_bf16 v[68:71], v[250:253], v[88:91], v[68:71]\n"
      "v_mfma_f32_16x16x32_bf16 v[112:115], v[176:179], a[144:147], v[112:115]\n"
      "v_mfma_f32_16x16x32_bf16 v[64:67], v[252:255], v[92:95], v[64:67]\n"
      "v_mfma_f32_16x16x32_bf16 v[116:119], v[176:179], a[148:151], v[116:119]\n"
      "v_mfma_f32_16x16x32_bf16 v[68:71], v[248:251], v[92:95], v[68:71]\n"
      "v_mfma_f32_16x16x32_bf16 v[120:123], v[176:179], a[152:155], v[120:123]\n"
      "s_lshr_b32 s43, s42, 7\n"
      "s_cmp_eq_u32 s43, s36\n"
      "s_cbranch_scc0 .Ls_e20_%=\n"
      "s_lshl2_add_u32 s44, s35, s34\n"
      "s_lshl2_add_u32 s43, s44, s33\n"
      "s_lshl_b32 s43, s43, 12\n"
      "s_add_u32 s43, s43, 524288\n"
      "v_add_u32 v60, s43, v242\n"
      "v_mov_b32 v62, 0\n"
      "global_load_dwordx4 v[24:27], v60, s[58:59] offset:0 sc1\n"
      "global_load_dwordx4 v[28:31], v60, s[58:59] offset:1024 sc1\n"
      "global_load_dwordx4 v[32:35], v60, s[58:59] offset:2048 sc1\n"
      "global_load_dwordx4 v[36:39], v60, s[58:59] offset:3072 sc1\n"
      "global_load_dwordx4 v[40:43], v62, s[58:59] offset:1024 sc1\n"
      "v_and_b32 v58, 15, v243\n"
      "v_lshrrev_b32 v59, 5, v243\n"
      "v_lshl_or_b32 v58, v59, 4, v58\n"
      "s_lshl4_add_u32 s44, s42, s44\n"
      "s_lshl_b32 s44, s44, 6\n"
      "v_lshl_add_u32 v59, v58, 1, s44\n"
      "global_load_ushort v57, v59, s[16:17]\n"
      "s_mov_b32 s44, 131072\n"
      "v_lshl_add_u32 v58, v58, 2, s44\n"
      "s_lshl_b32 s43, s33, 13\n"
      "v_add_u32 v59, s43, v58\n"
      ".Ls_e20_%=:\n"
      "v_mfma_f32_16x16x32_bf16 v[124:127], v[176:179], a[156:159], v[124:127]\n"
      "v_mfma_f32_16x16x32_bf16 v[100:103], v[248:251], v[16:19], 0\n"
      "v_mfma_f32_16x16x32_bf16 v[104:107], v[180:183], a[168:171], v[104:107]\n"
      "v_mfma_f32_16x16x32_bf16 v[108:111], v[180:183], a[172:175], v[108:111]\n"
      "v_mfma_f32_16x16x32_bf16 v[112:115], v[180:183], a[176:179], v[112:115]\n"
      "v_mfma_f32_16x16x32_bf16 v[116:119], v[180:183], a[180:183], v[116:119]\n"
      "v_add_u32 v5, 12288, v242\n"
      "v_mfma_f32_16x16x32_bf16 v[120:123], v[180:183], a[184:187], v[120:123]\n"
      "global_load_dwordx4 v[44:47], v5, s[62:63] offset:0 sc1\n"
      "v_mfma_f32_16x16x32_bf16 v[124:127], v[180:183], a[188:191], v[124:127]\n"
      "global_load_dwordx4 v[48:51], v5, s[62:63] offset:1024 sc1\n"
      "v_mfma_f32_16x16x32_bf16 v[104:107], v[184:187], a[200:203], v[104:107]\n"
      "global_load_dwordx4 v[52:55], v5, s[62:63] offset:2048 sc1\n"
      "v_mfma_f32_16x16x32_bf16 v[108:111], v[184:187], a[204:207], v[108:111]\n"
      "v_mfma_f32_16x16x32_bf16 v[112:115], v[184:187], a[208:211], v[112:115]\n"
      "v_mfma_f32_16x16x32_bf16 v[116:119], v[184:187], a[212:215], v[116:119]\n"
      "v_mfma_f32_16x16x32_bf16 v[120:123], v[184:187], a[216:219], v[120:123]\n"
      "v_mfma_f32_16x16x32_bf16 v[124:127], v[184:187], a[220:223], v[124:127]\n"
      "v_mfma_f32_16x16x32_bf16 v[104:107], v[188:191], a[232:235], v[104:107]\n"
      "v_mfma_f32_16x16x32_bf16 v[108:111], v[188:191], a[236:239], v[108:111]\n"
      "v_mfma_f32_16x16x32_bf16 v[112:115], v[188:191], a[240:243], v[112:115]\n"
      "v_mfma_f32_16x16x32_bf16 v[116:119], v[188:191], a[244:247], v[116:119]\n"
      "v_mfma_f32_16x16x32_bf16 v[120:123], v[188:191], a[248:251], v[120:123]\n"
      "s_nop 3\n"
      "v_cvt_pk_bf16_f32 v104, v104, v105\n"
      "v_mfma_f32_16x16x32_bf16 v[124:127], v[188:191], a[252:255], v[124:127]\n"
      "v_cvt_pk_bf16_f32 v105, v106, v107\n"
      "v_mfma_f32_16x16x32_bf16 v[64:67], v[128:131], a[0:3], v[64:67]\n"
      "ds_write_addtid_b32 v104 offset:9216\n"
      "v_cvt_pk_bf16_f32 v106, v108, v109\n"
      "v_mfma_f32_16x16x32_bf16 v[64:67], v[132:135], a[32:35], v[64:67]\n"
      "ds_write_addtid_b32 v105 offset:9472\n"
      "v_cvt_pk_bf16_f32 v107, v110, v111\n"
      "v_mfma_f32_16x16x32_bf16 v[64:67], v[136:139], a[64:67], v[64:67]\n"
      "ds_write_addtid_b32 v106 offset:9728\n"
      "v_cvt_pk_bf16_f32 v108, v112, v113\n"
      "v_mfma_f32_16x16x32_bf16 v[64:67], v[140:143], a[96:99], v[64:67]\n"
      "ds_write_addtid_b32 v107 offset:9984\n"
      "v_cvt_pk_bf16_f32 v109, v114, v115\n"
      "v_mfma_f32_16x16x32_bf16 v[64:67], v[144:147], a[128:131], v[64:67]\n"
      "ds_write_addtid_b32 v108 offset:10240\n"
      "v_cvt_pk_bf16_f32 v110, v116, v117\n"
      "v_mfma_f32_16x16x32_bf16 v[64:67], v[148:151], a[160:163], v[64:67]\n"
      "ds_write_addtid_b32 v109 offset:10496\n"
      "v_cvt_pk_bf16_f32 v111, v118, v119\n"
      "v_mfma_f32_16x16x32_bf16 v[64:67], v[152:155], a[192:195], v[64:67]\n"
      "ds_write_addtid_b32 v110 offset:10752\n"
      "v_cvt_pk_bf16_f32 v112, v120, v121\n"
      "v_mfma_f32_16x16x32_bf16 v[64:67], v[156:159], a[224:227], v[64:67]\n"
      "ds_write_addtid_b32 v111 offset:11008\n"
      "v_cvt_pk_bf16_f32 v113, v122, v123\n"
      "v_mfma_f32_16x16x32_bf16 v[68:71], v[128:131], a[4:7], v[68:71]\n"
      "ds_write_addtid_b32 v112 offset:11264\n"
      "v_cvt_pk_bf16_f32 v114, v124, v125\n"
      "v_mfma_f32_16x16x32_bf16 v[68:71], v[132:135], a[36:39], v[68:71]\n"
      "ds_write_addtid_b32 v113 offset:11520\n"
      "v_cvt_pk_bf16_f32 v115, v126, v127\n"
      "v_mfma_f32_16x16x32_bf16 v[68:71], v[136:139], a[68:71], v[68:71]\n"
      "ds_write_addtid_b32 v114 offset:11776\n"
      "v_add_u32 v6, 24576, v242\n"
      "v_mfma_f32_16x16x32_bf16 v[68:71], v[140:143], a[100:103], v[68:71]\n"
      "global_load_dwordx4 v[72:75], v6, s[62:63] offset:0 sc1\n"
      "v_mfma_f32_16x16x32_bf16 v[68:71], v[144:147], a[132:135], v[68:71]\n"
      "global_load_dwordx4 v[76:79], v6, s[62:63] offset:1024 sc1\n"
      "v_mfma_f32_16x16x32_bf16 v[68:71], v[148:151], a[164:167], v[68:71]\n"
      "global_load_dwordx4 v[80:83], v6, s[62:63] offset:2048 sc1\n"
      "v_mfma_f32_16x16x32_bf16 v[68:71], v[152:155], a[196:199], v[68:71]\n"
      "ds_write_addtid_b32 v115 offset:12032\n"
      "v_mfma_f32_16x16x32_bf16 v[68:71], v[156:159], a[228:231], v[68:71]\n"
      "v_mfma_f32_16x16x32_bf16 v[96:99], v[160:163], a[0:3], v[96:99]\n"
      "v_mfma_f32_16x16x32_bf16 v[96:99], v[164:167], a[32:35], v[96:99]\n"
      "s_lshr_b32 s43, s42, 7\n"
      "s_cmp_eq_u32 s43, s36\n"
      "s_cbranch_scc0 .Ls_e21_%=\n"
      "s_waitcnt vmcnt(6)\n"
      "v_bitop3_b32 v61, v25, v27, s37 bitop3:0x7e\n"
      "v_bitop3_b32 v61, v61, v29, s37 bitop3:0xf6\n"
      "v_bitop3_b32 v61, v61, v31, s37 bitop3:0xf6\n"
      "v_bitop3_b32 v61, v61, v33, s37 bitop3:0xf6\n"
      "v_bitop3_b32 v61, v61, v35, s37 bitop3:0xf6\n"
      "v_bitop3_b32 v61, v61, v37, s37 bitop3:0xf6\n"
      "v_bitop3_b32 v61, v61, v39, s37 bitop3:0xf6\n"
      "v_bitop3_b32 v61, v61, v43, s37 bitop3:0xf6\n"
      "v_cmp_ne_u32 vcc, 0, v61\n"
      "s_nop 1\n"
      "s_cbranch_vccnz .Ls_s23_%=\n"
      ".Ls_o24_%=:\n"
      "v_add_f32 v24, v24, v28\n"
      "v_add_f32 v26, v26, v30\n"
      "v_add_f32 v32, v32, v36\n"
      "v_add_f32 v34, v34, v38\n"
      "v_add_f32 v24, v24, v32\n"
      "v_add_f32 v26, v26, v34\n"
      "s_nop 1\n"
      "v_permlane32_swap_b32 v24, v26\n"
      "v_add_f32 v24, v24, v26\n"
      "v_mov_b32 v26, v24\n"
      "s_nop 1\n"
      "v_permlane16_swap_b32 v24, v26\n"
      "v_add_f32 v24, v24, v26\n"
      "ds_write_b32 v59, v24\n"
      "s_waitcnt lgkmcnt(0)\n"
      ".Ls_e21_%=:\n"
      "v_mfma_f32_16x16x32_bf16 v[96:99], v[168:171], a[64:67], v[96:99]\n"
      "s_waitcnt lgkmcnt(0)\n"
      "s_barrier\n"
      "v_mfma_f32_16x16x32_bf16 v[96:99], v[172:175], a[96:99], v[96:99]\n"
      "s_lshr_b32 s43, s42, 5\n"
      "s_lshl2_add_u32 s44, s36, s33\n"
      "s_cmp_eq_u32 s43, s44\n"
      "s_cbranch_scc0 .Ls_e25_%=\n"
      "ds_read_b32 v24, v58 offset:0\n"
      "ds_read_b32 v25, v58 offset:8192\n"
      "ds_read_b32 v26, v58 offset:16384\n"
      "ds_read_b32 v27, v58 offset:24576\n"
      "v_mov_b32 v63, s37\n"
      "s_or_b32 s43, s34, s35\n"
      "s_cmp_eq_u32 s43, 0\n"
      "s_cbranch_scc0 .Ls_z26_%=\n"
      "s_mov_b64 exec, 1\n"
      "global_store_dword v62, v63, s[58:59] sc1\n"
      ".Ls_z26_%=:\n"
      "s_mov_b64 exec, -1\n"
      "s_waitcnt vmcnt(6)\n"
      "v_lshlrev_b32 v57, 16, v57\n"
      "v_and_b32 v56, 48, v243\n"
      "s_mov_b32 s44, 131072\n"
      "v_lshl_add_u32 v56, v56, 1, s44\n"
      "s_waitcnt lgkmcnt(0)\n"
      "v_add_f32 v24, v24, v25\n"
      "s_waitcnt lgkmcnt(0)\n"
      "v_add_f32 v26, v26, v27\n"
      "v_add_f32 v24, v24, v26\n"
      "v_cvt_pk_bf16_f32 v24, v24, v24\n"
      "v_and_b32 v24, 0xffff0000, v24\n"
      "v_mul_f32 v24, v41, v24\n"
      "v_cvt_pk_bf16_f32 v24, v24, v24\n"
      "v_and_b32 v24, 0xffff0000, v24\n"
      "v_add_f32 v24, v24, v57\n"
      "v_cvt_pk_bf16_f32 v24, v24, v24\n"
      "v_and_b32 v24, 0xffff0000, v24\n"
      "v_sub_f32 v42, 1.0, v40\n"
      "v_cvt_pk_bf16_f32 v42, v42, v42\n"
      "v_and_b32 v42, 0xffff0000, v42\n"
      "v_mul_f32 v24, v42, v24\n"
      "ds_write_b32 v58, v24 offset:1024\n"
      "ds_read_b128 v[28:31], v56 offset:1024\n"
      "ds_read_b128 v[32:35], v56 offset:1040\n"
      ".Ls_e25_%=:\n"
      "v_mfma_f32_16x16x32_bf16 v[96:99], v[176:179], a[128:131], v[96:99]\n"
      "ds_read2st64_b32 v[116:117], v244 offset0:36 offset1:37\n"
      "v_mfma_f32_16x16x32_bf16 v[96:99], v[180:183], a[160:163], v[96:99]\n"
      "ds_read2st64_b32 v[118:119], v245 offset0:40 offset1:41\n"
      "v_mfma_f32_16x16x32_bf16 v[96:99], v[184:187], a[192:195], v[96:99]\n"
      "ds_read2st64_b32 v[120:121], v244 offset0:38 offset1:39\n"
      "v_mfma_f32_16x16x32_bf16 v[96:99], v[188:191], a[224:227], v[96:99]\n"
      "ds_read2st64_b32 v[122:123], v245 offset0:42 offset1:43\n"
      "v_mfma_f32_16x16x32_bf16 v[100:103], v[160:163], a[4:7], v[100:103]\n"
      "ds_read2st64_b32 v[124:125], v246 offset0:44 offset1:45\n"
      "v_mfma_f32_16x16x32_bf16 v[100:103], v[164:167], a[36:39], v[100:103]\n"
      "ds_read2st64_b32 v[126:127], v246 offset0:46 offset1:47\n"
      "v_mfma_f32_16x16x32_bf16 v[100:103], v[168:171], a[68:71], v[100:103]\n"
      "v_mfma_f32_16x16x32_bf16 v[100:103], v[172:175], a[100:103], v[100:103]\n"
      "v_mfma_f32_16x16x32_bf16 v[100:103], v[176:179], a[132:135], v[100:103]\n"
      "s_waitcnt vmcnt(3)\n"
      "v_bitop3_b32 v54, v47, v51, s37 bitop3:0x7e\n"
      "v_bitop3_b32 v54, v54, v55, s37 bitop3:0xf6\n"
      "v_cmp_ne_u32 vcc, 0, v54\n"
      "s_nop 1\n"
      "s_cbranch_vccnz .Ls_s28_%=\n"
      ".Ls_o29_%=:\n"
      "v_mfma_f32_16x16x32_bf16 v[100:103], v[180:183], a[164:167], v[100:103]\n"
      "s_waitcnt lgkmcnt(0)\n"
      "v_mfma_f32_16x16x32_bf16 v[96:99], v[250:253], v[116:119], v[96:99]\n"
      "v_mov_b32 v47, v52\n"
      "v_mfma_f32_16x16x32_bf16 v[100:103], v[184:187], a[196:199], v[100:103]\n"
      "v_mfma_f32_16x16x32_bf16 v[100:103], v[250:253], v[120:123], v[100:103]\n"
      "v_mov_b32 v51, v53\n"
      "v_mfma_f32_16x16x32_bf16 v[100:103], v[188:191], a[228:231], v[100:103]\n"
      "v_mfma_f32_16x16x32_bf16 v[96:99], v[252:255], v[124:127], v[96:99]\n"
      "v_mfma_f32_16x16x32_bf16 v[100:103], v[248:251], v[124:127], v[100:103]\n"
      "v_mfma_f32_16x16x32_bf16 v[64:67], v[252:255], v[44:47], v[64:67]\n"
      "v_mfma_f32_16x16x32_bf16 v[68:71], v[248:251], v[44:47], v[68:71]\n"
      "v_mfma_f32_16x16x32_bf16 v[96:99], v[252:255], v[48:51], v[96:99]\n"
      "v_mfma_f32_16x16x32_bf16 v[100:103], v[248:251], v[48:51], v[100:103]\n"
      "s_lshr_b32 s43, s42, 5\n"
      "s_lshl2_add_u32 s44, s36, s33\n"
      "s_cmp_eq_u32 s43, s44\n"
      "s_cbranch_scc0 .Ls_e30_%=\n"
      "v_lshrrev_b32 v24, 10, v7\n"
      "v_subrev_u32 v24, s42, v24\n"
      "v_cmp_eq_u32 vcc, 0, v24\n"
      "s_nop 1\n"
      "s_mov_b64 exec, vcc\n"
      "v_mov_b32 v204, v40\n"
      "v_mov_b32 v208, v28\n"
      "v_mov_b32 v209, v29\n"
      "v_mov_b32 v210, v30\n"
      "v_mov_b32 v211, v31\n"
      "v_mov_b32 v216, v32\n"
      "v_mov_b32 v217, v33\n"
      "v_mov_b32 v218, v34\n"
      "v_mov_b32 v219, v35\n"
      "s_mov_b64 exec, -1\n"
      "v_cmp_eq_u32 vcc, -16, v24\n"
      "s_nop 1\n"
      "s_mov_b64 exec, vcc\n"
      "v_mov_b32 v206, v40\n"
      "v_mov_b32 v212, v28\n"
      "v_mov_b32 v213, v29\n"
      "v_mov_b32 v214, v30\n"
      "v_mov_b32 v215, v31\n"
      "v_mov_b32 v220, v32\n"
      "v_mov_b32 v221, v33\n"
      "v_mov_b32 v222, v34\n"
      "v_mov_b32 v223, v35\n"
      "s_mov_b64 exec, -1\n"
      ".Ls_e30_%=:\n"
      "s_waitcnt vmcnt(0)\n"
      "v_bitop3_b32 v82, v75, v79, s37 bitop3:0x7e\n"
      "v_bitop3_b32 v82, v82, v83, s37 bitop3:0xf6\n"
      "v_cmp_ne_u32 vcc, 0, v82\n"
      "s_nop 1\n"
      "s_cbranch_vccnz .Ls_s32_%=\n"
      ".Ls_o33_%=:\n"
      "v_mov_b32 v75, v80\n"
      "v_mov_b32 v79, v81\n"
      "s_nop 0\n"
      "v_mfma_f32_16x16x32_bf16 v[64:67], v[252:255], v[72:75], v[64:67]\n"
      "v_mfma_f32_16x16x32_bf16 v[96:99], v[252:255], v[76:79], v[96:99]\n"
      "v_mfma_f32_16x16x32_bf16 v[68:71], v[248:251], v[72:75], v[68:71]\n"
      "v_mfma_f32_16x16x32_bf16 v[100:103], v[248:251], v[76:79], v[100:103]\n"
      "s_nop 4\n"
      "v_fma_f32 v208, v204, v64, v208\n"
      "v_fma_f32 v209, v204, v65, v209\n"
      "v_fma_f32 v210, v204, v66, v210\n"
      "v_fma_f32 v211, v204, v67, v211\n"
      "v_fma_f32 v216, v204, v96, v216\n"
      "v_fma_f32 v217, v204, v97, v217\n"
      "v_fma_f32 v218, v204, v98, v218\n"
      "v_fma_f32 v219, v204, v99, v219\n"
      "v_cvt_pk_bf16_f32 v104, v208, v209\n"
      "v_cvt_pk_bf16_f32 v105, v210, v211\n"
      "v_cvt_pk_bf16_f32 v106, v216, v217\n"
      "v_cvt_pk_bf16_f32 v107, v218, v219\n"
      "global_store_dwordx4 v7, v[104:107], s[20:21]\n"
      "v_fma_f32 v212, v206, v68, v212\n"
      "v_fma_f32 v213, v206, v69, v213\n"
      "v_fma_f32 v214, v206, v70, v214\n"
      "v_fma_f32 v215, v206, v71, v215\n"
      "v_fma_f32 v220, v206, v100, v220\n"
      "v_fma_f32 v221, v206, v101, v221\n"
      "v_fma_f32 v222, v206, v102, v222\n"
      "v_fma_f32 v223, v206, v103, v223\n"
      "v_cvt_pk_bf16_f32 v108, v212, v213\n"
      "v_cvt_pk_bf16_f32 v109, v214, v215\n"
      "v_cvt_pk_bf16_f32 v110, v220, v221\n"
      "v_cvt_pk_bf16_f32 v111, v222, v223\n"
      "global_store_dwordx4 v7, v[108:111], s[68:69]\n"
      "s_endpgm\n"
      ".Lprod_%=:\n"
      "s_waitcnt lgkmcnt(0)\n"
      "s_add_u32 s68, s16, s44\n"
      "s_addc_u32 s69, s17, 0\n"
      "s_add_u32 s12, s12, s45\n"
      "s_addc_u32 s13, s13, 0\n"
      "s_add_u32 s58, s18, 4718592\n"
      "s_addc_u32 s59, s19, 0\n"
      "v_mov_b32 v235, 0\n"
      "global_load_dword v235, v235, s[58:59] sc1\n"
      "v_and_b32 v238, 15, v243\n"
      "v_lshrrev_b32 v239, 4, v243\n"
      "v_xor_b32 v239, v239, v238\n"
      "v_and_b32 v239, 7, v239\n"
      "v_lshlrev_b32 v239, 4, v239\n"
      "v_lshl_or_b32 v238, v238, 7, v239\n"
      "v_add_u32 v248, s41, v238\n"
      "v_xor_b32 v249, 64, v248\n"
      "v_add_u32 v250, 65536, v238\n"
      "v_xor_b32 v251, 64, v250\n"
      "v_and_b32 v252, 8, v243\n"
      "v_lshlrev_b32 v252, 7, v252\n"
      "v_and_or_b32 v252, v243, 48, v252\n"
      "v_add_u32 v252, s41, v252\n"
      "v_add_u32 v238, s80, v242\n"
      "v_and_b32 v238, 0x3ff, v238\n"
      "v_lshl_or_b32 v244, v236, 12, v237\n"
      "v_add_u32 v245, 31744, v244\n"
      "v_add_u32 v246, 63488, v244\n"
      "v_add_u32 v247, 95232, v244\n"
      "s_bitcmp1_b32 s34, 1\n"
      "s_cselect_b64 s[8:9], s[10:11], s[8:9]\n"
      "s_and_b32 s45, s34, 1\n"
      "s_lshl2_add_u32 s45, s45, s33\n"
      "s_lshl_b32 s45, s45, 9\n"
      "s_lshl_b32 s76, s36, 19\n"
      "s_add_u32 s45, s45, s76\n"
      "s_add_u32 s8, s8, s45\n"
      "s_addc_u32 s9, s9, 0\n"
      "s_lshr_b32 s39, s32, 6\n"
      "s_and_b32 s45, s35, 2\n"
      "s_xor_b32 s39, s39, s45\n"
      "s_xor_b32 s39, s39, s33\n"
      "s_lshl_b32 s45, s39, 7\n"
      "s_add_u32 s76, s33, 0\n"
      "s_and_b32 s76, s76, 3\n"
      "s_lshl_b32 s76, s76, 17\n"
      "s_or_b32 s76, s76, s45\n"
      "s_add_u32 s77, s33, 1\n"
      "s_and_b32 s77, s77, 3\n"
      "s_lshl_b32 s77, s77, 17\n"
      "s_or_b32 s77, s77, s45\n"
      "s_add_u32 s78, s33, 2\n"
      "s_and_b32 s78, s78, 3\n"
      "s_lshl_b32 s78, s78, 17\n"
      "s_or_b32 s78, s78, s45\n"
      "s_add_u32 s79, s33, 3\n"
      "s_and_b32 s79, s79, 3\n"
      "s_lshl_b32 s79, s79, 17\n"
      "s_or_b32 s79, s79, s45\n"
      "s_lshl2_add_u32 s43, s34, s35\n"
      "s_and_b32 s44, s40, 31\n"
      "s_mul_i32 s45, s43, 266240\n"
      "s_lshl_b32 s42, s44, 13\n"
      "s_add_u32 s45, s45, s42\n"
      "s_add_u32 s45, s45, 4096\n"
      "s_add_u32 s62, s18, s45\n"
      "s_addc_u32 s63, s19, 0\n"
      "s_bitcmp1_b32 s34, 1\n"
      "s_cselect_b32 s85, 0x80008000, 0\n"
      "s_lshl3_add_u32 s42, s43, s83\n"
      "s_lshl2_add_u32 s42, s42, s33\n"
      "s_lshl_b32 s42, s42, 9\n"
      "s_add_u32 s42, s42, 1048576\n"
      "s_mov_b32 s64, 0xaaaaaaaa\n"
      "s_mov_b32 s65, 0xaaaaaaaa\n"
      "s_mov_b32 s66, 0xcccccccc\n"
      "s_mov_b32 s67, 0xcccccccc\n"
      "s_add_u32 s45, s80, 512\n"
      "s_and_b32 s45, s45, 0x380\n"
      "s_add_u32 s46, s68, s45\n"
      "s_addc_u32 s47, s69, 0\n"
      "s_add_u32 m0, s41, 8192\n"
      "s_nop 0\n"
      "global_load_lds_dwordx4 v240, s[46:47] offset:0\n"
      "global_load_lds_dwordx4 v241, s[46:47] offset:1024\n"
      "global_load_lds_dwordx4 v254, s[46:47] offset:2048\n"
      "global_load_lds_dwordx4 v255, s[46:47] offset:3072\n"
      "s_add_u32 s45, s80, 768\n"
      "s_and_b32 s45, s45, 0x380\n"
      "s_add_u32 s46, s68, s45\n"
      "s_addc_u32 s47, s69, 0\n"
      "s_add_u32 m0, s41, 12288\n"
      "s_nop 0\n"
      "global_load_lds_dwordx4 v240, s[46:47] offset:0\n"
      "global_load_lds_dwordx4 v241, s[46:47] offset:1024\n"
      "global_load_lds_dwordx4 v254, s[46:47] offset:2048\n"
      "global_load_lds_dwordx4 v255, s[46:47] offset:3072\n"
      "s_waitcnt vmcnt(13)\n"
      "s_barrier\n"
      "ds_read_b128 v[0:3], v248 offset:0\n"
      "ds_read_b128 v[4:7], v248 offset:2048\n"
      "ds_read_b128 v[8:11], v249 offset:0\n"
      "ds_read_b128 v[12:15], v249 offset:2048\n"
      "ds_read_b128 a[192:195], v250 offset:0\n"
      "ds_read_b128 a[196:199], v250 offset:2048\n"
      "ds_read_b128 a[200:203], v250 offset:32768\n"
      "ds_read_b128 a[204:207], v251 offset:0\n"
      "ds_read_b128 a[208:211], v251 offset:2048\n"
      "ds_read_b128 a[212:215], v251 offset:32768\n"
      "s_waitcnt lgkmcnt(5)\n"
      "v_mfma_f32_16x16x32_bf16 v[192:195], v[0:3], a[192:195], 0\n"
      "s_add_u32 s70, s34, 1\n"
      "s_and_b32 s70, s70, 3\n"
      "s_lshl_b32 s70, s70, 15\n"
      "s_or_b32 s70, s70, s81\n"
      "s_xor_b32 s70, s70, 128\n"
      "s_add_u32 s70, s12, s70\n"
      "s_addc_u32 s71, s13, 0\n"
      "s_add_u32 m0, s82, 81920\n"
      "v_mfma_f32_16x16x32_bf16 v[196:199], v[4:7], a[192:195], 0\n"
      "global_load_lds_dwordx4 v240, s[70:71] offset:0\n"
      "ds_read_b128 a[216:219], v250 offset:4096\n"
      "s_waitcnt lgkmcnt(5)\n"
      "v_mfma_f32_16x16x32_bf16 v[200:203], v[0:3], a[196:199], 0\n"
      "ds_read_b128 a[220:223], v250 offset:6144\n"
      "ds_read_b128 a[224:227], v250 offset:36864\n"
      "v_mfma_f32_16x16x32_bf16 v[204:207], v[4:7], a[196:199], 0\n"
      "global_load_lds_dwordx4 v241, s[70:71] offset:1024\n"
      "s_waitcnt vmcnt(11)\n"
      "ds_read_b128 v[16:19], v248 offset:4096\n"
      "s_waitcnt lgkmcnt(7)\n"
      "v_mfma_f32_16x16x32_bf16 v[208:211], v[0:3], a[200:203], 0\n"
      "ds_read_b128 v[20:23], v248 offset:6144\n"
      "ds_read_b128 v[24:27], v249 offset:4096\n"
      "v_mfma_f32_16x16x32_bf16 v[212:215], v[4:7], a[200:203], 0\n"
      "global_load_lds_dwordx4 v254, s[70:71] offset:2048\n"
      "ds_read_b128 v[28:31], v249 offset:6144\n"
      "s_waitcnt lgkmcnt(9)\n"
      "v_mfma_f32_16x16x32_bf16 v[192:195], v[8:11], a[204:207], v[192:195]\n"
      "ds_read_b128 a[228:231], v251 offset:4096\n"
      "ds_read_b128 a[232:235], v251 offset:6144\n"
      "v_mfma_f32_16x16x32_bf16 v[196:199], v[12:15], a[204:207], v[196:199]\n"
      "global_load_lds_dwordx4 v255, s[70:71] offset:3072\n"
      "ds_read_b128 a[236:239], v251 offset:36864\n"
      "s_waitcnt lgkmcnt(11)\n"
      "v_mfma_f32_16x16x32_bf16 v[200:203], v[8:11], a[208:211], v[200:203]\n"
      "s_add_u32 s70, s34, 2\n"
      "s_and_b32 s70, s70, 3\n"
      "s_lshl_b32 s70, s70, 15\n"
      "s_or_b32 s70, s70, s81\n"
      "s_xor_b32 s70, s70, 128\n"
      "s_add_u32 s70, s12, s70\n"
      "s_addc_u32 s71, s13, 0\n"
      "s_add_u32 m0, s82, 114688\n"
      "v_mfma_f32_16x16x32_bf16 v[204:207], v[12:15], a[208:211], v[204:207]\n"
      "global_load_lds_dwordx4 v240, s[70:71] offset:0\n"
      "s_waitcnt lgkmcnt(10)\n"
      "v_mfma_f32_16x16x32_bf16 v[208:211], v[8:11], a[212:215], v[208:211]\n"
      "v_mfma_f32_16x16x32_bf16 v[212:215], v[12:15], a[212:215], v[212:215]\n"
      "global_load_lds_dwordx4 v241, s[70:71] offset:1024\n"
      "s_waitcnt lgkmcnt(6)\n"
      "v_mfma_f32_16x16x32_bf16 v[192:195], v[16:19], a[216:219], v[192:195]\n"
      "s_add_u32 s70, s80, 128\n"
      "s_and_b32 s70, s70, 0x380\n"
      "s_add_u32 s70, s68, s70\n"
      "s_addc_u32 s71, s69, 0\n"
      "s_add_u32 m0, s41, 0\n"
      "s_waitcnt lgkmcnt(5)\n"
      "v_mfma_f32_16x16x32_bf16 v[196:199], v[20:23], a[216:219], v[196:199]\n"
      "global_load_lds_dwordx4 v240, s[70:71] offset:0\n"
      "ds_read_b128 a[240:243], v250 offset:8192\n"
      "v_mfma_f32_16x16x32_bf16 v[200:203], v[16:19], a[220:223], v[200:203]\n"
      "ds_read_b128 a[244:247], v250 offset:10240\n"
      "ds_read_b128 a[248:251], v250 offset:40960\n"
      "v_mfma_f32_16x16x32_bf16 v[204:207], v[20:23], a[220:223], v[204:207]\n"
      "global_load_lds_dwordx4 v241, s[70:71] offset:1024\n"
      "s_waitcnt vmcnt(12)\n"
      "ds_read_b128 v[32:35], v248 offset:8192\n"
      "v_mfma_f32_16x16x32_bf16 v[208:211], v[16:19], a[224:227], v[208:211]\n"
      "ds_read_b128 v[36:39], v248 offset:10240\n"
      "ds_read_b128 v[40:43], v249 offset:8192\n"
      "v_mfma_f32_16x16x32_bf16 v[212:215], v[20:23], a[224:227], v[212:215]\n"
      "global_load_lds_dwordx4 v254, s[70:71] offset:2048\n"
      "ds_read_b128 v[44:47], v249 offset:10240\n"
      "s_waitcnt lgkmcnt(9)\n"
      "v_mfma_f32_16x16x32_bf16 v[192:195], v[24:27], a[228:231], v[192:195]\n"
      "ds_read_b128 a[252:255], v251 offset:8192\n"
      "ds_read_b128 a[192:195], v251 offset:10240\n"
      "v_mfma_f32_16x16x32_bf16 v[196:199], v[28:31], a[228:231], v[196:199]\n"
      "global_load_lds_dwordx4 v255, s[70:71] offset:3072\n"
      "ds_read_b128 a[196:199], v251 offset:40960\n"
      "s_waitcnt lgkmcnt(11)\n"
      "v_mfma_f32_16x16x32_bf16 v[200:203], v[24:27], a[232:235], v[200:203]\n"
      "s_add_u32 s70, s80, 384\n"
      "s_and_b32 s70, s70, 0x380\n"
      "s_add_u32 s70, s68, s70\n"
      "s_addc_u32 s71, s69, 0\n"
      "s_add_u32 m0, s41, 4096\n"
      "v_mfma_f32_16x16x32_bf16 v[204:207], v[28:31], a[232:235], v[204:207]\n"
      "global_load_lds_dwordx4 v240, s[70:71] offset:0\n"
      "s_waitcnt lgkmcnt(10)\n"
      "v_mfma_f32_16x16x32_bf16 v[208:211], v[24:27], a[236:239], v[208:211]\n"
      "v_mfma_f32_16x16x32_bf16 v[212:215], v[28:31], a[236:239], v[212:215]\n"
      "global_load_lds_dwordx4 v241, s[70:71] offset:1024\n"
      "s_waitcnt lgkmcnt(6)\n"
      "v_mfma_f32_16x16x32_bf16 v[192:195], v[32:35], a[240:243], v[192:195]\n"
      "ds_read_b128 a[200:203], v250 offset:12288\n"
      "ds_read_b128 a[204:207], v250 offset:14336\n"
      "s_waitcnt lgkmcnt(7)\n"
      "v_mfma_f32_16x16x32_bf16 v[196:199], v[36:39], a[240:243], v[196:199]\n"
      "global_load_lds_dwordx4 v254, s[70:71] offset:2048\n"
      "ds_read_b128 a[208:211], v250 offset:45056\n"
      "v_mfma_f32_16x16x32_bf16 v[200:203], v[32:35], a[244:247], v[200:203]\n"
      "s_waitcnt vmcnt(13)\n"
      "ds_read_b128 v[48:51], v248 offset:12288\n"
      "v_mfma_f32_16x16x32_bf16 v[204:207], v[36:39], a[244:247], v[204:207]\n"
      "global_load_lds_dwordx4 v255, s[70:71] offset:3072\n"
      "ds_read_b128 v[52:55], v248 offset:14336\n"
      "v_mfma_f32_16x16x32_bf16 v[208:211], v[32:35], a[248:251], v[208:211]\n"
      "s_add_u32 s70, s80, 640\n"
      "s_and_b32 s70, s70, 0x380\n"
      "s_add_u32 s70, s68, s70\n"
      "s_addc_u32 s71, s69, 0\n"
      "s_waitcnt lgkmcnt(8)\n"
      "s_add_u32 m0, s41, 8192\n"
      "v_mfma_f32_16x16x32_bf16 v[212:215], v[36:39], a[248:251], v[212:215]\n"
      "global_load_lds_dwordx4 v240, s[70:71] offset:0\n"
      "ds_read_b128 v[56:59], v249 offset:12288\n"
      "s_waitcnt lgkmcnt(8)\n"
      "v_mfma_f32_16x16x32_bf16 v[192:195], v[40:43], a[252:255], v[192:195]\n"
      "ds_read_b128 v[60:63], v249 offset:14336\n"
      "ds_read_b128 a[212:215], v251 offset:12288\n"
      "v_readfirstlane_b32 s37, v235\n"
      "v_mfma_f32_16x16x32_bf16 v[196:199], v[44:47], a[252:255], v[196:199]\n"
      "s_nop 0\n"
      "s_mul_i32 s37, s37, 0x9E3779B1\n"
      "global_load_lds_dwordx4 v241, s[70:71] offset:1024\n"
      "s_waitcnt lgkmcnt(9)\n"
      "v_mfma_f32_16x16x32_bf16 v[200:203], v[40:43], a[192:195], v[200:203]\n"
      "s_add_u32 s37, s37, 0x7F4A7C15\n"
      "ds_read_b128 a[216:219], v251 offset:14336\n"
      "ds_read_b128 a[220:223], v251 offset:45056\n"
      "s_mov_b32 s38, 0x8000\n"
      "v_mfma_f32_16x16x32_bf16 v[204:207], v[44:47], a[192:195], v[204:207]\n"
      "global_load_lds_dwordx4 v254, s[70:71] offset:2048\n"
      "s_waitcnt lgkmcnt(10)\n"
      "v_mfma_f32_16x16x32_bf16 v[208:211], v[40:43], a[196:199], v[208:211]\n"
      "v_mfma_f32_16x16x32_bf16 v[212:215], v[44:47], a[196:199], v[212:215]\n"
      "global_load_lds_dwordx4 v255, s[70:71] offset:3072\n"
      "s_waitcnt lgkmcnt(6)\n"
      "v_mfma_f32_16x16x32_bf16 v[192:195], v[48:51], a[200:203], v[192:195]\n"
      "s_add_u32 s70, s80, 896\n"
      "s_and_b32 s70, s70, 0x380\n"
      "s_add_u32 s70, s68, s70\n"
      "s_addc_u32 s71, s69, 0\n"
      "s_waitcnt lgkmcnt(3)\n"
      "s_add_u32 m0, s41, 12288\n"
      "v_mfma_f32_16x16x32_bf16 v[196:199], v[52:55], a[200:203], v[196:199]\n"
      "global_load_lds_dwordx4 v240, s[70:71] offset:0\n"
      "s_waitcnt vmcnt(13)\n"
      "s_barrier\n"
      "v_mfma_f32_16x16x32_bf16 v[200:203], v[48:51], a[204:207], v[200:203]\n"
      "ds_read_b128 a[224:227], v250 offset:16384\n"
      "ds_read_b128 a[228:231], v250 offset:18432\n"
      "v_mfma_f32_16x16x32_bf16 v[204:207], v[52:55], a[204:207], v[204:207]\n"
      "global_load_lds_dwordx4 v241, s[70:71] offset:1024\n"
      "ds_read_b128 a[232:235], v250 offset:49152\n"
      "v_mfma_f32_16x16x32_bf16 v[208:211], v[48:51], a[208:211], v[208:211]\n"
      "s_waitcnt vmcnt(10)\n"
      "ds_read_b128 v[64:67], v248 offset:0\n"
      "v_mfma_f32_16x16x32_bf16 v[212:215], v[52:55], a[208:211], v[212:215]\n"
      "global_load_lds_dwordx4 v254, s[70:71] offset:2048\n"
      "ds_read_b128 v[68:71], v248 offset:2048\n"
      "s_waitcnt lgkmcnt(7)\n"
      "v_mfma_f32_16x16x32_bf16 v[192:195], v[56:59], a[212:215], v[192:195]\n"
      "ds_read_b128 v[72:75], v249 offset:0\n"
      "ds_read_b128 v[76:79], v249 offset:2048\n"
      "v_mfma_f32_16x16x32_bf16 v[196:199], v[60:63], a[212:215], v[196:199]\n"
      "global_load_lds_dwordx4 v255, s[70:71] offset:3072\n"
      "ds_read_b128 a[236:239], v251 offset:16384\n"
      "s_waitcnt lgkmcnt(9)\n"
      "v_mfma_f32_16x16x32_bf16 v[200:203], v[56:59], a[216:219], v[200:203]\n"
      "ds_read_b128 a[240:243], v251 offset:18432\n"
      "ds_read_b128 a[244:247], v251 offset:49152\n"
      "v_mfma_f32_16x16x32_bf16 v[204:207], v[60:63], a[216:219], v[204:207]\n"
      "s_waitcnt lgkmcnt(10)\n"
      "v_mfma_f32_16x16x32_bf16 v[208:211], v[56:59], a[220:223], v[208:211]\n"
      "v_mfma_f32_16x16x32_bf16 v[212:215], v[60:63], a[220:223], v[212:215]\n"
      "s_waitcnt lgkmcnt(6)\n"
      "v_mfma_f32_16x16x32_bf16 v[192:195], v[64:67], a[224:227], v[192:195]\n"
      "s_waitcnt lgkmcnt(3)\n"
      "s_add_u32 m0, s41, 0\n"
      "s_nop 0\n"
      "global_load_lds_dwordx4 v238, s[24:25]\n"
      "s_add_u32 m0, s41, 1024\n"
      "s_nop 0\n"
      "global_load_lds_dwordx4 v238, s[22:23]\n"
      "v_mfma_f32_16x16x32_bf16 v[196:199], v[68:71], a[224:227], v[196:199]\n"
      "ds_read_b128 a[248:251], v250 offset:20480\n"
      "ds_read_b128 a[252:255], v250 offset:22528\n"
      "v_mfma_f32_16x16x32_bf16 v[200:203], v[64:67], a[228:231], v[200:203]\n"
      "s_add_u32 s70, s34, 2\n"
      "s_and_b32 s70, s70, 3\n"
      "s_lshl_b32 s70, s70, 15\n"
      "s_or_b32 s70, s70, s81\n"
      "s_add_u32 s70, s12, s70\n"
      "s_addc_u32 s71, s13, 0\n"
      "s_add_u32 m0, s82, 98304\n"
      "v_mfma_f32_16x16x32_bf16 v[204:207], v[68:71], a[228:231], v[204:207]\n"
      "global_load_lds_dwordx4 v254, s[70:71] offset:2048\n"
      "ds_read_b128 a[192:195], v250 offset:53248\n"
      "v_mfma_f32_16x16x32_bf16 v[208:211], v[64:67], a[232:235], v[208:211]\n"
      "s_waitcnt vmcnt(11)\n"
      "ds_read_b128 v[80:83], v248 offset:4096\n"
      "v_mfma_f32_16x16x32_bf16 v[212:215], v[68:71], a[232:235], v[212:215]\n"
      "ds_read_b128 v[84:87], v248 offset:6144\n"
      "ds_read_b128 v[88:91], v249 offset:4096\n"
      "s_waitcnt lgkmcnt(8)\n"
      "v_mfma_f32_16x16x32_bf16 v[192:195], v[72:75], a[236:239], v[192:195]\n"
      "global_load_lds_dwordx4 v255, s[70:71] offset:3072\n"
      "ds_read_b128 v[92:95], v249 offset:6144\n"
      "v_mfma_f32_16x16x32_bf16 v[196:199], v[76:79], a[236:239], v[196:199]\n"
      "ds_read_b128 a[196:199], v251 offset:20480\n"
      "ds_read_b128 a[200:203], v251 offset:22528\n"
      "s_waitcnt lgkmcnt(10)\n"
      "v_mfma_f32_16x16x32_bf16 v[200:203], v[72:75], a[240:243], v[200:203]\n"
      "s_add_u32 s70, s34, 2\n"
      "s_and_b32 s70, s70, 3\n"
      "s_lshl_b32 s70, s70, 15\n"
      "s_or_b32 s70, s70, s81\n"
      "s_xor_b32 s70, s70, 128\n"
      "s_add_u32 s70, s12, s70\n"
      "s_addc_u32 s71, s13, 0\n"
      "s_add_u32 m0, s82, 114688\n"
      "v_mfma_f32_16x16x32_bf16 v[204:207], v[76:79], a[240:243], v[204:207]\n"
      "global_load_lds_dwordx4 v254, s[70:71] offset:2048\n"
      "ds_read_b128 a[204:207], v251 offset:53248\n"
      "s_waitcnt lgkmcnt(10)\n"
      "v_mfma_f32_16x16x32_bf16 v[208:211], v[72:75], a[244:247], v[208:211]\n"
      "v_mfma_f32_16x16x32_bf16 v[212:215], v[76:79], a[244:247], v[212:215]\n"
      "s_waitcnt lgkmcnt(6)\n"
      "v_mfma_f32_16x16x32_bf16 v[192:195], v[80:83], a[248:251], v[192:195]\n"
      "global_load_lds_dwordx4 v255, s[70:71] offset:3072\n"
      "ds_read_b128 a[208:211], v250 offset:24576\n"
      "s_waitcnt lgkmcnt(6)\n"
      "v_mfma_f32_16x16x32_bf16 v[196:199], v[84:87], a[248:251], v[196:199]\n"
      "ds_read_b128 a[212:215], v250 offset:26624\n"
      "ds_read_b128 a[216:219], v250 offset:57344\n"
      "v_mfma_f32_16x16x32_bf16 v[200:203], v[80:83], a[252:255], v[200:203]\n"
      "s_add_u32 s70, s34, 3\n"
      "s_and_b32 s70, s70, 3\n"
      "s_lshl_b32 s70, s70, 15\n"
      "s_or_b32 s70, s70, s81\n"
      "s_add_u32 s70, s12, s70\n"
      "s_addc_u32 s71, s13, 0\n"
      "s_add_u32 m0, s82, 131072\n"
      "v_mfma_f32_16x16x32_bf16 v[204:207], v[84:87], a[252:255], v[204:207]\n"
      "global_load_lds_dwordx4 v240, s[70:71] offset:0\n"
      "s_waitcnt vmcnt(11)\n"
      "ds_read_b128 v[96:99], v248 offset:8192\n"
      "v_mfma_f32_16x16x32_bf16 v[208:211], v[80:83], a[192:195], v[208:211]\n"
      "ds_read_b128 v[100:103], v248 offset:10240\n"
      "ds_read_b128 v[104:107], v249 offset:8192\n"
      "v_mfma_f32_16x16x32_bf16 v[212:215], v[84:87], a[192:195], v[212:215]\n"
      "ds_read_b128 v[108:111], v249 offset:10240\n"
      "ds_read_b128 a[220:223], v251 offset:24576\n"
      "s_waitcnt lgkmcnt(10)\n"
      "v_mfma_f32_16x16x32_bf16 v[192:195], v[88:91], a[196:199], v[192:195]\n"
      "global_load_lds_dwordx4 v241, s[70:71] offset:1024\n"
      "ds_read_b128 a[224:227], v251 offset:26624\n"
      "v_mfma_f32_16x16x32_bf16 v[196:199], v[92:95], a[196:199], v[196:199]\n"
      "ds_read_b128 a[228:231], v251 offset:57344\n"
      "s_waitcnt lgkmcnt(11)\n"
      "v_mfma_f32_16x16x32_bf16 v[200:203], v[88:91], a[200:203], v[200:203]\n"
      "v_mfma_f32_16x16x32_bf16 v[204:207], v[92:95], a[200:203], v[204:207]\n"
      "global_load_lds_dwordx4 v254, s[70:71] offset:2048\n"
      "s_waitcnt lgkmcnt(10)\n"
      "v_mfma_f32_16x16x32_bf16 v[208:211], v[88:91], a[204:207], v[208:211]\n"
      "v_mfma_f32_16x16x32_bf16 v[212:215], v[92:95], a[204:207], v[212:215]\n"
      "s_waitcnt lgkmcnt(6)\n"
      "v_mfma_f32_16x16x32_bf16 v[192:195], v[96:99], a[208:211], v[192:195]\n"
      "global_load_lds_dwordx4 v255, s[70:71] offset:3072\n"
      "ds_read_b128 a[232:235], v250 offset:28672\n"
      "s_waitcnt lgkmcnt(6)\n"
      "v_mfma_f32_16x16x32_bf16 v[196:199], v[100:103], a[208:211], v[196:199]\n"
      "ds_read_b128 a[236:239], v250 offset:30720\n"
      "ds_read_b128 a[240:243], v250 offset:61440\n"
      "v_mfma_f32_16x16x32_bf16 v[200:203], v[96:99], a[212:215], v[200:203]\n"
      "s_add_u32 s70, s34, 3\n"
      "s_and_b32 s70, s70, 3\n"
      "s_lshl_b32 s70, s70, 15\n"
      "s_or_b32 s70, s70, s81\n"
      "s_xor_b32 s70, s70, 128\n"
      "s_add_u32 s70, s12, s70\n"
      "s_addc_u32 s71, s13, 0\n"
      "s_add_u32 m0, s82, 147456\n"
      "v_mfma_f32_16x16x32_bf16 v[204:207], v[100:103], a[212:215], v[204:207]\n"
      "global_load_lds_dwordx4 v240, s[70:71] offset:0\n"
      "s_waitcnt vmcnt(11)\n"
      "ds_read_b128 v[112:115], v248 offset:12288\n"
      "v_mfma_f32_16x16x32_bf16 v[208:211], v[96:99], a[216:219], v[208:211]\n"
      "ds_read_b128 v[116:119], v248 offset:14336\n"
      "ds_read_b128 v[120:123], v249 offset:12288\n"
      "v_mfma_f32_16x16x32_bf16 v[212:215], v[100:103], a[216:219], v[212:215]\n"
      "ds_read_b128 v[124:127], v249 offset:14336\n"
      "ds_read_b128 a[244:247], v251 offset:28672\n"
      "s_waitcnt lgkmcnt(10)\n"
      "v_mfma_f32_16x16x32_bf16 v[192:195], v[104:107], a[220:223], v[192:195]\n"
      "global_load_lds_dwordx4 v241, s[70:71] offset:1024\n"
      "ds_read_b128 a[248:251], v251 offset:30720\n"
      "v_mfma_f32_16x16x32_bf16 v[196:199], v[108:111], a[220:223], v[196:199]\n"
      "ds_read_b128 a[252:255], v251 offset:61440\n"
      "s_waitcnt lgkmcnt(11)\n"
      "v_mfma_f32_16x16x32_bf16 v[200:203], v[104:107], a[224:227], v[200:203]\n"
      "v_mfma_f32_16x16x32_bf16 v[204:207], v[108:111], a[224:227], v[204:207]\n"
      "global_load_lds_dwordx4 v254, s[70:71] offset:2048\n"
      "s_waitcnt lgkmcnt(10)\n"
      "v_mfma_f32_16x16x32_bf16 v[208:211], v[104:107], a[228:231], v[208:211]\n"
      "v_mfma_f32_16x16x32_bf16 v[212:215], v[108:111], a[228:231], v[212:215]\n"
      "s_waitcnt lgkmcnt(6)\n"
      "v_mfma_f32_16x16x32_bf16 v[192:195], v[112:115], a[232:235], v[192:195]\n"
      "s_waitcnt vmcnt(11)\n"
      "ds_read_b128 v[136:139], v252 offset:0\n"
      "s_waitcnt lgkmcnt(6)\n"
      "v_mfma_f32_16x16x32_bf16 v[196:199], v[116:119], a[232:235], v[196:199]\n"
      "ds_read_b128 v[140:143], v252 offset:64\n"
      "global_load_lds_dwordx4 v255, s[70:71] offset:3072\n"
      "s_waitcnt lgkmcnt(4)\n"
      "v_mfma_f32_16x16x32_bf16 v[192:195], v[120:123], a[244:247], v[192:195]\n"
      "ds_read_b128 v[144:147], v252 offset:256\n"
      "v_mfma_f32_16x16x32_bf16 v[196:199], v[124:127], a[244:247], v[196:199]\n"
      "v_mfma_f32_16x16x32_bf16 v[200:203], v[112:115], a[236:239], v[200:203]\n"
      "ds_read_b128 v[148:151], v252 offset:320\n"
      "v_mfma_f32_16x16x32_bf16 v[204:207], v[116:119], a[236:239], v[204:207]\n"
      "s_waitcnt lgkmcnt(5)\n"
      "v_mfma_f32_16x16x32_bf16 v[200:203], v[120:123], a[248:251], v[200:203]\n"
      "ds_read_b128 v[152:155], v252 offset:512\n"
      "v_cvt_pk_bf16_f32 v224, v192, v193\n"
      "v_cvt_pk_bf16_f32 v225, v194, v195\n"
      "v_mfma_f32_16x16x32_bf16 v[204:207], v[124:127], a[248:251], v[204:207]\n"
      "v_cvt_pk_bf16_f32 v226, v196, v197\n"
      "v_cvt_pk_bf16_f32 v227, v198, v199\n"
      "v_xor_b32 v224, s85, v224\n"
      "v_mfma_f32_16x16x32_bf16 v[208:211], v[112:115], a[240:243], v[208:211]\n"
      "ds_read_b128 v[156:159], v252 offset:576\n"
      "v_xor_b32 v225, s85, v225\n"
      "v_xor_b32 v226, s85, v226\n"
      "v_mfma_f32_16x16x32_bf16 v[212:215], v[116:119], a[240:243], v[212:215]\n"
      "v_xor_b32 v227, s85, v227\n"
      "s_waitcnt lgkmcnt(6)\n"
      "v_mfma_f32_16x16x32_bf16 v[208:211], v[120:123], a[252:255], v[208:211]\n"
      "ds_read_b128 v[160:163], v252 offset:768\n"
      "global_store_dwordx4 v242, v[224:227], s[62:63] offset:-4096 sc1\n"
      "v_cvt_pk_bf16_f32 v228, v200, v201\n"
      "v_mfma_f32_16x16x32_bf16 v[212:215], v[124:127], a[252:255], v[212:215]\n"
      "v_cvt_pk_bf16_f32 v229, v202, v203\n"
      "v_cvt_pk_bf16_f32 v230, v204, v205\n"
      "v_cvt_pk_bf16_f32 v231, v206, v207\n"
      "s_waitcnt lgkmcnt(6)\n"
      "v_mfma_f32_16x16x32_bf16 v[128:131], v[0:3], v[136:139], 0\n"
      "ds_read_b128 v[164:167], v252 offset:832\n"
      "v_xor_b32 v228, s85, v228\n"
      "v_xor_b32 v229, s85, v229\n"
      "v_mfma_f32_16x16x32_bf16 v[132:135], v[4:7], v[136:139], 0\n"
      "v_xor_b32 v230, s85, v230\n"
      "v_xor_b32 v231, s85, v231\n"
      "s_waitcnt lgkmcnt(6)\n"
      "v_mfma_f32_16x16x32_bf16 v[128:131], v[8:11], v[140:143], v[128:131]\n"
      "ds_read_b128 v[136:139], v252 offset:128\n"
      "global_store_dwordx4 v242, v[228:231], s[62:63] offset:-3072 sc1\n"
      "v_cvt_pk_bf16_f32 v232, v208, v209\n"
      "v_mfma_f32_16x16x32_bf16 v[132:135], v[12:15], v[140:143], v[132:135]\n"
      "v_cvt_pk_bf16_f32 v233, v210, v211\n"
      "v_cvt_pk_bf16_f32 v234, v212, v213\n"
      "v_cvt_pk_bf16_f32 v235, v214, v215\n"
      "s_waitcnt lgkmcnt(6)\n"
      "v_mfma_f32_16x16x32_bf16 v[128:131], v[16:19], v[144:147], v[128:131]\n"
      "ds_read_b128 v[140:143], v252 offset:192\n"
      "v_xor_b32 v232, s85, v232\n"
      "v_xor_b32 v233, s85, v233\n"
      "v_mfma_f32_16x16x32_bf16 v[132:135], v[20:23], v[144:147], v[132:135]\n"
      "v_xor_b32 v234, s85, v234\n"
      "v_xor_b32 v235, s85, v235\n"
      "s_waitcnt lgkmcnt(6)\n"
      "v_mfma_f32_16x16x32_bf16 v[128:131], v[24:27], v[148:151], v[128:131]\n"
      "ds_read_b128 v[144:147], v252 offset:384\n"
      "global_store_dwordx4 v242, v[232:235], s[62:63] offset:-2048 sc1\n"
      "v_mfma_f32_16x16x32_bf16 v[132:135], v[28:31], v[148:151], v[132:135]\n"
      "s_xor_b32 s70, s76, 0\n"
      "s_add_u32 s70, s8, s70\n"
      "s_addc_u32 s71, s9, 0\n"
      "s_add_u32 m0, s41, 4096\n"
      "s_waitcnt lgkmcnt(6)\n"
      "v_mfma_f32_16x16x32_bf16 v[128:131], v[32:35], v[152:155], v[128:131]\n"
      "s_add_u32 s48, s82, 32768\n"
      "ds_read_b128 v[148:151], v252 offset:448\n"
      "global_load_lds_dwordx4 v244, s[70:71] offset:0\n"
      "v_subrev_u32 v236, s41, v248\n"
      "v_mfma_f32_16x16x32_bf16 v[132:135], v[36:39], v[152:155], v[132:135]\n"
      "s_add_u32 s48, s48, 65536\n"
      "global_load_lds_dwordx4 v245, s[70:71] offset:1024\n"
      "v_add_u32 v236, s48, v236\n"
      "v_xor_b32 v237, 64, v236\n"
      "s_waitcnt lgkmcnt(6)\n"
      "v_mfma_f32_16x16x32_bf16 v[128:131], v[40:43], v[156:159], v[128:131]\n"
      "ds_read_b128 v[152:155], v252 offset:640\n"
      "global_load_lds_dwordx4 v246, s[70:71] offset:2048\n"
      "v_mfma_f32_16x16x32_bf16 v[132:135], v[44:47], v[156:159], v[132:135]\n"
      "global_load_lds_dwordx4 v247, s[70:71] offset:3072\n"
      "s_xor_b32 s70, s77, 0\n"
      "s_add_u32 s70, s8, s70\n"
      "s_addc_u32 s71, s9, 0\n"
      "s_add_u32 m0, s41, 8192\n"
      "s_waitcnt lgkmcnt(6)\n"
      "v_mfma_f32_16x16x32_bf16 v[128:131], v[48:51], v[160:163], v[128:131]\n"
      "ds_read_b128 v[156:159], v252 offset:704\n"
      "global_load_lds_dwordx4 v244, s[70:71] offset:0\n"
      "v_mfma_f32_16x16x32_bf16 v[132:135], v[52:55], v[160:163], v[132:135]\n"
      "global_load_lds_dwordx4 v245, s[70:71] offset:1024\n"
      "s_waitcnt lgkmcnt(6)\n"
      "v_mfma_f32_16x16x32_bf16 v[128:131], v[56:59], v[164:167], v[128:131]\n"
      "ds_read_b128 v[160:163], v252 offset:896\n"
      "global_load_lds_dwordx4 v246, s[70:71] offset:2048\n"
      "v_mfma_f32_16x16x32_bf16 v[132:135], v[60:63], v[164:167], v[132:135]\n"
      "global_load_lds_dwordx4 v247, s[70:71] offset:3072\n"
      "s_waitcnt vmcnt(11)\n"
      "s_barrier\n"
      "s_waitcnt lgkmcnt(6)\n"
      "v_mfma_f32_16x16x32_bf16 v[128:131], v[64:67], v[136:139], v[128:131]\n"
      "ds_read_b128 v[164:167], v252 offset:960\n"
      "s_add_u32 s70, s34, 4\n"
      "s_and_b32 s70, s70, 3\n"
      "s_lshl_b32 s70, s70, 15\n"
      "s_or_b32 s70, s70, s81\n"
      "s_add_u32 s70, s12, s70\n"
      "s_addc_u32 s71, s13, 0\n"
      "s_add_u32 m0, s82, 65536\n"
      "v_mfma_f32_16x16x32_bf16 v[132:135], v[68:71], v[136:139], v[132:135]\n"
      "global_load_lds_dwordx4 v240, s[70:71] offset:0\n"
      "ds_read_b128 a[192:195], v250 offset:34816\n"
      "s_waitcnt lgkmcnt(7)\n"
      "v_mfma_f32_16x16x32_bf16 v[128:131], v[72:75], v[140:143], v[128:131]\n"
      "ds_read_b128 a[196:199], v251 offset:34816\n"
      "ds_read_b128 a[200:203], v250 offset:38912\n"
      "v_mfma_f32_16x16x32_bf16 v[132:135], v[76:79], v[140:143], v[132:135]\n"
      "ds_read_b128 a[204:207], v251 offset:38912\n"
      "ds_read_b128 a[208:211], v250 offset:43008\n"
      "s_waitcnt lgkmcnt(10)\n"
      "v_mfma_f32_16x16x32_bf16 v[128:131], v[80:83], v[144:147], v[128:131]\n"
      "ds_read_b128 a[212:215], v251 offset:43008\n"
      "ds_read_b128 a[216:219], v250 offset:47104\n"
      "v_mfma_f32_16x16x32_bf16 v[132:135], v[84:87], v[144:147], v[132:135]\n"
      "global_load_lds_dwordx4 v241, s[70:71] offset:1024\n"
      "ds_read_b128 a[220:223], v251 offset:47104\n"
      "s_waitcnt lgkmcnt(12)\n"
      "v_mfma_f32_16x16x32_bf16 v[128:131], v[88:91], v[148:151], v[128:131]\n"
      "ds_read_b128 a[224:227], v250 offset:51200\n"
      "ds_read_b128 a[228:231], v251 offset:51200\n"
      "v_mfma_f32_16x16x32_bf16 v[132:135], v[92:95], v[148:151], v[132:135]\n"
      "ds_read_b128 a[232:235], v250 offset:55296\n"
      "s_waitcnt lgkmcnt(14)\n"
      "v_mfma_f32_16x16x32_bf16 v[128:131], v[96:99], v[152:155], v[128:131]\n"
      "v_mfma_f32_16x16x32_bf16 v[132:135], v[100:103], v[152:155], v[132:135]\n"
      "global_load_lds_dwordx4 v254, s[70:71] offset:2048\n"
      "ds_read_b128 a[236:239], v251 offset:55296\n"
      "s_waitcnt lgkmcnt(14)\n"
      "v_mfma_f32_16x16x32_bf16 v[128:131], v[104:107], v[156:159], v[128:131]\n"
      "v_mfma_f32_16x16x32_bf16 v[132:135], v[108:111], v[156:159], v[132:135]\n"
      "ds_read_b128 a[240:243], v250 offset:59392\n"
      "s_waitcnt lgkmcnt(14)\n"
      "v_mfma_f32_16x16x32_bf16 v[128:131], v[112:115], v[160:163], v[128:131]\n"
      "v_mfma_f32_16x16x32_bf16 v[132:135], v[116:119], v[160:163], v[132:135]\n"
      "global_load_lds_dwordx4 v255, s[70:71] offset:3072\n"
      "ds_read_b128 a[244:247], v251 offset:59392\n"
      "s_waitcnt lgkmcnt(14)\n"
      "v_mfma_f32_16x16x32_bf16 v[128:131], v[120:123], v[164:167], v[128:131]\n"
      "v_mfma_f32_16x16x32_bf16 v[132:135], v[124:127], v[164:167], v[132:135]\n"
      "ds_read_b128 a[248:251], v250 offset:63488\n"
      "s_waitcnt lgkmcnt(14)\n"
      "v_mfma_f32_16x16x32_bf16 v[216:219], v[0:3], a[192:195], 0\n"
      "v_mfma_f32_16x16x32_bf16 v[220:223], v[4:7], a[192:195], 0\n"
      "s_add_u32 s70, s34, 4\n"
      "s_and_b32 s70, s70, 3\n"
      "s_lshl_b32 s70, s70, 15\n"
      "s_or_b32 s70, s70, s81\n"
      "s_xor_b32 s70, s70, 128\n"
      "s_add_u32 s70, s12, s70\n"
      "s_addc_u32 s71, s13, 0\n"
      "s_add_u32 m0, s82, 81920\n"
      "s_waitcnt lgkmcnt(13)\n"
      "v_mfma_f32_16x16x32_bf16 v[216:219], v[8:11], a[196:199], v[216:219]\n"
      "s_mov_b32 s22, 0xf0f0f0f0\n"
      "global_load_lds_dwordx4 v240, s[70:71] offset:0\n"
      "ds_read_b128 a[252:255], v251 offset:63488\n"
      "s_mov_b32 s23, 0xf0f0f0f0\n"
      "v_mfma_f32_16x16x32_bf16 v[220:223], v[12:15], a[196:199], v[220:223]\n"
      "v_xor_b32 v250, 196608, v250\n"
      "v_xor_b32 v251, 196608, v251\n"
      "s_waitcnt lgkmcnt(13)\n"
      "v_mfma_f32_16x16x32_bf16 v[216:219], v[16:19], a[200:203], v[216:219]\n"
      "ds_read_b128 a[192:195], v250 offset:0\n"
      "v_cndmask_b32 v128, v128, v129, s[64:65]\n"
      "v_cndmask_b32 v130, v130, v131, s[64:65]\n"
      "v_mfma_f32_16x16x32_bf16 v[220:223], v[20:23], a[200:203], v[220:223]\n"
      "ds_read_b128 a[196:199], v250 offset:2048\n"
      "v_cndmask_b32 v132, v132, v133, s[64:65]\n"
      "v_cndmask_b32 v134, v134, v135, s[64:65]\n"
      "s_waitcnt lgkmcnt(14)\n"
      "v_mfma_f32_16x16x32_bf16 v[216:219], v[24:27], a[204:207], v[216:219]\n"
      "global_load_lds_dwordx4 v241, s[70:71] offset:1024\n"
      "v_cndmask_b32 v128, v128, v130, s[66:67]\n"
      "v_cndmask_b32 v132, v132, v134, s[66:67]\n"
      "v_mfma_f32_16x16x32_bf16 v[220:223], v[28:31], a[204:207], v[220:223]\n"
      "ds_read_b128 a[200:203], v251 offset:0\n"
      "v_lshrrev_b32 v133, 4, v243\n"
      "v_cndmask_b32 v128, v128, v132, s[22:23]\n"
      "s_waitcnt lgkmcnt(14)\n"
      "v_mfma_f32_16x16x32_bf16 v[216:219], v[32:35], a[208:211], v[216:219]\n"
      "v_and_b32 v134, 7, v243\n"
      "v_cvt_pk_bf16_f32 v128, v128, v128\n"
      "s_lshl_b32 s48, s40, 5\n"
      "v_mfma_f32_16x16x32_bf16 v[220:223], v[36:39], a[208:211], v[220:223]\n"
      "ds_read_b128 a[204:207], v251 offset:2048\n"
      "v_and_b32 v128, 0xffff0000, v128\n"
      "v_lshl_or_b32 v133, v133, 3, v134\n"
      "s_waitcnt lgkmcnt(14)\n"
      "v_mfma_f32_16x16x32_bf16 v[216:219], v[40:43], a[212:215], v[216:219]\n"
      "global_load_lds_dwordx4 v254, s[70:71] offset:2048\n"
      "v_add_u32 v133, s48, v133\n"
      "v_ashrrev_i32 v129, 31, v128\n"
      "v_mfma_f32_16x16x32_bf16 v[220:223], v[44:47], a[212:215], v[220:223]\n"
      "ds_read_b128 a[208:211], v250 offset:4096\n"
      "v_mul_f32 v130, 0xbfb8aa3b, v128\n"
      "v_or_b32 v129, 0x80000000, v129\n"
      "s_waitcnt lgkmcnt(14)\n"
      "v_mfma_f32_16x16x32_bf16 v[216:219], v[48:51], a[216:219], v[216:219]\n"
      "v_exp_f32 v130, v130\n"
      "v_xor_b32 v129, v129, v128\n"
      "v_sub_u32 v132, 0xfff, v133\n"
      "v_mfma_f32_16x16x32_bf16 v[220:223], v[52:55], a[216:219], v[220:223]\n"
      "ds_read_b128 a[212:215], v250 offset:6144\n"
      "v_add_f32 v130, 1.0, v130\n"
      "s_mov_b32 s49, 0xffff0000\n"
      "s_waitcnt lgkmcnt(14)\n"
      "v_mfma_f32_16x16x32_bf16 v[216:219], v[56:59], a[220:223], v[216:219]\n"
      "global_load_lds_dwordx4 v255, s[70:71] offset:3072\n"
      "v_and_or_b32 v129, v129, s49, v132\n"
      "v_rcp_f32 v130, v130\n"
      "v_mfma_f32_16x16x32_bf16 v[220:223], v[60:63], a[220:223], v[220:223]\n"
      "s_mov_b32 s64, 0x00ff00ff\n"
      "ds_read_b128 a[216:219], v251 offset:4096\n"
      "s_mov_b32 s65, 0x00ff00ff\n"
      "v_cndmask_b32 v129, v129, 0, s[64:65]\n"
      "s_waitcnt lgkmcnt(14)\n"
      "v_mfma_f32_16x16x32_bf16 v[216:219], v[64:67], a[224:227], v[216:219]\n"
      "v_add_u32 v238, s42, v242\n"
      "v_mov_b32 v224, s37\n"
      "v_mov_b32 v225, s37\n"
      "v_mov_b32 v226, s37\n"
      "v_mov_b32 v227, s37\n"
      "s_waitcnt vmcnt(16)\n"
      "s_mov_b64 exec, 0xff\n"
      "global_store_dwordx4 v238, v[224:227], s[58:59] offset:0 sc1\n"
      "s_mov_b64 exec, -1\n"
      "v_mfma_f32_16x16x32_bf16 v[220:223], v[68:71], a[224:227], v[220:223]\n"
      "ds_read_b128 a[220:223], v251 offset:6144\n"
      "v_cvt_pk_bf16_f32 v130, v130, v130\n"
      "s_lshr_b32 s48, s34, 1\n"
      "s_waitcnt lgkmcnt(14)\n"
      "v_mfma_f32_16x16x32_bf16 v[216:219], v[72:75], a[228:231], v[216:219]\n"
      "s_lshl2_add_u32 s48, s48, s35\n"
      "s_xor_b32 s70, s78, 0\n"
      "s_add_u32 s70, s8, s70\n"
      "s_addc_u32 s71, s9, 0\n"
      "s_add_u32 m0, s41, 12288\n"
      "v_mfma_f32_16x16x32_bf16 v[220:223], v[76:79], a[228:231], v[220:223]\n"
      "global_load_lds_dwordx4 v244, s[70:71] offset:0\n"
      "ds_read_b128 a[224:227], v250 offset:8192\n"
      "v_max_u32_dpp v129, v129, v129 quad_perm:[1,0,3,2] row_mask:0xf bank_mask:0xf\n"
      "s_waitcnt lgkmcnt(14)\n"
      "v_mfma_f32_16x16x32_bf16 v[216:219], v[80:83], a[232:235], v[216:219]\n"
      "v_and_b32 v130, 0xffff0000, v130\n"
      "s_lshl_b32 s49, s48, 15\n"
      "v_max_u32_dpp v129, v129, v129 quad_perm:[2,3,0,1] row_mask:0xf bank_mask:0xf\n"
      "v_mfma_f32_16x16x32_bf16 v[220:223], v[84:87], a[232:235], v[220:223]\n"
      "s_add_u32 s49, s49, 786432\n"
      "ds_read_b128 a[228:231], v250 offset:10240\n"
      "v_lshl_add_u32 v133, v133, 4, s49\n"
      "v_max_u32_dpp v129, v129, v129 row_half_mirror row_mask:0xf bank_mask:0xf\n"
      "s_waitcnt lgkmcnt(14)\n"
      "v_mfma_f32_16x16x32_bf16 v[216:219], v[88:91], a[236:239], v[216:219]\n"
      "s_lshl_b32 s48, s48, 6\n"
      "s_add_u32 s48, s48, s40\n"
      "v_max_u32_dpp v129, v129, v129 row_mirror row_mask:0xf bank_mask:0xf\n"
      "s_lshl_b32 s48, s48, 6\n"
      "v_mfma_f32_16x16x32_bf16 v[220:223], v[92:95], a[236:239], v[220:223]\n"
      "s_add_u32 s48, s48, 4096\n"
      "global_load_lds_dwordx4 v245, s[70:71] offset:1024\n"
      "ds_read_b128 a[232:235], v251 offset:8192\n"
      "v_mov_b32 v131, v129\n"
      "s_waitcnt lgkmcnt(14)\n"
      "v_mfma_f32_16x16x32_bf16 v[216:219], v[96:99], a[240:243], v[216:219]\n"
      "v_lshl_add_u32 v132, v134, 4, s48\n"
      "v_mov_b32 v137, s37\n"
      "v_permlane16_swap_b32 v129, v131\n"
      "v_mfma_f32_16x16x32_bf16 v[220:223], v[100:103], a[240:243], v[220:223]\n"
      "ds_read_b128 a[236:239], v251 offset:10240\n"
      "v_mov_b32 v138, s37\n"
      "v_mov_b32 v139, s37\n"
      "s_waitcnt lgkmcnt(14)\n"
      "v_mfma_f32_16x16x32_bf16 v[216:219], v[104:107], a[244:247], v[216:219]\n"
      "v_max_u32 v129, v129, v131\n"
      "v_cndmask_b32 v133, v132, v133, s[64:65]\n"
      "v_mov_b32 v131, v129\n"
      "v_mfma_f32_16x16x32_bf16 v[220:223], v[108:111], a[244:247], v[220:223]\n"
      "global_load_lds_dwordx4 v246, s[70:71] offset:2048\n"
      "ds_read_b128 a[240:243], v250 offset:12288\n"
      "v_permlane32_swap_b32 v129, v131\n"
      "s_waitcnt lgkmcnt(14)\n"
      "v_mfma_f32_16x16x32_bf16 v[216:219], v[112:115], a[248:251], v[216:219]\n"
      "v_max_u32 v129, v129, v131\n"
      "v_cndmask_b32 v136, v129, v130, s[64:65]\n"
      "s_mov_b32 s64, 0x00ff0fff\n"
      "v_mfma_f32_16x16x32_bf16 v[220:223], v[116:119], a[248:251], v[220:223]\n"
      "s_mov_b64 exec, s[64:65]\n"
      "global_store_dwordx4 v133, v[136:139], s[58:59] sc1\n"
      "s_mov_b64 exec, -1\n"
      "s_waitcnt lgkmcnt(13)\n"
      "v_mfma_f32_16x16x32_bf16 v[216:219], v[120:123], a[252:255], v[216:219]\n"
      "ds_read_b128 a[244:247], v250 offset:14336\n"
      "v_mfma_f32_16x16x32_bf16 v[220:223], v[124:127], a[252:255], v[220:223]\n"
      "global_load_lds_dwordx4 v247, s[70:71] offset:3072\n"
      "ds_read_b128 a[248:251], v251 offset:12288\n"
      "s_waitcnt lgkmcnt(14)\n"
      "v_mfma_f32_16x16x32_bf16 v[192:195], v[0:3], a[192:195], 0\n"
      "v_mfma_f32_16x16x32_bf16 v[196:199], v[4:7], a[192:195], 0\n"
      "ds_read_b128 a[252:255], v251 offset:14336\n"
      "s_waitcnt lgkmcnt(14)\n"
      "v_mfma_f32_16x16x32_bf16 v[200:203], v[0:3], a[196:199], 0\n"
      "s_xor_b32 s70, s79, 0\n"
      "s_add_u32 s70, s8, s70\n"
      "s_addc_u32 s71, s9, 0\n"
      "s_add_u32 m0, s41, 0\n"
      "v_mfma_f32_16x16x32_bf16 v[204:207], v[4:7], a[196:199], 0\n"
      "global_load_lds_dwordx4 v244, s[70:71] offset:0\n"
      "ds_read_b128 a[192:195], v250 offset:16384\n"
      "v_cvt_pk_bf16_f32 v224, v216, v217\n"
      "s_waitcnt lgkmcnt(14)\n"
      "v_mfma_f32_16x16x32_bf16 v[192:195], v[8:11], a[200:203], v[192:195]\n"
      "v_cvt_pk_bf16_f32 v225, v218, v219\n"
      "v_cvt_pk_bf16_f32 v226, v220, v221\n"
      "v_cvt_pk_bf16_f32 v227, v222, v223\n"
      "v_mfma_f32_16x16x32_bf16 v[196:199], v[12:15], a[200:203], v[196:199]\n"
      "ds_read_b128 a[196:199], v250 offset:18432\n"
      "v_xor_b32 v224, s85, v224\n"
      "v_xor_b32 v225, s85, v225\n"
      "s_waitcnt lgkmcnt(14)\n"
      "v_mfma_f32_16x16x32_bf16 v[200:203], v[8:11], a[204:207], v[200:203]\n"
      "v_xor_b32 v226, s85, v226\n"
      "v_xor_b32 v227, s85, v227\n"
      "s_lshl2_add_u32 s54, s34, s35\n"
      "v_mfma_f32_16x16x32_bf16 v[204:207], v[12:15], a[204:207], v[204:207]\n"
      "s_lshr_b32 s56, s39, 1\n"
      "global_load_lds_dwordx4 v245, s[70:71] offset:1024\n"
      "ds_read_b128 a[200:203], v251 offset:16384\n"
      "s_lshl1_add_u32 s56, s33, s56\n"
      "s_waitcnt lgkmcnt(14)\n"
      "v_mfma_f32_16x16x32_bf16 v[192:195], v[16:19], a[208:211], v[192:195]\n"
      "s_lshl_b32 s56, s56, 15\n"
      "s_waitcnt vmcnt(20)\n"
      "ds_read_b128 a[0:3], v248 offset:4096\n"
      "v_mfma_f32_16x16x32_bf16 v[196:199], v[20:23], a[208:211], v[196:199]\n"
      "s_mul_i32 s55, s54, 266240\n"
      "ds_read_b128 a[204:207], v251 offset:18432\n"
      "ds_read_b128 a[4:7], v248 offset:6144\n"
      "s_add_u32 s55, s55, 4096\n"
      "s_waitcnt lgkmcnt(15)\n"
      "v_mfma_f32_16x16x32_bf16 v[200:203], v[16:19], a[212:215], v[200:203]\n"
      "s_add_u32 s14, s18, s55\n"
      "s_addc_u32 s15, s19, 0\n"
      "ds_read_b128 a[32:35], v249 offset:4096\n"
      "v_mfma_f32_16x16x32_bf16 v[204:207], v[20:23], a[212:215], v[204:207]\n"
      "s_add_u32 s10, s14, s56\n"
      "s_addc_u32 s11, s15, 0\n"
      "global_load_lds_dwordx4 v246, s[70:71] offset:2048\n"
      "v_mfma_f32_16x16x32_bf16 v[192:195], v[24:27], a[216:219], v[192:195]\n"
      "s_xor_b32 s56, s56, 0x8000\n"
      "ds_read_b128 a[208:211], v250 offset:20480\n"
      "ds_read_b128 a[36:39], v249 offset:6144\n"
      "s_add_u32 s14, s14, s56\n"
      "s_addc_u32 s15, s15, 0\n"
      "v_mfma_f32_16x16x32_bf16 v[196:199], v[28:31], a[216:219], v[196:199]\n"
      "s_and_b32 s55, s39, 1\n"
      "ds_read_b128 a[212:215], v250 offset:22528\n"
      "s_waitcnt vmcnt(17)\n"
      "ds_read_b128 a[8:11], v248 offset:8192\n"
      "s_waitcnt lgkmcnt(15)\n"
      "v_mfma_f32_16x16x32_bf16 v[200:203], v[24:27], a[220:223], v[200:203]\n"
      "s_lshl_b32 s56, s55, 14\n"
      "ds_read_b128 a[12:15], v248 offset:10240\n"
      "ds_read_b128 a[40:43], v249 offset:8192\n"
      "v_add_u32 v240, s56, v242\n"
      "v_mfma_f32_16x16x32_bf16 v[204:207], v[28:31], a[220:223], v[204:207]\n"
      "global_load_lds_dwordx4 v247, s[70:71] offset:3072\n"
      "ds_read_b128 a[216:219], v251 offset:20480\n"
      "v_xor_b32 v241, 0x4000, v240\n"
      "v_mfma_f32_16x16x32_bf16 v[192:195], v[32:35], a[224:227], v[192:195]\n"
      "s_add_u32 s72, s10, 0x2000\n"
      "s_addc_u32 s73, s11, 0\n"
      "ds_read_b128 a[44:47], v249 offset:10240\n"
      "v_mfma_f32_16x16x32_bf16 v[196:199], v[36:39], a[224:227], v[196:199]\n"
      "s_add_u32 s74, s14, 0x2000\n"
      "s_addc_u32 s75, s15, 0\n"
      "ds_read_b128 a[220:223], v251 offset:22528\n"
      "v_mfma_f32_16x16x32_bf16 v[200:203], v[32:35], a[228:231], v[200:203]\n"
      "s_lshl2_add_u32 s55, s54, s33\n"
      "s_waitcnt lgkmcnt(7)\n"
      "s_xor_b32 s70, s76, 128\n"
      "s_add_u32 s70, s8, s70\n"
      "s_addc_u32 s71, s9, 0\n"
      "s_add_u32 m0, s41, 4096\n"
      "v_mfma_f32_16x16x32_bf16 v[204:207], v[36:39], a[228:231], v[204:207]\n"
      "s_lshl_b32 s55, s55, 12\n"
      "global_load_lds_dwordx4 v244, s[70:71] offset:0\n"
      "s_waitcnt vmcnt(11)\n"
      "s_barrier\n"
      "v_mfma_f32_16x16x32_bf16 v[192:195], v[40:43], a[232:235], v[192:195]\n"
      "s_add_u32 s55, s55, 1048576\n"
      "ds_read_b128 a[224:227], v250 offset:24576\n"
      "v_and_b32 v253, 7, v243\n"
      "v_lshl_add_u32 v253, v253, 9, s55\n"
      "v_mfma_f32_16x16x32_bf16 v[196:199], v[44:47], a[232:235], v[196:199]\n"
      "ds_read_b128 a[228:231], v250 offset:26624\n"
      "v_mfma_f32_16x16x32_bf16 v[200:203], v[40:43], a[236:239], v[200:203]\n"
      "v_mfma_f32_16x16x32_bf16 v[204:207], v[44:47], a[236:239], v[204:207]\n"
      "global_load_lds_dwordx4 v245, s[70:71] offset:1024\n"
      "ds_read_b128 a[232:235], v251 offset:24576\n"
      "v_mfma_f32_16x16x32_bf16 v[192:195], v[48:51], a[240:243], v[192:195]\n"
      "v_mfma_f32_16x16x32_bf16 v[196:199], v[52:55], a[240:243], v[196:199]\n"
      "ds_read_b128 a[236:239], v251 offset:26624\n"
      "v_mfma_f32_16x16x32_bf16 v[200:203], v[48:51], a[244:247], v[200:203]\n"
      "v_mfma_f32_16x16x32_bf16 v[204:207], v[52:55], a[244:247], v[204:207]\n"
      "global_load_lds_dwordx4 v246, s[70:71] offset:2048\n"
      "ds_read_b128 a[240:243], v250 offset:28672\n"
      "v_mfma_f32_16x16x32_bf16 v[192:195], v[56:59], a[248:251], v[192:195]\n"
      "v_mfma_f32_16x16x32_bf16 v[196:199], v[60:63], a[248:251], v[196:199]\n"
      "ds_read_b128 a[244:247], v250 offset:30720\n"
      "v_mfma_f32_16x16x32_bf16 v[200:203], v[56:59], a[252:255], v[200:203]\n"
      "v_mfma_f32_16x16x32_bf16 v[204:207], v[60:63], a[252:255], v[204:207]\n"
      "global_load_lds_dwordx4 v247, s[70:71] offset:3072\n"
      "ds_read_b128 a[248:251], v251 offset:28672\n"
      "v_mfma_f32_16x16x32_bf16 v[192:195], v[64:67], a[192:195], v[192:195]\n"
      "v_mfma_f32_16x16x32_bf16 v[196:199], v[68:71], a[192:195], v[196:199]\n"
      "ds_read_b128 a[252:255], v251 offset:30720\n"
      "v_xor_b32 v250, 196608, v250\n"
      "v_xor_b32 v251, 196608, v251\n"
      "v_mfma_f32_16x16x32_bf16 v[200:203], v[64:67], a[196:199], v[200:203]\n"
      "s_xor_b32 s70, s77, 128\n"
      "s_add_u32 s70, s8, s70\n"
      "s_addc_u32 s71, s9, 0\n"
      "s_add_u32 m0, s82, 98304\n"
      "v_mfma_f32_16x16x32_bf16 v[204:207], v[68:71], a[196:199], v[204:207]\n"
      "global_load_lds_dwordx4 v244, s[70:71] offset:0\n"
      "ds_read_b128 a[192:195], v250 offset:0\n"
      "v_mfma_f32_16x16x32_bf16 v[192:195], v[72:75], a[200:203], v[192:195]\n"
      "v_mfma_f32_16x16x32_bf16 v[196:199], v[76:79], a[200:203], v[196:199]\n"
      "ds_read_b128 a[196:199], v250 offset:2048\n"
      "v_mfma_f32_16x16x32_bf16 v[200:203], v[72:75], a[204:207], v[200:203]\n"
      "v_mfma_f32_16x16x32_bf16 v[204:207], v[76:79], a[204:207], v[204:207]\n"
      "global_load_lds_dwordx4 v245, s[70:71] offset:1024\n"
      "ds_read_b128 a[200:203], v251 offset:0\n"
      "v_mfma_f32_16x16x32_bf16 v[192:195], v[80:83], a[208:211], v[192:195]\n"
      "v_mfma_f32_16x16x32_bf16 v[196:199], v[84:87], a[208:211], v[196:199]\n"
      "ds_read_b128 a[204:207], v251 offset:2048\n"
      "s_waitcnt lgkmcnt(15)\n"
      "v_mfma_f32_16x16x32_bf16 v[200:203], v[80:83], a[212:215], v[200:203]\n"
      "v_mfma_f32_16x16x32_bf16 v[204:207], v[84:87], a[212:215], v[204:207]\n"
      "global_load_lds_dwordx4 v246, s[70:71] offset:2048\n"
      "ds_read_b128 a[208:211], v250 offset:4096\n"
      "s_waitcnt lgkmcnt(15)\n"
      "v_mfma_f32_16x16x32_bf16 v[192:195], v[88:91], a[216:219], v[192:195]\n"
      "s_waitcnt vmcnt(11)\n"
      "ds_read_b128 a[16:19], v248 offset:12288\n"
      "v_mfma_f32_16x16x32_bf16 v[196:199], v[92:95], a[216:219], v[196:199]\n"
      "ds_read_b128 a[212:215], v250 offset:6144\n"
      "ds_read_b128 a[20:23], v248 offset:14336\n"
      "s_waitcnt lgkmcnt(15)\n"
      "v_mfma_f32_16x16x32_bf16 v[200:203], v[88:91], a[220:223], v[200:203]\n"
      "ds_read_b128 a[48:51], v249 offset:12288\n"
      "ds_read_b128 a[52:55], v249 offset:14336\n"
      "v_mfma_f32_16x16x32_bf16 v[204:207], v[92:95], a[220:223], v[204:207]\n"
      "global_load_lds_dwordx4 v247, s[70:71] offset:3072\n"
      "ds_read_b128 a[216:219], v251 offset:4096\n"
      "v_mfma_f32_16x16x32_bf16 v[192:195], v[96:99], a[224:227], v[192:195]\n"
      "v_mfma_f32_16x16x32_bf16 v[196:199], v[100:103], a[224:227], v[196:199]\n"
      "ds_read_b128 a[220:223], v251 offset:6144\n"
      "s_waitcnt lgkmcnt(15)\n"
      "v_mfma_f32_16x16x32_bf16 v[200:203], v[96:99], a[228:231], v[200:203]\n"
      "s_xor_b32 s70, s78, 128\n"
      "s_add_u32 s70, s8, s70\n"
      "s_addc_u32 s71, s9, 0\n"
      "s_add_u32 m0, s82, 114688\n"
      "v_mfma_f32_16x16x32_bf16 v[204:207], v[100:103], a[228:231], v[204:207]\n"
      "global_load_lds_dwordx4 v244, s[70:71] offset:0\n"
      "ds_read_b128 a[224:227], v250 offset:8192\n"
      "v_mfma_f32_16x16x32_bf16 v[192:195], v[104:107], a[232:235], v[192:195]\n"
      "v_mfma_f32_16x16x32_bf16 v[196:199], v[108:111], a[232:235], v[196:199]\n"
      "ds_read_b128 a[228:231], v250 offset:10240\n"
      "v_mfma_f32_16x16x32_bf16 v[200:203], v[104:107], a[236:239], v[200:203]\n"
      "v_mfma_f32_16x16x32_bf16 v[204:207], v[108:111], a[236:239], v[204:207]\n"
      "global_load_lds_dwordx4 v245, s[70:71] offset:1024\n"
      "ds_read_b128 a[232:235], v251 offset:8192\n"
      "v_mfma_f32_16x16x32_bf16 v[192:195], v[112:115], a[240:243], v[192:195]\n"
      "v_mfma_f32_16x16x32_bf16 v[196:199], v[116:119], a[240:243], v[196:199]\n"
      "ds_read_b128 a[236:239], v251 offset:10240\n"
      "s_waitcnt lgkmcnt(15)\n"
      "s_barrier\n"
      "v_mfma_f32_16x16x32_bf16 v[200:203], v[112:115], a[244:247], v[200:203]\n"
      "v_mfma_f32_16x16x32_bf16 v[204:207], v[116:119], a[244:247], v[204:207]\n"
      "global_load_lds_dwordx4 v246, s[70:71] offset:2048\n"
      "ds_read_b128 a[240:243], v250 offset:12288\n"
      "v_mfma_f32_16x16x32_bf16 v[192:195], v[120:123], a[248:251], v[192:195]\n"
      "s_waitcnt vmcnt(11)\n"
      "ds_read_b128 a[24:27], v248 offset:0\n"
      "v_mfma_f32_16x16x32_bf16 v[196:199], v[124:127], a[248:251], v[196:199]\n"
      "ds_read_b128 a[244:247], v250 offset:14336\n"
      "ds_read_b128 a[28:31], v248 offset:2048\n"
      "v_mfma_f32_16x16x32_bf16 v[200:203], v[120:123], a[252:255], v[200:203]\n"
      "ds_read_b128 a[56:59], v249 offset:0\n"
      "ds_read_b128 a[60:63], v249 offset:2048\n"
      "v_mfma_f32_16x16x32_bf16 v[204:207], v[124:127], a[252:255], v[204:207]\n"
      "global_load_lds_dwordx4 v247, s[70:71] offset:3072\n"
      "ds_read_b128 a[248:251], v251 offset:12288\n"
      "v_mfma_f32_16x16x32_bf16 v[208:211], v[0:3], a[192:195], 0\n"
      "v_mfma_f32_16x16x32_bf16 v[212:215], v[4:7], a[192:195], 0\n"
      "ds_read_b128 a[252:255], v251 offset:14336\n"
      "s_waitcnt lgkmcnt(15)\n"
      "v_mfma_f32_16x16x32_bf16 v[216:219], v[0:3], a[196:199], 0\n"
      "global_load_dword v252, v253, s[58:59] offset:0 sc1\n"
      "s_xor_b32 s70, s79, 128\n"
      "s_add_u32 s70, s8, s70\n"
      "s_addc_u32 s71, s9, 0\n"
      "s_add_u32 m0, s82, 131072\n"
      "v_mfma_f32_16x16x32_bf16 v[220:223], v[4:7], a[196:199], 0\n"
      "global_load_lds_dwordx4 v244, s[70:71] offset:0\n"
      "ds_read_b128 a[192:195], v250 offset:16384\n"
      "v_cvt_pk_bf16_f32 v228, v192, v193\n"
      "v_mfma_f32_16x16x32_bf16 v[208:211], v[8:11], a[200:203], v[208:211]\n"
      "v_cvt_pk_bf16_f32 v229, v194, v195\n"
      "v_cvt_pk_bf16_f32 v230, v196, v197\n"
      "v_cvt_pk_bf16_f32 v231, v198, v199\n"
      "v_mfma_f32_16x16x32_bf16 v[212:215], v[12:15], a[200:203], v[212:215]\n"
      "ds_read_b128 a[196:199], v250 offset:18432\n"
      "v_xor_b32 v228, s85, v228\n"
      "v_xor_b32 v229, s85, v229\n"
      "v_mfma_f32_16x16x32_bf16 v[216:219], v[8:11], a[204:207], v[216:219]\n"
      "v_xor_b32 v230, s85, v230\n"
      "v_xor_b32 v231, s85, v231\n"
      "v_cvt_pk_bf16_f32 v232, v200, v201\n"
      "v_mfma_f32_16x16x32_bf16 v[220:223], v[12:15], a[204:207], v[220:223]\n"
      "global_load_lds_dwordx4 v245, s[70:71] offset:1024\n"
      "ds_read_b128 a[200:203], v251 offset:16384\n"
      "v_cvt_pk_bf16_f32 v233, v202, v203\n"
      "v_mfma_f32_16x16x32_bf16 v[208:211], v[16:19], a[208:211], v[208:211]\n"
      "v_cvt_pk_bf16_f32 v234, v204, v205\n"
      "v_cvt_pk_bf16_f32 v235, v206, v207\n"
      "v_xor_b32 v232, s85, v232\n"
      "v_mfma_f32_16x16x32_bf16 v[212:215], v[20:23], a[208:211], v[212:215]\n"
      "ds_read_b128 a[204:207], v251 offset:18432\n"
      "v_xor_b32 v233, s85, v233\n"
      "v_xor_b32 v234, s85, v234\n"
      "v_mfma_f32_16x16x32_bf16 v[216:219], v[16:19], a[212:215], v[216:219]\n"
      "v_xor_b32 v235, s85, v235\n"
      "v_mfma_f32_16x16x32_bf16 v[220:223], v[20:23], a[212:215], v[220:223]\n"
      "global_load_lds_dwordx4 v246, s[70:71] offset:2048\n"
      "ds_read_b128 a[208:211], v250 offset:20480\n"
      "s_waitcnt lgkmcnt(15)\n"
      "v_mfma_f32_16x16x32_bf16 v[208:211], v[24:27], a[216:219], v[208:211]\n"
      "s_waitcnt vmcnt(12)\n"
      "ds_read_b128 a[64:67], v248 offset:4096\n"
      "v_mfma_f32_16x16x32_bf16 v[212:215], v[28:31], a[216:219], v[212:215]\n"
      "ds_read_b128 a[212:215], v250 offset:22528\n"
      "ds_read_b128 a[68:71], v248 offset:6144\n"
      "v_mfma_f32_16x16x32_bf16 v[216:219], v[24:27], a[220:223], v[216:219]\n"
      "ds_read_b128 a[96:99], v249 offset:4096\n"
      "ds_read_b128 a[100:103], v249 offset:6144\n"
      "v_mfma_f32_16x16x32_bf16 v[220:223], v[28:31], a[220:223], v[220:223]\n"
      "global_load_lds_dwordx4 v247, s[70:71] offset:3072\n"
      "ds_read_b128 a[216:219], v251 offset:20480\n"
      "v_mfma_f32_16x16x32_bf16 v[208:211], v[32:35], a[224:227], v[208:211]\n"
      "v_mfma_f32_16x16x32_bf16 v[212:215], v[36:39], a[224:227], v[212:215]\n"
      "ds_read_b128 a[220:223], v251 offset:22528\n"
      "v_mfma_f32_16x16x32_bf16 v[216:219], v[32:35], a[228:231], v[216:219]\n"
      "s_xor_b32 s70, s76, 256\n"
      "s_add_u32 s70, s8, s70\n"
      "s_addc_u32 s71, s9, 0\n"
      "s_add_u32 m0, s82, 147456\n"
      "v_mfma_f32_16x16x32_bf16 v[220:223], v[36:39], a[228:231], v[220:223]\n"
      "global_load_lds_dwordx4 v244, s[70:71] offset:0\n"
      "ds_read_b128 a[224:227], v250 offset:24576\n"
      "s_waitcnt lgkmcnt(15)\n"
      "v_mfma_f32_16x16x32_bf16 v[208:211], v[40:43], a[232:235], v[208:211]\n"
      "v_mfma_f32_16x16x32_bf16 v[212:215], v[44:47], a[232:235], v[212:215]\n"
      "ds_read_b128 a[228:231], v250 offset:26624\n"
      "v_mfma_f32_16x16x32_bf16 v[216:219], v[40:43], a[236:239], v[216:219]\n"
      "v_mfma_f32_16x16x32_bf16 v[220:223], v[44:47], a[236:239], v[220:223]\n"
      "global_load_lds_dwordx4 v245, s[70:71] offset:1024\n"
      "ds_read_b128 a[232:235], v251 offset:24576\n"
      "v_mfma_f32_16x16x32_bf16 v[208:211], v[48:51], a[240:243], v[208:211]\n"
      "v_mfma_f32_16x16x32_bf16 v[212:215], v[52:55], a[240:243], v[212:215]\n"
      "ds_read_b128 a[236:239], v251 offset:26624\n"
      "v_mfma_f32_16x16x32_bf16 v[216:219], v[48:51], a[244:247], v[216:219]\n"
      "v_mfma_f32_16x16x32_bf16 v[220:223], v[52:55], a[244:247], v[220:223]\n"
      "global_load_lds_dwordx4 v246, s[70:71] offset:2048\n"
      "ds_read_b128 a[240:243], v250 offset:28672\n"
      "s_waitcnt lgkmcnt(15)\n"
      "v_mfma_f32_16x16x32_bf16 v[208:211], v[56:59], a[248:251], v[208:211]\n"
      "s_waitcnt vmcnt(12)\n"
      "ds_read_b128 a[72:75], v236 offset:0\n"
      "v_mfma_f32_16x16x32_bf16 v[212:215], v[60:63], a[248:251], v[212:215]\n"
      "ds_read_b128 a[244:247], v250 offset:30720\n"
      "ds_read_b128 a[76:79], v236 offset:2048\n"
      "v_mfma_f32_16x16x32_bf16 v[216:219], v[56:59], a[252:255], v[216:219]\n"
      "ds_read_b128 a[104:107], v237 offset:0\n"
      "ds_read_b128 a[108:111], v237 offset:2048\n"
      "v_mfma_f32_16x16x32_bf16 v[220:223], v[60:63], a[252:255], v[220:223]\n"
      "global_load_lds_dwordx4 v247, s[70:71] offset:3072\n"
      "ds_read_b128 a[248:251], v251 offset:28672\n"
      "v_mfma_f32_16x16x32_bf16 v[208:211], v[64:67], a[192:195], v[208:211]\n"
      "v_mfma_f32_16x16x32_bf16 v[212:215], v[68:71], a[192:195], v[212:215]\n"
      "ds_read_b128 a[252:255], v251 offset:30720\n"
      "v_mfma_f32_16x16x32_bf16 v[216:219], v[64:67], a[196:199], v[216:219]\n"
      "s_waitcnt vmcnt(8)\n"
      "v_cmp_ne_u32 vcc, s37, v252\n"
      "s_nop 1\n"
      "s_cbranch_vccnz .Lp_s2_%=\n"
      ".Lp_o3_%=:\n"
      "v_mfma_f32_16x16x32_bf16 v[220:223], v[68:71], a[196:199], v[220:223]\n"
      "global_load_dwordx4 v[128:131], v240, s[10:11] offset:-4096 sc1\n"
      "s_waitcnt lgkmcnt(15)\n"
      "v_mfma_f32_16x16x32_bf16 v[208:211], v[72:75], a[200:203], v[208:211]\n"
      "v_mfma_f32_16x16x32_bf16 v[212:215], v[76:79], a[200:203], v[212:215]\n"
      "v_mfma_f32_16x16x32_bf16 v[216:219], v[72:75], a[204:207], v[216:219]\n"
      "v_mfma_f32_16x16x32_bf16 v[220:223], v[76:79], a[204:207], v[220:223]\n"
      "global_load_dwordx4 v[132:135], v240, s[10:11] offset:-3072 sc1\n"
      "v_mfma_f32_16x16x32_bf16 v[208:211], v[80:83], a[208:211], v[208:211]\n"
      "v_mfma_f32_16x16x32_bf16 v[212:215], v[84:87], a[208:211], v[212:215]\n"
      "v_mfma_f32_16x16x32_bf16 v[216:219], v[80:83], a[212:215], v[216:219]\n"
      "v_mfma_f32_16x16x32_bf16 v[220:223], v[84:87], a[212:215], v[220:223]\n"
      "global_load_dwordx4 v[136:139], v240, s[10:11] offset:-2048 sc1\n"
      "s_waitcnt lgkmcnt(13)\n"
      "v_mfma_f32_16x16x32_bf16 v[208:211], v[88:91], a[216:219], v[208:211]\n"
      "ds_read_b128 a[80:83], v236 offset:16384\n"
      "ds_read_b128 a[84:87], v236 offset:18432\n"
      "v_mfma_f32_16x16x32_bf16 v[212:215], v[92:95], a[216:219], v[212:215]\n"
      "ds_read_b128 a[112:115], v237 offset:16384\n"
      "ds_read_b128 a[116:119], v237 offset:18432\n"
      "s_waitcnt lgkmcnt(15)\n"
      "v_mfma_f32_16x16x32_bf16 v[216:219], v[88:91], a[220:223], v[216:219]\n"
      "v_mfma_f32_16x16x32_bf16 v[220:223], v[92:95], a[220:223], v[220:223]\n"
      "global_load_dwordx4 v[140:143], v240, s[72:73] offset:-4096 sc1\n"
      "v_mfma_f32_16x16x32_bf16 v[208:211], v[96:99], a[224:227], v[208:211]\n"
      "v_mfma_f32_16x16x32_bf16 v[212:215], v[100:103], a[224:227], v[212:215]\n"
      "s_waitcnt lgkmcnt(14)\n"
      "v_mfma_f32_16x16x32_bf16 v[216:219], v[96:99], a[228:231], v[216:219]\n"
      "v_mfma_f32_16x16x32_bf16 v[220:223], v[100:103], a[228:231], v[220:223]\n"
      "global_load_dwordx4 v[144:147], v240, s[72:73] offset:-3072 sc1\n"
      "s_waitcnt lgkmcnt(13)\n"
      "v_mfma_f32_16x16x32_bf16 v[208:211], v[104:107], a[232:235], v[208:211]\n"
      "v_mfma_f32_16x16x32_bf16 v[212:215], v[108:111], a[232:235], v[212:215]\n"
      "s_waitcnt lgkmcnt(12)\n"
      "v_mfma_f32_16x16x32_bf16 v[216:219], v[104:107], a[236:239], v[216:219]\n"
      "v_mfma_f32_16x16x32_bf16 v[220:223], v[108:111], a[236:239], v[220:223]\n"
      "global_load_dwordx4 v[148:151], v240, s[72:73] offset:-2048 sc1\n"
      "s_waitcnt lgkmcnt(11)\n"
      "v_mfma_f32_16x16x32_bf16 v[208:211], v[112:115], a[240:243], v[208:211]\n"
      "global_store_dwordx4 v242, v[224:227], s[62:63] offset:-1024 sc1\n"
      "global_store_dwordx4 v242, v[228:231], s[62:63] offset:0 sc1\n"
      "v_mfma_f32_16x16x32_bf16 v[212:215], v[116:119], a[240:243], v[212:215]\n"
      "global_store_dwordx4 v242, v[232:235], s[62:63] offset:1024 sc1\n"
      "s_waitcnt lgkmcnt(9)\n"
      "v_mfma_f32_16x16x32_bf16 v[216:219], v[112:115], a[244:247], v[216:219]\n"
      "v_mfma_f32_16x16x32_bf16 v[220:223], v[116:119], a[244:247], v[220:223]\n"
      "s_waitcnt lgkmcnt(5)\n"
      "v_mfma_f32_16x16x32_bf16 v[208:211], v[120:123], a[248:251], v[208:211]\n"
      "s_waitcnt vmcnt(13)\n"
      "ds_read_b128 a[88:91], v236 offset:32768\n"
      "v_mfma_f32_16x16x32_bf16 v[212:215], v[124:127], a[248:251], v[212:215]\n"
      "ds_read_b128 a[92:95], v236 offset:34816\n"
      "ds_read_b128 a[120:123], v237 offset:32768\n"
      "s_waitcnt lgkmcnt(7)\n"
      "v_mfma_f32_16x16x32_bf16 v[216:219], v[120:123], a[252:255], v[216:219]\n"
      "ds_read_b128 a[124:127], v237 offset:34816\n"
      "v_mfma_f32_16x16x32_bf16 v[220:223], v[124:127], a[252:255], v[220:223]\n"
      "s_waitcnt vmcnt(8)\n"
      "v_mfma_f32_16x16x32_bf16 v[0:3], v[128:131], a[0:3], 0\n"
      "v_mfma_f32_16x16x32_bf16 v[4:7], v[128:131], a[4:7], 0\n"
      "v_mfma_f32_16x16x32_bf16 v[8:11], v[128:131], a[8:11], 0\n"
      "v_cvt_pk_bf16_f32 v192, v208, v209\n"
      "v_cvt_pk_bf16_f32 v193, v210, v211\n"
      "v_mfma_f32_16x16x32_bf16 v[12:15], v[128:131], a[12:15], 0\n"
      "v_cvt_pk_bf16_f32 v194, v212, v213\n"
      "v_cvt_pk_bf16_f32 v195, v214, v215\n"
      "v_mfma_f32_16x16x32_bf16 v[16:19], v[128:131], a[16:19], 0\n"
      "v_xor_b32 v192, s85, v192\n"
      "v_xor_b32 v193, s85, v193\n"
      "v_mfma_f32_16x16x32_bf16 v[20:23], v[128:131], a[20:23], 0\n"
      "v_xor_b32 v194, s85, v194\n"
      "v_xor_b32 v195, s85, v195\n"
      "v_mfma_f32_16x16x32_bf16 v[24:27], v[128:131], a[24:27], 0\n"
      "v_cvt_pk_bf16_f32 v196, v216, v217\n"
      "v_cvt_pk_bf16_f32 v197, v218, v219\n"
      "v_mfma_f32_16x16x32_bf16 v[28:31], v[128:131], a[28:31], 0\n"
      "v_cvt_pk_bf16_f32 v198, v220, v221\n"
      "v_cvt_pk_bf16_f32 v199, v222, v223\n"
      "s_waitcnt vmcnt(7)\n"
      "v_mfma_f32_16x16x32_bf16 v[32:35], v[132:135], a[0:3], 0\n"
      "v_xor_b32 v196, s85, v196\n"
      "v_xor_b32 v197, s85, v197\n"
      "v_mfma_f32_16x16x32_bf16 v[36:39], v[132:135], a[4:7], 0\n"
      "v_xor_b32 v198, s85, v198\n"
      "v_xor_b32 v199, s85, v199\n"
      "v_mfma_f32_16x16x32_bf16 v[40:43], v[132:135], a[8:11], 0\n"
      "global_store_dwordx4 v242, v[192:195], s[62:63] offset:2048 sc1\n"
      "global_store_dwordx4 v242, v[196:199], s[62:63] offset:3072 sc1\n"
      "v_mfma_f32_16x16x32_bf16 v[44:47], v[132:135], a[12:15], 0\n"
      "v_mfma_f32_16x16x32_bf16 v[48:51], v[132:135], a[16:19], 0\n"
      "ds_read_b128 a[128:131], v236 offset:49152\n"
      "ds_read_b128 a[132:135], v236 offset:51200\n"
      "v_mfma_f32_16x16x32_bf16 v[52:55], v[132:135], a[20:23], 0\n"
      "s_xor_b32 s70, s77, 256\n"
      "s_add_u32 s70, s8, s70\n"
      "s_addc_u32 s71, s9, 0\n"
      "s_add_u32 m0, s41, 8192\n"
      "v_mfma_f32_16x16x32_bf16 v[56:59], v[132:135], a[24:27], 0\n"
      "global_load_lds_dwordx4 v244, s[70:71] offset:0\n"
      "ds_read_b128 a[160:163], v237 offset:49152\n"
      "v_mfma_f32_16x16x32_bf16 v[60:63], v[132:135], a[28:31], 0\n"
      "s_waitcnt vmcnt(8)\n"
      "ds_read_b128 a[164:167], v237 offset:51200\n"
      "v_mfma_f32_16x16x32_bf16 v[64:67], v[136:139], a[0:3], 0\n"
      "v_mfma_f32_16x16x32_bf16 v[68:71], v[136:139], a[4:7], 0\n"
      "global_load_lds_dwordx4 v245, s[70:71] offset:1024\n"
      "v_mfma_f32_16x16x32_bf16 v[72:75], v[136:139], a[8:11], 0\n"
      "v_mfma_f32_16x16x32_bf16 v[76:79], v[136:139], a[12:15], 0\n"
      "v_mfma_f32_16x16x32_bf16 v[80:83], v[136:139], a[16:19], 0\n"
      "global_load_lds_dwordx4 v246, s[70:71] offset:2048\n"
      "v_mfma_f32_16x16x32_bf16 v[84:87], v[136:139], a[20:23], 0\n"
      "v_mfma_f32_16x16x32_bf16 v[88:91], v[136:139], a[24:27], 0\n"
      "v_mfma_f32_16x16x32_bf16 v[92:95], v[136:139], a[28:31], 0\n"
      "global_load_lds_dwordx4 v247, s[70:71] offset:3072\n"
      "v_mfma_f32_16x16x32_bf16 v[0:3], v[140:143], a[32:35], v[0:3]\n"
      "v_mfma_f32_16x16x32_bf16 v[4:7], v[140:143], a[36:39], v[4:7]\n"
      "global_load_dwordx4 v[152:155], v241, s[10:11] offset:-4096 sc1\n"
      "v_mfma_f32_16x16x32_bf16 v[8:11], v[140:143], a[40:43], v[8:11]\n"
      "v_mfma_f32_16x16x32_bf16 v[12:15], v[140:143], a[44:47], v[12:15]\n"
      "v_mfma_f32_16x16x32_bf16 v[16:19], v[140:143], a[48:51], v[16:19]\n"
      "global_load_dwordx4 v[156:159], v241, s[10:11] offset:-3072 sc1\n"
      "v_mfma_f32_16x16x32_bf16 v[20:23], v[140:143], a[52:55], v[20:23]\n"
      "v_mfma_f32_16x16x32_bf16 v[24:27], v[140:143], a[56:59], v[24:27]\n"
      "v_mfma_f32_16x16x32_bf16 v[28:31], v[140:143], a[60:63], v[28:31]\n"
      "s_waitcnt vmcnt(11)\n"
      "global_load_dwordx4 v[160:163], v241, s[10:11] offset:-2048 sc1\n"
      "v_mfma_f32_16x16x32_bf16 v[32:35], v[144:147], a[32:35], v[32:35]\n"
      "v_add_u32 v238, s42, v242\n"
      "v_mov_b32 v200, s37\n"
      "v_mov_b32 v201, s37\n"
      "v_mov_b32 v202, s37\n"
      "v_mov_b32 v203, s37\n"
      "s_waitcnt vmcnt(7)\n"
      "s_mov_b64 exec, 0xff\n"
      "global_store_dwordx4 v238, v[200:203], s[58:59] offset:128 sc1\n"
      "s_mov_b64 exec, -1\n"
      "v_mfma_f32_16x16x32_bf16 v[36:39], v[144:147], a[36:39], v[36:39]\n"
      "s_lshl2_add_u32 s48, s36, s35\n"
      "s_mul_i32 s48, s48, 147712\n"
      "s_mul_i32 s49, s33, 3072\n"
      "v_mfma_f32_16x16x32_bf16 v[40:43], v[144:147], a[40:43], v[40:43]\n"
      "s_add_u32 s48, s48, s49\n"
      "s_xor_b32 s70, s78, 256\n"
      "s_add_u32 s70, s8, s70\n"
      "s_addc_u32 s71, s9, 0\n"
      "s_add_u32 m0, s41, 12288\n"
      "v_mfma_f32_16x16x32_bf16 v[44:47], v[144:147], a[44:47], v[44:47]\n"
      "s_add_u32 s48, s48, 6291456\n"
      "global_load_lds_dwordx4 v244, s[70:71] offset:0\n"
      "s_add_u32 s60, s18, s48\n"
      "s_addc_u32 s61, s19, 0\n"
      "v_mfma_f32_16x16x32_bf16 v[48:51], v[144:147], a[48:51], v[48:51]\n"
      "s_mul_i32 s49, s34, 36864\n"
      "s_add_u32 s62, s60, s49\n"
      "s_addc_u32 s63, s61, 0\n"
      "v_mfma_f32_16x16x32_bf16 v[52:55], v[144:147], a[52:55], v[52:55]\n"
      "v_mfma_f32_16x16x32_bf16 v[56:59], v[144:147], a[56:59], v[56:59]\n"
      "global_load_lds_dwordx4 v245, s[70:71] offset:1024\n"
      "v_mfma_f32_16x16x32_bf16 v[60:63], v[144:147], a[60:63], v[60:63]\n"
      "v_mfma_f32_16x16x32_bf16 v[64:67], v[148:151], a[32:35], v[64:67]\n"
      "v_mfma_f32_16x16x32_bf16 v[68:71], v[148:151], a[36:39], v[68:71]\n"
      "global_load_lds_dwordx4 v246, s[70:71] offset:2048\n"
      "v_mfma_f32_16x16x32_bf16 v[72:75], v[148:151], a[40:43], v[72:75]\n"
      "v_mfma_f32_16x16x32_bf16 v[76:79], v[148:151], a[44:47], v[76:79]\n"
      "v_mfma_f32_16x16x32_bf16 v[80:83], v[148:151], a[48:51], v[80:83]\n"
      "global_load_lds_dwordx4 v247, s[70:71] offset:3072\n"
      "v_mfma_f32_16x16x32_bf16 v[84:87], v[148:151], a[52:55], v[84:87]\n"
      "v_mfma_f32_16x16x32_bf16 v[88:91], v[148:151], a[56:59], v[88:91]\n"
      "s_xor_b32 s70, s79, 256\n"
      "s_add_u32 s70, s8, s70\n"
      "s_addc_u32 s71, s9, 0\n"
      "s_add_u32 m0, s41, 0\n"
      "v_mfma_f32_16x16x32_bf16 v[92:95], v[148:151], a[60:63], v[92:95]\n"
      "s_waitcnt vmcnt(6)\n"
      "global_load_lds_dwordx4 v244, s[70:71] offset:0\n"
      "v_mfma_f32_16x16x32_bf16 v[0:3], v[152:155], a[64:67], v[0:3]\n"
      "v_mfma_f32_16x16x32_bf16 v[4:7], v[152:155], a[68:71], v[4:7]\n"
      "v_mfma_f32_16x16x32_bf16 v[8:11], v[152:155], a[72:75], v[8:11]\n"
      "global_load_lds_dwordx4 v245, s[70:71] offset:1024\n"
      "v_mfma_f32_16x16x32_bf16 v[12:15], v[152:155], a[76:79], v[12:15]\n"
      "s_waitcnt lgkmcnt(11)\n"
      "v_mfma_f32_16x16x32_bf16 v[16:19], v[152:155], a[80:83], v[16:19]\n"
      "s_waitcnt lgkmcnt(10)\n"
      "v_mfma_f32_16x16x32_bf16 v[20:23], v[152:155], a[84:87], v[20:23]\n"
      "global_load_lds_dwordx4 v246, s[70:71] offset:2048\n"
      "s_waitcnt lgkmcnt(7)\n"
      "v_mfma_f32_16x16x32_bf16 v[24:27], v[152:155], a[88:91], v[24:27]\n"
      "s_waitcnt lgkmcnt(6)\n"
      "v_mfma_f32_16x16x32_bf16 v[28:31], v[152:155], a[92:95], v[28:31]\n"
      "v_mfma_f32_16x16x32_bf16 v[32:35], v[156:159], a[64:67], v[32:35]\n"
      "global_load_lds_dwordx4 v247, s[70:71] offset:3072\n"
      "v_mfma_f32_16x16x32_bf16 v[36:39], v[156:159], a[68:71], v[36:39]\n"
      "v_mfma_f32_16x16x32_bf16 v[40:43], v[156:159], a[72:75], v[40:43]\n"
      "global_load_dwordx4 v[164:167], v241, s[72:73] offset:-4096 sc1\n"
      "v_mfma_f32_16x16x32_bf16 v[44:47], v[156:159], a[76:79], v[44:47]\n"
      "v_mfma_f32_16x16x32_bf16 v[48:51], v[156:159], a[80:83], v[48:51]\n"
      "v_mfma_f32_16x16x32_bf16 v[52:55], v[156:159], a[84:87], v[52:55]\n"
      "global_load_dwordx4 v[168:171], v241, s[72:73] offset:-3072 sc1\n"
      "v_mfma_f32_16x16x32_bf16 v[56:59], v[156:159], a[88:91], v[56:59]\n"
      "v_mfma_f32_16x16x32_bf16 v[60:63], v[156:159], a[92:95], v[60:63]\n"
      "s_waitcnt vmcnt(1)\n"
      "v_mfma_f32_16x16x32_bf16 v[64:67], v[160:163], a[64:67], v[64:67]\n"
      "global_load_dwordx4 v[172:175], v241, s[72:73] offset:-2048 sc1\n"
      "v_mfma_f32_16x16x32_bf16 v[68:71], v[160:163], a[68:71], v[68:71]\n"
      "v_mfma_f32_16x16x32_bf16 v[72:75], v[160:163], a[72:75], v[72:75]\n"
      "v_mfma_f32_16x16x32_bf16 v[76:79], v[160:163], a[76:79], v[76:79]\n"
      "global_load_dwordx4 v[176:179], v240, s[14:15] offset:-4096 sc1\n"
      "v_mfma_f32_16x16x32_bf16 v[80:83], v[160:163], a[80:83], v[80:83]\n"
      "ds_read_b128 a[136:139], v248 offset:8192\n"
      "ds_read_b128 a[140:143], v248 offset:10240\n"
      "v_mfma_f32_16x16x32_bf16 v[84:87], v[160:163], a[84:87], v[84:87]\n"
      "ds_read_b128 a[168:171], v249 offset:8192\n"
      "ds_read_b128 a[172:175], v249 offset:10240\n"
      "v_mfma_f32_16x16x32_bf16 v[88:91], v[160:163], a[88:91], v[88:91]\n"
      "global_load_dwordx4 v[180:183], v240, s[14:15] offset:-3072 sc1\n"
      "v_mfma_f32_16x16x32_bf16 v[92:95], v[160:163], a[92:95], v[92:95]\n"
      "v_mfma_f32_16x16x32_bf16 v[0:3], v[164:167], a[96:99], v[0:3]\n"
      "v_mfma_f32_16x16x32_bf16 v[4:7], v[164:167], a[100:103], v[4:7]\n"
      "global_load_dwordx4 v[184:187], v240, s[14:15] offset:-2048 sc1\n"
      "global_load_dwordx4 v[188:191], v240, s[74:75] offset:-4096 sc1\n"
      "v_mfma_f32_16x16x32_bf16 v[8:11], v[164:167], a[104:107], v[8:11]\n"
      "global_load_dwordx4 v[128:131], v240, s[74:75] offset:-3072 sc1\n"
      "global_load_dwordx4 v[132:135], v240, s[74:75] offset:-2048 sc1\n"
      "v_mfma_f32_16x16x32_bf16 v[12:15], v[164:167], a[108:111], v[12:15]\n"
      "global_load_dword v252, v253, s[58:59] offset:128 sc1\n"
      "v_mfma_f32_16x16x32_bf16 v[16:19], v[164:167], a[112:115], v[16:19]\n"
      "v_mfma_f32_16x16x32_bf16 v[20:23], v[164:167], a[116:119], v[20:23]\n"
      "s_waitcnt lgkmcnt(9)\n"
      "v_mfma_f32_16x16x32_bf16 v[24:27], v[164:167], a[120:123], v[24:27]\n"
      "s_waitcnt lgkmcnt(8)\n"
      "v_mfma_f32_16x16x32_bf16 v[28:31], v[164:167], a[124:127], v[28:31]\n"
      "s_waitcnt vmcnt(7)\n"
      "v_mfma_f32_16x16x32_bf16 v[32:35], v[168:171], a[96:99], v[32:35]\n"
      "v_mfma_f32_16x16x32_bf16 v[36:39], v[168:171], a[100:103], v[36:39]\n"
      "v_mfma_f32_16x16x32_bf16 v[40:43], v[168:171], a[104:107], v[40:43]\n"
      "v_mfma_f32_16x16x32_bf16 v[44:47], v[168:171], a[108:111], v[44:47]\n"
      "v_mfma_f32_16x16x32_bf16 v[48:51], v[168:171], a[112:115], v[48:51]\n"
      "v_mfma_f32_16x16x32_bf16 v[52:55], v[168:171], a[116:119], v[52:55]\n"
      "s_lshr_b32 s54, s34, 1\n"
      "s_lshl2_add_u32 s54, s54, s35\n"
      "s_lshl_b32 s54, s54, 12\n"
      "v_mfma_f32_16x16x32_bf16 v[56:59], v[168:171], a[120:123], v[56:59]\n"
      "s_add_u32 s54, s54, 4096\n"
      "v_lshl_add_u32 v232, v243, 6, s54\n"
      "global_load_dwordx2 v[234:235], v232, s[58:59] sc1\n"
      "v_mfma_f32_16x16x32_bf16 v[60:63], v[168:171], a[124:127], v[60:63]\n"
      "v_mfma_f32_16x16x32_bf16 v[64:67], v[172:175], a[96:99], v[64:67]\n"
      "v_mfma_f32_16x16x32_bf16 v[68:71], v[172:175], a[100:103], v[68:71]\n"
      "ds_read_b128 a[144:147], v248 offset:12288\n"
      "ds_read_b128 a[148:151], v248 offset:14336\n"
      "v_mfma_f32_16x16x32_bf16 v[72:75], v[172:175], a[104:107], v[72:75]\n"
      "ds_read_b128 a[176:179], v249 offset:12288\n"
      "ds_read_b128 a[180:183], v249 offset:14336\n"
      "v_mfma_f32_16x16x32_bf16 v[76:79], v[172:175], a[108:111], v[76:79]\n"
      "v_mfma_f32_16x16x32_bf16 v[80:83], v[172:175], a[112:115], v[80:83]\n"
      "v_mfma_f32_16x16x32_bf16 v[84:87], v[172:175], a[116:119], v[84:87]\n"
      "s_xor_b32 s70, s76, 384\n"
      "s_add_u32 s70, s8, s70\n"
      "s_addc_u32 s71, s9, 0\n"
      "s_add_u32 m0, s41, 4096\n"
      "v_mfma_f32_16x16x32_bf16 v[88:91], v[172:175], a[120:123], v[88:91]\n"
      "global_load_lds_dwordx4 v244, s[70:71] offset:0\n"
      "v_mfma_f32_16x16x32_bf16 v[92:95], v[172:175], a[124:127], v[92:95]\n"
      "s_waitcnt vmcnt(7)\n"
      "s_waitcnt lgkmcnt(11)\n"
      "v_mfma_f32_16x16x32_bf16 v[0:3], v[176:179], a[128:131], v[0:3]\n"
      "s_waitcnt lgkmcnt(10)\n"
      "v_mfma_f32_16x16x32_bf16 v[4:7], v[176:179], a[132:135], v[4:7]\n"
      "global_load_lds_dwordx4 v245, s[70:71] offset:1024\n"
      "s_waitcnt lgkmcnt(7)\n"
      "v_mfma_f32_16x16x32_bf16 v[8:11], v[176:179], a[136:139], v[8:11]\n"
      "s_waitcnt lgkmcnt(6)\n"
      "v_mfma_f32_16x16x32_bf16 v[12:15], v[176:179], a[140:143], v[12:15]\n"
      "s_waitcnt lgkmcnt(3)\n"
      "v_mfma_f32_16x16x32_bf16 v[16:19], v[176:179], a[144:147], v[16:19]\n"
      "global_load_lds_dwordx4 v246, s[70:71] offset:2048\n"
      "s_waitcnt lgkmcnt(2)\n"
      "v_mfma_f32_16x16x32_bf16 v[20:23], v[176:179], a[148:151], v[20:23]\n"
      "ds_read_b128 a[152:155], v248 offset:0\n"
      "ds_read_b128 a[156:159], v248 offset:2048\n"
      "ds_read_b128 a[184:187], v249 offset:0\n"
      "ds_read_b128 a[188:191], v249 offset:2048\n"
      "s_waitcnt lgkmcnt(3)\n"
      "v_mfma_f32_16x16x32_bf16 v[24:27], v[176:179], a[152:155], v[24:27]\n"
      "s_waitcnt lgkmcnt(2)\n"
      "v_mfma_f32_16x16x32_bf16 v[28:31], v[176:179], a[156:159], v[28:31]\n"
      "global_load_lds_dwordx4 v247, s[70:71] offset:3072\n"
      "v_mfma_f32_16x16x32_bf16 v[32:35], v[180:183], a[128:131], v[32:35]\n"
      "v_mfma_f32_16x16x32_bf16 v[36:39], v[180:183], a[132:135], v[36:39]\n"
      "s_xor_b32 s70, s77, 384\n"
      "s_add_u32 s70, s8, s70\n"
      "s_addc_u32 s71, s9, 0\n"
      "s_add_u32 m0, s82, 98304\n"
      "v_mfma_f32_16x16x32_bf16 v[40:43], v[180:183], a[136:139], v[40:43]\n"
      "global_load_lds_dwordx4 v244, s[70:71] offset:0\n"
      "v_mfma_f32_16x16x32_bf16 v[44:47], v[180:183], a[140:143], v[44:47]\n"
      "v_mfma_f32_16x16x32_bf16 v[48:51], v[180:183], a[144:147], v[48:51]\n"
      "v_mfma_f32_16x16x32_bf16 v[52:55], v[180:183], a[148:151], v[52:55]\n"
      "global_load_lds_dwordx4 v245, s[70:71] offset:1024\n"
      "v_mfma_f32_16x16x32_bf16 v[56:59], v[180:183], a[152:155], v[56:59]\n"
      "v_mfma_f32_16x16x32_bf16 v[60:63], v[180:183], a[156:159], v[60:63]\n"
      "s_waitcnt vmcnt(10)\n"
      "v_mfma_f32_16x16x32_bf16 v[64:67], v[184:187], a[128:131], v[64:67]\n"
      "global_load_lds_dwordx4 v246, s[70:71] offset:2048\n"
      "v_mfma_f32_16x16x32_bf16 v[68:71], v[184:187], a[132:135], v[68:71]\n"
      "v_mfma_f32_16x16x32_bf16 v[72:75], v[184:187], a[136:139], v[72:75]\n"
      "v_mfma_f32_16x16x32_bf16 v[76:79], v[184:187], a[140:143], v[76:79]\n"
      "global_load_lds_dwordx4 v247, s[70:71] offset:3072\n"
      "v_mfma_f32_16x16x32_bf16 v[80:83], v[184:187], a[144:147], v[80:83]\n"
      "v_mfma_f32_16x16x32_bf16 v[84:87], v[184:187], a[148:151], v[84:87]\n"
      "s_xor_b32 s70, s78, 384\n"
      "s_add_u32 s70, s8, s70\n"
      "s_addc_u32 s71, s9, 0\n"
      "s_add_u32 m0, s82, 114688\n"
      "v_mfma_f32_16x16x32_bf16 v[88:91], v[184:187], a[152:155], v[88:91]\n"
      "global_load_lds_dwordx4 v244, s[70:71] offset:0\n"
      "s_waitcnt vmcnt(10)\n"
      "v_cmp_ne_u32 vcc, s37, v252\n"
      "s_nop 1\n"
      "s_cbranch_vccnz .Lp_s5_%=\n"
      ".Lp_o6_%=:\n"
      "v_mfma_f32_16x16x32_bf16 v[92:95], v[184:187], a[156:159], v[92:95]\n"
      "v_mfma_f32_16x16x32_bf16 v[0:3], v[188:191], a[160:163], v[0:3]\n"
      "global_load_dwordx4 v[136:139], v240, s[10:11] offset:-1024 sc1\n"
      "v_mfma_f32_16x16x32_bf16 v[4:7], v[188:191], a[164:167], v[4:7]\n"
      "global_load_lds_dwordx4 v245, s[70:71] offset:1024\n"
      "v_mfma_f32_16x16x32_bf16 v[8:11], v[188:191], a[168:171], v[8:11]\n"
      "v_mfma_f32_16x16x32_bf16 v[12:15], v[188:191], a[172:175], v[12:15]\n"
      "v_mfma_f32_16x16x32_bf16 v[16:19], v[188:191], a[176:179], v[16:19]\n"
      "global_load_lds_dwordx4 v246, s[70:71] offset:2048\n"
      "global_load_dwordx4 v[140:143], v240, s[72:73] offset:-1024 sc1\n"
      "v_mfma_f32_16x16x32_bf16 v[20:23], v[188:191], a[180:183], v[20:23]\n"
      "s_waitcnt lgkmcnt(1)\n"
      "v_mfma_f32_16x16x32_bf16 v[24:27], v[188:191], a[184:187], v[24:27]\n"
      "s_waitcnt lgkmcnt(0)\n"
      "v_mfma_f32_16x16x32_bf16 v[28:31], v[188:191], a[188:191], v[28:31]\n"
      "global_load_lds_dwordx4 v247, s[70:71] offset:3072\n"
      "v_mfma_f32_16x16x32_bf16 v[32:35], v[128:131], a[160:163], v[32:35]\n"
      "global_load_dwordx4 v[144:147], v241, s[10:11] offset:-1024 sc1\n"
      "v_mfma_f32_16x16x32_bf16 v[36:39], v[128:131], a[164:167], v[36:39]\n"
      "s_xor_b32 s70, s79, 384\n"
      "s_add_u32 s70, s8, s70\n"
      "s_addc_u32 s71, s9, 0\n"
      "s_add_u32 m0, s82, 131072\n"
      "v_mfma_f32_16x16x32_bf16 v[40:43], v[128:131], a[168:171], v[40:43]\n"
      "global_load_lds_dwordx4 v244, s[70:71] offset:0\n"
      "v_mfma_f32_16x16x32_bf16 v[44:47], v[128:131], a[172:175], v[44:47]\n"
      "v_mfma_f32_16x16x32_bf16 v[48:51], v[128:131], a[176:179], v[48:51]\n"
      "global_load_dwordx4 v[148:151], v241, s[14:15] offset:-4096 sc1\n"
      "v_mfma_f32_16x16x32_bf16 v[52:55], v[128:131], a[180:183], v[52:55]\n"
      "global_load_lds_dwordx4 v245, s[70:71] offset:1024\n"
      "v_mfma_f32_16x16x32_bf16 v[56:59], v[128:131], a[184:187], v[56:59]\n"
      "v_mfma_f32_16x16x32_bf16 v[60:63], v[128:131], a[188:191], v[60:63]\n"
      "v_mfma_f32_16x16x32_bf16 v[64:67], v[132:135], a[160:163], v[64:67]\n"
      "global_load_lds_dwordx4 v246, s[70:71] offset:2048\n"
      "global_load_dwordx4 v[152:155], v241, s[74:75] offset:-4096 sc1\n"
      "v_mfma_f32_16x16x32_bf16 v[68:71], v[132:135], a[164:167], v[68:71]\n"
      "v_mfma_f32_16x16x32_bf16 v[72:75], v[132:135], a[168:171], v[72:75]\n"
      "v_mfma_f32_16x16x32_bf16 v[76:79], v[132:135], a[172:175], v[76:79]\n"
      "global_load_lds_dwordx4 v247, s[70:71] offset:3072\n"
      "v_mfma_f32_16x16x32_bf16 v[80:83], v[132:135], a[176:179], v[80:83]\n"
      "global_load_dwordx4 v[156:159], v241, s[14:15] offset:-3072 sc1\n"
      "v_mfma_f32_16x16x32_bf16 v[84:87], v[132:135], a[180:183], v[84:87]\n"
      "s_barrier\n"
      "s_lshr_b32 s80, s32, 4\n"
      "s_bfe_u32 s43, s32, 0x10002\n"
      "s_lshl1_add_u32 s80, s80, s43\n"
      "s_and_b32 s81, s32, 3\n"
      "s_cmp_lt_u32 s80, 28\n"
      "s_cselect_b32 s86, -1, 0\n"
      "s_lshr_b32 s83, s33, 1\n"
      "s_lshl1_add_u32 s83, s81, s83\n"
      "s_min_u32 s43, s80, 27\n"
      "v_lshrrev_b32 v226, 3, v243\n"
      "v_and_b32 v227, 0x70, v242\n"
      "v_lshl_or_b32 v224, v226, 10, v227\n"
      "v_add_u32 v225, 7168, v224\n"
      "v_add_u32 v226, 14336, v224\n"
      "v_add_u32 v227, 21504, v224\n"
      "s_lshr_b32 s44, s33, 1\n"
      "s_lshl1_add_u32 s44, s43, s44\n"
      "s_lshl_b32 s44, s44, 15\n"
      "s_and_b32 s45, s33, 1\n"
      "s_lshl1_add_u32 s45, s81, s45\n"
      "s_lshl_b32 s45, s45, 7\n"
      "s_add_u32 s44, s44, s45\n"
      "s_add_u32 s70, s30, s44\n"
      "s_addc_u32 s71, s31, 0\n"
      "s_lshl_b32 s43, s33, 12\n"
      "s_add_u32 m0, s43, 65536\n"
      "s_nop 0\n"
      "global_load_lds_dwordx4 v224, s[70:71] offset:0\n"
      "global_load_lds_dwordx4 v225, s[70:71] offset:1024\n"
      "global_load_lds_dwordx4 v226, s[70:71] offset:2048\n"
      "global_load_lds_dwordx4 v227, s[70:71] offset:3072\n"
      "v_mfma_f32_16x16x32_bf16 v[88:91], v[132:135], a[184:187], v[88:91]\n"
      "v_mfma_f32_16x16x32_bf16 v[92:95], v[132:135], a[188:191], v[92:95]\n"
      "s_waitcnt vmcnt(13)\n"
      "v_mfma_f32_16x16x32_bf16 v[96:99], v[136:139], a[0:3], 0\n"
      "global_load_dwordx4 v[160:163], v241, s[74:75] offset:-3072 sc1\n"
      "v_mfma_f32_16x16x32_bf16 v[100:103], v[136:139], a[4:7], 0\n"
      "v_mfma_f32_16x16x32_bf16 v[104:107], v[136:139], a[8:11], 0\n"
      "v_mfma_f32_16x16x32_bf16 v[108:111], v[136:139], a[12:15], 0\n"
      "v_mfma_f32_16x16x32_bf16 v[112:115], v[136:139], a[16:19], 0\n"
      "global_load_dwordx4 v[164:167], v241, s[14:15] offset:-2048 sc1\n"
      "ds_read_b128 a[192:195], v248 offset:4096\n"
      "v_mfma_f32_16x16x32_bf16 v[116:119], v[136:139], a[20:23], 0\n"
      "ds_read_b128 a[196:199], v248 offset:6144\n"
      "ds_read_b128 a[224:227], v249 offset:4096\n"
      "v_mfma_f32_16x16x32_bf16 v[120:123], v[136:139], a[24:27], 0\n"
      "ds_read_b128 a[228:231], v249 offset:6144\n"
      "v_mfma_f32_16x16x32_bf16 v[124:127], v[136:139], a[28:31], 0\n"
      "v_mfma_f32_16x16x32_bf16 v[96:99], v[140:143], a[32:35], v[96:99]\n"
      "global_load_dwordx4 v[168:171], v241, s[74:75] offset:-2048 sc1\n"
      "v_mfma_f32_16x16x32_bf16 v[100:103], v[140:143], a[36:39], v[100:103]\n"
      "v_mfma_f32_16x16x32_bf16 v[104:107], v[140:143], a[40:43], v[104:107]\n"
      "v_mfma_f32_16x16x32_bf16 v[108:111], v[140:143], a[44:47], v[108:111]\n"
      "v_mfma_f32_16x16x32_bf16 v[112:115], v[140:143], a[48:51], v[112:115]\n"
      "global_load_dwordx4 v[172:175], v241, s[72:73] offset:-1024 sc1\n"
      "v_mfma_f32_16x16x32_bf16 v[116:119], v[140:143], a[52:55], v[116:119]\n"
      "v_mfma_f32_16x16x32_bf16 v[120:123], v[140:143], a[56:59], v[120:123]\n"
      "v_mfma_f32_16x16x32_bf16 v[124:127], v[140:143], a[60:63], v[124:127]\n"
      "s_waitcnt vmcnt(13)\n"
      "v_mfma_f32_16x16x32_bf16 v[96:99], v[144:147], a[64:67], v[96:99]\n"
      "global_load_dwordx4 v[176:179], v240, s[14:15] offset:-1024 sc1\n"
      "ds_read_b128 a[200:203], v236 offset:0\n"
      "v_mfma_f32_16x16x32_bf16 v[100:103], v[144:147], a[68:71], v[100:103]\n"
      "ds_read_b128 a[204:207], v236 offset:2048\n"
      "ds_read_b128 a[232:235], v237 offset:0\n"
      "v_mfma_f32_16x16x32_bf16 v[104:107], v[144:147], a[72:75], v[104:107]\n"
      "ds_read_b128 a[236:239], v237 offset:2048\n"
      "v_mfma_f32_16x16x32_bf16 v[108:111], v[144:147], a[76:79], v[108:111]\n"
      "v_mfma_f32_16x16x32_bf16 v[112:115], v[144:147], a[80:83], v[112:115]\n"
      "global_load_dwordx4 v[180:183], v240, s[74:75] offset:-1024 sc1\n"
      "v_mfma_f32_16x16x32_bf16 v[116:119], v[144:147], a[84:87], v[116:119]\n"
      "s_lshl2_add_u32 s54, s36, s33\n"
      "s_lshl_b32 s55, s54, 15\n"
      "s_lshl2_add_u32 s56, s35, s34\n"
      "v_mfma_f32_16x16x32_bf16 v[120:123], v[144:147], a[88:91], v[120:123]\n"
      "s_lshl_b32 s56, s56, 6\n"
      "s_add_u32 s55, s55, s56\n"
      "v_and_b32 v209, 15, v243\n"
      "v_mfma_f32_16x16x32_bf16 v[124:127], v[144:147], a[92:95], v[124:127]\n"
      "v_and_b32 v208, 48, v243\n"
      "v_lshl_or_b32 v208, v209, 10, v208\n"
      "s_waitcnt lgkmcnt(7)\n"
      "v_mfma_f32_16x16x32_bf16 v[0:3], v[148:151], a[192:195], v[0:3]\n"
      "v_add_u32 v208, s55, v208\n"
      "v_add_u32 v210, 16384, v208\n"
      "s_waitcnt lgkmcnt(6)\n"
      "v_mfma_f32_16x16x32_bf16 v[4:7], v[148:151], a[196:199], v[4:7]\n"
      "global_load_dwordx4 v[216:219], v208, s[16:17]\n"
      "global_load_dwordx4 v[220:223], v210, s[16:17]\n"
      "s_waitcnt lgkmcnt(3)\n"
      "v_mfma_f32_16x16x32_bf16 v[8:11], v[148:151], a[200:203], v[8:11]\n"
      "s_lshr_b32 s55, s34, 1\n"
      "global_load_dwordx4 v[184:187], v241, s[14:15] offset:-1024 sc1\n"
      "s_lshl2_add_u32 s55, s55, s35\n"
      "s_waitcnt lgkmcnt(2)\n"
      "v_mfma_f32_16x16x32_bf16 v[12:15], v[148:151], a[204:207], v[12:15]\n"
      "s_lshl_b32 s55, s55, 15\n"
      "s_lshl_b32 s54, s54, 9\n"
      "s_add_u32 s54, s54, s55\n"
      "ds_read_b128 a[208:211], v236 offset:16384\n"
      "ds_read_b128 a[212:215], v236 offset:18432\n"
      "ds_read_b128 a[240:243], v237 offset:16384\n"
      "ds_read_b128 a[244:247], v237 offset:18432\n"
      "s_waitcnt lgkmcnt(3)\n"
      "v_mfma_f32_16x16x32_bf16 v[16:19], v[148:151], a[208:211], v[16:19]\n"
      "s_add_u32 s54, s54, 786432\n"
      "s_waitcnt vmcnt(14)\n"
      "ds_read_b128 a[216:219], v236 offset:32768\n"
      "s_waitcnt lgkmcnt(3)\n"
      "v_mfma_f32_16x16x32_bf16 v[20:23], v[148:151], a[212:215], v[20:23]\n"
      "ds_read_b128 a[220:223], v236 offset:34816\n"
      "v_lshl_add_u32 v209, v209, 4, s54\n"
      "ds_read_b128 a[248:251], v237 offset:32768\n"
      "ds_read_b128 a[252:255], v237 offset:34816\n"
      "s_waitcnt lgkmcnt(3)\n"
      "v_mfma_f32_16x16x32_bf16 v[24:27], v[148:151], a[216:219], v[24:27]\n"
      "s_sub_u32 s48, s33, 1\n"
      "global_load_dwordx2 v[204:205], v209, s[58:59] offset:0 sc1\n"
      "s_and_b32 s48, s48, 3\n"
      "s_waitcnt lgkmcnt(2)\n"
      "v_mfma_f32_16x16x32_bf16 v[28:31], v[148:151], a[220:223], v[28:31]\n"
      "s_waitcnt vmcnt(14)\n"
      "global_load_dwordx2 v[206:207], v209, s[58:59] offset:256 sc1\n"
      "s_lshl_b32 s48, s48, 14\n"
      "v_mfma_f32_16x16x32_bf16 v[0:3], v[152:155], a[224:227], v[0:3]\n"
      "s_mov_b32 m0, s41\n"
      "v_lshl_add_u32 v244, v243, 2, s48\n"
      "s_sub_u32 s48, s33, 2\n"
      "v_mfma_f32_16x16x32_bf16 v[4:7], v[152:155], a[228:231], v[4:7]\n"
      "s_and_b32 s48, s48, 3\n"
      "s_lshl_b32 s48, s48, 14\n"
      "v_lshl_add_u32 v245, v243, 2, s48\n"
      "v_mfma_f32_16x16x32_bf16 v[8:11], v[152:155], a[232:235], v[8:11]\n"
      "s_sub_u32 s48, s33, 3\n"
      "global_load_dwordx4 v[188:191], v241, s[74:75] offset:-1024 sc1\n"
      "s_and_b32 s48, s48, 3\n"
      "v_mfma_f32_16x16x32_bf16 v[12:15], v[152:155], a[236:239], v[12:15]\n"
      "s_lshl_b32 s48, s48, 14\n"
      "v_lshl_add_u32 v246, v243, 2, s48\n"
      "v_bfe_u32 v248, v243, 1, 3\n"
      "v_mfma_f32_16x16x32_bf16 v[16:19], v[152:155], a[240:243], v[16:19]\n"
      "s_add_u32 s68, s20, 16384\n"
      "s_addc_u32 s69, s21, 0\n"
      "v_cmp_ne_u32 vcc, s37, v235\n"
      "s_nop 1\n"
      "s_cbranch_vccnz .Lp_s8_%=\n"
      ".Lp_o9_%=:\n"
      "v_mfma_f32_16x16x32_bf16 v[20:23], v[152:155], a[244:247], v[20:23]\n"
      "v_lshrrev_b32 v249, 4, v243\n"
      "v_lshlrev_b32 v249, 1, v249\n"
      "s_waitcnt lgkmcnt(1)\n"
      "v_mfma_f32_16x16x32_bf16 v[24:27], v[152:155], a[248:251], v[24:27]\n"
      "v_sub_u32 v248, v248, v249\n"
      "v_and_b32 v249, 16, v242\n"
      "s_waitcnt lgkmcnt(0)\n"
      "v_mfma_f32_16x16x32_bf16 v[28:31], v[152:155], a[252:255], v[28:31]\n"
      "v_mov_b32 v254, 0x3f80\n"
      "v_lshlrev_b32 v249, v249, v254\n"
      "v_mfma_f32_16x16x32_bf16 v[32:35], v[156:159], a[192:195], v[32:35]\n"
      "v_cmp_eq_u32 vcc, 0, v248\n"
      "s_nop 1\n"
      "v_cndmask_b32 v250, 0, v249, vcc\n"
      "v_mfma_f32_16x16x32_bf16 v[36:39], v[156:159], a[196:199], v[36:39]\n"
      "v_cmp_eq_u32 vcc, 1, v248\n"
      "s_nop 1\n"
      "v_cndmask_b32 v251, 0, v249, vcc\n"
      "v_mfma_f32_16x16x32_bf16 v[40:43], v[156:159], a[200:203], v[40:43]\n"
      "global_load_dwordx4 v[128:131], v240, s[10:11] offset:0 sc1\n"
      "v_mov_b32 v252, v250\n"
      "v_mfma_f32_16x16x32_bf16 v[44:47], v[156:159], a[204:207], v[44:47]\n"
      "v_mov_b32 v253, v251\n"
      "v_mov_b32 v248, 0\n"
      "v_mfma_f32_16x16x32_bf16 v[48:51], v[156:159], a[208:211], v[48:51]\n"
      "v_mov_b32 v249, 0\n"
      "v_mov_b32 v254, 0\n"
      "v_mfma_f32_16x16x32_bf16 v[52:55], v[156:159], a[212:215], v[52:55]\n"
      "v_mov_b32 v255, 0\n"
      "v_lshrrev_b32 v201, 4, v243\n"
      "v_mfma_f32_16x16x32_bf16 v[56:59], v[156:159], a[216:219], v[56:59]\n"
      "s_add_u32 s48, s34, 1\n"
      "v_bfe_u32 v224, v243, 2, 2\n"
      "v_lshl_or_b32 v201, v201, 3, v224\n"
      "v_mfma_f32_16x16x32_bf16 v[60:63], v[156:159], a[220:223], v[60:63]\n"
      "s_waitcnt vmcnt(11)\n"
      "v_and_b32 v224, 3, v243\n"
      "v_lshlrev_b32 v224, 3, v224\n"
      "v_mfma_f32_16x16x32_bf16 v[32:35], v[160:163], a[224:227], v[32:35]\n"
      "s_and_b32 s48, s48, 3\n"
      "v_lshl_or_b32 v201, v201, 7, v224\n"
      "s_mov_b32 s64, 0xffff0000\n"
      "v_mfma_f32_16x16x32_bf16 v[36:39], v[160:163], a[228:231], v[36:39]\n"
      "s_mov_b32 s65, 0xffff0000\n"
      "s_mov_b32 s66, -1\n"
      "s_mov_b32 s67, 0\n"
      "v_mfma_f32_16x16x32_bf16 v[40:43], v[160:163], a[232:235], v[40:43]\n"
      "s_mul_i32 s48, s48, 36864\n"
      "global_load_dwordx4 v[132:135], v240, s[72:73] offset:0 sc1\n"
      "v_bfe_u32 v224, v243, 4, 1\n"
      "v_mfma_f32_16x16x32_bf16 v[44:47], v[160:163], a[236:239], v[44:47]\n"
      "s_add_u32 s48, s48, 0\n"
      "v_and_b32 v203, 0xf0, v242\n"
      "v_lshl_or_b32 v203, v224, 14, v203\n"
      "v_mfma_f32_16x16x32_bf16 v[48:51], v[160:163], a[240:243], v[48:51]\n"
      "s_lshr_b32 s54, s33, 1\n"
      "s_lshl1_add_u32 s54, s80, s54\n"
      "s_lshl_b32 s54, s54, 12\n"
      "v_mfma_f32_16x16x32_bf16 v[52:55], v[160:163], a[244:247], v[52:55]\n"
      "s_add_u32 s54, s54, 262144\n"
      "v_add_u32 v200, s54, v242\n"
      "s_lshl_b32 s54, s33, 12\n"
      "v_mfma_f32_16x16x32_bf16 v[56:59], v[160:163], a[248:251], v[56:59]\n"
      "s_add_u32 s54, s54, 65536\n"
      "v_add_u32 v201, s54, v201\n"
      "v_and_b32 v202, 15, v243\n"
      "v_mfma_f32_16x16x32_bf16 v[60:63], v[160:163], a[252:255], v[60:63]\n"
      "s_add_u32 s52, s60, s48\n"
      "s_addc_u32 s53, s61, 0\n"
      "v_lshrrev_b32 v199, 5, v243\n"
      "v_mfma_f32_16x16x32_bf16 v[64:67], v[164:167], a[192:195], v[64:67]\n"
      "v_lshl_or_b32 v202, v199, 4, v202\n"
      "s_add_u32 s54, s41, 12288\n"
      "v_mfma_f32_16x16x32_bf16 v[68:71], v[164:167], a[196:199], v[68:71]\n"
      "v_lshl_add_u32 v202, v202, 1, s54\n"
      "v_and_b32 v199, 48, v243\n"
      "v_mfma_f32_16x16x32_bf16 v[72:75], v[164:167], a[200:203], v[72:75]\n"
      "global_load_dwordx4 v[136:139], v241, s[10:11] offset:0 sc1\n"
      "v_add_u32 v199, s54, v199\n"
      "v_mfma_f32_16x16x32_bf16 v[76:79], v[164:167], a[204:207], v[76:79]\n"
      "s_and_b32 s54, s33, 1\n"
      "s_lshl1_add_u32 s54, s81, s54\n"
      "s_lshl_b32 s54, s54, 15\n"
      "v_mfma_f32_16x16x32_bf16 v[80:83], v[164:167], a[208:211], v[80:83]\n"
      "s_lshr_b32 s55, s33, 1\n"
      "s_lshl1_add_u32 s55, s80, s55\n"
      "s_lshl_b32 s55, s55, 8\n"
      "v_mfma_f32_16x16x32_bf16 v[84:87], v[164:167], a[212:215], v[84:87]\n"
      "s_add_u32 s54, s54, s55\n"
      "s_add_u32 s54, s54, 524288\n"
      "v_add_u32 v203, s54, v203\n"
      "v_mfma_f32_16x16x32_bf16 v[88:91], v[164:167], a[216:219], v[88:91]\n"
      "global_load_dwordx4 v[140:143], v241, s[72:73] offset:0 sc1\n"
      "v_cvt_pk_bf16_f32 v8, v8, v9\n"
      "v_mfma_f32_16x16x32_bf16 v[92:95], v[164:167], a[220:223], v[92:95]\n"
      "s_waitcnt vmcnt(12)\n"
      "v_cvt_pk_bf16_f32 v9, v10, v11\n"
      "v_max_u32_dpp v234, v234, v234 quad_perm:[1,0,3,2] row_mask:0xf bank_mask:0xf\n"
      "v_mfma_f32_16x16x32_bf16 v[64:67], v[168:171], a[224:227], v[64:67]\n"
      "ds_write_addtid_b32 v8 offset:0\n"
      "v_cvt_pk_bf16_f32 v10, v12, v13\n"
      "v_mfma_f32_16x16x32_bf16 v[68:71], v[168:171], a[228:231], v[68:71]\n"
      "ds_write_addtid_b32 v9 offset:256\n"
      "v_cvt_pk_bf16_f32 v11, v14, v15\n"
      "v_mfma_f32_16x16x32_bf16 v[72:75], v[168:171], a[232:235], v[72:75]\n"
      "global_load_dwordx4 v[144:147], v240, s[14:15] offset:0 sc1\n"
      "v_max_u32_dpp v234, v234, v234 quad_perm:[2,3,0,1] row_mask:0xf bank_mask:0xf\n"
      "v_mfma_f32_16x16x32_bf16 v[76:79], v[168:171], a[236:239], v[76:79]\n"
      "ds_write_addtid_b32 v10 offset:512\n"
      "v_cvt_pk_bf16_f32 v12, v16, v17\n"
      "v_mfma_f32_16x16x32_bf16 v[80:83], v[168:171], a[240:243], v[80:83]\n"
      "ds_write_addtid_b32 v11 offset:768\n"
      "v_cvt_pk_bf16_f32 v13, v18, v19\n"
      "v_mfma_f32_16x16x32_bf16 v[84:87], v[168:171], a[244:247], v[84:87]\n"
      "ds_write_addtid_b32 v12 offset:1024\n"
      "v_cvt_pk_bf16_f32 v14, v20, v21\n"
      "v_mfma_f32_16x16x32_bf16 v[88:91], v[168:171], a[248:251], v[88:91]\n"
      "global_load_dwordx4 v[148:151], v240, s[74:75] offset:0 sc1\n"
      "v_max_u32_dpp v234, v234, v234 row_half_mirror row_mask:0xf bank_mask:0xf\n"
      "v_mfma_f32_16x16x32_bf16 v[92:95], v[168:171], a[252:255], v[92:95]\n"
      "ds_write_addtid_b32 v13 offset:1280\n"
      "v_cvt_pk_bf16_f32 v15, v22, v23\n"
      "v_mfma_f32_16x16x32_bf16 v[96:99], v[172:175], a[96:99], v[96:99]\n"
      "ds_write_addtid_b32 v14 offset:1536\n"
      "v_cvt_pk_bf16_f32 v16, v24, v25\n"
      "v_mfma_f32_16x16x32_bf16 v[100:103], v[172:175], a[100:103], v[100:103]\n"
      "ds_write_addtid_b32 v15 offset:1792\n"
      "v_cvt_pk_bf16_f32 v17, v26, v27\n"
      "v_mfma_f32_16x16x32_bf16 v[104:107], v[172:175], a[104:107], v[104:107]\n"
      "global_load_dwordx4 v[152:155], v241, s[14:15] offset:0 sc1\n"
      "v_max_u32_dpp v234, v234, v234 row_mirror row_mask:0xf bank_mask:0xf\n"
      "v_mfma_f32_16x16x32_bf16 v[108:111], v[172:175], a[108:111], v[108:111]\n"
      "ds_write_addtid_b32 v16 offset:2048\n"
      "v_cvt_pk_bf16_f32 v18, v28, v29\n"
      "v_mfma_f32_16x16x32_bf16 v[112:115], v[172:175], a[112:115], v[112:115]\n"
      "ds_write_addtid_b32 v17 offset:2304\n"
      "v_cvt_pk_bf16_f32 v19, v30, v31\n"
      "v_mfma_f32_16x16x32_bf16 v[116:119], v[172:175], a[116:119], v[116:119]\n"
      "ds_write_addtid_b32 v18 offset:2560\n"
      "v_mov_b32 v233, v234\n"
      "v_mfma_f32_16x16x32_bf16 v[120:123], v[172:175], a[120:123], v[120:123]\n"
      "global_load_dwordx4 v[156:159], v241, s[74:75] offset:0 sc1\n"
      "v_permlane16_swap_b32 v234, v233\n"
      "v_mfma_f32_16x16x32_bf16 v[124:127], v[172:175], a[124:127], v[124:127]\n"
      "s_waitcnt vmcnt(14)\n"
      "ds_write_addtid_b32 v19 offset:2816\n"
      "v_cvt_pk_bf16_f32 v40, v40, v41\n"
      "v_mfma_f32_16x16x32_bf16 v[96:99], v[176:179], a[128:131], v[96:99]\n"
      "v_cvt_pk_bf16_f32 v41, v42, v43\n"
      "v_max_u32 v234, v234, v233\n"
      "v_mfma_f32_16x16x32_bf16 v[100:103], v[176:179], a[132:135], v[100:103]\n"
      "ds_write_addtid_b32 v40 offset:3072\n"
      "v_cvt_pk_bf16_f32 v42, v44, v45\n"
      "v_mfma_f32_16x16x32_bf16 v[104:107], v[176:179], a[136:139], v[104:107]\n"
      "global_load_dwordx4 v[160:163], v240, s[10:11] offset:1024 sc1\n"
      "v_mov_b32 v233, v234\n"
      "v_mfma_f32_16x16x32_bf16 v[108:111], v[176:179], a[140:143], v[108:111]\n"
      "ds_write_addtid_b32 v41 offset:3328\n"
      "v_cvt_pk_bf16_f32 v43, v46, v47\n"
      "v_mfma_f32_16x16x32_bf16 v[112:115], v[176:179], a[144:147], v[112:115]\n"
      "ds_write_addtid_b32 v42 offset:3584\n"
      "v_cvt_pk_bf16_f32 v44, v48, v49\n"
      "v_mfma_f32_16x16x32_bf16 v[116:119], v[176:179], a[148:151], v[116:119]\n"
      "ds_write_addtid_b32 v43 offset:3840\n"
      "v_cvt_pk_bf16_f32 v45, v50, v51\n"
      "v_mfma_f32_16x16x32_bf16 v[120:123], v[176:179], a[152:155], v[120:123]\n"
      "global_load_dwordx4 v[164:167], v240, s[72:73] offset:1024 sc1\n"
      "v_permlane32_swap_b32 v234, v233\n"
      "v_mfma_f32_16x16x32_bf16 v[124:127], v[176:179], a[156:159], v[124:127]\n"
      "ds_write_addtid_b32 v44 offset:4096\n"
      "v_cvt_pk_bf16_f32 v46, v52, v53\n"
      "v_mfma_f32_16x16x32_bf16 v[96:99], v[180:183], a[160:163], v[96:99]\n"
      "ds_write_addtid_b32 v45 offset:4352\n"
      "v_cvt_pk_bf16_f32 v47, v54, v55\n"
      "v_mfma_f32_16x16x32_bf16 v[100:103], v[180:183], a[164:167], v[100:103]\n"
      "ds_write_addtid_b32 v46 offset:4608\n"
      "v_cvt_pk_bf16_f32 v48, v56, v57\n"
      "v_mfma_f32_16x16x32_bf16 v[104:107], v[180:183], a[168:171], v[104:107]\n"
      "global_load_dwordx4 v[168:171], v241, s[10:11] offset:1024 sc1\n"
      "v_max_u32 v234, v234, v233\n"
      "v_mfma_f32_16x16x32_bf16 v[108:111], v[180:183], a[172:175], v[108:111]\n"
      "ds_write_addtid_b32 v47 offset:4864\n"
      "v_cvt_pk_bf16_f32 v49, v58, v59\n"
      "v_mfma_f32_16x16x32_bf16 v[112:115], v[180:183], a[176:179], v[112:115]\n"
      "ds_write_addtid_b32 v48 offset:5120\n"
      "v_cvt_pk_bf16_f32 v50, v60, v61\n"
      "v_mfma_f32_16x16x32_bf16 v[116:119], v[180:183], a[180:183], v[116:119]\n"
      "ds_write_addtid_b32 v49 offset:5376\n"
      "v_cvt_pk_bf16_f32 v51, v62, v63\n"
      "v_mfma_f32_16x16x32_bf16 v[120:123], v[180:183], a[184:187], v[120:123]\n"
      "global_load_dwordx4 v[172:175], v241, s[72:73] offset:1024 sc1\n"
      "v_readfirstlane_b32 s40, v234\n"
      "v_mfma_f32_16x16x32_bf16 v[124:127], v[180:183], a[188:191], v[124:127]\n"
      "s_waitcnt vmcnt(12)\n"
      "ds_write_addtid_b32 v50 offset:5632\n"
      "s_andn2_b32 s42, 0x7ff, s40\n"
      "v_mfma_f32_16x16x32_bf16 v[96:99], v[184:187], a[192:195], v[96:99]\n"
      "ds_write_addtid_b32 v51 offset:5888\n"
      "v_mfma_f32_16x16x32_bf16 v[100:103], v[184:187], a[196:199], v[100:103]\n"
      "v_mfma_f32_16x16x32_bf16 v[104:107], v[184:187], a[200:203], v[104:107]\n"
      "global_load_dwordx4 v[176:179], v240, s[14:15] offset:1024 sc1\n"
      "v_mfma_f32_16x16x32_bf16 v[108:111], v[184:187], a[204:207], v[108:111]\n"
      "s_waitcnt lgkmcnt(0)\n"
      "s_barrier\n"
      "v_mfma_f32_16x16x32_bf16 v[112:115], v[184:187], a[208:211], v[112:115]\n"
      "ds_read2st64_b32 v[20:21], v244 offset0:0 offset1:1\n"
      "v_mfma_f32_16x16x32_bf16 v[116:119], v[184:187], a[212:215], v[116:119]\n"
      "ds_read2st64_b32 v[22:23], v245 offset0:4 offset1:5\n"
      "v_mfma_f32_16x16x32_bf16 v[120:123], v[184:187], a[216:219], v[120:123]\n"
      "global_load_dwordx4 v[180:183], v240, s[74:75] offset:1024 sc1\n"
      "v_mfma_f32_16x16x32_bf16 v[124:127], v[184:187], a[220:223], v[124:127]\n"
      "ds_read2st64_b32 v[24:25], v244 offset0:2 offset1:3\n"
      "v_mfma_f32_16x16x32_bf16 v[96:99], v[188:191], a[224:227], v[96:99]\n"
      "ds_read2st64_b32 v[26:27], v245 offset0:6 offset1:7\n"
      "v_mfma_f32_16x16x32_bf16 v[100:103], v[188:191], a[228:231], v[100:103]\n"
      "ds_read2st64_b32 v[28:29], v246 offset0:8 offset1:9\n"
      "v_mfma_f32_16x16x32_bf16 v[104:107], v[188:191], a[232:235], v[104:107]\n"
      "global_load_dwordx4 v[184:187], v241, s[14:15] offset:1024 sc1\n"
      "v_mfma_f32_16x16x32_bf16 v[108:111], v[188:191], a[236:239], v[108:111]\n"
      "ds_read2st64_b32 v[30:31], v246 offset0:10 offset1:11\n"
      "v_mfma_f32_16x16x32_bf16 v[112:115], v[188:191], a[240:243], v[112:115]\n"
      "ds_read2st64_b32 v[52:53], v244 offset0:12 offset1:13\n"
      "v_mfma_f32_16x16x32_bf16 v[116:119], v[188:191], a[244:247], v[116:119]\n"
      "ds_read2st64_b32 v[54:55], v245 offset0:16 offset1:17\n"
      "v_mfma_f32_16x16x32_bf16 v[120:123], v[188:191], a[248:251], v[120:123]\n"
      "ds_read2st64_b32 v[56:57], v244 offset0:14 offset1:15\n"
      "v_mfma_f32_16x16x32_bf16 v[124:127], v[188:191], a[252:255], v[124:127]\n"
      "s_waitcnt vmcnt(13)\n"
      "global_load_dwordx4 v[188:191], v241, s[74:75] offset:1024 sc1\n"
      "s_waitcnt lgkmcnt(3)\n"
      "v_mfma_f32_16x16x32_bf16 v[0:3], v[250:253], v[20:23], v[0:3]\n"
      "v_mfma_f32_16x16x32_bf16 v[4:7], v[250:253], v[24:27], v[4:7]\n"
      "v_mfma_f32_16x16x32_bf16 v[0:3], v[252:255], v[28:31], v[0:3]\n"
      "v_mfma_f32_16x16x32_bf16 v[4:7], v[248:251], v[28:31], v[4:7]\n"
      "v_mov_b32 v11, s37\n"
      "s_nop 5\n"
      "v_cvt_pk_bf16_f32 v8, v0, v1\n"
      "v_cvt_pk_bf16_f32 v9, v2, v3\n"
      "v_cvt_pk_bf16_f32 v10, v4, v5\n"
      "v_cvt_pk_bf16_f32 v247, v6, v7\n"
      "global_store_dwordx4 v242, v[8:11], s[52:53] offset:0 sc1\n"
      "v_mfma_f32_16x16x32_bf16 v[0:3], v[128:131], a[0:3], 0\n"
      "ds_read2st64_b32 v[58:59], v245 offset0:18 offset1:19\n"
      "v_mfma_f32_16x16x32_bf16 v[4:7], v[128:131], a[4:7], 0\n"
      "ds_read2st64_b32 v[60:61], v246 offset0:20 offset1:21\n"
      "v_mfma_f32_16x16x32_bf16 v[8:11], v[128:131], a[8:11], 0\n"
      "ds_read2st64_b32 v[62:63], v246 offset0:22 offset1:23\n"
      "v_cvt_pk_bf16_f32 v72, v72, v73\n"
      "v_mfma_f32_16x16x32_bf16 v[12:15], v[128:131], a[12:15], 0\n"
      "v_bitop3_b32 v210, v205, v207, s37 bitop3:0x7e\n"
      "v_cmp_ne_u32 vcc, 0, v210\n"
      "s_nop 1\n"
      "s_cbranch_vccnz .Lp_s11_%=\n"
      ".Lp_o12_%=:\n"
      "v_mfma_f32_16x16x32_bf16 v[16:19], v[128:131], a[16:19], 0\n"
      "v_cvt_pk_bf16_f32 v73, v74, v75\n"
      "v_sub_f32 v205, 1.0, v204\n"
      "v_mfma_f32_16x16x32_bf16 v[20:23], v[128:131], a[20:23], 0\n"
      "ds_write_addtid_b32 v72 offset:6144\n"
      "v_cvt_pk_bf16_f32 v74, v76, v77\n"
      "v_mfma_f32_16x16x32_bf16 v[24:27], v[128:131], a[24:27], 0\n"
      "s_waitcnt lgkmcnt(1)\n"
      "v_mfma_f32_16x16x32_bf16 v[32:35], v[250:253], v[52:55], v[32:35]\n"
      "v_cvt_pk_bf16_f32 v205, v205, v205\n"
      "v_mfma_f32_16x16x32_bf16 v[28:31], v[128:131], a[28:31], 0\n"
      "global_load_dwordx4 v[128:131], v240, s[10:11] offset:2048 sc1\n"
      "v_and_b32 v205, 0xffff0000, v205\n"
      "v_mfma_f32_16x16x32_bf16 v[0:3], v[132:135], a[32:35], v[0:3]\n"
      "v_mfma_f32_16x16x32_bf16 v[36:39], v[250:253], v[56:59], v[36:39]\n"
      "v_sub_f32 v207, 1.0, v206\n"
      "v_mfma_f32_16x16x32_bf16 v[4:7], v[132:135], a[36:39], v[4:7]\n"
      "v_mfma_f32_16x16x32_bf16 v[32:35], v[252:255], v[60:63], v[32:35]\n"
      "v_cvt_pk_bf16_f32 v207, v207, v207\n"
      "v_mfma_f32_16x16x32_bf16 v[8:11], v[132:135], a[40:43], v[8:11]\n"
      "v_mfma_f32_16x16x32_bf16 v[36:39], v[248:251], v[60:63], v[36:39]\n"
      "v_mov_b32 v43, s37\n"
      "v_mfma_f32_16x16x32_bf16 v[12:15], v[132:135], a[44:47], v[12:15]\n"
      "ds_write_addtid_b32 v73 offset:6400\n"
      "v_mov_b32 v44, v247\n"
      "v_mfma_f32_16x16x32_bf16 v[16:19], v[132:135], a[48:51], v[16:19]\n"
      "v_mov_b32 v47, s37\n"
      "v_cvt_pk_bf16_f32 v75, v78, v79\n"
      "v_mfma_f32_16x16x32_bf16 v[20:23], v[132:135], a[52:55], v[20:23]\n"
      "ds_write_addtid_b32 v74 offset:6656\n"
      "v_cvt_pk_bf16_f32 v76, v80, v81\n"
      "v_mfma_f32_16x16x32_bf16 v[24:27], v[132:135], a[56:59], v[24:27]\n"
      "ds_write_addtid_b32 v75 offset:6912\n"
      "v_cvt_pk_bf16_f32 v40, v32, v33\n"
      "v_mfma_f32_16x16x32_bf16 v[28:31], v[132:135], a[60:63], v[28:31]\n"
      "s_waitcnt vmcnt(14)\n"
      "global_load_dwordx4 v[132:135], v240, s[72:73] offset:2048 sc1\n"
      "v_cvt_pk_bf16_f32 v41, v34, v35\n"
      "v_mfma_f32_16x16x32_bf16 v[0:3], v[136:139], a[64:67], v[0:3]\n"
      "v_cvt_pk_bf16_f32 v42, v36, v37\n"
      "v_cvt_pk_bf16_f32 v45, v38, v39\n"
      "v_mfma_f32_16x16x32_bf16 v[4:7], v[136:139], a[68:71], v[4:7]\n"
      "global_store_dwordx4 v242, v[40:43], s[52:53] offset:1024 sc1\n"
      "v_cvt_pk_bf16_f32 v77, v82, v83\n"
      "v_mfma_f32_16x16x32_bf16 v[8:11], v[136:139], a[72:75], v[8:11]\n"
      "global_store_dwordx4 v242, v[44:47], s[52:53] offset:2048 sc1\n"
      "s_add_u32 s48, s34, 2\n"
      "v_mfma_f32_16x16x32_bf16 v[12:15], v[136:139], a[76:79], v[12:15]\n"
      "s_and_b32 s48, s48, 3\n"
      "ds_write_addtid_b32 v76 offset:7168\n"
      "s_mul_i32 s48, s48, 36864\n"
      "v_mfma_f32_16x16x32_bf16 v[16:19], v[136:139], a[80:83], v[16:19]\n"
      "s_add_u32 s48, s48, 12288\n"
      "s_add_u32 s52, s60, s48\n"
      "s_addc_u32 s53, s61, 0\n"
      "v_mfma_f32_16x16x32_bf16 v[20:23], v[136:139], a[84:87], v[20:23]\n"
      "v_cvt_pk_bf16_f32 v78, v84, v85\n"
      "v_and_b32 v207, 0xffff0000, v207\n"
      "v_mfma_f32_16x16x32_bf16 v[24:27], v[136:139], a[88:91], v[24:27]\n"
      "ds_write_addtid_b32 v77 offset:7424\n"
      "v_cvt_pk_bf16_f32 v79, v86, v87\n"
      "v_mfma_f32_16x16x32_bf16 v[28:31], v[136:139], a[92:95], v[28:31]\n"
      "global_load_dwordx4 v[136:139], v241, s[10:11] offset:2048 sc1\n"
      "v_lshlrev_b32 v208, 16, v216\n"
      "v_mfma_f32_16x16x32_bf16 v[0:3], v[140:143], a[96:99], v[0:3]\n"
      "ds_write_addtid_b32 v78 offset:7680\n"
      "v_cvt_pk_bf16_f32 v80, v88, v89\n"
      "v_mfma_f32_16x16x32_bf16 v[4:7], v[140:143], a[100:103], v[4:7]\n"
      "ds_write_addtid_b32 v79 offset:7936\n"
      "v_cvt_pk_bf16_f32 v81, v90, v91\n"
      "v_mfma_f32_16x16x32_bf16 v[8:11], v[140:143], a[104:107], v[8:11]\n"
      "ds_write_addtid_b32 v80 offset:8192\n"
      "v_cvt_pk_bf16_f32 v82, v92, v93\n"
      "v_mfma_f32_16x16x32_bf16 v[12:15], v[140:143], a[108:111], v[12:15]\n"
      "ds_write_addtid_b32 v81 offset:8448\n"
      "v_cvt_pk_bf16_f32 v83, v94, v95\n"
      "v_mfma_f32_16x16x32_bf16 v[16:19], v[140:143], a[112:115], v[16:19]\n"
      "ds_write_addtid_b32 v82 offset:8704\n"
      "v_and_b32 v209, 0xffff0000, v216\n"
      "v_mfma_f32_16x16x32_bf16 v[20:23], v[140:143], a[116:119], v[20:23]\n"
      "ds_write_addtid_b32 v83 offset:8960\n"
      "v_cvt_pk_bf16_f32 v104, v104, v105\n"
      "v_mfma_f32_16x16x32_bf16 v[24:27], v[140:143], a[120:123], v[24:27]\n"
      "v_cvt_pk_bf16_f32 v105, v106, v107\n"
      "v_mul_f32 v208, v205, v208\n"
      "v_mfma_f32_16x16x32_bf16 v[28:31], v[140:143], a[124:127], v[28:31]\n"
      "s_waitcnt vmcnt(16)\n"
      "global_load_dwordx4 v[140:143], v241, s[72:73] offset:2048 sc1\n"
      "v_mul_f32 v209, v205, v209\n"
      "v_mfma_f32_16x16x32_bf16 v[0:3], v[144:147], a[128:131], v[0:3]\n"
      "ds_write_addtid_b32 v104 offset:9216\n"
      "v_cvt_pk_bf16_f32 v106, v108, v109\n"
      "v_mfma_f32_16x16x32_bf16 v[4:7], v[144:147], a[132:135], v[4:7]\n"
      "ds_write_addtid_b32 v105 offset:9472\n"
      "v_cvt_pk_bf16_f32 v107, v110, v111\n"
      "v_mfma_f32_16x16x32_bf16 v[8:11], v[144:147], a[136:139], v[8:11]\n"
      "global_load_dwordx4 v[224:227], v200, s[58:59] offset:0 sc1\n"
      "v_lshlrev_b32 v212, 16, v220\n"
      "v_mfma_f32_16x16x32_bf16 v[12:15], v[144:147], a[140:143], v[12:15]\n"
      "global_load_dwordx4 v[228:231], v200, s[58:59] offset:1024 sc1\n"
      "v_and_b32 v213, 0xffff0000, v220\n"
      "v_mfma_f32_16x16x32_bf16 v[16:19], v[144:147], a[144:147], v[16:19]\n"
      "global_load_dwordx4 v[232:235], v200, s[58:59] offset:2048 sc1\n"
      "v_mul_f32 v212, v207, v212\n"
      "v_mfma_f32_16x16x32_bf16 v[20:23], v[144:147], a[148:151], v[20:23]\n"
      "global_load_dwordx4 v[236:239], v200, s[58:59] offset:3072 sc1\n"
      "v_mul_f32 v213, v207, v213\n"
      "v_mfma_f32_16x16x32_bf16 v[24:27], v[144:147], a[152:155], v[24:27]\n"
      "ds_write_addtid_b32 v106 offset:9728\n"
      "v_cvt_pk_bf16_f32 v108, v112, v113\n"
      "v_mfma_f32_16x16x32_bf16 v[28:31], v[144:147], a[156:159], v[28:31]\n"
      "global_load_dwordx4 v[144:147], v240, s[14:15] offset:2048 sc1\n"
      "v_lshlrev_b32 v210, 16, v217\n"
      "v_mfma_f32_16x16x32_bf16 v[0:3], v[148:151], a[160:163], v[0:3]\n"
      "ds_write_addtid_b32 v107 offset:9984\n"
      "v_cvt_pk_bf16_f32 v109, v114, v115\n"
      "v_mfma_f32_16x16x32_bf16 v[4:7], v[148:151], a[164:167], v[4:7]\n"
      "ds_write_addtid_b32 v108 offset:10240\n"
      "v_cvt_pk_bf16_f32 v110, v116, v117\n"
      "v_mfma_f32_16x16x32_bf16 v[8:11], v[148:151], a[168:171], v[8:11]\n"
      "ds_write_addtid_b32 v109 offset:10496\n"
      "v_cvt_pk_bf16_f32 v111, v118, v119\n"
      "v_mfma_f32_16x16x32_bf16 v[12:15], v[148:151], a[172:175], v[12:15]\n"
      "ds_write_addtid_b32 v110 offset:10752\n"
      "v_cvt_pk_bf16_f32 v112, v120, v121\n"
      "v_mfma_f32_16x16x32_bf16 v[16:19], v[148:151], a[176:179], v[16:19]\n"
      "ds_write_addtid_b32 v111 offset:11008\n"
      "v_cvt_pk_bf16_f32 v113, v122, v123\n"
      "v_mfma_f32_16x16x32_bf16 v[20:23], v[148:151], a[180:183], v[20:23]\n"
      "ds_write_addtid_b32 v112 offset:11264\n"
      "v_cvt_pk_bf16_f32 v114, v124, v125\n"
      "v_mfma_f32_16x16x32_bf16 v[24:27], v[148:151], a[184:187], v[24:27]\n"
      "ds_write_addtid_b32 v113 offset:11520\n"
      "v_cvt_pk_bf16_f32 v115, v126, v127\n"
      "v_mfma_f32_16x16x32_bf16 v[28:31], v[148:151], a[188:191], v[28:31]\n"
      "s_waitcnt vmcnt(20)\n"
      "global_load_dwordx4 v[148:151], v240, s[74:75] offset:2048 sc1\n"
      "v_and_b32 v211, 0xffff0000, v217\n"
      "v_mfma_f32_16x16x32_bf16 v[0:3], v[152:155], a[192:195], v[0:3]\n"
      "ds_write_addtid_b32 v114 offset:11776\n"
      "v_mul_f32 v210, v205, v210\n"
      "v_mfma_f32_16x16x32_bf16 v[4:7], v[152:155], a[196:199], v[4:7]\n"
      "ds_write_addtid_b32 v115 offset:12032\n"
      "v_mul_f32 v211, v205, v211\n"
      "v_mfma_f32_16x16x32_bf16 v[8:11], v[152:155], a[200:203], v[8:11]\n"
      "v_lshlrev_b32 v214, 16, v221\n"
      "v_and_b32 v215, 0xffff0000, v221\n"
      "v_mfma_f32_16x16x32_bf16 v[12:15], v[152:155], a[204:207], v[12:15]\n"
      "v_mul_f32 v214, v207, v214\n"
      "v_mul_f32 v215, v207, v215\n"
      "v_mfma_f32_16x16x32_bf16 v[16:19], v[152:155], a[208:211], v[16:19]\n"
      "s_waitcnt lgkmcnt(0)\n"
      "s_barrier\n"
      "v_mfma_f32_16x16x32_bf16 v[20:23], v[152:155], a[212:215], v[20:23]\n"
      "ds_read2st64_b32 v[84:85], v244 offset0:24 offset1:25\n"
      "v_lshlrev_b32 v216, 16, v218\n"
      "v_mfma_f32_16x16x32_bf16 v[24:27], v[152:155], a[216:219], v[24:27]\n"
      "ds_read2st64_b32 v[86:87], v245 offset0:28 offset1:29\n"
      "v_and_b32 v217, 0xffff0000, v218\n"
      "v_mfma_f32_16x16x32_bf16 v[28:31], v[152:155], a[220:223], v[28:31]\n"
      "global_load_dwordx4 v[152:155], v241, s[14:15] offset:2048 sc1\n"
      "v_mul_f32 v216, v205, v216\n"
      "v_mfma_f32_16x16x32_bf16 v[0:3], v[156:159], a[224:227], v[0:3]\n"
      "ds_read2st64_b32 v[88:89], v244 offset0:26 offset1:27\n"
      "v_mul_f32 v217, v205, v217\n"
      "v_mfma_f32_16x16x32_bf16 v[4:7], v[156:159], a[228:231], v[4:7]\n"
      "ds_read2st64_b32 v[90:91], v245 offset0:30 offset1:31\n"
      "v_lshlrev_b32 v220, 16, v222\n"
      "v_mfma_f32_16x16x32_bf16 v[8:11], v[156:159], a[232:235], v[8:11]\n"
      "ds_read2st64_b32 v[92:93], v246 offset0:32 offset1:33\n"
      "v_and_b32 v221, 0xffff0000, v222\n"
      "v_mfma_f32_16x16x32_bf16 v[12:15], v[156:159], a[236:239], v[12:15]\n"
      "ds_read2st64_b32 v[94:95], v246 offset0:34 offset1:35\n"
      "v_mul_f32 v220, v207, v220\n"
      "v_mfma_f32_16x16x32_bf16 v[16:19], v[156:159], a[240:243], v[16:19]\n"
      "ds_read2st64_b32 v[116:117], v244 offset0:36 offset1:37\n"
      "v_mul_f32 v221, v207, v221\n"
      "v_mfma_f32_16x16x32_bf16 v[20:23], v[156:159], a[244:247], v[20:23]\n"
      "ds_read2st64_b32 v[118:119], v245 offset0:40 offset1:41\n"
      "v_lshlrev_b32 v218, 16, v219\n"
      "v_mfma_f32_16x16x32_bf16 v[24:27], v[156:159], a[248:251], v[24:27]\n"
      "ds_read2st64_b32 v[120:121], v244 offset0:38 offset1:39\n"
      "v_and_b32 v219, 0xffff0000, v219\n"
      "v_mfma_f32_16x16x32_bf16 v[28:31], v[156:159], a[252:255], v[28:31]\n"
      "s_waitcnt vmcnt(20)\n"
      "global_load_dwordx4 v[156:159], v241, s[74:75] offset:2048 sc1\n"
      "s_waitcnt lgkmcnt(3)\n"
      "v_mfma_f32_16x16x32_bf16 v[32:35], v[160:163], a[0:3], 0\n"
      "v_mfma_f32_16x16x32_bf16 v[64:67], v[250:253], v[84:87], v[64:67]\n"
      "v_mul_f32 v218, v205, v218\n"
      "v_mfma_f32_16x16x32_bf16 v[36:39], v[160:163], a[4:7], 0\n"
      "v_mfma_f32_16x16x32_bf16 v[68:71], v[250:253], v[88:91], v[68:71]\n"
      "v_mul_f32 v219, v205, v219\n"
      "v_mfma_f32_16x16x32_bf16 v[40:43], v[160:163], a[8:11], 0\n"
      "v_mfma_f32_16x16x32_bf16 v[64:67], v[252:255], v[92:95], v[64:67]\n"
      "v_lshlrev_b32 v222, 16, v223\n"
      "v_mfma_f32_16x16x32_bf16 v[44:47], v[160:163], a[12:15], 0\n"
      "v_mfma_f32_16x16x32_bf16 v[68:71], v[248:251], v[92:95], v[68:71]\n"
      "v_mov_b32 v75, s37\n"
      "v_mfma_f32_16x16x32_bf16 v[48:51], v[160:163], a[16:19], 0\n"
      "ds_read2st64_b32 v[122:123], v245 offset0:42 offset1:43\n"
      "v_and_b32 v223, 0xffff0000, v223\n"
      "v_mfma_f32_16x16x32_bf16 v[52:55], v[160:163], a[20:23], 0\n"
      "ds_read2st64_b32 v[124:125], v246 offset0:44 offset1:45\n"
      "v_cvt_pk_bf16_f32 v72, v64, v65\n"
      "v_mfma_f32_16x16x32_bf16 v[56:59], v[160:163], a[24:27], 0\n"
      "ds_read2st64_b32 v[126:127], v246 offset0:46 offset1:47\n"
      "v_cvt_pk_bf16_f32 v73, v66, v67\n"
      "v_mfma_f32_16x16x32_bf16 v[60:63], v[160:163], a[28:31], 0\n"
      "global_load_dwordx4 v[160:163], v240, s[10:11] offset:3072 sc1\n"
      "v_cvt_pk_bf16_f32 v74, v68, v69\n"
      "v_mfma_f32_16x16x32_bf16 v[32:35], v[164:167], a[32:35], v[32:35]\n"
      "v_cvt_pk_bf16_f32 v247, v70, v71\n"
      "v_cvt_pk_bf16_f32 v8, v8, v9\n"
      "v_mfma_f32_16x16x32_bf16 v[36:39], v[164:167], a[36:39], v[36:39]\n"
      "global_store_dwordx4 v242, v[72:75], s[52:53] offset:0 sc1\n"
      "v_cvt_pk_bf16_f32 v9, v10, v11\n"
      "v_mfma_f32_16x16x32_bf16 v[40:43], v[164:167], a[40:43], v[40:43]\n"
      "s_waitcnt lgkmcnt(0)\n"
      "v_mfma_f32_16x16x32_bf16 v[96:99], v[250:253], v[116:119], v[96:99]\n"
      "v_mul_f32 v222, v207, v222\n"
      "v_mfma_f32_16x16x32_bf16 v[44:47], v[164:167], a[44:47], v[44:47]\n"
      "v_mfma_f32_16x16x32_bf16 v[100:103], v[250:253], v[120:123], v[100:103]\n"
      "v_mul_f32 v223, v207, v223\n"
      "v_mfma_f32_16x16x32_bf16 v[48:51], v[164:167], a[48:51], v[48:51]\n"
      "v_mfma_f32_16x16x32_bf16 v[96:99], v[252:255], v[124:127], v[96:99]\n"
      "v_mfma_f32_16x16x32_bf16 v[52:55], v[164:167], a[52:55], v[52:55]\n"
      "s_waitcnt vmcnt(6)\n"
      "v_bitop3_b32 v192, v225, v227, s37 bitop3:0x7e\n"
      "v_bitop3_b32 v192, v192, v229, s37 bitop3:0xf6\n"
      "v_bitop3_b32 v192, v192, v231, s37 bitop3:0xf6\n"
      "v_bitop3_b32 v192, v192, v233, s37 bitop3:0xf6\n"
      "v_bitop3_b32 v192, v192, v235, s37 bitop3:0xf6\n"
      "v_bitop3_b32 v192, v192, v237, s37 bitop3:0xf6\n"
      "v_bitop3_b32 v192, v192, v239, s37 bitop3:0xf6\n"
      "v_cmp_ne_u32 vcc, 0, v192\n"
      "s_nop 1\n"
      "s_cbranch_vccnz .Lp_s14_%=\n"
      ".Lp_o15_%=:\n"
      "v_mfma_f32_16x16x32_bf16 v[56:59], v[164:167], a[56:59], v[56:59]\n"
      "v_mfma_f32_16x16x32_bf16 v[100:103], v[248:251], v[124:127], v[100:103]\n"
      "v_add_f32 v192, v224, v228\n"
      "v_mfma_f32_16x16x32_bf16 v[60:63], v[164:167], a[60:63], v[60:63]\n"
      "global_load_dwordx4 v[164:167], v240, s[72:73] offset:3072 sc1\n"
      "v_add_f32 v193, v226, v230\n"
      "v_mfma_f32_16x16x32_bf16 v[32:35], v[168:171], a[64:67], v[32:35]\n"
      "ds_write_addtid_b32 v8 offset:0\n"
      "v_add_f32 v194, v232, v236\n"
      "v_mfma_f32_16x16x32_bf16 v[36:39], v[168:171], a[68:71], v[36:39]\n"
      "v_add_f32 v195, v234, v238\n"
      "v_mov_b32 v107, s37\n"
      "v_mfma_f32_16x16x32_bf16 v[40:43], v[168:171], a[72:75], v[40:43]\n"
      "ds_read_b64_tr_b16 v[224:225], v201 offset:0\n"
      "v_permlane32_swap_b32 v192, v193\n"
      "v_mfma_f32_16x16x32_bf16 v[44:47], v[168:171], a[76:79], v[44:47]\n"
      "ds_read_b64_tr_b16 v[226:227], v201 offset:512\n"
      "v_permlane32_swap_b32 v194, v195\n"
      "v_mfma_f32_16x16x32_bf16 v[48:51], v[168:171], a[80:83], v[48:51]\n"
      "ds_read_b64_tr_b16 v[228:229], v201 offset:32\n"
      "v_add_f32 v192, v192, v193\n"
      "v_mfma_f32_16x16x32_bf16 v[52:55], v[168:171], a[84:87], v[52:55]\n"
      "ds_read_b64_tr_b16 v[230:231], v201 offset:544\n"
      "v_add_f32 v194, v194, v195\n"
      "v_mfma_f32_16x16x32_bf16 v[56:59], v[168:171], a[88:91], v[56:59]\n"
      "ds_read_b64_tr_b16 v[232:233], v201 offset:64\n"
      "v_mov_b32 v193, v192\n"
      "v_mfma_f32_16x16x32_bf16 v[60:63], v[168:171], a[92:95], v[60:63]\n"
      "global_load_dwordx4 v[168:171], v241, s[10:11] offset:3072 sc1\n"
      "v_mov_b32 v108, v247\n"
      "v_mfma_f32_16x16x32_bf16 v[32:35], v[172:175], a[96:99], v[32:35]\n"
      "ds_read_b64_tr_b16 v[234:235], v201 offset:576\n"
      "v_mov_b32 v195, v194\n"
      "v_mfma_f32_16x16x32_bf16 v[36:39], v[172:175], a[100:103], v[36:39]\n"
      "ds_read_b64_tr_b16 v[236:237], v201 offset:96\n"
      "v_permlane16_swap_b32 v192, v193\n"
      "v_mfma_f32_16x16x32_bf16 v[40:43], v[172:175], a[104:107], v[40:43]\n"
      "ds_read_b64_tr_b16 v[238:239], v201 offset:608\n"
      "v_permlane16_swap_b32 v194, v195\n"
      "v_mfma_f32_16x16x32_bf16 v[44:47], v[172:175], a[108:111], v[44:47]\n"
      "v_add_f32 v192, v192, v193\n"
      "v_add_f32 v194, v194, v195\n"
      "v_mfma_f32_16x16x32_bf16 v[48:51], v[172:175], a[112:115], v[48:51]\n"
      "v_cvt_pk_bf16_f32 v192, v192, v194\n"
      "v_and_b32 v194, 0xffff0000, v192\n"
      "v_mfma_f32_16x16x32_bf16 v[52:55], v[172:175], a[116:119], v[52:55]\n"
      "v_lshlrev_b32 v192, 16, v192\n"
      "v_mul_f32 v193, 0xbfb8aa3b, v192\n"
      "v_mfma_f32_16x16x32_bf16 v[56:59], v[172:175], a[120:123], v[56:59]\n"
      "v_exp_f32 v193, v193\n"
      "s_nop 0\n"
      "v_add_f32 v193, 1.0, v193\n"
      "v_mfma_f32_16x16x32_bf16 v[60:63], v[172:175], a[124:127], v[60:63]\n"
      "global_load_dwordx4 v[172:175], v241, s[72:73] offset:3072 sc1\n"
      "v_rcp_f32 v193, v193\n"
      "v_mfma_f32_16x16x32_bf16 v[32:35], v[176:179], a[128:131], v[32:35]\n"
      "v_mul_f32 v192, v192, v193\n"
      "v_cvt_pk_bf16_f32 v192, v192, v192\n"
      "v_mfma_f32_16x16x32_bf16 v[36:39], v[176:179], a[132:135], v[36:39]\n"
      "v_and_b32 v192, 0xffff0000, v192\n"
      "v_mul_f32 v192, v192, v194\n"
      "v_mfma_f32_16x16x32_bf16 v[40:43], v[176:179], a[136:139], v[40:43]\n"
      "v_cvt_pk_bf16_f32 v192, v192, v192\n"
      "v_mov_b32 v111, s37\n"
      "v_mfma_f32_16x16x32_bf16 v[44:47], v[176:179], a[140:143], v[44:47]\n"
      "ds_write_b16 v202, v192\n"
      "v_cvt_pk_bf16_f32 v10, v12, v13\n"
      "v_mfma_f32_16x16x32_bf16 v[48:51], v[176:179], a[144:147], v[48:51]\n"
      "ds_read_b128 v[192:195], v199\n"
      "v_cvt_pk_bf16_f32 v104, v96, v97\n"
      "v_mfma_f32_16x16x32_bf16 v[52:55], v[176:179], a[148:151], v[52:55]\n"
      "ds_write_addtid_b32 v9 offset:256\n"
      "v_cvt_pk_bf16_f32 v105, v98, v99\n"
      "v_mfma_f32_16x16x32_bf16 v[56:59], v[176:179], a[152:155], v[56:59]\n"
      "v_cvt_pk_bf16_f32 v106, v100, v101\n"
      "v_cvt_pk_bf16_f32 v109, v102, v103\n"
      "v_mfma_f32_16x16x32_bf16 v[60:63], v[176:179], a[156:159], v[60:63]\n"
      "global_load_dwordx4 v[176:179], v240, s[14:15] offset:3072 sc1\n"
      "v_cvt_pk_bf16_f32 v11, v14, v15\n"
      "v_mfma_f32_16x16x32_bf16 v[32:35], v[180:183], a[160:163], v[32:35]\n"
      "global_store_dwordx4 v242, v[104:107], s[52:53] offset:1024 sc1\n"
      "v_mfma_f32_16x16x32_bf16 v[36:39], v[180:183], a[164:167], v[36:39]\n"
      "global_store_dwordx4 v242, v[108:111], s[52:53] offset:2048 sc1\n"
      "s_add_u32 s48, s34, 3\n"
      "v_mfma_f32_16x16x32_bf16 v[40:43], v[180:183], a[168:171], v[40:43]\n"
      "s_and_b32 s48, s48, 3\n"
      "s_waitcnt lgkmcnt(1)\n"
      "v_mfma_f32_16x16x32_bf16 v[196:199], v[192:195], v[224:227], 0\n"
      "v_mfma_f32_16x16x32_bf16 v[44:47], v[180:183], a[172:175], v[44:47]\n"
      "s_mul_i32 s48, s48, 36864\n"
      "v_mfma_f32_16x16x32_bf16 v[224:227], v[192:195], v[228:231], 0\n"
      "s_add_u32 s48, s48, 24576\n"
      "v_mfma_f32_16x16x32_bf16 v[48:51], v[180:183], a[176:179], v[48:51]\n"
      "s_add_u32 s52, s60, s48\n"
      "s_addc_u32 s53, s61, 0\n"
      "v_mfma_f32_16x16x32_bf16 v[228:231], v[192:195], v[232:235], 0\n"
      "v_mfma_f32_16x16x32_bf16 v[52:55], v[180:183], a[180:183], v[52:55]\n"
      "v_mfma_f32_16x16x32_bf16 v[232:235], v[192:195], v[236:239], 0\n"
      "ds_write_addtid_b32 v10 offset:512\n"
      "v_mov_b32 v193, s37\n"
      "v_mfma_f32_16x16x32_bf16 v[56:59], v[180:183], a[184:187], v[56:59]\n"
      "v_mov_b32 v195, s37\n"
      "v_cvt_pk_bf16_f32 v12, v16, v17\n"
      "v_mfma_f32_16x16x32_bf16 v[60:63], v[180:183], a[188:191], v[60:63]\n"
      "global_load_dwordx4 v[180:183], v240, s[74:75] offset:3072 sc1\n"
      "ds_write_addtid_b32 v11 offset:768\n"
      "v_cvt_pk_bf16_f32 v13, v18, v19\n"
      "v_mfma_f32_16x16x32_bf16 v[40:43], v[184:187], a[200:203], v[40:43]\n"
      "ds_write_addtid_b32 v12 offset:1024\n"
      "v_cndmask_b32 v192, v196, v228, s[64:65]\n"
      "v_cndmask_b32 v194, v224, v232, s[64:65]\n"
      "v_mfma_f32_16x16x32_bf16 v[44:47], v[184:187], a[204:207], v[44:47]\n"
      "v_and_b32 v192, s86, v192\n"
      "v_and_b32 v194, s86, v194\n"
      "v_cvt_pk_bf16_f32 v14, v20, v21\n"
      "v_mfma_f32_16x16x32_bf16 v[48:51], v[184:187], a[208:211], v[48:51]\n"
      "s_mov_b64 exec, s[66:67]\n"
      "global_store_dwordx4 v203, v[192:195], s[58:59] sc1\n"
      "s_mov_b64 exec, -1\n"
      "v_mfma_f32_16x16x32_bf16 v[52:55], v[184:187], a[212:215], v[52:55]\n"
      "ds_write_addtid_b32 v13 offset:1280\n"
      "v_cvt_pk_bf16_f32 v15, v22, v23\n"
      "v_mfma_f32_16x16x32_bf16 v[56:59], v[184:187], a[216:219], v[56:59]\n"
      "ds_write_addtid_b32 v14 offset:1536\n"
      "v_cvt_pk_bf16_f32 v16, v24, v25\n"
      "v_mfma_f32_16x16x32_bf16 v[60:63], v[184:187], a[220:223], v[60:63]\n"
      "ds_write_addtid_b32 v15 offset:1792\n"
      "v_cvt_pk_bf16_f32 v17, v26, v27\n"
      "v_mfma_f32_16x16x32_bf16 v[40:43], v[188:191], a[232:235], v[40:43]\n"
      "ds_write_addtid_b32 v16 offset:2048\n"
      "v_cvt_pk_bf16_f32 v18, v28, v29\n"
      "v_mfma_f32_16x16x32_bf16 v[44:47], v[188:191], a[236:239], v[44:47]\n"
      "ds_write_addtid_b32 v17 offset:2304\n"
      "v_cvt_pk_bf16_f32 v19, v30, v31\n"
      "v_mfma_f32_16x16x32_bf16 v[48:51], v[188:191], a[240:243], v[48:51]\n"
      "ds_write_addtid_b32 v18 offset:2560\n"
      "ds_write_addtid_b32 v19 offset:2816\n"
      "v_mfma_f32_16x16x32_bf16 v[52:55], v[188:191], a[244:247], v[52:55]\n"
      "v_mfma_f32_16x16x32_bf16 v[56:59], v[188:191], a[248:251], v[56:59]\n"
      "v_mfma_f32_16x16x32_bf16 v[60:63], v[188:191], a[252:255], v[60:63]\n"
      "s_waitcnt lgkmcnt(0)\n"
      "s_barrier\n"
      "v_mfma_f32_16x16x32_bf16 v[32:35], v[184:187], a[192:195], v[32:35]\n"
      "ds_read2st64_b32 v[20:21], v244 offset0:0 offset1:1\n"
      "ds_read2st64_b32 v[22:23], v245 offset0:4 offset1:5\n"
      "v_mfma_f32_16x16x32_bf16 v[36:39], v[184:187], a[196:199], v[36:39]\n"
      "global_load_dwordx4 v[184:187], v241, s[14:15] offset:3072 sc1\n"
      "ds_read2st64_b32 v[24:25], v244 offset0:2 offset1:3\n"
      "v_mfma_f32_16x16x32_bf16 v[32:35], v[188:191], a[224:227], v[32:35]\n"
      "ds_read2st64_b32 v[26:27], v245 offset0:6 offset1:7\n"
      "ds_read2st64_b32 v[28:29], v246 offset0:8 offset1:9\n"
      "v_mfma_f32_16x16x32_bf16 v[36:39], v[188:191], a[228:231], v[36:39]\n"
      "ds_read2st64_b32 v[30:31], v246 offset0:10 offset1:11\n"
      "v_cvt_pk_bf16_f32 v40, v40, v41\n"
      "v_cvt_pk_bf16_f32 v41, v42, v43\n"
      "v_mfma_f32_16x16x32_bf16 v[72:75], v[128:131], a[8:11], 0\n"
      "ds_write_addtid_b32 v40 offset:3072\n"
      "v_cvt_pk_bf16_f32 v42, v44, v45\n"
      "v_mfma_f32_16x16x32_bf16 v[76:79], v[128:131], a[12:15], 0\n"
      "global_load_dwordx4 v[188:191], v241, s[74:75] offset:3072 sc1\n"
      "ds_write_addtid_b32 v41 offset:3328\n"
      "v_cvt_pk_bf16_f32 v43, v46, v47\n"
      "v_mfma_f32_16x16x32_bf16 v[80:83], v[128:131], a[16:19], 0\n"
      "ds_write_addtid_b32 v42 offset:3584\n"
      "v_cvt_pk_bf16_f32 v44, v48, v49\n"
      "v_mfma_f32_16x16x32_bf16 v[84:87], v[128:131], a[20:23], 0\n"
      "s_waitcnt lgkmcnt(3)\n"
      "v_mfma_f32_16x16x32_bf16 v[0:3], v[250:253], v[20:23], v[0:3]\n"
      "v_mfma_f32_16x16x32_bf16 v[4:7], v[250:253], v[24:27], v[4:7]\n"
      "v_mfma_f32_16x16x32_bf16 v[88:91], v[128:131], a[24:27], 0\n"
      "v_mfma_f32_16x16x32_bf16 v[0:3], v[252:255], v[28:31], v[0:3]\n"
      "v_mfma_f32_16x16x32_bf16 v[4:7], v[248:251], v[28:31], v[4:7]\n"
      "v_mov_b32 v11, s37\n"
      "v_mfma_f32_16x16x32_bf16 v[92:95], v[128:131], a[28:31], 0\n"
      "ds_write_addtid_b32 v43 offset:3840\n"
      "v_cvt_pk_bf16_f32 v45, v50, v51\n"
      "v_mfma_f32_16x16x32_bf16 v[72:75], v[132:135], a[40:43], v[72:75]\n"
      "ds_write_addtid_b32 v44 offset:4096\n"
      "s_nop 0\n"
      "v_cvt_pk_bf16_f32 v8, v0, v1\n"
      "v_mfma_f32_16x16x32_bf16 v[76:79], v[132:135], a[44:47], v[76:79]\n"
      "v_cvt_pk_bf16_f32 v9, v2, v3\n"
      "v_cvt_pk_bf16_f32 v10, v4, v5\n"
      "v_cvt_pk_bf16_f32 v247, v6, v7\n"
      "v_mfma_f32_16x16x32_bf16 v[80:83], v[132:135], a[48:51], v[80:83]\n"
      "global_store_dwordx4 v242, v[8:11], s[52:53] offset:0 sc1\n"
      "v_cvt_pk_bf16_f32 v46, v52, v53\n"
      "s_lshl2_add_u32 s54, s36, s33\n"
      "v_mfma_f32_16x16x32_bf16 v[84:87], v[132:135], a[52:55], v[84:87]\n"
      "s_lshl_b32 s55, s54, 15\n"
      "ds_write_addtid_b32 v45 offset:4352\n"
      "v_cvt_pk_bf16_f32 v47, v54, v55\n"
      "s_lshl2_add_u32 s56, s35, s34\n"
      "v_mfma_f32_16x16x32_bf16 v[88:91], v[132:135], a[56:59], v[88:91]\n"
      "s_lshl_b32 s56, s56, 6\n"
      "ds_write_addtid_b32 v46 offset:4608\n"
      "v_cvt_pk_bf16_f32 v48, v56, v57\n"
      "s_add_u32 s55, s55, s56\n"
      "v_mfma_f32_16x16x32_bf16 v[92:95], v[132:135], a[60:63], v[92:95]\n"
      "ds_write_addtid_b32 v47 offset:4864\n"
      "v_cvt_pk_bf16_f32 v49, v58, v59\n"
      "v_and_b32 v8, 15, v243\n"
      "v_mfma_f32_16x16x32_bf16 v[72:75], v[136:139], a[72:75], v[72:75]\n"
      "ds_write_addtid_b32 v48 offset:5120\n"
      "v_cvt_pk_bf16_f32 v50, v60, v61\n"
      "v_and_b32 v7, 48, v243\n"
      "v_mfma_f32_16x16x32_bf16 v[76:79], v[136:139], a[76:79], v[76:79]\n"
      "ds_write_addtid_b32 v49 offset:5376\n"
      "v_cvt_pk_bf16_f32 v51, v62, v63\n"
      "v_lshl_or_b32 v7, v8, 10, v7\n"
      "v_mfma_f32_16x16x32_bf16 v[80:83], v[136:139], a[80:83], v[80:83]\n"
      "ds_write_addtid_b32 v50 offset:5632\n"
      "ds_write_addtid_b32 v51 offset:5888\n"
      "v_add_u32 v7, s55, v7\n"
      "v_mfma_f32_16x16x32_bf16 v[84:87], v[136:139], a[84:87], v[84:87]\n"
      "v_mfma_f32_16x16x32_bf16 v[88:91], v[136:139], a[88:91], v[88:91]\n"
      "v_add_u32 v4, 0, v242\n"
      "v_mfma_f32_16x16x32_bf16 v[92:95], v[136:139], a[92:95], v[92:95]\n"
      "global_load_dwordx4 v[12:15], v4, s[62:63] offset:0 sc1\n"
      "global_load_dwordx4 v[16:19], v4, s[62:63] offset:1024 sc1\n"
      "v_mfma_f32_16x16x32_bf16 v[72:75], v[140:143], a[104:107], v[72:75]\n"
      "global_load_dwordx4 v[20:23], v4, s[62:63] offset:2048 sc1\n"
      "s_waitcnt lgkmcnt(0)\n"
      "s_barrier\n"
      "v_mfma_f32_16x16x32_bf16 v[76:79], v[140:143], a[108:111], v[76:79]\n"
      "ds_read2st64_b32 v[52:53], v244 offset0:12 offset1:13\n"
      "ds_read2st64_b32 v[54:55], v245 offset0:16 offset1:17\n"
      "v_mfma_f32_16x16x32_bf16 v[80:83], v[140:143], a[112:115], v[80:83]\n"
      "ds_read2st64_b32 v[56:57], v244 offset0:14 offset1:15\n"
      "ds_read2st64_b32 v[58:59], v245 offset0:18 offset1:19\n"
      "v_mfma_f32_16x16x32_bf16 v[84:87], v[140:143], a[116:119], v[84:87]\n"
      "ds_read2st64_b32 v[60:61], v246 offset0:20 offset1:21\n"
      "ds_read2st64_b32 v[62:63], v246 offset0:22 offset1:23\n"
      "v_mfma_f32_16x16x32_bf16 v[88:91], v[140:143], a[120:123], v[88:91]\n"
      "v_mfma_f32_16x16x32_bf16 v[92:95], v[140:143], a[124:127], v[92:95]\n"
      "s_waitcnt vmcnt(18)\n"
      "v_mfma_f32_16x16x32_bf16 v[72:75], v[144:147], a[136:139], v[72:75]\n"
      "v_mfma_f32_16x16x32_bf16 v[76:79], v[144:147], a[140:143], v[76:79]\n"
      "s_waitcnt lgkmcnt(0)\n"
      "v_mfma_f32_16x16x32_bf16 v[32:35], v[250:253], v[52:55], v[32:35]\n"
      "v_mfma_f32_16x16x32_bf16 v[36:39], v[250:253], v[56:59], v[36:39]\n"
      "v_mfma_f32_16x16x32_bf16 v[80:83], v[144:147], a[144:147], v[80:83]\n"
      "v_mfma_f32_16x16x32_bf16 v[32:35], v[252:255], v[60:63], v[32:35]\n"
      "v_mfma_f32_16x16x32_bf16 v[84:87], v[144:147], a[148:151], v[84:87]\n"
      "v_mfma_f32_16x16x32_bf16 v[36:39], v[248:251], v[60:63], v[36:39]\n"
      "v_mov_b32 v43, s37\n"
      "v_mfma_f32_16x16x32_bf16 v[88:91], v[144:147], a[152:155], v[88:91]\n"
      "v_mov_b32 v44, v247\n"
      "v_mov_b32 v47, s37\n"
      "v_mfma_f32_16x16x32_bf16 v[92:95], v[144:147], a[156:159], v[92:95]\n"
      "v_mfma_f32_16x16x32_bf16 v[72:75], v[148:151], a[168:171], v[72:75]\n"
      "v_cvt_pk_bf16_f32 v40, v32, v33\n"
      "v_cvt_pk_bf16_f32 v41, v34, v35\n"
      "v_mfma_f32_16x16x32_bf16 v[76:79], v[148:151], a[172:175], v[76:79]\n"
      "v_cvt_pk_bf16_f32 v42, v36, v37\n"
      "v_cvt_pk_bf16_f32 v45, v38, v39\n"
      "v_mfma_f32_16x16x32_bf16 v[80:83], v[148:151], a[176:179], v[80:83]\n"
      "global_store_dwordx4 v242, v[40:43], s[52:53] offset:1024 sc1\n"
      "v_mfma_f32_16x16x32_bf16 v[84:87], v[148:151], a[180:183], v[84:87]\n"
      "global_store_dwordx4 v242, v[44:47], s[52:53] offset:2048 sc1\n"
      "v_mfma_f32_16x16x32_bf16 v[88:91], v[148:151], a[184:187], v[88:91]\n"
      "v_mfma_f32_16x16x32_bf16 v[92:95], v[148:151], a[188:191], v[92:95]\n"
      "s_waitcnt vmcnt(18)\n"
      "v_mfma_f32_16x16x32_bf16 v[72:75], v[152:155], a[200:203], v[72:75]\n"
      "v_mfma_f32_16x16x32_bf16 v[76:79], v[152:155], a[204:207], v[76:79]\n"
      "v_mfma_f32_16x16x32_bf16 v[80:83], v[152:155], a[208:211], v[80:83]\n"
      "v_mfma_f32_16x16x32_bf16 v[84:87], v[152:155], a[212:215], v[84:87]\n"
      "v_mfma_f32_16x16x32_bf16 v[88:91], v[152:155], a[216:219], v[88:91]\n"
      "v_mfma_f32_16x16x32_bf16 v[92:95], v[152:155], a[220:223], v[92:95]\n"
      "v_mfma_f32_16x16x32_bf16 v[72:75], v[156:159], a[232:235], v[72:75]\n"
      "v_mfma_f32_16x16x32_bf16 v[76:79], v[156:159], a[236:239], v[76:79]\n"
      "v_mfma_f32_16x16x32_bf16 v[80:83], v[156:159], a[240:243], v[80:83]\n"
      "v_mfma_f32_16x16x32_bf16 v[84:87], v[156:159], a[244:247], v[84:87]\n"
      "v_mfma_f32_16x16x32_bf16 v[88:91], v[156:159], a[248:251], v[88:91]\n"
      "s_nop 3\n"
      "v_cvt_pk_bf16_f32 v72, v72, v73\n"
      "v_mfma_f32_16x16x32_bf16 v[92:95], v[156:159], a[252:255], v[92:95]\n"
      "s_waitcnt vmcnt(15)\n"
      "v_cvt_pk_bf16_f32 v73, v74, v75\n"
      "v_mfma_f32_16x16x32_bf16 v[104:107], v[160:163], a[8:11], 0\n"
      "ds_write_addtid_b32 v72 offset:6144\n"
      "v_cvt_pk_bf16_f32 v74, v76, v77\n"
      "v_mfma_f32_16x16x32_bf16 v[108:111], v[160:163], a[12:15], 0\n"
      "ds_write_addtid_b32 v73 offset:6400\n"
      "v_cvt_pk_bf16_f32 v75, v78, v79\n"
      "v_mfma_f32_16x16x32_bf16 v[112:115], v[160:163], a[16:19], 0\n"
      "ds_write_addtid_b32 v74 offset:6656\n"
      "v_cvt_pk_bf16_f32 v76, v80, v81\n"
      "v_mfma_f32_16x16x32_bf16 v[116:119], v[160:163], a[20:23], 0\n"
      "ds_write_addtid_b32 v75 offset:6912\n"
      "v_cvt_pk_bf16_f32 v77, v82, v83\n"
      "v_mfma_f32_16x16x32_bf16 v[120:123], v[160:163], a[24:27], 0\n"
      "ds_write_addtid_b32 v76 offset:7168\n"
      "v_cvt_pk_bf16_f32 v78, v84, v85\n"
      "v_mfma_f32_16x16x32_bf16 v[124:127], v[160:163], a[28:31], 0\n"
      "ds_write_addtid_b32 v77 offset:7424\n"
      "v_cvt_pk_bf16_f32 v79, v86, v87\n"
      "v_mfma_f32_16x16x32_bf16 v[104:107], v[164:167], a[40:43], v[104:107]\n"
      "ds_write_addtid_b32 v78 offset:7680\n"
      "v_cvt_pk_bf16_f32 v80, v88, v89\n"
      "v_mfma_f32_16x16x32_bf16 v[108:111], v[164:167], a[44:47], v[108:111]\n"
      "ds_write_addtid_b32 v79 offset:7936\n"
      "v_cvt_pk_bf16_f32 v81, v90, v91\n"
      "v_mfma_f32_16x16x32_bf16 v[112:115], v[164:167], a[48:51], v[112:115]\n"
      "ds_write_addtid_b32 v80 offset:8192\n"
      "v_cvt_pk_bf16_f32 v82, v92, v93\n"
      "v_mfma_f32_16x16x32_bf16 v[116:119], v[164:167], a[52:55], v[116:119]\n"
      "ds_write_addtid_b32 v81 offset:8448\n"
      "v_cvt_pk_bf16_f32 v83, v94, v95\n"
      "v_mfma_f32_16x16x32_bf16 v[120:123], v[164:167], a[56:59], v[120:123]\n"
      "ds_write_addtid_b32 v82 offset:8704\n"
      "v_mfma_f32_16x16x32_bf16 v[124:127], v[164:167], a[60:63], v[124:127]\n"
      "s_waitcnt vmcnt(13)\n"
      "ds_write_addtid_b32 v83 offset:8960\n"
      "v_mfma_f32_16x16x32_bf16 v[104:107], v[168:171], a[72:75], v[104:107]\n"
      "v_mfma_f32_16x16x32_bf16 v[108:111], v[168:171], a[76:79], v[108:111]\n"
      "v_mfma_f32_16x16x32_bf16 v[112:115], v[168:171], a[80:83], v[112:115]\n"
      "v_mfma_f32_16x16x32_bf16 v[116:119], v[168:171], a[84:87], v[116:119]\n"
      "v_mfma_f32_16x16x32_bf16 v[120:123], v[168:171], a[88:91], v[120:123]\n"
      "v_mfma_f32_16x16x32_bf16 v[124:127], v[168:171], a[92:95], v[124:127]\n"
      "v_mfma_f32_16x16x32_bf16 v[104:107], v[172:175], a[104:107], v[104:107]\n"
      "v_mfma_f32_16x16x32_bf16 v[108:111], v[172:175], a[108:111], v[108:111]\n"
      "v_mfma_f32_16x16x32_bf16 v[112:115], v[172:175], a[112:115], v[112:115]\n"
      "v_mfma_f32_16x16x32_bf16 v[116:119], v[172:175], a[116:119], v[116:119]\n"
      "v_mfma_f32_16x16x32_bf16 v[120:123], v[172:175], a[120:123], v[120:123]\n"
      "v_mfma_f32_16x16x32_bf16 v[124:127], v[172:175], a[124:127], v[124:127]\n"
      "s_waitcnt vmcnt(9)\n"
      "v_mfma_f32_16x16x32_bf16 v[104:107], v[176:179], a[136:139], v[104:107]\n"
      "v_mfma_f32_16x16x32_bf16 v[108:111], v[176:179], a[140:143], v[108:111]\n"
      "v_mfma_f32_16x16x32_bf16 v[112:115], v[176:179], a[144:147], v[112:115]\n"
      "v_mfma_f32_16x16x32_bf16 v[116:119], v[176:179], a[148:151], v[116:119]\n"
      "v_mfma_f32_16x16x32_bf16 v[120:123], v[176:179], a[152:155], v[120:123]\n"
      "s_lshr_b32 s43, s42, 7\n"
      "s_cmp_eq_u32 s43, s36\n"
      "s_cbranch_scc0 .Lp_e16_%=\n"
      "s_lshl2_add_u32 s44, s35, s34\n"
      "s_lshl2_add_u32 s43, s44, s33\n"
      "s_lshl_b32 s43, s43, 12\n"
      "s_add_u32 s43, s43, 524288\n"
      "v_add_u32 v60, s43, v242\n"
      "v_mov_b32 v62, 0\n"
      "global_load_dwordx4 v[24:27], v60, s[58:59] offset:0 sc1\n"
      "global_load_dwordx4 v[28:31], v60, s[58:59] offset:1024 sc1\n"
      "global_load_dwordx4 v[32:35], v60, s[58:59] offset:2048 sc1\n"
      "global_load_dwordx4 v[36:39], v60, s[58:59] offset:3072 sc1\n"
      "global_load_dwordx4 v[40:43], v62, s[58:59] offset:1024 sc1\n"
      "v_and_b32 v58, 15, v243\n"
      "v_lshrrev_b32 v59, 5, v243\n"
      "v_lshl_or_b32 v58, v59, 4, v58\n"
      "s_lshl4_add_u32 s44, s42, s44\n"
      "s_lshl_b32 s44, s44, 6\n"
      "v_lshl_add_u32 v59, v58, 1, s44\n"
      "global_load_ushort v57, v59, s[16:17]\n"
      "s_mov_b32 s44, 131072\n"
      "v_lshl_add_u32 v58, v58, 2, s44\n"
      "s_lshl_b32 s43, s33, 13\n"
      "v_add_u32 v59, s43, v58\n"
      ".Lp_e16_%=:\n"
      "v_mfma_f32_16x16x32_bf16 v[124:127], v[176:179], a[156:159], v[124:127]\n"
      "v_mfma_f32_16x16x32_bf16 v[104:107], v[180:183], a[168:171], v[104:107]\n"
      "v_mfma_f32_16x16x32_bf16 v[108:111], v[180:183], a[172:175], v[108:111]\n"
      "v_mfma_f32_16x16x32_bf16 v[112:115], v[180:183], a[176:179], v[112:115]\n"
      "v_mfma_f32_16x16x32_bf16 v[116:119], v[180:183], a[180:183], v[116:119]\n"
      "v_add_u32 v5, 12288, v242\n"
      "v_mfma_f32_16x16x32_bf16 v[120:123], v[180:183], a[184:187], v[120:123]\n"
      "global_load_dwordx4 v[44:47], v5, s[62:63] offset:0 sc1\n"
      "v_mfma_f32_16x16x32_bf16 v[124:127], v[180:183], a[188:191], v[124:127]\n"
      "s_waitcnt vmcnt(7)\n"
      "global_load_dwordx4 v[48:51], v5, s[62:63] offset:1024 sc1\n"
      "v_mfma_f32_16x16x32_bf16 v[104:107], v[184:187], a[200:203], v[104:107]\n"
      "global_load_dwordx4 v[52:55], v5, s[62:63] offset:2048 sc1\n"
      "v_mfma_f32_16x16x32_bf16 v[108:111], v[184:187], a[204:207], v[108:111]\n"
      "v_mfma_f32_16x16x32_bf16 v[112:115], v[184:187], a[208:211], v[112:115]\n"
      "v_mfma_f32_16x16x32_bf16 v[116:119], v[184:187], a[212:215], v[116:119]\n"
      "v_mfma_f32_16x16x32_bf16 v[120:123], v[184:187], a[216:219], v[120:123]\n"
      "v_mfma_f32_16x16x32_bf16 v[124:127], v[184:187], a[220:223], v[124:127]\n"
      "v_mfma_f32_16x16x32_bf16 v[104:107], v[188:191], a[232:235], v[104:107]\n"
      "v_mfma_f32_16x16x32_bf16 v[108:111], v[188:191], a[236:239], v[108:111]\n"
      "v_mfma_f32_16x16x32_bf16 v[112:115], v[188:191], a[240:243], v[112:115]\n"
      "v_mfma_f32_16x16x32_bf16 v[116:119], v[188:191], a[244:247], v[116:119]\n"
      "s_waitcnt vmcnt(5)\n"
      "v_bitop3_b32 v22, v15, v19, s37 bitop3:0x7e\n"
      "v_bitop3_b32 v22, v22, v23, s37 bitop3:0xf6\n"
      "v_cmp_ne_u32 vcc, 0, v22\n"
      "s_nop 1\n"
      "s_cbranch_vccnz .Lp_s18_%=\n"
      ".Lp_o19_%=:\n"
      "v_mfma_f32_16x16x32_bf16 v[120:123], v[188:191], a[248:251], v[120:123]\n"
      "v_cvt_pk_bf16_f32 v104, v104, v105\n"
      "v_cvt_pk_bf16_f32 v105, v106, v107\n"
      "v_mfma_f32_16x16x32_bf16 v[124:127], v[188:191], a[252:255], v[124:127]\n"
      "ds_write_addtid_b32 v104 offset:9216\n"
      "v_cvt_pk_bf16_f32 v106, v108, v109\n"
      "v_mfma_f32_16x16x32_bf16 v[64:67], v[128:131], a[0:3], 0\n"
      "ds_write_addtid_b32 v105 offset:9472\n"
      "v_cvt_pk_bf16_f32 v107, v110, v111\n"
      "v_mfma_f32_16x16x32_bf16 v[64:67], v[132:135], a[32:35], v[64:67]\n"
      "ds_write_addtid_b32 v106 offset:9728\n"
      "v_cvt_pk_bf16_f32 v108, v112, v113\n"
      "v_mfma_f32_16x16x32_bf16 v[64:67], v[136:139], a[64:67], v[64:67]\n"
      "ds_write_addtid_b32 v107 offset:9984\n"
      "v_cvt_pk_bf16_f32 v109, v114, v115\n"
      "v_mfma_f32_16x16x32_bf16 v[64:67], v[140:143], a[96:99], v[64:67]\n"
      "ds_write_addtid_b32 v108 offset:10240\n"
      "v_cvt_pk_bf16_f32 v110, v116, v117\n"
      "v_mfma_f32_16x16x32_bf16 v[64:67], v[144:147], a[128:131], v[64:67]\n"
      "ds_write_addtid_b32 v109 offset:10496\n"
      "v_cvt_pk_bf16_f32 v111, v118, v119\n"
      "v_mfma_f32_16x16x32_bf16 v[64:67], v[148:151], a[160:163], v[64:67]\n"
      "ds_write_addtid_b32 v110 offset:10752\n"
      "v_cvt_pk_bf16_f32 v112, v120, v121\n"
      "v_mfma_f32_16x16x32_bf16 v[64:67], v[152:155], a[192:195], v[64:67]\n"
      "ds_write_addtid_b32 v111 offset:11008\n"
      "v_cvt_pk_bf16_f32 v113, v122, v123\n"
      "v_mfma_f32_16x16x32_bf16 v[64:67], v[156:159], a[224:227], v[64:67]\n"
      "ds_write_addtid_b32 v112 offset:11264\n"
      "v_cvt_pk_bf16_f32 v114, v124, v125\n"
      "v_mfma_f32_16x16x32_bf16 v[68:71], v[128:131], a[4:7], 0\n"
      "ds_write_addtid_b32 v113 offset:11520\n"
      "v_cvt_pk_bf16_f32 v115, v126, v127\n"
      "v_mfma_f32_16x16x32_bf16 v[68:71], v[132:135], a[36:39], v[68:71]\n"
      "ds_write_addtid_b32 v114 offset:11776\n"
      "v_mov_b32 v15, v20\n"
      "v_mfma_f32_16x16x32_bf16 v[68:71], v[136:139], a[68:71], v[68:71]\n"
      "ds_write_addtid_b32 v115 offset:12032\n"
      "v_add_u32 v6, 24576, v242\n"
      "v_mfma_f32_16x16x32_bf16 v[68:71], v[140:143], a[100:103], v[68:71]\n"
      "global_load_dwordx4 v[72:75], v6, s[62:63] offset:0 sc1\n"
      "v_mov_b32 v19, v21\n"
      "v_mfma_f32_16x16x32_bf16 v[68:71], v[144:147], a[132:135], v[68:71]\n"
      "global_load_dwordx4 v[76:79], v6, s[62:63] offset:1024 sc1\n"
      "v_mfma_f32_16x16x32_bf16 v[68:71], v[148:151], a[164:167], v[68:71]\n"
      "global_load_dwordx4 v[80:83], v6, s[62:63] offset:2048 sc1\n"
      "v_mfma_f32_16x16x32_bf16 v[68:71], v[152:155], a[196:199], v[68:71]\n"
      "s_lshr_b32 s43, s42, 7\n"
      "s_cmp_eq_u32 s43, s36\n"
      "s_cbranch_scc0 .Lp_e20_%=\n"
      "s_waitcnt vmcnt(6)\n"
      "v_bitop3_b32 v61, v25, v27, s37 bitop3:0x7e\n"
      "v_bitop3_b32 v61, v61, v29, s37 bitop3:0xf6\n"
      "v_bitop3_b32 v61, v61, v31, s37 bitop3:0xf6\n"
      "v_bitop3_b32 v61, v61, v33, s37 bitop3:0xf6\n"
      "v_bitop3_b32 v61, v61, v35, s37 bitop3:0xf6\n"
      "v_bitop3_b32 v61, v61, v37, s37 bitop3:0xf6\n"
      "v_bitop3_b32 v61, v61, v39, s37 bitop3:0xf6\n"
      "v_bitop3_b32 v61, v61, v43, s37 bitop3:0xf6\n"
      "v_cmp_ne_u32 vcc, 0, v61\n"
      "s_nop 1\n"
      "s_cbranch_vccnz .Lp_s22_%=\n"
      ".Lp_o23_%=:\n"
      "v_add_f32 v24, v24, v28\n"
      "v_add_f32 v26, v26, v30\n"
      "v_add_f32 v32, v32, v36\n"
      "v_add_f32 v34, v34, v38\n"
      "v_add_f32 v24, v24, v32\n"
      "v_add_f32 v26, v26, v34\n"
      "s_nop 1\n"
      "v_permlane32_swap_b32 v24, v26\n"
      "v_add_f32 v24, v24, v26\n"
      "v_mov_b32 v26, v24\n"
      "s_nop 1\n"
      "v_permlane16_swap_b32 v24, v26\n"
      "v_add_f32 v24, v24, v26\n"
      "ds_write_b32 v59, v24\n"
      "s_waitcnt lgkmcnt(0)\n"
      ".Lp_e20_%=:\n"
      "v_mfma_f32_16x16x32_bf16 v[68:71], v[156:159], a[228:231], v[68:71]\n"
      "s_waitcnt lgkmcnt(0)\n"
      "s_barrier\n"
      "v_mfma_f32_16x16x32_bf16 v[96:99], v[160:163], a[0:3], 0\n"
      "s_lshr_b32 s43, s42, 5\n"
      "s_lshl2_add_u32 s44, s36, s33\n"
      "s_cmp_eq_u32 s43, s44\n"
      "s_cbranch_scc0 .Lp_e24_%=\n"
      "ds_read_b32 v24, v58 offset:0\n"
      "ds_read_b32 v25, v58 offset:8192\n"
      "ds_read_b32 v26, v58 offset:16384\n"
      "ds_read_b32 v27, v58 offset:24576\n"
      "v_mov_b32 v63, s37\n"
      "s_or_b32 s43, s34, s35\n"
      "s_cmp_eq_u32 s43, 0\n"
      "s_cbranch_scc0 .Lp_z25_%=\n"
      "s_mov_b64 exec, 1\n"
      "global_store_dword v62, v63, s[58:59] sc1\n"
      ".Lp_z25_%=:\n"
      "s_mov_b64 exec, -1\n"
      "s_waitcnt vmcnt(6)\n"
      "v_lshlrev_b32 v57, 16, v57\n"
      "v_and_b32 v56, 48, v243\n"
      "s_mov_b32 s44, 131072\n"
      "v_lshl_add_u32 v56, v56, 1, s44\n"
      "s_waitcnt lgkmcnt(0)\n"
      "v_add_f32 v24, v24, v25\n"
      "s_waitcnt lgkmcnt(0)\n"
      "v_add_f32 v26, v26, v27\n"
      "v_add_f32 v24, v24, v26\n"
      "v_cvt_pk_bf16_f32 v24, v24, v24\n"
      "v_and_b32 v24, 0xffff0000, v24\n"
      "v_mul_f32 v24, v41, v24\n"
      "v_cvt_pk_bf16_f32 v24, v24, v24\n"
      "v_and_b32 v24, 0xffff0000, v24\n"
      "v_add_f32 v24, v24, v57\n"
      "v_cvt_pk_bf16_f32 v24, v24, v24\n"
      "v_and_b32 v24, 0xffff0000, v24\n"
      "v_sub_f32 v42, 1.0, v40\n"
      "v_cvt_pk_bf16_f32 v42, v42, v42\n"
      "v_and_b32 v42, 0xffff0000, v42\n"
      "v_mul_f32 v24, v42, v24\n"
      "ds_write_b32 v58, v24 offset:1024\n"
      "ds_read_b128 v[28:31], v56 offset:1024\n"
      "ds_read_b128 v[32:35], v56 offset:1040\n"
      ".Lp_e24_%=:\n"
      "v_mfma_f32_16x16x32_bf16 v[96:99], v[164:167], a[32:35], v[96:99]\n"
      "ds_read2st64_b32 v[84:85], v244 offset0:24 offset1:25\n"
      "v_mfma_f32_16x16x32_bf16 v[96:99], v[168:171], a[64:67], v[96:99]\n"
      "ds_read2st64_b32 v[86:87], v245 offset0:28 offset1:29\n"
      "v_mfma_f32_16x16x32_bf16 v[96:99], v[172:175], a[96:99], v[96:99]\n"
      "ds_read2st64_b32 v[88:89], v244 offset0:26 offset1:27\n"
      "v_mfma_f32_16x16x32_bf16 v[96:99], v[176:179], a[128:131], v[96:99]\n"
      "ds_read2st64_b32 v[90:91], v245 offset0:30 offset1:31\n"
      "v_mfma_f32_16x16x32_bf16 v[96:99], v[180:183], a[160:163], v[96:99]\n"
      "ds_read2st64_b32 v[92:93], v246 offset0:32 offset1:33\n"
      "v_mfma_f32_16x16x32_bf16 v[96:99], v[184:187], a[192:195], v[96:99]\n"
      "ds_read2st64_b32 v[94:95], v246 offset0:34 offset1:35\n"
      "v_mfma_f32_16x16x32_bf16 v[96:99], v[188:191], a[224:227], v[96:99]\n"
      "ds_read2st64_b32 v[116:117], v244 offset0:36 offset1:37\n"
      "v_mfma_f32_16x16x32_bf16 v[100:103], v[160:163], a[4:7], 0\n"
      "ds_read2st64_b32 v[118:119], v245 offset0:40 offset1:41\n"
      "v_mfma_f32_16x16x32_bf16 v[100:103], v[164:167], a[36:39], v[100:103]\n"
      "ds_read2st64_b32 v[120:121], v244 offset0:38 offset1:39\n"
      "v_mfma_f32_16x16x32_bf16 v[100:103], v[168:171], a[68:71], v[100:103]\n"
      "s_waitcnt lgkmcnt(3)\n"
      "v_mfma_f32_16x16x32_bf16 v[64:67], v[250:253], v[84:87], v[64:67]\n"
      "v_mfma_f32_16x16x32_bf16 v[100:103], v[172:175], a[100:103], v[100:103]\n"
      "v_mfma_f32_16x16x32_bf16 v[68:71], v[250:253], v[88:91], v[68:71]\n"
      "v_mfma_f32_16x16x32_bf16 v[100:103], v[176:179], a[132:135], v[100:103]\n"
      "v_mfma_f32_16x16x32_bf16 v[64:67], v[252:255], v[92:95], v[64:67]\n"
      "v_mfma_f32_16x16x32_bf16 v[100:103], v[180:183], a[164:167], v[100:103]\n"
      "v_mfma_f32_16x16x32_bf16 v[68:71], v[248:251], v[92:95], v[68:71]\n"
      "v_mfma_f32_16x16x32_bf16 v[100:103], v[184:187], a[196:199], v[100:103]\n"
      "ds_read2st64_b32 v[122:123], v245 offset0:42 offset1:43\n"
      "v_mfma_f32_16x16x32_bf16 v[100:103], v[188:191], a[228:231], v[100:103]\n"
      "ds_read2st64_b32 v[124:125], v246 offset0:44 offset1:45\n"
      "ds_read2st64_b32 v[126:127], v246 offset0:46 offset1:47\n"
      "v_mfma_f32_16x16x32_bf16 v[64:67], v[252:255], v[12:15], v[64:67]\n"
      "v_mfma_f32_16x16x32_bf16 v[68:71], v[248:251], v[12:15], v[68:71]\n"
      "v_mfma_f32_16x16x32_bf16 v[96:99], v[252:255], v[16:19], v[96:99]\n"
      "v_mfma_f32_16x16x32_bf16 v[100:103], v[248:251], v[16:19], v[100:103]\n"
      "s_waitcnt vmcnt(3)\n"
      "v_bitop3_b32 v54, v47, v51, s37 bitop3:0x7e\n"
      "v_bitop3_b32 v54, v54, v55, s37 bitop3:0xf6\n"
      "v_cmp_ne_u32 vcc, 0, v54\n"
      "s_nop 1\n"
      "s_cbranch_vccnz .Lp_s27_%=\n"
      ".Lp_o28_%=:\n"
      "v_mov_b32 v47, v52\n"
      "v_mov_b32 v51, v53\n"
      "s_nop 0\n"
      "v_mfma_f32_16x16x32_bf16 v[64:67], v[252:255], v[44:47], v[64:67]\n"
      "v_mfma_f32_16x16x32_bf16 v[68:71], v[248:251], v[44:47], v[68:71]\n"
      "v_mfma_f32_16x16x32_bf16 v[96:99], v[252:255], v[48:51], v[96:99]\n"
      "v_mfma_f32_16x16x32_bf16 v[100:103], v[248:251], v[48:51], v[100:103]\n"
      "s_waitcnt lgkmcnt(0)\n"
      "v_mfma_f32_16x16x32_bf16 v[96:99], v[250:253], v[116:119], v[96:99]\n"
      "v_mfma_f32_16x16x32_bf16 v[100:103], v[250:253], v[120:123], v[100:103]\n"
      "v_mfma_f32_16x16x32_bf16 v[96:99], v[252:255], v[124:127], v[96:99]\n"
      "v_mfma_f32_16x16x32_bf16 v[100:103], v[248:251], v[124:127], v[100:103]\n"
      "s_lshr_b32 s43, s42, 5\n"
      "s_lshl2_add_u32 s44, s36, s33\n"
      "s_cmp_eq_u32 s43, s44\n"
      "s_cbranch_scc0 .Lp_e29_%=\n"
      "v_lshrrev_b32 v24, 10, v7\n"
      "v_subrev_u32 v24, s42, v24\n"
      "v_cmp_eq_u32 vcc, 0, v24\n"
      "s_nop 1\n"
      "s_mov_b64 exec, vcc\n"
      "v_mov_b32 v204, v40\n"
      "v_mov_b32 v208, v28\n"
      "v_mov_b32 v209, v29\n"
      "v_mov_b32 v210, v30\n"
      "v_mov_b32 v211, v31\n"
      "v_mov_b32 v216, v32\n"
      "v_mov_b32 v217, v33\n"
      "v_mov_b32 v218, v34\n"
      "v_mov_b32 v219, v35\n"
      "s_mov_b64 exec, -1\n"
      "v_cmp_eq_u32 vcc, -16, v24\n"
      "s_nop 1\n"
      "s_mov_b64 exec, vcc\n"
      "v_mov_b32 v206, v40\n"
      "v_mov_b32 v212, v28\n"
      "v_mov_b32 v213, v29\n"
      "v_mov_b32 v214, v30\n"
      "v_mov_b32 v215, v31\n"
      "v_mov_b32 v220, v32\n"
      "v_mov_b32 v221, v33\n"
      "v_mov_b32 v222, v34\n"
      "v_mov_b32 v223, v35\n"
      "s_mov_b64 exec, -1\n"
      ".Lp_e29_%=:\n"
      "s_waitcnt vmcnt(0)\n"
      "v_bitop3_b32 v82, v75, v79, s37 bitop3:0x7e\n"
      "v_bitop3_b32 v82, v82, v83, s37 bitop3:0xf6\n"
      "v_cmp_ne_u32 vcc, 0, v82\n"
      "s_nop 1\n"
      "s_cbranch_vccnz .Lp_s31_%=\n"
      ".Lp_o32_%=:\n"
      "v_mov_b32 v75, v80\n"
      "v_mov_b32 v79, v81\n"
      "s_nop 0\n"
      "v_mfma_f32_16x16x32_bf16 v[64:67], v[252:255], v[72:75], v[64:67]\n"
      "v_mfma_f32_16x16x32_bf16 v[96:99], v[252:255], v[76:79], v[96:99]\n"
      "v_mfma_f32_16x16x32_bf16 v[68:71], v[248:251], v[72:75], v[68:71]\n"
      "v_mfma_f32_16x16x32_bf16 v[100:103], v[248:251], v[76:79], v[100:103]\n"
      "s_nop 4\n"
      "v_fma_f32 v208, v204, v64, v208\n"
      "v_fma_f32 v209, v204, v65, v209\n"
      "v_fma_f32 v210, v204, v66, v210\n"
      "v_fma_f32 v211, v204, v67, v211\n"
      "v_fma_f32 v216, v204, v96, v216\n"
      "v_fma_f32 v217, v204, v97, v217\n"
      "v_fma_f32 v218, v204, v98, v218\n"
      "v_fma_f32 v219, v204, v99, v219\n"
      "v_cvt_pk_bf16_f32 v104, v208, v209\n"
      "v_cvt_pk_bf16_f32 v105, v210, v211\n"
      "v_cvt_pk_bf16_f32 v106, v216, v217\n"
      "v_cvt_pk_bf16_f32 v107, v218, v219\n"
      "global_store_dwordx4 v7, v[104:107], s[20:21]\n"
      "v_fma_f32 v212, v206, v68, v212\n"
      "v_fma_f32 v213, v206, v69, v213\n"
      "v_fma_f32 v214, v206, v70, v214\n"
      "v_fma_f32 v215, v206, v71, v215\n"
      "v_fma_f32 v220, v206, v100, v220\n"
      "v_fma_f32 v221, v206, v101, v221\n"
      "v_fma_f32 v222, v206, v102, v222\n"
      "v_fma_f32 v223, v206, v103, v223\n"
      "v_cvt_pk_bf16_f32 v108, v212, v213\n"
      "v_cvt_pk_bf16_f32 v109, v214, v215\n"
      "v_cvt_pk_bf16_f32 v110, v220, v221\n"
      "v_cvt_pk_bf16_f32 v111, v222, v223\n"
      "global_store_dwordx4 v7, v[108:111], s[68:69]\n"
      "s_endpgm\n"
      ".Lp_s2_%=:\n"
      "s_cmp_eq_u32 s38, 0\n"
      "s_cbranch_scc1 .Lp_o3_%=\n"
      "s_sub_u32 s38, s38, 1\n"
      "s_sleep 4\n"
      "s_nop 7\n"
      "global_load_dword v252, v253, s[58:59] offset:0 sc1\n"
      "s_waitcnt vmcnt(0)\n"
      "v_cmp_ne_u32 vcc, s37, v252\n"
      "s_nop 1\n"
      "s_cbranch_vccnz .Lp_s2_%=\n"
      "s_nop 7\n"
      "s_branch .Lp_o3_%=\n"
      ".Lp_s5_%=:\n"
      "s_cmp_eq_u32 s38, 0\n"
      "s_cbranch_scc1 .Lp_o6_%=\n"
      "s_sub_u32 s38, s38, 1\n"
      "s_sleep 4\n"
      "s_nop 7\n"
      "global_load_dword v252, v253, s[58:59] offset:128 sc1\n"
      "s_waitcnt vmcnt(0)\n"
      "v_cmp_ne_u32 vcc, s37, v252\n"
      "s_nop 1\n"
      "s_cbranch_vccnz .Lp_s5_%=\n"
      "s_nop 7\n"
      "s_branch .Lp_o6_%=\n"
      ".Lp_s8_%=:\n"
      "s_cmp_eq_u32 s38, 0\n"
      "s_cbranch_scc1 .Lp_o9_%=\n"
      "s_sub_u32 s38, s38, 1\n"
      "s_sleep 2\n"
      "s_nop 7\n"
      "global_load_dwordx2 v[234:235], v232, s[58:59] sc1\n"
      "s_waitcnt vmcnt(0)\n"
      "v_cmp_ne_u32 vcc, s37, v235\n"
      "s_nop 1\n"
      "s_cbranch_vccnz .Lp_s8_%=\n"
      "s_nop 7\n"
      "s_branch .Lp_o9_%=\n"
      ".Lp_s11_%=:\n"
      "s_cmp_eq_u32 s38, 0\n"
      "s_cbranch_scc1 .Lp_o12_%=\n"
      "s_sub_u32 s38, s38, 1\n"
      "s_sleep 1\n"
      "s_nop 7\n"
      "global_load_dwordx2 v[204:205], v209, s[58:59] offset:0 sc1\n"
      "global_load_dwordx2 v[206:207], v209, s[58:59] offset:256 sc1\n"
      "s_waitcnt vmcnt(0)\n"
      "v_bitop3_b32 v210, v205, v207, s37 bitop3:0x7e\n"
      "v_cmp_ne_u32 vcc, 0, v210\n"
      "s_nop 1\n"
      "s_cbranch_vccnz .Lp_s11_%=\n"
      "s_nop 7\n"
      "s_branch .Lp_o12_%=\n"
      ".Lp_s14_%=:\n"
      "s_cmp_eq_u32 s38, 0\n"
      "s_cbranch_scc1 .Lp_o15_%=\n"
      "s_sub_u32 s38, s38, 1\n"
      "s_sleep 8\n"
      "s_nop 7\n"
      "global_load_dwordx4 v[224:227], v200, s[58:59] offset:0 sc1\n"
      "global_load_dwordx4 v[228:231], v200, s[58:59] offset:1024 sc1\n"
      "global_load_dwordx4 v[232:235], v200, s[58:59] offset:2048 sc1\n"
      "global_load_dwordx4 v[236:239], v200, s[58:59] offset:3072 sc1\n"
      "s_waitcnt vmcnt(0)\n"
      "v_bitop3_b32 v192, v225, v227, s37 bitop3:0x7e\n"
      "v_bitop3_b32 v192, v192, v229, s37 bitop3:0xf6\n"
      "v_bitop3_b32 v192, v192, v231, s37 bitop3:0xf6\n"
      "v_bitop3_b32 v192, v192, v233, s37 bitop3:0xf6\n"
      "v_bitop3_b32 v192, v192, v235, s37 bitop3:0xf6\n"
      "v_bitop3_b32 v192, v192, v237, s37 bitop3:0xf6\n"
      "v_bitop3_b32 v192, v192, v239, s37 bitop3:0xf6\n"
      "v_cmp_ne_u32 vcc, 0, v192\n"
      "s_nop 1\n"
      "s_cbranch_vccnz .Lp_s14_%=\n"
      "s_nop 7\n"
      "s_branch .Lp_o15_%=\n"
      ".Lp_s18_%=:\n"
      "s_cmp_eq_u32 s38, 0\n"
      "s_cbranch_scc1 .Lp_o19_%=\n"
      "s_sub_u32 s38, s38, 1\n"
      "s_nop 7\n"
      "global_load_dwordx4 v[12:15], v4, s[62:63] offset:0 sc1\n"
      "global_load_dwordx4 v[16:19], v4, s[62:63] offset:1024 sc1\n"
      "global_load_dwordx4 v[20:23], v4, s[62:63] offset:2048 sc1\n"
      "s_waitcnt vmcnt(0)\n"
      "v_bitop3_b32 v22, v15, v19, s37 bitop3:0x7e\n"
      "v_bitop3_b32 v22, v22, v23, s37 bitop3:0xf6\n"
      "v_cmp_ne_u32 vcc, 0, v22\n"
      "s_nop 1\n"
      "s_cbranch_vccnz .Lp_s18_%=\n"
      "s_nop 7\n"
      "s_branch .Lp_o19_%=\n"
      ".Lp_s22_%=:\n"
      "s_cmp_eq_u32 s38, 0\n"
      "s_cbranch_scc1 .Lp_o23_%=\n"
      "s_sub_u32 s38, s38, 1\n"
      "s_sleep 2\n"
      "s_nop 7\n"
      "global_load_dwordx4 v[24:27], v60, s[58:59] offset:0 sc1\n"
      "global_load_dwordx4 v[28:31], v60, s[58:59] offset:1024 sc1\n"
      "global_load_dwordx4 v[32:35], v60, s[58:59] offset:2048 sc1\n"
      "global_load_dwordx4 v[36:39], v60, s[58:59] offset:3072 sc1\n"
      "global_load_dwordx4 v[40:43], v62, s[58:59] offset:1024 sc1\n"
      "s_waitcnt vmcnt(0)\n"
      "v_bitop3_b32 v61, v25, v27, s37 bitop3:0x7e\n"
      "v_bitop3_b32 v61, v61, v29, s37 bitop3:0xf6\n"
      "v_bitop3_b32 v61, v61, v31, s37 bitop3:0xf6\n"
      "v_bitop3_b32 v61, v61, v33, s37 bitop3:0xf6\n"
      "v_bitop3_b32 v61, v61, v35, s37 bitop3:0xf6\n"
      "v_bitop3_b32 v61, v61, v37, s37 bitop3:0xf6\n"
      "v_bitop3_b32 v61, v61, v39, s37 bitop3:0xf6\n"
      "v_bitop3_b32 v61, v61, v43, s37 bitop3:0xf6\n"
      "v_cmp_ne_u32 vcc, 0, v61\n"
      "s_nop 1\n"
      "s_cbranch_vccnz .Lp_s22_%=\n"
      "s_nop 7\n"
      "s_branch .Lp_o23_%=\n"
      ".Lp_s27_%=:\n"
      "s_cmp_eq_u32 s38, 0\n"
      "s_cbranch_scc1 .Lp_o28_%=\n"
      "s_sub_u32 s38, s38, 1\n"
      "s_nop 7\n"
      "global_load_dwordx4 v[44:47], v5, s[62:63] offset:0 sc1\n"
      "global_load_dwordx4 v[48:51], v5, s[62:63] offset:1024 sc1\n"
      "global_load_dwordx4 v[52:55], v5, s[62:63] offset:2048 sc1\n"
      "s_waitcnt vmcnt(0)\n"
      "v_bitop3_b32 v54, v47, v51, s37 bitop3:0x7e\n"
      "v_bitop3_b32 v54, v54, v55, s37 bitop3:0xf6\n"
      "v_cmp_ne_u32 vcc, 0, v54\n"
      "s_nop 1\n"
      "s_cbranch_vccnz .Lp_s27_%=\n"
      "s_nop 7\n"
      "s_branch .Lp_o28_%=\n"
      ".Lp_s31_%=:\n"
      "s_cmp_eq_u32 s38, 0\n"
      "s_cbranch_scc1 .Lp_o32_%=\n"
      "s_sub_u32 s38, s38, 1\n"
      "s_nop 7\n"
      "global_load_dwordx4 v[72:75], v6, s[62:63] offset:0 sc1\n"
      "global_load_dwordx4 v[76:79], v6, s[62:63] offset:1024 sc1\n"
      "global_load_dwordx4 v[80:83], v6, s[62:63] offset:2048 sc1\n"
      "s_waitcnt vmcnt(0)\n"
      "v_bitop3_b32 v82, v75, v79, s37 bitop3:0x7e\n"
      "v_bitop3_b32 v82, v82, v83, s37 bitop3:0xf6\n"
      "v_cmp_ne_u32 vcc, 0, v82\n"
      "s_nop 1\n"
      "s_cbranch_vccnz .Lp_s31_%=\n"
      "s_nop 7\n"
      "s_branch .Lp_o32_%=\n"
      ".Ls_s3_%=:\n"
      "s_cmp_eq_u32 s38, 0\n"
      "s_cbranch_scc1 .Ls_o4_%=\n"
      "s_sub_u32 s38, s38, 1\n"
      "s_sleep 4\n"
      "s_nop 7\n"
      "global_load_dword v252, v253, s[58:59] offset:0 sc1\n"
      "s_waitcnt vmcnt(0)\n"
      "v_cmp_ne_u32 vcc, s37, v252\n"
      "s_nop 1\n"
      "s_cbranch_vccnz .Ls_s3_%=\n"
      "s_nop 7\n"
      "s_branch .Ls_o4_%=\n"
      ".Ls_s6_%=:\n"
      "s_cmp_eq_u32 s38, 0\n"
      "s_cbranch_scc1 .Ls_o7_%=\n"
      "s_sub_u32 s38, s38, 1\n"
      "s_sleep 2\n"
      "s_nop 7\n"
      "global_load_dwordx2 v[206:207], v204, s[58:59] sc1\n"
      "s_waitcnt vmcnt(0)\n"
      "v_cmp_ne_u32 vcc, s37, v207\n"
      "s_nop 1\n"
      "s_cbranch_vccnz .Ls_s6_%=\n"
      "s_nop 7\n"
      "s_branch .Ls_o7_%=\n"
      ".Ls_s12_%=:\n"
      "s_cmp_eq_u32 s38, 0\n"
      "s_cbranch_scc1 .Ls_o13_%=\n"
      "s_sub_u32 s38, s38, 1\n"
      "s_sleep 4\n"
      "s_nop 7\n"
      "global_load_dword v252, v253, s[58:59] offset:128 sc1\n"
      "s_waitcnt vmcnt(0)\n"
      "v_cmp_ne_u32 vcc, s37, v252\n"
      "s_nop 1\n"
      "s_cbranch_vccnz .Ls_s12_%=\n"
      "s_nop 7\n"
      "s_branch .Ls_o13_%=\n"
      ".Ls_s15_%=:\n"
      "s_cmp_eq_u32 s38, 0\n"
      "s_cbranch_scc1 .Ls_o16_%=\n"
      "s_sub_u32 s38, s38, 1\n"
      "s_sleep 1\n"
      "s_nop 7\n"
      "global_load_dwordx2 v[204:205], v209, s[58:59] offset:0 sc1\n"
      "global_load_dwordx2 v[206:207], v209, s[58:59] offset:256 sc1\n"
      "s_waitcnt vmcnt(0)\n"
      "v_bitop3_b32 v210, v205, v207, s37 bitop3:0x7e\n"
      "v_cmp_ne_u32 vcc, 0, v210\n"
      "s_nop 1\n"
      "s_cbranch_vccnz .Ls_s15_%=\n"
      "s_nop 7\n"
      "s_branch .Ls_o16_%=\n"
      ".Ls_s18_%=:\n"
      "s_cmp_eq_u32 s38, 0\n"
      "s_cbranch_scc1 .Ls_o19_%=\n"
      "s_sub_u32 s38, s38, 1\n"
      "s_nop 7\n"
      "global_load_dwordx4 v[12:15], v4, s[62:63] offset:0 sc1\n"
      "global_load_dwordx4 v[16:19], v4, s[62:63] offset:1024 sc1\n"
      "global_load_dwordx4 v[20:23], v4, s[62:63] offset:2048 sc1\n"
      "s_waitcnt vmcnt(0)\n"
      "v_bitop3_b32 v22, v15, v19, s37 bitop3:0x7e\n"
      "v_bitop3_b32 v22, v22, v23, s37 bitop3:0xf6\n"
      "v_cmp_ne_u32 vcc, 0, v22\n"
      "s_nop 1\n"
      "s_cbranch_vccnz .Ls_s18_%=\n"
      "s_nop 7\n"
      "s_branch .Ls_o19_%=\n"
      ".Ls_s23_%=:\n"
      "s_cmp_eq_u32 s38, 0\n"
      "s_cbranch_scc1 .Ls_o24_%=\n"
      "s_sub_u32 s38, s38, 1\n"
      "s_sleep 2\n"
      "s_nop 7\n"
      "global_load_dwordx4 v[24:27], v60, s[58:59] offset:0 sc1\n"
      "global_load_dwordx4 v[28:31], v60, s[58:59] offset:1024 sc1\n"
      "global_load_dwordx4 v[32:35], v60, s[58:59] offset:2048 sc1\n"
      "global_load_dwordx4 v[36:39], v60, s[58:59] offset:3072 sc1\n"
      "global_load_dwordx4 v[40:43], v62, s[58:59] offset:1024 sc1\n"
      "s_waitcnt vmcnt(0)\n"
      "v_bitop3_b32 v61, v25, v27, s37 bitop3:0x7e\n"
      "v_bitop3_b32 v61, v61, v29, s37 bitop3:0xf6\n"
      "v_bitop3_b32 v61, v61, v31, s37 bitop3:0xf6\n"
      "v_bitop3_b32 v61, v61, v33, s37 bitop3:0xf6\n"
      "v_bitop3_b32 v61, v61, v35, s37 bitop3:0xf6\n"
      "v_bitop3_b32 v61, v61, v37, s37 bitop3:0xf6\n"
      "v_bitop3_b32 v61, v61, v39, s37 bitop3:0xf6\n"
      "v_bitop3_b32 v61, v61, v43, s37 bitop3:0xf6\n"
      "v_cmp_ne_u32 vcc, 0, v61\n"
      "s_nop 1\n"
      "s_cbranch_vccnz .Ls_s23_%=\n"
      "s_nop 7\n"
      "s_branch .Ls_o24_%=\n"
      ".Ls_s28_%=:\n"
      "s_cmp_eq_u32 s38, 0\n"
      "s_cbranch_scc1 .Ls_o29_%=\n"
      "s_sub_u32 s38, s38, 1\n"
      "s_nop 7\n"
      "global_load_dwordx4 v[44:47], v5, s[62:63] offset:0 sc1\n"
      "global_load_dwordx4 v[48:51], v5, s[62:63] offset:1024 sc1\n"
      "global_load_dwordx4 v[52:55], v5, s[62:63] offset:2048 sc1\n"
      "s_waitcnt vmcnt(0)\n"
      "v_bitop3_b32 v54, v47, v51, s37 bitop3:0x7e\n"
      "v_bitop3_b32 v54, v54, v55, s37 bitop3:0xf6\n"
      "v_cmp_ne_u32 vcc, 0, v54\n"
      "s_nop 1\n"
      "s_cbranch_vccnz .Ls_s28_%=\n"
      "s_nop 7\n"
      "s_branch .Ls_o29_%=\n"
      ".Ls_s32_%=:\n"
      "s_cmp_eq_u32 s38, 0\n"
      "s_cbranch_scc1 .Ls_o33_%=\n"
      "s_sub_u32 s38, s38, 1\n"
      "s_nop 7\n"
      "global_load_dwordx4 v[72:75], v6, s[62:63] offset:0 sc1\n"
      "global_load_dwordx4 v[76:79], v6, s[62:63] offset:1024 sc1\n"
      "global_load_dwordx4 v[80:83], v6, s[62:63] offset:2048 sc1\n"
      "s_waitcnt vmcnt(0)\n"
      "v_bitop3_b32 v82, v75, v79, s37 bitop3:0x7e\n"
      "v_bitop3_b32 v82, v82, v83, s37 bitop3:0xf6\n"
      "v_cmp_ne_u32 vcc, 0, v82\n"
      "s_nop 1\n"
      "s_cbranch_vccnz .Ls_s32_%=\n"
      "s_nop 7\n"
      "s_branch .Ls_o33_%=\n"
      ".Lend_%=:\n"
      :
      : "s"(__builtin_amdgcn_kernarg_segment_ptr()), "s"(blockIdx.x), "s"(__builtin_amdgcn_readfirstlane(threadIdx.x >> 6)),
        "s"((unsigned)(size_t)smem)
      : "memory", "scc", "vcc",
        "v0", "v1", "v2", "v3", "v4", "v5", "v6", "v7", "v8", "v9", "v10", "v11", "v12", "v13", "v14", "v15", "v16", "v17", "v18", "v19", "v20", "v21", "v22", "v23", "v24", "v25", "v26", "v27", "v28", "v29", "v30", "v31", "v32", "v33", "v34", "v35", "v36", "v37", "v38", "v39", "v40", "v41", "v42", "v43", "v44", "v45", "v46", "v47", "v48", "v49", "v50", "v51", "v52", "v53", "v54", "v55", "v56", "v57", "v58", "v59", "v60", "v61", "v62", "v63", "v64", "v65", "v66", "v67", "v68", "v69", "v70", "v71", "v72", "v73", "v74", "v75", "v76", "v77", "v78", "v79", "v80", "v81", "v82", "v83", "v84", "v85", "v86", "v87", "v88", "v89", "v90", "v91", "v92", "v93", "v94", "v95", "v96", "v97", "v98", "v99", "v100", "v101", "v102", "v103", "v104", "v105", "v106", "v107", "v108", "v109", "v110", "v111", "v112", "v113", "v114", "v115", "v116", "v117", "v118", "v119", "v120", "v121", "v122", "v123", "v124", "v125", "v126", "v127", "v128", "v129", "v130", "v131", "v132", "v133", "v134", "v135", "v136", "v137", "v138", "v139", "v140", "v141", "v142", "v143", "v144", "v145", "v146", "v147", "v148", "v149", "v150", "v151", "v152", "v153", "v154", "v155", "v156", "v157", "v158", "v159", "v160", "v161", "v162", "v163", "v164", "v165", "v166", "v167", "v168", "v169", "v170", "v171", "v172", "v173", "v174", "v175", "v176", "v177", "v178", "v179", "v180", "v181", "v182", "v183", "v184", "v185", "v186", "v187", "v188", "v189", "v190", "v191", "v192", "v193", "v194", "v195", "v196", "v197", "v198", "v199", "v200", "v201", "v202", "v203", "v204", "v205", "v206", "v207", "v208", "v209", "v210", "v211", "v212", "v213", "v214", "v215", "v216", "v217", "v218", "v219", "v220", "v221", "v222", "v223", "v224", "v225", "v226", "v227", "v228", "v229", "v230", "v231", "v232", "v233", "v234", "v235", "v236", "v237", "v238", "v239", "v240", "v241", "v242", "v243", "v244", "v245", "v246", "v247", "v248", "v249", "v250", "v251", "v252", "v253", "v254", "v255",
        "a0", "a1", "a2", "a3", "a4", "a5", "a6", "a7", "a8", "a9", "a10", "a11", "a12", "a13", "a14", "a15", "a16", "a17", "a18", "a19", "a20", "a21", "a22", "a23", "a24", "a25", "a26", "a27", "a28", "a29", "a30", "a31", "a32", "a33", "a34", "a35", "a36", "a37", "a38", "a39", "a40", "a41", "a42", "a43", "a44", "a45", "a46", "a47", "a48", "a49", "a50", "a51", "a52", "a53", "a54", "a55", "a56", "a57", "a58", "a59", "a60", "a61", "a62", "a63", "a64", "a65", "a66", "a67", "a68", "a69", "a70", "a71", "a72", "a73", "a74", "a75", "a76", "a77", "a78", "a79", "a80", "a81", "a82", "a83", "a84", "a85", "a86", "a87", "a88", "a89", "a90", "a91", "a92", "a93", "a94", "a95", "a96", "a97", "a98", "a99", "a100", "a101", "a102", "a103", "a104", "a105", "a106", "a107", "a108", "a109", "a110", "a111", "a112", "a113", "a114", "a115", "a116", "a117", "a118", "a119", "a120", "a121", "a122", "a123", "a124", "a125", "a126", "a127", "a128", "a129", "a130", "a131", "a132", "a133", "a134", "a135", "a136", "a137", "a138", "a139", "a140", "a141", "a142", "a143", "a144", "a145", "a146", "a147", "a148", "a149", "a150", "a151", "a152", "a153", "a154", "a155", "a156", "a157", "a158", "a159", "a160", "a161", "a162", "a163", "a164", "a165", "a166", "a167", "a168", "a169", "a170", "a171", "a172", "a173", "a174", "a175", "a176", "a177", "a178", "a179", "a180", "a181", "a182", "a183", "a184", "a185", "a186", "a187", "a188", "a189", "a190", "a191", "a192", "a193", "a194", "a195", "a196", "a197", "a198", "a199", "a200", "a201", "a202", "a203", "a204", "a205", "a206", "a207", "a208", "a209", "a210", "a211", "a212", "a213", "a214", "a215", "a216", "a217", "a218", "a219", "a220", "a221", "a222", "a223", "a224", "a225", "a226", "a227", "a228", "a229", "a230", "a231", "a232", "a233", "a234", "a235", "a236", "a237", "a238", "a239", "a240", "a241", "a242", "a243", "a244", "a245", "a246", "a247", "a248", "a249", "a250", "a251", "a252", "a253", "a254", "a255",
        "s8", "s9", "s10", "s11", "s12", "s13", "s14", "s15", "s16", "s17", "s18", "s19", "s20", "s21", "s22", "s23", "s24", "s25", "s26", "s27", "s28", "s29", "s30", "s31", "s32", "s33", "s34", "s35", "s36", "s37", "s38", "s39", "s40", "s41", "s42", "s43", "s44", "s45", "s46", "s47", "s48", "s49", "s50", "s51", "s52", "s53", "s54", "s55", "s56", "s57", "s58", "s59", "s60", "s61", "s62", "s63", "s64", "s65", "s66", "s67", "s68", "s69", "s70", "s71", "s72", "s73", "s74", "s75", "s76", "s77", "s78", "s79", "s80", "s81", "s82", "s83", "s84", "s85", "s86", "s87");
}

extern "C" void kernel_launch(void* const* d_in, const int* in_sizes, int n_in, void* d_out, int out_size, void* d_ws, size_t ws_size,
                              hipStream_t stream) {
  fused_k<<<256, 256, 0, stream>>>((const u16*)d_in[1], (const u16*)d_in[2], (const u16*)d_in[3], (const u16*)d_in[4], (const u16*)d_in[0],
                                   (char*)d_ws, (u16*)d_out, (const u16*)d_in[6], (const u16*)d_in[10], (const u16*)d_in[7],
                                   (const u16*)d_in[8], (const u16*)d_in[9]);
}
```
